```python
import torch

def _softmax(x, axis=-1):
    return torch.softmax(x, dim=axis)

def _gelu(x):
    return torch.nn.functional.gelu(x, approximate='tanh')

def _silu(x):
    return torch.nn.functional.silu(x)

def _sigmoid(x):
    return torch.sigmoid(x)

def _layernorm(x, axis=-1, eps=1e-05):
    m = x.mean(dim=axis, keepdim=True)
    v = x.var(dim=axis, keepdim=True, unbiased=False)
    return (x - m) / torch.sqrt(v + eps)

def _scatter_add(tgt, idx, src, axis=0):
    return tgt.clone().index_add_(axis, idx, src)

def _segment_sum(src, seg, n):
    out = torch.zeros((n,) + src.shape[1:], dtype=src.dtype)
    return out.index_add_(0, seg, src)

def _pad(x, pw):
    flat = tuple((v for pair in reversed(pw) for v in pair))
    return torch.nn.functional.pad(x, flat)
kernel_name = 'interleave_layers__memory_aug_recurrent_mem_tr__spectral_gfnet__prompt'

def setup_inputs():

    _w = lambda a: (lambda t: t.to(torch.bfloat16) if t.is_floating_point() else t)(torch.as_tensor(a))

    def _A_setup():
        B, S, Dm = (1, 2048, 640)
        H, Dh = (32, 128)
        M, seg = (8, 32)
        seg = min(seg, S)
        S = S // seg * seg
        return {'x': _w((lambda *s: torch.randn(*s, dtype=torch.bfloat16) * 0.05)(B, S, Dm)), 'mem0': _w((lambda *s: torch.randn(*s, dtype=torch.bfloat16) * 0.2)(B, M, Dm)), 'Wq': _w((lambda *s: torch.randn(*s, dtype=torch.bfloat16) * 1.6)(Dm, H, Dh) * Dm ** (-0.5)), 'Wk': _w((lambda *s: torch.randn(*s, dtype=torch.bfloat16) * 1.6)(Dm, H, Dh) * Dm ** (-0.5)), 'Wv': _w((lambda *s: torch.randn(*s, dtype=torch.bfloat16) * 0.2)(Dm, H, Dh) * Dm ** (-0.5)), 'Wo': _w((lambda *s: torch.randn(*s, dtype=torch.bfloat16) * 0.2)(H, Dh, Dm) * (H * Dh) ** (-0.5)), 'seg_len': seg}
    _A_in = _A_setup()

    def _B_setup():
        B, S, D = (1, 2048, 640)
        Df = D // 2 + 1
        return {'x': _w((lambda *s: torch.randn(*s, dtype=torch.bfloat16) * 0.05)(B, S, D)), 'w_r': _w((lambda *s: torch.randn(*s, dtype=torch.bfloat16) * 0.4)(S, Df) * 0.02), 'w_i': _w((lambda *s: torch.randn(*s, dtype=torch.bfloat16) * 0.4)(S, Df) * 0.02)}
    _B_in = _B_setup()
    return {'A_x': _A_in['x'], 'A_mem0': _A_in['mem0'], 'A_Wq': _A_in['Wq'], 'A_Wk': _A_in['Wk'], 'A_Wv': _A_in['Wv'], 'A_Wo': _A_in['Wo'], 'A_seg_len': _A_in['seg_len'], 'B_x': _B_in['x'], 'B_w_r': _B_in['w_r'], 'B_w_i': _B_in['w_i']}

def reference(A_x, A_mem0, A_Wq, A_Wk, A_Wv, A_Wo, A_seg_len, B_x, B_w_r, B_w_i):

    def _A_ref(x, mem0, Wq, Wk, Wv, Wo, seg_len):
        B, S, Dm = x.shape
        H, Dh = (Wq.shape[1], Wq.shape[2])
        M = mem0.shape[1]
        scale = Dh ** (-0.5)
        n_seg = S // seg_len
        L = M + seg_len
        idx = torch.arange(L)
        mask = (idx[:, None] < M) | (idx[:, None] >= idx[None, :])
        mem = mem0
        outs = []
        for s in range(n_seg):
            xs = x[:, s * seg_len:(s + 1) * seg_len, :].clone()
            h = torch.concatenate([mem, xs], axis=1)
            q = torch.einsum('btd,dhe->bhte', h, Wq)
            k = torch.einsum('btd,dhe->bhte', h, Wk)
            v = torch.einsum('btd,dhe->bhte', h, Wv)
            scores = torch.einsum('bhqe,bhke->bhqk', q, k) * scale
            scores = torch.where(mask[None, None], scores, -1000000000.0)
            p = _softmax(scores, axis=-1)
            o = torch.einsum('bhqk,bhke->bhqe', p, v)
            o = torch.einsum('bhte,hed->btd', o, Wo)
            mem = o[:, :M, :].clone()
            outs.append(o[:, M:, :])
        return torch.concatenate(outs, axis=1)

    def _B_ref(x, w_r, w_i):
        B, S, D = x.shape
        X = (lambda x, axes=(-2, -1): torch.fft.rfft2(x.float(), dim=axes))(x, axes=(-2, -1))
        W = w_r + 1j * w_i
        X = X * W
        y = (lambda x, s, axes=(-2, -1): torch.fft.irfft2(x, s=s, dim=axes).to(torch.bfloat16))(X, (S, D), axes=(-2, -1))
        return y
    x = A_x
    x = x + _A_ref(_layernorm(x, axis=-1), A_mem0, A_Wq, A_Wk, A_Wv, A_Wo, A_seg_len)
    x = x + _B_ref(_layernorm(x, axis=-1), B_w_r, B_w_i)
    x = x + _A_ref(_layernorm(x, axis=-1), A_mem0, A_Wq, A_Wk, A_Wv, A_Wo, A_seg_len)
    x = x + _B_ref(_layernorm(x, axis=-1), B_w_r, B_w_i)
    return x
```

```cpp
#include <hip/hip_runtime.h>
#include <hip/hip_bf16.h>
#include <stdint.h>

typedef unsigned v4u __attribute__((ext_vector_type(4)));
typedef unsigned v2u __attribute__((ext_vector_type(2)));
typedef float f32x4 __attribute__((ext_vector_type(4)));
typedef float f32x2 __attribute__((ext_vector_type(2)));
typedef __bf16 bf16x8 __attribute__((ext_vector_type(8)));
typedef __bf16 bf16x2 __attribute__((ext_vector_type(2)));
typedef unsigned short u16;

constexpr int S_ = 2048, D_ = 640, NSEG = 64, KT = 20, RT = 128;
constexpr int NFT = 40;
constexpr int NFK = 20;
constexpr int WIN = 32768, REG = 16 * WIN;
constexpr int W_MEM = 10 * WIN, W_MISC = 15 * WIN;
__host__ __device__ constexpr int copy_off(int c) { return (c & 3) * 2048 + (c >> 2) * 16384; }
constexpr int M_STAMP = W_MISC + copy_off(4), M_TM = M_STAMP + 80 * 64, M_CAL = W_MISC + copy_off(3);
constexpr size_t SZ_POOL = (size_t)(NSEG + 1) * REG;

constexpr size_t OFF_CNT = 0;
constexpr int CNT_IDFT = 0, CNT_COL = 1, CNT_OUT = 2;
constexpr size_t OFF_PROG = OFF_CNT + (size_t)4 * 32 * 128;
constexpr size_t OFF_KQF = OFF_PROG + (size_t)NSEG * 7168;
constexpr size_t OFF_TICK = OFF_KQF + (size_t)NSEG * 4096;
constexpr size_t OFF_FAN = OFF_TICK + (size_t)16 * 128;
constexpr size_t HO_LAYER = OFF_FAN + (size_t)2 * 64 * 128;
constexpr size_t OFF_HO = SZ_POOL;
constexpr size_t OFF_SEL = OFF_HO + 2 * HO_LAYER;
__host__ __device__ constexpr int prog_off(int layer, int rb, int slot) { return (int)(OFF_HO + layer * HO_LAYER + OFF_PROG) + (rb * 112 + slot) * 64; }
__host__ __device__ constexpr int kqf_off(int layer, int sg, int h) { return (int)(OFF_HO + layer * HO_LAYER + OFF_KQF) + (sg * 64 + h) * 64; }
__host__ __device__ constexpr int cnt_off(int layer, int kind, int rb) { return (int)(OFF_HO + layer * HO_LAYER + OFF_CNT) + (kind * 32 + rb) * 128; }
__host__ __device__ constexpr int tick_off(int layer, int i) { return (int)(OFF_HO + layer * HO_LAYER + OFF_TICK) + i * 128; }
__host__ __device__ constexpr int fan_off(int layer, int set, int i) { return (int)(OFF_HO + layer * HO_LAYER + OFF_FAN) + (set * 64 + i) * 128; }
constexpr size_t OFF_WF = (OFF_SEL + 8192 + 4095) / 4096 * 4096;
constexpr size_t OFF_WOF = OFF_WF + (size_t)3 * 32 * 8 * 20 * 1024;
constexpr size_t OFF_BF = OFF_WOF + (size_t)32 * 40 * 4 * 1024;
constexpr size_t OFF_XR = OFF_BF + (size_t)80 * 4 * 4 * 20 * 1024;
constexpr size_t OFF_ST = OFF_XR + (size_t)RT * KT * 1024;
constexpr size_t OFF_QF = OFF_ST + (size_t)S_ * 48;
constexpr size_t OFF_KF = OFF_QF + (size_t)32 * RT * 4 * 1024;
constexpr size_t OFF_UF = OFF_KF + (size_t)32 * RT * 4 * 1024;
constexpr size_t OFF_PXF = OFF_UF + (size_t)NSEG * 80 * 16 * 1024;
constexpr size_t OFF_PXM = OFF_PXF + (size_t)NSEG * 32 * 2 * 1024;
constexpr size_t OFF_YF = OFF_PXM + (size_t)NSEG * 8 * 2 * 1024;
constexpr size_t OFF_FWD = OFF_YF + (size_t)NSEG * 8 * 80 * 512;
constexpr size_t OFF_INV = OFF_FWD + (size_t)NFT * KT * 1024;
constexpr size_t OFF_X1T = OFF_INV + (size_t)40 * NFK * 1024;
constexpr size_t OFF_ZT = OFF_X1T + (size_t)NFT * 16 * S_ * 4;
constexpr size_t OFF_WT = OFF_ZT + (size_t)NFK * 32 * S_ * 4;
constexpr size_t OFF_TW = OFF_WT + (size_t)321 * S_ * 4;
constexpr size_t OFF_DBG = OFF_TW + (size_t)2048 * 8;
constexpr size_t OFF_END = OFF_DBG + (size_t)1048576;
static_assert(OFF_END <= ((size_t)256 << 20), "workspace too large");

constexpr int AUX_SC1 = 16;

__device__ __forceinline__ f32x4 mfma(v4u a, v4u b, f32x4 c) {
  return __builtin_amdgcn_mfma_f32_16x16x32_bf16(__builtin_bit_cast(bf16x8, a), __builtin_bit_cast(bf16x8, b), c, 0, 0, 0);
}
__device__ __forceinline__ unsigned pk(float a, float b) {
  f32x2 v = {a, b};
  return __builtin_bit_cast(unsigned, __builtin_convertvector(v, bf16x2));
}
__device__ __forceinline__ float lo_f(unsigned u) { return __builtin_bit_cast(float, u << 16); }
__device__ __forceinline__ float hi_f(unsigned u) { return __builtin_bit_cast(float, u & 0xFFFF0000u); }
__device__ __forceinline__ float bfr(float x) { return lo_f(pk(x, x)); }
__device__ __forceinline__ v4u pk8(f32x4 a, f32x4 b) { return v4u{pk(a.x, a.y), pk(a.z, a.w), pk(b.x, b.y), pk(b.z, b.w)}; }
__device__ __forceinline__ int sigma(int g, int jj) { return 16 * (jj >> 2) + 4 * g + (jj & 3); }
__device__ __forceinline__ int colmap(int c8, int jj) {
  const int p = c8 >> 3, ga = (c8 >> 1) & 3, half = c8 & 1;
  return 32 * (2 * p + (ga >> 1)) + 16 * ((jj >> 1) & 1) + 4 * (2 * (ga & 1) + (jj >> 2)) + 2 * half + (jj & 1);
}
__device__ __forceinline__ int pool_base(const void* ws) { return (int)((0u - (unsigned)(uintptr_t)ws) & (unsigned)(REG - 1)); }
__device__ __forceinline__ unsigned xcc_id() { return (unsigned)__builtin_amdgcn_s_getreg(20 | (3 << 11)) & 7u; }
__device__ __forceinline__ float shx(float v, int m) { return __shfl_xor(v, m); }
__device__ __forceinline__ unsigned nosent(unsigned x) { return x < 0xFFFFFFFEu ? x : 0xFFFFFFFEu; }

__device__ __forceinline__ f32x2 frag_stat(v4u c) {
  const float v[8] = {lo_f(c.x), hi_f(c.x), lo_f(c.y), hi_f(c.y), lo_f(c.z), hi_f(c.z), lo_f(c.w), hi_f(c.w)};
  float q = 0.f;
#pragma unroll
  for (int j = 0; j < 8; ++j) q = __builtin_fmaf(v[j], v[j], q);
  return f32x2{((v[0] + v[1]) + (v[2] + v[3])) + ((v[4] + v[5]) + (v[6] + v[7])), q};
}
__device__ __forceinline__ f32x2 rows_sum(f32x2 a) {
  a.x += shx(a.x, 16); a.y += shx(a.y, 16); a.x += shx(a.x, 32); a.y += shx(a.y, 32);
  return a;
}

__device__ __forceinline__ void dft_frag(unsigned char* ws, bool fwd, int fi, int lane) {
  const int g = lane >> 4, r = lane & 15;
  float hi[8];
#pragma unroll
  for (int jj = 0; jj < 8; ++jj) {
    int f, d;
    if (fwd) { const int kt = fi % KT, ft = fi / KT; f = 16 * ft + r; d = 32 * kt + sigma(g, jj); }
    else { const int fk = fi % NFK, dt = fi / NFK; d = 16 * dt + r; f = 32 * fk + 8 * g + jj; }
    const int k = f == 1 ? 320 : f >> 1;
    const int idx = (k * d) % D_;
    const float a = (float)idx * (2.0f / D_);
    float v = ((f & 1) && f != 1) ? -sinpif(a) : cospif(a);
    if (!fwd && f > 1) v *= 2.f;
    hi[jj] = v;
  }
  v4u* dst = (v4u*)(ws + (fwd ? OFF_FWD : OFF_INV));
  dst[(size_t)fi * 64 + lane] = v4u{pk(hi[0], hi[1]), pk(hi[2], hi[3]), pk(hi[4], hi[5]), pk(hi[6], hi[7])};
}

constexpr int PB_WF = 2 * 32 * 5;
constexpr int PB_XR = RT * KT / 16;
constexpr int PB_FWD = 2 * NFT * KT / 4;
constexpr int PB_WT = 32 * 6 + 8;
constexpr int PB_HO = (int)((2 * HO_LAYER + 32767) / 32768);
constexpr int PB_CAL = 4 * NSEG;
constexpr int PB_BF = 32 * 5 * 5;
constexpr int NPRE = 8, PB_FILL = (NPRE + 1) * 16;
constexpr int PB_TOTAL = PB_CAL + PB_BF + PB_WF + PB_XR + PB_FWD + PB_WT + PB_FILL + PB_HO;
static_assert(HO_LAYER % 16 == 0, "fill granularity");
template <int AUX>
__device__ __forceinline__ void fill_pool(unsigned char* ws, int rg, int half, int tid, int q0 = 0, int q1 = 64) {
  const __amdgpu_buffer_rsrc_t rs = __builtin_amdgcn_make_buffer_rsrc(ws, 0, (int)SZ_POOL, 0x00020000);
  const int base = pool_base(ws) + rg * REG + half * 8192 + tid * 16;
#pragma unroll 8
  for (int q = q0; q < q1; ++q) {
    const bool skip = (rg == NSEG - 1 && q >= 40 && q < 60) || ((q | 2) == 63 && tid >= 128);
    __builtin_amdgcn_raw_buffer_store_b128(v4u{0xFFFFFFFFu, 0xFFFFFFFFu, 0xFFFFFFFFu, 0xFFFFFFFFu}, rs,
                                           skip ? 0x7FFFFFF0 : base + (q >> 1) * 16384 + (q & 1) * 4096, 0, AUX);
  }
}
__device__ __forceinline__ void fill_mem0(unsigned char* ws, const u16* __restrict__ mem0, int w, int tid) {
  const int c = tid & 127, c8 = (w * 2 + (c >> 6)) * 8 + ((c >> 3) & 7), row = c & 7;
  unsigned t[4];
#pragma unroll
  for (int k = 0; k < 4; ++k) t[k] = nosent(*(const unsigned*)(mem0 + (size_t)row * D_ + colmap(c8, 2 * k)));
  v4u* dst = (v4u*)(ws + pool_base(ws) + (size_t)(NSEG - 1) * REG + W_MEM + w * WIN);
#pragma unroll
  for (int q = 0; q < 8; ++q) dst[q * 256 + tid] = v4u{t[0], t[1], t[2], t[3]};
}
typedef int v4i __attribute__((ext_vector_type(4)));
__device__ __forceinline__ v4i calibrate(unsigned char* ws, int rg, int part) {
  const __amdgpu_buffer_rsrc_t rs = __builtin_amdgcn_make_buffer_rsrc(ws, 0, (int)SZ_POOL, 0x00020000);
  const int base = pool_base(ws) + rg * REG + M_CAL + part * 512, lane = threadIdx.x;
  unsigned best[4] = {0xFFFFu, 0xFFFFu, 0xFFFFu, 0xFFFFu};
#pragma unroll
  for (int i = 0; i < 4; ++i) __builtin_amdgcn_raw_buffer_store_b128(v4u{1u, 2u, 3u, 4u}, rs, base + i * 8192 + (lane & 31) * 16, 0, AUX_SC1);
#pragma unroll 1
  for (int i = 0; i < 4; ++i) {
#pragma unroll
    for (int c = 0; c < 4; ++c) {
      unsigned long long t0, t1;
      asm volatile("s_waitcnt vmcnt(0)\n\ts_memtime %0\n\ts_waitcnt lgkmcnt(0)" : "=s"(t0)::"memory");
      unsigned x = __builtin_amdgcn_raw_buffer_load_b128(rs, base + c * 8192 + i * 128 + (lane & 3) * 16, 0, AUX_SC1)[0];
      asm volatile("s_waitcnt vmcnt(0)\n\ts_memtime %0\n\ts_waitcnt lgkmcnt(0)" : "=s"(t1), "+v"(x)::"memory");
      best[c] = min(best[c], (unsigned)(t1 - t0));
    }
  }
  const int x = (int)xcc_id(), sg = x < 4 ? 1 - 2 * (x & 1) : 0;
  const int d = min(max((int)(best[0] + best[2]) - (int)(best[1] + best[3]), -400), 400);
  return v4i{x < 4 ? d : -d, sg * min(max((int)best[0] - (int)best[2], -80), 80), sg * min(max((int)best[1] - (int)best[3], -80), 80), 0};
}
__device__ __forceinline__ unsigned pool_sel(const unsigned char* ws, int rg) {
  const v4i* t = (const v4i*)(ws + OFF_SEL) + 16;
  const int pos = (int)((((unsigned)(uintptr_t)ws + (unsigned)pool_base(ws)) / REG + rg) & 3u);
  v4i a = {0, 0, 0, 0};
#pragma unroll
  for (int i = 0; i < 16; ++i) {
    const v4i e = t[(rg - pos) * 4 + i];
    const int sg = (((i >> 2) ^ pos) & 1) ? -1 : 1;
    a.x += e.x; a.y += sg * e.y; a.z += sg * e.z;
  }
  return (a.x > 0 ? 1u : 0u) | ((a.x > 0 ? a.z : a.y) > 0 ? 2u : 0u);
}

typedef short v4s __attribute__((ext_vector_type(4)));
__device__ __forceinline__ void tile_frags(const u16* tile, v4u* dst, int wv, int lane) {
  typedef __attribute__((address_space(3))) v4s* lp;
  const u16* p = tile + (32 * wv + 4 * (lane >> 4) + ((lane >> 2) & 3)) * 136 + (lane & 3) * 4;
#pragma unroll
  for (int et = 0; et < 8; ++et) {
    const v2u a = __builtin_bit_cast(v2u, __builtin_amdgcn_ds_read_tr16_b64_v4i16((lp)(p + 16 * et)));
    const v2u b = __builtin_bit_cast(v2u, __builtin_amdgcn_ds_read_tr16_b64_v4i16((lp)(p + 16 * 136 + 16 * et)));
    dst[(size_t)(et * 20 + wv) * 64 + lane] = v4u{a.x, a.y, b.x, b.y};
  }
}

__global__ void __launch_bounds__(256) k_prep(const u16* __restrict__ x, const u16* __restrict__ mem0, const u16* __restrict__ Wq,
                                              const u16* __restrict__ Wk, const u16* __restrict__ Wv, const u16* __restrict__ Wo,
                                              const u16* __restrict__ wr, const u16* __restrict__ wi, unsigned char* __restrict__ ws) {
  __shared__ __attribute__((aligned(16))) u16 tile[128 * 136];
  int b = blockIdx.x;
  const int tid = threadIdx.x, lane = tid & 63, wv = tid >> 6, g = lane >> 4, r = lane & 15;
  if (b < PB_CAL) {
    if (b < 5) fill_mem0(ws, mem0, b, tid);
    if (tid < 64) {
      const v4i sel = calibrate(ws, b >> 2, b & 3);
      if (tid == 0) ((v4i*)(ws + OFF_SEL))[16 + b] = sel;
      if (b == 0 && tid >= 32) ((v4i*)(ws + OFF_SEL))[tid < 48 ? tid - 32 : tid + 224] = v4i{0, 0, 0, 0};
    }
    return;
  }
  b -= PB_CAL;
  if (b < PB_BF) {
    const int h = b & 31, part = (b >> 5) % 5, dq = b / 160;
    const u16* so = Wo + (size_t)h * 128 * D_ + 128 * part;
    const u16* sv = Wv + ((size_t)(128 * dq) * 32 + h) * 128;
    v4u to[8], tv[8];
#pragma unroll
    for (int q = 0; q < 8; ++q) {
      const int i = tid + 256 * q, row = i >> 4, ch = i & 15;
      to[q] = *(const v4u*)(so + (size_t)row * D_ + ch * 8);
      tv[q] = *(const v4u*)(sv + (size_t)row * 4096 + ch * 8);
    }
#pragma unroll
    for (int q = 0; q < 8; ++q) { const int i = tid + 256 * q; *(v4u*)(tile + (i >> 4) * 136 + (i & 15) * 8) = to[q]; }
    __syncthreads();
    v4u bw[2][4];
#pragma unroll
    for (int c = 0; c < 2; ++c) {
      const int col = colmap(2 * (2 * wv + c) + (r >> 3), r & 7);
#pragma unroll
      for (int t = 0; t < 4; ++t) {
        u16 v[8];
#pragma unroll
        for (int jj = 0; jj < 8; ++jj) v[jj] = tile[(32 * t + sigma(g, jj)) * 136 + col];
        bw[c][t] = v4u{v[0] | ((unsigned)v[1] << 16), v[2] | ((unsigned)v[3] << 16), v[4] | ((unsigned)v[5] << 16), v[6] | ((unsigned)v[7] << 16)};
        if (dq == 0) ((v4u*)(ws + OFF_WOF))[(size_t)((h * 40 + 8 * part + 2 * wv + c) * 4 + t) * 64 + lane] = bw[c][t];
      }
    }
    __syncthreads();
#pragma unroll
    for (int q = 0; q < 8; ++q) { const int i = tid + 256 * q; *(v4u*)(tile + (i >> 4) * 136 + (i & 15) * 8) = tv[q]; }
    __syncthreads();
    if (part == 1) tile_frags(tile, (v4u*)(ws + OFF_WF) + (size_t)((64 + h) * 8 * 20 + 4 * dq) * 64, wv, lane);
    v4u* BF = (v4u*)(ws + OFF_BF);
#pragma unroll
    for (int i = 0; i < 4; ++i) {
      const int kt = dq * 4 + i;
      v4u a[2][4];
#pragma unroll
      for (int q = 0; q < 2; ++q)
#pragma unroll
        for (int t = 0; t < 4; ++t) {
          const u16* p = tile + (32 * i + 16 * q + r) * 136 + 32 * t + 4 * g;
          const v2u x0 = *(const v2u*)p, x1 = *(const v2u*)(p + 16);
          a[q][t] = v4u{x0.x, x0.y, x1.x, x1.y};
        }
#pragma unroll
      for (int c = 0; c < 2; ++c) {
        f32x4 acc0 = {0, 0, 0, 0}, acc1 = {0, 0, 0, 0};
#pragma unroll
        for (int t = 0; t < 4; ++t) { acc0 = mfma(a[0][t], bw[c][t], acc0); acc1 = mfma(a[1][t], bw[c][t], acc1); }
        const int c8 = 2 * (8 * part + 2 * wv + c) + (r >> 3);
        BF[(size_t)((((c8 * 4 + (h >> 3)) * 4 + ((h >> 1) & 3)) * 20 + kt) * 64 + g * 16 + (h & 1) * 8 + (r & 7))] = pk8(acc0, acc1);
      }
    }
    return;
  }
  b -= PB_BF;
  if (b < PB_WF) {
    const int h = b & 31, dq = (b >> 5) % 5, which = b / 160;
    const u16* src = (which == 0 ? Wq : Wk) + ((size_t)(128 * dq) * 32 + h) * 128;
    v4u t[8];
#pragma unroll
    for (int q = 0; q < 8; ++q) { const int i = tid + 256 * q; t[q] = *(const v4u*)(src + (size_t)(i >> 4) * 4096 + (i & 15) * 8); }
#pragma unroll
    for (int q = 0; q < 8; ++q) { const int i = tid + 256 * q; *(v4u*)(tile + (i >> 4) * 136 + (i & 15) * 8) = t[q]; }
    __syncthreads();
    tile_frags(tile, (v4u*)(ws + OFF_WF) + (size_t)((which * 32 + h) * 8 * 20 + 4 * dq) * 64, wv, lane);
    return;
  }
  b -= PB_WF;
  if (b < PB_XR) {
    f32x2 st = {0.f, 0.f};
#pragma unroll
    for (int q = 0; q < 4; ++q) {
      const int f = b * 16 + wv * 4 + q, kt = f % KT, rt = f / KT;
      const u16* xr = x + (size_t)(rt * 16 + r) * D_ + 32 * kt + 4 * g;
      const v2u c0 = *(const v2u*)xr, c1 = *(const v2u*)(xr + 16);
      const v4u c = v4u{c0.x, c0.y, c1.x, c1.y};
      ((v4u*)(ws + OFF_XR))[(size_t)f * 64 + lane] = c;
      st += frag_stat(c);
    }
    st = rows_sum(st);
    const int f4 = b * 4 + wv;
    if (g == 0) ((f32x2*)(ws + OFF_ST))[((f4 / 5) * 16 + r) * 6 + f4 % 5] = st;
    return;
  }
  b -= PB_XR;
  if (b < PB_FWD) { dft_frag(ws, b < PB_FWD / 2, (b * 4 + wv) % (NFT * KT), lane); return; }
  b -= PB_FWD;
  if (b < PB_WT) {
    if (b >= 192) {
      const int i = (b - 192) * 256 + tid;
      const float a = (float)i * (2.0f / 2048.f);
      ((f32x2*)(ws + OFF_TW))[i] = f32x2{cospif(a), -sinpif(a)};
      return;
    }
    unsigned* t32 = (unsigned*)tile;
    const int m0 = (b & 31) * 64, k0 = (b >> 5) * 64;
#pragma unroll
    for (int q = 0; q < 16; ++q) {
      const int m = q * 4 + wv, k = k0 + lane;
      if (k < 321) t32[m * 65 + lane] = (unsigned)wr[(size_t)(m0 + m) * 321 + k] | ((unsigned)wi[(size_t)(m0 + m) * 321 + k] << 16);
    }
    __syncthreads();
#pragma unroll
    for (int q = 0; q < 16; ++q) {
      const int k = q * 4 + wv;
      if (k0 + k < 321) ((unsigned*)(ws + OFF_WT))[(size_t)(k0 + k) * S_ + m0 + lane] = t32[lane * 65 + k];
    }
    return;
  }
  b -= PB_WT;
  if (b < PB_FILL) { fill_pool<0>(ws, ((b >> 4) + 63) & 63, (b >> 3) & 1, tid, (b & 7) * 8, (b & 7) * 8 + 8); return; }
  b -= PB_FILL;
#pragma unroll
  for (int q = 0; q < 8; ++q) {
    const int i = b * 2048 + q * 256 + tid;
    if ((size_t)i * 16 < 2 * HO_LAYER) ((v4u*)(ws + OFF_HO))[i] = v4u{0xFFFFFFFFu, 0xFFFFFFFFu, 0xFFFFFFFFu, 0xFFFFFFFFu};
  }
}

__device__ __forceinline__ int lane_id() {
  int l;
  asm volatile("v_mbcnt_lo_u32_b32 %0, -1, 0\n\tv_mbcnt_hi_u32_b32 %0, -1, %0" : "=v"(l));
  return l;
}
#define TIDX (wv_ * 64 + lane_id())
typedef __amdgpu_buffer_rsrc_t rsrc_t;
__device__ __forceinline__ rsrc_t ws_rsrc(unsigned char* ws) { return __builtin_amdgcn_make_buffer_rsrc(ws, 0, (int)OFF_END, 0x00020000); }
__device__ __forceinline__ v4u ldc(rsrc_t rs, int off) { return __builtin_amdgcn_raw_buffer_load_b128(rs, off, 0, AUX_SC1); }
__device__ __forceinline__ v4u ldp(rsrc_t rs, int off) { return __builtin_amdgcn_raw_buffer_load_b128(rs, off, 0, 0); }
__device__ __forceinline__ void stc(rsrc_t rs, int off, v4u v) { __builtin_amdgcn_raw_buffer_store_b128(v, rs, off, 0, AUX_SC1); }

typedef __attribute__((address_space(1))) unsigned gu32;
__device__ __forceinline__ void dep_wait(int wv_, unsigned char* ws, int off, unsigned want) {
  if (TIDX == 0) {
    gu32* p = (gu32*)(ws + off);
    for (unsigned spins = 0; spins < (1u << 17); ++spins) {
      if (__hip_atomic_load(p, __ATOMIC_RELAXED, __HIP_MEMORY_SCOPE_AGENT) + 1u >= want) break;
      __builtin_amdgcn_s_sleep(8);
    }
  }
  __syncthreads();
}
__device__ __forceinline__ void prog_prewait(int wv_, unsigned char* ws, int off) {
  if (TIDX == 0) {
    gu32* p = (gu32*)(ws + off);
    for (unsigned spins = 0; spins < (1u << 16); ++spins) {
      if (__hip_atomic_load(p, __ATOMIC_RELAXED, __HIP_MEMORY_SCOPE_AGENT) != 0xFFFFFFFFu) break;
      __builtin_amdgcn_s_sleep(48);
    }
  }
}
__device__ __forceinline__ void prog_wait(int wv_, unsigned char* ws, int off) {
  if (wv_ == 0) {
    const int l = lane_id();
    gu32* p0 = (gu32*)(ws + off + l * 64);
    gu32* p1 = (gu32*)(ws + off + (l < 48 ? l + 64 : 0) * 64);
    for (unsigned spins = 0; spins < (1u << 17); ++spins) {
      const unsigned a = __hip_atomic_load(p0, __ATOMIC_RELAXED, __HIP_MEMORY_SCOPE_AGENT);
      const unsigned b = __hip_atomic_load(p1, __ATOMIC_RELAXED, __HIP_MEMORY_SCOPE_AGENT);
      if (!__any(a == 0xFFFFFFFFu || b == 0xFFFFFFFFu)) break;
      __builtin_amdgcn_s_sleep(8);
    }
  }
  __syncthreads();
}
__device__ __forceinline__ void flags32_wait(int wv_, unsigned char* ws, int off) {
  if (wv_ == 0) {
    const int l = lane_id();
    __attribute__((address_space(1))) unsigned long long* p0 = (__attribute__((address_space(1))) unsigned long long*)(ws + off + (l & 31) * 64 + (l >> 5) * 8);
    for (unsigned spins = 0; spins < (1u << 17); ++spins) {
      const unsigned long long v = __hip_atomic_load(p0, __ATOMIC_RELAXED, __HIP_MEMORY_SCOPE_AGENT);
      if (!__any(max((unsigned)v, (unsigned)(v >> 32)) == 0xFFFFFFFFu)) break;
      __builtin_amdgcn_s_sleep(16);
    }
  }
  __syncthreads();
}
__device__ __forceinline__ void dep_signal(int wv_, unsigned char* ws, int off, unsigned inc = 1u) {
  asm volatile("s_waitcnt vmcnt(0)" ::: "memory");
  __syncthreads();
  if (TIDX == 0) __hip_atomic_fetch_add((gu32*)(ws + off), inc, __ATOMIC_RELAXED, __HIP_MEMORY_SCOPE_AGENT);
}

__device__ __forceinline__ void stat_put(v4u* lds, int wv, int rt, int lane, v4u y) {
  const f32x2 st = rows_sum(frag_stat(y));
  if (lane < 16) ((f32x2*)(lds + 5120))[(wv * 2 + rt) * 16 + lane] = st;
}
__device__ __forceinline__ void stat_store(rsrc_t rs, v4u* lds, int wv, int lane, int sg, int cg) {
  __syncthreads();
  if (wv == 0) {
    const f32x2* sp = (const f32x2*)(lds + 5120) + (lane & 31);
    const f32x2 st = (sp[0] + sp[32]) + (sp[64] + sp[96]);
    __builtin_amdgcn_raw_buffer_store_b64(__builtin_bit_cast(v2u, st), rs, lane < 32 ? (int)OFF_ST + ((sg * 32 + lane) * 6 + cg) * 8 : 0x7FFFFFF0, 0, AUX_SC1);
  }
}

__device__ __forceinline__ void job_out(int wv_, unsigned char* ws, rsrc_t rs, v4u* lds, int layer, int s, int cg) {
  const int lane = lane_id(), wv = wv_, g = lane >> 4, r = lane & 15;
  const int kt = cg * 4 + wv;
  const int c8 = 4 * kt + (r >> 2);
  const int ubase = (int)OFF_UF + (((s * 80 + c8) * 16) * 64 + g * 16 + (r & 3)) * 16;
  const int ybase = (int)OFF_YF + (((s * 8 * 80 + c8) * 4 + g) * 8 + (r & 3)) * 16;
  v4u z[40][2];
#pragma unroll
  for (int ks = 0; ks < 32; ++ks) {
    const int o = ubase + (ks >> 1) * 1024 + (ks & 1) * 128;
    z[ks][0] = ldc(rs, o); z[ks][1] = ldc(rs, o + 64);
  }
  const int xo = (int)OFF_XR + ((2 * s * KT + kt) * 64 + lane) * 16;
  const v4u x0 = ldc(rs, xo), x1 = ldc(rs, xo + KT * 1024);
  if (s > 1) prog_prewait(wv_, ws, prog_off(layer, s - 2, (s + cg * 7) & 31));
#define OSTAMP(i)
  OSTAMP(4)
  prog_wait(wv_, ws, prog_off(layer, s, 0));
  OSTAMP(0)
  {
    const unsigned l0 = (unsigned)(uintptr_t)(__attribute__((address_space(3))) v4u*)lds;
    const int vo = lane * 16;
#pragma unroll
    for (int q = 0; q < 5; ++q) {
      const unsigned d = l0 + (q < 4 ? wv * 16384 + q * 4096 : 65536 + wv * 4096);
      const int so = q < 4 ? (int)OFF_PXF + s * 65536 + wv * 16384 + q * 4096 : (int)OFF_PXM + s * 16384 + wv * 4096;
      asm volatile("s_mov_b32 m0, %0\n\ts_nop 0\n\tbuffer_load_dwordx4 %1, %2, %3 offen sc1 lds\n\t"
                   "buffer_load_dwordx4 %1, %2, %3 offen offset:1024 sc1 lds\n\tbuffer_load_dwordx4 %1, %2, %3 offen offset:2048 sc1 lds\n\t"
                   "buffer_load_dwordx4 %1, %2, %3 offen offset:3072 sc1 lds" ::"s"(d), "v"(vo), "s"(rs), "s"(so) : "m0", "memory");
    }
#pragma unroll
    for (int hq = 0; hq < 8; ++hq) { z[32 + hq][0] = ldc(rs, ybase + hq * 80 * 512); z[32 + hq][1] = ldc(rs, ybase + hq * 80 * 512 + 64); }
  }
  f32x4 acc[2][2] = {{{0, 0, 0, 0}, {0, 0, 0, 0}}, {{0, 0, 0, 0}, {0, 0, 0, 0}}};
  asm volatile("s_waitcnt vmcnt(16)\n\ts_barrier" ::: "memory");
  OSTAMP(1)
  v4u pf[2][8];
#pragma unroll
  for (int i = 0; i < 8; ++i) pf[0][i] = lds[i * 64 + lane];
#pragma unroll
  for (int b = 0; b < 10; ++b) {
    if (b < 9) {
#pragma unroll
      for (int i = 0; i < 8; ++i) pf[(b + 1) & 1][i] = lds[((b + 1) * 8 + i) * 64 + lane];
    }
    __builtin_amdgcn_sched_barrier(0);
#pragma unroll
    for (int i = 0; i < 8; ++i) {
      acc[0][i & 1] = mfma(z[b * 4 + (i >> 1)][0], pf[b & 1][i], acc[0][i & 1]);
      acc[1][i & 1] = mfma(z[b * 4 + (i >> 1)][1], pf[b & 1][i], acc[1][i & 1]);
    }
    __builtin_amdgcn_sched_barrier(0);
  }
#pragma unroll
  for (int qt = 0; qt < 2; ++qt) {
    const v4u x = qt ? x1 : x0;
    const auto s0 = __builtin_amdgcn_permlane16_swap(pk(acc[0][qt].x, acc[0][qt].y), pk(acc[1][qt].x, acc[1][qt].y), false, false);
    const auto s1 = __builtin_amdgcn_permlane16_swap(pk(acc[0][qt].z, acc[0][qt].w), pk(acc[1][qt].z, acc[1][qt].w), false, false);
    v4u y;
    y.x = pk(lo_f(x.x) + lo_f(s0[0]), hi_f(x.x) + hi_f(s0[0]));
    y.y = pk(lo_f(x.y) + lo_f(s0[1]), hi_f(x.y) + hi_f(s0[1]));
    y.z = pk(lo_f(x.z) + lo_f(s1[0]), hi_f(x.z) + hi_f(s1[0]));
    y.w = pk(lo_f(x.w) + lo_f(s1[1]), hi_f(x.w) + hi_f(s1[1]));
    stc(rs, xo + qt * KT * 1024, y);
    stat_put(lds, wv, qt, lane, y);
  }
  OSTAMP(2)
  stat_store(rs, lds, wv, lane, s, cg);
}

__device__ __forceinline__ f32x2 cmul(f32x2 a, f32x2 b) { return f32x2{a.x * b.x - a.y * b.y, a.x * b.y + a.y * b.x}; }
struct C2 { f32x2 r, i; };
__device__ __forceinline__ C2 operator+(C2 a, C2 b) { return C2{a.r + b.r, a.i + b.i}; }
__device__ __forceinline__ C2 operator-(C2 a, C2 b) { return C2{a.r - b.r, a.i - b.i}; }
__device__ __forceinline__ C2 cmul(C2 a, f32x2 w) {
  return C2{__builtin_elementwise_fma(a.r, f32x2{w.x, w.x}, -(a.i * f32x2{w.y, w.y})), __builtin_elementwise_fma(a.r, f32x2{w.y, w.y}, a.i * f32x2{w.x, w.x})};
}
__device__ __forceinline__ C2 cmul(C2 a, C2 w) { return C2{__builtin_elementwise_fma(a.r, w.r, -(a.i * w.i)), __builtin_elementwise_fma(a.r, w.i, a.i * w.r)}; }
template <int SGN> __device__ __forceinline__ C2 addrot(C2 a, C2 b) { return SGN < 0 ? C2{a.r + b.i, a.i - b.r} : C2{a.r - b.i, a.i + b.r}; }
template <int SGN> __device__ __forceinline__ void fft4(C2& a0, C2& a1, C2& a2, C2& a3) {
  const C2 t0 = a0 + a2, t1 = a0 - a2, t2 = a1 + a3, t3 = a1 - a3;
  a0 = t0 + t2; a1 = addrot<SGN>(t1, t3); a2 = t0 - t2; a3 = addrot<-SGN>(t1, t3);
}
template <int SGN> __device__ __forceinline__ void fft8(C2* v) {
  C2 e0 = v[0], e1 = v[2], e2 = v[4], e3 = v[6], o0 = v[1], o1 = v[3], o2 = v[5], o3 = v[7];
  fft4<SGN>(e0, e1, e2, e3);
  fft4<SGN>(o0, o1, o2, o3);
  const f32x2 h = {0.70710678118654752f, 0.70710678118654752f}, nh = -h;
  const C2 u1 = addrot<SGN>(o1, o1), u3 = addrot<SGN>(o3, o3);
  v[0] = e0 + o0; v[4] = e0 - o0;
  v[1] = C2{__builtin_elementwise_fma(u1.r, h, e1.r), __builtin_elementwise_fma(u1.i, h, e1.i)};
  v[5] = C2{__builtin_elementwise_fma(u1.r, nh, e1.r), __builtin_elementwise_fma(u1.i, nh, e1.i)};
  v[2] = addrot<SGN>(e2, o2); v[6] = addrot<-SGN>(e2, o2);
  v[3] = C2{__builtin_elementwise_fma(u3.i, SGN < 0 ? h : nh, e3.r), __builtin_elementwise_fma(u3.r, SGN < 0 ? nh : h, e3.i)};
  v[7] = C2{__builtin_elementwise_fma(u3.i, SGN < 0 ? nh : h, e3.r), __builtin_elementwise_fma(u3.r, SGN < 0 ? h : nh, e3.i)};
}
__device__ __forceinline__ int lpad(int i) { return i + (i >> 3); }
__device__ __forceinline__ C2 c2(f32x4 t) { return C2{f32x2{t.x, t.y}, f32x2{t.z, t.w}}; }
template <int SGN> __device__ __forceinline__ void pass8_load(const f32x4* in, f32x2 w1, int j, C2* v) {
  f32x4 t[8];
#pragma unroll
  for (int q = 0; q < 8; ++q) t[q] = in[lpad(j + 256 * q)];
  if (SGN > 0) w1.y = -w1.y;
  f32x2 w[8];
  w[1] = w1; w[2] = cmul(w1, w1); w[3] = cmul(w[2], w1); w[4] = cmul(w[2], w[2]); w[5] = cmul(w[4], w1); w[6] = cmul(w[3], w[3]); w[7] = cmul(w[4], w[3]);
  v[0] = c2(t[0]);
#pragma unroll
  for (int q = 1; q < 8; ++q) v[q] = cmul(c2(t[q]), w[q]);
  fft8<SGN>(v);
}
template <int NS, int R> __device__ __forceinline__ void pass_put(f32x4* out, int j, const C2* v) {
  const int k = j & (NS - 1);
  const int base = (j - k) * R + k;
#pragma unroll
  for (int q = 0; q < R; ++q) out[lpad(base + q * NS)] = f32x4{v[q].r.x, v[q].r.y, v[q].i.x, v[q].i.y};
}

constexpr int NCOL = 161, FB = 2048 + 256;
__device__ __forceinline__ void job_col(int wv_, unsigned char* ws, rsrc_t rs, f32x2* lds, int jb, int go) {
  const int j = wv_ * 64 + lane_id();
  f32x4 *bufA = (f32x4*)lds, *bufB = bufA + FB;
  const int xin = (int)OFF_X1T + ((jb == 160 ? 0 : jb) * S_ + j) * 16;
  const f32x2* tw = (const f32x2*)(ws + OFF_TW);
  C2 v[8];
  unsigned wf[2][8];
#pragma unroll
  for (int c = 0; c < 2; ++c) {
    const unsigned* wt = (const unsigned*)(ws + OFF_WT) + (size_t)(jb == 160 ? 320 : 2 * jb + c) * S_;
#pragma unroll
    for (int q = 0; q < 8; ++q) wf[c][q] = wt[j + 256 * q];
  }
  const f32x2 tA = tw[(j & 7) * 32], tB = tw[(j & 63) * 4], tC0 = tw[j], tC1 = tw[j + 256];
  const f32x2 tD = tw[(j & 3) * 64], tE = tw[(j & 31) * 8];
  if (go >= 0) dep_wait(wv_, ws, go, 1u);
#pragma unroll
  for (int q = 0; q < 8; ++q) {
    const f32x4 t = __builtin_bit_cast(f32x4, ldc(rs, xin + q * 4096));
    v[q] = C2{f32x2{jb == 160 ? t.y : t.x, t.z}, f32x2{(jb == 0 || jb == 160) ? 0.f : t.y, t.w}};
  }
#define CSTAMP(i)
  CSTAMP(0)
  fft8<-1>(v);
  pass_put<1, 8>(bufA, j, v);
  __syncthreads();
  CSTAMP(1)
  pass8_load<-1>(bufA, tA, j, v);
  pass_put<8, 8>(bufB, j, v);
  __syncthreads();
  pass8_load<-1>(bufB, tB, j, v);
  pass_put<64, 8>(bufA, j, v);
  __syncthreads();
  const float sc = 1.0f / ((float)S_ * (float)D_);
  {
    f32x4 t[8];
#pragma unroll
    for (int q = 0; q < 8; ++q) t[q] = bufA[lpad(j + 256 * q)];
#pragma unroll
    for (int hh = 0; hh < 2; ++hh) {
      const f32x2 w1 = hh ? tC1 : tC0, w2 = cmul(w1, w1), w3 = cmul(w2, w1);
      C2 a[4] = {c2(t[hh]), cmul(c2(t[hh + 2]), w1), cmul(c2(t[hh + 4]), w2), cmul(c2(t[hh + 6]), w3)};
      fft4<-1>(a[0], a[1], a[2], a[3]);
#pragma unroll
      for (int q = 0; q < 4; ++q) {
        const unsigned w0 = wf[0][hh + 2 * q], w1_ = wf[1][hh + 2 * q];
        a[q] = cmul(a[q], C2{f32x2{lo_f(w0), lo_f(w1_)} * sc, f32x2{hi_f(w0), hi_f(w1_)} * sc});
      }
      fft4<1>(a[0], a[1], a[2], a[3]);
      pass_put<1, 4>(bufB, j + 256 * hh, a);
    }
  }
  __syncthreads();
  pass8_load<1>(bufB, tD, j, v);
  pass_put<4, 8>(bufA, j, v);
  __syncthreads();
  pass8_load<1>(bufA, tE, j, v);
  pass_put<32, 8>(bufB, j, v);
  __syncthreads();
  CSTAMP(2)
  {
    pass8_load<1>(bufB, tC0, j, v);
#pragma unroll
    for (int q = 0; q < 8; ++q)
      __builtin_amdgcn_raw_buffer_store_b64(v2u{pk(v[q].r.x, v[q].i.x), pk(v[q].r.y, v[q].i.y)}, rs, (int)OFF_ZT + (jb * S_ + j + 256 * q) * 8, 0, AUX_SC1);
  }
  CSTAMP(3)
}
__device__ __forceinline__ void fan_signal(int wv_, unsigned char* ws, int cnt, unsigned want, int top, unsigned wtop, int fan) {
  asm volatile("s_waitcnt vmcnt(0)" ::: "memory");
  __syncthreads();
  if (wv_ == 0) {
    const int l = lane_id();
    unsigned last = 0;
    if (l == 0) {
      last = __hip_atomic_fetch_add((gu32*)(ws + cnt), 1u, __ATOMIC_RELAXED, __HIP_MEMORY_SCOPE_AGENT) + 2u == want;
      if (last && top >= 0) last = __hip_atomic_fetch_add((gu32*)(ws + top), 1u, __ATOMIC_RELAXED, __HIP_MEMORY_SCOPE_AGENT) + 2u == wtop;
    }
    if (__builtin_amdgcn_readfirstlane(last)) __hip_atomic_store((gu32*)(ws + fan + l * 128), 1u, __ATOMIC_RELAXED, __HIP_MEMORY_SCOPE_AGENT);
  }
}
__device__ __forceinline__ void col_signal(int wv_, unsigned char* ws, int set, int jb) {
  fan_signal(wv_, ws, cnt_off(set, CNT_COL, jb & 7), (jb & 7) == 0 ? 21u : 20u, cnt_off(set, CNT_COL, 8), 8u, fan_off(set, 0, 0));
}

template <bool FINAL>
__device__ __forceinline__ void job_idft(int wv_, unsigned char* ws, int colcnt, rsrc_t rs, v4u* lds, int sg0, int sgstep, int dq, u16* out, unsigned long long* dbg) {
  const int lane = lane_id(), wv = wv_, g = lane >> 4, r = lane & 15;
  const int kt = dq * 4 + wv;
  v4u a[2][NFK];
#pragma unroll
  for (int fk = 0; fk < NFK; ++fk) {
    a[0][fk] = ldp(rs, (int)OFF_INV + ((2 * kt) * NFK + fk) * 1024 + lane * 16);
    a[1][fk] = ldp(rs, (int)OFF_INV + ((2 * kt + 1) * NFK + fk) * 1024 + lane * 16);
  }
#define ISTAMP(i)
  ISTAMP(0)
  dep_wait(wv_, ws, colcnt, 1u);
  ISTAMP(1)
#pragma unroll 1
  for (int sg = sg0; sg < NSEG; sg += sgstep) {
    const int xo = (int)OFF_XR + ((sg * 2 * KT + kt) * 64 + lane) * 16;
    {
      const int jj = lane >> 3, rp = lane & 7, row = sg * 32 + (wv & 1) * 16 + 2 * rp;
      const int src = (int)OFF_ZT + (((wv >> 1) * 80 + jj) * S_ + row) * 8;
      v4u t[10];
#pragma unroll
      for (int i = 0; i < 10; ++i) t[i] = ldc(rs, src + i * 8 * S_ * 8);
      const v4u ny = ldc(rs, (int)OFF_ZT + (160 * S_ + row) * 8);
      if (wv < 2 && jj == 0) {
        t[0].x = (t[0].x & 0xFFFFu) | (ny.x << 16);
        t[0].z = (t[0].z & 0xFFFFu) | (ny.z << 16);
      }
      v2u* dst = (v2u*)(lds + ((wv & 1) * NFK + (wv >> 1) * 10) * 64 + (jj >> 1) * 16 + 2 * rp) + (jj & 1);
#pragma unroll
      for (int i = 0; i < 10; ++i) { dst[i * 128] = v2u{t[i].x, t[i].y}; dst[i * 128 + 2] = v2u{t[i].z, t[i].w}; }
    }
    const v4u x0 = ldc(rs, xo), x1 = ldc(rs, xo + KT * 1024);
    f32x4 acc[2][2] = {{{0, 0, 0, 0}, {0, 0, 0, 0}}, {{0, 0, 0, 0}, {0, 0, 0, 0}}};
    __syncthreads();
    ISTAMP(2)
#pragma unroll
    for (int k0 = 0; k0 < NFK; k0 += 5) {
      v4u b0[5], b1[5];
#pragma unroll
      for (int d = 0; d < 5; ++d) { b0[d] = lds[(k0 + d) * 64 + lane]; b1[d] = lds[(NFK + k0 + d) * 64 + lane]; }
#pragma unroll
      for (int d = 0; d < 5; ++d) {
        acc[0][0] = mfma(a[0][k0 + d], b0[d], acc[0][0]); acc[0][1] = mfma(a[0][k0 + d], b1[d], acc[0][1]);
        acc[1][0] = mfma(a[1][k0 + d], b0[d], acc[1][0]); acc[1][1] = mfma(a[1][k0 + d], b1[d], acc[1][1]);
      }
    }
#pragma unroll
    for (int rt = 0; rt < 2; ++rt) {
      const v4u x = rt ? x1 : x0;
      v4u y;
      y.x = pk(lo_f(x.x) + bfr(acc[0][rt].x), hi_f(x.x) + bfr(acc[0][rt].y));
      y.y = pk(lo_f(x.y) + bfr(acc[0][rt].z), hi_f(x.y) + bfr(acc[0][rt].w));
      y.z = pk(lo_f(x.z) + bfr(acc[1][rt].x), hi_f(x.z) + bfr(acc[1][rt].y));
      y.w = pk(lo_f(x.w) + bfr(acc[1][rt].z), hi_f(x.w) + bfr(acc[1][rt].w));
      if (FINAL) {
        u16* op = out + (size_t)(sg * 32 + rt * 16 + r) * D_ + 32 * kt + 4 * g;
        *(v2u*)op = v2u{y.x, y.y};
        *(v2u*)(op + 16) = v2u{y.z, y.w};
      } else {
        stc(rs, xo + rt * KT * 1024, y);
        stat_put(lds, wv, rt, lane, y);
      }
    }
    ISTAMP(3)
    if (!FINAL) {
      stat_store(rs, lds, wv, lane, sg, dq);
      if (sg < 12) fan_signal(wv_, ws, cnt_off(1, CNT_IDFT, sg >> 1), 10u, cnt_off(1, CNT_COL, 9), 6u, fan_off(1, 1, 0));
      else dep_signal(wv_, ws, cnt_off(1, CNT_IDFT, sg >> 1));
    }
  }
}

constexpr int JOB_LDS = 4 * NFK * 1024;
extern __shared__ __attribute__((aligned(16))) unsigned char dyn_lds[];
constexpr int FIN_LDS = 4 * FB * 8;
#define FIN_DELAY 3
__global__ void __launch_bounds__(256, 2) k_fin(unsigned char* ws, u16* out) {
  const int wv_ = __builtin_amdgcn_readfirstlane(threadIdx.x >> 6), b = blockIdx.x;
  const rsrc_t rs = ws_rsrc(ws);
  if (b < NCOL) {
    job_col(wv_, ws, rs, (f32x2*)dyn_lds, b, -1);
    col_signal(wv_, ws, 0, b);
  } else {
    for (int i = 0; i < FIN_DELAY; ++i) __builtin_amdgcn_s_sleep(32);
    job_idft<true>(wv_, ws, fan_off(0, 0, b & 63), rs, (v4u*)dyn_lds, (b - NCOL) / 5, NSEG, (b - NCOL) % 5, out, nullptr);
  }
}

constexpr unsigned SPIN_CAP = 1u << 16;
typedef unsigned v8u __attribute__((ext_vector_type(8)));

#define VM_LD128(dst, off, so, IMM) asm volatile("buffer_load_dwordx4 %0, %1, %2, %3 offen offset:" #IMM " sc1" : "=v"(dst) : "v"(off), "s"(rs), "s"(so) : "memory")
#define VM_LD32(dst, off, so) asm volatile("buffer_load_dword %0, %1, %2, %3 offen sc1" : "=v"(dst) : "v"(off), "s"(rs), "s"(so) : "memory")
#define VM_ST32(val, off, so) asm volatile("buffer_store_dword %0, %1, %2, %3 offen sc1" ::"v"(val), "v"(off), "s"(rs), "s"(so) : "memory")
#define VM_ST128(val, off, so) asm volatile("buffer_store_dwordx4 %0, %1, %2, %3 offen sc1\n\ts_nop 1" ::"v"(val), "v"(off), "s"(rs), "s"(so) : "memory")
#define VM_WAIT(N) asm volatile("s_waitcnt vmcnt(" #N ")" ::: "memory")
#define VM_WAIT1(N, a) asm volatile("s_waitcnt vmcnt(" #N ")" : "+v"(a)::"memory")
__device__ __forceinline__ v4u make_rs(const void* p, unsigned bytes) {
  const unsigned long long a = (unsigned long long)p;
  v4u r;
  r.x = (unsigned)__builtin_amdgcn_readfirstlane((unsigned)a);
  r.y = (unsigned)__builtin_amdgcn_readfirstlane((unsigned)(a >> 32) & 0xFFFFu);
  r.z = bytes;
  r.w = 0x00020000u;
  return r;
}
__device__ __forceinline__ unsigned rtc() { return (unsigned)__builtin_amdgcn_s_memrealtime(); }
struct Clk0 { unsigned r, c; };
__device__ __forceinline__ Clk0 clk_sample() {
  unsigned long long r, c;
  asm volatile("s_memrealtime %0\n\ts_memtime %1\n\ts_waitcnt lgkmcnt(0)" : "=s"(r), "=s"(c)::"memory");
  return Clk0{(unsigned)r, (unsigned)c};
}
__device__ __forceinline__ v4u clk_init(Clk0 a) {
  const Clk0 b = clk_sample();
  unsigned k = 1400u;
  if (b.r - a.r >= 100u) k = (unsigned)((float)(b.r - a.r) * 32768.f / (float)(b.c - a.c));
  k = (unsigned)__builtin_amdgcn_readfirstlane((int)min(max(k, 1200u), 3000u));
  asm volatile("" : "+s"(k));
  return v4u{b.r * 8u + 16u, b.c, k, 0u};
}
#define LDSBAR() asm volatile("s_waitcnt lgkmcnt(0)\n\ts_barrier" ::: "memory")
#define MEMPIN() asm volatile("" ::: "memory")
#define LDS_V4(a) (*(__attribute__((address_space(3))) v4u*)(uintptr_t)(a))
#define LDS_U32(a) (*(__attribute__((address_space(3))) unsigned*)(uintptr_t)(a))
#define LDS_V2(a) (*(__attribute__((address_space(3))) v2u*)(uintptr_t)(a))

__device__ __forceinline__ int wave_imax(int v) {
  asm volatile("s_nop 1\n\tv_max_i32_dpp %0, %0, %0 quad_perm:[1,0,3,2] row_mask:0xf bank_mask:0xf\n\t"
               "s_nop 1\n\tv_max_i32_dpp %0, %0, %0 quad_perm:[2,3,0,1] row_mask:0xf bank_mask:0xf\n\t"
               "s_nop 1\n\tv_max_i32_dpp %0, %0, %0 row_half_mirror row_mask:0xf bank_mask:0xf\n\t"
               "s_nop 1\n\tv_max_i32_dpp %0, %0, %0 row_mirror row_mask:0xf bank_mask:0xf\n\ts_nop 0"
               : "+v"(v));
  const int a = __builtin_amdgcn_readlane(v, 0), b = __builtin_amdgcn_readlane(v, 16), c = __builtin_amdgcn_readlane(v, 32),
            d = __builtin_amdgcn_readlane(v, 48);
  return max(max(a, b), max(c, d));
}

constexpr int OOB = 0x7FFFFFF0;
__device__ __forceinline__ int ladder(int x, int i) { return copy_off(((x + (i >> 1)) & 3) | ((i & 1) << 2)); }

struct RecTune { int m1, m2, dl1, rob, c2, vis, dlt, du; };

#define STAT(r)
__device__ __forceinline__ unsigned max4(v4u v) { return max(max(v.x, v.y), max(v.z, v.w)); }
template <int NAP = 6>
__device__ __forceinline__ unsigned flag_finish(const v4u rs, int off, unsigned cv) {
  for (unsigned spins = 0; __any(cv == 0xFFFFFFFFu) && spins < SPIN_CAP; ++spins) {
    __builtin_amdgcn_s_sleep(NAP);
    v4u t;
    VM_LD128(t, off, 0, 0);
    VM_WAIT1(0, t);
    cv = max4(t);
  }
  return cv;
}
struct Pool {
  unsigned long long sel, s3; int base;
  __device__ __forceinline__ int near(int s) const { return (int)((s3 >> s) & 1ull) * 16384; }
  __device__ __forceinline__ int region(int s) const { return base + s * REG + (int)((sel >> s) & 1ull) * 8192; }
};
__device__ __forceinline__ Pool pool_init(unsigned char* ws) {
  Pool p;
  const unsigned v = pool_sel(ws, lane_id());
  p.sel = __ballot((v & 1u) != 0u);
  p.s3 = __ballot((v & 2u) != 0u);
  p.base = __builtin_amdgcn_readfirstlane(pool_base(ws));
  return p;
}
constexpr int L_QT = 10240, L_KT = 12288, L_CL = 14336, L_KQ = 16384;
constexpr int L_Y = 10240, L_T = 14336, L_SD = 16384;
constexpr int L_M2 = L_KQ + 65536;
constexpr int REC_ST = L_M2 + 12288;
constexpr int REC_LDS = REC_ST + 4096 + 64;

#define RSTAMP(i)
#define RST_CLOB
#define P1TRACE
#define FINTRACE
#define P1_ASM \
   "s_mov_b32 s64, -1\n\t" "s_mov_b32 s76, 0\n\t" "s_mov_b32 s77, 0\n\t" "s_mov_b32 s71, 63\n\t" "s_mov_b32 s47, 1300\n\t" "s_mov_b32 s49, 0\n\t" \
   "s_mov_b32 s51, 0\n\t" "s_mov_b32 s52, 0\n\t" "s_mov_b32 s70, 0\n\t" "s_mov_b32 s69, 0\n\t" "v_mov_b32 v15, 0\n\t" \
   "v_cmp_gt_i32 s[20:21], 0, v126\n\t" "v_cmp_gt_i32 s[22:23], 1, v126\n\t" "v_cmp_gt_i32 s[24:25], 2, v126\n\t" "v_cmp_gt_i32 s[26:27], 3, v126\n\t" \
   "v_cmp_gt_i32 s[28:29], 16, v126\n\t" "v_cmp_gt_i32 s[30:31], 17, v126\n\t" "v_cmp_gt_i32 s[32:33], 18, v126\n\t" \
   "v_cmp_gt_i32 s[34:35], 19, v126\n\t" "s_waitcnt vmcnt(0)\n\t" "s_lshr_b64 s[72:73], %[sel], s71\n\t" "s_lshl_b32 s65, s71, 19\n\t" \
   "s_and_b32 s72, s72, 1\n\t" "s_add_u32 s65, s65, %[pool]\n\t" "s_lshl_b32 s72, s72, 13\n\t" "s_add_u32 s65, s65, s72\n\t" \
   "s_lshr_b64 s[72:73], %[s3], s71\n\t" "s_and_b32 s54, s72, 1\n\t" "s_lshl_b32 s54, s54, 14\n\t" "s_memtime s[62:63]\n\t" \
   "s_memrealtime s[60:61]\n\t" "40:\n\t" "s_cmp_eq_u32 s64, 0\n\t" "s_cbranch_scc1 95f\n\t" "96:\n\t" "s_mov_b32 m0, %[lfs]\n\t" \
   "s_cmp_gt_u32 %[wv], 1\n\t" "s_waitcnt vmcnt(3)\n\t" "s_cbranch_scc1 44f\n\t" "ds_write_b128 v127, v[16:19] offset:0\n\t" \
   "ds_write_b128 v127, v[20:23] offset:1024\n\t" "ds_write_b128 v127, v[24:27] offset:2048\n\t" "ds_write_b128 v127, v[28:31] offset:3072\n\t" \
   "s_waitcnt lgkmcnt(0)\n\t" "s_sub_u32 s74, s62, s57\n\t" "s_mul_i32 s74, s74, s58\n\t" "s_lshr_b32 s74, s74, 12\n\t" "s_add_u32 s74, s74, s56\n\t" \
   "s_lshl_b32 s60, s60, 3\n\t" "s_sub_u32 s75, s74, s60\n\t" "s_cmp_lt_u32 s75, 32\n\t" "s_cbranch_scc1 38f\n\t" "s_cmp_lt_i32 s75, 0\n\t" \
   "s_cselect_b32 s61, 0, 31\n\t" "s_add_u32 s74, s60, s61\n\t" "s_sub_i32 s75, s61, s75\n\t" "s_add_i32 s75, s75, 1\n\t" "s_ashr_i32 s75, s75, 1\n\t" \
   "s_min_i32 s75, s75, 8\n\t" "s_max_i32 s75, s75, -8\n\t" "s_mov_b32 s56, s74\n\t" "s_mov_b32 s57, s62\n\t" "s_add_u32 s58, s58, s75\n\t" "38:\n\t" \
   "s_min_u32 s72, s70, 1\n\t" "s_and_b32 s71, s74, -4\n\t" "s_or_b32 s71, s71, s72\n\t" "v_mov_b32 v111, s71\n\t" \
   "buffer_store_dword v111, v131, %[rs], s65 offen sc1\n\t" P1TRACE "s_barrier\n\t" "ds_read_b128 v[160:163], v122 offset:0\n\t" \
   "ds_read_b128 v[164:167], v122 offset:1024\n\t" "ds_read_b128 v[168:171], v122 offset:2048\n\t" "ds_read_b128 v[172:175], v122 offset:3072\n\t" \
   "ds_read_b128 v[176:179], v122 offset:4096\n\t" "ds_read_b128 v[180:183], v122 offset:5120\n\t" "ds_read_b128 v[184:187], v122 offset:6144\n\t" \
   "ds_read_b128 v[188:191], v122 offset:7168\n\t" "buffer_load_dword v139, %[rs], s65 offen sc1 lds\n\t" "s_cmp_lt_i32 s64, 1\n\t" \
   "s_cselect_b32 s72, 0x7ffffff0, 0\n\t" "s_max_i32 s71, s64, 1\n\t" "v_or_b32 v109, s72, v143\n\t" "s_mul_i32 s71, s71, 7168\n\t" \
   "v_mov_b32 v110, 1\n\t" "s_sub_u32 s71, s71, 7168\n\t" "buffer_store_dword v110, v109, %[rs], s71 offen sc1\n\t" "v_cmp_eq_u32 vcc, -1, v15\n\t" \
   "s_add_i32 s72, s64, 2\n\t" "s_add_i32 s71, s64, 3\n\t" "s_min_i32 s71, s71, 63\n\t" "s_lshl_b32 s59, s71, 12\n\t" "s_cbranch_vccnz 141f\n\t" \
   "41:\n\t" "s_min_i32 s72, s72, 63\n\t" "s_lshl_b32 s79, s72, 13\n\t" "s_add_u32 s48, s69, s47\n\t" "s_cmp_eq_u32 s70, 0\n\t" \
   "s_cselect_b32 s48, s48, -1\n\t" "s_mov_b32 s66, s65\n\t" "s_add_i32 s64, s64, 1\n\t" "s_and_b32 s71, s64, 63\n\t" \
   "s_lshr_b64 s[72:73], %[sel], s71\n\t" "s_lshl_b32 s65, s71, 19\n\t" "s_and_b32 s72, s72, 1\n\t" "s_add_u32 s65, s65, %[pool]\n\t" \
   "s_lshl_b32 s72, s72, 13\n\t" "s_add_u32 s65, s65, s72\n\t" "s_xor_b32 s67, s54, %[lad0]\n\t" "s_xor_b32 s68, s67, 0x4000\n\t" \
   "s_add_u32 s67, s67, s66\n\t" "s_add_u32 s68, s68, s66\n\t" "s_lshr_b64 s[72:73], %[s3], s71\n\t" "s_and_b32 s54, s72, 1\n\t" \
   "s_lshl_b32 s54, s54, 14\n\t" "s_cmp_eq_u32 s64, 0\n\t" "s_cselect_b32 s70, 1, 0\n\t" "s_and_b32 s55, %[wv], 1\n\t" "s_lshl_b32 s55, s55, 14\n\t" \
   "s_xor_b32 s55, s55, s54\n\t" "s_xor_b32 s50, s55, 0x4000\n\t" "s_add_u32 s55, s55, s65\n\t" "s_add_u32 s50, s50, s65\n\t" "s_waitcnt vmcnt(1)\n\t" \
   "s_branch 48f\n\t" "44:\n\t" "v_mfma_f32_16x16x32_bf16 v[32:35], v[160:163], v[16:19], 0\n\t" "s_max_i32 s51, s64, 0\n\t" \
   "s_lshl_b32 s52, s51, 16\n\t" "s_lshl_b32 s51, s51, 14\n\t" "v_mfma_f32_16x16x32_bf16 v[36:39], v[176:179], v[16:19], 0\n\t" \
   "s_cmp_lt_i32 s64, 0\n\t" "s_cselect_b32 s49, 0x7ffffff0, 0\n\t" "s_add_i32 s72, s64, 1\n\t" \
   "v_mfma_f32_16x16x32_bf16 v[32:35], v[164:167], v[20:23], v[32:35]\n\t" "s_min_i32 s72, s72, 63\n\t" "s_lshl_b32 s79, s72, 13\n\t" \
   "s_mov_b32 s59, 0\n\t" "v_mfma_f32_16x16x32_bf16 v[36:39], v[180:183], v[20:23], v[36:39]\n\t" \
   "v_mfma_f32_16x16x32_bf16 v[32:35], v[168:171], v[24:27], v[32:35]\n\t" "s_waitcnt lgkmcnt(0)\n\t" \
   "v_mfma_f32_16x16x32_bf16 v[36:39], v[184:187], v[24:27], v[36:39]\n\t" "s_sub_u32 s74, s62, s57\n\t" "s_mul_i32 s74, s74, s58\n\t" \
   "s_lshr_b32 s74, s74, 12\n\t" "v_mfma_f32_16x16x32_bf16 v[32:35], v[172:175], v[28:31], v[32:35]\n\t" "s_add_u32 s74, s74, s56\n\t" \
   "s_lshl_b32 s60, s60, 3\n\t" "s_sub_u32 s75, s74, s60\n\t" "v_mfma_f32_16x16x32_bf16 v[36:39], v[188:191], v[28:31], v[36:39]\n\t" \
   "s_cmp_lt_u32 s75, 32\n\t" "s_cbranch_scc1 39f\n\t" "s_cmp_lt_i32 s75, 0\n\t" "s_cselect_b32 s61, 0, 31\n\t" "s_add_u32 s74, s60, s61\n\t" \
   "s_sub_i32 s75, s61, s75\n\t" "s_add_i32 s75, s75, 1\n\t" "s_ashr_i32 s75, s75, 1\n\t" "s_min_i32 s75, s75, 8\n\t" "s_max_i32 s75, s75, -8\n\t" \
   "s_mov_b32 s56, s74\n\t" "s_mov_b32 s57, s62\n\t" "s_add_u32 s58, s58, s75\n\t" "39:\n\t" "s_barrier\n\t" \
   "buffer_load_dword v139, %[rs], s65 offen sc1 lds\n\t" "v_mfma_f32_16x16x32_bf16 v[40:43], v[144:147], v[16:19], 0\n\t" "s_min_u32 s72, s70, 1\n\t" \
   "s_and_b32 s71, s74, -4\n\t" "v_mfma_f32_16x16x32_bf16 v[40:43], v[148:151], v[20:23], v[40:43]\n\t" "s_or_b32 s71, s71, s72\n\t" \
   "v_mov_b32 v111, s71\n\t" "v_mfma_f32_16x16x32_bf16 v[40:43], v[152:155], v[24:27], v[40:43]\n\t" \
   "buffer_store_dword v111, v131, %[rs], s65 offen sc1\n\t" P1TRACE "v_mfma_f32_16x16x32_bf16 v[40:43], v[156:159], v[28:31], v[40:43]\n\t" \
   "v_cndmask_b32 v32, v32, v125, s[20:21]\n\t" "v_cndmask_b32 v33, v33, v125, s[22:23]\n\t" "v_cndmask_b32 v34, v34, v125, s[24:25]\n\t" \
   "v_cndmask_b32 v35, v35, v125, s[26:27]\n\t" "v_cndmask_b32 v36, v36, v125, s[28:29]\n\t" "v_cndmask_b32 v37, v37, v125, s[30:31]\n\t" \
   "v_cndmask_b32 v38, v38, v125, s[32:33]\n\t" "v_cndmask_b32 v39, v39, v125, s[34:35]\n\t" "v_max3_f32 v80, v32, v33, v34\n\t" \
   "v_max3_f32 v81, v35, v36, v37\n\t" "v_max3_f32 v82, v38, v39, v40\n\t" "v_max3_f32 v83, v41, v42, v43\n\t" "v_max3_f32 v80, v80, v81, v82\n\t" \
   "v_max_f32 v81, v80, v83\n\t" "v_max_f32 v80, v80, v83\n\t" "ds_read_b128 v[160:163], v122 offset:0\n\t" \
   "ds_read_b128 v[164:167], v122 offset:1024\n\t" "v_permlane16_swap_b32 v80, v81\n\t" "v_max_f32 v82, v80, v81\n\t" "v_max_f32 v80, v80, v81\n\t" \
   "ds_read_b128 v[168:171], v122 offset:2048\n\t" "ds_read_b128 v[172:175], v122 offset:3072\n\t" "v_permlane32_swap_b32 v80, v82\n\t" \
   "v_max_f32 v80, v80, v82\n\t" "v_mul_f32 v80, 0xbe0293ee, v80\n\t" "v_fmamk_f32 v32, v32, 0x3e0293ee, v80\n\t" \
   "v_fmamk_f32 v33, v33, 0x3e0293ee, v80\n\t" "v_fmamk_f32 v34, v34, 0x3e0293ee, v80\n\t" "v_fmamk_f32 v35, v35, 0x3e0293ee, v80\n\t" \
   "v_fmamk_f32 v36, v36, 0x3e0293ee, v80\n\t" "v_fmamk_f32 v37, v37, 0x3e0293ee, v80\n\t" "v_fmamk_f32 v38, v38, 0x3e0293ee, v80\n\t" \
   "v_fmamk_f32 v39, v39, 0x3e0293ee, v80\n\t" "v_fmamk_f32 v40, v40, 0x3e0293ee, v80\n\t" "v_fmamk_f32 v41, v41, 0x3e0293ee, v80\n\t" \
   "v_fmamk_f32 v42, v42, 0x3e0293ee, v80\n\t" "v_fmamk_f32 v43, v43, 0x3e0293ee, v80\n\t" "v_exp_f32 v32, v32\n\t" "s_add_u32 s48, s69, s47\n\t" \
   "s_cmp_eq_u32 s70, 0\n\t" "v_exp_f32 v33, v33\n\t" "s_cselect_b32 s48, s48, -1\n\t" "s_mov_b32 s66, s65\n\t" "v_exp_f32 v34, v34\n\t" \
   "s_add_i32 s64, s64, 1\n\t" "s_and_b32 s71, s64, 63\n\t" "v_exp_f32 v35, v35\n\t" "s_lshr_b64 s[72:73], %[sel], s71\n\t" \
   "s_lshl_b32 s65, s71, 19\n\t" "v_exp_f32 v36, v36\n\t" "s_and_b32 s72, s72, 1\n\t" "s_add_u32 s65, s65, %[pool]\n\t" "v_exp_f32 v37, v37\n\t" \
   "s_lshl_b32 s72, s72, 13\n\t" "s_add_u32 s65, s65, s72\n\t" "v_exp_f32 v38, v38\n\t" "s_xor_b32 s67, s54, %[lad0]\n\t" \
   "s_xor_b32 s68, s67, 0x4000\n\t" "v_exp_f32 v39, v39\n\t" "s_add_u32 s67, s67, s66\n\t" "s_add_u32 s68, s68, s66\n\t" "v_exp_f32 v40, v40\n\t" \
   "s_lshr_b64 s[72:73], %[s3], s71\n\t" "s_and_b32 s54, s72, 1\n\t" "v_exp_f32 v41, v41\n\t" "s_lshl_b32 s54, s54, 14\n\t" "s_cmp_eq_u32 s64, 0\n\t" \
   "v_exp_f32 v42, v42\n\t" "s_cselect_b32 s70, 1, 0\n\t" "s_and_b32 s55, %[wv], 1\n\t" "v_exp_f32 v43, v43\n\t" "s_lshl_b32 s55, s55, 14\n\t" \
   "s_xor_b32 s55, s55, s54\n\t" "s_xor_b32 s50, s55, 0x4000\n\t" "s_add_u32 s55, s55, s65\n\t" "s_add_u32 s50, s50, s65\n\t" \
   "v_add_f32 v80, v32, v33\n\t" "v_add_f32 v81, v34, v35\n\t" "v_add_f32 v82, v36, v37\n\t" "v_add_f32 v83, v38, v39\n\t" \
   "v_add_f32 v84, v40, v41\n\t" "v_add_f32 v85, v42, v43\n\t" "v_add_f32 v80, v80, v81\n\t" "v_add_f32 v82, v82, v83\n\t" \
   "v_add_f32 v84, v84, v85\n\t" "v_add_f32 v80, v80, v82\n\t" "v_fma_f32 v82, v80, 2.0, v84\n\t" "v_fma_f32 v80, v80, 2.0, v84\n\t" \
   "ds_read_b128 v[176:179], v122 offset:4096\n\t" "ds_read_b128 v[180:183], v122 offset:5120\n\t" "v_permlane16_swap_b32 v80, v82\n\t" \
   "v_add_f32 v81, v80, v82\n\t" "v_add_f32 v80, v80, v82\n\t" "ds_read_b128 v[184:187], v122 offset:6144\n\t" \
   "ds_read_b128 v[188:191], v122 offset:7168\n\t" "v_permlane32_swap_b32 v80, v81\n\t" "v_add_f32 v80, v80, v81\n\t" "v_rcp_f32 v90, v80\n\t" \
   "s_nop 0\n\t" "v_add_f32 v90, v90, v90\n\t" "v_pk_mul_f32 v[32:33], v[32:33], v[90:91] op_sel_hi:[1,0]\n\t" \
   "v_pk_mul_f32 v[34:35], v[34:35], v[90:91] op_sel_hi:[1,0]\n\t" "v_pk_mul_f32 v[36:37], v[36:37], v[90:91] op_sel_hi:[1,0]\n\t" \
   "v_pk_mul_f32 v[38:39], v[38:39], v[90:91] op_sel_hi:[1,0]\n\t" "v_pk_mul_f32 v[40:41], v[40:41], v[90:91] op_sel_hi:[1,0]\n\t" \
   "v_pk_mul_f32 v[42:43], v[42:43], v[90:91] op_sel_hi:[1,0]\n\t" "v_cvt_pk_bf16_f32 v44, v32, v33\n\t" "v_cvt_pk_bf16_f32 v45, v34, v35\n\t" \
   "v_cvt_pk_bf16_f32 v46, v36, v37\n\t" "v_cvt_pk_bf16_f32 v47, v38, v39\n\t" "v_cvt_pk_bf16_f32 v32, v40, v41\n\t" \
   "v_cvt_pk_bf16_f32 v33, v42, v43\n\t" "s_waitcnt vmcnt(1)\n\t" "48:\n\t" "ds_read_b64 v[108:109], v123\n\t" "v_mov_b32 v48, 0\n\t" \
   "v_mov_b32 v49, 0\n\t" "v_mov_b32 v50, 0\n\t" "v_mov_b32 v51, 0\n\t" "v_mov_b32 v52, 0\n\t" "v_mov_b32 v53, 0\n\t" "v_mov_b32 v54, 0\n\t" \
   "v_mov_b32 v55, 0\n\t" "v_mov_b32 v56, 0\n\t" "v_mov_b32 v57, 0\n\t" "v_mov_b32 v58, 0\n\t" "v_mov_b32 v59, 0\n\t" "v_mov_b32 v60, 0\n\t" \
   "v_mov_b32 v61, 0\n\t" "v_mov_b32 v62, 0\n\t" "v_mov_b32 v63, 0\n\t" "s_cmp_eq_u32 s64, 64\n\t" "s_cbranch_scc1 90f\n\t" "s_waitcnt lgkmcnt(0)\n\t" \
   "v_readfirstlane_b32 s69, v108\n\t" "v_readfirstlane_b32 s71, v109\n\t" "s_cmp_eq_u32 s69, -1\n\t" "s_cbranch_scc1 63f\n\t" \
   "s_cmp_eq_u32 s71, -1\n\t" "s_cselect_b32 s47, s47, s71\n\t" "50:\n\t" "s_add_u32 s73, s69, %[m1]\n\t" RSTAMP(5) "51:\n\t" \
   "s_memrealtime s[60:61]\n\t" "s_memtime s[62:63]\n\t" "s_waitcnt lgkmcnt(0)\n\t" "s_sub_u32 s74, s62, s57\n\t" "s_mul_i32 s74, s74, s58\n\t" \
   "s_lshr_b32 s74, s74, 12\n\t" "s_add_u32 s74, s74, s56\n\t" "s_lshl_b32 s60, s60, 3\n\t" "s_sub_u32 s75, s74, s60\n\t" "s_cmp_lt_u32 s75, 32\n\t" \
   "s_cbranch_scc1 52f\n\t" "s_cmp_lt_i32 s75, 0\n\t" "s_cselect_b32 s61, 0, 31\n\t" "s_add_u32 s74, s60, s61\n\t" "s_sub_i32 s75, s61, s75\n\t" \
   "s_add_i32 s75, s75, 1\n\t" "s_ashr_i32 s75, s75, 1\n\t" "s_min_i32 s75, s75, 8\n\t" "s_max_i32 s75, s75, -8\n\t" "s_mov_b32 s56, s74\n\t" \
   "s_mov_b32 s57, s62\n\t" "s_add_u32 s58, s58, s75\n\t" "52:\n\t" "s_sub_i32 s74, s73, s74\n\t" "s_cmp_le_i32 s74, 150\n\t" "s_cbranch_scc1 53f\n\t" \
   "s_cmp_lt_i32 s74, 260\n\t" "s_cbranch_scc1 51b\n\t" "s_sleep 2\n\t" "s_cmp_lt_i32 s74, 32000\n\t" "s_cbranch_scc1 51b\n\t" "s_mov_b32 s74, 0\n\t" \
   "53:\n\t" "s_sub_i32 s74, s74, 56\n\t" "s_max_i32 s74, s74, 0\n\t" "s_mul_i32 s74, s74, 23\n\t" "s_lshr_b32 s74, s74, 5\n\t" \
   "s_and_b32 s74, s74, -4\n\t" "s_getpc_b64 s[60:61]\n\t" "54:\n\t" "s_sub_u32 s74, 55f-54b, s74\n\t" "s_add_u32 s60, s60, s74\n\t" \
   "s_addc_u32 s61, s61, 0\n\t" "s_setpc_b64 s[60:61]\n\t" "s_nop 3\n\t" "s_nop 3\n\t" "s_nop 3\n\t" "s_nop 3\n\t" "s_nop 3\n\t" "s_nop 3\n\t" \
   "s_nop 3\n\t" "s_nop 3\n\t" "s_nop 3\n\t" "s_nop 3\n\t" "s_nop 3\n\t" "s_nop 3\n\t" "s_nop 3\n\t" "s_nop 3\n\t" "s_nop 3\n\t" "s_nop 3\n\t" \
   "s_nop 3\n\t" "s_nop 3\n\t" "s_nop 3\n\t" "s_nop 3\n\t" "s_nop 3\n\t" "s_nop 3\n\t" "s_nop 3\n\t" "s_nop 3\n\t" "55:\n\t" "20:\n\t" RSTAMP(4) \
   "buffer_load_dwordx4 v[64:67], v128, %[rs], s67 offen sc1\n\t" "buffer_load_dwordx4 v[68:71], v129, %[rs], s67 offen sc1\n\t" \
   "buffer_load_dwordx4 v[72:75], v130, %[rs], s67 offen sc1\n\t" "s_nop 7\n\t" "s_nop 7\n\t" "s_nop 7\n\t" "s_cmp_lg_u32 s70, 0\n\t" \
   "s_cbranch_scc1 27f\n\t" "buffer_load_dwordx4 v[16:19], v138, %[rs], s79 offen offset:0 sc1\n\t" \
   "buffer_load_dwordx4 v[20:23], v138, %[rs], s79 offen offset:1024 sc1\n\t" \
   "buffer_load_dwordx4 v[24:27], v138, %[rs], s79 offen offset:2048 sc1\n\t" \
   "buffer_load_dwordx4 v[28:31], v138, %[rs], s79 offen offset:3072 sc1\n\t" "buffer_load_dword v15, v140, %[rs], s59 offen sc1\n\t" \
   "v_or_b32 v108, s49, v141\n\t" "v_or_b32 v109, s49, v142\n\t" "buffer_store_dwordx4 v[44:47], v108, %[rs], s52 offen sc1\n\t" \
   "buffer_store_dwordx2 v[32:33], v109, %[rs], s51 offen sc1\n\t" "28:\n\t" RSTAMP(3) "s_waitcnt vmcnt(9)\n\t" "ds_write_b128 v112, v[64:67]\n\t" \
   "v_smfmac_f32_16x16x64_bf16 v[48:51], v[64:67], a[0:7], v134\n\t" "s_waitcnt vmcnt(8)\n\t" "ds_write_b128 v113, v[68:71]\n\t" \
   "s_waitcnt vmcnt(7)\n\t" "ds_write_b128 v114, v[72:75]\n\t" RSTAMP(7) "2:\n\t" "v_smfmac_f32_16x16x64_bf16 v[52:55], v[64:67], a[64:71], v134\n\t" \
   "v_max3_u32 v110, v64, v66, v68\n\t" "v_max3_u32 v111, v70, v72, v74\n\t" "v_smfmac_f32_16x16x64_bf16 v[48:51], v[68:71], a[8:15], v134\n\t" \
   "v_max_u32 v110, v110, v111\n\t" "v_cmp_eq_u32 vcc, -1, v110\n\t" "v_smfmac_f32_16x16x64_bf16 v[52:55], v[68:71], a[72:79], v134\n\t" \
   "s_cbranch_vccnz 8f\n\t" "v_smfmac_f32_16x16x64_bf16 v[48:51], v[72:75], a[16:23], v134\n\t" "s_waitcnt lgkmcnt(0)\n\t" "s_barrier\n\t" RSTAMP(0) \
   "ds_read_b128 v[76:79], v115\n\t" "ds_read_b128 v[80:83], v116\n\t" "v_smfmac_f32_16x16x64_bf16 v[52:55], v[72:75], a[80:87], v134\n\t" \
   "ds_read_b128 v[84:87], v117\n\t" "ds_read_b128 v[88:91], v118\n\t" "v_smfmac_f32_16x16x64_bf16 v[56:59], v[64:67], a[128:135], v134\n\t" \
   "ds_read_b128 v[92:95], v119\n\t" "ds_read_b128 v[96:99], v120\n\t" "v_smfmac_f32_16x16x64_bf16 v[60:63], v[64:67], a[192:199], v134\n\t" \
   "ds_read_b128 v[100:103], v121\n\t" "v_smfmac_f32_16x16x64_bf16 v[56:59], v[68:71], a[136:143], v134\n\t" \
   "v_smfmac_f32_16x16x64_bf16 v[60:63], v[68:71], a[200:207], v134\n\t" "s_waitcnt lgkmcnt(6)\n\t" \
   "v_smfmac_f32_16x16x64_bf16 v[48:51], v[76:79], a[24:31], v134\n\t" "v_smfmac_f32_16x16x64_bf16 v[52:55], v[76:79], a[88:95], v134\n\t" \
   "s_waitcnt lgkmcnt(5)\n\t" "v_smfmac_f32_16x16x64_bf16 v[48:51], v[80:83], a[32:39], v134\n\t" \
   "v_smfmac_f32_16x16x64_bf16 v[52:55], v[80:83], a[96:103], v134\n\t" "s_waitcnt lgkmcnt(4)\n\t" \
   "v_smfmac_f32_16x16x64_bf16 v[48:51], v[84:87], a[40:47], v134\n\t" "v_smfmac_f32_16x16x64_bf16 v[52:55], v[84:87], a[104:111], v134\n\t" \
   "s_waitcnt lgkmcnt(3)\n\t" "v_smfmac_f32_16x16x64_bf16 v[48:51], v[88:91], a[48:55], v134\n\t" \
   "v_smfmac_f32_16x16x64_bf16 v[52:55], v[88:91], a[112:119], v134\n\t" "s_waitcnt lgkmcnt(2)\n\t" \
   "v_smfmac_f32_16x16x64_bf16 v[48:51], v[92:95], a[56:63], v134\n\t" "v_smfmac_f32_16x16x64_bf16 v[52:55], v[92:95], a[120:127], v134\n\t" \
   "s_waitcnt lgkmcnt(0)\n\t" "v_smfmac_f32_16x16x64_bf16 v[48:51], v[96:99], v[192:199], v134\n\t" \
   "v_smfmac_f32_16x16x64_bf16 v[48:51], v[100:103], v[200:207], v134\n\t" "v_smfmac_f32_16x16x64_bf16 v[52:55], v[96:99], v[208:215], v134\n\t" \
   "v_smfmac_f32_16x16x64_bf16 v[52:55], v[100:103], v[216:223], v134\n\t" "v_smfmac_f32_16x16x64_bf16 v[56:59], v[72:75], a[144:151], v134\n\t" \
   "v_permlane32_swap_b32 v48, v50\n\t" "v_permlane32_swap_b32 v49, v51\n\t" "v_smfmac_f32_16x16x64_bf16 v[56:59], v[76:79], a[152:159], v134\n\t" \
   "v_add_f32 v48, v48, v50\n\t" "v_add_f32 v49, v49, v51\n\t" "v_mul_f32 v48, 0x3e0293ee, v48\n\t" "v_mul_f32 v49, 0x3e0293ee, v49\n\t" \
   "v_smfmac_f32_16x16x64_bf16 v[56:59], v[80:83], a[160:167], v134\n\t" "v_permlane32_swap_b32 v52, v54\n\t" "v_permlane32_swap_b32 v53, v55\n\t" \
   "v_smfmac_f32_16x16x64_bf16 v[56:59], v[84:87], a[168:175], v134\n\t" "v_add_f32 v52, v52, v54\n\t" "v_add_f32 v53, v53, v55\n\t" \
   "v_mul_f32 v52, 0x3e0293ee, v52\n\t" "v_mul_f32 v53, 0x3e0293ee, v53\n\t" "v_smfmac_f32_16x16x64_bf16 v[60:63], v[72:75], a[208:215], v134\n\t" \
   "v_cvt_pk_bf16_f32 v52, v52, v53\n\t" "ds_write_b32 v135, v52 offset:10496\n\t" "v_cvt_pk_bf16_f32 v48, v48, v49\n\t" \
   "ds_write_b32 v135, v48 offset:10240\n\t" "v_smfmac_f32_16x16x64_bf16 v[56:59], v[88:91], a[176:183], v134\n\t" \
   "v_smfmac_f32_16x16x64_bf16 v[56:59], v[92:95], a[184:191], v134\n\t" "v_smfmac_f32_16x16x64_bf16 v[56:59], v[96:99], v[224:231], v134\n\t" \
   "s_waitcnt lgkmcnt(0)\n\t" "s_barrier\n\t" "ds_read_b64_tr_b16 v[64:65], v136 offset:10240\n\t" "ds_read_b64_tr_b16 v[66:67], v136 offset:10496\n\t" \
   "v_smfmac_f32_16x16x64_bf16 v[56:59], v[100:103], v[232:239], v134\n\t" "ds_read_b64_tr_b16 v[68:69], v136 offset:10752\n\t" \
   "v_smfmac_f32_16x16x64_bf16 v[60:63], v[76:79], a[216:223], v134\n\t" "ds_read_b64_tr_b16 v[70:71], v136 offset:11008\n\t" \
   "v_smfmac_f32_16x16x64_bf16 v[60:63], v[80:83], a[224:231], v134\n\t" "ds_read_b64_tr_b16 v[72:73], v136 offset:11264\n\t" \
   "v_smfmac_f32_16x16x64_bf16 v[60:63], v[84:87], a[232:239], v134\n\t" "ds_read_b64_tr_b16 v[74:75], v136 offset:11520\n\t" \
   "v_smfmac_f32_16x16x64_bf16 v[60:63], v[88:91], a[240:247], v134\n\t" "ds_read_b64_tr_b16 v[104:105], v136 offset:11776\n\t" \
   "ds_read_b64_tr_b16 v[106:107], v136 offset:12032\n\t" "v_smfmac_f32_16x16x64_bf16 v[60:63], v[92:95], a[248:255], v134\n\t" \
   "v_permlane32_swap_b32 v56, v58\n\t" "v_permlane32_swap_b32 v57, v59\n\t" "v_add_f32 v56, v56, v58\n\t" "v_add_f32 v57, v57, v59\n\t" \
   "v_smfmac_f32_16x16x64_bf16 v[60:63], v[96:99], v[240:247], v134\n\t" "v_cvt_pk_bf16_f32 v56, v56, v57\n\t" \
   "ds_write_b32 v135, v56 offset:12288\n\t" "v_smfmac_f32_16x16x64_bf16 v[60:63], v[100:103], v[248:255], v134\n\t" RSTAMP(6) \
   "s_waitcnt lgkmcnt(7)\n\t" "v_mfma_f32_16x16x32_bf16 v[48:51], v[160:163], v[64:67], 0\n\t" \
   "v_mfma_f32_16x16x32_bf16 v[52:55], v[176:179], v[64:67], 0\n\t" "s_waitcnt lgkmcnt(3)\n\t" "s_nop 0\n\t" "v_permlane32_swap_b32 v60, v62\n\t" \
   "v_permlane32_swap_b32 v61, v63\n\t" "v_add_f32 v60, v60, v62\n\t" "v_add_f32 v61, v61, v63\n\t" "v_cvt_pk_bf16_f32 v60, v60, v61\n\t" \
   "ds_write_b32 v135, v60 offset:12544\n\t" "v_mfma_f32_16x16x32_bf16 v[48:51], v[164:167], v[68:71], v[48:51]\n\t" \
   "v_mfma_f32_16x16x32_bf16 v[52:55], v[180:183], v[68:71], v[52:55]\n\t" "v_mfma_f32_16x16x32_bf16 v[48:51], v[168:171], v[72:75], v[48:51]\n\t" \
   "v_mfma_f32_16x16x32_bf16 v[52:55], v[184:187], v[72:75], v[52:55]\n\t" "s_waitcnt lgkmcnt(2)\n\t" \
   "v_mfma_f32_16x16x32_bf16 v[48:51], v[172:175], v[104:107], v[48:51]\n\t" "v_mfma_f32_16x16x32_bf16 v[52:55], v[188:191], v[104:107], v[52:55]\n\t" \
   "s_waitcnt lgkmcnt(0)\n\t" "s_barrier\n\t" "ds_read_b64_tr_b16 v[144:145], v136 offset:12288\n\t" \
   "ds_read_b64_tr_b16 v[146:147], v136 offset:12544\n\t" "ds_read_b64_tr_b16 v[148:149], v136 offset:12800\n\t" \
   "ds_read_b64_tr_b16 v[150:151], v136 offset:13056\n\t" "ds_read_b64_tr_b16 v[152:153], v136 offset:13312\n\t" \
   "ds_read_b64_tr_b16 v[154:155], v136 offset:13568\n\t" "ds_read_b64_tr_b16 v[156:157], v136 offset:13824\n\t" \
   "ds_read_b64_tr_b16 v[158:159], v136 offset:14080\n\t" "s_cmp_eq_u32 s64, 0\n\t" "s_cbranch_scc1 92f\n\t" "93:\n\t" RSTAMP(1) \
   "v_cndmask_b32 v48, v48, v52, %[hi8]\n\t" "v_cndmask_b32 v49, v49, v53, %[hi8]\n\t" "v_cndmask_b32 v50, v50, v54, %[hi8]\n\t" \
   "v_cndmask_b32 v51, v51, v55, %[hi8]\n\t" "v_exp_f32 v80, v48\n\t" "v_exp_f32 v81, v49\n\t" "s_waitcnt lgkmcnt(6)\n\t" \
   "v_mfma_f32_16x16x32_bf16 v[76:79], v[144:147], v[64:67], 0\n\t" "v_exp_f32 v82, v50\n\t" "v_exp_f32 v83, v51\n\t" "s_waitcnt lgkmcnt(4)\n\t" \
   "v_mfma_f32_16x16x32_bf16 v[76:79], v[148:151], v[68:71], v[76:79]\n\t" "v_add_f32 v96, v80, v81\n\t" "v_add_f32 v103, v82, v83\n\t" \
   "s_waitcnt lgkmcnt(2)\n\t" "v_mfma_f32_16x16x32_bf16 v[76:79], v[152:155], v[72:75], v[76:79]\n\t" "v_add_f32 v94, v96, v103\n\t" \
   "v_add_f32 v96, v96, v103\n\t" "s_waitcnt lgkmcnt(0)\n\t" "v_mfma_f32_16x16x32_bf16 v[76:79], v[156:159], v[104:107], v[76:79]\n\t" \
   "v_permlane32_swap_b32 v96, v94\n\t" "v_add_f32 v96, v96, v94\n\t" "s_nop 1\n\t" \
   "v_add_f32_dpp v96, v96, v96 row_ror:8 row_mask:0xf bank_mask:0xf\n\t" "s_nop 2\n\t" "v_exp_f32 v88, v76\n\t" "v_exp_f32 v89, v77\n\t" \
   "v_add_f32 v103, v96, v88\n\t" "v_exp_f32 v90, v78\n\t" "v_add_f32 v103, v103, v89\n\t" "v_exp_f32 v91, v79\n\t" "v_add_f32 v103, v103, v90\n\t" \
   "v_add_f32 v97, v103, v91\n\t" "v_add_f32 v103, v103, v91\n\t" "s_nop 1\n\t" "v_permlane16_swap_b32 v103, v97\n\t" "v_add_f32 v103, v103, v97\n\t" \
   "v_rcp_f32 v100, v103\n\t" "v_add_u32 v94, 0xe0800000, v103\n\t" "v_pk_mul_f32 v[80:81], v[80:81], v[100:101] op_sel_hi:[1,0]\n\t" \
   "v_cmp_lt_u32 vcc, 0x40000000, v94\n\t" "v_pk_mul_f32 v[82:83], v[82:83], v[100:101] op_sel_hi:[1,0]\n\t" "s_cbranch_vccnz 70f\n\t" \
   "v_cvt_pk_bf16_f32 v48, v80, v81\n\t" "v_cvt_pk_bf16_f32 v49, v82, v83\n\t" "buffer_store_dwordx2 v[48:49], v132, %[rs], s55 offen sc1\n\t" \
   "v_pk_mul_f32 v[88:89], v[88:89], v[100:101] op_sel_hi:[1,0]\n\t" "v_pk_mul_f32 v[90:91], v[90:91], v[100:101] op_sel_hi:[1,0]\n\t" \
   "v_cvt_pk_bf16_f32 v52, v88, v89\n\t" "v_cvt_pk_bf16_f32 v53, v90, v91\n\t" "71:\n\t" \
   "buffer_store_dwordx2 v[52:53], v133, %[rs], s65 offen sc1\n\t" "s_memtime s[62:63]\n\t" "s_memrealtime s[60:61]\n\t" \
   "buffer_store_dwordx2 v[48:49], v132, %[rs], s50 offen sc1\n\t" RSTAMP(2) "s_branch 40b\n\t" "70:\n\t" "v_max3_f32 v94, v48, v49, v50\n\t" \
   "v_max3_f32 v102, v76, v77, v78\n\t" "v_max3_f32 v94, v94, v51, v79\n\t" "v_max_f32 v92, v94, v102\n\t" "s_nop 1\n\t" \
   "v_max_f32_dpp v102, v92, v92 row_ror:8 row_mask:0xf bank_mask:0xf\n\t" "v_max_f32_dpp v94, v92, v92 row_ror:8 row_mask:0xf bank_mask:0xf\n\t" \
   "s_nop 1\n\t" "v_permlane32_swap_b32 v102, v94\n\t" "v_max_f32 v95, v102, v94\n\t" "v_max_f32 v102, v102, v94\n\t" "s_nop 1\n\t" \
   "v_permlane16_swap_b32 v102, v95\n\t" "v_max_f32 v102, v102, v95\n\t" "v_sub_f32 v80, v48, v102\n\t" "v_sub_f32 v88, v76, v102\n\t" \
   "v_sub_f32 v81, v49, v102\n\t" "v_sub_f32 v89, v77, v102\n\t" "v_sub_f32 v82, v50, v102\n\t" "v_sub_f32 v90, v78, v102\n\t" \
   "v_sub_f32 v83, v51, v102\n\t" "v_sub_f32 v91, v79, v102\n\t" "v_exp_f32 v80, v80\n\t" "v_exp_f32 v88, v88\n\t" "v_exp_f32 v81, v81\n\t" \
   "v_exp_f32 v89, v89\n\t" "v_exp_f32 v82, v82\n\t" "v_exp_f32 v90, v90\n\t" "v_exp_f32 v83, v83\n\t" "v_exp_f32 v91, v91\n\t" "s_nop 0\n\t" \
   "v_add_f32 v96, v80, v81\n\t" "v_add_f32 v103, v82, v83\n\t" "v_add_f32 v94, v96, v103\n\t" "v_add_f32 v96, v96, v103\n\t" "s_nop 1\n\t" \
   "v_permlane32_swap_b32 v96, v94\n\t" "v_add_f32 v96, v96, v94\n\t" "s_nop 1\n\t" \
   "v_add_f32_dpp v96, v96, v96 row_ror:8 row_mask:0xf bank_mask:0xf\n\t" "v_add_f32 v103, v96, v88\n\t" "v_add_f32 v103, v103, v89\n\t" \
   "v_add_f32 v103, v103, v90\n\t" "v_add_f32 v97, v103, v91\n\t" "v_add_f32 v103, v103, v91\n\t" "s_nop 1\n\t" "v_permlane16_swap_b32 v103, v97\n\t" \
   "v_add_f32 v103, v103, v97\n\t" "v_rcp_f32 v100, v103\n\t" "s_nop 0\n\t" "v_pk_mul_f32 v[80:81], v[80:81], v[100:101] op_sel_hi:[1,0]\n\t" \
   "v_pk_mul_f32 v[82:83], v[82:83], v[100:101] op_sel_hi:[1,0]\n\t" "v_pk_mul_f32 v[88:89], v[88:89], v[100:101] op_sel_hi:[1,0]\n\t" \
   "v_pk_mul_f32 v[90:91], v[90:91], v[100:101] op_sel_hi:[1,0]\n\t" "v_cvt_pk_bf16_f32 v48, v80, v81\n\t" "v_cvt_pk_bf16_f32 v49, v82, v83\n\t" \
   "v_cvt_pk_bf16_f32 v52, v88, v89\n\t" "v_cvt_pk_bf16_f32 v53, v90, v91\n\t" "v_min_u32 v48, -2, v48\n\t" "v_min_u32 v52, -2, v52\n\t" \
   "buffer_store_dwordx2 v[48:49], v132, %[rs], s55 offen sc1\n\t" "s_branch 71b\n\t" "63:\n\t" "s_cmp_lg_u32 s64, 0\n\t" "s_cbranch_scc1 64f\n\t" \
   "s_sub_u32 s69, s56, 0x10000\n\t" "s_and_b32 s69, s69, -2\n\t" "s_branch 50b\n\t" "64:\n\t" "s_mov_b32 s69, s48\n\t" "s_cmp_lt_u32 s64, 3\n\t" \
   "s_cselect_b32 s69, -1, s69\n\t" "s_cmp_lg_u32 s69, -1\n\t" "s_cbranch_scc1 50b\n\t" "s_add_u32 s76, s76, 0x1000000\n\t" "s_mov_b32 s73, 0\n\t" \
   "65:\n\t" "s_add_u32 s73, s73, 1\n\t" "s_min_u32 s72, s73, 7\n\t" "s_mul_i32 s72, s72, 5\n\t" "s_add_u32 s72, s72, %[xs]\n\t" \
   "s_and_b32 s72, s72, 7\n\t" "s_lshl_b32 s71, %[xs], 7\n\t" "v_subrev_u32 v109, s71, v139\n\t" "s_lshl_b32 s72, s72, 7\n\t" \
   "s_add_u32 s72, s72, s66\n\t" "s_cmp_lt_u32 s73, 7\n\t" "s_cbranch_scc1 66f\n\t" "s_sleep 4\n\t" "66:\n\t" \
   "buffer_load_dword v108, v109, %[rs], s72 offen sc1\n\t" "s_waitcnt vmcnt(0)\n\t" "v_readfirstlane_b32 s69, v108\n\t" \
   "s_cmp_gt_u32 s73, 0x10000\n\t" "s_cbranch_scc1 50b\n\t" "s_cmp_eq_u32 s69, -1\n\t" "s_cbranch_scc1 65b\n\t" "s_branch 50b\n\t" "8:\n\t" \
   "s_nop 7\n\t" "s_waitcnt vmcnt(7) lgkmcnt(0)\n\t" "s_add_u32 s70, s70, 1\n\t" "s_cmp_gt_u32 s70, 2\n\t" "s_cbranch_scc1 81f\n\t" \
   "s_lshl_b32 s71, s70, 3\n\t" "s_lshl_b32 s71, 1, s71\n\t" "s_add_u32 s76, s76, s71\n\t" "81:\n\t" "v_mov_b32 v48, 0\n\t" "v_mov_b32 v49, 0\n\t" \
   "v_mov_b32 v50, 0\n\t" "v_mov_b32 v51, 0\n\t" "v_mov_b32 v52, 0\n\t" "v_mov_b32 v53, 0\n\t" "v_mov_b32 v54, 0\n\t" "v_mov_b32 v55, 0\n\t" \
   "v_mov_b32 v56, 0\n\t" "v_mov_b32 v57, 0\n\t" "v_mov_b32 v58, 0\n\t" "v_mov_b32 v59, 0\n\t" "v_mov_b32 v60, 0\n\t" "v_mov_b32 v61, 0\n\t" \
   "v_mov_b32 v62, 0\n\t" "v_mov_b32 v63, 0\n\t" "s_min_u32 s71, s70, 7\n\t" "s_lshr_b32 s67, s71, 1\n\t" "s_add_u32 s67, s67, %[xs]\n\t" \
   "s_and_b32 s67, s67, 3\n\t" "s_lshl_b32 s67, s67, 11\n\t" "s_and_b32 s72, s71, 1\n\t" "s_lshl_b32 s72, s72, 14\n\t" "s_add_u32 s67, s67, s72\n\t" \
   "s_add_u32 s67, s67, s66\n\t" "s_cmp_eq_u32 s70, 1\n\t" "s_cselect_b32 s67, s68, s67\n\t" "s_cmp_lt_u32 s70, 7\n\t" "s_cbranch_scc1 20b\n\t" \
   "s_sleep 3\n\t" "s_cmp_lt_u32 s70, 0x20000\n\t" "s_cbranch_scc1 20b\n\t" "s_mov_b32 s67, %[esc]\n\t" "s_mov_b32 s68, %[esc]\n\t" "s_branch 20b\n\t" \
   "27:\n\t" "v_mov_b32 v98, 0x7ffffff0\n\t" "buffer_store_dword v98, v98, %[rs], 0 offen\n\t" "buffer_store_dword v98, v98, %[rs], 0 offen\n\t" \
   "buffer_store_dword v98, v98, %[rs], 0 offen\n\t" "buffer_store_dword v98, v98, %[rs], 0 offen\n\t" \
   "buffer_store_dword v98, v98, %[rs], 0 offen\n\t" "buffer_store_dword v98, v98, %[rs], 0 offen\n\t" \
   "buffer_store_dword v98, v98, %[rs], 0 offen\n\t" "s_branch 28b\n\t" "141:\n\t" "s_min_i32 s71, s72, 63\n\t" "s_lshl_b32 s71, s71, 12\n\t" \
   "s_mov_b32 s74, 0\n\t" "42:\n\t" "buffer_load_dword v15, v140, %[rs], s71 offen sc1\n\t" "s_add_u32 s74, s74, 1\n\t" "s_waitcnt vmcnt(0)\n\t" \
   "v_cmp_eq_u32 vcc, -1, v15\n\t" "s_cmp_gt_u32 s74, 0x10000\n\t" "s_cbranch_scc1 41b\n\t" "s_cbranch_vccz 41b\n\t" "s_sleep 2\n\t" "s_branch 42b\n\t" \
   "95:\n\t" "s_mov_b32 s72, 1\n\t" "s_branch 197f\n\t" "97:\n\t" "buffer_load_dwordx4 v[16:19], v138, %[rs], s79 offen offset:0 sc1\n\t" \
   "buffer_load_dwordx4 v[20:23], v138, %[rs], s79 offen offset:1024 sc1\n\t" \
   "buffer_load_dwordx4 v[24:27], v138, %[rs], s79 offen offset:2048 sc1\n\t" \
   "buffer_load_dwordx4 v[28:31], v138, %[rs], s79 offen offset:3072 sc1\n\t" "buffer_load_dword v15, v140, %[rs], s59 offen sc1\n\t" \
   "v_or_b32 v108, s49, v141\n\t" "v_or_b32 v109, s49, v142\n\t" "buffer_store_dwordx4 v[44:47], v108, %[rs], s52 offen sc1\n\t" \
   "buffer_store_dwordx2 v[32:33], v109, %[rs], s51 offen sc1\n\t" "s_waitcnt vmcnt(0)\n\t" "s_branch 96b\n\t" "197:\n\t" "s_min_i32 s71, s72, 63\n\t" \
   "s_lshl_b32 s71, s71, 12\n\t" "s_mov_b32 s74, 0\n\t" "98:\n\t" "buffer_load_dword v15, v140, %[rs], s71 offen sc1\n\t" "s_add_u32 s74, s74, 1\n\t" \
   "s_waitcnt vmcnt(0)\n\t" "v_cmp_eq_u32 vcc, -1, v15\n\t" "s_cmp_gt_u32 s74, 0x10000\n\t" "s_cbranch_scc1 97b\n\t" "s_cbranch_vccz 97b\n\t" \
   "s_sleep 2\n\t" "s_branch 98b\n\t" "92:\n\t" "s_mov_b32 s72, 0\n\t" "s_branch 145f\n\t" "45:\n\t" "v_mbcnt_lo_u32_b32 v98, -1, 0\n\t" \
   "v_mbcnt_hi_u32_b32 v98, -1, v98\n\t" "v_lshlrev_b32 v98, 4, v98\n\t" "s_add_u32 s72, %[kf0], 4096\n\t" \
   "buffer_load_dwordx4 v[160:163], v98, %[rs], %[kf0] offen offset:0 sc1\n\t" \
   "buffer_load_dwordx4 v[164:167], v98, %[rs], %[kf0] offen offset:1024 sc1\n\t" \
   "buffer_load_dwordx4 v[168:171], v98, %[rs], %[kf0] offen offset:2048 sc1\n\t" \
   "buffer_load_dwordx4 v[172:175], v98, %[rs], %[kf0] offen offset:3072 sc1\n\t" \
   "buffer_load_dwordx4 v[176:179], v98, %[rs], s72 offen offset:0 sc1\n\t" "buffer_load_dwordx4 v[180:183], v98, %[rs], s72 offen offset:1024 sc1\n\t" \
   "buffer_load_dwordx4 v[184:187], v98, %[rs], s72 offen offset:2048 sc1\n\t" \
   "buffer_load_dwordx4 v[188:191], v98, %[rs], s72 offen offset:3072 sc1\n\t" "s_waitcnt vmcnt(0)\n\t" \
   "v_mfma_f32_16x16x32_bf16 v[48:51], v[160:163], v[64:67], 0\n\t" "v_mfma_f32_16x16x32_bf16 v[52:55], v[176:179], v[64:67], 0\n\t" \
   "v_mfma_f32_16x16x32_bf16 v[48:51], v[164:167], v[68:71], v[48:51]\n\t" "v_mfma_f32_16x16x32_bf16 v[52:55], v[180:183], v[68:71], v[52:55]\n\t" \
   "v_mfma_f32_16x16x32_bf16 v[48:51], v[168:171], v[72:75], v[48:51]\n\t" "v_mfma_f32_16x16x32_bf16 v[52:55], v[184:187], v[72:75], v[52:55]\n\t" \
   "v_mfma_f32_16x16x32_bf16 v[48:51], v[172:175], v[104:107], v[48:51]\n\t" "v_mfma_f32_16x16x32_bf16 v[52:55], v[188:191], v[104:107], v[52:55]\n\t" \
   "s_nop 7\n\t" "s_branch 93b\n\t" "145:\n\t" "s_min_i32 s71, s72, 63\n\t" "s_lshl_b32 s71, s71, 12\n\t" "s_mov_b32 s74, 0\n\t" "46:\n\t" \
   "buffer_load_dword v15, v140, %[rs], s71 offen sc1\n\t" "s_add_u32 s74, s74, 1\n\t" "s_waitcnt vmcnt(0)\n\t" "v_cmp_eq_u32 vcc, -1, v15\n\t" \
   "s_cmp_gt_u32 s74, 0x10000\n\t" "s_cbranch_scc1 45b\n\t" "s_cbranch_vccz 45b\n\t" "s_sleep 2\n\t" "s_branch 46b\n\t" "90:\n\t" \
   "v_or_b32 v108, s49, v141\n\t" "v_or_b32 v109, s49, v142\n\t" "buffer_store_dwordx4 v[44:47], v108, %[rs], s52 offen sc1\n\t" \
   "buffer_store_dwordx2 v[32:33], v109, %[rs], s51 offen sc1\n\t" "s_waitcnt vmcnt(0) lgkmcnt(0)"
#define P1_CLOB "v15", "v16", "v17", "v18", "v19", "v20", "v21", "v22", "v23", "v24", "v25", "v26", "v27", "v28", "v29", "v30", "v31", "v32", "v33", "v34", "v35", "v36", "v37", "v38", "v39", "v40", "v41", "v42", "v43", "v44", "v45", "v46", "v47", "v48", "v49", "v50", "v51", "v52", "v53", "v54", "v55", "v56", "v57", "v58", "v59", "v60", "v61", "v62", "v63", "v64", "v65", "v66", "v67", "v68", "v69", "v70", "v71", "v72", "v73", "v74", "v75", "v76", "v77", "v78", "v79", "v80", "v81", "v82", "v83", "v84", "v85", "v86", "v87", "v88", "v89", "v90", "v91", "v92", "v93", "v94", "v95", "v96", "v97", "v98", "v99", "v100", "v101", "v102", "v103", "v104", "v105", "v106", "v107", "v108", "v109", "v110", "v111", "v144", "v145", "v146", "v147", "v148", "v149", "v150", "v151", "v152", "v153", "v154", "v155", "v156", "v157", "v158", "v159", "v160", "v161", "v162", "v163", "v164", "v165", "v166", "v167", "v168", "v169", "v170", "v171", "v172", "v173", "v174", "v175", "v176", "v177", "v178", "v179", "v180", "v181", "v182", "v183", "v184", "v185", "v186", "v187", "v188", "v189", "v190", "v191", "s60", "s61", "s62", "s63", "s64", "s65", "s66", "s67", "s68", "s69", "s70", "s71", "s72", "s73", "s74", "s75", "s47", "s48", "s49", "s50", "s51", "s52", "s54", "s55", "s79", "s20", "s21", "s22", "s23", "s24", "s25", "s26", "s27", "s28", "s29", "s30", "s31", "s32", "s33", "s34", "s35"
#define P2_ASM \
   "11:\n\t" "s_memrealtime s[60:61]\n\t" "s_memtime s[62:63]\n\t" "s_waitcnt lgkmcnt(0)\n\t" "s_sub_u32 s74, s62, s57\n\t" \
   "s_mul_i32 s74, s74, s58\n\t" "s_lshr_b32 s74, s74, 12\n\t" "s_add_u32 s74, s74, s56\n\t" "s_lshl_b32 s60, s60, 3\n\t" "s_sub_u32 s75, s74, s60\n\t" \
   "s_cmp_lt_u32 s75, 32\n\t" "s_cbranch_scc1 12f\n\t" "s_cmp_lt_i32 s75, 0\n\t" "s_cselect_b32 s61, 0, 31\n\t" "s_add_u32 s74, s60, s61\n\t" \
   "s_sub_i32 s75, s61, s75\n\t" "s_add_i32 s75, s75, 1\n\t" "s_ashr_i32 s75, s75, 1\n\t" "s_min_i32 s75, s75, 8\n\t" "s_max_i32 s75, s75, -8\n\t" \
   "s_mov_b32 s56, s74\n\t" "s_mov_b32 s57, s62\n\t" "s_add_u32 s58, s58, s75\n\t" "12:\n\t" "s_sub_i32 s74, %[ta], s74\n\t" \
   "s_cmp_le_i32 s74, 150\n\t" "s_cbranch_scc1 13f\n\t" "s_cmp_lt_i32 s74, 260\n\t" "s_cbranch_scc1 11b\n\t" "s_sleep 2\n\t" \
   "s_cmp_lt_i32 s74, 32000\n\t" "s_cbranch_scc1 11b\n\t" "s_mov_b32 s74, 0\n\t" "13:\n\t" "s_sub_i32 s74, s74, 56\n\t" "s_max_i32 s74, s74, 0\n\t" \
   "s_mul_i32 s74, s74, 23\n\t" "s_lshr_b32 s74, s74, 5\n\t" "s_and_b32 s74, s74, -4\n\t" "s_getpc_b64 s[60:61]\n\t" "14:\n\t" \
   "s_sub_u32 s74, 15f-14b, s74\n\t" "s_add_u32 s60, s60, s74\n\t" "s_addc_u32 s61, s61, 0\n\t" "s_setpc_b64 s[60:61]\n\t" "s_nop 3\n\t" "s_nop 3\n\t" \
   "s_nop 3\n\t" "s_nop 3\n\t" "s_nop 3\n\t" "s_nop 3\n\t" "s_nop 3\n\t" "s_nop 3\n\t" "s_nop 3\n\t" "s_nop 3\n\t" "s_nop 3\n\t" "s_nop 3\n\t" \
   "s_nop 3\n\t" "s_nop 3\n\t" "s_nop 3\n\t" "s_nop 3\n\t" "s_nop 3\n\t" "s_nop 3\n\t" "s_nop 3\n\t" "s_nop 3\n\t" "s_nop 3\n\t" "s_nop 3\n\t" \
   "s_nop 3\n\t" "s_nop 3\n\t" "15:\n\t" "buffer_load_dwordx4 v[64:67], v128, %[rs], %[sa] offen sc1\n\t" \
   "buffer_load_dwordx4 v[68:71], v129, %[rs], %[sa] offen sc1\n\t" "buffer_load_dwordx4 v[72:75], v130, %[rs], %[sa] offen sc1\n\t" \
   "v_mov_b32 v48, 0\n\t" "v_mov_b32 v49, 0\n\t" "v_mov_b32 v50, 0\n\t" "v_mov_b32 v51, 0\n\t" "v_mov_b32 v52, 0\n\t" "v_mov_b32 v53, 0\n\t" \
   "v_mov_b32 v54, 0\n\t" "v_mov_b32 v55, 0\n\t" "v_mov_b32 v56, 0\n\t" "v_mov_b32 v57, 0\n\t" "v_mov_b32 v58, 0\n\t" "v_mov_b32 v59, 0\n\t" \
   "v_mov_b32 v60, 0\n\t" "v_mov_b32 v61, 0\n\t" "v_mov_b32 v62, 0\n\t" "v_mov_b32 v63, 0\n\t" "s_mov_b32 s79, 1\n\t" "s_waitcnt vmcnt(2)\n\t" \
   "ds_write_b128 v112, v[64:67]\n\t" "s_waitcnt vmcnt(1)\n\t" "ds_write_b128 v113, v[68:71]\n\t" "s_waitcnt vmcnt(0)\n\t" \
   "ds_write_b128 v114, v[72:75]\n\t" "v_smfmac_f32_16x16x64_bf16 v[48:51], v[64:67], a[0:7], v134\n\t" "v_max3_u32 v110, v64, v66, v68\n\t" \
   "v_max3_u32 v111, v70, v72, v74\n\t" "v_smfmac_f32_16x16x64_bf16 v[52:55], v[64:67], a[64:71], v134\n\t" "v_max_u32 v110, v110, v111\n\t" \
   "v_cmp_eq_u32 vcc, -1, v110\n\t" "v_smfmac_f32_16x16x64_bf16 v[56:59], v[64:67], a[128:135], v134\n\t" "s_cbranch_vccnz 9f\n\t" \
   "v_smfmac_f32_16x16x64_bf16 v[60:63], v[64:67], a[192:199], v134\n\t" "s_add_u32 m0, %[l2s], 0\n\t" "s_nop 0\n\t" \
   "buffer_load_dwordx4 v131, %[rs], %[sd] offen sc1 lds\n\t" "s_add_u32 m0, %[l2s], 1024\n\t" "s_nop 0\n\t" \
   "buffer_load_dwordx4 v132, %[rs], %[sd] offen sc1 lds\n\t" "s_waitcnt lgkmcnt(0)\n\t" "s_barrier\n\t" "ds_read_b128 v[76:79], v115\n\t" \
   "v_smfmac_f32_16x16x64_bf16 v[48:51], v[68:71], a[8:15], v134\n\t" "ds_read_b128 v[80:83], v116\n\t" \
   "v_smfmac_f32_16x16x64_bf16 v[52:55], v[68:71], a[72:79], v134\n\t" "ds_read_b128 v[84:87], v117\n\t" \
   "v_smfmac_f32_16x16x64_bf16 v[56:59], v[68:71], a[136:143], v134\n\t" "ds_read_b128 v[88:91], v118\n\t" \
   "v_smfmac_f32_16x16x64_bf16 v[60:63], v[68:71], a[200:207], v134\n\t" "ds_read_b128 v[92:95], v119\n\t" \
   "v_smfmac_f32_16x16x64_bf16 v[48:51], v[72:75], a[16:23], v134\n\t" "ds_read_b128 v[96:99], v120\n\t" \
   "v_smfmac_f32_16x16x64_bf16 v[52:55], v[72:75], a[80:87], v134\n\t" "ds_read_b128 v[100:103], v121\n\t" \
   "v_smfmac_f32_16x16x64_bf16 v[56:59], v[72:75], a[144:151], v134\n\t" "v_smfmac_f32_16x16x64_bf16 v[60:63], v[72:75], a[208:215], v134\n\t" \
   "s_waitcnt lgkmcnt(6)\n\t" "v_smfmac_f32_16x16x64_bf16 v[48:51], v[76:79], a[24:31], v134\n\t" \
   "v_smfmac_f32_16x16x64_bf16 v[52:55], v[76:79], a[88:95], v134\n\t" "v_smfmac_f32_16x16x64_bf16 v[56:59], v[76:79], a[152:159], v134\n\t" \
   "v_smfmac_f32_16x16x64_bf16 v[60:63], v[76:79], a[216:223], v134\n\t" "s_waitcnt lgkmcnt(5)\n\t" \
   "v_smfmac_f32_16x16x64_bf16 v[48:51], v[80:83], a[32:39], v134\n\t" "v_smfmac_f32_16x16x64_bf16 v[52:55], v[80:83], a[96:103], v134\n\t" \
   "v_smfmac_f32_16x16x64_bf16 v[56:59], v[80:83], a[160:167], v134\n\t" "v_smfmac_f32_16x16x64_bf16 v[60:63], v[80:83], a[224:231], v134\n\t" \
   "s_waitcnt lgkmcnt(4)\n\t" "v_smfmac_f32_16x16x64_bf16 v[48:51], v[84:87], a[40:47], v134\n\t" \
   "v_smfmac_f32_16x16x64_bf16 v[52:55], v[84:87], a[104:111], v134\n\t" "v_smfmac_f32_16x16x64_bf16 v[56:59], v[84:87], a[168:175], v134\n\t" \
   "v_smfmac_f32_16x16x64_bf16 v[60:63], v[84:87], a[232:239], v134\n\t" "s_waitcnt lgkmcnt(3)\n\t" \
   "v_smfmac_f32_16x16x64_bf16 v[48:51], v[88:91], a[48:55], v134\n\t" "v_smfmac_f32_16x16x64_bf16 v[52:55], v[88:91], a[112:119], v134\n\t" \
   "v_smfmac_f32_16x16x64_bf16 v[56:59], v[88:91], a[176:183], v134\n\t" "v_smfmac_f32_16x16x64_bf16 v[60:63], v[88:91], a[240:247], v134\n\t" \
   "s_waitcnt lgkmcnt(2)\n\t" "v_smfmac_f32_16x16x64_bf16 v[48:51], v[92:95], a[56:63], v134\n\t" \
   "v_smfmac_f32_16x16x64_bf16 v[52:55], v[92:95], a[120:127], v134\n\t" "v_smfmac_f32_16x16x64_bf16 v[56:59], v[92:95], a[184:191], v134\n\t" \
   "v_smfmac_f32_16x16x64_bf16 v[60:63], v[92:95], a[248:255], v134\n\t" "s_waitcnt lgkmcnt(1)\n\t" \
   "v_smfmac_f32_16x16x64_bf16 v[48:51], v[96:99], v[192:199], v134\n\t" "v_smfmac_f32_16x16x64_bf16 v[52:55], v[96:99], v[208:215], v134\n\t" \
   "v_smfmac_f32_16x16x64_bf16 v[56:59], v[96:99], v[224:231], v134\n\t" "v_smfmac_f32_16x16x64_bf16 v[60:63], v[96:99], v[240:247], v134\n\t" \
   "s_waitcnt lgkmcnt(0)\n\t" "v_smfmac_f32_16x16x64_bf16 v[48:51], v[100:103], v[200:207], v134\n\t" \
   "v_smfmac_f32_16x16x64_bf16 v[52:55], v[100:103], v[216:223], v134\n\t" "v_smfmac_f32_16x16x64_bf16 v[56:59], v[100:103], v[232:239], v134\n\t" \
   "v_smfmac_f32_16x16x64_bf16 v[60:63], v[100:103], v[248:255], v134\n\t" "v_permlane32_swap_b32 v48, v50\n\t" "v_permlane32_swap_b32 v49, v51\n\t" \
   "v_add_f32 v48, v48, v50\n\t" "v_add_f32 v49, v49, v51\n\t" "v_cvt_pk_bf16_f32 v48, v48, v49\n\t" "ds_write_b32 v135, v48 offset:0\n\t" \
   "v_permlane32_swap_b32 v52, v54\n\t" "v_permlane32_swap_b32 v53, v55\n\t" "v_add_f32 v52, v52, v54\n\t" "v_add_f32 v53, v53, v55\n\t" \
   "v_cvt_pk_bf16_f32 v52, v52, v53\n\t" "ds_write_b32 v135, v52 offset:256\n\t" "v_permlane32_swap_b32 v56, v58\n\t" \
   "v_permlane32_swap_b32 v57, v59\n\t" "v_add_f32 v56, v56, v58\n\t" "v_add_f32 v57, v57, v59\n\t" "v_cvt_pk_bf16_f32 v56, v56, v57\n\t" \
   "ds_write_b32 v135, v56 offset:512\n\t" "v_permlane32_swap_b32 v60, v62\n\t" "v_permlane32_swap_b32 v61, v63\n\t" "v_add_f32 v60, v60, v62\n\t" \
   "v_add_f32 v61, v61, v63\n\t" "v_cvt_pk_bf16_f32 v60, v60, v61\n\t" "ds_write_b32 v135, v60 offset:768\n\t" "s_mov_b32 s79, 0\n\t" \
   "s_waitcnt lgkmcnt(0)\n\t" "s_barrier\n\t" "9:\n\t" "s_nop 7\n\t" "s_waitcnt lgkmcnt(0)"
#define P2_CLOB "v48", "v49", "v50", "v51", "v52", "v53", "v54", "v55", "v56", "v57", "v58", "v59", "v60", "v61", "v62", "v63", "v64", "v65", "v66", "v67", "v68", "v69", "v70", "v71", "v72", "v73", "v74", "v75", "v76", "v77", "v78", "v79", "v80", "v81", "v82", "v83", "v84", "v85", "v86", "v87", "v88", "v89", "v90", "v91", "v92", "v93", "v94", "v95", "v96", "v97", "v98", "v99", "v100", "v101", "v102", "v103", "v104", "v105", "v106", "v107", "v108", "v109", "v110", "v111", "s60", "s61", "s62", "s63", "s74", "s75"
#define FIN_ASM \
   "s_mov_b32 s40, 0\n\t" "s_mov_b32 s76, 0\n\t" "s_mov_b32 s77, 0\n\t" "s_mov_b32 s71, 63\n\t" "s_lshr_b64 s[72:73], %[sel], s71\n\t" \
   "s_lshl_b32 s41, s71, 19\n\t" "s_and_b32 s72, s72, 1\n\t" "s_add_u32 s41, s41, %[pool]\n\t" "s_lshl_b32 s72, s72, 13\n\t" \
   "s_add_u32 s41, s41, s72\n\t" "s_lshr_b64 s[72:73], %[s3], s71\n\t" "s_and_b32 s47, s72, 1\n\t" "s_lshl_b32 s47, s47, 14\n\t" \
   "s_sub_u32 s45, s56, 0x10000\n\t" "v_mov_b32 v13, 0x7ffffff0\n\t" "30:\n\t" "s_mov_b32 s42, s41\n\t" "s_lshr_b64 s[72:73], %[sel], s40\n\t" \
   "s_lshl_b32 s41, s40, 19\n\t" "s_and_b32 s72, s72, 1\n\t" "s_add_u32 s41, s41, %[pool]\n\t" "s_lshl_b32 s72, s72, 13\n\t" \
   "s_add_u32 s41, s41, s72\n\t" "s_mov_b32 s43, 0\n\t" "s_xor_b32 s70, s47, %[lad0]\n\t" "s_add_u32 s70, s70, s42\n\t" \
   "s_lshr_b64 s[72:73], %[s3], s40\n\t" "s_and_b32 s47, s72, 1\n\t" "s_lshl_b32 s47, s47, 14\n\t" "s_min_u32 s71, s40, 62\n\t" \
   "s_mul_i32 s71, s71, 1310720\n\t" "s_add_u32 s71, s71, %[su]\n\t" "s_add_u32 s69, s45, %[m1c]\n\t" "s_add_u32 s72, s70, 458752\n\t" \
   "s_add_u32 s73, s70, 327680\n\t" "11:\n\t" "s_memrealtime s[60:61]\n\t" "s_memtime s[62:63]\n\t" "s_waitcnt lgkmcnt(0)\n\t" \
   "s_sub_u32 s74, s62, s57\n\t" "s_mul_i32 s74, s74, s58\n\t" "s_lshr_b32 s74, s74, 12\n\t" "s_add_u32 s74, s74, s56\n\t" "s_lshl_b32 s60, s60, 3\n\t" \
   "s_sub_u32 s75, s74, s60\n\t" "s_cmp_lt_u32 s75, 32\n\t" "s_cbranch_scc1 12f\n\t" "s_cmp_lt_i32 s75, 0\n\t" "s_cselect_b32 s61, 0, 31\n\t" \
   "s_add_u32 s74, s60, s61\n\t" "s_sub_i32 s75, s61, s75\n\t" "s_add_i32 s75, s75, 1\n\t" "s_ashr_i32 s75, s75, 1\n\t" "s_min_i32 s75, s75, 8\n\t" \
   "s_max_i32 s75, s75, -8\n\t" "s_mov_b32 s56, s74\n\t" "s_mov_b32 s57, s62\n\t" "s_add_u32 s58, s58, s75\n\t" "12:\n\t" "s_sub_i32 s74, s69, s74\n\t" \
   "s_cmp_le_i32 s74, 150\n\t" "s_cbranch_scc1 13f\n\t" "s_cmp_lt_i32 s74, 260\n\t" "s_cbranch_scc1 11b\n\t" "s_sleep 2\n\t" \
   "s_cmp_lt_i32 s74, 32000\n\t" "s_cbranch_scc1 11b\n\t" "s_mov_b32 s74, 0\n\t" "13:\n\t" "s_sub_i32 s74, s74, 56\n\t" "s_max_i32 s74, s74, 0\n\t" \
   "s_mul_i32 s74, s74, 23\n\t" "s_lshr_b32 s74, s74, 5\n\t" "s_and_b32 s74, s74, -4\n\t" "s_getpc_b64 s[60:61]\n\t" "14:\n\t" \
   "s_sub_u32 s74, 15f-14b, s74\n\t" "s_add_u32 s60, s60, s74\n\t" "s_addc_u32 s61, s61, 0\n\t" "s_setpc_b64 s[60:61]\n\t" "s_nop 3\n\t" "s_nop 3\n\t" \
   "s_nop 3\n\t" "s_nop 3\n\t" "s_nop 3\n\t" "s_nop 3\n\t" "s_nop 3\n\t" "s_nop 3\n\t" "s_nop 3\n\t" "s_nop 3\n\t" "s_nop 3\n\t" "s_nop 3\n\t" \
   "s_nop 3\n\t" "s_nop 3\n\t" "s_nop 3\n\t" "s_nop 3\n\t" "s_nop 3\n\t" "s_nop 3\n\t" "s_nop 3\n\t" "s_nop 3\n\t" "s_nop 3\n\t" "s_nop 3\n\t" \
   "s_nop 3\n\t" "s_nop 3\n\t" "15:\n\t" "31:\n\t" RSTAMP(4) "buffer_load_dwordx4 v[64:67], v16, %[rs], s72 offen sc1\n\t" \
   "buffer_load_dwordx4 v[68:71], v16, %[rs], s72 offen offset:1024 sc1\n\t" "buffer_load_dwordx4 v[72:75], v16, %[rs], s73 offen sc1\n\t" \
   "v_mov_b32 v48, 0\n\t" "v_mov_b32 v49, 0\n\t" "v_mov_b32 v50, 0\n\t" "v_mov_b32 v51, 0\n\t" "v_mov_b32 v52, 0\n\t" "v_mov_b32 v53, 0\n\t" \
   "v_mov_b32 v54, 0\n\t" "v_mov_b32 v55, 0\n\t" "v_mov_b32 v56, 0\n\t" "v_mov_b32 v57, 0\n\t" "v_mov_b32 v58, 0\n\t" "v_mov_b32 v59, 0\n\t" \
   "v_mov_b32 v60, 0\n\t" "v_mov_b32 v61, 0\n\t" "v_mov_b32 v62, 0\n\t" "v_mov_b32 v63, 0\n\t" "s_waitcnt vmcnt(2)\n\t" \
   "ds_write_b128 v18, v[64:67] offset:8192\n\t" "s_waitcnt vmcnt(1)\n\t" "ds_write_b128 v18, v[68:71] offset:9216\n\t" "s_waitcnt vmcnt(0)\n\t" \
   "ds_write_b128 v18, v[72:75] offset:0\n\t" "v_smfmac_f32_16x16x64_bf16 v[48:51], v[64:67], a[0:7], v22\n\t" "v_max3_u32 v110, v64, v66, v68\n\t" \
   "v_max3_u32 v111, v70, v72, v74\n\t" "v_smfmac_f32_16x16x64_bf16 v[52:55], v[64:67], a[64:71], v22\n\t" "v_max_u32 v110, v110, v111\n\t" \
   "v_cmp_eq_u32 vcc, -1, v110\n\t" "v_smfmac_f32_16x16x64_bf16 v[56:59], v[64:67], a[128:135], v22\n\t" "s_cbranch_vccnz 80f\n\t" \
   "v_smfmac_f32_16x16x64_bf16 v[60:63], v[64:67], a[192:199], v22\n\t" "s_waitcnt lgkmcnt(0)\n\t" "s_barrier\n\t" RSTAMP(0) \
   "ds_read_b128 v[76:79], v18 offset:1024\n\t" "v_smfmac_f32_16x16x64_bf16 v[48:51], v[68:71], a[8:15], v22\n\t" \
   "ds_read_b128 v[80:83], v18 offset:2048\n\t" "s_add_u32 s74, s71, 0\n\t" "buffer_load_dwordx4 v[128:131], v13, %[rs], s74 offen offset:0 sc1\n\t" \
   "v_smfmac_f32_16x16x64_bf16 v[52:55], v[68:71], a[72:79], v22\n\t" "ds_read_b128 v[84:87], v18 offset:3072\n\t" \
   "v_smfmac_f32_16x16x64_bf16 v[56:59], v[68:71], a[136:143], v22\n\t" "ds_read_b128 v[88:91], v18 offset:4096\n\t" \
   "buffer_load_dwordx4 v[132:135], v13, %[rs], s74 offen offset:1024 sc1\n\t" "v_smfmac_f32_16x16x64_bf16 v[60:63], v[68:71], a[200:207], v22\n\t" \
   "ds_read_b128 v[92:95], v18 offset:5120\n\t" "v_smfmac_f32_16x16x64_bf16 v[48:51], v[72:75], a[16:23], v22\n\t" \
   "ds_read_b128 v[96:99], v18 offset:6144\n\t" "buffer_load_dwordx4 v[136:139], v13, %[rs], s74 offen offset:2048 sc1\n\t" \
   "v_smfmac_f32_16x16x64_bf16 v[52:55], v[72:75], a[80:87], v22\n\t" "ds_read_b128 v[100:103], v18 offset:7168\n\t" \
   "v_smfmac_f32_16x16x64_bf16 v[56:59], v[72:75], a[144:151], v22\n\t" "buffer_load_dwordx4 v[140:143], v13, %[rs], s74 offen offset:3072 sc1\n\t" \
   "v_smfmac_f32_16x16x64_bf16 v[60:63], v[72:75], a[208:215], v22\n\t" "s_waitcnt lgkmcnt(6)\n\t" \
   "v_smfmac_f32_16x16x64_bf16 v[48:51], v[76:79], a[24:31], v22\n\t" "s_add_u32 s74, s71, 4096\n\t" \
   "buffer_load_dwordx4 v[144:147], v13, %[rs], s74 offen offset:0 sc1\n\t" "v_smfmac_f32_16x16x64_bf16 v[52:55], v[76:79], a[88:95], v22\n\t" \
   "v_smfmac_f32_16x16x64_bf16 v[56:59], v[76:79], a[152:159], v22\n\t" "buffer_load_dwordx4 v[148:151], v13, %[rs], s74 offen offset:1024 sc1\n\t" \
   "v_smfmac_f32_16x16x64_bf16 v[60:63], v[76:79], a[216:223], v22\n\t" "s_waitcnt lgkmcnt(5)\n\t" \
   "v_smfmac_f32_16x16x64_bf16 v[48:51], v[80:83], a[32:39], v22\n\t" "buffer_load_dwordx4 v[152:155], v13, %[rs], s74 offen offset:2048 sc1\n\t" \
   "v_smfmac_f32_16x16x64_bf16 v[52:55], v[80:83], a[96:103], v22\n\t" "v_smfmac_f32_16x16x64_bf16 v[56:59], v[80:83], a[160:167], v22\n\t" \
   "buffer_load_dwordx4 v[156:159], v13, %[rs], s74 offen offset:3072 sc1\n\t" "v_smfmac_f32_16x16x64_bf16 v[60:63], v[80:83], a[224:231], v22\n\t" \
   "s_waitcnt lgkmcnt(4)\n\t" "v_smfmac_f32_16x16x64_bf16 v[48:51], v[84:87], a[40:47], v22\n\t" "s_add_u32 s74, s71, 8192\n\t" \
   "buffer_load_dwordx4 v[160:163], v13, %[rs], s74 offen offset:0 sc1\n\t" "v_smfmac_f32_16x16x64_bf16 v[52:55], v[84:87], a[104:111], v22\n\t" \
   "v_smfmac_f32_16x16x64_bf16 v[56:59], v[84:87], a[168:175], v22\n\t" "buffer_load_dwordx4 v[164:167], v13, %[rs], s74 offen offset:1024 sc1\n\t" \
   "v_smfmac_f32_16x16x64_bf16 v[60:63], v[84:87], a[232:239], v22\n\t" "s_waitcnt lgkmcnt(3)\n\t" \
   "v_smfmac_f32_16x16x64_bf16 v[48:51], v[88:91], a[48:55], v22\n\t" "buffer_load_dwordx4 v[168:171], v13, %[rs], s74 offen offset:2048 sc1\n\t" \
   "v_smfmac_f32_16x16x64_bf16 v[52:55], v[88:91], a[112:119], v22\n\t" "v_smfmac_f32_16x16x64_bf16 v[56:59], v[88:91], a[176:183], v22\n\t" \
   "buffer_load_dwordx4 v[172:175], v13, %[rs], s74 offen offset:3072 sc1\n\t" "v_smfmac_f32_16x16x64_bf16 v[60:63], v[88:91], a[240:247], v22\n\t" \
   "s_waitcnt lgkmcnt(2)\n\t" "v_smfmac_f32_16x16x64_bf16 v[48:51], v[92:95], a[56:63], v22\n\t" "s_add_u32 s74, s71, 12288\n\t" \
   "buffer_load_dwordx4 v[176:179], v13, %[rs], s74 offen offset:0 sc1\n\t" "v_smfmac_f32_16x16x64_bf16 v[52:55], v[92:95], a[120:127], v22\n\t" \
   "v_smfmac_f32_16x16x64_bf16 v[56:59], v[92:95], a[184:191], v22\n\t" "buffer_load_dwordx4 v[180:183], v13, %[rs], s74 offen offset:1024 sc1\n\t" \
   "v_smfmac_f32_16x16x64_bf16 v[60:63], v[92:95], a[248:255], v22\n\t" "s_waitcnt lgkmcnt(1)\n\t" \
   "v_smfmac_f32_16x16x64_bf16 v[48:51], v[96:99], v[192:199], v22\n\t" "buffer_load_dwordx4 v[184:187], v13, %[rs], s74 offen offset:2048 sc1\n\t" \
   "v_smfmac_f32_16x16x64_bf16 v[52:55], v[96:99], v[208:215], v22\n\t" "v_smfmac_f32_16x16x64_bf16 v[56:59], v[96:99], v[224:231], v22\n\t" \
   "buffer_load_dwordx4 v[188:191], v13, %[rs], s74 offen offset:3072 sc1\n\t" "v_smfmac_f32_16x16x64_bf16 v[60:63], v[96:99], v[240:247], v22\n\t" \
   "s_waitcnt lgkmcnt(0)\n\t" "v_smfmac_f32_16x16x64_bf16 v[48:51], v[100:103], v[200:207], v22\n\t" \
   "v_smfmac_f32_16x16x64_bf16 v[52:55], v[100:103], v[216:223], v22\n\t" "v_smfmac_f32_16x16x64_bf16 v[56:59], v[100:103], v[232:239], v22\n\t" \
   "v_smfmac_f32_16x16x64_bf16 v[60:63], v[100:103], v[248:255], v22\n\t" RSTAMP(5) "v_permlane32_swap_b32 v48, v50\n\t" \
   "v_permlane32_swap_b32 v49, v51\n\t" "v_add_f32 v48, v48, v50\n\t" "v_add_f32 v49, v49, v51\n\t" "v_cvt_pk_bf16_f32 v48, v48, v49\n\t" \
   "ds_write_b32 v19, v48 offset:0\n\t" "v_permlane32_swap_b32 v52, v54\n\t" "v_permlane32_swap_b32 v53, v55\n\t" "v_add_f32 v52, v52, v54\n\t" \
   "v_add_f32 v53, v53, v55\n\t" "v_cvt_pk_bf16_f32 v52, v52, v53\n\t" "ds_write_b32 v19, v52 offset:256\n\t" "v_permlane32_swap_b32 v56, v58\n\t" \
   "v_permlane32_swap_b32 v57, v59\n\t" "v_add_f32 v56, v56, v58\n\t" "v_add_f32 v57, v57, v59\n\t" "v_cvt_pk_bf16_f32 v56, v56, v57\n\t" \
   "ds_write_b32 v19, v56 offset:512\n\t" "v_permlane32_swap_b32 v60, v62\n\t" "v_permlane32_swap_b32 v61, v63\n\t" "v_add_f32 v60, v60, v62\n\t" \
   "v_add_f32 v61, v61, v63\n\t" "v_cvt_pk_bf16_f32 v60, v60, v61\n\t" "ds_write_b32 v19, v60 offset:768\n\t" "s_waitcnt lgkmcnt(0)\n\t" \
   "s_barrier\n\t" RSTAMP(1) "ds_read_b128 v[32:35], v18 offset:10240\n\t" "ds_read_b128 v[36:39], v18 offset:11264\n\t" \
   "ds_read_b128 v[40:43], v18 offset:12288\n\t" "ds_read_b128 v[44:47], v18 offset:13312\n\t" "s_add_u32 s40, s40, 1\n\t" "s_cmp_eq_u32 s40, 64\n\t" \
   "s_cbranch_scc1 90f\n\t" "s_cmp_eq_u32 s40, 1\n\t" "s_cbranch_scc1 95f\n\t" "96:\n\t" "s_mov_b32 s46, 0\n\t" "s_xor_b32 s64, s47, %[lad0]\n\t" \
   "s_add_u32 s64, s64, s41\n\t" "v_xor_b32 v14, s47, v20\n\t" "v_xor_b32 v15, 0x4000, v14\n\t" "s_add_u32 s65, s64, 32768\n\t" \
   "s_add_u32 s66, s64, 65536\n\t" "s_add_u32 s67, s64, 98304\n\t" "s_add_u32 s68, s64, 131072\n\t" "s_add_u32 s69, s64, 163840\n\t" \
   "s_add_u32 s70, s64, 196608\n\t" "s_add_u32 s71, s64, 229376\n\t" "s_add_u32 s72, s64, 262144\n\t" "s_add_u32 s73, s64, 294912\n\t" \
   "v_mov_b32 v28, 0\n\t" "v_mov_b32 v29, 0\n\t" "v_mov_b32 v30, 0\n\t" "v_mov_b32 v31, 0\n\t" "v_mov_b32 v27, 0\n\t" RSTAMP(7) "21:\n\t" \
   "ds_read_b64 v[24:25], v23\n\t" "s_memrealtime s[60:61]\n\t" "s_memtime s[62:63]\n\t" "s_waitcnt lgkmcnt(0)\n\t" "v_readfirstlane_b32 s44, v24\n\t" \
   "s_sub_u32 s74, s62, s57\n\t" "s_mul_i32 s74, s74, s58\n\t" "s_lshr_b32 s74, s74, 12\n\t" "s_add_u32 s74, s74, s56\n\t" "s_lshl_b32 s60, s60, 3\n\t" \
   "s_sub_u32 s75, s74, s60\n\t" "s_cmp_lt_u32 s75, 32\n\t" "s_cbranch_scc1 22f\n\t" "s_cmp_lt_i32 s75, 0\n\t" "s_cselect_b32 s61, 0, 31\n\t" \
   "s_add_u32 s74, s60, s61\n\t" "s_sub_i32 s75, s61, s75\n\t" "s_add_i32 s75, s75, 1\n\t" "s_ashr_i32 s75, s75, 1\n\t" "s_min_i32 s75, s75, 8\n\t" \
   "s_max_i32 s75, s75, -8\n\t" "s_mov_b32 s56, s74\n\t" "s_mov_b32 s57, s62\n\t" "s_add_u32 s58, s58, s75\n\t" "22:\n\t" "s_sub_i32 s74, s44, s74\n\t" \
   "s_cmp_le_i32 s74, 150\n\t" "s_cbranch_scc1 23f\n\t" "s_cmp_lt_i32 s74, 260\n\t" "s_cbranch_scc1 21b\n\t" "s_sleep 2\n\t" \
   "s_cmp_lt_i32 s74, 32000\n\t" "s_cbranch_scc1 21b\n\t" "s_mov_b32 s74, 0\n\t" "23:\n\t" "s_sub_i32 s74, s74, 56\n\t" "s_max_i32 s74, s74, 0\n\t" \
   "s_mul_i32 s74, s74, 23\n\t" "s_lshr_b32 s74, s74, 5\n\t" "s_and_b32 s74, s74, -4\n\t" "s_getpc_b64 s[60:61]\n\t" "24:\n\t" \
   "s_sub_u32 s74, 25f-24b, s74\n\t" "s_add_u32 s60, s60, s74\n\t" "s_addc_u32 s61, s61, 0\n\t" "s_setpc_b64 s[60:61]\n\t" "s_nop 3\n\t" "s_nop 3\n\t" \
   "s_nop 3\n\t" "s_nop 3\n\t" "s_nop 3\n\t" "s_nop 3\n\t" "s_nop 3\n\t" "s_nop 3\n\t" "s_nop 3\n\t" "s_nop 3\n\t" "s_nop 3\n\t" "s_nop 3\n\t" \
   "s_nop 3\n\t" "s_nop 3\n\t" "s_nop 3\n\t" "s_nop 3\n\t" "s_nop 3\n\t" "s_nop 3\n\t" "s_nop 3\n\t" "s_nop 3\n\t" "s_nop 3\n\t" "s_nop 3\n\t" \
   "s_nop 3\n\t" "s_nop 3\n\t" "25:\n\t" "7:\n\t" RSTAMP(6) "buffer_load_dwordx4 v[48:51], v17, %[rs], s64 offen offset:0 sc1\n\t" \
   "buffer_load_dwordx4 v[52:55], v17, %[rs], s64 offen offset:1024 sc1\n\t" "buffer_load_dwordx4 v[56:59], v17, %[rs], s65 offen offset:0 sc1\n\t" \
   "buffer_load_dwordx4 v[60:63], v17, %[rs], s65 offen offset:1024 sc1\n\t" "buffer_load_dwordx4 v[64:67], v17, %[rs], s66 offen offset:0 sc1\n\t" \
   "buffer_load_dwordx4 v[68:71], v17, %[rs], s66 offen offset:1024 sc1\n\t" "buffer_load_dwordx4 v[72:75], v17, %[rs], s67 offen offset:0 sc1\n\t" \
   "buffer_load_dwordx4 v[76:79], v17, %[rs], s67 offen offset:1024 sc1\n\t" "buffer_load_dwordx4 v[80:83], v17, %[rs], s68 offen offset:0 sc1\n\t" \
   "buffer_load_dwordx4 v[84:87], v17, %[rs], s68 offen offset:1024 sc1\n\t" "buffer_load_dwordx4 v[88:91], v17, %[rs], s69 offen offset:0 sc1\n\t" \
   "buffer_load_dwordx4 v[92:95], v17, %[rs], s69 offen offset:1024 sc1\n\t" "buffer_load_dwordx4 v[96:99], v17, %[rs], s70 offen offset:0 sc1\n\t" \
   "buffer_load_dwordx4 v[100:103], v17, %[rs], s70 offen offset:1024 sc1\n\t" "buffer_load_dwordx4 v[104:107], v17, %[rs], s71 offen offset:0 sc1\n\t" \
   "buffer_load_dwordx4 v[108:111], v17, %[rs], s71 offen offset:1024 sc1\n\t" "buffer_load_dwordx4 v[112:115], v17, %[rs], s72 offen offset:0 sc1\n\t" \
   "buffer_load_dwordx4 v[116:119], v17, %[rs], s72 offen offset:1024 sc1\n\t" "buffer_load_dwordx4 v[120:123], v17, %[rs], s73 offen offset:0 sc1\n\t" \
   "buffer_load_dwordx4 v[124:127], v17, %[rs], s73 offen offset:1024 sc1\n\t" "s_waitcnt vmcnt(19)\n\t" \
   "v_mfma_f32_16x16x32_bf16 v[28:31], v[128:131], v[48:51], v[28:31]\n\t" "v_max3_u32 v27, v27, v48, v50\n\t" "s_waitcnt vmcnt(18)\n\t" \
   "v_mfma_f32_16x16x32_bf16 v[28:31], v[132:135], v[52:55], v[28:31]\n\t" "v_max3_u32 v27, v27, v52, v54\n\t" "s_waitcnt vmcnt(17)\n\t" \
   "v_mfma_f32_16x16x32_bf16 v[28:31], v[136:139], v[56:59], v[28:31]\n\t" "v_max3_u32 v27, v27, v56, v58\n\t" "s_waitcnt vmcnt(16)\n\t" \
   "v_mfma_f32_16x16x32_bf16 v[28:31], v[140:143], v[60:63], v[28:31]\n\t" "v_max3_u32 v27, v27, v60, v62\n\t" "s_waitcnt vmcnt(15)\n\t" \
   "v_mfma_f32_16x16x32_bf16 v[28:31], v[144:147], v[64:67], v[28:31]\n\t" "v_max3_u32 v27, v27, v64, v66\n\t" "s_waitcnt vmcnt(14)\n\t" \
   "v_mfma_f32_16x16x32_bf16 v[28:31], v[148:151], v[68:71], v[28:31]\n\t" "v_max3_u32 v27, v27, v68, v70\n\t" "s_waitcnt vmcnt(13)\n\t" \
   "v_mfma_f32_16x16x32_bf16 v[28:31], v[152:155], v[72:75], v[28:31]\n\t" "v_max3_u32 v27, v27, v72, v74\n\t" "s_waitcnt vmcnt(12)\n\t" \
   "v_mfma_f32_16x16x32_bf16 v[28:31], v[156:159], v[76:79], v[28:31]\n\t" "v_max3_u32 v27, v27, v76, v78\n\t" "s_waitcnt vmcnt(11)\n\t" \
   "v_mfma_f32_16x16x32_bf16 v[28:31], v[160:163], v[80:83], v[28:31]\n\t" "v_max3_u32 v27, v27, v80, v82\n\t" "s_waitcnt vmcnt(10)\n\t" \
   "v_mfma_f32_16x16x32_bf16 v[28:31], v[164:167], v[84:87], v[28:31]\n\t" "v_max3_u32 v27, v27, v84, v86\n\t" "s_waitcnt vmcnt(9)\n\t" \
   "v_mfma_f32_16x16x32_bf16 v[28:31], v[168:171], v[88:91], v[28:31]\n\t" "v_max3_u32 v27, v27, v88, v90\n\t" "s_waitcnt vmcnt(8)\n\t" \
   "v_mfma_f32_16x16x32_bf16 v[28:31], v[172:175], v[92:95], v[28:31]\n\t" "v_max3_u32 v27, v27, v92, v94\n\t" "s_waitcnt vmcnt(7)\n\t" \
   "v_mfma_f32_16x16x32_bf16 v[28:31], v[176:179], v[96:99], v[28:31]\n\t" "v_max3_u32 v27, v27, v96, v98\n\t" "s_waitcnt vmcnt(6)\n\t" \
   "v_mfma_f32_16x16x32_bf16 v[28:31], v[180:183], v[100:103], v[28:31]\n\t" "v_max3_u32 v27, v27, v100, v102\n\t" "s_waitcnt vmcnt(5)\n\t" \
   "v_mfma_f32_16x16x32_bf16 v[28:31], v[184:187], v[104:107], v[28:31]\n\t" "v_max3_u32 v27, v27, v104, v106\n\t" "s_waitcnt vmcnt(4)\n\t" \
   "v_mfma_f32_16x16x32_bf16 v[28:31], v[188:191], v[108:111], v[28:31]\n\t" "v_max3_u32 v27, v27, v108, v110\n\t" "s_waitcnt vmcnt(3)\n\t" \
   "v_mfma_f32_16x16x32_bf16 v[28:31], v[32:35], v[112:115], v[28:31]\n\t" "v_max3_u32 v27, v27, v112, v114\n\t" "s_waitcnt vmcnt(2)\n\t" \
   "v_mfma_f32_16x16x32_bf16 v[28:31], v[36:39], v[116:119], v[28:31]\n\t" "v_max3_u32 v27, v27, v116, v118\n\t" "s_waitcnt vmcnt(1)\n\t" \
   "v_mfma_f32_16x16x32_bf16 v[28:31], v[40:43], v[120:123], v[28:31]\n\t" "v_max3_u32 v27, v27, v120, v122\n\t" "s_waitcnt vmcnt(0)\n\t" \
   "v_mfma_f32_16x16x32_bf16 v[28:31], v[44:47], v[124:127], v[28:31]\n\t" "v_max3_u32 v27, v27, v124, v126\n\t" "v_cmp_eq_u32 vcc, -1, v27\n\t" \
   "s_nop 0\n\t" "s_cbranch_vccnz 8f\n\t" "2:\n\t" "s_nop 3\n\t" RSTAMP(2) "v_mov_b32 v48, v28\n\t" "v_mov_b32 v49, v29\n\t" "v_mov_b32 v50, v30\n\t" \
   "v_mov_b32 v51, v31\n\t" "v_permlane32_swap_b32 v28, v48\n\t" "v_permlane32_swap_b32 v29, v49\n\t" "v_permlane32_swap_b32 v30, v50\n\t" \
   "v_permlane32_swap_b32 v31, v51\n\t" "v_add_f32_dpp v28, v48, v28 row_ror:8 row_mask:0xf bank_mask:0xf\n\t" \
   "v_add_f32_dpp v29, v49, v29 row_ror:8 row_mask:0xf bank_mask:0xf\n\t" "v_add_f32_dpp v30, v50, v30 row_ror:8 row_mask:0xf bank_mask:0xf\n\t" \
   "v_add_f32_dpp v31, v51, v31 row_ror:8 row_mask:0xf bank_mask:0xf\n\t" "v_cvt_pk_bf16_f32 v24, v28, v29\n\t" "v_cvt_pk_bf16_f32 v25, v30, v31\n\t" \
   "v_min_u32 v24, -2, v24\n\t" "s_nop 0\n\t" "v_mov_b32_dpp v25, v25 row_ror:8 row_mask:0xf bank_mask:0xc\n\t" \
   "v_mov_b32_dpp v24, v24 row_ror:8 row_mask:0xf bank_mask:0xc\n\t" "buffer_store_dwordx2 v[24:25], v14, %[rs], s41 offen sc1\n\t" \
   "s_memtime s[62:63]\n\t" "s_memrealtime s[60:61]\n\t" "buffer_store_dwordx2 v[24:25], v15, %[rs], s41 offen sc1\n\t" "ds_read_b64 v[28:29], v23\n\t" \
   RSTAMP(3) "s_waitcnt lgkmcnt(0)\n\t" "v_readfirstlane_b32 s45, v29\n\t" "s_sub_u32 s74, s62, s57\n\t" "s_mul_i32 s74, s74, s58\n\t" \
   "s_lshr_b32 s74, s74, 12\n\t" "s_add_u32 s74, s74, s56\n\t" "s_lshl_b32 s60, s60, 3\n\t" "s_sub_u32 s75, s74, s60\n\t" "s_cmp_lt_u32 s75, 32\n\t" \
   "s_cbranch_scc1 38f\n\t" "s_cmp_lt_i32 s75, 0\n\t" "s_cselect_b32 s61, 0, 31\n\t" "s_add_u32 s74, s60, s61\n\t" "s_sub_i32 s75, s61, s75\n\t" \
   "s_add_i32 s75, s75, 1\n\t" "s_ashr_i32 s75, s75, 1\n\t" "s_min_i32 s75, s75, 8\n\t" "s_max_i32 s75, s75, -8\n\t" "s_mov_b32 s56, s74\n\t" \
   "s_mov_b32 s57, s62\n\t" "s_add_u32 s58, s58, s75\n\t" "38:\n\t" "s_min_u32 s75, s46, 1\n\t" "s_and_b32 s74, s74, -4\n\t" \
   "s_or_b32 s74, s74, s75\n\t" "v_mov_b32 v26, s74\n\t" "buffer_store_dword v26, v21, %[rs], s41 offen sc1\n\t" FINTRACE "v_xor_b32 v23, 32, v23\n\t" \
   "s_branch 30b\n\t" "8:\n\t" "s_add_u32 s46, s46, 1\n\t" "s_cmp_gt_u32 s46, 0x10000\n\t" "s_cbranch_scc1 2b\n\t" "s_cmp_gt_u32 s46, 2\n\t" \
   "s_cbranch_scc1 81f\n\t" "s_lshl_b32 s74, s46, 3\n\t" "s_lshl_b32 s74, 1, s74\n\t" "s_add_u32 s77, s77, s74\n\t" "81:\n\t" \
   "s_min_u32 s74, s46, 7\n\t" "s_lshr_b32 s64, s74, 1\n\t" "s_add_u32 s64, s64, %[xs]\n\t" "s_and_b32 s64, s64, 3\n\t" "s_lshl_b32 s64, s64, 11\n\t" \
   "s_and_b32 s75, s74, 1\n\t" "s_lshl_b32 s75, s75, 14\n\t" "s_xor_b32 s75, s75, s47\n\t" "s_xor_b32 s75, s75, %[lad0]\n\t" \
   "s_and_b32 s75, s75, 0x4000\n\t" "s_add_u32 s64, s64, s75\n\t" "s_add_u32 s64, s64, s41\n\t" "s_add_u32 s65, s64, 32768\n\t" \
   "s_add_u32 s66, s64, 65536\n\t" "s_add_u32 s67, s64, 98304\n\t" "s_add_u32 s68, s64, 131072\n\t" "s_add_u32 s69, s64, 163840\n\t" \
   "s_add_u32 s70, s64, 196608\n\t" "s_add_u32 s71, s64, 229376\n\t" "s_add_u32 s72, s64, 262144\n\t" "s_add_u32 s73, s64, 294912\n\t" \
   "v_mov_b32 v28, 0\n\t" "v_mov_b32 v29, 0\n\t" "v_mov_b32 v30, 0\n\t" "v_mov_b32 v31, 0\n\t" "v_mov_b32 v27, 0\n\t" "s_cmp_lt_u32 s46, 7\n\t" \
   "s_cbranch_scc1 7b\n\t" "s_sleep 3\n\t" "s_branch 7b\n\t" "80:\n\t" "s_add_u32 s74, s56, 16000\n\t" "v_mov_b32 v24, s74\n\t" \
   "ds_write_b32 v23, v24\n\t" "s_nop 7\n\t" "s_add_u32 s43, s43, 1\n\t" "s_cmp_gt_u32 s43, 2\n\t" "s_cbranch_scc1 82f\n\t" \
   "s_lshl_b32 s74, s43, 3\n\t" "s_lshl_b32 s74, 1, s74\n\t" "s_add_u32 s76, s76, s74\n\t" "82:\n\t" "s_add_u32 s74, s43, 1\n\t" \
   "s_min_u32 s74, s74, 7\n\t" "s_lshr_b32 s70, s74, 1\n\t" "s_add_u32 s70, s70, %[xs]\n\t" "s_and_b32 s70, s70, 3\n\t" "s_lshl_b32 s70, s70, 11\n\t" \
   "s_and_b32 s75, s74, 1\n\t" "s_lshl_b32 s75, s75, 14\n\t" "s_add_u32 s70, s70, s75\n\t" "s_add_u32 s70, s70, s42\n\t" "s_cmp_lt_u32 s43, 6\n\t" \
   "s_cbranch_scc1 83f\n\t" "s_sleep 3\n\t" "s_cmp_lt_u32 s43, 0x10000\n\t" "s_cbranch_scc1 83f\n\t" "s_mov_b32 s70, %[esc]\n\t" "83:\n\t" \
   "s_add_u32 s72, s70, 458752\n\t" "s_add_u32 s73, s70, 327680\n\t" "s_branch 31b\n\t" "95:\n\t" "s_mov_b32 s74, 0\n\t" \
   "v_lshlrev_b32 v14, 2, v17\n\t" "v_and_b32 v14, 0x7ff, v14\n\t" "v_mov_b32 v13, v17\n\t" "97:\n\t" \
   "buffer_load_dwordx4 v[24:27], v14, %[rs], %[uf] offen sc1\n\t" "s_add_u32 s74, s74, 1\n\t" "s_waitcnt vmcnt(0)\n\t" \
   "v_max3_u32 v15, v24, v25, v26\n\t" "v_max_u32 v15, v15, v27\n\t" "v_cmp_eq_u32 vcc, -1, v15\n\t" "s_cmp_gt_u32 s74, 0x10000\n\t" \
   "s_cbranch_scc1 98f\n\t" "s_cbranch_vccz 98f\n\t" "s_sleep 2\n\t" "s_branch 97b\n\t" "98:\n\t" "s_add_u32 s74, s71, 0\n\t" \
   "buffer_load_dwordx4 v[128:131], v13, %[rs], s74 offen offset:0 sc1\n\t" "buffer_load_dwordx4 v[132:135], v13, %[rs], s74 offen offset:1024 sc1\n\t" \
   "buffer_load_dwordx4 v[136:139], v13, %[rs], s74 offen offset:2048 sc1\n\t" \
   "buffer_load_dwordx4 v[140:143], v13, %[rs], s74 offen offset:3072 sc1\n\t" "s_add_u32 s74, s71, 4096\n\t" \
   "buffer_load_dwordx4 v[144:147], v13, %[rs], s74 offen offset:0 sc1\n\t" "buffer_load_dwordx4 v[148:151], v13, %[rs], s74 offen offset:1024 sc1\n\t" \
   "buffer_load_dwordx4 v[152:155], v13, %[rs], s74 offen offset:2048 sc1\n\t" \
   "buffer_load_dwordx4 v[156:159], v13, %[rs], s74 offen offset:3072 sc1\n\t" "s_add_u32 s74, s71, 8192\n\t" \
   "buffer_load_dwordx4 v[160:163], v13, %[rs], s74 offen offset:0 sc1\n\t" "buffer_load_dwordx4 v[164:167], v13, %[rs], s74 offen offset:1024 sc1\n\t" \
   "buffer_load_dwordx4 v[168:171], v13, %[rs], s74 offen offset:2048 sc1\n\t" \
   "buffer_load_dwordx4 v[172:175], v13, %[rs], s74 offen offset:3072 sc1\n\t" "s_add_u32 s74, s71, 12288\n\t" \
   "buffer_load_dwordx4 v[176:179], v13, %[rs], s74 offen offset:0 sc1\n\t" "buffer_load_dwordx4 v[180:183], v13, %[rs], s74 offen offset:1024 sc1\n\t" \
   "buffer_load_dwordx4 v[184:187], v13, %[rs], s74 offen offset:2048 sc1\n\t" \
   "buffer_load_dwordx4 v[188:191], v13, %[rs], s74 offen offset:3072 sc1\n\t" "s_branch 96b\n\t" "90:\n\t" "s_waitcnt vmcnt(0) lgkmcnt(0)"
#define FIN_CLOB "v13", "v14", "v15", "v24", "v25", "v26", "v27", "v28", "v29", "v30", "v31", "v32", "v33", "v34", "v35", "v36", "v37", "v38", "v39", "v40", "v41", "v42", "v43", "v44", "v45", "v46", "v47", "v48", "v49", "v50", "v51", "v52", "v53", "v54", "v55", "v56", "v57", "v58", "v59", "v60", "v61", "v62", "v63", "v64", "v65", "v66", "v67", "v68", "v69", "v70", "v71", "v72", "v73", "v74", "v75", "v76", "v77", "v78", "v79", "v80", "v81", "v82", "v83", "v84", "v85", "v86", "v87", "v88", "v89", "v90", "v91", "v92", "v93", "v94", "v95", "v96", "v97", "v98", "v99", "v100", "v101", "v102", "v103", "v104", "v105", "v106", "v107", "v108", "v109", "v110", "v111", "v112", "v113", "v114", "v115", "v116", "v117", "v118", "v119", "v120", "v121", "v122", "v123", "v124", "v125", "v126", "v127", "v128", "v129", "v130", "v131", "v132", "v133", "v134", "v135", "v136", "v137", "v138", "v139", "v140", "v141", "v142", "v143", "v144", "v145", "v146", "v147", "v148", "v149", "v150", "v151", "v152", "v153", "v154", "v155", "v156", "v157", "v158", "v159", "v160", "v161", "v162", "v163", "v164", "v165", "v166", "v167", "v168", "v169", "v170", "v171", "v172", "v173", "v174", "v175", "v176", "v177", "v178", "v179", "v180", "v181", "v182", "v183", "v184", "v185", "v186", "v187", "v188", "v189", "v190", "v191", "s40", "s41", "s42", "s43", "s44", "s45", "s46", "s47", "s60", "s61", "s62", "s63", "s64", "s65", "s66", "s67", "s68", "s69", "s70", "s71", "s72", "s73", "s74", "s75"
typedef unsigned v16u __attribute__((ext_vector_type(16)));
typedef unsigned v32u __attribute__((ext_vector_type(32)));
__device__ __forceinline__ void put4(v32u& d, int i, v4u v) { d[4 * i] = v.x; d[4 * i + 1] = v.y; d[4 * i + 2] = v.z; d[4 * i + 3] = v.w; }
__device__ __forceinline__ void load_weights(v32u (&w)[10], rsrc_t rs, int base, int tstride0, int tstride1, int p0) {
#pragma unroll
  for (int lp = 0; lp < 10; ++lp) {
    int p = p0 + lp; p = p >= 10 ? p - 10 : p;
#pragma unroll
    for (int t = 0; t < 4; ++t) {
      const int vo = base + ((t >> 1) * tstride1 + (t & 1) * tstride0) * 1024;
      v32u& d = lp < 8 ? w[2 * t + (lp >> 2)] : w[8 + (t >> 1)];
      const int i = lp < 8 ? (lp & 3) * 2 : (t & 1) * 4 + (lp - 8) * 2;
      put4(d, i, __builtin_amdgcn_raw_buffer_load_b128(rs, vo, p * 2048, 0));
      put4(d, i + 1, __builtin_amdgcn_raw_buffer_load_b128(rs, vo + 1024, p * 2048, 0));
    }
  }
}
#define W_OPS "{a[0:31]}"(w[0]), "{a[32:63]}"(w[1]), "{a[64:95]}"(w[2]), "{a[96:127]}"(w[3]), "{a[128:159]}"(w[4]), "{a[160:191]}"(w[5]), \
              "{a[192:223]}"(w[6]), "{a[224:255]}"(w[7]), "{v[192:223]}"(w[8]), "{v[224:255]}"(w[9])

__device__ __forceinline__ int mem_lane(int lane) { return lane * 16; }
__device__ __forceinline__ int pair_of(int g) { return (g & 1) * 2 + (g >> 1); }

__device__ __forceinline__ void rec_p1(int wv_, const v32u (&w)[10], unsigned char* ws, int layer, int h, int xs, RecTune tn, unsigned long long* stamps) {
  const Clk0 c0 = clk_sample();
  const unsigned lds0 = (unsigned)(uintptr_t)(__attribute__((address_space(3))) unsigned char*)dyn_lds;
  const int lane = lane_id(), wv = wv_, g = lane >> 4, r = lane & 15;
  const v4u rs = make_rs(ws, (unsigned)OFF_END);
  const Pool pool = pool_init(ws);
  const int p0 = wv < 3 ? 3 * wv : 8;
  v16u ad, la = (unsigned)lane;
  const int kqo = (int)(wv < 2 ? OFF_KF : OFF_QF) + ((h * RT + (wv & 1)) * 4 * 64 + lane) * 16;
  const int progo = (wv == 0 && lane == 0) ? prog_off(layer, 0, h) : OOB;
  const unsigned lfs = lds0 + L_CL + wv * 512;
  {
    const int p2 = wv < 3 ? p0 + 2 : 0;
    ad[0] = W_MEM + (p0 >> 1) * WIN + (p0 & 1) * 1024 + mem_lane(lane);
    ad[1] = W_MEM + ((p0 + 1) >> 1) * WIN + ((p0 + 1) & 1) * 1024 + mem_lane(lane);
    ad[2] = W_MEM + (p2 >> 1) * WIN + (p2 & 1) * 1024 + mem_lane(lane);
    ad[3] = lane == 0 ? W_MISC + h * 64 + wv * 4 : OOB;
    ad[4] = copy_off(wv) + (h >> 2) * WIN + ((h >> 1) & 1) * 1024 + (g * 16 + (h & 1) * 8 + (r & 7)) * 16 + (r >> 3) * 8;
    ad[5] = g < 2 ? copy_off(wv + 4 * (r >> 3)) + (8 + (h >> 4)) * WIN + ((h >> 3) & 1) * 1024 + (((h >> 1) & 3) * 16 + (h & 1) * 8 + (r & 7)) * 16 + g * 8 : OOB;
    ad[6] = (r & 8) ? 0xEEEEu : 0x4444u;
    ad[7] = lds0 + (32 * wv + r) * 16 + pair_of(g) * 4;
    ad[8] = lds0 + (4 * g + (r >> 2)) * 16 + (r & 1) * 8;
    ad[9] = lds0 + L_M2 + wv * 3072 + lane * 16;
    ad[10] = kqo;
    ad[11] = M_TM + xs * 128 + (lane & 1) * 12;
    ad[12] = kqf_off(layer, 0, h) + (lane & 3) * 4;
    ad[13] = wv > 1 ? (int)OFF_PXF + ((h * 2 + (wv & 1)) * 64 + lane) * 16 : OOB;
    ad[14] = (g < 2 && wv > 1) ? (int)OFF_PXM + (((h >> 2) * 2 + (wv & 1)) * 64 + (h & 3) * 16 + r) * 16 + g * 8 : OOB;
    ad[15] = progo;
#pragma unroll
    for (int i = 0; i < 10; ++i) { int p = p0 + i; p = p >= 10 ? p - 10 : p; la[i] = lds0 + p * 1024 + lane * 16; }
    la[10] = lds0 + L_KQ + lane * 16;
    la[11] = lfs;
    la[12] = lds0 + REC_ST + wv * 8;
    asm volatile("v_mov_b32 %0, 0xd028965f" : "=v"(la[13]));
    la[14] = 16 * (wv & 1) + r - 4 * g;
    la[15] = lds0 + L_KQ + (wv & 1) * 4096 + lane * 16;
  }
  __builtin_amdgcn_s_waitcnt(0x0F70);
  if (layer) __builtin_amdgcn_s_sleep(64);
  v4u clk = clk_init(c0);
  unsigned long long stat;
  asm volatile(P1_ASM
               : "=&{s[76:77]}"(stat), "+{s[56:59]}"(clk)
               : W_OPS, "{v[128:143]}"(ad), "{v[112:127]}"(la), [rs] "s"(rs), [sel] "s"(pool.sel), [pool] "s"(pool.base), [xs] "s"(xs),
                 [lad0] "s"(ladder(xs, xs & 1)), [s3] "s"(pool.s3), [m1] "s"(tn.m1), [l2s] "s"(lds0 + L_M2 + wv * 3072),
                 [hi8] "s"(0xFF00FF00FF00FF00ull), [lfs] "s"(lfs), [wv] "s"(wv), [kf0] "s"((int)OFF_KF + h * RT * 4096),
                 [esc] "s"((int)OFF_FWD - W_MEM)
               : P1_CLOB RST_CLOB, "vcc", "scc", "m0", "memory");
  LDSBAR();
  {
    const unsigned one = 1u;
    const int so = (NSEG - 1) * 112 * 64;
    VM_ST32(one, progo, so);
    VM_WAIT(0);
  }
}

#define TSTART 4400
#define TD1 1000u
#define TR2 470u
#define TEMA 1
#define TEMB 2
#define TBUP 48
#define TBDN 6
#define TLATE 520
#define TSEC 50
__device__ __forceinline__ unsigned track_dur(unsigned e, int m, int rob, int s) {
  const int lo = (int)e - (s < 8 ? 64 : (rob & 255)), hi = (int)e + (rob >> 8);
  if (s >= 8) m = (int)e + ((m - (int)e + (m > (int)e ? (1 << TEMA) - 1 : 0)) >> (m > (int)e ? TEMA : TEMB));
  return (unsigned)min(max(m, lo), hi);
}
__device__ __forceinline__ void rec_p2(int wv_, const v32u (&w)[10], unsigned char* ws, int layer, int c8, int xs, RecTune tn, unsigned long long* stamps) {
  const Clk0 c0 = clk_sample();
  const unsigned lds0 = (unsigned)(uintptr_t)(__attribute__((address_space(3))) unsigned char*)dyn_lds;
  const int lane = lane_id(), wv = wv_, g = lane >> 4, r = lane & 15;
  const v4u rs = make_rs(ws, (unsigned)OFF_END);
  const Pool pool = pool_init(ws);
  const int p0 = 3 * wv;
  unsigned long long stat = 0;
  unsigned tm = 0, tp = 0, anchor = 0, r2a = TR2, d1a = TD1;
  v16u ad = (unsigned)lane, la = (unsigned)lane;
#pragma unroll
  for (int i = 0; i < 3; ++i) ad[i] = W_MEM + ((p0 + i) >> 1) * WIN + ((p0 + i) & 1) * 1024 + mem_lane(lane);
  if (wv == 0) { ad[3] = M_STAMP + lane * 64; ad[4] = M_STAMP + ((lane & 15) + 64) * 64; }
  else { ad[3] = wv == 1 ? W_MISC + (lane & 31) * 64 : kqf_off(layer, 0, 32 + (lane & 31)); ad[4] = OOB; }
  ad[6] = (r & 8) ? 0xEEEEu : 0x4444u;
  ad[7] = lds0 + L_Y + (wv * 64 + r) * 16 + pair_of(g) * 4;
  const unsigned l2s = lds0 + L_SD + wv * 2048, sdl = l2s + lane * 16;
#pragma unroll
  for (int i = 0; i < 10; ++i) { int p = p0 + i; p = p >= 10 ? p - 10 : p; la[i] = lds0 + p * 1024 + lane * 16; }
  int yo, oc;
  if (wv == 0) { yo = M_TM + xs * 128; oc = (c8 == 0 && lane < 8) ? M_TM + lane * 128 : OOB; }
  else {
    const int head = 16 * (wv - 1) + 2 * g + (r >> 3);
    yo = (int)OFF_YF + ((((head >> 2) * 80 + c8) * 4 + (head & 3)) * 8 + (r & 7)) * 16;
    oc = lane == 0 ? prog_off(layer, 0, 32 + c8) : OOB;
  }
  int fl = c8 < 64 ? (c8 >> 1) * 16384 + (c8 & 1) * 4096 + (wv - 1) * 2048 + lane * 16 : OOB;
  unsigned ff;
  asm volatile("v_mov_b32 %0, -1" : "=v"(ff));
  v4u sent = {ff, ff, ff, ff};
  asm volatile("" : "+v"(yo), "+v"(oc), "+v"(fl), "+v"(sent));
  const int lad0 = ladder(xs, xs & 1);
  if (wv == 0) LDS_U32(lds0 + L_T + 16) = TD1;
  __builtin_amdgcn_s_waitcnt(0x0070);
  if (layer) __builtin_amdgcn_s_sleep(64);
  v4u clk = clk_init(c0);
  tm = clk.x - 0x10000u;
  unsigned dd = TD1 + tn.m2, rr = 0, tmp = 0;
  int bonus = 0;
  for (int s = 0; s < NSEG; ++s) {
    const int rgm = pool.region((s + NSEG - 1) & (NSEG - 1)), rg = pool.region(s);
    int sa = rgm + (lad0 ^ pool.near((s + NSEG - 1) & (NSEG - 1)));
    const int sd = wv < 2 ? rgm : (s + 1 < NSEG ? s + 1 : s) * 4096;
    const unsigned ta = tm + tn.m1 + tn.c2, slot = lds0 + L_T + (s & 1) * 32;
    v4u so;
#pragma unroll 1
    for (unsigned tries = 0;; ++tries) {
      asm volatile(P2_ASM
                   : "=&{s[76:79]}"(so), "+{s[56:59]}"(clk)
                   : W_OPS, "{v[128:143]}"(ad), "{v[112:127]}"(la), [rs] "s"(rs), [sa] "s"(sa), [sd] "s"(sd), [l2s] "s"(l2s), [ta] "s"(ta)
                   : P2_CLOB, "vcc", "scc", "m0", "memory");
      if (__builtin_expect(so.w == 0, 1)) break;
      if (tries < 2) { STAT(tries + 1); }
      if (tries >= 5) __builtin_amdgcn_s_sleep(3);
      sa = rgm + ladder(xs, tries < 5 ? tries + 2 : 7);
      if (tries > SPIN_CAP) sa = (int)OFF_FWD - W_MEM;
      LDS_U32(slot) = rtc() * 8u + 16000u;
    }
    if (wv == 0) {
      if (s == NSEG - 1) break;
      VM_WAIT(0);
      const unsigned b2a = LDS_U32(sdl), b2b = LDS_U32(sdl + 1024), dn = LDS_U32(lds0 + L_T + 16);
      const unsigned thr = tm + tn.m1 - tn.vis;
      const bool have = !__any(max(b2a, b2b) == 0xFFFFFFFFu);
      anchor = tm;
      int e = 0;
      if (__builtin_expect(!have || __any(max((int)(b2a - thr), (int)(b2b - thr)) > 0), 0)) {
        STAT(3);
        e = wave_imax(max((int)(b2a - tp), (int)(b2b - tp)));
        const int last = have ? e + (int)(tp - tm) : (int)(rtc() * 8u - tm) - 600;
        const int d = last + tn.vis - tn.m1;
        anchor = tm + (d <= 0 ? 0 : (d <= tn.dlt ? tn.dlt + TSEC : d + TLATE));
        if (s == 0) {
          const unsigned cv = flag_finish<2>(rs, kqf_off(layer, 0, 32 + (lane & 31)), 0xFFFFFFFFu);
          const unsigned now = rtc();
          anchor = now * 8u - dd;
          rr = (unsigned)min(max(wave_imax((int)(cv - now)) * 8 + TSTART, 300), 4000);
        }
      }
      const unsigned tpo = tp, tmo = tm;
      tp = anchor + dd;
      tm = nosent(tp + rr);
      LDS_V4(slot) = v4u{tp, tm, (unsigned)(s + 1), anchor};
      if (anchor != tmo && s > 2) bonus = TBUP; else bonus = max(bonus - TBDN, 0);
      dd = (unsigned)__builtin_amdgcn_readfirstlane(dn) + tn.m2 + bonus;
      rr = r2a + bonus;
      {
        const v4u wq = v4u{tm, dd, rr, dd + rr};
        VM_ST128(wq, oc, rg);
      }
      if (s > 1 && !__any(((b2a | b2b) & 1u) != 0u)) r2a = track_dur(r2a, wave_imax(max((int)(b2a - tpo), (int)(b2b - tpo))), tn.rob, s);
      while ((int)(tp + 40u - rtc() * 8u) > 0 && (int)(tp - rtc() * 8u) < 32000) __builtin_amdgcn_s_sleep(1);
      v4u lw;
      VM_LD128(lw, yo, rg, 0);
      VM_WAIT1(0, lw);
      const unsigned l0 = (unsigned)__builtin_amdgcn_readfirstlane(lw.x), l1 = (unsigned)__builtin_amdgcn_readfirstlane(lw.y),
                     l2 = (unsigned)__builtin_amdgcn_readfirstlane(lw.z);
      if (max(max(l0, l1), l2) != 0xFFFFFFFFu) { tm = l0; dd = l1; rr = l2; }
      LDS_V2(slot ^ 32u) = v2u{tm + dd, nosent(tm + dd + rr)};
    } else {
      {
        const v4u y0 = LDS_V4(lds0 + L_Y + ((wv - 1) * 2) * 1024 + lane * 16), y1 = LDS_V4(lds0 + L_Y + ((wv - 1) * 2 + 1) * 1024 + lane * 16);
        const int s0 = s * 8 * 80 * 512, s1 = s0 + 2 * 80 * 512;
        VM_ST128(y0, yo, s0); VM_ST128(y1, yo, s1);
      }
      {
        const int so = pool.region((s + NPRE) & (NSEG - 1));
        const int fo = (s + NPRE == NSEG - 1 && c8 >= 40 && c8 < 60) ? OOB : fl;
        asm volatile("buffer_store_dwordx4 %0, %1, %2, %3 offen sc1\n\tbuffer_store_dwordx4 %0, %1, %2, %3 offen offset:1024 sc1"
                     ::"v"(sent), "v"(fo), "s"(rs), "s"(so) : "memory");
      }
      VM_WAIT(4);
      if (wv == 1) {
        if (s > 0) {
          const unsigned one = 1u;
          const int so = (s - 1) * 112 * 64;
          VM_ST32(one, oc, so);
        }
        const v4u b1 = LDS_V4(sdl);
        if (s > 1 && anchor == tmp && !__any(((b1.x | b1.y | b1.z | b1.w) & 1u) != 0u)) {
          d1a = track_dur(d1a, wave_imax(max(max((int)(b1.x - anchor), (int)(b1.y - anchor)), max((int)(b1.z - anchor), (int)(b1.w - anchor)))), tn.rob, s);
          LDS_U32(lds0 + L_T + 16) = d1a;
        }
        tmp = tm;
      } else if (s < NSEG - 1) {
        flag_finish<2>(rs, (int)ad[3] + sd, max4(LDS_V4(sdl)));
      }
      if (s == NSEG - 1) break;
      for (unsigned spins = 0; spins < (1u << 20); ++spins) {
        MEMPIN();
        const v4u tv = LDS_V4(slot);
        tp = (unsigned)__builtin_amdgcn_readfirstlane(tv.x);
        tm = (unsigned)__builtin_amdgcn_readfirstlane(tv.y);
        anchor = (unsigned)__builtin_amdgcn_readfirstlane(tv.w);
        if (__builtin_amdgcn_readfirstlane(tv.z) == s + 1) break;
        __builtin_amdgcn_s_sleep(2);
      }
    }
  }
  VM_WAIT(0);
  LDSBAR();
  if (wv == 1) {
    const unsigned one = 1u;
    const int so = (NSEG - 1) * 112 * 64;
    VM_ST32(one, oc, so);
    VM_WAIT(0);
  }
}

__device__ __forceinline__ void rec_fin(const v32u (&w)[10], unsigned char* ws, int layer, int c8, int xs, RecTune tn, unsigned long long* stamps) {
  const Clk0 c0 = clk_sample();
  const unsigned lds0 = (unsigned)(uintptr_t)(__attribute__((address_space(3))) unsigned char*)dyn_lds;
  const int lane = lane_id(), g = lane >> 4, r = lane & 15;
  const v4u rs = make_rs(ws, (unsigned)OFF_END);
  const Pool pool = pool_init(ws);
  v8u ad;
  ad[0] = mem_lane(lane);
  ad[1] = lane * 16;
  ad[2] = lds0 + lane * 16;
  ad[3] = lds0 + L_Y + (3 * 64 + r) * 16 + pair_of(g) * 4;
  ad[4] = W_MEM + copy_off((lane >> 5) * 2 + (r >> 3) * 5) + (c8 >> 4) * WIN + ((c8 >> 3) & 1) * 1024 + ((c8 & 7) * 8 + (r & 7)) * 16 + (g & 1) * 8;
  ad[5] = lane == 0 ? M_STAMP + c8 * 64 : OOB;
  ad[6] = (r & 8) ? 0xEEEEu : 0x4444u;
  ad[7] = lds0 + L_T;
  __builtin_amdgcn_s_waitcnt(0x0070);
  if (layer) __builtin_amdgcn_s_sleep(64);
  v4u clk = clk_init(c0);
  LDS_U32(lds0 + L_T) = clk.x + 16000u;
  LDS_U32(lds0 + L_T + 8) = 0u;
  LDS_U32(lds0 + L_T + 40) = 0u;
  unsigned long long stat;
  asm volatile(FIN_ASM
               : "=&{s[76:77]}"(stat), "+{s[56:59]}"(clk), "+{v[16:23]}"(ad)
               : W_OPS, [rs] "s"(rs), [sel] "s"(pool.sel), [pool] "s"(pool.base), [xs] "s"(xs), [lad0] "s"(ladder(xs, xs & 1)), [s3] "s"(pool.s3),
                 [su] "s"((int)OFF_UF + c8 * 16384), [m1c] "s"(tn.m1 + tn.c2), [esc] "s"((int)OFF_FWD - W_MEM), [stl] "s"(lds0 + REC_ST - 32),
                 [uf] "s"(kqf_off(layer, 0, 32))
               : FIN_CLOB RST_CLOB, "vcc", "scc", "memory");
  LDSBAR();
}

constexpr int PJ_BUF = 43 * 1024, PJ_X = 16384, PJ_LDS = PJ_X + 3 * PJ_BUF;
#define KQ_ASM \
   "s_mov_b32 s44, %[rdy]\n\t" "s_mov_b32 s45, 0\n\t" "s_mov_b32 s46, 0\n\t" "s_cmp_eq_u32 s44, -1\n\t" "s_cbranch_scc1 68f\n\t" \
   "s_mul_i32 s52, s45, 1536\n\t" "s_mul_i32 s51, s45, 40960\n\t" "s_add_u32 m0, s46, %[ldx]\n\t" "s_mov_b32 s53, s46\n\t" \
   "buffer_load_dwordx4 v16, %[rs], s51 offen offset:0 sc1 lds\n\t" "buffer_load_dwordx4 v16, %[rs], s51 offen offset:1024 sc1 lds\n\t" \
   "buffer_load_dwordx4 v16, %[rs], s51 offen offset:2048 sc1 lds\n\t" "buffer_load_dwordx4 v16, %[rs], s51 offen offset:3072 sc1 lds\n\t" \
   "s_add_u32 m0, m0, 4096\n\t" "s_add_u32 s51, s51, 4096\n\t" "buffer_load_dwordx4 v16, %[rs], s51 offen offset:0 sc1 lds\n\t" \
   "buffer_load_dwordx4 v16, %[rs], s51 offen offset:1024 sc1 lds\n\t" "buffer_load_dwordx4 v16, %[rs], s51 offen offset:2048 sc1 lds\n\t" \
   "buffer_load_dwordx4 v16, %[rs], s51 offen offset:3072 sc1 lds\n\t" "s_add_u32 m0, m0, 4096\n\t" "s_add_u32 s51, s51, 4096\n\t" \
   "buffer_load_dwordx4 v16, %[rs], s51 offen offset:0 sc1 lds\n\t" "buffer_load_dwordx4 v16, %[rs], s51 offen offset:1024 sc1 lds\n\t" \
   "s_add_u32 m0, s53, %[ld1]\n\t" "s_nop 0\n\t" "buffer_load_dwordx4 v17, %[rs], s52 offen sc1 lds\n\t" "s_mov_b32 s45, 1\n\t" \
   "s_mov_b32 s46, 44032\n\t" "s_mul_i32 s52, s45, 1536\n\t" "s_mul_i32 s51, s45, 40960\n\t" "s_add_u32 m0, s46, %[ldx]\n\t" "s_mov_b32 s53, s46\n\t" \
   "buffer_load_dwordx4 v16, %[rs], s51 offen offset:0 sc1 lds\n\t" "buffer_load_dwordx4 v16, %[rs], s51 offen offset:1024 sc1 lds\n\t" \
   "buffer_load_dwordx4 v16, %[rs], s51 offen offset:2048 sc1 lds\n\t" "buffer_load_dwordx4 v16, %[rs], s51 offen offset:3072 sc1 lds\n\t" \
   "s_add_u32 m0, m0, 4096\n\t" "s_add_u32 s51, s51, 4096\n\t" "buffer_load_dwordx4 v16, %[rs], s51 offen offset:0 sc1 lds\n\t" \
   "buffer_load_dwordx4 v16, %[rs], s51 offen offset:1024 sc1 lds\n\t" "buffer_load_dwordx4 v16, %[rs], s51 offen offset:2048 sc1 lds\n\t" \
   "buffer_load_dwordx4 v16, %[rs], s51 offen offset:3072 sc1 lds\n\t" "s_add_u32 m0, m0, 4096\n\t" "s_add_u32 s51, s51, 4096\n\t" \
   "buffer_load_dwordx4 v16, %[rs], s51 offen offset:0 sc1 lds\n\t" "buffer_load_dwordx4 v16, %[rs], s51 offen offset:1024 sc1 lds\n\t" \
   "s_add_u32 m0, s53, %[ld1]\n\t" "s_nop 0\n\t" "buffer_load_dwordx4 v17, %[rs], s52 offen sc1 lds\n\t" "68:\n\t" "v_mov_b32 v80, 0x3f803f80\n\t" \
   "v_mov_b32 v81, 0x3f803f80\n\t" "v_mov_b32 v82, 0x3f803f80\n\t" "v_mov_b32 v83, 0x3f803f80\n\t" "v_mov_b32 v31, 1\n\t" "s_nop 1\n\t" \
   "v_mfma_f32_16x16x32_bf16 v[32:35], a[0:3], v[80:83], 0\n\t" "v_mfma_f32_16x16x32_bf16 v[36:39], a[4:7], v[80:83], 0\n\t" \
   "v_mfma_f32_16x16x32_bf16 v[40:43], a[8:11], v[80:83], 0\n\t" "v_mfma_f32_16x16x32_bf16 v[44:47], a[12:15], v[80:83], 0\n\t" \
   "v_mfma_f32_16x16x32_bf16 v[32:35], a[16:19], v[80:83], v[32:35]\n\t" "v_mfma_f32_16x16x32_bf16 v[36:39], a[20:23], v[80:83], v[36:39]\n\t" \
   "v_mfma_f32_16x16x32_bf16 v[40:43], a[24:27], v[80:83], v[40:43]\n\t" "v_mfma_f32_16x16x32_bf16 v[44:47], a[28:31], v[80:83], v[44:47]\n\t" \
   "v_mfma_f32_16x16x32_bf16 v[32:35], a[32:35], v[80:83], v[32:35]\n\t" "v_mfma_f32_16x16x32_bf16 v[36:39], a[36:39], v[80:83], v[36:39]\n\t" \
   "v_mfma_f32_16x16x32_bf16 v[40:43], a[40:43], v[80:83], v[40:43]\n\t" "v_mfma_f32_16x16x32_bf16 v[44:47], a[44:47], v[80:83], v[44:47]\n\t" \
   "v_mfma_f32_16x16x32_bf16 v[32:35], a[48:51], v[80:83], v[32:35]\n\t" "v_mfma_f32_16x16x32_bf16 v[36:39], a[52:55], v[80:83], v[36:39]\n\t" \
   "v_mfma_f32_16x16x32_bf16 v[40:43], a[56:59], v[80:83], v[40:43]\n\t" "v_mfma_f32_16x16x32_bf16 v[44:47], a[60:63], v[80:83], v[44:47]\n\t" \
   "v_mfma_f32_16x16x32_bf16 v[32:35], a[64:67], v[80:83], v[32:35]\n\t" "v_mfma_f32_16x16x32_bf16 v[36:39], a[68:71], v[80:83], v[36:39]\n\t" \
   "v_mfma_f32_16x16x32_bf16 v[40:43], a[72:75], v[80:83], v[40:43]\n\t" "v_mfma_f32_16x16x32_bf16 v[44:47], a[76:79], v[80:83], v[44:47]\n\t" \
   "v_mfma_f32_16x16x32_bf16 v[32:35], a[80:83], v[80:83], v[32:35]\n\t" "v_mfma_f32_16x16x32_bf16 v[36:39], a[84:87], v[80:83], v[36:39]\n\t" \
   "v_mfma_f32_16x16x32_bf16 v[40:43], a[88:91], v[80:83], v[40:43]\n\t" "v_mfma_f32_16x16x32_bf16 v[44:47], a[92:95], v[80:83], v[44:47]\n\t" \
   "v_mfma_f32_16x16x32_bf16 v[32:35], a[96:99], v[80:83], v[32:35]\n\t" "v_mfma_f32_16x16x32_bf16 v[36:39], a[100:103], v[80:83], v[36:39]\n\t" \
   "v_mfma_f32_16x16x32_bf16 v[40:43], a[104:107], v[80:83], v[40:43]\n\t" "v_mfma_f32_16x16x32_bf16 v[44:47], a[108:111], v[80:83], v[44:47]\n\t" \
   "v_mfma_f32_16x16x32_bf16 v[32:35], a[112:115], v[80:83], v[32:35]\n\t" "v_mfma_f32_16x16x32_bf16 v[36:39], a[116:119], v[80:83], v[36:39]\n\t" \
   "v_mfma_f32_16x16x32_bf16 v[40:43], a[120:123], v[80:83], v[40:43]\n\t" "v_mfma_f32_16x16x32_bf16 v[44:47], a[124:127], v[80:83], v[44:47]\n\t" \
   "v_mfma_f32_16x16x32_bf16 v[32:35], a[128:131], v[80:83], v[32:35]\n\t" "v_mfma_f32_16x16x32_bf16 v[36:39], a[132:135], v[80:83], v[36:39]\n\t" \
   "v_mfma_f32_16x16x32_bf16 v[40:43], a[136:139], v[80:83], v[40:43]\n\t" "v_mfma_f32_16x16x32_bf16 v[44:47], a[140:143], v[80:83], v[44:47]\n\t" \
   "v_mfma_f32_16x16x32_bf16 v[32:35], a[144:147], v[80:83], v[32:35]\n\t" "v_mfma_f32_16x16x32_bf16 v[36:39], a[148:151], v[80:83], v[36:39]\n\t" \
   "v_mfma_f32_16x16x32_bf16 v[40:43], a[152:155], v[80:83], v[40:43]\n\t" "v_mfma_f32_16x16x32_bf16 v[44:47], a[156:159], v[80:83], v[44:47]\n\t" \
   "v_mfma_f32_16x16x32_bf16 v[32:35], a[160:163], v[80:83], v[32:35]\n\t" "v_mfma_f32_16x16x32_bf16 v[36:39], a[164:167], v[80:83], v[36:39]\n\t" \
   "v_mfma_f32_16x16x32_bf16 v[40:43], a[168:171], v[80:83], v[40:43]\n\t" "v_mfma_f32_16x16x32_bf16 v[44:47], a[172:175], v[80:83], v[44:47]\n\t" \
   "v_mfma_f32_16x16x32_bf16 v[32:35], a[176:179], v[80:83], v[32:35]\n\t" "v_mfma_f32_16x16x32_bf16 v[36:39], a[180:183], v[80:83], v[36:39]\n\t" \
   "v_mfma_f32_16x16x32_bf16 v[40:43], a[184:187], v[80:83], v[40:43]\n\t" "v_mfma_f32_16x16x32_bf16 v[44:47], a[188:191], v[80:83], v[44:47]\n\t" \
   "v_mfma_f32_16x16x32_bf16 v[32:35], a[192:195], v[80:83], v[32:35]\n\t" "v_mfma_f32_16x16x32_bf16 v[36:39], a[196:199], v[80:83], v[36:39]\n\t" \
   "v_mfma_f32_16x16x32_bf16 v[40:43], a[200:203], v[80:83], v[40:43]\n\t" "v_mfma_f32_16x16x32_bf16 v[44:47], a[204:207], v[80:83], v[44:47]\n\t" \
   "v_mfma_f32_16x16x32_bf16 v[32:35], a[208:211], v[80:83], v[32:35]\n\t" "v_mfma_f32_16x16x32_bf16 v[36:39], a[212:215], v[80:83], v[36:39]\n\t" \
   "v_mfma_f32_16x16x32_bf16 v[40:43], a[216:219], v[80:83], v[40:43]\n\t" "v_mfma_f32_16x16x32_bf16 v[44:47], a[220:223], v[80:83], v[44:47]\n\t" \
   "v_mfma_f32_16x16x32_bf16 v[32:35], a[224:227], v[80:83], v[32:35]\n\t" "v_mfma_f32_16x16x32_bf16 v[36:39], a[228:231], v[80:83], v[36:39]\n\t" \
   "v_mfma_f32_16x16x32_bf16 v[40:43], a[232:235], v[80:83], v[40:43]\n\t" "v_mfma_f32_16x16x32_bf16 v[44:47], a[236:239], v[80:83], v[44:47]\n\t" \
   "v_mfma_f32_16x16x32_bf16 v[32:35], a[240:243], v[80:83], v[32:35]\n\t" "v_mfma_f32_16x16x32_bf16 v[36:39], a[244:247], v[80:83], v[36:39]\n\t" \
   "v_mfma_f32_16x16x32_bf16 v[40:43], a[248:251], v[80:83], v[40:43]\n\t" "v_mfma_f32_16x16x32_bf16 v[44:47], a[252:255], v[80:83], v[44:47]\n\t" \
   "v_mfma_f32_16x16x32_bf16 v[32:35], v[192:195], v[80:83], v[32:35]\n\t" "v_mfma_f32_16x16x32_bf16 v[36:39], v[196:199], v[80:83], v[36:39]\n\t" \
   "v_mfma_f32_16x16x32_bf16 v[40:43], v[200:203], v[80:83], v[40:43]\n\t" "v_mfma_f32_16x16x32_bf16 v[44:47], v[204:207], v[80:83], v[44:47]\n\t" \
   "v_mfma_f32_16x16x32_bf16 v[32:35], v[208:211], v[80:83], v[32:35]\n\t" "v_mfma_f32_16x16x32_bf16 v[36:39], v[212:215], v[80:83], v[36:39]\n\t" \
   "v_mfma_f32_16x16x32_bf16 v[40:43], v[216:219], v[80:83], v[40:43]\n\t" "v_mfma_f32_16x16x32_bf16 v[44:47], v[220:223], v[80:83], v[44:47]\n\t" \
   "v_mfma_f32_16x16x32_bf16 v[32:35], v[224:227], v[80:83], v[32:35]\n\t" "v_mfma_f32_16x16x32_bf16 v[36:39], v[228:231], v[80:83], v[36:39]\n\t" \
   "v_mfma_f32_16x16x32_bf16 v[40:43], v[232:235], v[80:83], v[40:43]\n\t" "v_mfma_f32_16x16x32_bf16 v[44:47], v[236:239], v[80:83], v[44:47]\n\t" \
   "v_mfma_f32_16x16x32_bf16 v[32:35], v[240:243], v[80:83], v[32:35]\n\t" "v_mfma_f32_16x16x32_bf16 v[36:39], v[244:247], v[80:83], v[36:39]\n\t" \
   "v_mfma_f32_16x16x32_bf16 v[40:43], v[248:251], v[80:83], v[40:43]\n\t" "v_mfma_f32_16x16x32_bf16 v[44:47], v[252:255], v[80:83], v[44:47]\n\t" \
   "s_mov_b32 s40, 0\n\t" "s_mov_b32 s41, 0\n\t" "s_mov_b32 s42, 0\n\t" "s_mov_b32 s43, 44032\n\t" "v_mov_b32 v28, v22\n\t" "v_mov_b32 v29, v23\n\t" \
   "s_mov_b32 s45, 2\n\t" "s_mov_b32 s46, 88064\n\t" "s_waitcnt vmcnt(11)\n\t" "s_barrier\n\t" "s_lshr_b32 s48, s45, 1\n\t" \
   "s_bitcmp1_b32 s44, s48\n\t" "s_cbranch_scc0 172f\n\t" "72:\n\t" "s_mul_i32 s52, s45, 1536\n\t" "s_mul_i32 s51, s45, 40960\n\t" \
   "s_add_u32 m0, s46, %[ldx]\n\t" "s_mov_b32 s53, s46\n\t" "buffer_load_dwordx4 v16, %[rs], s51 offen offset:0 sc1 lds\n\t" \
   "ds_read_b128 v[80:83], v28 offset:0\n\t" "ds_read_b128 v[84:87], v28 offset:1024\n\t" "ds_read_b128 v[88:91], v28 offset:2048\n\t" \
   "ds_read_b128 v[92:95], v28 offset:3072\n\t" "1:\n\t" "v_mov_b32 v26, v28\n\t" "v_mov_b32 v27, v29\n\t" "s_waitcnt lgkmcnt(2)\n\t" \
   "v_mfma_f32_16x16x32_bf16 v[48:51], a[0:3], v[80:83], 0\n\t" "ds_read_b128 v[112:115], v27 offset:0\n\t" \
   "v_mfma_f32_16x16x32_bf16 v[52:55], a[4:7], v[80:83], 0\n\t" "ds_read_b128 v[96:99], v26 offset:4096\n\t" \
   "v_mfma_f32_16x16x32_bf16 v[56:59], a[8:11], v[80:83], 0\n\t" "ds_read_b128 v[116:119], v27 offset:16\n\t" \
   "v_mfma_f32_16x16x32_bf16 v[60:63], a[12:15], v[80:83], 0\n\t" "v_fmac_f32 v64, v32, v12\n\t" "v_fmac_f32 v65, v33, v12\n\t" \
   "v_mfma_f32_16x16x32_bf16 v[48:51], a[16:19], v[84:87], v[48:51]\n\t" "v_fmac_f32 v66, v34, v12\n\t" "v_fmac_f32 v67, v35, v12\n\t" \
   "v_mfma_f32_16x16x32_bf16 v[52:55], a[20:23], v[84:87], v[52:55]\n\t" "ds_read_b128 v[100:103], v26 offset:5120\n\t" \
   "v_mfma_f32_16x16x32_bf16 v[56:59], a[24:27], v[84:87], v[56:59]\n\t" "v_fmac_f32 v68, v36, v12\n\t" "v_fmac_f32 v69, v37, v12\n\t" \
   "v_mfma_f32_16x16x32_bf16 v[60:63], a[28:31], v[84:87], v[60:63]\n\t" "ds_read_b64 v[120:121], v27 offset:32\n\t" "s_waitcnt lgkmcnt(5)\n\t" \
   "v_mfma_f32_16x16x32_bf16 v[48:51], a[32:35], v[88:91], v[48:51]\n\t" "ds_read_b128 v[122:125], v27 offset:768\n\t" \
   "v_mfma_f32_16x16x32_bf16 v[52:55], a[36:39], v[88:91], v[52:55]\n\t" "ds_read_b128 v[104:107], v26 offset:6144\n\t" \
   "ds_read_b128 v[126:129], v27 offset:784\n\t" "v_mfma_f32_16x16x32_bf16 v[56:59], a[40:43], v[88:91], v[56:59]\n\t" \
   "ds_read_b64 v[130:131], v27 offset:800\n\t" "v_mfma_f32_16x16x32_bf16 v[60:63], a[44:47], v[88:91], v[60:63]\n\t" "v_fmac_f32 v70, v38, v12\n\t" \
   "v_fmac_f32 v71, v39, v12\n\t" "v_mfma_f32_16x16x32_bf16 v[48:51], a[48:51], v[92:95], v[48:51]\n\t" "v_fmac_f32 v72, v40, v12\n\t" \
   "v_fmac_f32 v73, v41, v12\n\t" "v_mfma_f32_16x16x32_bf16 v[52:55], a[52:55], v[92:95], v[52:55]\n\t" "ds_read_b128 v[108:111], v26 offset:7168\n\t" \
   "v_mfma_f32_16x16x32_bf16 v[56:59], a[56:59], v[92:95], v[56:59]\n\t" "v_fmac_f32 v74, v42, v12\n\t" "v_fmac_f32 v75, v43, v12\n\t" \
   "v_mfma_f32_16x16x32_bf16 v[60:63], a[60:63], v[92:95], v[60:63]\n\t" "v_fmac_f32 v76, v44, v12\n\t" "s_waitcnt lgkmcnt(6)\n\t" \
   "v_mfma_f32_16x16x32_bf16 v[48:51], a[64:67], v[96:99], v[48:51]\n\t" "v_fmac_f32 v77, v45, v12\n\t" "v_fmac_f32 v78, v46, v12\n\t" \
   "v_mfma_f32_16x16x32_bf16 v[52:55], a[68:71], v[96:99], v[52:55]\n\t" "ds_read_b128 v[80:83], v26 offset:8192\n\t" \
   "v_mfma_f32_16x16x32_bf16 v[56:59], a[72:75], v[96:99], v[56:59]\n\t" "v_fmac_f32 v79, v47, v12\n\t" "v_mul_f32 v64, v64, v132\n\t" \
   "v_mfma_f32_16x16x32_bf16 v[60:63], a[76:79], v[96:99], v[60:63]\n\t" "v_mul_f32 v65, v65, v132\n\t" "v_mul_f32 v66, v66, v132\n\t" \
   "v_mfma_f32_16x16x32_bf16 v[48:51], a[80:83], v[100:103], v[48:51]\n\t" "v_mul_f32 v67, v67, v132\n\t" "v_mul_f32 v68, v68, v132\n\t" \
   "v_mfma_f32_16x16x32_bf16 v[52:55], a[84:87], v[100:103], v[52:55]\n\t" "ds_read_b128 v[84:87], v26 offset:9216\n\t" \
   "v_mfma_f32_16x16x32_bf16 v[56:59], a[88:91], v[100:103], v[56:59]\n\t" "v_mul_f32 v69, v69, v132\n\t" "v_mul_f32 v70, v70, v132\n\t" \
   "v_mfma_f32_16x16x32_bf16 v[60:63], a[92:95], v[100:103], v[60:63]\n\t" "v_mul_f32 v71, v71, v132\n\t" "s_waitcnt lgkmcnt(2)\n\t" \
   "v_mfma_f32_16x16x32_bf16 v[48:51], a[96:99], v[104:107], v[48:51]\n\t" "v_mul_f32 v72, v72, v132\n\t" "v_mul_f32 v73, v73, v132\n\t" \
   "v_mfma_f32_16x16x32_bf16 v[52:55], a[100:103], v[104:107], v[52:55]\n\t" "ds_read_b128 v[88:91], v26 offset:10240\n\t" \
   "v_mfma_f32_16x16x32_bf16 v[56:59], a[104:107], v[104:107], v[56:59]\n\t" "v_mul_f32 v74, v74, v132\n\t" "v_mul_f32 v75, v75, v132\n\t" \
   "v_mfma_f32_16x16x32_bf16 v[60:63], a[108:111], v[104:107], v[60:63]\n\t" "v_mul_f32 v76, v76, v132\n\t" "v_mul_f32 v77, v77, v132\n\t" \
   "v_mfma_f32_16x16x32_bf16 v[48:51], a[112:115], v[108:111], v[48:51]\n\t" "v_mul_f32 v78, v78, v132\n\t" "v_mul_f32 v79, v79, v132\n\t" \
   "v_mfma_f32_16x16x32_bf16 v[52:55], a[116:119], v[108:111], v[52:55]\n\t" "ds_read_b128 v[92:95], v26 offset:11264\n\t" \
   "v_mfma_f32_16x16x32_bf16 v[56:59], a[120:123], v[108:111], v[56:59]\n\t" "v_cvt_pk_bf16_f32 v136, v64, v65\n\t" \
   "v_cvt_pk_bf16_f32 v137, v66, v67\n\t" "v_mfma_f32_16x16x32_bf16 v[60:63], a[124:127], v[108:111], v[60:63]\n\t" \
   "v_cvt_pk_bf16_f32 v138, v68, v69\n\t" "s_waitcnt lgkmcnt(2)\n\t" "v_mfma_f32_16x16x32_bf16 v[48:51], a[128:131], v[80:83], v[48:51]\n\t" \
   "v_cvt_pk_bf16_f32 v139, v70, v71\n\t" "v_cvt_pk_bf16_f32 v140, v72, v73\n\t" \
   "v_mfma_f32_16x16x32_bf16 v[52:55], a[132:135], v[80:83], v[52:55]\n\t" "ds_read_b128 v[96:99], v26 offset:12288\n\t" \
   "v_mfma_f32_16x16x32_bf16 v[56:59], a[136:139], v[80:83], v[56:59]\n\t" "v_cvt_pk_bf16_f32 v141, v74, v75\n\t" \
   "v_cvt_pk_bf16_f32 v142, v76, v77\n\t" "v_mfma_f32_16x16x32_bf16 v[60:63], a[140:143], v[80:83], v[60:63]\n\t" \
   "v_cvt_pk_bf16_f32 v143, v78, v79\n\t" "s_lshl_b32 s47, s41, 13\n\t" "v_mfma_f32_16x16x32_bf16 v[48:51], a[144:147], v[84:87], v[48:51]\n\t" \
   "s_add_i32 s47, s47, -4096\n\t" "s_max_i32 s47, s47, 0\n\t" "v_mfma_f32_16x16x32_bf16 v[52:55], a[148:151], v[84:87], v[52:55]\n\t" \
   "ds_read_b128 v[100:103], v26 offset:13312\n\t" "v_mfma_f32_16x16x32_bf16 v[56:59], a[152:155], v[84:87], v[56:59]\n\t" "s_cmp_eq_u32 s40, 0\n\t" \
   "s_cselect_b32 s48, 0x7ffffff0, 0\n\t" "v_mfma_f32_16x16x32_bf16 v[60:63], a[156:159], v[84:87], v[60:63]\n\t" "v_or_b32 v30, s48, v21\n\t" \
   "s_waitcnt lgkmcnt(2)\n\t" "v_mfma_f32_16x16x32_bf16 v[48:51], a[160:163], v[88:91], v[48:51]\n\t" "s_add_u32 s48, s47, %[qf]\n\t" \
   "v_add_u32 v28, s43, v22\n\t" "v_mfma_f32_16x16x32_bf16 v[52:55], a[164:167], v[88:91], v[52:55]\n\t" \
   "ds_read_b128 v[104:107], v26 offset:14336\n\t" "buffer_store_dwordx4 v[136:139], v30, %[rs], s48 offen sc1\n\t" \
   "v_mfma_f32_16x16x32_bf16 v[56:59], a[168:171], v[88:91], v[56:59]\n\t" "s_add_u32 s48, s47, %[kf]\n\t" "v_add_u32 v29, s43, v23\n\t" \
   "v_mfma_f32_16x16x32_bf16 v[60:63], a[172:175], v[88:91], v[60:63]\n\t" "buffer_store_dwordx4 v[140:143], v30, %[rs], s48 offen sc1\n\t" \
   "v_mfma_f32_16x16x32_bf16 v[48:51], a[176:179], v[92:95], v[48:51]\n\t" "v_add_f32 v112, v112, v114\n\t" \
   "v_mfma_f32_16x16x32_bf16 v[52:55], a[180:183], v[92:95], v[52:55]\n\t" "ds_read_b128 v[108:111], v26 offset:15360\n\t" \
   "v_mfma_f32_16x16x32_bf16 v[56:59], a[184:187], v[92:95], v[56:59]\n\t" "v_add_f32 v113, v113, v115\n\t" "v_add_f32 v116, v116, v118\n\t" \
   "v_mfma_f32_16x16x32_bf16 v[60:63], a[188:191], v[92:95], v[60:63]\n\t" "v_add_f32 v117, v117, v119\n\t" "s_waitcnt lgkmcnt(2)\n\t" \
   "v_mfma_f32_16x16x32_bf16 v[48:51], a[192:195], v[96:99], v[48:51]\n\t" "v_add_f32 v112, v112, v120\n\t" "v_add_f32 v113, v113, v121\n\t" \
   "v_mfma_f32_16x16x32_bf16 v[52:55], a[196:199], v[96:99], v[52:55]\n\t" "ds_read_b128 v[80:83], v26 offset:16384\n\t" \
   "v_mfma_f32_16x16x32_bf16 v[56:59], a[200:203], v[96:99], v[56:59]\n\t" "v_add_f32 v112, v112, v116\n\t" "v_add_f32 v113, v113, v117\n\t" \
   "v_mfma_f32_16x16x32_bf16 v[60:63], a[204:207], v[96:99], v[60:63]\n\t" "v_mul_f32 v112, 0xbacccccd, v112\n\t" \
   "v_mul_f32 v113, 0x3acccccd, v113\n\t" "v_mfma_f32_16x16x32_bf16 v[48:51], a[208:211], v[100:103], v[48:51]\n\t" \
   "v_fma_f32 v113, -v112, v112, v113\n\t" "v_max_f32 v113, 0, v113\n\t" "v_mfma_f32_16x16x32_bf16 v[52:55], a[212:215], v[100:103], v[52:55]\n\t" \
   "ds_read_b128 v[84:87], v26 offset:17408\n\t" "v_mfma_f32_16x16x32_bf16 v[56:59], a[216:219], v[100:103], v[56:59]\n\t" \
   "v_cvt_pk_bf16_f32 v113, v113, v112\n\t" "v_and_b32 v8, 0xffff0000, v113\n\t" \
   "v_mfma_f32_16x16x32_bf16 v[60:63], a[220:223], v[100:103], v[60:63]\n\t" "v_lshlrev_b32 v113, 16, v113\n\t" "s_waitcnt lgkmcnt(2)\n\t" \
   "v_mfma_f32_16x16x32_bf16 v[48:51], a[224:227], v[104:107], v[48:51]\n\t" "v_add_f32 v113, 0x3727c5ac, v113\n\t" \
   "v_cvt_pk_bf16_f32 v113, v113, v113\n\t" "v_mfma_f32_16x16x32_bf16 v[52:55], a[228:231], v[104:107], v[52:55]\n\t" \
   "ds_read_b128 v[88:91], v26 offset:18432\n\t" "v_mfma_f32_16x16x32_bf16 v[56:59], a[232:235], v[104:107], v[56:59]\n\t" \
   "v_lshlrev_b32 v113, 16, v113\n\t" "v_mfma_f32_16x16x32_bf16 v[60:63], a[236:239], v[104:107], v[60:63]\n\t" "v_sqrt_f32 v113, v113\n\t" \
   "v_mfma_f32_16x16x32_bf16 v[48:51], a[240:243], v[108:111], v[48:51]\n\t" "buffer_load_dwordx4 v16, %[rs], s51 offen offset:1024 sc1 lds\n\t" \
   "v_mfma_f32_16x16x32_bf16 v[52:55], a[244:247], v[108:111], v[52:55]\n\t" "ds_read_b128 v[92:95], v26 offset:19456\n\t" \
   "v_mfma_f32_16x16x32_bf16 v[56:59], a[248:251], v[108:111], v[56:59]\n\t" "v_add_f32 v122, v122, v124\n\t" \
   "v_mfma_f32_16x16x32_bf16 v[60:63], a[252:255], v[108:111], v[60:63]\n\t" "s_nop 0\n\t" "s_waitcnt lgkmcnt(2)\n\t" \
   "v_mfma_f32_16x16x32_bf16 v[48:51], v[192:195], v[80:83], v[48:51]\n\t" "v_add_f32 v123, v123, v125\n\t" "v_add_f32 v126, v126, v128\n\t" \
   "v_mfma_f32_16x16x32_bf16 v[52:55], v[196:199], v[80:83], v[52:55]\n\t" "ds_read_b128 v[96:99], v26 offset:20480\n\t" \
   "v_mfma_f32_16x16x32_bf16 v[56:59], v[200:203], v[80:83], v[56:59]\n\t" "v_cvt_pk_bf16_f32 v113, v113, v113\n\t" "v_add_f32 v127, v127, v129\n\t" \
   "v_mfma_f32_16x16x32_bf16 v[60:63], v[204:207], v[80:83], v[60:63]\n\t" "buffer_load_dwordx4 v16, %[rs], s51 offen offset:2048 sc1 lds\n\t" \
   "v_mfma_f32_16x16x32_bf16 v[48:51], v[208:211], v[84:87], v[48:51]\n\t" "v_add_f32 v122, v122, v130\n\t" "v_add_f32 v123, v123, v131\n\t" \
   "v_mfma_f32_16x16x32_bf16 v[52:55], v[212:215], v[84:87], v[52:55]\n\t" "ds_read_b128 v[100:103], v26 offset:21504\n\t" \
   "v_mfma_f32_16x16x32_bf16 v[56:59], v[216:219], v[84:87], v[56:59]\n\t" "v_lshlrev_b32 v113, 16, v113\n\t" "v_add_f32 v122, v122, v126\n\t" \
   "v_mfma_f32_16x16x32_bf16 v[60:63], v[220:223], v[84:87], v[60:63]\n\t" "v_add_f32 v123, v123, v127\n\t" "s_waitcnt lgkmcnt(2)\n\t" \
   "v_mfma_f32_16x16x32_bf16 v[48:51], v[224:227], v[88:91], v[48:51]\n\t" "v_rcp_f32 v10, v113\n\t" \
   "v_mfma_f32_16x16x32_bf16 v[52:55], v[228:231], v[88:91], v[52:55]\n\t" "ds_read_b128 v[104:107], v26 offset:22528\n\t" \
   "v_mfma_f32_16x16x32_bf16 v[56:59], v[232:235], v[88:91], v[56:59]\n\t" "v_mul_f32 v122, 0xbacccccd, v122\n\t" \
   "v_mul_f32 v123, 0x3acccccd, v123\n\t" "v_mfma_f32_16x16x32_bf16 v[60:63], v[236:239], v[88:91], v[60:63]\n\t" \
   "buffer_load_dwordx4 v16, %[rs], s51 offen offset:3072 sc1 lds\n\t" "s_add_u32 m0, m0, 4096\n\t" "s_add_u32 s51, s51, 4096\n\t" \
   "v_mfma_f32_16x16x32_bf16 v[48:51], v[240:243], v[92:95], v[48:51]\n\t" "v_fma_f32 v123, -v122, v122, v123\n\t" "v_max_f32 v123, 0, v123\n\t" \
   "v_mfma_f32_16x16x32_bf16 v[52:55], v[244:247], v[92:95], v[52:55]\n\t" "ds_read_b128 v[108:111], v26 offset:23552\n\t" \
   "v_mfma_f32_16x16x32_bf16 v[56:59], v[248:251], v[92:95], v[56:59]\n\t" "v_cvt_pk_bf16_f32 v123, v123, v122\n\t" \
   "v_and_b32 v12, 0xffff0000, v123\n\t" "v_mfma_f32_16x16x32_bf16 v[60:63], v[252:255], v[92:95], v[60:63]\n\t" "v_lshlrev_b32 v123, 16, v123\n\t" \
   "s_waitcnt lgkmcnt(2)\n\t" "v_mfma_f32_16x16x32_bf16 v[64:67], a[0:3], v[96:99], 0\n\t" "v_add_f32 v123, 0x3727c5ac, v123\n\t" \
   "v_cvt_pk_bf16_f32 v123, v123, v123\n\t" "v_mfma_f32_16x16x32_bf16 v[68:71], a[4:7], v[96:99], 0\n\t" "ds_read_b128 v[80:83], v26 offset:24576\n\t" \
   "v_mfma_f32_16x16x32_bf16 v[72:75], a[8:11], v[96:99], 0\n\t" "buffer_load_dwordx4 v16, %[rs], s51 offen offset:0 sc1 lds\n\t" \
   "v_mfma_f32_16x16x32_bf16 v[76:79], a[12:15], v[96:99], 0\n\t" "v_fmac_f32 v48, v32, v8\n\t" "v_fmac_f32 v49, v33, v8\n\t" \
   "v_mfma_f32_16x16x32_bf16 v[64:67], a[16:19], v[100:103], v[64:67]\n\t" "v_fmac_f32 v50, v34, v8\n\t" "v_fmac_f32 v51, v35, v8\n\t" \
   "v_mfma_f32_16x16x32_bf16 v[68:71], a[20:23], v[100:103], v[68:71]\n\t" "ds_read_b128 v[84:87], v26 offset:25600\n\t" \
   "v_mfma_f32_16x16x32_bf16 v[72:75], a[24:27], v[100:103], v[72:75]\n\t" "s_cmp_lt_i32 s40, 1\n\t" "s_cselect_b32 s49, 0x7ffffff0, 0\n\t" \
   "v_mfma_f32_16x16x32_bf16 v[76:79], a[28:31], v[100:103], v[76:79]\n\t" "v_or_b32 v9, s49, v20\n\t" "s_waitcnt lgkmcnt(2)\n\t" \
   "v_mfma_f32_16x16x32_bf16 v[64:67], a[32:35], v[104:107], v[64:67]\n\t" "s_sub_i32 s49, s41, 1\n\t" "s_max_i32 s49, s49, 0\n\t" \
   "v_mfma_f32_16x16x32_bf16 v[68:71], a[36:39], v[104:107], v[68:71]\n\t" "ds_read_b128 v[88:91], v26 offset:26624\n\t" \
   "v_mfma_f32_16x16x32_bf16 v[72:75], a[40:43], v[104:107], v[72:75]\n\t" "s_lshl_b32 s49, s49, 12\n\t" "s_waitcnt vmcnt(4)\n\t" \
   "v_mfma_f32_16x16x32_bf16 v[76:79], a[44:47], v[104:107], v[76:79]\n\t" "buffer_store_dword v31, v9, %[rs], s49 offen sc1\n\t" \
   "v_mfma_f32_16x16x32_bf16 v[64:67], a[48:51], v[108:111], v[64:67]\n\t" "v_fmac_f32 v52, v36, v8\n\t" "v_fmac_f32 v53, v37, v8\n\t" \
   "v_mfma_f32_16x16x32_bf16 v[68:71], a[52:55], v[108:111], v[68:71]\n\t" "ds_read_b128 v[92:95], v26 offset:27648\n\t" \
   "v_mfma_f32_16x16x32_bf16 v[72:75], a[56:59], v[108:111], v[72:75]\n\t" "v_fmac_f32 v54, v38, v8\n\t" "v_fmac_f32 v55, v39, v8\n\t" \
   "v_mfma_f32_16x16x32_bf16 v[76:79], a[60:63], v[108:111], v[76:79]\n\t" "v_fmac_f32 v56, v40, v8\n\t" "s_waitcnt lgkmcnt(2)\n\t" \
   "v_mfma_f32_16x16x32_bf16 v[64:67], a[64:67], v[80:83], v[64:67]\n\t" "buffer_load_dwordx4 v16, %[rs], s51 offen offset:1024 sc1 lds\n\t" \
   "v_mfma_f32_16x16x32_bf16 v[68:71], a[68:71], v[80:83], v[68:71]\n\t" "ds_read_b128 v[96:99], v26 offset:28672\n\t" \
   "v_mfma_f32_16x16x32_bf16 v[72:75], a[72:75], v[80:83], v[72:75]\n\t" "v_fmac_f32 v57, v41, v8\n\t" "v_fmac_f32 v58, v42, v8\n\t" \
   "v_mfma_f32_16x16x32_bf16 v[76:79], a[76:79], v[80:83], v[76:79]\n\t" "v_fmac_f32 v59, v43, v8\n\t" "v_fmac_f32 v60, v44, v8\n\t" \
   "v_mfma_f32_16x16x32_bf16 v[64:67], a[80:83], v[84:87], v[64:67]\n\t" "v_fmac_f32 v61, v45, v8\n\t" "v_fmac_f32 v62, v46, v8\n\t" \
   "v_mfma_f32_16x16x32_bf16 v[68:71], a[84:87], v[84:87], v[68:71]\n\t" "ds_read_b128 v[100:103], v26 offset:29696\n\t" \
   "v_mfma_f32_16x16x32_bf16 v[72:75], a[88:91], v[84:87], v[72:75]\n\t" "v_fmac_f32 v63, v47, v8\n\t" "v_mul_f32 v48, v48, v10\n\t" \
   "v_mfma_f32_16x16x32_bf16 v[76:79], a[92:95], v[84:87], v[76:79]\n\t" "v_mul_f32 v49, v49, v10\n\t" "s_waitcnt lgkmcnt(2)\n\t" \
   "v_mfma_f32_16x16x32_bf16 v[64:67], a[96:99], v[88:91], v[64:67]\n\t" "buffer_load_dwordx4 v16, %[rs], s51 offen offset:2048 sc1 lds\n\t" \
   "v_mfma_f32_16x16x32_bf16 v[68:71], a[100:103], v[88:91], v[68:71]\n\t" "ds_read_b128 v[104:107], v26 offset:30720\n\t" \
   "v_mfma_f32_16x16x32_bf16 v[72:75], a[104:107], v[88:91], v[72:75]\n\t" "v_mul_f32 v50, v50, v10\n\t" "v_mul_f32 v51, v51, v10\n\t" \
   "v_mfma_f32_16x16x32_bf16 v[76:79], a[108:111], v[88:91], v[76:79]\n\t" "v_mul_f32 v52, v52, v10\n\t" "v_mul_f32 v53, v53, v10\n\t" \
   "v_mfma_f32_16x16x32_bf16 v[64:67], a[112:115], v[92:95], v[64:67]\n\t" "v_mul_f32 v54, v54, v10\n\t" "v_mul_f32 v55, v55, v10\n\t" \
   "v_mfma_f32_16x16x32_bf16 v[68:71], a[116:119], v[92:95], v[68:71]\n\t" "ds_read_b128 v[108:111], v26 offset:31744\n\t" \
   "v_mfma_f32_16x16x32_bf16 v[72:75], a[120:123], v[92:95], v[72:75]\n\t" "v_mul_f32 v56, v56, v10\n\t" "v_mul_f32 v57, v57, v10\n\t" \
   "v_mfma_f32_16x16x32_bf16 v[76:79], a[124:127], v[92:95], v[76:79]\n\t" "v_mul_f32 v58, v58, v10\n\t" "s_waitcnt lgkmcnt(2)\n\t" \
   "v_mfma_f32_16x16x32_bf16 v[64:67], a[128:131], v[96:99], v[64:67]\n\t" "buffer_load_dwordx4 v16, %[rs], s51 offen offset:3072 sc1 lds\n\t" \
   "s_add_u32 m0, m0, 4096\n\t" "s_add_u32 s51, s51, 4096\n\t" "v_mfma_f32_16x16x32_bf16 v[68:71], a[132:135], v[96:99], v[68:71]\n\t" \
   "ds_read_b128 v[80:83], v26 offset:32768\n\t" "v_mfma_f32_16x16x32_bf16 v[72:75], a[136:139], v[96:99], v[72:75]\n\t" "v_mul_f32 v59, v59, v10\n\t" \
   "v_mul_f32 v60, v60, v10\n\t" "v_mfma_f32_16x16x32_bf16 v[76:79], a[140:143], v[96:99], v[76:79]\n\t" "v_mul_f32 v61, v61, v10\n\t" \
   "v_mul_f32 v62, v62, v10\n\t" "v_mfma_f32_16x16x32_bf16 v[64:67], a[144:147], v[100:103], v[64:67]\n\t" "v_mul_f32 v63, v63, v10\n\t" \
   "v_cvt_pk_bf16_f32 v136, v48, v49\n\t" "v_mfma_f32_16x16x32_bf16 v[68:71], a[148:151], v[100:103], v[68:71]\n\t" \
   "ds_read_b128 v[84:87], v26 offset:33792\n\t" "v_mfma_f32_16x16x32_bf16 v[72:75], a[152:155], v[100:103], v[72:75]\n\t" \
   "v_cvt_pk_bf16_f32 v137, v50, v51\n\t" "v_cvt_pk_bf16_f32 v138, v52, v53\n\t" \
   "v_mfma_f32_16x16x32_bf16 v[76:79], a[156:159], v[100:103], v[76:79]\n\t" "v_cvt_pk_bf16_f32 v139, v54, v55\n\t" "s_waitcnt lgkmcnt(2)\n\t" \
   "v_mfma_f32_16x16x32_bf16 v[64:67], a[160:163], v[104:107], v[64:67]\n\t" "buffer_load_dwordx4 v16, %[rs], s51 offen offset:0 sc1 lds\n\t" \
   "v_mfma_f32_16x16x32_bf16 v[68:71], a[164:167], v[104:107], v[68:71]\n\t" "ds_read_b128 v[88:91], v26 offset:34816\n\t" \
   "v_mfma_f32_16x16x32_bf16 v[72:75], a[168:171], v[104:107], v[72:75]\n\t" "v_cvt_pk_bf16_f32 v140, v56, v57\n\t" \
   "v_cvt_pk_bf16_f32 v141, v58, v59\n\t" "v_mfma_f32_16x16x32_bf16 v[76:79], a[172:175], v[104:107], v[76:79]\n\t" \
   "v_cvt_pk_bf16_f32 v142, v60, v61\n\t" "v_cvt_pk_bf16_f32 v143, v62, v63\n\t" \
   "v_mfma_f32_16x16x32_bf16 v[64:67], a[176:179], v[108:111], v[64:67]\n\t" "v_lshlrev_b32 v123, 16, v123\n\t" "s_lshl_b32 s47, s41, 13\n\t" \
   "v_mfma_f32_16x16x32_bf16 v[68:71], a[180:183], v[108:111], v[68:71]\n\t" "ds_read_b128 v[92:95], v26 offset:35840\n\t" \
   "v_mfma_f32_16x16x32_bf16 v[72:75], a[184:187], v[108:111], v[72:75]\n\t" "buffer_load_dwordx4 v16, %[rs], s51 offen offset:1024 sc1 lds\n\t" \
   "s_add_u32 m0, s53, %[ld1]\n\t" "s_nop 0\n\t" "v_mfma_f32_16x16x32_bf16 v[76:79], a[188:191], v[108:111], v[76:79]\n\t" "s_add_i32 s47, s47, 0\n\t" \
   "s_waitcnt lgkmcnt(2)\n\t" "v_mfma_f32_16x16x32_bf16 v[64:67], a[192:195], v[80:83], v[64:67]\n\t" "v_sqrt_f32 v123, v123\n\t" \
   "v_mfma_f32_16x16x32_bf16 v[68:71], a[196:199], v[80:83], v[68:71]\n\t" "ds_read_b128 v[96:99], v26 offset:36864\n\t" \
   "v_mfma_f32_16x16x32_bf16 v[72:75], a[200:203], v[80:83], v[72:75]\n\t" "s_add_u32 s48, s47, %[qf]\n\t" "s_nop 0\n\t" \
   "v_mfma_f32_16x16x32_bf16 v[76:79], a[204:207], v[80:83], v[76:79]\n\t" "buffer_store_dwordx4 v[136:139], v21, %[rs], s48 offen sc1\n\t" \
   "v_mfma_f32_16x16x32_bf16 v[64:67], a[208:211], v[84:87], v[64:67]\n\t" "v_cvt_pk_bf16_f32 v123, v123, v123\n\t" "v_lshlrev_b32 v123, 16, v123\n\t" \
   "v_mfma_f32_16x16x32_bf16 v[68:71], a[212:215], v[84:87], v[68:71]\n\t" "ds_read_b128 v[100:103], v26 offset:37888\n\t" \
   "v_mfma_f32_16x16x32_bf16 v[72:75], a[216:219], v[84:87], v[72:75]\n\t" "buffer_load_dwordx4 v17, %[rs], s52 offen sc1 lds\n\t" \
   "v_mfma_f32_16x16x32_bf16 v[76:79], a[220:223], v[84:87], v[76:79]\n\t" "s_add_u32 s48, s47, %[kf]\n\t" "s_waitcnt lgkmcnt(2)\n\t" \
   "v_mfma_f32_16x16x32_bf16 v[64:67], a[224:227], v[88:91], v[64:67]\n\t" "v_rcp_f32 v132, v123\n\t" \
   "v_mfma_f32_16x16x32_bf16 v[68:71], a[228:231], v[88:91], v[68:71]\n\t" "ds_read_b128 v[104:107], v26 offset:38912\n\t" \
   "v_mfma_f32_16x16x32_bf16 v[72:75], a[232:235], v[88:91], v[72:75]\n\t" "buffer_store_dwordx4 v[140:143], v21, %[rs], s48 offen sc1\n\t" \
   "v_mfma_f32_16x16x32_bf16 v[76:79], a[236:239], v[88:91], v[76:79]\n\t" "v_mfma_f32_16x16x32_bf16 v[64:67], a[240:243], v[92:95], v[64:67]\n\t" \
   "v_mfma_f32_16x16x32_bf16 v[68:71], a[244:247], v[92:95], v[68:71]\n\t" "ds_read_b128 v[108:111], v26 offset:39936\n\t" \
   "v_mfma_f32_16x16x32_bf16 v[72:75], a[248:251], v[92:95], v[72:75]\n\t" "v_mfma_f32_16x16x32_bf16 v[76:79], a[252:255], v[92:95], v[76:79]\n\t" \
   "s_waitcnt vmcnt(13) lgkmcnt(0)\n\t" "s_barrier\n\t" "v_mfma_f32_16x16x32_bf16 v[64:67], v[192:195], v[96:99], v[64:67]\n\t" \
   "s_add_u32 s45, s40, 3\n\t" "s_min_u32 s45, s45, 63\n\t" "v_mfma_f32_16x16x32_bf16 v[68:71], v[196:199], v[96:99], v[68:71]\n\t" \
   "ds_read_b128 v[80:83], v28 offset:0\n\t" "v_mfma_f32_16x16x32_bf16 v[72:75], v[200:203], v[96:99], v[72:75]\n\t" "s_mov_b32 s46, s42\n\t" \
   "v_mfma_f32_16x16x32_bf16 v[76:79], v[204:207], v[96:99], v[76:79]\n\t" "s_lshr_b32 s48, s45, 1\n\t" "s_bitcmp1_b32 s44, s48\n\t" \
   "s_cbranch_scc0 174f\n\t" "74:\n\t" "v_mfma_f32_16x16x32_bf16 v[64:67], v[208:211], v[100:103], v[64:67]\n\t" "s_mul_i32 s52, s45, 1536\n\t" \
   "s_mul_i32 s51, s45, 40960\n\t" "s_add_u32 m0, s46, %[ldx]\n\t" "s_mov_b32 s53, s46\n\t" \
   "v_mfma_f32_16x16x32_bf16 v[68:71], v[212:215], v[100:103], v[68:71]\n\t" "ds_read_b128 v[84:87], v28 offset:1024\n\t" \
   "v_mfma_f32_16x16x32_bf16 v[72:75], v[216:219], v[100:103], v[72:75]\n\t" "buffer_load_dwordx4 v16, %[rs], s51 offen offset:0 sc1 lds\n\t" \
   "v_mfma_f32_16x16x32_bf16 v[76:79], v[220:223], v[100:103], v[76:79]\n\t" "s_mov_b32 s42, s43\n\t" \
   "v_mfma_f32_16x16x32_bf16 v[64:67], v[224:227], v[104:107], v[64:67]\n\t" "s_add_u32 s43, s43, 44032\n\t" \
   "v_mfma_f32_16x16x32_bf16 v[68:71], v[228:231], v[104:107], v[68:71]\n\t" "ds_read_b128 v[88:91], v28 offset:2048\n\t" \
   "v_mfma_f32_16x16x32_bf16 v[72:75], v[232:235], v[104:107], v[72:75]\n\t" "s_cmp_eq_u32 s43, 132096\n\t" "s_cselect_b32 s43, 0, s43\n\t" \
   "v_mfma_f32_16x16x32_bf16 v[76:79], v[236:239], v[104:107], v[76:79]\n\t" "s_add_u32 s41, s41, 1\n\t" "s_add_u32 s40, s40, 1\n\t" \
   "v_mfma_f32_16x16x32_bf16 v[64:67], v[240:243], v[108:111], v[64:67]\n\t" "v_mfma_f32_16x16x32_bf16 v[68:71], v[244:247], v[108:111], v[68:71]\n\t" \
   "ds_read_b128 v[92:95], v28 offset:3072\n\t" "v_mfma_f32_16x16x32_bf16 v[72:75], v[248:251], v[108:111], v[72:75]\n\t" \
   "v_mfma_f32_16x16x32_bf16 v[76:79], v[252:255], v[108:111], v[76:79]\n\t" "s_cmp_lt_u32 s40, 64\n\t" "s_cbranch_scc1 1b\n\t" "s_nop 7\n\t" \
   "v_fmac_f32 v64, v32, v12\n\t" "v_fmac_f32 v65, v33, v12\n\t" "v_fmac_f32 v66, v34, v12\n\t" "v_fmac_f32 v67, v35, v12\n\t" \
   "v_fmac_f32 v68, v36, v12\n\t" "v_fmac_f32 v69, v37, v12\n\t" "v_fmac_f32 v70, v38, v12\n\t" "v_fmac_f32 v71, v39, v12\n\t" \
   "v_fmac_f32 v72, v40, v12\n\t" "v_fmac_f32 v73, v41, v12\n\t" "v_fmac_f32 v74, v42, v12\n\t" "v_fmac_f32 v75, v43, v12\n\t" \
   "v_fmac_f32 v76, v44, v12\n\t" "v_fmac_f32 v77, v45, v12\n\t" "v_fmac_f32 v78, v46, v12\n\t" "v_fmac_f32 v79, v47, v12\n\t" \
   "v_mul_f32 v64, v64, v132\n\t" "v_mul_f32 v65, v65, v132\n\t" "v_mul_f32 v66, v66, v132\n\t" "v_mul_f32 v67, v67, v132\n\t" \
   "v_mul_f32 v68, v68, v132\n\t" "v_mul_f32 v69, v69, v132\n\t" "v_mul_f32 v70, v70, v132\n\t" "v_mul_f32 v71, v71, v132\n\t" \
   "v_mul_f32 v72, v72, v132\n\t" "v_mul_f32 v73, v73, v132\n\t" "v_mul_f32 v74, v74, v132\n\t" "v_mul_f32 v75, v75, v132\n\t" \
   "v_mul_f32 v76, v76, v132\n\t" "v_mul_f32 v77, v77, v132\n\t" "v_mul_f32 v78, v78, v132\n\t" "v_mul_f32 v79, v79, v132\n\t" \
   "v_cvt_pk_bf16_f32 v136, v64, v65\n\t" "v_cvt_pk_bf16_f32 v137, v66, v67\n\t" "v_cvt_pk_bf16_f32 v138, v68, v69\n\t" \
   "v_cvt_pk_bf16_f32 v139, v70, v71\n\t" "v_cvt_pk_bf16_f32 v140, v72, v73\n\t" "v_cvt_pk_bf16_f32 v141, v74, v75\n\t" \
   "v_cvt_pk_bf16_f32 v142, v76, v77\n\t" "v_cvt_pk_bf16_f32 v143, v78, v79\n\t" "s_lshl_b32 s47, s41, 13\n\t" "s_add_i32 s47, s47, -4096\n\t" \
   "s_max_i32 s47, s47, 0\n\t" "s_cmp_eq_u32 s40, 0\n\t" "s_cselect_b32 s48, 0x7ffffff0, 0\n\t" "v_or_b32 v30, s48, v21\n\t" \
   "s_add_u32 s48, s47, %[qf]\n\t" "buffer_store_dwordx4 v[136:139], v30, %[rs], s48 offen sc1\n\t" "s_add_u32 s48, s47, %[kf]\n\t" \
   "buffer_store_dwordx4 v[140:143], v30, %[rs], s48 offen sc1\n\t" "s_waitcnt vmcnt(0) lgkmcnt(0)\n\t" "s_cmp_lt_i32 s40, 1\n\t" \
   "s_cselect_b32 s47, 0x7ffffff0, 0\n\t" "v_or_b32 v30, s47, v20\n\t" "s_sub_i32 s47, s41, 1\n\t" "s_max_i32 s47, s47, 0\n\t" \
   "s_lshl_b32 s47, s47, 12\n\t" "buffer_store_dword v31, v30, %[rs], s47 offen sc1\n\t" "s_branch 9f\n\t" "172:\n\t" "s_mov_b32 s47, 0\n\t" \
   "buffer_load_dword v30, v19, %[rs], 0 offen sc1\n\t" "s_waitcnt vmcnt(0)\n\t" "v_add_u32 v30, 1, v30\n\t" "v_cmp_lt_u32 vcc, 9, v30\n\t" \
   "s_mov_b32 s44, vcc_lo\n\t" "s_bitcmp1_b32 s44, s48\n\t" "s_cbranch_scc1 72b\n\t" "s_bitset1_b32 s44, s48\n\t" "s_lshl_b32 s48, s48, 7\n\t" \
   "s_add_u32 s48, s48, %[cnt]\n\t" "73:\n\t" "s_sleep 10\n\t" "buffer_load_dword v30, off, %[rs], s48 sc1\n\t" "s_add_u32 s47, s47, 1\n\t" \
   "s_waitcnt vmcnt(0)\n\t" "v_add_u32 v30, 1, v30\n\t" "v_cmp_lt_u32 vcc, 9, v30\n\t" "s_cmp_gt_u32 s47, 0x20000\n\t" "s_cbranch_scc1 72b\n\t" \
   "s_cbranch_vccz 73b\n\t" "s_branch 72b\n\t" "174:\n\t" "s_mov_b32 s47, 0\n\t" "buffer_load_dword v30, v19, %[rs], 0 offen sc1\n\t" \
   "s_waitcnt vmcnt(0)\n\t" "v_add_u32 v30, 1, v30\n\t" "v_cmp_lt_u32 vcc, 9, v30\n\t" "s_mov_b32 s44, vcc_lo\n\t" "s_bitcmp1_b32 s44, s48\n\t" \
   "s_cbranch_scc1 74b\n\t" "s_bitset1_b32 s44, s48\n\t" "s_lshl_b32 s48, s48, 7\n\t" "s_add_u32 s48, s48, %[cnt]\n\t" "75:\n\t" "s_sleep 10\n\t" \
   "buffer_load_dword v30, off, %[rs], s48 sc1\n\t" "s_add_u32 s47, s47, 1\n\t" "s_waitcnt vmcnt(0)\n\t" "v_add_u32 v30, 1, v30\n\t" \
   "v_cmp_lt_u32 vcc, 9, v30\n\t" "s_cmp_gt_u32 s47, 0x20000\n\t" "s_cbranch_scc1 74b\n\t" "s_cbranch_vccz 75b\n\t" "s_branch 74b\n\t" "9:\n\t" \
   "s_waitcnt vmcnt(0)"
#define KQ_CLOB "v8", "v9", "v10", "v11", "v12", "v13", "v14", "v15", "v32", "v33", "v34", "v35", "v36", "v37", "v38", "v39", "v40", "v41", "v42", "v43", "v44", "v45", "v46", "v47", "v48", "v49", "v50", "v51", "v52", "v53", "v54", "v55", "v56", "v57", "v58", "v59", "v60", "v61", "v62", "v63", "v64", "v65", "v66", "v67", "v68", "v69", "v70", "v71", "v72", "v73", "v74", "v75", "v76", "v77", "v78", "v79", "v80", "v81", "v82", "v83", "v84", "v85", "v86", "v87", "v88", "v89", "v90", "v91", "v92", "v93", "v94", "v95", "v96", "v97", "v98", "v99", "v100", "v101", "v102", "v103", "v104", "v105", "v106", "v107", "v108", "v109", "v110", "v111", "v112", "v113", "v114", "v115", "v116", "v117", "v118", "v119", "v120", "v121", "v122", "v123", "v124", "v125", "v126", "v127", "v128", "v129", "v130", "v131", "v132", "v133", "v134", "v135", "v136", "v137", "v138", "v139", "v140", "v141", "v142", "v143", "v144", "v145", "v146", "v147", "v148", "v149", "v150", "v151", "s40", "s41", "s42", "s43", "s44", "s45", "s46", "s47", "s48", "s49", "s50", "s51", "s52", "s53"
#define VU_ASM \
   "s_mov_b32 s44, %[rdy]\n\t" "s_mov_b32 s45, 0\n\t" "s_mov_b32 s46, 0\n\t" "s_cmp_eq_u32 s44, -1\n\t" "s_cbranch_scc1 68f\n\t" \
   "s_mul_i32 s52, s45, 1536\n\t" "s_mul_i32 s51, s45, 40960\n\t" "s_add_u32 m0, s46, %[ldx]\n\t" "s_mov_b32 s53, s46\n\t" \
   "buffer_load_dwordx4 v16, %[rs], s51 offen offset:0 sc1 lds\n\t" "buffer_load_dwordx4 v16, %[rs], s51 offen offset:1024 sc1 lds\n\t" \
   "buffer_load_dwordx4 v16, %[rs], s51 offen offset:2048 sc1 lds\n\t" "buffer_load_dwordx4 v16, %[rs], s51 offen offset:3072 sc1 lds\n\t" \
   "s_add_u32 m0, m0, 4096\n\t" "s_add_u32 s51, s51, 4096\n\t" "buffer_load_dwordx4 v16, %[rs], s51 offen offset:0 sc1 lds\n\t" \
   "buffer_load_dwordx4 v16, %[rs], s51 offen offset:1024 sc1 lds\n\t" "buffer_load_dwordx4 v16, %[rs], s51 offen offset:2048 sc1 lds\n\t" \
   "buffer_load_dwordx4 v16, %[rs], s51 offen offset:3072 sc1 lds\n\t" "s_add_u32 m0, m0, 4096\n\t" "s_add_u32 s51, s51, 4096\n\t" \
   "buffer_load_dwordx4 v16, %[rs], s51 offen offset:0 sc1 lds\n\t" "buffer_load_dwordx4 v16, %[rs], s51 offen offset:1024 sc1 lds\n\t" \
   "s_add_u32 m0, s53, %[ld1]\n\t" "s_nop 0\n\t" "buffer_load_dwordx4 v17, %[rs], s52 offen sc1 lds\n\t" "s_mov_b32 s45, 1\n\t" \
   "s_mov_b32 s46, 44032\n\t" "s_mul_i32 s52, s45, 1536\n\t" "s_mul_i32 s51, s45, 40960\n\t" "s_add_u32 m0, s46, %[ldx]\n\t" "s_mov_b32 s53, s46\n\t" \
   "buffer_load_dwordx4 v16, %[rs], s51 offen offset:0 sc1 lds\n\t" "buffer_load_dwordx4 v16, %[rs], s51 offen offset:1024 sc1 lds\n\t" \
   "buffer_load_dwordx4 v16, %[rs], s51 offen offset:2048 sc1 lds\n\t" "buffer_load_dwordx4 v16, %[rs], s51 offen offset:3072 sc1 lds\n\t" \
   "s_add_u32 m0, m0, 4096\n\t" "s_add_u32 s51, s51, 4096\n\t" "buffer_load_dwordx4 v16, %[rs], s51 offen offset:0 sc1 lds\n\t" \
   "buffer_load_dwordx4 v16, %[rs], s51 offen offset:1024 sc1 lds\n\t" "buffer_load_dwordx4 v16, %[rs], s51 offen offset:2048 sc1 lds\n\t" \
   "buffer_load_dwordx4 v16, %[rs], s51 offen offset:3072 sc1 lds\n\t" "s_add_u32 m0, m0, 4096\n\t" "s_add_u32 s51, s51, 4096\n\t" \
   "buffer_load_dwordx4 v16, %[rs], s51 offen offset:0 sc1 lds\n\t" "buffer_load_dwordx4 v16, %[rs], s51 offen offset:1024 sc1 lds\n\t" \
   "s_add_u32 m0, s53, %[ld1]\n\t" "s_nop 0\n\t" "buffer_load_dwordx4 v17, %[rs], s52 offen sc1 lds\n\t" "68:\n\t" "v_mov_b32 v80, 0x3f803f80\n\t" \
   "v_mov_b32 v81, 0x3f803f80\n\t" "v_mov_b32 v82, 0x3f803f80\n\t" "v_mov_b32 v83, 0x3f803f80\n\t" "v_mov_b32 v31, 1\n\t" "s_nop 1\n\t" \
   "v_mfma_f32_16x16x32_bf16 v[32:35], a[0:3], v[80:83], 0\n\t" "v_mfma_f32_16x16x32_bf16 v[36:39], a[4:7], v[80:83], 0\n\t" \
   "v_mfma_f32_16x16x32_bf16 v[32:35], a[8:11], v[80:83], v[32:35]\n\t" "v_mfma_f32_16x16x32_bf16 v[36:39], a[12:15], v[80:83], v[36:39]\n\t" \
   "v_mfma_f32_16x16x32_bf16 v[32:35], a[16:19], v[80:83], v[32:35]\n\t" "v_mfma_f32_16x16x32_bf16 v[36:39], a[20:23], v[80:83], v[36:39]\n\t" \
   "v_mfma_f32_16x16x32_bf16 v[32:35], a[24:27], v[80:83], v[32:35]\n\t" "v_mfma_f32_16x16x32_bf16 v[36:39], a[28:31], v[80:83], v[36:39]\n\t" \
   "v_mfma_f32_16x16x32_bf16 v[32:35], a[32:35], v[80:83], v[32:35]\n\t" "v_mfma_f32_16x16x32_bf16 v[36:39], a[36:39], v[80:83], v[36:39]\n\t" \
   "v_mfma_f32_16x16x32_bf16 v[32:35], a[40:43], v[80:83], v[32:35]\n\t" "v_mfma_f32_16x16x32_bf16 v[36:39], a[44:47], v[80:83], v[36:39]\n\t" \
   "v_mfma_f32_16x16x32_bf16 v[32:35], a[48:51], v[80:83], v[32:35]\n\t" "v_mfma_f32_16x16x32_bf16 v[36:39], a[52:55], v[80:83], v[36:39]\n\t" \
   "v_mfma_f32_16x16x32_bf16 v[32:35], a[56:59], v[80:83], v[32:35]\n\t" "v_mfma_f32_16x16x32_bf16 v[36:39], a[60:63], v[80:83], v[36:39]\n\t" \
   "v_mfma_f32_16x16x32_bf16 v[32:35], a[64:67], v[80:83], v[32:35]\n\t" "v_mfma_f32_16x16x32_bf16 v[36:39], a[68:71], v[80:83], v[36:39]\n\t" \
   "v_mfma_f32_16x16x32_bf16 v[32:35], a[72:75], v[80:83], v[32:35]\n\t" "v_mfma_f32_16x16x32_bf16 v[36:39], a[76:79], v[80:83], v[36:39]\n\t" \
   "v_mfma_f32_16x16x32_bf16 v[32:35], a[80:83], v[80:83], v[32:35]\n\t" "v_mfma_f32_16x16x32_bf16 v[36:39], a[84:87], v[80:83], v[36:39]\n\t" \
   "v_mfma_f32_16x16x32_bf16 v[32:35], a[88:91], v[80:83], v[32:35]\n\t" "v_mfma_f32_16x16x32_bf16 v[36:39], a[92:95], v[80:83], v[36:39]\n\t" \
   "v_mfma_f32_16x16x32_bf16 v[32:35], a[96:99], v[80:83], v[32:35]\n\t" "v_mfma_f32_16x16x32_bf16 v[36:39], a[100:103], v[80:83], v[36:39]\n\t" \
   "v_mfma_f32_16x16x32_bf16 v[32:35], a[104:107], v[80:83], v[32:35]\n\t" "v_mfma_f32_16x16x32_bf16 v[36:39], a[108:111], v[80:83], v[36:39]\n\t" \
   "v_mfma_f32_16x16x32_bf16 v[32:35], a[112:115], v[80:83], v[32:35]\n\t" "v_mfma_f32_16x16x32_bf16 v[36:39], a[116:119], v[80:83], v[36:39]\n\t" \
   "v_mfma_f32_16x16x32_bf16 v[32:35], a[120:123], v[80:83], v[32:35]\n\t" "v_mfma_f32_16x16x32_bf16 v[36:39], a[124:127], v[80:83], v[36:39]\n\t" \
   "v_mfma_f32_16x16x32_bf16 v[32:35], a[128:131], v[80:83], v[32:35]\n\t" "v_mfma_f32_16x16x32_bf16 v[36:39], a[132:135], v[80:83], v[36:39]\n\t" \
   "v_mfma_f32_16x16x32_bf16 v[32:35], a[136:139], v[80:83], v[32:35]\n\t" "v_mfma_f32_16x16x32_bf16 v[36:39], a[140:143], v[80:83], v[36:39]\n\t" \
   "v_mfma_f32_16x16x32_bf16 v[32:35], a[144:147], v[80:83], v[32:35]\n\t" "v_mfma_f32_16x16x32_bf16 v[36:39], a[148:151], v[80:83], v[36:39]\n\t" \
   "v_mfma_f32_16x16x32_bf16 v[32:35], a[152:155], v[80:83], v[32:35]\n\t" "v_mfma_f32_16x16x32_bf16 v[36:39], a[156:159], v[80:83], v[36:39]\n\t" \
   "s_mov_b32 s40, 0\n\t" "s_mov_b32 s41, 0\n\t" "s_mov_b32 s42, 0\n\t" "s_mov_b32 s43, 44032\n\t" "v_mov_b32 v28, v22\n\t" "v_mov_b32 v29, v23\n\t" \
   "s_mov_b32 s45, 2\n\t" "s_mov_b32 s46, 88064\n\t" "s_waitcnt vmcnt(11)\n\t" "s_barrier\n\t" "s_lshr_b32 s48, s45, 1\n\t" \
   "s_bitcmp1_b32 s44, s48\n\t" "s_cbranch_scc0 172f\n\t" "72:\n\t" "s_mul_i32 s52, s45, 1536\n\t" "s_mul_i32 s51, s45, 40960\n\t" \
   "s_add_u32 m0, s46, %[ldx]\n\t" "s_mov_b32 s53, s46\n\t" "buffer_load_dwordx4 v16, %[rs], s51 offen offset:0 sc1 lds\n\t" \
   "ds_read_b128 v[80:83], v28 offset:0\n\t" "ds_read_b128 v[84:87], v28 offset:1024\n\t" "ds_read_b128 v[88:91], v28 offset:2048\n\t" \
   "ds_read_b128 v[92:95], v28 offset:3072\n\t" "ds_read_b128 v[96:99], v28 offset:4096\n\t" "ds_read_b128 v[100:103], v28 offset:5120\n\t" \
   "ds_read_b128 v[104:107], v28 offset:6144\n\t" "v_mov_b32 v14, v24\n\t" "v_mov_b32 v15, v25\n\t" "v_mov_b32 v18, 0x7ffffff0\n\t" \
   "s_mov_b32 s49, 0\n\t" "v_mov_b32 v26, v28\n\t" "v_mov_b32 v27, v29\n\t" "s_waitcnt lgkmcnt(5)\n\t" \
   "v_mfma_f32_16x16x32_bf16 v[48:51], a[0:3], v[80:83], 0\n\t" "ds_read_b128 v[112:115], v27 offset:0\n\t" \
   "v_mfma_f32_16x16x32_bf16 v[52:55], a[4:7], v[80:83], 0\n\t" "ds_read_b128 v[108:111], v26 offset:7168\n\t" \
   "v_mfma_f32_16x16x32_bf16 v[48:51], a[8:11], v[84:87], v[48:51]\n\t" "ds_read_b128 v[116:119], v27 offset:16\n\t" \
   "v_mfma_f32_16x16x32_bf16 v[52:55], a[12:15], v[84:87], v[52:55]\n\t" "ds_read_b128 v[80:83], v26 offset:8192\n\t" "s_waitcnt lgkmcnt(7)\n\t" \
   "v_mfma_f32_16x16x32_bf16 v[48:51], a[16:19], v[88:91], v[48:51]\n\t" "ds_read_b64 v[120:121], v27 offset:32\n\t" \
   "v_mfma_f32_16x16x32_bf16 v[52:55], a[20:23], v[88:91], v[52:55]\n\t" "ds_read_b128 v[84:87], v26 offset:9216\n\t" \
   "v_mfma_f32_16x16x32_bf16 v[48:51], a[24:27], v[92:95], v[48:51]\n\t" "ds_read_b128 v[122:125], v27 offset:768\n\t" \
   "v_mfma_f32_16x16x32_bf16 v[52:55], a[28:31], v[92:95], v[52:55]\n\t" "ds_read_b128 v[88:91], v26 offset:10240\n\t" "s_waitcnt lgkmcnt(9)\n\t" \
   "v_mfma_f32_16x16x32_bf16 v[48:51], a[32:35], v[96:99], v[48:51]\n\t" "buffer_load_dwordx4 v16, %[rs], s51 offen offset:1024 sc1 lds\n\t" \
   "v_mfma_f32_16x16x32_bf16 v[52:55], a[36:39], v[96:99], v[52:55]\n\t" "ds_read_b128 v[92:95], v26 offset:11264\n\t" \
   "v_mfma_f32_16x16x32_bf16 v[48:51], a[40:43], v[100:103], v[48:51]\n\t" "ds_read_b128 v[126:129], v27 offset:784\n\t" \
   "v_mfma_f32_16x16x32_bf16 v[52:55], a[44:47], v[100:103], v[52:55]\n\t" "ds_read_b128 v[96:99], v26 offset:12288\n\t" "s_waitcnt lgkmcnt(9)\n\t" \
   "v_mfma_f32_16x16x32_bf16 v[48:51], a[48:51], v[104:107], v[48:51]\n\t" "ds_read_b64 v[130:131], v27 offset:800\n\t" \
   "v_mfma_f32_16x16x32_bf16 v[52:55], a[52:55], v[104:107], v[52:55]\n\t" "ds_read_b128 v[100:103], v26 offset:13312\n\t" \
   "v_mfma_f32_16x16x32_bf16 v[48:51], a[56:59], v[108:111], v[48:51]\n\t" "s_waitcnt lgkmcnt(8)\n\t" "v_add_f32 v112, v112, v114\n\t" \
   "v_mfma_f32_16x16x32_bf16 v[52:55], a[60:63], v[108:111], v[52:55]\n\t" "ds_read_b128 v[104:107], v26 offset:14336\n\t" "s_waitcnt lgkmcnt(8)\n\t" \
   "v_mfma_f32_16x16x32_bf16 v[48:51], a[64:67], v[80:83], v[48:51]\n\t" "buffer_load_dwordx4 v16, %[rs], s51 offen offset:2048 sc1 lds\n\t" \
   "v_mfma_f32_16x16x32_bf16 v[52:55], a[68:71], v[80:83], v[52:55]\n\t" "ds_read_b128 v[108:111], v26 offset:15360\n\t" \
   "v_mfma_f32_16x16x32_bf16 v[48:51], a[72:75], v[84:87], v[48:51]\n\t" "v_add_f32 v113, v113, v115\n\t" "v_add_f32 v116, v116, v118\n\t" \
   "v_mfma_f32_16x16x32_bf16 v[52:55], a[76:79], v[84:87], v[52:55]\n\t" "ds_read_b128 v[80:83], v26 offset:16384\n\t" "s_waitcnt lgkmcnt(7)\n\t" \
   "v_mfma_f32_16x16x32_bf16 v[48:51], a[80:83], v[88:91], v[48:51]\n\t" "v_add_f32 v117, v117, v119\n\t" "v_add_f32 v112, v112, v120\n\t" \
   "v_mfma_f32_16x16x32_bf16 v[52:55], a[84:87], v[88:91], v[52:55]\n\t" "ds_read_b128 v[84:87], v26 offset:17408\n\t" \
   "v_mfma_f32_16x16x32_bf16 v[48:51], a[88:91], v[92:95], v[48:51]\n\t" "v_add_f32 v113, v113, v121\n\t" "v_add_f32 v112, v112, v116\n\t" \
   "v_mfma_f32_16x16x32_bf16 v[52:55], a[92:95], v[92:95], v[52:55]\n\t" "ds_read_b128 v[88:91], v26 offset:18432\n\t" \
   "buffer_load_dwordx4 v16, %[rs], s51 offen offset:3072 sc1 lds\n\t" "s_add_u32 m0, m0, 4096\n\t" "s_add_u32 s51, s51, 4096\n\t" \
   "s_waitcnt lgkmcnt(5)\n\t" "v_mfma_f32_16x16x32_bf16 v[48:51], a[96:99], v[96:99], v[48:51]\n\t" "v_add_f32 v113, v113, v117\n\t" \
   "v_mul_f32 v112, 0xbacccccd, v112\n\t" "v_mfma_f32_16x16x32_bf16 v[52:55], a[100:103], v[96:99], v[52:55]\n\t" \
   "ds_read_b128 v[92:95], v26 offset:19456\n\t" "v_mfma_f32_16x16x32_bf16 v[48:51], a[104:107], v[100:103], v[48:51]\n\t" \
   "v_mul_f32 v113, 0x3acccccd, v113\n\t" "v_fma_f32 v113, -v112, v112, v113\n\t" \
   "v_mfma_f32_16x16x32_bf16 v[52:55], a[108:111], v[100:103], v[52:55]\n\t" "ds_read_b128 v[96:99], v26 offset:20480\n\t" "s_waitcnt lgkmcnt(5)\n\t" \
   "v_mfma_f32_16x16x32_bf16 v[48:51], a[112:115], v[104:107], v[48:51]\n\t" "v_max_f32 v113, 0, v113\n\t" "v_cvt_pk_bf16_f32 v113, v113, v112\n\t" \
   "v_mfma_f32_16x16x32_bf16 v[52:55], a[116:119], v[104:107], v[52:55]\n\t" "ds_read_b128 v[100:103], v26 offset:21504\n\t" \
   "v_mfma_f32_16x16x32_bf16 v[48:51], a[120:123], v[108:111], v[48:51]\n\t" "buffer_load_dwordx4 v16, %[rs], s51 offen offset:0 sc1 lds\n\t" \
   "v_mfma_f32_16x16x32_bf16 v[52:55], a[124:127], v[108:111], v[52:55]\n\t" "ds_read_b128 v[104:107], v26 offset:22528\n\t" "s_waitcnt lgkmcnt(5)\n\t" \
   "v_mfma_f32_16x16x32_bf16 v[48:51], a[128:131], v[80:83], v[48:51]\n\t" "v_and_b32 v8, 0xffff0000, v113\n\t" "v_lshlrev_b32 v113, 16, v113\n\t" \
   "v_mfma_f32_16x16x32_bf16 v[52:55], a[132:135], v[80:83], v[52:55]\n\t" "ds_read_b128 v[108:111], v26 offset:23552\n\t" \
   "v_mfma_f32_16x16x32_bf16 v[48:51], a[136:139], v[84:87], v[48:51]\n\t" "v_add_f32 v113, 0x3727c5ac, v113\n\t" \
   "v_cvt_pk_bf16_f32 v113, v113, v113\n\t" "v_mfma_f32_16x16x32_bf16 v[52:55], a[140:143], v[84:87], v[52:55]\n\t" \
   "ds_read_b128 v[80:83], v26 offset:24576\n\t" "s_waitcnt lgkmcnt(5)\n\t" "v_mfma_f32_16x16x32_bf16 v[48:51], a[144:147], v[88:91], v[48:51]\n\t" \
   "v_lshlrev_b32 v113, 16, v113\n\t" "v_add_u32 v28, s43, v22\n\t" "v_mfma_f32_16x16x32_bf16 v[52:55], a[148:151], v[88:91], v[52:55]\n\t" \
   "ds_read_b128 v[84:87], v26 offset:25600\n\t" "buffer_load_dwordx4 v16, %[rs], s51 offen offset:1024 sc1 lds\n\t" \
   "v_mfma_f32_16x16x32_bf16 v[48:51], a[152:155], v[92:95], v[48:51]\n\t" "v_sqrt_f32 v113, v113\n\t" \
   "v_mfma_f32_16x16x32_bf16 v[52:55], a[156:159], v[92:95], v[52:55]\n\t" "ds_read_b128 v[88:91], v26 offset:26624\n\t" "s_nop 0\n\t" \
   "s_waitcnt lgkmcnt(5)\n\t" "v_mfma_f32_16x16x32_bf16 v[56:59], a[0:3], v[96:99], 0\n\t" "v_cvt_pk_bf16_f32 v113, v113, v113\n\t" \
   "v_lshlrev_b32 v113, 16, v113\n\t" "v_mfma_f32_16x16x32_bf16 v[60:63], a[4:7], v[96:99], 0\n\t" "ds_read_b128 v[92:95], v26 offset:27648\n\t" \
   "v_rcp_f32 v10, v113\n\t" "v_mfma_f32_16x16x32_bf16 v[56:59], a[8:11], v[100:103], v[56:59]\n\t" "v_add_f32 v122, v122, v124\n\t" \
   "v_mfma_f32_16x16x32_bf16 v[60:63], a[12:15], v[100:103], v[60:63]\n\t" "ds_read_b128 v[96:99], v26 offset:28672\n\t" "s_waitcnt lgkmcnt(5)\n\t" \
   "v_mfma_f32_16x16x32_bf16 v[56:59], a[16:19], v[104:107], v[56:59]\n\t" "buffer_load_dwordx4 v16, %[rs], s51 offen offset:2048 sc1 lds\n\t" \
   "v_mfma_f32_16x16x32_bf16 v[60:63], a[20:23], v[104:107], v[60:63]\n\t" "ds_read_b128 v[100:103], v26 offset:29696\n\t" \
   "v_mfma_f32_16x16x32_bf16 v[56:59], a[24:27], v[108:111], v[56:59]\n\t" "v_add_f32 v123, v123, v125\n\t" "v_add_f32 v126, v126, v128\n\t" \
   "v_mfma_f32_16x16x32_bf16 v[60:63], a[28:31], v[108:111], v[60:63]\n\t" "ds_read_b128 v[104:107], v26 offset:30720\n\t" "s_waitcnt lgkmcnt(5)\n\t" \
   "v_mfma_f32_16x16x32_bf16 v[56:59], a[32:35], v[80:83], v[56:59]\n\t" "v_fmac_f32 v48, v32, v8\n\t" "v_add_f32 v127, v127, v129\n\t" \
   "v_mfma_f32_16x16x32_bf16 v[60:63], a[36:39], v[80:83], v[60:63]\n\t" "ds_read_b128 v[108:111], v26 offset:31744\n\t" \
   "v_mfma_f32_16x16x32_bf16 v[56:59], a[40:43], v[84:87], v[56:59]\n\t" "v_fmac_f32 v49, v33, v8\n\t" "v_add_f32 v122, v122, v130\n\t" \
   "v_mfma_f32_16x16x32_bf16 v[60:63], a[44:47], v[84:87], v[60:63]\n\t" "ds_read_b128 v[80:83], v26 offset:32768\n\t" \
   "buffer_load_dwordx4 v16, %[rs], s51 offen offset:3072 sc1 lds\n\t" "s_add_u32 m0, m0, 4096\n\t" "s_add_u32 s51, s51, 4096\n\t" \
   "s_waitcnt lgkmcnt(5)\n\t" "v_mfma_f32_16x16x32_bf16 v[56:59], a[48:51], v[88:91], v[56:59]\n\t" "v_fmac_f32 v50, v34, v8\n\t" \
   "v_add_f32 v123, v123, v131\n\t" "v_mfma_f32_16x16x32_bf16 v[60:63], a[52:55], v[88:91], v[60:63]\n\t" "ds_read_b128 v[84:87], v26 offset:33792\n\t" \
   "v_mfma_f32_16x16x32_bf16 v[56:59], a[56:59], v[92:95], v[56:59]\n\t" "v_fmac_f32 v51, v35, v8\n\t" "v_add_f32 v122, v122, v126\n\t" \
   "v_mfma_f32_16x16x32_bf16 v[60:63], a[60:63], v[92:95], v[60:63]\n\t" "ds_read_b128 v[88:91], v26 offset:34816\n\t" "s_waitcnt lgkmcnt(5)\n\t" \
   "v_mfma_f32_16x16x32_bf16 v[56:59], a[64:67], v[96:99], v[56:59]\n\t" "v_fmac_f32 v52, v36, v8\n\t" "v_add_f32 v123, v123, v127\n\t" \
   "v_mfma_f32_16x16x32_bf16 v[60:63], a[68:71], v[96:99], v[60:63]\n\t" "ds_read_b128 v[92:95], v26 offset:35840\n\t" \
   "v_mfma_f32_16x16x32_bf16 v[56:59], a[72:75], v[100:103], v[56:59]\n\t" "buffer_load_dwordx4 v16, %[rs], s51 offen offset:0 sc1 lds\n\t" \
   "v_mfma_f32_16x16x32_bf16 v[60:63], a[76:79], v[100:103], v[60:63]\n\t" "ds_read_b128 v[96:99], v26 offset:36864\n\t" "s_waitcnt lgkmcnt(5)\n\t" \
   "v_mfma_f32_16x16x32_bf16 v[56:59], a[80:83], v[104:107], v[56:59]\n\t" "v_fmac_f32 v53, v37, v8\n\t" "v_mul_f32 v122, 0xbacccccd, v122\n\t" \
   "v_mfma_f32_16x16x32_bf16 v[60:63], a[84:87], v[104:107], v[60:63]\n\t" "ds_read_b128 v[100:103], v26 offset:37888\n\t" \
   "v_mfma_f32_16x16x32_bf16 v[56:59], a[88:91], v[108:111], v[56:59]\n\t" "v_fmac_f32 v54, v38, v8\n\t" "v_mul_f32 v123, 0x3acccccd, v123\n\t" \
   "v_mfma_f32_16x16x32_bf16 v[60:63], a[92:95], v[108:111], v[60:63]\n\t" "ds_read_b128 v[104:107], v26 offset:38912\n\t" "s_waitcnt lgkmcnt(5)\n\t" \
   "v_mfma_f32_16x16x32_bf16 v[56:59], a[96:99], v[80:83], v[56:59]\n\t" "v_fmac_f32 v55, v39, v8\n\t" "v_fma_f32 v123, -v122, v122, v123\n\t" \
   "v_mfma_f32_16x16x32_bf16 v[60:63], a[100:103], v[80:83], v[60:63]\n\t" "ds_read_b128 v[108:111], v26 offset:39936\n\t" \
   "buffer_load_dwordx4 v16, %[rs], s51 offen offset:1024 sc1 lds\n\t" "s_add_u32 m0, s53, %[ld1]\n\t" "s_nop 0\n\t" \
   "v_mfma_f32_16x16x32_bf16 v[56:59], a[104:107], v[84:87], v[56:59]\n\t" "v_mul_f32 v48, v48, v10\n\t" "v_max_f32 v123, 0, v123\n\t" \
   "v_mfma_f32_16x16x32_bf16 v[60:63], a[108:111], v[84:87], v[60:63]\n\t" "v_mul_f32 v49, v49, v10\n\t" "s_waitcnt lgkmcnt(4)\n\t" \
   "v_mfma_f32_16x16x32_bf16 v[56:59], a[112:115], v[88:91], v[56:59]\n\t" "v_cvt_pk_bf16_f32 v123, v123, v122\n\t" "v_mul_f32 v50, v50, v10\n\t" \
   "v_mfma_f32_16x16x32_bf16 v[60:63], a[116:119], v[88:91], v[60:63]\n\t" "v_and_b32 v12, 0xffff0000, v123\n\t" "v_mul_f32 v51, v51, v10\n\t" \
   "v_mfma_f32_16x16x32_bf16 v[56:59], a[120:123], v[92:95], v[56:59]\n\t" "v_lshlrev_b32 v123, 16, v123\n\t" "v_mul_f32 v52, v52, v10\n\t" \
   "v_mfma_f32_16x16x32_bf16 v[60:63], a[124:127], v[92:95], v[60:63]\n\t" "v_add_f32 v123, 0x3727c5ac, v123\n\t" "s_waitcnt lgkmcnt(2)\n\t" \
   "v_mfma_f32_16x16x32_bf16 v[56:59], a[128:131], v[96:99], v[56:59]\n\t" "v_mul_f32 v53, v53, v10\n\t" "v_cvt_pk_bf16_f32 v123, v123, v123\n\t" \
   "buffer_load_dwordx4 v17, %[rs], s52 offen sc1 lds\n\t" "v_mfma_f32_16x16x32_bf16 v[60:63], a[132:135], v[96:99], v[60:63]\n\t" \
   "v_mul_f32 v54, v54, v10\n\t" "v_lshlrev_b32 v123, 16, v123\n\t" "v_mfma_f32_16x16x32_bf16 v[56:59], a[136:139], v[100:103], v[56:59]\n\t" \
   "v_mul_f32 v55, v55, v10\n\t" "v_cvt_pk_bf16_f32 v136, v48, v49\n\t" "v_mfma_f32_16x16x32_bf16 v[60:63], a[140:143], v[100:103], v[60:63]\n\t" \
   "v_sqrt_f32 v123, v123\n\t" "s_waitcnt lgkmcnt(0)\n\t" "v_mfma_f32_16x16x32_bf16 v[56:59], a[144:147], v[104:107], v[56:59]\n\t" \
   "v_cvt_pk_bf16_f32 v137, v50, v51\n\t" "s_nop 0\n\t" "v_mfma_f32_16x16x32_bf16 v[60:63], a[148:151], v[104:107], v[60:63]\n\t" \
   "v_cvt_pk_bf16_f32 v138, v52, v53\n\t" "v_cvt_pk_bf16_f32 v123, v123, v123\n\t" \
   "v_mfma_f32_16x16x32_bf16 v[56:59], a[152:155], v[108:111], v[56:59]\n\t" "v_cvt_pk_bf16_f32 v139, v54, v55\n\t" "v_lshlrev_b32 v123, 16, v123\n\t" \
   "ds_write_b128 v14, v[136:139] offset:0\n\t" "v_mfma_f32_16x16x32_bf16 v[60:63], a[156:159], v[108:111], v[60:63]\n\t" "v_rcp_f32 v132, v123\n\t" \
   "v_add_u32 v29, s43, v23\n\t" "s_memrealtime s[54:55]\n\t" "s_nop 6\n\t" "v_fmac_f32 v56, v32, v12\n\t" "v_fmac_f32 v57, v33, v12\n\t" \
   "v_fmac_f32 v58, v34, v12\n\t" "v_fmac_f32 v59, v35, v12\n\t" "v_fmac_f32 v60, v36, v12\n\t" "v_fmac_f32 v61, v37, v12\n\t" \
   "v_fmac_f32 v62, v38, v12\n\t" "v_fmac_f32 v63, v39, v12\n\t" "v_mul_f32 v56, v56, v132\n\t" "v_mul_f32 v57, v57, v132\n\t" \
   "v_mul_f32 v58, v58, v132\n\t" "v_mul_f32 v59, v59, v132\n\t" "v_mul_f32 v60, v60, v132\n\t" "v_mul_f32 v61, v61, v132\n\t" \
   "v_mul_f32 v62, v62, v132\n\t" "v_mul_f32 v63, v63, v132\n\t" "v_cvt_pk_bf16_f32 v140, v56, v57\n\t" "v_cvt_pk_bf16_f32 v141, v58, v59\n\t" \
   "v_cvt_pk_bf16_f32 v142, v60, v61\n\t" "v_cvt_pk_bf16_f32 v143, v62, v63\n\t" "ds_write_b128 v14, v[140:143] offset:4096\n\t" \
   "s_waitcnt vmcnt(11) lgkmcnt(0)\n\t" "s_barrier\n\t" "ds_read_b128 v[80:83], v28 offset:0\n\t" "ds_read_b128 v[84:87], v28 offset:1024\n\t" \
   "ds_read_b128 v[88:91], v28 offset:2048\n\t" "ds_read_b128 v[92:95], v28 offset:3072\n\t" "ds_read_b128 v[96:99], v28 offset:4096\n\t" \
   "ds_read_b128 v[100:103], v28 offset:5120\n\t" "ds_read_b128 v[104:107], v28 offset:6144\n\t" "s_add_u32 s45, s40, 3\n\t" \
   "s_min_u32 s45, s45, 63\n\t" "s_mov_b32 s46, s42\n\t" "s_lshr_b32 s48, s45, 1\n\t" "s_bitcmp1_b32 s44, s48\n\t" "s_cbranch_scc0 176f\n\t" "76:\n\t" \
   "s_mul_i32 s52, s45, 1536\n\t" "s_mul_i32 s51, s45, 40960\n\t" "s_add_u32 m0, s46, %[ldx]\n\t" "s_mov_b32 s53, s46\n\t" \
   "buffer_load_dwordx4 v16, %[rs], s51 offen offset:0 sc1 lds\n\t" "s_mov_b32 s42, s43\n\t" "s_add_u32 s43, s43, 44032\n\t" \
   "s_cmp_eq_u32 s43, 132096\n\t" "s_cselect_b32 s43, 0, s43\n\t" "s_add_u32 s41, s41, 1\n\t" "s_add_u32 s40, s40, 1\n\t" \
   "v_xor_b32 v14, 0x2000, v14\n\t" "v_xor_b32 v15, 0x2000, v15\n\t" "v_mov_b32 v31, s54\n\t" "v_min_u32 v31, -2, v31\n\t" "1:\n\t" \
   "v_mov_b32 v26, v28\n\t" "v_mov_b32 v27, v29\n\t" "s_waitcnt lgkmcnt(5)\n\t" "v_mfma_f32_16x16x32_bf16 v[48:51], a[0:3], v[80:83], 0\n\t" \
   "ds_read_b128 v[152:155], v15 offset:0\n\t" "ds_read_b128 v[168:171], v15 offset:4096\n\t" \
   "v_mfma_f32_16x16x32_bf16 v[52:55], a[4:7], v[80:83], 0\n\t" "ds_read_b128 v[108:111], v26 offset:7168\n\t" \
   "ds_read_b128 v[156:159], v15 offset:1024\n\t" "v_mfma_f32_16x16x32_bf16 v[48:51], a[8:11], v[84:87], v[48:51]\n\t" \
   "ds_read_b128 v[172:175], v15 offset:5120\n\t" "v_mfma_f32_16x16x32_bf16 v[52:55], a[12:15], v[84:87], v[52:55]\n\t" \
   "ds_read_b128 v[80:83], v26 offset:8192\n\t" "ds_read_b128 v[160:163], v15 offset:2048\n\t" "s_waitcnt lgkmcnt(10)\n\t" \
   "v_mfma_f32_16x16x32_bf16 v[48:51], a[16:19], v[88:91], v[48:51]\n\t" "ds_read_b128 v[176:179], v15 offset:6144\n\t" \
   "v_mfma_f32_16x16x32_bf16 v[52:55], a[20:23], v[88:91], v[52:55]\n\t" "ds_read_b128 v[84:87], v26 offset:9216\n\t" \
   "ds_read_b128 v[164:167], v15 offset:3072\n\t" "v_mfma_f32_16x16x32_bf16 v[48:51], a[24:27], v[92:95], v[48:51]\n\t" \
   "ds_read_b128 v[180:183], v15 offset:7168\n\t" "v_mfma_f32_16x16x32_bf16 v[52:55], a[28:31], v[92:95], v[52:55]\n\t" \
   "ds_read_b128 v[88:91], v26 offset:10240\n\t" "s_waitcnt lgkmcnt(13)\n\t" "v_mfma_f32_16x16x32_bf16 v[48:51], a[32:35], v[96:99], v[48:51]\n\t" \
   "v_cvt_pk_bf16_f32 v148, v72, v73\n\t" "v_cvt_pk_bf16_f32 v149, v74, v75\n\t" "v_mfma_f32_16x16x32_bf16 v[52:55], a[36:39], v[96:99], v[52:55]\n\t" \
   "ds_read_b128 v[92:95], v26 offset:11264\n\t" "s_waitcnt lgkmcnt(8)\n\t" "v_mfma_f32_16x16x32_bf16 v[64:67], v[152:155], a[160:163], 0\n\t" \
   "v_cvt_pk_bf16_f32 v150, v76, v77\n\t" "v_cvt_pk_bf16_f32 v151, v78, v79\n\t" \
   "v_mfma_f32_16x16x32_bf16 v[48:51], a[40:43], v[100:103], v[48:51]\n\t" "s_add_u32 s50, s49, 294912\n\t" \
   "v_mfma_f32_16x16x32_bf16 v[52:55], a[44:47], v[100:103], v[52:55]\n\t" "ds_read_b128 v[96:99], v26 offset:12288\n\t" \
   "v_mfma_f32_16x16x32_bf16 v[68:71], v[168:171], a[160:163], 0\n\t" "v_mfma_f32_16x16x32_bf16 v[48:51], a[48:51], v[104:107], v[48:51]\n\t" \
   "buffer_store_dwordx4 v[148:151], v18, %[rs], s50 offen sc1\n\t" "v_mfma_f32_16x16x32_bf16 v[52:55], a[52:55], v[104:107], v[52:55]\n\t" \
   "ds_read_b128 v[100:103], v26 offset:13312\n\t" "s_sub_i32 s49, s41, 1\n\t" \
   "v_mfma_f32_16x16x32_bf16 v[64:67], v[156:159], a[164:167], v[64:67]\n\t" "s_mul_i32 s49, s49, 1310720\n\t" \
   "v_mfma_f32_16x16x32_bf16 v[48:51], a[56:59], v[108:111], v[48:51]\n\t" "ds_read_b128 v[112:115], v27 offset:0\n\t" \
   "v_mfma_f32_16x16x32_bf16 v[52:55], a[60:63], v[108:111], v[52:55]\n\t" "ds_read_b128 v[104:107], v26 offset:14336\n\t" \
   "v_mfma_f32_16x16x32_bf16 v[68:71], v[172:175], a[164:167], v[68:71]\n\t" "s_waitcnt lgkmcnt(8)\n\t" \
   "v_mfma_f32_16x16x32_bf16 v[48:51], a[64:67], v[80:83], v[48:51]\n\t" "ds_read_b128 v[116:119], v27 offset:16\n\t" \
   "v_mfma_f32_16x16x32_bf16 v[52:55], a[68:71], v[80:83], v[52:55]\n\t" "ds_read_b128 v[108:111], v26 offset:15360\n\t" \
   "ds_read_b64 v[120:121], v27 offset:32\n\t" "s_waitcnt lgkmcnt(9)\n\t" "v_mfma_f32_16x16x32_bf16 v[64:67], v[160:163], a[168:171], v[64:67]\n\t" \
   "ds_read_b128 v[122:125], v27 offset:768\n\t" "v_mfma_f32_16x16x32_bf16 v[48:51], a[72:75], v[84:87], v[48:51]\n\t" \
   "ds_read_b128 v[126:129], v27 offset:784\n\t" "v_mfma_f32_16x16x32_bf16 v[52:55], a[76:79], v[84:87], v[52:55]\n\t" \
   "ds_read_b128 v[80:83], v26 offset:16384\n\t" "ds_read_b64 v[130:131], v27 offset:800\n\t" \
   "v_mfma_f32_16x16x32_bf16 v[68:71], v[176:179], a[168:171], v[68:71]\n\t" "v_add_u32 v28, s43, v22\n\t" "s_waitcnt lgkmcnt(11)\n\t" \
   "v_mfma_f32_16x16x32_bf16 v[48:51], a[80:83], v[88:91], v[48:51]\n\t" "buffer_load_dwordx4 v16, %[rs], s51 offen offset:1024 sc1 lds\n\t" \
   "v_mfma_f32_16x16x32_bf16 v[52:55], a[84:87], v[88:91], v[52:55]\n\t" "ds_read_b128 v[84:87], v26 offset:17408\n\t" \
   "v_mfma_f32_16x16x32_bf16 v[48:51], a[88:91], v[92:95], v[48:51]\n\t" "s_waitcnt lgkmcnt(5)\n\t" "v_add_f32 v112, v112, v114\n\t" \
   "v_mfma_f32_16x16x32_bf16 v[64:67], v[164:167], a[172:175], v[64:67]\n\t" "v_add_f32 v113, v113, v115\n\t" "v_add_f32 v116, v116, v118\n\t" \
   "v_mfma_f32_16x16x32_bf16 v[52:55], a[92:95], v[92:95], v[52:55]\n\t" "ds_read_b128 v[88:91], v26 offset:18432\n\t" \
   "v_mfma_f32_16x16x32_bf16 v[48:51], a[96:99], v[96:99], v[48:51]\n\t" "v_add_f32 v117, v117, v119\n\t" "v_add_f32 v112, v112, v120\n\t" \
   "v_mfma_f32_16x16x32_bf16 v[68:71], v[180:183], a[172:175], v[68:71]\n\t" "v_add_f32 v113, v113, v121\n\t" "v_add_f32 v112, v112, v116\n\t" \
   "v_mfma_f32_16x16x32_bf16 v[52:55], a[100:103], v[96:99], v[52:55]\n\t" "ds_read_b128 v[92:95], v26 offset:19456\n\t" \
   "v_mfma_f32_16x16x32_bf16 v[48:51], a[104:107], v[100:103], v[48:51]\n\t" "v_add_f32 v113, v113, v117\n\t" "v_mul_f32 v112, 0xbacccccd, v112\n\t" \
   "v_mfma_f32_16x16x32_bf16 v[72:75], v[152:155], a[176:179], 0\n\t" "v_cvt_pk_bf16_f32 v144, v64, v65\n\t" "v_mul_f32 v113, 0x3acccccd, v113\n\t" \
   "v_mfma_f32_16x16x32_bf16 v[52:55], a[108:111], v[100:103], v[52:55]\n\t" "ds_read_b128 v[96:99], v26 offset:20480\n\t" \
   "buffer_load_dwordx4 v16, %[rs], s51 offen offset:2048 sc1 lds\n\t" "v_mfma_f32_16x16x32_bf16 v[48:51], a[112:115], v[104:107], v[48:51]\n\t" \
   "v_cvt_pk_bf16_f32 v145, v66, v67\n\t" "v_cvt_pk_bf16_f32 v146, v68, v69\n\t" "v_mfma_f32_16x16x32_bf16 v[76:79], v[168:171], a[176:179], 0\n\t" \
   "v_fma_f32 v113, -v112, v112, v113\n\t" "v_cvt_pk_bf16_f32 v147, v70, v71\n\t" \
   "v_mfma_f32_16x16x32_bf16 v[52:55], a[116:119], v[104:107], v[52:55]\n\t" "ds_read_b128 v[100:103], v26 offset:21504\n\t" \
   "v_mfma_f32_16x16x32_bf16 v[48:51], a[120:123], v[108:111], v[48:51]\n\t" "s_add_u32 s50, s49, 0\n\t" "v_max_f32 v113, 0, v113\n\t" \
   "v_mfma_f32_16x16x32_bf16 v[72:75], v[156:159], a[180:183], v[72:75]\n\t" "buffer_store_dwordx4 v[144:147], v21, %[rs], s50 offen sc1\n\t" \
   "v_mfma_f32_16x16x32_bf16 v[52:55], a[124:127], v[108:111], v[52:55]\n\t" "ds_read_b128 v[104:107], v26 offset:22528\n\t" "s_waitcnt lgkmcnt(5)\n\t" \
   "v_mfma_f32_16x16x32_bf16 v[48:51], a[128:131], v[80:83], v[48:51]\n\t" "v_cvt_pk_bf16_f32 v113, v113, v112\n\t" \
   "v_and_b32 v8, 0xffff0000, v113\n\t" "v_mfma_f32_16x16x32_bf16 v[52:55], a[132:135], v[80:83], v[52:55]\n\t" \
   "ds_read_b128 v[108:111], v26 offset:23552\n\t" "v_mfma_f32_16x16x32_bf16 v[76:79], v[172:175], a[180:183], v[76:79]\n\t" \
   "v_lshlrev_b32 v113, 16, v113\n\t" "v_add_f32 v113, 0x3727c5ac, v113\n\t" "v_mfma_f32_16x16x32_bf16 v[48:51], a[136:139], v[84:87], v[48:51]\n\t" \
   "buffer_load_dwordx4 v16, %[rs], s51 offen offset:3072 sc1 lds\n\t" "s_add_u32 m0, m0, 4096\n\t" "s_add_u32 s51, s51, 4096\n\t" \
   "v_mfma_f32_16x16x32_bf16 v[52:55], a[140:143], v[84:87], v[52:55]\n\t" "ds_read_b128 v[80:83], v26 offset:24576\n\t" \
   "v_mfma_f32_16x16x32_bf16 v[72:75], v[160:163], a[184:187], v[72:75]\n\t" "v_cvt_pk_bf16_f32 v113, v113, v113\n\t" "s_waitcnt lgkmcnt(5)\n\t" \
   "v_mfma_f32_16x16x32_bf16 v[48:51], a[144:147], v[88:91], v[48:51]\n\t" "v_lshlrev_b32 v113, 16, v113\n\t" "v_add_u32 v29, s43, v23\n\t" \
   "v_mfma_f32_16x16x32_bf16 v[52:55], a[148:151], v[88:91], v[52:55]\n\t" "ds_read_b128 v[84:87], v26 offset:25600\n\t" \
   "v_mfma_f32_16x16x32_bf16 v[76:79], v[176:179], a[184:187], v[76:79]\n\t" "v_sqrt_f32 v113, v113\n\t" \
   "v_mfma_f32_16x16x32_bf16 v[48:51], a[152:155], v[92:95], v[48:51]\n\t" "s_nop 0\n\t" "v_cvt_pk_bf16_f32 v113, v113, v113\n\t" \
   "v_mfma_f32_16x16x32_bf16 v[52:55], a[156:159], v[92:95], v[52:55]\n\t" "ds_read_b128 v[88:91], v26 offset:26624\n\t" \
   "v_mfma_f32_16x16x32_bf16 v[72:75], v[164:167], a[188:191], v[72:75]\n\t" "v_lshlrev_b32 v113, 16, v113\n\t" "v_rcp_f32 v10, v113\n\t" \
   "s_waitcnt lgkmcnt(5)\n\t" "v_mfma_f32_16x16x32_bf16 v[56:59], a[0:3], v[96:99], 0\n\t" \
   "buffer_load_dwordx4 v16, %[rs], s51 offen offset:0 sc1 lds\n\t" "v_mfma_f32_16x16x32_bf16 v[60:63], a[4:7], v[96:99], 0\n\t" \
   "ds_read_b128 v[92:95], v26 offset:27648\n\t" "v_mfma_f32_16x16x32_bf16 v[76:79], v[180:183], a[188:191], v[76:79]\n\t" \
   "v_add_f32 v122, v122, v124\n\t" "v_mfma_f32_16x16x32_bf16 v[56:59], a[8:11], v[100:103], v[56:59]\n\t" "v_add_f32 v123, v123, v125\n\t" \
   "v_add_f32 v126, v126, v128\n\t" "v_mfma_f32_16x16x32_bf16 v[60:63], a[12:15], v[100:103], v[60:63]\n\t" \
   "ds_read_b128 v[96:99], v26 offset:28672\n\t" "s_waitcnt lgkmcnt(5)\n\t" "v_mfma_f32_16x16x32_bf16 v[56:59], a[16:19], v[104:107], v[56:59]\n\t" \
   "v_cvt_pk_bf16_f32 v148, v72, v73\n\t" "v_cvt_pk_bf16_f32 v149, v74, v75\n\t" "v_mfma_f32_16x16x32_bf16 v[64:67], v[152:155], a[192:195], 0\n\t" \
   "v_cvt_pk_bf16_f32 v150, v76, v77\n\t" "v_cvt_pk_bf16_f32 v151, v78, v79\n\t" \
   "v_mfma_f32_16x16x32_bf16 v[60:63], a[20:23], v[104:107], v[60:63]\n\t" "ds_read_b128 v[100:103], v26 offset:29696\n\t" \
   "v_mfma_f32_16x16x32_bf16 v[56:59], a[24:27], v[108:111], v[56:59]\n\t" "s_add_u32 s50, s49, 32768\n\t" "v_fmac_f32 v48, v32, v8\n\t" \
   "v_mfma_f32_16x16x32_bf16 v[68:71], v[168:171], a[192:195], 0\n\t" "buffer_store_dwordx4 v[148:151], v21, %[rs], s50 offen sc1\n\t" \
   "v_mfma_f32_16x16x32_bf16 v[60:63], a[28:31], v[108:111], v[60:63]\n\t" "ds_read_b128 v[104:107], v26 offset:30720\n\t" "s_waitcnt lgkmcnt(5)\n\t" \
   "v_mfma_f32_16x16x32_bf16 v[56:59], a[32:35], v[80:83], v[56:59]\n\t" "buffer_load_dwordx4 v16, %[rs], s51 offen offset:1024 sc1 lds\n\t" \
   "v_mfma_f32_16x16x32_bf16 v[64:67], v[156:159], a[196:199], v[64:67]\n\t" "v_add_f32 v127, v127, v129\n\t" "v_fmac_f32 v49, v33, v8\n\t" \
   "v_mfma_f32_16x16x32_bf16 v[60:63], a[36:39], v[80:83], v[60:63]\n\t" "ds_read_b128 v[108:111], v26 offset:31744\n\t" \
   "v_mfma_f32_16x16x32_bf16 v[56:59], a[40:43], v[84:87], v[56:59]\n\t" "v_add_f32 v122, v122, v130\n\t" "v_fmac_f32 v50, v34, v8\n\t" \
   "v_mfma_f32_16x16x32_bf16 v[68:71], v[172:175], a[196:199], v[68:71]\n\t" "v_add_f32 v123, v123, v131\n\t" "v_fmac_f32 v51, v35, v8\n\t" \
   "v_mfma_f32_16x16x32_bf16 v[60:63], a[44:47], v[84:87], v[60:63]\n\t" "ds_read_b128 v[80:83], v26 offset:32768\n\t" "s_waitcnt lgkmcnt(5)\n\t" \
   "v_mfma_f32_16x16x32_bf16 v[56:59], a[48:51], v[88:91], v[56:59]\n\t" "v_add_f32 v122, v122, v126\n\t" "v_fmac_f32 v52, v36, v8\n\t" \
   "v_mfma_f32_16x16x32_bf16 v[64:67], v[160:163], a[200:203], v[64:67]\n\t" "v_add_f32 v123, v123, v127\n\t" "v_fmac_f32 v53, v37, v8\n\t" \
   "v_mfma_f32_16x16x32_bf16 v[60:63], a[52:55], v[88:91], v[60:63]\n\t" "ds_read_b128 v[84:87], v26 offset:33792\n\t" \
   "v_mfma_f32_16x16x32_bf16 v[56:59], a[56:59], v[92:95], v[56:59]\n\t" "s_cmp_lt_i32 s40, 2\n\t" "s_cselect_b32 s47, 0x7ffffff0, 0\n\t" \
   "v_mfma_f32_16x16x32_bf16 v[68:71], v[176:179], a[200:203], v[68:71]\n\t" "v_or_b32 v30, s47, v20\n\t" "s_sub_i32 s47, s41, 2\n\t" \
   "buffer_load_dwordx4 v16, %[rs], s51 offen offset:2048 sc1 lds\n\t" "v_mfma_f32_16x16x32_bf16 v[60:63], a[60:63], v[92:95], v[60:63]\n\t" \
   "ds_read_b128 v[88:91], v26 offset:34816\n\t" "s_max_i32 s47, s47, 0\n\t" "s_waitcnt lgkmcnt(5)\n\t" \
   "v_mfma_f32_16x16x32_bf16 v[56:59], a[64:67], v[96:99], v[56:59]\n\t" "s_lshl_b32 s47, s47, 12\n\t" "s_waitcnt vmcnt(8)\n\t" \
   "v_mfma_f32_16x16x32_bf16 v[60:63], a[68:71], v[96:99], v[60:63]\n\t" "ds_read_b128 v[92:95], v26 offset:35840\n\t" \
   "buffer_store_dword v31, v30, %[rs], s47 offen sc1\n\t" "v_mfma_f32_16x16x32_bf16 v[64:67], v[164:167], a[204:207], v[64:67]\n\t" \
   "v_fmac_f32 v54, v38, v8\n\t" "v_mul_f32 v122, 0xbacccccd, v122\n\t" "v_mfma_f32_16x16x32_bf16 v[56:59], a[72:75], v[100:103], v[56:59]\n\t" \
   "v_mul_f32 v123, 0x3acccccd, v123\n\t" "v_fmac_f32 v55, v39, v8\n\t" "v_mfma_f32_16x16x32_bf16 v[60:63], a[76:79], v[100:103], v[60:63]\n\t" \
   "ds_read_b128 v[96:99], v26 offset:36864\n\t" "v_mfma_f32_16x16x32_bf16 v[68:71], v[180:183], a[204:207], v[68:71]\n\t" \
   "v_fma_f32 v123, -v122, v122, v123\n\t" "s_waitcnt lgkmcnt(5)\n\t" "v_mfma_f32_16x16x32_bf16 v[56:59], a[80:83], v[104:107], v[56:59]\n\t" \
   "v_mul_f32 v48, v48, v10\n\t" "v_max_f32 v123, 0, v123\n\t" "v_mfma_f32_16x16x32_bf16 v[60:63], a[84:87], v[104:107], v[60:63]\n\t" \
   "ds_read_b128 v[100:103], v26 offset:37888\n\t" "v_mfma_f32_16x16x32_bf16 v[72:75], v[152:155], a[208:211], 0\n\t" \
   "buffer_load_dwordx4 v16, %[rs], s51 offen offset:3072 sc1 lds\n\t" "s_add_u32 m0, m0, 4096\n\t" "s_add_u32 s51, s51, 4096\n\t" \
   "v_mfma_f32_16x16x32_bf16 v[56:59], a[88:91], v[108:111], v[56:59]\n\t" "v_cvt_pk_bf16_f32 v144, v64, v65\n\t" \
   "v_cvt_pk_bf16_f32 v145, v66, v67\n\t" "v_mfma_f32_16x16x32_bf16 v[60:63], a[92:95], v[108:111], v[60:63]\n\t" \
   "ds_read_b128 v[104:107], v26 offset:38912\n\t" "v_mfma_f32_16x16x32_bf16 v[76:79], v[168:171], a[208:211], 0\n\t" \
   "v_cvt_pk_bf16_f32 v146, v68, v69\n\t" "s_waitcnt lgkmcnt(5)\n\t" "v_mfma_f32_16x16x32_bf16 v[56:59], a[96:99], v[80:83], v[56:59]\n\t" \
   "v_cvt_pk_bf16_f32 v147, v70, v71\n\t" "s_add_u32 s50, s49, 65536\n\t" "v_mfma_f32_16x16x32_bf16 v[60:63], a[100:103], v[80:83], v[60:63]\n\t" \
   "ds_read_b128 v[108:111], v26 offset:39936\n\t" "v_mfma_f32_16x16x32_bf16 v[72:75], v[156:159], a[212:215], v[72:75]\n\t" \
   "buffer_store_dwordx4 v[144:147], v21, %[rs], s50 offen sc1\n\t" "v_mfma_f32_16x16x32_bf16 v[56:59], a[104:107], v[84:87], v[56:59]\n\t" \
   "v_mul_f32 v49, v49, v10\n\t" "v_mul_f32 v50, v50, v10\n\t" "v_mfma_f32_16x16x32_bf16 v[60:63], a[108:111], v[84:87], v[60:63]\n\t" \
   "v_cvt_pk_bf16_f32 v123, v123, v122\n\t" "s_waitcnt lgkmcnt(4)\n\t" "v_mfma_f32_16x16x32_bf16 v[56:59], a[112:115], v[88:91], v[56:59]\n\t" \
   "buffer_load_dwordx4 v16, %[rs], s51 offen offset:0 sc1 lds\n\t" "v_mfma_f32_16x16x32_bf16 v[76:79], v[172:175], a[212:215], v[76:79]\n\t" \
   "v_mul_f32 v51, v51, v10\n\t" "v_and_b32 v12, 0xffff0000, v123\n\t" "v_mfma_f32_16x16x32_bf16 v[60:63], a[116:119], v[88:91], v[60:63]\n\t" \
   "v_mul_f32 v52, v52, v10\n\t" "v_lshlrev_b32 v123, 16, v123\n\t" "v_mfma_f32_16x16x32_bf16 v[56:59], a[120:123], v[92:95], v[56:59]\n\t" \
   "v_mul_f32 v53, v53, v10\n\t" "v_add_f32 v123, 0x3727c5ac, v123\n\t" "v_mfma_f32_16x16x32_bf16 v[72:75], v[160:163], a[216:219], v[72:75]\n\t" \
   "v_mul_f32 v54, v54, v10\n\t" "v_cvt_pk_bf16_f32 v123, v123, v123\n\t" "v_mfma_f32_16x16x32_bf16 v[60:63], a[124:127], v[92:95], v[60:63]\n\t" \
   "v_mul_f32 v55, v55, v10\n\t" "s_waitcnt lgkmcnt(2)\n\t" "v_mfma_f32_16x16x32_bf16 v[56:59], a[128:131], v[96:99], v[56:59]\n\t" \
   "v_lshlrev_b32 v123, 16, v123\n\t" "v_cvt_pk_bf16_f32 v136, v48, v49\n\t" "v_mfma_f32_16x16x32_bf16 v[76:79], v[176:179], a[216:219], v[76:79]\n\t" \
   "v_sqrt_f32 v123, v123\n\t" "v_mfma_f32_16x16x32_bf16 v[60:63], a[132:135], v[96:99], v[60:63]\n\t" "v_cvt_pk_bf16_f32 v137, v50, v51\n\t" \
   "s_nop 0\n\t" "v_mfma_f32_16x16x32_bf16 v[56:59], a[136:139], v[100:103], v[56:59]\n\t" "v_cvt_pk_bf16_f32 v138, v52, v53\n\t" \
   "v_cvt_pk_bf16_f32 v123, v123, v123\n\t" "v_mfma_f32_16x16x32_bf16 v[72:75], v[164:167], a[220:223], v[72:75]\n\t" \
   "buffer_load_dwordx4 v16, %[rs], s51 offen offset:1024 sc1 lds\n\t" "s_add_u32 m0, s53, %[ld1]\n\t" "s_nop 0\n\t" \
   "v_mfma_f32_16x16x32_bf16 v[60:63], a[140:143], v[100:103], v[60:63]\n\t" "v_cvt_pk_bf16_f32 v139, v54, v55\n\t" "s_waitcnt lgkmcnt(0)\n\t" \
   "v_mfma_f32_16x16x32_bf16 v[56:59], a[144:147], v[104:107], v[56:59]\n\t" "v_lshlrev_b32 v123, 16, v123\n\t" \
   "v_mfma_f32_16x16x32_bf16 v[76:79], v[180:183], a[220:223], v[76:79]\n\t" "ds_write_b128 v14, v[136:139] offset:0\n\t" \
   "v_mfma_f32_16x16x32_bf16 v[60:63], a[148:151], v[104:107], v[60:63]\n\t" "v_rcp_f32 v132, v123\n\t" \
   "v_mfma_f32_16x16x32_bf16 v[56:59], a[152:155], v[108:111], v[56:59]\n\t" "buffer_load_dwordx4 v17, %[rs], s52 offen sc1 lds\n\t" \
   "v_mfma_f32_16x16x32_bf16 v[60:63], a[156:159], v[108:111], v[60:63]\n\t" "v_cvt_pk_bf16_f32 v148, v72, v73\n\t" \
   "v_cvt_pk_bf16_f32 v149, v74, v75\n\t" "v_mfma_f32_16x16x32_bf16 v[64:67], v[152:155], a[224:227], 0\n\t" "v_cvt_pk_bf16_f32 v150, v76, v77\n\t" \
   "v_cvt_pk_bf16_f32 v151, v78, v79\n\t" "v_mfma_f32_16x16x32_bf16 v[68:71], v[168:171], a[224:227], 0\n\t" "s_add_u32 s50, s49, 98304\n\t" \
   "v_mfma_f32_16x16x32_bf16 v[64:67], v[156:159], a[228:231], v[64:67]\n\t" "v_fmac_f32 v56, v32, v12\n\t" "v_fmac_f32 v57, v33, v12\n\t" \
   "v_mfma_f32_16x16x32_bf16 v[68:71], v[172:175], a[228:231], v[68:71]\n\t" "v_fmac_f32 v58, v34, v12\n\t" "v_fmac_f32 v59, v35, v12\n\t" \
   "v_mfma_f32_16x16x32_bf16 v[64:67], v[160:163], a[232:235], v[64:67]\n\t" "v_fmac_f32 v60, v36, v12\n\t" "v_fmac_f32 v61, v37, v12\n\t" \
   "v_mfma_f32_16x16x32_bf16 v[68:71], v[176:179], a[232:235], v[68:71]\n\t" "v_fmac_f32 v62, v38, v12\n\t" "v_fmac_f32 v63, v39, v12\n\t" \
   "buffer_store_dwordx4 v[148:151], v21, %[rs], s50 offen sc1\n\t" "v_mfma_f32_16x16x32_bf16 v[64:67], v[164:167], a[236:239], v[64:67]\n\t" \
   "v_mul_f32 v56, v56, v132\n\t" "v_mul_f32 v57, v57, v132\n\t" "v_mfma_f32_16x16x32_bf16 v[68:71], v[180:183], a[236:239], v[68:71]\n\t" \
   "v_mul_f32 v58, v58, v132\n\t" "v_mul_f32 v59, v59, v132\n\t" "v_mfma_f32_16x16x32_bf16 v[72:75], v[152:155], a[240:243], 0\n\t" \
   "v_mul_f32 v60, v60, v132\n\t" "v_mul_f32 v61, v61, v132\n\t" "v_mfma_f32_16x16x32_bf16 v[76:79], v[168:171], a[240:243], 0\n\t" \
   "v_mul_f32 v62, v62, v132\n\t" "v_mul_f32 v63, v63, v132\n\t" "v_mfma_f32_16x16x32_bf16 v[72:75], v[156:159], a[244:247], v[72:75]\n\t" \
   "v_cvt_pk_bf16_f32 v144, v64, v65\n\t" "v_cvt_pk_bf16_f32 v145, v66, v67\n\t" \
   "v_mfma_f32_16x16x32_bf16 v[76:79], v[172:175], a[244:247], v[76:79]\n\t" "v_cvt_pk_bf16_f32 v146, v68, v69\n\t" \
   "v_cvt_pk_bf16_f32 v140, v56, v57\n\t" "v_mfma_f32_16x16x32_bf16 v[72:75], v[160:163], a[248:251], v[72:75]\n\t" \
   "v_cvt_pk_bf16_f32 v147, v70, v71\n\t" "v_cvt_pk_bf16_f32 v141, v58, v59\n\t" \
   "v_mfma_f32_16x16x32_bf16 v[76:79], v[176:179], a[248:251], v[76:79]\n\t" "s_add_u32 s50, s49, 131072\n\t" "v_cvt_pk_bf16_f32 v142, v60, v61\n\t" \
   "v_mfma_f32_16x16x32_bf16 v[72:75], v[164:167], a[252:255], v[72:75]\n\t" "v_cvt_pk_bf16_f32 v143, v62, v63\n\t" \
   "v_mfma_f32_16x16x32_bf16 v[76:79], v[180:183], a[252:255], v[76:79]\n\t" "buffer_store_dwordx4 v[144:147], v21, %[rs], s50 offen sc1\n\t" \
   "v_mfma_f32_16x16x32_bf16 v[64:67], v[152:155], v[192:195], 0\n\t" "ds_write_b128 v14, v[140:143] offset:4096\n\t" \
   "v_mfma_f32_16x16x32_bf16 v[68:71], v[168:171], v[192:195], 0\n\t" "v_mfma_f32_16x16x32_bf16 v[64:67], v[156:159], v[196:199], v[64:67]\n\t" \
   "v_cvt_pk_bf16_f32 v148, v72, v73\n\t" "v_cvt_pk_bf16_f32 v149, v74, v75\n\t" \
   "v_mfma_f32_16x16x32_bf16 v[68:71], v[172:175], v[196:199], v[68:71]\n\t" "v_cvt_pk_bf16_f32 v150, v76, v77\n\t" \
   "v_cvt_pk_bf16_f32 v151, v78, v79\n\t" "s_waitcnt vmcnt(16) lgkmcnt(0)\n\t" "s_barrier\n\t" \
   "v_mfma_f32_16x16x32_bf16 v[64:67], v[160:163], v[200:203], v[64:67]\n\t" "s_add_u32 s45, s40, 3\n\t" "s_min_u32 s45, s45, 63\n\t" \
   "v_mfma_f32_16x16x32_bf16 v[68:71], v[176:179], v[200:203], v[68:71]\n\t" "s_add_u32 s50, s49, 163840\n\t" "s_mov_b32 s46, s42\n\t" \
   "v_mfma_f32_16x16x32_bf16 v[64:67], v[164:167], v[204:207], v[64:67]\n\t" "s_lshr_b32 s48, s45, 1\n\t" "s_bitcmp1_b32 s44, s48\n\t" \
   "s_cbranch_scc0 174f\n\t" "74:\n\t" "v_mfma_f32_16x16x32_bf16 v[68:71], v[180:183], v[204:207], v[68:71]\n\t" \
   "buffer_store_dwordx4 v[148:151], v21, %[rs], s50 offen sc1\n\t" "v_mfma_f32_16x16x32_bf16 v[72:75], v[152:155], v[208:211], 0\n\t" \
   "s_mul_i32 s52, s45, 1536\n\t" "s_mul_i32 s51, s45, 40960\n\t" "s_add_u32 m0, s46, %[ldx]\n\t" "s_mov_b32 s53, s46\n\t" \
   "v_mfma_f32_16x16x32_bf16 v[76:79], v[168:171], v[208:211], 0\n\t" "buffer_load_dwordx4 v16, %[rs], s51 offen offset:0 sc1 lds\n\t" \
   "v_mfma_f32_16x16x32_bf16 v[72:75], v[156:159], v[212:215], v[72:75]\n\t" "v_cvt_pk_bf16_f32 v144, v64, v65\n\t" \
   "v_cvt_pk_bf16_f32 v145, v66, v67\n\t" "v_mfma_f32_16x16x32_bf16 v[76:79], v[172:175], v[212:215], v[76:79]\n\t" \
   "v_cvt_pk_bf16_f32 v146, v68, v69\n\t" "v_cvt_pk_bf16_f32 v147, v70, v71\n\t" \
   "v_mfma_f32_16x16x32_bf16 v[72:75], v[160:163], v[216:219], v[72:75]\n\t" "ds_read_b128 v[80:83], v28 offset:0\n\t" \
   "v_mfma_f32_16x16x32_bf16 v[76:79], v[176:179], v[216:219], v[76:79]\n\t" "s_add_u32 s50, s49, 196608\n\t" "s_mov_b32 s42, s43\n\t" \
   "v_mfma_f32_16x16x32_bf16 v[72:75], v[164:167], v[220:223], v[72:75]\n\t" "ds_read_b128 v[84:87], v28 offset:1024\n\t" \
   "v_mfma_f32_16x16x32_bf16 v[76:79], v[180:183], v[220:223], v[76:79]\n\t" "buffer_store_dwordx4 v[144:147], v21, %[rs], s50 offen sc1\n\t" \
   "v_mfma_f32_16x16x32_bf16 v[64:67], v[152:155], v[224:227], 0\n\t" "ds_read_b128 v[88:91], v28 offset:2048\n\t" \
   "v_mfma_f32_16x16x32_bf16 v[68:71], v[168:171], v[224:227], 0\n\t" "s_add_u32 s43, s43, 44032\n\t" \
   "v_mfma_f32_16x16x32_bf16 v[64:67], v[156:159], v[228:231], v[64:67]\n\t" "v_cvt_pk_bf16_f32 v148, v72, v73\n\t" \
   "v_cvt_pk_bf16_f32 v149, v74, v75\n\t" "v_mfma_f32_16x16x32_bf16 v[68:71], v[172:175], v[228:231], v[68:71]\n\t" \
   "v_cvt_pk_bf16_f32 v150, v76, v77\n\t" "v_cvt_pk_bf16_f32 v151, v78, v79\n\t" \
   "v_mfma_f32_16x16x32_bf16 v[64:67], v[160:163], v[232:235], v[64:67]\n\t" "s_cmp_eq_u32 s43, 132096\n\t" "s_cselect_b32 s43, 0, s43\n\t" \
   "v_mfma_f32_16x16x32_bf16 v[68:71], v[176:179], v[232:235], v[68:71]\n\t" "ds_read_b128 v[92:95], v28 offset:3072\n\t" \
   "v_mfma_f32_16x16x32_bf16 v[64:67], v[164:167], v[236:239], v[64:67]\n\t" "s_add_u32 s50, s49, 229376\n\t" "s_add_u32 s41, s41, 1\n\t" \
   "v_mfma_f32_16x16x32_bf16 v[68:71], v[180:183], v[236:239], v[68:71]\n\t" "buffer_store_dwordx4 v[148:151], v21, %[rs], s50 offen sc1\n\t" \
   "v_mfma_f32_16x16x32_bf16 v[72:75], v[152:155], v[240:243], 0\n\t" "ds_read_b128 v[96:99], v28 offset:4096\n\t" \
   "v_mfma_f32_16x16x32_bf16 v[76:79], v[168:171], v[240:243], 0\n\t" "s_add_u32 s40, s40, 1\n\t" "v_xor_b32 v14, 0x2000, v14\n\t" \
   "v_mfma_f32_16x16x32_bf16 v[72:75], v[156:159], v[244:247], v[72:75]\n\t" "v_cvt_pk_bf16_f32 v144, v64, v65\n\t" \
   "v_cvt_pk_bf16_f32 v145, v66, v67\n\t" "v_mfma_f32_16x16x32_bf16 v[76:79], v[172:175], v[244:247], v[76:79]\n\t" \
   "v_cvt_pk_bf16_f32 v146, v68, v69\n\t" "v_cvt_pk_bf16_f32 v147, v70, v71\n\t" \
   "v_mfma_f32_16x16x32_bf16 v[72:75], v[160:163], v[248:251], v[72:75]\n\t" "ds_read_b128 v[100:103], v28 offset:5120\n\t" \
   "v_mfma_f32_16x16x32_bf16 v[76:79], v[176:179], v[248:251], v[76:79]\n\t" "s_add_u32 s50, s49, 262144\n\t" "v_xor_b32 v15, 0x2000, v15\n\t" \
   "v_mfma_f32_16x16x32_bf16 v[72:75], v[164:167], v[252:255], v[72:75]\n\t" "ds_read_b128 v[104:107], v28 offset:6144\n\t" \
   "buffer_store_dwordx4 v[144:147], v21, %[rs], s50 offen sc1\n\t" "v_mfma_f32_16x16x32_bf16 v[76:79], v[180:183], v[252:255], v[76:79]\n\t" \
   "v_mov_b32 v18, v21\n\t" "s_cmp_lt_u32 s40, 64\n\t" "s_cbranch_scc1 1b\n\t" "s_nop 7\n\t" "s_waitcnt lgkmcnt(0)\n\t" \
   "v_cvt_pk_bf16_f32 v148, v72, v73\n\t" "v_cvt_pk_bf16_f32 v149, v74, v75\n\t" "v_cvt_pk_bf16_f32 v150, v76, v77\n\t" \
   "v_cvt_pk_bf16_f32 v151, v78, v79\n\t" "s_add_u32 s50, s49, 294912\n\t" "buffer_store_dwordx4 v[148:151], v21, %[rs], s50 offen sc1\n\t" \
   "s_sub_i32 s49, s41, 1\n\t" "s_mul_i32 s49, s49, 1310720\n\t" "ds_read_b128 v[152:155], v15 offset:0\n\t" \
   "ds_read_b128 v[168:171], v15 offset:4096\n\t" "ds_read_b128 v[156:159], v15 offset:1024\n\t" "ds_read_b128 v[172:175], v15 offset:5120\n\t" \
   "ds_read_b128 v[160:163], v15 offset:2048\n\t" "ds_read_b128 v[176:179], v15 offset:6144\n\t" "ds_read_b128 v[164:167], v15 offset:3072\n\t" \
   "ds_read_b128 v[180:183], v15 offset:7168\n\t" "s_waitcnt lgkmcnt(4)\n\t" "v_mfma_f32_16x16x32_bf16 v[64:67], v[152:155], a[160:163], 0\n\t" \
   "v_mfma_f32_16x16x32_bf16 v[68:71], v[168:171], a[160:163], 0\n\t" "v_mfma_f32_16x16x32_bf16 v[64:67], v[156:159], a[164:167], v[64:67]\n\t" \
   "v_mfma_f32_16x16x32_bf16 v[68:71], v[172:175], a[164:167], v[68:71]\n\t" "s_waitcnt lgkmcnt(0)\n\t" \
   "v_mfma_f32_16x16x32_bf16 v[64:67], v[160:163], a[168:171], v[64:67]\n\t" "v_mfma_f32_16x16x32_bf16 v[68:71], v[176:179], a[168:171], v[68:71]\n\t" \
   "v_mfma_f32_16x16x32_bf16 v[64:67], v[164:167], a[172:175], v[64:67]\n\t" "v_mfma_f32_16x16x32_bf16 v[68:71], v[180:183], a[172:175], v[68:71]\n\t" \
   "v_mfma_f32_16x16x32_bf16 v[72:75], v[152:155], a[176:179], 0\n\t" "v_mfma_f32_16x16x32_bf16 v[76:79], v[168:171], a[176:179], 0\n\t" \
   "v_mfma_f32_16x16x32_bf16 v[72:75], v[156:159], a[180:183], v[72:75]\n\t" "v_cvt_pk_bf16_f32 v144, v64, v65\n\t" \
   "v_cvt_pk_bf16_f32 v145, v66, v67\n\t" "v_mfma_f32_16x16x32_bf16 v[76:79], v[172:175], a[180:183], v[76:79]\n\t" \
   "v_cvt_pk_bf16_f32 v146, v68, v69\n\t" "v_cvt_pk_bf16_f32 v147, v70, v71\n\t" \
   "v_mfma_f32_16x16x32_bf16 v[72:75], v[160:163], a[184:187], v[72:75]\n\t" "s_add_u32 s50, s49, 0\n\t" \
   "v_mfma_f32_16x16x32_bf16 v[76:79], v[176:179], a[184:187], v[76:79]\n\t" "buffer_store_dwordx4 v[144:147], v21, %[rs], s50 offen sc1\n\t" \
   "v_mfma_f32_16x16x32_bf16 v[72:75], v[164:167], a[188:191], v[72:75]\n\t" "v_mfma_f32_16x16x32_bf16 v[76:79], v[180:183], a[188:191], v[76:79]\n\t" \
   "v_mfma_f32_16x16x32_bf16 v[64:67], v[152:155], a[192:195], 0\n\t" "v_mfma_f32_16x16x32_bf16 v[68:71], v[168:171], a[192:195], 0\n\t" \
   "v_mfma_f32_16x16x32_bf16 v[64:67], v[156:159], a[196:199], v[64:67]\n\t" "v_cvt_pk_bf16_f32 v148, v72, v73\n\t" \
   "v_cvt_pk_bf16_f32 v149, v74, v75\n\t" "v_mfma_f32_16x16x32_bf16 v[68:71], v[172:175], a[196:199], v[68:71]\n\t" \
   "v_cvt_pk_bf16_f32 v150, v76, v77\n\t" "v_cvt_pk_bf16_f32 v151, v78, v79\n\t" \
   "v_mfma_f32_16x16x32_bf16 v[64:67], v[160:163], a[200:203], v[64:67]\n\t" "s_add_u32 s50, s49, 32768\n\t" \
   "v_mfma_f32_16x16x32_bf16 v[68:71], v[176:179], a[200:203], v[68:71]\n\t" "buffer_store_dwordx4 v[148:151], v21, %[rs], s50 offen sc1\n\t" \
   "v_mfma_f32_16x16x32_bf16 v[64:67], v[164:167], a[204:207], v[64:67]\n\t" "v_mfma_f32_16x16x32_bf16 v[68:71], v[180:183], a[204:207], v[68:71]\n\t" \
   "v_mfma_f32_16x16x32_bf16 v[72:75], v[152:155], a[208:211], 0\n\t" "v_mfma_f32_16x16x32_bf16 v[76:79], v[168:171], a[208:211], 0\n\t" \
   "v_mfma_f32_16x16x32_bf16 v[72:75], v[156:159], a[212:215], v[72:75]\n\t" "v_cvt_pk_bf16_f32 v144, v64, v65\n\t" \
   "v_cvt_pk_bf16_f32 v145, v66, v67\n\t" "v_mfma_f32_16x16x32_bf16 v[76:79], v[172:175], a[212:215], v[76:79]\n\t" \
   "v_cvt_pk_bf16_f32 v146, v68, v69\n\t" "v_cvt_pk_bf16_f32 v147, v70, v71\n\t" \
   "v_mfma_f32_16x16x32_bf16 v[72:75], v[160:163], a[216:219], v[72:75]\n\t" "s_add_u32 s50, s49, 65536\n\t" \
   "v_mfma_f32_16x16x32_bf16 v[76:79], v[176:179], a[216:219], v[76:79]\n\t" "buffer_store_dwordx4 v[144:147], v21, %[rs], s50 offen sc1\n\t" \
   "v_mfma_f32_16x16x32_bf16 v[72:75], v[164:167], a[220:223], v[72:75]\n\t" "v_mfma_f32_16x16x32_bf16 v[76:79], v[180:183], a[220:223], v[76:79]\n\t" \
   "v_mfma_f32_16x16x32_bf16 v[64:67], v[152:155], a[224:227], 0\n\t" "v_mfma_f32_16x16x32_bf16 v[68:71], v[168:171], a[224:227], 0\n\t" \
   "v_mfma_f32_16x16x32_bf16 v[64:67], v[156:159], a[228:231], v[64:67]\n\t" "v_cvt_pk_bf16_f32 v148, v72, v73\n\t" \
   "v_cvt_pk_bf16_f32 v149, v74, v75\n\t" "v_mfma_f32_16x16x32_bf16 v[68:71], v[172:175], a[228:231], v[68:71]\n\t" \
   "v_cvt_pk_bf16_f32 v150, v76, v77\n\t" "v_cvt_pk_bf16_f32 v151, v78, v79\n\t" \
   "v_mfma_f32_16x16x32_bf16 v[64:67], v[160:163], a[232:235], v[64:67]\n\t" "s_add_u32 s50, s49, 98304\n\t" \
   "v_mfma_f32_16x16x32_bf16 v[68:71], v[176:179], a[232:235], v[68:71]\n\t" "buffer_store_dwordx4 v[148:151], v21, %[rs], s50 offen sc1\n\t" \
   "v_mfma_f32_16x16x32_bf16 v[64:67], v[164:167], a[236:239], v[64:67]\n\t" "v_mfma_f32_16x16x32_bf16 v[68:71], v[180:183], a[236:239], v[68:71]\n\t" \
   "v_mfma_f32_16x16x32_bf16 v[72:75], v[152:155], a[240:243], 0\n\t" "v_mfma_f32_16x16x32_bf16 v[76:79], v[168:171], a[240:243], 0\n\t" \
   "v_mfma_f32_16x16x32_bf16 v[72:75], v[156:159], a[244:247], v[72:75]\n\t" "v_cvt_pk_bf16_f32 v144, v64, v65\n\t" \
   "v_cvt_pk_bf16_f32 v145, v66, v67\n\t" "v_mfma_f32_16x16x32_bf16 v[76:79], v[172:175], a[244:247], v[76:79]\n\t" \
   "v_cvt_pk_bf16_f32 v146, v68, v69\n\t" "v_cvt_pk_bf16_f32 v147, v70, v71\n\t" \
   "v_mfma_f32_16x16x32_bf16 v[72:75], v[160:163], a[248:251], v[72:75]\n\t" "s_add_u32 s50, s49, 131072\n\t" \
   "v_mfma_f32_16x16x32_bf16 v[76:79], v[176:179], a[248:251], v[76:79]\n\t" "buffer_store_dwordx4 v[144:147], v21, %[rs], s50 offen sc1\n\t" \
   "v_mfma_f32_16x16x32_bf16 v[72:75], v[164:167], a[252:255], v[72:75]\n\t" "v_mfma_f32_16x16x32_bf16 v[76:79], v[180:183], a[252:255], v[76:79]\n\t" \
   "v_mfma_f32_16x16x32_bf16 v[64:67], v[152:155], v[192:195], 0\n\t" "v_mfma_f32_16x16x32_bf16 v[68:71], v[168:171], v[192:195], 0\n\t" \
   "v_mfma_f32_16x16x32_bf16 v[64:67], v[156:159], v[196:199], v[64:67]\n\t" "v_cvt_pk_bf16_f32 v148, v72, v73\n\t" \
   "v_cvt_pk_bf16_f32 v149, v74, v75\n\t" "v_mfma_f32_16x16x32_bf16 v[68:71], v[172:175], v[196:199], v[68:71]\n\t" \
   "v_cvt_pk_bf16_f32 v150, v76, v77\n\t" "v_cvt_pk_bf16_f32 v151, v78, v79\n\t" \
   "v_mfma_f32_16x16x32_bf16 v[64:67], v[160:163], v[200:203], v[64:67]\n\t" "s_add_u32 s50, s49, 163840\n\t" \
   "v_mfma_f32_16x16x32_bf16 v[68:71], v[176:179], v[200:203], v[68:71]\n\t" "buffer_store_dwordx4 v[148:151], v21, %[rs], s50 offen sc1\n\t" \
   "v_mfma_f32_16x16x32_bf16 v[64:67], v[164:167], v[204:207], v[64:67]\n\t" "v_mfma_f32_16x16x32_bf16 v[68:71], v[180:183], v[204:207], v[68:71]\n\t" \
   "v_mfma_f32_16x16x32_bf16 v[72:75], v[152:155], v[208:211], 0\n\t" "v_mfma_f32_16x16x32_bf16 v[76:79], v[168:171], v[208:211], 0\n\t" \
   "v_mfma_f32_16x16x32_bf16 v[72:75], v[156:159], v[212:215], v[72:75]\n\t" "v_cvt_pk_bf16_f32 v144, v64, v65\n\t" \
   "v_cvt_pk_bf16_f32 v145, v66, v67\n\t" "v_mfma_f32_16x16x32_bf16 v[76:79], v[172:175], v[212:215], v[76:79]\n\t" \
   "v_cvt_pk_bf16_f32 v146, v68, v69\n\t" "v_cvt_pk_bf16_f32 v147, v70, v71\n\t" \
   "v_mfma_f32_16x16x32_bf16 v[72:75], v[160:163], v[216:219], v[72:75]\n\t" "s_add_u32 s50, s49, 196608\n\t" \
   "v_mfma_f32_16x16x32_bf16 v[76:79], v[176:179], v[216:219], v[76:79]\n\t" "buffer_store_dwordx4 v[144:147], v21, %[rs], s50 offen sc1\n\t" \
   "v_mfma_f32_16x16x32_bf16 v[72:75], v[164:167], v[220:223], v[72:75]\n\t" "v_mfma_f32_16x16x32_bf16 v[76:79], v[180:183], v[220:223], v[76:79]\n\t" \
   "v_mfma_f32_16x16x32_bf16 v[64:67], v[152:155], v[224:227], 0\n\t" "v_mfma_f32_16x16x32_bf16 v[68:71], v[168:171], v[224:227], 0\n\t" \
   "v_mfma_f32_16x16x32_bf16 v[64:67], v[156:159], v[228:231], v[64:67]\n\t" "v_cvt_pk_bf16_f32 v148, v72, v73\n\t" \
   "v_cvt_pk_bf16_f32 v149, v74, v75\n\t" "v_mfma_f32_16x16x32_bf16 v[68:71], v[172:175], v[228:231], v[68:71]\n\t" \
   "v_cvt_pk_bf16_f32 v150, v76, v77\n\t" "v_cvt_pk_bf16_f32 v151, v78, v79\n\t" \
   "v_mfma_f32_16x16x32_bf16 v[64:67], v[160:163], v[232:235], v[64:67]\n\t" "s_add_u32 s50, s49, 229376\n\t" \
   "v_mfma_f32_16x16x32_bf16 v[68:71], v[176:179], v[232:235], v[68:71]\n\t" "buffer_store_dwordx4 v[148:151], v21, %[rs], s50 offen sc1\n\t" \
   "v_mfma_f32_16x16x32_bf16 v[64:67], v[164:167], v[236:239], v[64:67]\n\t" "v_mfma_f32_16x16x32_bf16 v[68:71], v[180:183], v[236:239], v[68:71]\n\t" \
   "v_mfma_f32_16x16x32_bf16 v[72:75], v[152:155], v[240:243], 0\n\t" "v_mfma_f32_16x16x32_bf16 v[76:79], v[168:171], v[240:243], 0\n\t" \
   "v_mfma_f32_16x16x32_bf16 v[72:75], v[156:159], v[244:247], v[72:75]\n\t" "v_cvt_pk_bf16_f32 v144, v64, v65\n\t" \
   "v_cvt_pk_bf16_f32 v145, v66, v67\n\t" "v_mfma_f32_16x16x32_bf16 v[76:79], v[172:175], v[244:247], v[76:79]\n\t" \
   "v_cvt_pk_bf16_f32 v146, v68, v69\n\t" "v_cvt_pk_bf16_f32 v147, v70, v71\n\t" \
   "v_mfma_f32_16x16x32_bf16 v[72:75], v[160:163], v[248:251], v[72:75]\n\t" "s_add_u32 s50, s49, 262144\n\t" \
   "v_mfma_f32_16x16x32_bf16 v[76:79], v[176:179], v[248:251], v[76:79]\n\t" "buffer_store_dwordx4 v[144:147], v21, %[rs], s50 offen sc1\n\t" \
   "v_mfma_f32_16x16x32_bf16 v[72:75], v[164:167], v[252:255], v[72:75]\n\t" "v_mfma_f32_16x16x32_bf16 v[76:79], v[180:183], v[252:255], v[76:79]\n\t" \
   "s_nop 7\n\t" "v_cvt_pk_bf16_f32 v148, v72, v73\n\t" "v_cvt_pk_bf16_f32 v149, v74, v75\n\t" "v_cvt_pk_bf16_f32 v150, v76, v77\n\t" \
   "v_cvt_pk_bf16_f32 v151, v78, v79\n\t" "s_add_u32 s50, s49, 294912\n\t" "buffer_store_dwordx4 v[148:151], v21, %[rs], s50 offen sc1\n\t" \
   "s_waitcnt vmcnt(0)\n\t" "s_cmp_lt_i32 s40, 2\n\t" "s_cselect_b32 s47, 0x7ffffff0, 0\n\t" "v_or_b32 v30, s47, v20\n\t" "s_sub_i32 s47, s41, 2\n\t" \
   "s_max_i32 s47, s47, 0\n\t" "s_lshl_b32 s47, s47, 12\n\t" "buffer_store_dword v31, v30, %[rs], s47 offen sc1\n\t" "s_cmp_lt_i32 s40, 1\n\t" \
   "s_cselect_b32 s47, 0x7ffffff0, 0\n\t" "v_or_b32 v30, s47, v20\n\t" "s_sub_i32 s47, s41, 1\n\t" "s_max_i32 s47, s47, 0\n\t" \
   "s_lshl_b32 s47, s47, 12\n\t" "buffer_store_dword v31, v30, %[rs], s47 offen sc1\n\t" "s_branch 9f\n\t" "172:\n\t" "s_mov_b32 s47, 0\n\t" \
   "buffer_load_dword v30, v19, %[rs], 0 offen sc1\n\t" "s_waitcnt vmcnt(0)\n\t" "v_add_u32 v30, 1, v30\n\t" "v_cmp_lt_u32 vcc, 9, v30\n\t" \
   "s_mov_b32 s44, vcc_lo\n\t" "s_bitcmp1_b32 s44, s48\n\t" "s_cbranch_scc1 72b\n\t" "s_bitset1_b32 s44, s48\n\t" "s_lshl_b32 s48, s48, 7\n\t" \
   "s_add_u32 s48, s48, %[cnt]\n\t" "73:\n\t" "s_sleep 10\n\t" "buffer_load_dword v30, off, %[rs], s48 sc1\n\t" "s_add_u32 s47, s47, 1\n\t" \
   "s_waitcnt vmcnt(0)\n\t" "v_add_u32 v30, 1, v30\n\t" "v_cmp_lt_u32 vcc, 9, v30\n\t" "s_cmp_gt_u32 s47, 0x20000\n\t" "s_cbranch_scc1 72b\n\t" \
   "s_cbranch_vccz 73b\n\t" "s_branch 72b\n\t" "176:\n\t" "s_mov_b32 s47, 0\n\t" "buffer_load_dword v30, v19, %[rs], 0 offen sc1\n\t" \
   "s_waitcnt vmcnt(0)\n\t" "v_add_u32 v30, 1, v30\n\t" "v_cmp_lt_u32 vcc, 9, v30\n\t" "s_mov_b32 s44, vcc_lo\n\t" "s_bitcmp1_b32 s44, s48\n\t" \
   "s_cbranch_scc1 76b\n\t" "s_bitset1_b32 s44, s48\n\t" "s_lshl_b32 s48, s48, 7\n\t" "s_add_u32 s48, s48, %[cnt]\n\t" "77:\n\t" "s_sleep 10\n\t" \
   "buffer_load_dword v30, off, %[rs], s48 sc1\n\t" "s_add_u32 s47, s47, 1\n\t" "s_waitcnt vmcnt(0)\n\t" "v_add_u32 v30, 1, v30\n\t" \
   "v_cmp_lt_u32 vcc, 9, v30\n\t" "s_cmp_gt_u32 s47, 0x20000\n\t" "s_cbranch_scc1 76b\n\t" "s_cbranch_vccz 77b\n\t" "s_branch 76b\n\t" "174:\n\t" \
   "s_mov_b32 s47, 0\n\t" "buffer_load_dword v30, v19, %[rs], 0 offen sc1\n\t" "s_waitcnt vmcnt(0)\n\t" "v_add_u32 v30, 1, v30\n\t" \
   "v_cmp_lt_u32 vcc, 9, v30\n\t" "s_mov_b32 s44, vcc_lo\n\t" "s_bitcmp1_b32 s44, s48\n\t" "s_cbranch_scc1 74b\n\t" "s_bitset1_b32 s44, s48\n\t" \
   "s_lshl_b32 s48, s48, 7\n\t" "s_add_u32 s48, s48, %[cnt]\n\t" "75:\n\t" "s_sleep 10\n\t" "buffer_load_dword v30, off, %[rs], s48 sc1\n\t" \
   "s_add_u32 s47, s47, 1\n\t" "s_waitcnt vmcnt(0)\n\t" "v_add_u32 v30, 1, v30\n\t" "v_cmp_lt_u32 vcc, 9, v30\n\t" "s_cmp_gt_u32 s47, 0x20000\n\t" \
   "s_cbranch_scc1 74b\n\t" "s_cbranch_vccz 75b\n\t" "s_branch 74b\n\t" "9:\n\t" "s_waitcnt vmcnt(0)"
#define VU_CLOB "v8", "v9", "v10", "v11", "v12", "v13", "v14", "v15", "v32", "v33", "v34", "v35", "v36", "v37", "v38", "v39", "v40", "v41", "v42", "v43", "v44", "v45", "v46", "v47", "v48", "v49", "v50", "v51", "v52", "v53", "v54", "v55", "v56", "v57", "v58", "v59", "v60", "v61", "v62", "v63", "v64", "v65", "v66", "v67", "v68", "v69", "v70", "v71", "v72", "v73", "v74", "v75", "v76", "v77", "v78", "v79", "v80", "v81", "v82", "v83", "v84", "v85", "v86", "v87", "v88", "v89", "v90", "v91", "v92", "v93", "v94", "v95", "v96", "v97", "v98", "v99", "v100", "v101", "v102", "v103", "v104", "v105", "v106", "v107", "v108", "v109", "v110", "v111", "v112", "v113", "v114", "v115", "v116", "v117", "v118", "v119", "v120", "v121", "v122", "v123", "v124", "v125", "v126", "v127", "v128", "v129", "v130", "v131", "v132", "v133", "v134", "v135", "v136", "v137", "v138", "v139", "v140", "v141", "v142", "v143", "v144", "v145", "v146", "v147", "v148", "v149", "v150", "v151", "v152", "v153", "v154", "v155", "v156", "v157", "v158", "v159", "v160", "v161", "v162", "v163", "v164", "v165", "v166", "v167", "v168", "v169", "v170", "v171", "v172", "v173", "v174", "v175", "v176", "v177", "v178", "v179", "v180", "v181", "v182", "v183", "s40", "s41", "s42", "s43", "s44", "s45", "s46", "s47", "s48", "s49", "s50", "s51", "s52", "s53", "s54", "s55"
#define DF_ASM \
   "v_mov_b32 v80, 0x3f803f80\n\t" "v_mov_b32 v81, 0x3f803f80\n\t" "v_mov_b32 v82, 0x3f803f80\n\t" "v_mov_b32 v83, 0x3f803f80\n\t" \
   "s_mov_b32 s45, %[set]\n\t" "s_mov_b32 s46, 0\n\t" "v_mfma_f32_16x16x32_bf16 v[32:35], a[0:3], v[80:83], 0\n\t" \
   "v_mfma_f32_16x16x32_bf16 v[32:35], a[4:7], v[80:83], v[32:35]\n\t" "v_mfma_f32_16x16x32_bf16 v[32:35], a[8:11], v[80:83], v[32:35]\n\t" \
   "v_mfma_f32_16x16x32_bf16 v[32:35], a[12:15], v[80:83], v[32:35]\n\t" "v_mfma_f32_16x16x32_bf16 v[32:35], a[16:19], v[80:83], v[32:35]\n\t" \
   "v_mfma_f32_16x16x32_bf16 v[32:35], a[20:23], v[80:83], v[32:35]\n\t" "v_mfma_f32_16x16x32_bf16 v[32:35], a[24:27], v[80:83], v[32:35]\n\t" \
   "v_mfma_f32_16x16x32_bf16 v[32:35], a[28:31], v[80:83], v[32:35]\n\t" "v_mfma_f32_16x16x32_bf16 v[32:35], a[32:35], v[80:83], v[32:35]\n\t" \
   "v_mfma_f32_16x16x32_bf16 v[32:35], a[36:39], v[80:83], v[32:35]\n\t" "v_mfma_f32_16x16x32_bf16 v[32:35], a[40:43], v[80:83], v[32:35]\n\t" \
   "v_mfma_f32_16x16x32_bf16 v[32:35], a[44:47], v[80:83], v[32:35]\n\t" "v_mfma_f32_16x16x32_bf16 v[32:35], a[48:51], v[80:83], v[32:35]\n\t" \
   "v_mfma_f32_16x16x32_bf16 v[32:35], a[52:55], v[80:83], v[32:35]\n\t" "v_mfma_f32_16x16x32_bf16 v[32:35], a[56:59], v[80:83], v[32:35]\n\t" \
   "v_mfma_f32_16x16x32_bf16 v[32:35], a[60:63], v[80:83], v[32:35]\n\t" "v_mfma_f32_16x16x32_bf16 v[32:35], a[64:67], v[80:83], v[32:35]\n\t" \
   "v_mfma_f32_16x16x32_bf16 v[32:35], a[68:71], v[80:83], v[32:35]\n\t" "v_mfma_f32_16x16x32_bf16 v[32:35], a[72:75], v[80:83], v[32:35]\n\t" \
   "v_mfma_f32_16x16x32_bf16 v[32:35], a[76:79], v[80:83], v[32:35]\n\t" "v_mfma_f32_16x16x32_bf16 v[36:39], a[80:83], v[80:83], 0\n\t" \
   "v_mfma_f32_16x16x32_bf16 v[36:39], a[84:87], v[80:83], v[36:39]\n\t" "v_mfma_f32_16x16x32_bf16 v[36:39], a[88:91], v[80:83], v[36:39]\n\t" \
   "v_mfma_f32_16x16x32_bf16 v[36:39], a[92:95], v[80:83], v[36:39]\n\t" "v_mfma_f32_16x16x32_bf16 v[36:39], a[96:99], v[80:83], v[36:39]\n\t" \
   "v_mfma_f32_16x16x32_bf16 v[36:39], a[100:103], v[80:83], v[36:39]\n\t" "v_mfma_f32_16x16x32_bf16 v[36:39], a[104:107], v[80:83], v[36:39]\n\t" \
   "v_mfma_f32_16x16x32_bf16 v[36:39], a[108:111], v[80:83], v[36:39]\n\t" "v_mfma_f32_16x16x32_bf16 v[36:39], a[112:115], v[80:83], v[36:39]\n\t" \
   "v_mfma_f32_16x16x32_bf16 v[36:39], a[116:119], v[80:83], v[36:39]\n\t" "v_mfma_f32_16x16x32_bf16 v[36:39], a[120:123], v[80:83], v[36:39]\n\t" \
   "v_mfma_f32_16x16x32_bf16 v[36:39], a[124:127], v[80:83], v[36:39]\n\t" "v_mfma_f32_16x16x32_bf16 v[36:39], a[128:131], v[80:83], v[36:39]\n\t" \
   "v_mfma_f32_16x16x32_bf16 v[36:39], a[132:135], v[80:83], v[36:39]\n\t" "v_mfma_f32_16x16x32_bf16 v[36:39], a[136:139], v[80:83], v[36:39]\n\t" \
   "v_mfma_f32_16x16x32_bf16 v[36:39], a[140:143], v[80:83], v[36:39]\n\t" "v_mfma_f32_16x16x32_bf16 v[36:39], a[144:147], v[80:83], v[36:39]\n\t" \
   "v_mfma_f32_16x16x32_bf16 v[36:39], a[148:151], v[80:83], v[36:39]\n\t" "v_mfma_f32_16x16x32_bf16 v[36:39], a[152:155], v[80:83], v[36:39]\n\t" \
   "v_mfma_f32_16x16x32_bf16 v[36:39], a[156:159], v[80:83], v[36:39]\n\t" "1:\n\t" "s_lshl_b32 s48, s45, 7\n\t" "s_mov_b32 s47, 0\n\t" \
   "s_cmp_lg_u32 %[ldx], %[ld0]\n\t" "s_cbranch_scc1 3f\n\t" "2:\n\t" "buffer_load_dword v30, v19, %[rs], s48 offen sc1\n\t" \
   "s_add_u32 s47, s47, 1\n\t" "s_waitcnt vmcnt(0)\n\t" "v_readfirstlane_b32 s44, v30\n\t" "s_add_u32 s44, s44, 1\n\t" "s_cmp_ge_u32 s44, 5\n\t" \
   "s_cbranch_scc1 3f\n\t" "s_cmp_gt_u32 s47, 0x20000\n\t" "s_cbranch_scc1 3f\n\t" "s_sleep 3\n\t" "s_cmp_lg_u32 s44, 0\n\t" "s_cbranch_scc1 2b\n\t" \
   "s_sleep 8\n\t" "s_branch 2b\n\t" "3:\n\t" "s_barrier\n\t" "s_mul_i32 s52, s45, 1536\n\t" "s_mul_i32 s51, s45, 40960\n\t" \
   "s_add_u32 m0, s46, %[ldx]\n\t" "s_mov_b32 s53, s46\n\t" "buffer_load_dwordx4 v16, %[rs], s51 offen offset:0 sc1 lds\n\t" \
   "buffer_load_dwordx4 v16, %[rs], s51 offen offset:1024 sc1 lds\n\t" "buffer_load_dwordx4 v16, %[rs], s51 offen offset:2048 sc1 lds\n\t" \
   "buffer_load_dwordx4 v16, %[rs], s51 offen offset:3072 sc1 lds\n\t" "s_add_u32 m0, m0, 4096\n\t" "s_add_u32 s51, s51, 4096\n\t" \
   "buffer_load_dwordx4 v16, %[rs], s51 offen offset:0 sc1 lds\n\t" "buffer_load_dwordx4 v16, %[rs], s51 offen offset:1024 sc1 lds\n\t" \
   "buffer_load_dwordx4 v16, %[rs], s51 offen offset:2048 sc1 lds\n\t" "buffer_load_dwordx4 v16, %[rs], s51 offen offset:3072 sc1 lds\n\t" \
   "s_add_u32 m0, m0, 4096\n\t" "s_add_u32 s51, s51, 4096\n\t" "buffer_load_dwordx4 v16, %[rs], s51 offen offset:0 sc1 lds\n\t" \
   "buffer_load_dwordx4 v16, %[rs], s51 offen offset:1024 sc1 lds\n\t" "s_add_u32 m0, s53, %[ld1]\n\t" "s_nop 0\n\t" \
   "buffer_load_dwordx4 v17, %[rs], s52 offen sc1 lds\n\t" "v_add_u32 v26, s46, v22\n\t" "v_add_u32 v27, s46, v23\n\t" "s_waitcnt vmcnt(0)\n\t" \
   "s_barrier\n\t" "ds_read_b128 v[80:83], v26 offset:0\n\t" "ds_read_b128 v[84:87], v26 offset:1024\n\t" "ds_read_b128 v[88:91], v26 offset:2048\n\t" \
   "ds_read_b128 v[92:95], v26 offset:3072\n\t" "ds_read_b128 v[96:99], v26 offset:4096\n\t" "ds_read_b128 v[100:103], v26 offset:5120\n\t" \
   "ds_read_b128 v[104:107], v26 offset:6144\n\t" "s_waitcnt lgkmcnt(5)\n\t" "v_mfma_f32_16x16x32_bf16 v[52:55], a[80:83], v[80:83], 0\n\t" \
   "ds_read_b128 v[112:115], v27 offset:0\n\t" "v_mfma_f32_16x16x32_bf16 v[48:51], a[0:3], v[80:83], 0\n\t" \
   "ds_read_b128 v[108:111], v26 offset:7168\n\t" "ds_read_b128 v[116:119], v27 offset:16\n\t" \
   "v_mfma_f32_16x16x32_bf16 v[52:55], a[84:87], v[84:87], v[52:55]\n\t" "ds_read_b64 v[120:121], v27 offset:32\n\t" \
   "v_mfma_f32_16x16x32_bf16 v[48:51], a[4:7], v[84:87], v[48:51]\n\t" "ds_read_b128 v[80:83], v26 offset:8192\n\t" "s_waitcnt lgkmcnt(8)\n\t" \
   "v_mfma_f32_16x16x32_bf16 v[52:55], a[88:91], v[88:91], v[52:55]\n\t" "ds_read_b128 v[122:125], v27 offset:768\n\t" \
   "v_mfma_f32_16x16x32_bf16 v[48:51], a[8:11], v[88:91], v[48:51]\n\t" "ds_read_b128 v[84:87], v26 offset:9216\n\t" \
   "ds_read_b128 v[126:129], v27 offset:784\n\t" "v_mfma_f32_16x16x32_bf16 v[52:55], a[92:95], v[92:95], v[52:55]\n\t" \
   "v_mfma_f32_16x16x32_bf16 v[48:51], a[12:15], v[92:95], v[48:51]\n\t" "ds_read_b128 v[88:91], v26 offset:10240\n\t" \
   "ds_read_b64 v[130:131], v27 offset:800\n\t" "s_waitcnt lgkmcnt(11)\n\t" "v_mfma_f32_16x16x32_bf16 v[52:55], a[96:99], v[96:99], v[52:55]\n\t" \
   "s_lshl_b32 s48, s45, 9\n\t" "v_mfma_f32_16x16x32_bf16 v[48:51], a[16:19], v[96:99], v[48:51]\n\t" "ds_read_b128 v[92:95], v26 offset:11264\n\t" \
   "s_add_u32 s47, s48, 131072\n\t" "v_mfma_f32_16x16x32_bf16 v[52:55], a[100:103], v[100:103], v[52:55]\n\t" \
   "v_mfma_f32_16x16x32_bf16 v[48:51], a[20:23], v[100:103], v[48:51]\n\t" "ds_read_b128 v[96:99], v26 offset:12288\n\t" "s_waitcnt lgkmcnt(8)\n\t" \
   "v_add_f32 v112, v112, v114\n\t" "v_mfma_f32_16x16x32_bf16 v[52:55], a[104:107], v[104:107], v[52:55]\n\t" "v_add_f32 v113, v113, v115\n\t" \
   "v_mfma_f32_16x16x32_bf16 v[48:51], a[24:27], v[104:107], v[48:51]\n\t" "ds_read_b128 v[100:103], v26 offset:13312\n\t" \
   "v_mfma_f32_16x16x32_bf16 v[52:55], a[108:111], v[108:111], v[52:55]\n\t" "v_add_f32 v116, v116, v118\n\t" \
   "v_mfma_f32_16x16x32_bf16 v[48:51], a[28:31], v[108:111], v[48:51]\n\t" "ds_read_b128 v[104:107], v26 offset:14336\n\t" \
   "v_add_f32 v117, v117, v119\n\t" "s_waitcnt lgkmcnt(7)\n\t" "v_mfma_f32_16x16x32_bf16 v[52:55], a[112:115], v[80:83], v[52:55]\n\t" \
   "v_mfma_f32_16x16x32_bf16 v[48:51], a[32:35], v[80:83], v[48:51]\n\t" "ds_read_b128 v[108:111], v26 offset:15360\n\t" \
   "v_add_f32 v112, v112, v120\n\t" "v_mfma_f32_16x16x32_bf16 v[52:55], a[116:119], v[84:87], v[52:55]\n\t" "v_add_f32 v113, v113, v121\n\t" \
   "v_mfma_f32_16x16x32_bf16 v[48:51], a[36:39], v[84:87], v[48:51]\n\t" "ds_read_b128 v[80:83], v26 offset:16384\n\t" "v_add_f32 v112, v112, v116\n\t" \
   "s_waitcnt lgkmcnt(5)\n\t" "v_mfma_f32_16x16x32_bf16 v[52:55], a[120:123], v[88:91], v[52:55]\n\t" \
   "v_mfma_f32_16x16x32_bf16 v[48:51], a[40:43], v[88:91], v[48:51]\n\t" "ds_read_b128 v[84:87], v26 offset:17408\n\t" "v_add_f32 v113, v113, v117\n\t" \
   "v_mfma_f32_16x16x32_bf16 v[52:55], a[124:127], v[92:95], v[52:55]\n\t" "v_mul_f32 v112, 0xbacccccd, v112\n\t" \
   "v_mfma_f32_16x16x32_bf16 v[48:51], a[44:47], v[92:95], v[48:51]\n\t" "ds_read_b128 v[88:91], v26 offset:18432\n\t" "s_waitcnt lgkmcnt(5)\n\t" \
   "v_mfma_f32_16x16x32_bf16 v[52:55], a[128:131], v[96:99], v[52:55]\n\t" "v_mul_f32 v113, 0x3acccccd, v113\n\t" \
   "v_mfma_f32_16x16x32_bf16 v[48:51], a[48:51], v[96:99], v[48:51]\n\t" "ds_read_b128 v[92:95], v26 offset:19456\n\t" \
   "v_fma_f32 v113, -v112, v112, v113\n\t" "v_mfma_f32_16x16x32_bf16 v[52:55], a[132:135], v[100:103], v[52:55]\n\t" "v_max_f32 v113, 0, v113\n\t" \
   "v_mfma_f32_16x16x32_bf16 v[48:51], a[52:55], v[100:103], v[48:51]\n\t" "ds_read_b128 v[96:99], v26 offset:20480\n\t" "s_waitcnt lgkmcnt(5)\n\t" \
   "v_mfma_f32_16x16x32_bf16 v[52:55], a[136:139], v[104:107], v[52:55]\n\t" "v_cvt_pk_bf16_f32 v113, v113, v112\n\t" \
   "v_mfma_f32_16x16x32_bf16 v[48:51], a[56:59], v[104:107], v[48:51]\n\t" "ds_read_b128 v[100:103], v26 offset:21504\n\t" \
   "v_and_b32 v8, 0xffff0000, v113\n\t" "v_mfma_f32_16x16x32_bf16 v[52:55], a[140:143], v[108:111], v[52:55]\n\t" \
   "v_mfma_f32_16x16x32_bf16 v[48:51], a[60:63], v[108:111], v[48:51]\n\t" "ds_read_b128 v[104:107], v26 offset:22528\n\t" \
   "v_lshlrev_b32 v113, 16, v113\n\t" "s_waitcnt lgkmcnt(5)\n\t" "v_mfma_f32_16x16x32_bf16 v[52:55], a[144:147], v[80:83], v[52:55]\n\t" \
   "v_add_f32 v113, 0x3727c5ac, v113\n\t" "v_mfma_f32_16x16x32_bf16 v[48:51], a[64:67], v[80:83], v[48:51]\n\t" \
   "ds_read_b128 v[108:111], v26 offset:23552\n\t" "v_mfma_f32_16x16x32_bf16 v[52:55], a[148:151], v[84:87], v[52:55]\n\t" \
   "v_cvt_pk_bf16_f32 v113, v113, v113\n\t" "v_mfma_f32_16x16x32_bf16 v[48:51], a[68:71], v[84:87], v[48:51]\n\t" \
   "ds_read_b128 v[80:83], v26 offset:24576\n\t" "v_lshlrev_b32 v113, 16, v113\n\t" "s_waitcnt lgkmcnt(5)\n\t" \
   "v_mfma_f32_16x16x32_bf16 v[52:55], a[152:155], v[88:91], v[52:55]\n\t" "v_sqrt_f32 v113, v113\n\t" \
   "v_mfma_f32_16x16x32_bf16 v[48:51], a[72:75], v[88:91], v[48:51]\n\t" "ds_read_b128 v[84:87], v26 offset:25600\n\t" \
   "v_mfma_f32_16x16x32_bf16 v[52:55], a[156:159], v[92:95], v[52:55]\n\t" "s_nop 0\n\t" \
   "v_mfma_f32_16x16x32_bf16 v[48:51], a[76:79], v[92:95], v[48:51]\n\t" "ds_read_b128 v[88:91], v26 offset:26624\n\t" \
   "v_cvt_pk_bf16_f32 v113, v113, v113\n\t" "s_waitcnt lgkmcnt(5)\n\t" "v_mfma_f32_16x16x32_bf16 v[60:63], a[80:83], v[96:99], 0\n\t" \
   "v_mfma_f32_16x16x32_bf16 v[56:59], a[0:3], v[96:99], 0\n\t" "ds_read_b128 v[92:95], v26 offset:27648\n\t" "v_lshlrev_b32 v113, 16, v113\n\t" \
   "v_mfma_f32_16x16x32_bf16 v[60:63], a[84:87], v[100:103], v[60:63]\n\t" "v_rcp_f32 v10, v113\n\t" \
   "v_mfma_f32_16x16x32_bf16 v[56:59], a[4:7], v[100:103], v[56:59]\n\t" "ds_read_b128 v[96:99], v26 offset:28672\n\t" "s_waitcnt lgkmcnt(5)\n\t" \
   "v_mfma_f32_16x16x32_bf16 v[60:63], a[88:91], v[104:107], v[60:63]\n\t" "v_fmac_f32 v48, v32, v8\n\t" "v_fmac_f32 v49, v33, v8\n\t" \
   "v_mfma_f32_16x16x32_bf16 v[56:59], a[8:11], v[104:107], v[56:59]\n\t" "ds_read_b128 v[100:103], v26 offset:29696\n\t" "v_fmac_f32 v50, v34, v8\n\t" \
   "v_mfma_f32_16x16x32_bf16 v[60:63], a[92:95], v[108:111], v[60:63]\n\t" "v_fmac_f32 v51, v35, v8\n\t" \
   "v_mfma_f32_16x16x32_bf16 v[56:59], a[12:15], v[108:111], v[56:59]\n\t" "ds_read_b128 v[104:107], v26 offset:30720\n\t" \
   "v_fmac_f32 v52, v36, v8\n\t" "s_waitcnt lgkmcnt(5)\n\t" "v_mfma_f32_16x16x32_bf16 v[60:63], a[96:99], v[80:83], v[60:63]\n\t" \
   "v_fmac_f32 v53, v37, v8\n\t" "v_mfma_f32_16x16x32_bf16 v[56:59], a[16:19], v[80:83], v[56:59]\n\t" "ds_read_b128 v[108:111], v26 offset:31744\n\t" \
   "v_fmac_f32 v54, v38, v8\n\t" "v_mfma_f32_16x16x32_bf16 v[60:63], a[100:103], v[84:87], v[60:63]\n\t" "v_fmac_f32 v55, v39, v8\n\t" \
   "v_mfma_f32_16x16x32_bf16 v[56:59], a[20:23], v[84:87], v[56:59]\n\t" "ds_read_b128 v[80:83], v26 offset:32768\n\t" "v_mul_f32 v48, v48, v10\n\t" \
   "v_mul_f32 v49, v49, v10\n\t" "s_waitcnt lgkmcnt(5)\n\t" "v_mfma_f32_16x16x32_bf16 v[60:63], a[104:107], v[88:91], v[60:63]\n\t" \
   "v_mul_f32 v50, v50, v10\n\t" "v_mfma_f32_16x16x32_bf16 v[56:59], a[24:27], v[88:91], v[56:59]\n\t" "ds_read_b128 v[84:87], v26 offset:33792\n\t" \
   "v_mul_f32 v51, v51, v10\n\t" "v_mfma_f32_16x16x32_bf16 v[60:63], a[108:111], v[92:95], v[60:63]\n\t" "v_mul_f32 v52, v52, v10\n\t" \
   "v_mfma_f32_16x16x32_bf16 v[56:59], a[28:31], v[92:95], v[56:59]\n\t" "ds_read_b128 v[88:91], v26 offset:34816\n\t" "v_mul_f32 v53, v53, v10\n\t" \
   "s_waitcnt lgkmcnt(5)\n\t" "v_mfma_f32_16x16x32_bf16 v[60:63], a[112:115], v[96:99], v[60:63]\n\t" "v_mul_f32 v54, v54, v10\n\t" \
   "v_mfma_f32_16x16x32_bf16 v[56:59], a[32:35], v[96:99], v[56:59]\n\t" "ds_read_b128 v[92:95], v26 offset:35840\n\t" "v_mul_f32 v55, v55, v10\n\t" \
   "v_mfma_f32_16x16x32_bf16 v[60:63], a[116:119], v[100:103], v[60:63]\n\t" "buffer_store_dwordx4 v[48:51], v21, %[rs], s48 offen offset:0 sc1\n\t" \
   "buffer_store_dwordx4 v[52:55], v21, %[rs], s47 offen offset:0 sc1\n\t" "v_mfma_f32_16x16x32_bf16 v[56:59], a[36:39], v[100:103], v[56:59]\n\t" \
   "ds_read_b128 v[96:99], v26 offset:36864\n\t" "v_add_f32 v122, v122, v124\n\t" "s_waitcnt lgkmcnt(5)\n\t" \
   "v_mfma_f32_16x16x32_bf16 v[60:63], a[120:123], v[104:107], v[60:63]\n\t" "v_add_f32 v123, v123, v125\n\t" \
   "v_mfma_f32_16x16x32_bf16 v[56:59], a[40:43], v[104:107], v[56:59]\n\t" "ds_read_b128 v[100:103], v26 offset:37888\n\t" \
   "v_add_f32 v126, v126, v128\n\t" "v_mfma_f32_16x16x32_bf16 v[60:63], a[124:127], v[108:111], v[60:63]\n\t" "v_add_f32 v127, v127, v129\n\t" \
   "v_mfma_f32_16x16x32_bf16 v[56:59], a[44:47], v[108:111], v[56:59]\n\t" "ds_read_b128 v[104:107], v26 offset:38912\n\t" \
   "v_add_f32 v122, v122, v130\n\t" "s_waitcnt lgkmcnt(5)\n\t" "v_mfma_f32_16x16x32_bf16 v[60:63], a[128:131], v[80:83], v[60:63]\n\t" \
   "v_add_f32 v123, v123, v131\n\t" "v_mfma_f32_16x16x32_bf16 v[56:59], a[48:51], v[80:83], v[56:59]\n\t" \
   "ds_read_b128 v[108:111], v26 offset:39936\n\t" "v_add_f32 v122, v122, v126\n\t" "v_add_f32 v123, v123, v127\n\t" \
   "v_mfma_f32_16x16x32_bf16 v[60:63], a[132:135], v[84:87], v[60:63]\n\t" "v_mul_f32 v122, 0xbacccccd, v122\n\t" \
   "v_mfma_f32_16x16x32_bf16 v[56:59], a[52:55], v[84:87], v[56:59]\n\t" "v_mul_f32 v123, 0x3acccccd, v123\n\t" "s_waitcnt lgkmcnt(4)\n\t" \
   "v_mfma_f32_16x16x32_bf16 v[60:63], a[136:139], v[88:91], v[60:63]\n\t" "v_fma_f32 v123, -v122, v122, v123\n\t" \
   "v_mfma_f32_16x16x32_bf16 v[56:59], a[56:59], v[88:91], v[56:59]\n\t" "v_max_f32 v123, 0, v123\n\t" \
   "v_mfma_f32_16x16x32_bf16 v[60:63], a[140:143], v[92:95], v[60:63]\n\t" "v_cvt_pk_bf16_f32 v123, v123, v122\n\t" \
   "v_mfma_f32_16x16x32_bf16 v[56:59], a[60:63], v[92:95], v[56:59]\n\t" "v_and_b32 v12, 0xffff0000, v123\n\t" "s_waitcnt lgkmcnt(2)\n\t" \
   "v_mfma_f32_16x16x32_bf16 v[60:63], a[144:147], v[96:99], v[60:63]\n\t" "v_lshlrev_b32 v123, 16, v123\n\t" "v_add_f32 v123, 0x3727c5ac, v123\n\t" \
   "v_mfma_f32_16x16x32_bf16 v[56:59], a[64:67], v[96:99], v[56:59]\n\t" "v_cvt_pk_bf16_f32 v123, v123, v123\n\t" \
   "v_mfma_f32_16x16x32_bf16 v[60:63], a[148:151], v[100:103], v[60:63]\n\t" "v_lshlrev_b32 v123, 16, v123\n\t" \
   "v_mfma_f32_16x16x32_bf16 v[56:59], a[68:71], v[100:103], v[56:59]\n\t" "v_sqrt_f32 v123, v123\n\t" "s_waitcnt lgkmcnt(0)\n\t" \
   "v_mfma_f32_16x16x32_bf16 v[60:63], a[152:155], v[104:107], v[60:63]\n\t" "s_nop 0\n\t" \
   "v_mfma_f32_16x16x32_bf16 v[56:59], a[72:75], v[104:107], v[56:59]\n\t" "v_cvt_pk_bf16_f32 v123, v123, v123\n\t" \
   "v_mfma_f32_16x16x32_bf16 v[60:63], a[156:159], v[108:111], v[60:63]\n\t" "v_lshlrev_b32 v123, 16, v123\n\t" \
   "v_mfma_f32_16x16x32_bf16 v[56:59], a[76:79], v[108:111], v[56:59]\n\t" "v_rcp_f32 v132, v123\n\t" "s_xor_b32 s46, s46, 44032\n\t" \
   "s_add_u32 s45, s45, 4\n\t" "s_nop 5\n\t" "v_fmac_f32 v56, v32, v12\n\t" "v_fmac_f32 v57, v33, v12\n\t" "v_fmac_f32 v58, v34, v12\n\t" \
   "v_fmac_f32 v59, v35, v12\n\t" "v_fmac_f32 v60, v36, v12\n\t" "v_fmac_f32 v61, v37, v12\n\t" "v_fmac_f32 v62, v38, v12\n\t" \
   "v_fmac_f32 v63, v39, v12\n\t" "v_mul_f32 v56, v56, v132\n\t" "v_mul_f32 v57, v57, v132\n\t" "v_mul_f32 v58, v58, v132\n\t" \
   "v_mul_f32 v59, v59, v132\n\t" "v_mul_f32 v60, v60, v132\n\t" "v_mul_f32 v61, v61, v132\n\t" "v_mul_f32 v62, v62, v132\n\t" \
   "v_mul_f32 v63, v63, v132\n\t" "buffer_store_dwordx4 v[56:59], v21, %[rs], s48 offen offset:256 sc1\n\t" \
   "buffer_store_dwordx4 v[60:63], v21, %[rs], s47 offen offset:256 sc1\n\t" "s_cmp_lt_u32 s45, 64\n\t" "s_cbranch_scc1 1b\n\t" \
   "s_waitcnt vmcnt(0) lgkmcnt(0)"
#define DF_CLOB "v8", "v9", "v10", "v11", "v12", "v13", "v14", "v15", "v32", "v33", "v34", "v35", "v36", "v37", "v38", "v39", "v40", "v41", "v42", "v43", "v44", "v45", "v46", "v47", "v48", "v49", "v50", "v51", "v52", "v53", "v54", "v55", "v56", "v57", "v58", "v59", "v60", "v61", "v62", "v63", "v64", "v65", "v66", "v67", "v68", "v69", "v70", "v71", "v72", "v73", "v74", "v75", "v76", "v77", "v78", "v79", "v80", "v81", "v82", "v83", "v84", "v85", "v86", "v87", "v88", "v89", "v90", "v91", "v92", "v93", "v94", "v95", "v96", "v97", "v98", "v99", "v100", "v101", "v102", "v103", "v104", "v105", "v106", "v107", "v108", "v109", "v110", "v111", "v112", "v113", "v114", "v115", "v116", "v117", "v118", "v119", "v120", "v121", "v122", "v123", "v124", "v125", "v126", "v127", "v128", "v129", "v130", "v131", "v132", "v133", "v134", "v135", "s40", "s41", "s42", "s43", "s44", "s45", "s46", "s47", "s48", "s49", "s50", "s51", "s52", "s53"
#define PRE_ASM \
   "s_mov_b32 s45, 0\n\t" "s_mov_b32 s46, 0\n\t" "s_mul_i32 s52, s45, 1536\n\t" "s_mul_i32 s51, s45, 40960\n\t" "s_add_u32 m0, s46, %[ldx]\n\t" \
   "s_mov_b32 s53, s46\n\t" "buffer_load_dwordx4 v16, %[rs], s51 offen offset:0 sc1 lds\n\t" \
   "buffer_load_dwordx4 v16, %[rs], s51 offen offset:1024 sc1 lds\n\t" "buffer_load_dwordx4 v16, %[rs], s51 offen offset:2048 sc1 lds\n\t" \
   "buffer_load_dwordx4 v16, %[rs], s51 offen offset:3072 sc1 lds\n\t" "s_add_u32 m0, m0, 4096\n\t" "s_add_u32 s51, s51, 4096\n\t" \
   "buffer_load_dwordx4 v16, %[rs], s51 offen offset:0 sc1 lds\n\t" "buffer_load_dwordx4 v16, %[rs], s51 offen offset:1024 sc1 lds\n\t" \
   "buffer_load_dwordx4 v16, %[rs], s51 offen offset:2048 sc1 lds\n\t" "buffer_load_dwordx4 v16, %[rs], s51 offen offset:3072 sc1 lds\n\t" \
   "s_add_u32 m0, m0, 4096\n\t" "s_add_u32 s51, s51, 4096\n\t" "buffer_load_dwordx4 v16, %[rs], s51 offen offset:0 sc1 lds\n\t" \
   "buffer_load_dwordx4 v16, %[rs], s51 offen offset:1024 sc1 lds\n\t" "s_add_u32 m0, s53, %[ld1]\n\t" "s_nop 0\n\t" \
   "buffer_load_dwordx4 v17, %[rs], s52 offen sc1 lds\n\t" "s_mov_b32 s45, 1\n\t" "s_mov_b32 s46, 44032\n\t" "s_mul_i32 s52, s45, 1536\n\t" \
   "s_mul_i32 s51, s45, 40960\n\t" "s_add_u32 m0, s46, %[ldx]\n\t" "s_mov_b32 s53, s46\n\t" \
   "buffer_load_dwordx4 v16, %[rs], s51 offen offset:0 sc1 lds\n\t" "buffer_load_dwordx4 v16, %[rs], s51 offen offset:1024 sc1 lds\n\t" \
   "buffer_load_dwordx4 v16, %[rs], s51 offen offset:2048 sc1 lds\n\t" "buffer_load_dwordx4 v16, %[rs], s51 offen offset:3072 sc1 lds\n\t" \
   "s_add_u32 m0, m0, 4096\n\t" "s_add_u32 s51, s51, 4096\n\t" "buffer_load_dwordx4 v16, %[rs], s51 offen offset:0 sc1 lds\n\t" \
   "buffer_load_dwordx4 v16, %[rs], s51 offen offset:1024 sc1 lds\n\t" "buffer_load_dwordx4 v16, %[rs], s51 offen offset:2048 sc1 lds\n\t" \
   "buffer_load_dwordx4 v16, %[rs], s51 offen offset:3072 sc1 lds\n\t" "s_add_u32 m0, m0, 4096\n\t" "s_add_u32 s51, s51, 4096\n\t" \
   "buffer_load_dwordx4 v16, %[rs], s51 offen offset:0 sc1 lds\n\t" "buffer_load_dwordx4 v16, %[rs], s51 offen offset:1024 sc1 lds\n\t" \
   "s_add_u32 m0, s53, %[ld1]\n\t" "s_nop 0\n\t" "buffer_load_dwordx4 v17, %[rs], s52 offen sc1 lds\n\t" ""
#define PRE_CLOB "s45", "s46", "s47", "s48", "s49", "s50", "s51", "s52", "s53"
__device__ __forceinline__ void load_proj(int wv, v32u (&w)[10], rsrc_t rb, bool vu, int h) {
  const int lane = lane_id();
  {
    const unsigned lds0 = (unsigned)(uintptr_t)(__attribute__((address_space(3))) unsigned char*)dyn_lds;
    asm volatile(PRE_ASM ::"{v16}"((int)OFF_XR + wv * 10240 + lane * 16), "{v17}"(wv * 64 + lane < 96 ? (int)OFF_ST + wv * 1024 + lane * 16 : OOB), [rs] "s"(rb),
                 [ldx] "s"(lds0 + PJ_X + wv * 10240), [ld1] "s"(lds0 + PJ_X + 40960 + (wv < 2 ? wv : 2) * 1024) : PRE_CLOB, "scc", "m0", "memory");
  }
  const int b0 = (int)OFF_WF + (((vu ? 64 : 0) + h) * 8 + 2 * wv) * 20480 + lane * 16, b1 = (int)OFF_WOF + (h * 40 + wv * 10) * 4096 + lane * 16;
  const int sa = vu ? 1024 : 32 * 8 * 20480, sb = vu ? 2048 : 1024;
  const int c0 = vu ? b1 : b0 + 10 * 1024, c1 = vu ? 1024 : 20480, c2 = vu ? 2048 : sa, c3 = vu ? 4096 : 1024;
#pragma unroll
  for (int i = 0; i < 40; ++i) put4(w[i >> 3], i & 7, __builtin_amdgcn_raw_buffer_load_b128(rb, b0, (i & 1) * 20480 + ((i >> 1) & 1) * sa + (i >> 2) * sb, 0));
#pragma unroll
  for (int i = 0; i < 40; ++i) put4(w[5 + (i >> 3)], i & 7, __builtin_amdgcn_raw_buffer_load_b128(rb, c0, (i & 1) * c1 + ((i >> 1) & 1) * c2 + (i >> 2) * c3, 0));
}
template <bool VU>
__device__ __forceinline__ void job_proj(int wv_, const v32u (&w)[10], unsigned char* ws, int layer, int h) {
  const unsigned lds0 = (unsigned)(uintptr_t)(__attribute__((address_space(3))) unsigned char*)dyn_lds;
  const int lane = lane_id(), wv = wv_, g = lane >> 4, r = lane & 15;
  const v4u rs = make_rs(ws, (unsigned)OFF_END);
  v16u ad = (unsigned)lane;
  ad[0] = (int)OFF_XR + wv * 10240 + lane * 16;
  ad[1] = wv * 64 + lane < 96 ? (int)OFF_ST + wv * 1024 + lane * 16 : OOB;
  ad[3] = cnt_off(layer, CNT_IDFT, lane & 31);
  ad[4] = lane == 0 ? kqf_off(layer, 0, (VU ? 32 : 0) + h) + wv * 4 : OOB;
  ad[5] = VU ? (int)OFF_UF + (((wv * 20 + (r >> 3)) * 16 + (h >> 1)) * 64 + g * 16 + (h & 1) * 8 + (r & 7)) * 16 : (h * RT * 4 + wv) * 1024 + lane * 16;
  ad[6] = lds0 + PJ_X + lane * 16;
  ad[7] = lds0 + PJ_X + 40960 + r * 48;
  ad[8] = lds0 + wv * 1024 + lane * 16;
  ad[9] = lds0 + 8192 + lane * 16;
  if (layer > 0) dep_wait(wv_, ws, fan_off(1, 1, (VU ? 32 : 0) + h), 1u);
#define PJ_OPS                                                                                                                      \
  : "+{v[16:31]}"(ad)                                                                                                               \
  : W_OPS, [rs] "s"(rs), [rdy] "s"((layer - 1) | 63), [ldx] "s"(lds0 + PJ_X + wv * 10240),                        \
    [ld1] "s"(lds0 + PJ_X + 40960 + (wv < 2 ? wv : 2) * 1024), [qf] "s"((int)OFF_QF), [kf] "s"((int)OFF_KF), [cnt] "s"(cnt_off(layer, CNT_IDFT, 0))
  if constexpr (VU) asm volatile(VU_ASM PJ_OPS : VU_CLOB, "vcc", "scc", "m0", "memory");
  else asm volatile(KQ_ASM PJ_OPS : KQ_CLOB, "vcc", "scc", "m0", "memory");
  LDSBAR();
}

__device__ __forceinline__ void job_dft(int wv_, unsigned char* ws, int layer, int fg, int set) {
  const unsigned lds0 = (unsigned)(uintptr_t)(__attribute__((address_space(3))) unsigned char*)dyn_lds;
  const int lane = lane_id(), wv = wv_, g = lane >> 4, r = lane & 15;
  const v4u rs = make_rs(ws, (unsigned)OFF_END);
  const rsrc_t rb = ws_rsrc(ws);
  v32u w[5];
#pragma unroll
  for (int i = 0; i < 40; ++i) put4(w[i >> 3], i & 7, ldp(rb, (int)OFF_FWD + ((fg * 8 + wv * 2) * KT + i) * 1024 + lane * 16));
  v16u ad = (unsigned)lane;
  ad[0] = (int)OFF_XR + wv * 10240 + lane * 16;
  ad[1] = wv * 64 + lane < 96 ? (int)OFF_ST + wv * 1024 + lane * 16 : OOB;
  ad[3] = cnt_off(layer, CNT_OUT, 0);
  ad[5] = (int)OFF_X1T + (((fg * 8 + wv * 2) * 4 + g) * S_ + r) * 16;
  ad[6] = lds0 + lane * 16;
  ad[7] = lds0 + 40960 + r * 48;
  asm volatile(DF_ASM
               : "+{v[16:31]}"(ad)
               : "{a[0:31]}"(w[0]), "{a[32:63]}"(w[1]), "{a[64:95]}"(w[2]), "{a[96:127]}"(w[3]), "{a[128:159]}"(w[4]), [rs] "s"(rs), [set] "s"(set),
                 [ldx] "s"(lds0 + wv * 10240), [ld0] "s"(lds0), [ld1] "s"(lds0 + 40960 + (wv < 2 ? wv : 2) * 1024)
               : DF_CLOB, "vcc", "scc", "m0", "memory");
}

constexpr int NOUT = 44;
constexpr int MAIN_LDS = PJ_LDS;
static_assert(MAIN_LDS >= JOB_LDS && MAIN_LDS >= REC_LDS && MAIN_LDS <= 160 * 1024, "LDS");
__device__ __forceinline__ int take_role(int wv_, unsigned char* ws) {
  int* slot = (int*)dyn_lds;
  if (TIDX == 0) {
    const int x = (int)xcc_id();
    const int first = x;
    int role = 0;
#pragma unroll 1
    for (int i = 0; i < 8; ++i) {
      const int pl = first ^ i;
      const unsigned t = __hip_atomic_fetch_add((gu32*)(ws + tick_off(0, pl)), 1u, __ATOMIC_RELAXED, __HIP_MEMORY_SCOPE_AGENT) + 1u;
      if (t < 32u) { role = pl * 32 + (int)t; break; }
    }
    *slot = role;
  }
  __syncthreads();
  const int role = __builtin_amdgcn_readfirstlane(*slot);
  __syncthreads();
  return role;
}
#define TROB (24 * 256 + 16)
#define TC2 30
#define TVIS 32
#define TDLT 170
#define TM1 88
#define TM2 40
__global__ void __launch_bounds__(256, 1) k_main(unsigned char* ws, int nlayer
                                                ) {
  v4u* smem = (v4u*)dyn_lds;
  const int wv_ = __builtin_amdgcn_readfirstlane(threadIdx.x >> 6);
  const RecTune tn = {TM1, TM2, 4, TROB, TC2, TVIS, TDLT, 1};
  unsigned long long* dbg = nullptr;
#define RSTAMP_(i)
  const rsrc_t rs = ws_rsrc(ws);
  const int tk = take_role(wv_, ws), pl = tk >> 5, tt = tk & 31;
  const int role = pl < 4 ? (tt < 8 ? pl * 8 + tt : (tt < 28 ? 32 + pl * 20 + tt - 8 : 112 + pl * 4 + tt - 28)) : tk;
  if (role >= 112 && role < 128) return;
  if (role < 112 || tt < 16) {
    v32u w[10];
    const int hd = (pl - 4) * 8 + (tt & 7);
    if (role < 112) {
      const int base = role < 32 ? (int)OFF_WF + (role * 8 + 2 * wv_) * 20480 : (int)OFF_BF + ((role - 32) * 4 + wv_) * 81920;
      load_weights(w, rs, base + lane_id() * 16, 20, role < 32 ? 32 * 8 * 20 : 40, wv_ < 3 ? 3 * wv_ : 8);
    } else load_proj(wv_, w, rs, (tt & 8) != 0, hd);
#pragma unroll 1
    for (int layer = 0; layer < nlayer; ++layer) {
      int ro = role, xs = pl, wq = wv_, h2 = hd | (tt & 8) << 8;
      asm volatile("" : "+s"(ro), "+s"(xs), "+s"(wq), "+s"(h2), "+s"(ws));
      if (layer > 0) {
        const int jb = ro < 112 ? ro + 49 : (xs - 4) * 16 + (ro & 31);
        if (ro < 112 || jb < 49) {
          __syncthreads();
          job_col(wq, ws, ws_rsrc(ws), (f32x2*)dyn_lds, jb, fan_off(0, 1, jb & 63));
          col_signal(wq, ws, 1, jb);
          RSTAMP_(5)
        }
      }
      if (ro < 32) rec_p1(wq, w, ws, layer, ro, xs, tn, dbg);
      else if (ro < 112) {
        if (wq == 3) rec_fin(w, ws, layer, ro - 32, xs, tn, dbg);
        else rec_p2(wq, w, ws, layer, ro - 32, xs, tn, dbg);
      } else {
        if (h2 >> 8) job_proj<true>(wq, w, ws, layer, h2 & 31);
        else job_proj<false>(wq, w, ws, layer, h2 & 31);
        RSTAMP_(2)
      }
    }
    return;
  }
#pragma unroll 1
  for (int layer = 0; layer < nlayer; ++layer) {
    if (layer > 0 && tt < 31) {
      const int i = tt - 16;
      job_idft<false>(wv_, ws, fan_off(1, 0, (pl - 4) * 16 + i), rs, smem, (i / 5) * 4 + pl - 4, 12, i % 5, nullptr, dbg + 60000 + role * 8);
    }
    RSTAMP_(2)
    __syncthreads();
    if (tt >= 27) {
      job_dft(wv_, ws, layer, tt - 27, pl - 4);
      if (layer == 0) fan_signal(wv_, ws, cnt_off(0, CNT_IDFT, 0), 20u, -1, 0u, fan_off(0, 1, 0));
    } else {
#pragma unroll 1
      for (int j = (pl - 4) * 11 + tt - 16; j < 320; j += NOUT) {
        flags32_wait(wv_, ws, kqf_off(layer, j / 5, 32));
        job_out(wv_, ws, rs, smem, layer, j / 5, j % 5);
        dep_signal(wv_, ws, cnt_off(layer, CNT_OUT, j / 5));
      }
    }
    RSTAMP_(3)
  }
}

extern "C" void kernel_launch(void* const* d_in, const int* in_sizes, int n_in, void* d_out, int out_size, void* d_ws, size_t ws_size,
                              hipStream_t stream) {
  const u16* x = (const u16*)d_in[0];
  const u16* mem0 = (const u16*)d_in[1];
  const u16* Wq = (const u16*)d_in[2];
  const u16* Wk = (const u16*)d_in[3];
  const u16* Wv = (const u16*)d_in[4];
  const u16* Wo = (const u16*)d_in[5];
  const u16* wr = (const u16*)d_in[8];
  const u16* wi = (const u16*)d_in[9];
  unsigned char* ws = (unsigned char*)d_ws;
  u16* out = (u16*)d_out;
  (void)hipFuncSetAttribute((const void*)k_main, hipFuncAttributeMaxDynamicSharedMemorySize, MAIN_LDS);
  (void)hipFuncSetAttribute((const void*)k_fin, hipFuncAttributeMaxDynamicSharedMemorySize, FIN_LDS);
  k_prep<<<PB_TOTAL, 256, 0, stream>>>(x, mem0, Wq, Wk, Wv, Wo, wr, wi, ws);
  k_main<<<256, 256, MAIN_LDS, stream>>>(ws, 2);
  k_fin<<<NCOL + 320, 256, FIN_LDS, stream>>>(ws, out);
}
```

```cpp
#include <hip/hip_runtime.h>
#include <hip/hip_bf16.h>
#include <stdint.h>

typedef unsigned v4u __attribute__((ext_vector_type(4)));
typedef unsigned v2u __attribute__((ext_vector_type(2)));
typedef float f32x4 __attribute__((ext_vector_type(4)));
typedef float f32x2 __attribute__((ext_vector_type(2)));
typedef __bf16 bf16x8 __attribute__((ext_vector_type(8)));
typedef __bf16 bf16x2 __attribute__((ext_vector_type(2)));
typedef unsigned short u16;

constexpr int S_ = 2048, D_ = 640, NSEG = 64, KT = 20, RT = 128;
constexpr int NFT = 40;
constexpr int NFK = 20;
constexpr int WIN = 32768, REG = 16 * WIN;
constexpr int W_MEM = 10 * WIN, W_MISC = 15 * WIN;
__host__ __device__ constexpr int copy_off(int c) { return (c & 3) * 2048 + (c >> 2) * 16384; }
constexpr int M_STAMP = W_MISC + copy_off(4), M_TM = M_STAMP + 40 * 64, M_CAL = W_MISC + copy_off(3);
constexpr int PAGE = 4 * REG, NYP = 22, P_Y = NSEG * REG;
constexpr size_t SZ_POOL = (size_t)P_Y + (size_t)(NYP + 1) * PAGE;

constexpr size_t OFF_CNT = 0;
constexpr int CNT_IDFT = 0, CNT_COL = 1, CNT_OUT = 2;
constexpr size_t OFF_PROG = OFF_CNT + (size_t)4 * 32 * 128;
constexpr size_t OFF_KQF = OFF_PROG + (size_t)NSEG * 4096;
constexpr size_t OFF_TICK = OFF_KQF + (size_t)NSEG * 4096;
constexpr size_t OFF_FAN = OFF_TICK + (size_t)16 * 128;
constexpr size_t HO_LAYER = OFF_FAN + (size_t)2 * 64 * 128;
constexpr size_t OFF_HO = SZ_POOL;
constexpr size_t OFF_SEL = OFF_HO + 2 * HO_LAYER;
__host__ __device__ constexpr int prog_off(int layer, int rb, int slot) { return (int)(OFF_HO + layer * HO_LAYER + OFF_PROG) + (rb * 64 + slot) * 64; }
__host__ __device__ constexpr int kqf_off(int layer, int sg, int h) { return (int)(OFF_HO + layer * HO_LAYER + OFF_KQF) + (sg * 64 + h) * 64; }
__host__ __device__ constexpr int cnt_off(int layer, int kind, int rb) { return (int)(OFF_HO + layer * HO_LAYER + OFF_CNT) + (kind * 32 + rb) * 128; }
__host__ __device__ constexpr int tick_off(int layer, int i) { return (int)(OFF_HO + layer * HO_LAYER + OFF_TICK) + i * 128; }
__host__ __device__ constexpr int fan_off(int layer, int set, int i) { return (int)(OFF_HO + layer * HO_LAYER + OFF_FAN) + (set * 64 + i) * 128; }
constexpr size_t OFF_XR = (OFF_SEL + 8192 + 4095) / 4096 * 4096;
constexpr size_t OFF_ST = OFF_XR + (size_t)RT * KT * 1024;
constexpr size_t OFF_QF = OFF_ST + (size_t)S_ * 48;
constexpr size_t OFF_KF = OFF_QF + (size_t)32 * RT * 4 * 1024;
constexpr size_t OFF_UF = OFF_KF + (size_t)32 * RT * 4 * 1024;
constexpr size_t OFF_PXF = OFF_UF + (size_t)NSEG * 80 * 16 * 1024;
constexpr size_t OFF_PXM = OFF_PXF + (size_t)NSEG * 32 * 2 * 1024;
constexpr size_t OFF_FWD = OFF_PXM + (size_t)NSEG * 8 * 2 * 1024;
constexpr size_t OFF_INV = OFF_FWD + (size_t)NFT * KT * 1024;
constexpr size_t OFF_X1T = OFF_INV + (size_t)40 * NFK * 1024;
constexpr size_t OFF_ZT = OFF_X1T + (size_t)NFT * 16 * S_ * 4;
constexpr size_t OFF_WT = OFF_ZT + (size_t)NFK * 32 * S_ * 4;
constexpr size_t OFF_TW = OFF_WT + (size_t)321 * S_ * 4;
constexpr size_t OFF_DBG = OFF_TW + (size_t)2048 * 8;
constexpr size_t OFF_END = OFF_DBG + (size_t)1048576;
static_assert(OFF_END <= ((size_t)256 << 20), "workspace too large");

constexpr int AUX_SC1 = 16;
#define PH(k)

__device__ __forceinline__ f32x4 mfma(v4u a, v4u b, f32x4 c) {
  return __builtin_amdgcn_mfma_f32_16x16x32_bf16(__builtin_bit_cast(bf16x8, a), __builtin_bit_cast(bf16x8, b), c, 0, 0, 0);
}
__device__ __forceinline__ unsigned pk(float a, float b) {
  f32x2 v = {a, b};
  return __builtin_bit_cast(unsigned, __builtin_convertvector(v, bf16x2));
}
__device__ __forceinline__ float lo_f(unsigned u) { return __builtin_bit_cast(float, u << 16); }
__device__ __forceinline__ float hi_f(unsigned u) { return __builtin_bit_cast(float, u & 0xFFFF0000u); }
__device__ __forceinline__ float bfr(float x) { return lo_f(pk(x, x)); }
__device__ __forceinline__ v4u pk8(f32x4 a, f32x4 b) { return v4u{pk(a.x, a.y), pk(a.z, a.w), pk(b.x, b.y), pk(b.z, b.w)}; }
__device__ __forceinline__ int sigma(int g, int jj) { return 16 * (jj >> 2) + 4 * g + (jj & 3); }
__device__ __forceinline__ int colmap(int c8, int jj) {
  const int p = c8 >> 3, ga = (c8 >> 1) & 3, half = c8 & 1;
  return 32 * (2 * p + (ga >> 1)) + 16 * ((jj >> 1) & 1) + 4 * (2 * (ga & 1) + (jj >> 2)) + 2 * half + (jj & 1);
}
__device__ __forceinline__ int pool_base(const void* ws) { return (int)((0u - (unsigned)(uintptr_t)ws) & (unsigned)(PAGE - 1)); }
__device__ __forceinline__ unsigned xcc_id() { return (unsigned)__builtin_amdgcn_s_getreg(20 | (3 << 11)) & 7u; }
__device__ __forceinline__ float shx(float v, int m) { return __shfl_xor(v, m); }
__device__ __forceinline__ unsigned nosent(unsigned x) { return x < 0xFFFFFFFEu ? x : 0xFFFFFFFEu; }

__device__ __forceinline__ f32x2 frag_stat(v4u c) {
  const float v[8] = {lo_f(c.x), hi_f(c.x), lo_f(c.y), hi_f(c.y), lo_f(c.z), hi_f(c.z), lo_f(c.w), hi_f(c.w)};
  float q = 0.f;
#pragma unroll
  for (int j = 0; j < 8; ++j) q = __builtin_fmaf(v[j], v[j], q);
  return f32x2{((v[0] + v[1]) + (v[2] + v[3])) + ((v[4] + v[5]) + (v[6] + v[7])), q};
}
__device__ __forceinline__ f32x2 rows_sum(f32x2 a) {
  a.x += shx(a.x, 16); a.y += shx(a.y, 16); a.x += shx(a.x, 32); a.y += shx(a.y, 32);
  return a;
}

__device__ __forceinline__ void dft_frag(unsigned char* ws, bool fwd, int fi, int lane) {
  const int g = lane >> 4, r = lane & 15;
  float hi[8];
#pragma unroll
  for (int jj = 0; jj < 8; ++jj) {
    int f, d;
    if (fwd) { const int kt = fi % KT, ft = fi / KT; f = 16 * ft + r; d = 32 * kt + sigma(g, jj); }
    else { const int fk = fi % NFK, dt = fi / NFK; d = 16 * dt + r; f = 32 * fk + 8 * g + jj; }
    const int k = f == 1 ? 320 : f >> 1;
    const int idx = (k * d) % D_;
    const float a = (float)idx * (2.0f / D_);
    float v = ((f & 1) && f != 1) ? -sinpif(a) : cospif(a);
    if (!fwd && f > 1) v *= 2.f;
    hi[jj] = v;
  }
  v4u* dst = (v4u*)(ws + (fwd ? OFF_FWD : OFF_INV));
  dst[(size_t)fi * 64 + lane] = v4u{pk(hi[0], hi[1]), pk(hi[2], hi[3]), pk(hi[4], hi[5]), pk(hi[6], hi[7])};
}

constexpr int PB_XR = RT * KT / 16;
constexpr int PB_FWD = 2 * NFT * KT / 4;
constexpr int PB_WT = 32 * 6 + 8;
constexpr int PB_HO = (int)((2 * HO_LAYER + 32767) / 32768);
constexpr int PB_CAL = 4 * NSEG + 8 * NYP;
constexpr int PB_FILL = 2 * 16, PB_YF = 20;
constexpr int PB_TOTAL = PB_CAL + PB_XR + PB_FWD + PB_WT + PB_FILL + PB_YF + PB_HO;
#define PB_LIST {PB_CAL, 0, PB_XR, PB_FWD, PB_WT, PB_FILL, PB_YF, PB_HO}
static_assert(HO_LAYER % 16 == 0, "fill granularity");
template <int AUX>
__device__ __forceinline__ void fill_pool(unsigned char* ws, int rg, int half, int tid, int q0 = 0, int q1 = 64) {
  const __amdgpu_buffer_rsrc_t rs = __builtin_amdgcn_make_buffer_rsrc(ws, 0, (int)SZ_POOL, 0x00020000);
  const int base = pool_base(ws) + rg * REG + half * 8192 + tid * 16;
#pragma unroll 8
  for (int q = q0; q < q1; ++q) {
    const bool skip = (rg == NSEG - 1 && q >= 40 && q < 60) || ((q | 2) == 63 && tid >= 128);
    __builtin_amdgcn_raw_buffer_store_b128(v4u{0xFFFFFFFFu, 0xFFFFFFFFu, 0xFFFFFFFFu, 0xFFFFFFFFu}, rs,
                                           skip ? 0x7FFFFFF0 : base + (q >> 1) * 16384 + (q & 1) * 4096, 0, AUX);
  }
}
__device__ __forceinline__ void fill_mem0(unsigned char* ws, const u16* __restrict__ mem0, int w, int tid) {
  const int c = tid & 127, c8 = (w * 8 + (c >> 4)) * 2 + (c & 1), row = (c >> 1) & 7;
  unsigned t[4];
#pragma unroll
  for (int k = 0; k < 4; ++k) t[k] = nosent(*(const unsigned*)(mem0 + (size_t)row * D_ + colmap(c8, 2 * k)));
  v4u* dst = (v4u*)(ws + pool_base(ws) + (size_t)(NSEG - 1) * REG + W_MEM + w * WIN);
#pragma unroll
  for (int q = 0; q < 8; ++q) dst[q * 256 + tid] = v4u{t[0], t[1], t[2], t[3]};
}
typedef int v4i __attribute__((ext_vector_type(4)));
__device__ __forceinline__ v4i calibrate(unsigned char* ws, unsigned* lds, int off) {
  const __amdgpu_buffer_rsrc_t rs = __builtin_amdgcn_make_buffer_rsrc(ws, 0, (int)SZ_POOL, 0x00020000);
  const int base = pool_base(ws) + off, tid = threadIdx.x, wv = __builtin_amdgcn_readfirstlane(tid >> 6);
  __builtin_amdgcn_raw_buffer_store_b128(v4u{1u, 2u, 3u, 4u}, rs, base + wv * 128 + ((tid >> 3) & 3) * 8192 + (tid & 7) * 16, 0, AUX_SC1);
  const unsigned char* p = ws + base;
  unsigned t0, t1, t2, t3, t4;
  asm volatile("s_load_dword %0, %5, 0x180\n\ts_load_dword %1, %5, 0x2180\n\ts_load_dword %2, %5, 0x4180\n\ts_load_dword %3, %5, 0x6180\n\t"
               "s_waitcnt vmcnt(0) lgkmcnt(0)\n\ts_memtime s[40:41]\n\ts_waitcnt lgkmcnt(0)\n\ts_mov_b32 %0, s40\n\t"
               "s_load_dword %1, %6, 0\n\ts_waitcnt lgkmcnt(0)\n\ts_memtime s[40:41]\n\ts_waitcnt lgkmcnt(0)\n\ts_mov_b32 %1, s40\n\t"
               "s_load_dword %2, %6, 0x2000\n\ts_waitcnt lgkmcnt(0)\n\ts_memtime s[40:41]\n\ts_waitcnt lgkmcnt(0)\n\ts_mov_b32 %2, s40\n\t"
               "s_load_dword %3, %6, 0x4000\n\ts_waitcnt lgkmcnt(0)\n\ts_memtime s[40:41]\n\ts_waitcnt lgkmcnt(0)\n\ts_mov_b32 %3, s40\n\t"
               "s_load_dword %4, %6, 0x6000\n\ts_waitcnt lgkmcnt(0)\n\ts_memtime s[40:41]\n\ts_waitcnt lgkmcnt(0)\n\ts_mov_b32 %4, s40"
               : "=&s"(t0), "=&s"(t1), "=&s"(t2), "=&s"(t3), "=&s"(t4) : "s"(p), "s"(p + wv * 128) : "s40", "s41", "memory");
  *(v4u*)(lds + wv * 4) = v4u{t1 - t0, t2 - t1, t3 - t2, t4 - t3};
  __syncthreads();
  unsigned best[4];
#pragma unroll
  for (int c = 0; c < 4; ++c) best[c] = min(min(lds[c], lds[4 + c]), lds[8 + c]);
  const int x = (int)xcc_id(), sg = x < 4 ? 1 - 2 * (x & 1) : 0;
  const int d = min(max((int)(best[0] + best[2]) - (int)(best[1] + best[3]), -400), 400);
  return v4i{x < 4 ? d : -d, sg * min(max((int)best[0] - (int)best[2], -80), 80), sg * min(max((int)best[1] - (int)best[3], -80), 80), 0};
}
__device__ __forceinline__ int y_region(unsigned long long sel, int s) { return (s * 80 + (s * 43 >> 7) * 16 + (int)((sel >> s) & 1ull)) * 8192; }

__global__ void __launch_bounds__(256) k_prep(const u16* __restrict__ x,
                                              const u16* __restrict__ mem0, const u16* __restrict__ Wq,
                                              const u16* __restrict__ Wk, const u16* __restrict__ Wv, const u16* __restrict__ Wo,
                                              const u16* __restrict__ wr, const u16* __restrict__ wi, unsigned char* __restrict__ ws) {
  __shared__ __attribute__((aligned(16))) unsigned t32[64 * 65];
  int b = blockIdx.x;
  const int tid = threadIdx.x, lane = tid & 63, wv = tid >> 6, g = lane >> 4, r = lane & 15;
  if (b < PB_CAL) {
    PH(0)
    const int c = b - 4 * NSEG;
    const v4i sel = calibrate(ws, t32, (c < 0 ? (b >> 2) * REG : P_Y + (c >> 3) * PAGE + 3 * REG - (c & 4) * (WIN / 4)) + M_CAL + (b & 3) * 512);
    if (tid == 0) { if (c < 0) ((v4i*)(ws + OFF_SEL))[b] = sel; else ((int*)(ws + OFF_SEL + 4096))[c] = sel.x; }
    if (b < 5) fill_mem0(ws, mem0, b, tid);
    return;
  }
  b -= PB_CAL;
  if (b < PB_XR) {
    f32x2 st = {0.f, 0.f};
#pragma unroll
    for (int q = 0; q < 4; ++q) {
      const int f = b * 16 + wv * 4 + q, kt = f % KT, rt = f / KT;
      const u16* xr = x + (size_t)(rt * 16 + r) * D_ + 32 * kt + 4 * g;
      const v2u c0 = *(const v2u*)xr, c1 = *(const v2u*)(xr + 16);
      const v4u c = v4u{c0.x, c0.y, c1.x, c1.y};
      ((v4u*)(ws + OFF_XR))[(size_t)f * 64 + lane] = c;
      st += frag_stat(c);
    }
    st = rows_sum(st);
    const int f4 = b * 4 + wv;
    if (g == 0) ((f32x2*)(ws + OFF_ST))[((f4 / 5) * 16 + r) * 6 + f4 % 5] = st;
    return;
  }
  b -= PB_XR;
  if (b < PB_FWD) { dft_frag(ws, b < PB_FWD / 2, (b * 4 + wv) % (NFT * KT), lane); return; }
  b -= PB_FWD;
  if (b < PB_WT) {
    if (b >= 192) {
      const int i = (b - 192) * 256 + tid;
      const float a = (float)i * (2.0f / 2048.f);
      ((f32x2*)(ws + OFF_TW))[i] = f32x2{cospif(a), -sinpif(a)};
      return;
    }
    const int m0 = (b & 31) * 64, k0 = (b >> 5) * 64;
#pragma unroll
    for (int q = 0; q < 16; ++q) {
      const int m = q * 4 + wv, k = k0 + lane;
      if (k < 321) t32[m * 65 + lane] = (unsigned)wr[(size_t)(m0 + m) * 321 + k] | ((unsigned)wi[(size_t)(m0 + m) * 321 + k] << 16);
    }
    __syncthreads();
#pragma unroll
    for (int q = 0; q < 16; ++q) {
      const int k = q * 4 + wv;
      if (k0 + k < 321) ((unsigned*)(ws + OFF_WT))[(size_t)(k0 + k) * S_ + m0 + lane] = t32[lane * 65 + k];
    }
    return;
  }
  b -= PB_WT;
  if (b < PB_FILL) { fill_pool<0>(ws, ((b >> 4) + 63) & 63, (b >> 3) & 1, tid, (b & 7) * 8, (b & 7) * 8 + 8); return; }
  b -= PB_FILL;
  if (b < PB_YF) {
#pragma unroll
    for (int q = 0; q < 8; ++q) ((v4u*)(ws + pool_base(ws) + P_Y))[b * 2048 + q * 256 + tid] = v4u{0xFFFFFFFFu, 0xFFFFFFFFu, 0xFFFFFFFFu, 0xFFFFFFFFu};
    return;
  }
  b -= PB_YF;
#pragma unroll
  for (int q = 0; q < 8; ++q) {
    const int i = b * 2048 + q * 256 + tid;
    if ((size_t)i * 16 < 2 * HO_LAYER) ((v4u*)(ws + OFF_HO))[i] = v4u{0xFFFFFFFFu, 0xFFFFFFFFu, 0xFFFFFFFFu, 0xFFFFFFFFu};
  }
}

__device__ __forceinline__ int lane_id() {
  int l;
  asm volatile("v_mbcnt_lo_u32_b32 %0, -1, 0\n\tv_mbcnt_hi_u32_b32 %0, -1, %0" : "=v"(l));
  return l;
}
#define TIDX (wv_ * 64 + lane_id())
typedef __amdgpu_buffer_rsrc_t rsrc_t;
__device__ __forceinline__ rsrc_t ws_rsrc(unsigned char* ws) { return __builtin_amdgcn_make_buffer_rsrc(ws, 0, (int)OFF_END, 0x00020000); }
__device__ __forceinline__ v4u ldc(rsrc_t rs, int off) { return __builtin_amdgcn_raw_buffer_load_b128(rs, off, 0, AUX_SC1); }
__device__ __forceinline__ v4u ldp(rsrc_t rs, int off) { return __builtin_amdgcn_raw_buffer_load_b128(rs, off, 0, 0); }
__device__ __forceinline__ void stc(rsrc_t rs, int off, v4u v) { __builtin_amdgcn_raw_buffer_store_b128(v, rs, off, 0, AUX_SC1); }

typedef __attribute__((address_space(1))) unsigned gu32;
__device__ __forceinline__ void dep_wait(int wv_, unsigned char* ws, int off, unsigned want) {
  if (TIDX == 0) {
    gu32* p = (gu32*)(ws + off);
    for (unsigned spins = 0; spins < (1u << 17); ++spins) {
      if (__hip_atomic_load(p, __ATOMIC_RELAXED, __HIP_MEMORY_SCOPE_AGENT) + 1u >= want) break;
      __builtin_amdgcn_s_sleep(8);
    }
  }
  __syncthreads();
}
__device__ __forceinline__ void prog_prewait(int wv_, unsigned char* ws, int off) {
  if (TIDX == 0) {
    gu32* p = (gu32*)(ws + off);
    for (unsigned spins = 0; spins < (1u << 16); ++spins) {
      if (__hip_atomic_load(p, __ATOMIC_RELAXED, __HIP_MEMORY_SCOPE_AGENT) != 0xFFFFFFFFu) break;
      __builtin_amdgcn_s_sleep(48);
    }
  }
}
__device__ __forceinline__ void prog_wait(int wv_, unsigned char* ws, int off) {
  if (wv_ == 0) {
    const int l = lane_id();
    gu32* p0 = (gu32*)(ws + off + l * 64);
    for (unsigned spins = 0; spins < (1u << 17); ++spins) {
      const unsigned a = __hip_atomic_load(p0, __ATOMIC_RELAXED, __HIP_MEMORY_SCOPE_AGENT);
      if (!__any(a == 0xFFFFFFFFu)) break;
      __builtin_amdgcn_s_sleep(8);
    }
  }
  __syncthreads();
}
__device__ __forceinline__ void flags32_wait(int wv_, unsigned char* ws, int off) {
  if (wv_ == 0) {
    const int l = lane_id();
    __attribute__((address_space(1))) unsigned long long* p0 = (__attribute__((address_space(1))) unsigned long long*)(ws + off + (l & 31) * 64 + (l >> 5) * 8);
    for (unsigned spins = 0; spins < (1u << 17); ++spins) {
      const unsigned long long v = __hip_atomic_load(p0, __ATOMIC_RELAXED, __HIP_MEMORY_SCOPE_AGENT);
      if (!__any(max((unsigned)v, (unsigned)(v >> 32)) == 0xFFFFFFFFu)) break;
      __builtin_amdgcn_s_sleep(16);
    }
  }
  __syncthreads();
}
__device__ __forceinline__ void dep_signal(int wv_, unsigned char* ws, int off, unsigned inc = 1u) {
  asm volatile("s_waitcnt vmcnt(0)" ::: "memory");
  __syncthreads();
  if (TIDX == 0) __hip_atomic_fetch_add((gu32*)(ws + off), inc, __ATOMIC_RELAXED, __HIP_MEMORY_SCOPE_AGENT);
}

__device__ __forceinline__ void stat_put(v4u* lds, int wv, int rt, int lane, v4u y) {
  const f32x2 st = rows_sum(frag_stat(y));
  if (lane < 16) ((f32x2*)(lds + 5120))[(wv * 2 + rt) * 16 + lane] = st;
}
__device__ __forceinline__ void stat_store(rsrc_t rs, v4u* lds, int wv, int lane, int sg, int cg) {
  __syncthreads();
  if (wv == 0) {
    const f32x2* sp = (const f32x2*)(lds + 5120) + (lane & 31);
    const f32x2 st = (sp[0] + sp[32]) + (sp[64] + sp[96]);
    __builtin_amdgcn_raw_buffer_store_b64(__builtin_bit_cast(v2u, st), rs, lane < 32 ? (int)OFF_ST + ((sg * 32 + lane) * 6 + cg) * 8 : 0x7FFFFFF0, 0, AUX_SC1);
  }
}

__device__ __forceinline__ void job_out(int wv_, unsigned char* ws, rsrc_t rs, v4u* lds, unsigned long long ysel, int layer, int s, int cg) {
  const int lane = lane_id(), wv = wv_, g = lane >> 4, r = lane & 15;
  const int kt = cg * 4 + wv;
  const int c16 = 2 * kt + (r >> 3), ci = (g * 16 + (r & 4) * 2 + (r & 3)) * 16;
  const int ubase = (int)OFF_UF + (s * 40 + c16) * 32768 + ci;
  const int ybase = pool_base(ws) + P_Y + y_region(ysel, s) + c16 * 16384 + ci;
  v4u z[40][2];
#pragma unroll
  for (int ks = 0; ks < 32; ++ks) { z[ks][0] = ldc(rs, ubase + ks * 1024); z[ks][1] = ldc(rs, ubase + ks * 1024 + 64); }
  const int xo = (int)OFF_XR + ((2 * s * KT + kt) * 64 + lane) * 16;
  const v4u x0 = ldc(rs, xo), x1 = ldc(rs, xo + KT * 1024);
  if (s > 1) prog_prewait(wv_, ws, prog_off(layer, s - 2, (s + cg * 7) & 31));
#define OSTAMP(i)
  OSTAMP(4)
  prog_wait(wv_, ws, prog_off(layer, s, 0));
  OSTAMP(0)
  {
    const unsigned l0 = (unsigned)(uintptr_t)(__attribute__((address_space(3))) v4u*)lds;
    const int vo = lane * 16;
#pragma unroll
    for (int q = 0; q < 5; ++q) {
      const unsigned d = l0 + (q < 4 ? wv * 16384 + q * 4096 : 65536 + wv * 4096);
      const int so = q < 4 ? (int)OFF_PXF + s * 65536 + wv * 16384 + q * 4096 : (int)OFF_PXM + s * 16384 + wv * 4096;
      asm volatile("s_mov_b32 m0, %0\n\ts_nop 0\n\tbuffer_load_dwordx4 %1, %2, %3 offen sc1 lds\n\t"
                   "buffer_load_dwordx4 %1, %2, %3 offen offset:1024 sc1 lds\n\tbuffer_load_dwordx4 %1, %2, %3 offen offset:2048 sc1 lds\n\t"
                   "buffer_load_dwordx4 %1, %2, %3 offen offset:3072 sc1 lds" ::"s"(d), "v"(vo), "s"(rs), "s"(so) : "m0", "memory");
    }
#pragma unroll
    for (int hq = 0; hq < 8; ++hq) { z[32 + hq][0] = ldc(rs, ybase + hq * 1024); z[32 + hq][1] = ldc(rs, ybase + hq * 1024 + 64); }
  }
  f32x4 acc[2][2] = {{{0, 0, 0, 0}, {0, 0, 0, 0}}, {{0, 0, 0, 0}, {0, 0, 0, 0}}};
  asm volatile("s_waitcnt vmcnt(16)\n\ts_barrier" ::: "memory");
  OSTAMP(1)
  v4u pf[2][8];
#pragma unroll
  for (int i = 0; i < 8; ++i) pf[0][i] = lds[i * 64 + lane];
#pragma unroll
  for (int b = 0; b < 10; ++b) {
    if (b < 9) {
#pragma unroll
      for (int i = 0; i < 8; ++i) pf[(b + 1) & 1][i] = lds[((b + 1) * 8 + i) * 64 + lane];
    }
    __builtin_amdgcn_sched_barrier(0);
#pragma unroll
    for (int i = 0; i < 8; ++i) {
      const v4u z0 = z[b * 4 + (i >> 1)][0], z1 = z[b * 4 + (i >> 1)][1];
      acc[0][i & 1] = mfma(b < 8 ? z0 : z0.xzyw, pf[b & 1][i], acc[0][i & 1]);
      acc[1][i & 1] = mfma(b < 8 ? z1 : z1.xzyw, pf[b & 1][i], acc[1][i & 1]);
    }
    __builtin_amdgcn_sched_barrier(0);
  }
#pragma unroll
  for (int qt = 0; qt < 2; ++qt) {
    const v4u x = qt ? x1 : x0;
    const auto s0 = __builtin_amdgcn_permlane16_swap(pk(acc[0][qt].x, acc[0][qt].y), pk(acc[1][qt].x, acc[1][qt].y), false, false);
    const auto s1 = __builtin_amdgcn_permlane16_swap(pk(acc[0][qt].z, acc[0][qt].w), pk(acc[1][qt].z, acc[1][qt].w), false, false);
    v4u y;
    y.x = pk(lo_f(x.x) + lo_f(s0[0]), hi_f(x.x) + hi_f(s0[0]));
    y.y = pk(lo_f(x.y) + lo_f(s0[1]), hi_f(x.y) + hi_f(s0[1]));
    y.z = pk(lo_f(x.z) + lo_f(s1[0]), hi_f(x.z) + hi_f(s1[0]));
    y.w = pk(lo_f(x.w) + lo_f(s1[1]), hi_f(x.w) + hi_f(s1[1]));
    stc(rs, xo + qt * KT * 1024, y);
    stat_put(lds, wv, qt, lane, y);
  }
  OSTAMP(2)
  stat_store(rs, lds, wv, lane, s, cg);
}

__device__ __forceinline__ f32x2 cmul(f32x2 a, f32x2 b) { return f32x2{a.x * b.x - a.y * b.y, a.x * b.y + a.y * b.x}; }
struct C2 { f32x2 r, i; };
__device__ __forceinline__ C2 operator+(C2 a, C2 b) { return C2{a.r + b.r, a.i + b.i}; }
__device__ __forceinline__ C2 operator-(C2 a, C2 b) { return C2{a.r - b.r, a.i - b.i}; }
__device__ __forceinline__ C2 cmul(C2 a, f32x2 w) {
  return C2{__builtin_elementwise_fma(a.r, f32x2{w.x, w.x}, -(a.i * f32x2{w.y, w.y})), __builtin_elementwise_fma(a.r, f32x2{w.y, w.y}, a.i * f32x2{w.x, w.x})};
}
__device__ __forceinline__ C2 cmul(C2 a, C2 w) { return C2{__builtin_elementwise_fma(a.r, w.r, -(a.i * w.i)), __builtin_elementwise_fma(a.r, w.i, a.i * w.r)}; }
template <int SGN> __device__ __forceinline__ C2 addrot(C2 a, C2 b) { return SGN < 0 ? C2{a.r + b.i, a.i - b.r} : C2{a.r - b.i, a.i + b.r}; }
template <int SGN> __device__ __forceinline__ void fft4(C2& a0, C2& a1, C2& a2, C2& a3) {
  const C2 t0 = a0 + a2, t1 = a0 - a2, t2 = a1 + a3, t3 = a1 - a3;
  a0 = t0 + t2; a1 = addrot<SGN>(t1, t3); a2 = t0 - t2; a3 = addrot<-SGN>(t1, t3);
}
template <int SGN> __device__ __forceinline__ void fft8(C2* v) {
  C2 e0 = v[0], e1 = v[2], e2 = v[4], e3 = v[6], o0 = v[1], o1 = v[3], o2 = v[5], o3 = v[7];
  fft4<SGN>(e0, e1, e2, e3);
  fft4<SGN>(o0, o1, o2, o3);
  const f32x2 h = {0.70710678118654752f, 0.70710678118654752f}, nh = -h;
  const C2 u1 = addrot<SGN>(o1, o1), u3 = addrot<SGN>(o3, o3);
  v[0] = e0 + o0; v[4] = e0 - o0;
  v[1] = C2{__builtin_elementwise_fma(u1.r, h, e1.r), __builtin_elementwise_fma(u1.i, h, e1.i)};
  v[5] = C2{__builtin_elementwise_fma(u1.r, nh, e1.r), __builtin_elementwise_fma(u1.i, nh, e1.i)};
  v[2] = addrot<SGN>(e2, o2); v[6] = addrot<-SGN>(e2, o2);
  v[3] = C2{__builtin_elementwise_fma(u3.i, SGN < 0 ? h : nh, e3.r), __builtin_elementwise_fma(u3.r, SGN < 0 ? nh : h, e3.i)};
  v[7] = C2{__builtin_elementwise_fma(u3.i, SGN < 0 ? nh : h, e3.r), __builtin_elementwise_fma(u3.r, SGN < 0 ? h : nh, e3.i)};
}
__device__ __forceinline__ int lpad(int i) { return i + (i >> 3); }
__device__ __forceinline__ C2 c2(f32x4 t) { return C2{f32x2{t.x, t.y}, f32x2{t.z, t.w}}; }
template <int SGN> __device__ __forceinline__ void pass8_load(const f32x4* in, f32x2 w1, int j, C2* v) {
  f32x4 t[8];
#pragma unroll
  for (int q = 0; q < 8; ++q) t[q] = in[lpad(j + 256 * q)];
  if (SGN > 0) w1.y = -w1.y;
  f32x2 w[8];
  w[1] = w1; w[2] = cmul(w1, w1); w[3] = cmul(w[2], w1); w[4] = cmul(w[2], w[2]); w[5] = cmul(w[4], w1); w[6] = cmul(w[3], w[3]); w[7] = cmul(w[4], w[3]);
  v[0] = c2(t[0]);
#pragma unroll
  for (int q = 1; q < 8; ++q) v[q] = cmul(c2(t[q]), w[q]);
  fft8<SGN>(v);
}
template <int NS, int R> __device__ __forceinline__ void pass_put(f32x4* out, int j, const C2* v) {
  const int k = j & (NS - 1);
  const int base = (j - k) * R + k;
#pragma unroll
  for (int q = 0; q < R; ++q) out[lpad(base + q * NS)] = f32x4{v[q].r.x, v[q].r.y, v[q].i.x, v[q].i.y};
}

constexpr int NCOL = 161, FB = 2048 + 256;
__device__ __forceinline__ void job_col(int wv_, unsigned char* ws, rsrc_t rs, f32x2* lds, int jb, int go) {
  const int j = wv_ * 64 + lane_id();
  f32x4 *bufA = (f32x4*)lds, *bufB = bufA + FB;
  const int xin = (int)OFF_X1T + ((jb == 160 ? 0 : jb) * S_ + j) * 16;
  const f32x2* tw = (const f32x2*)(ws + OFF_TW);
  C2 v[8];
  unsigned wf[2][8];
#pragma unroll
  for (int c = 0; c < 2; ++c) {
    const unsigned* wt = (const unsigned*)(ws + OFF_WT) + (size_t)(jb == 160 ? 320 : 2 * jb + c) * S_;
#pragma unroll
    for (int q = 0; q < 8; ++q) wf[c][q] = wt[j + 256 * q];
  }
  const f32x2 tA = tw[(j & 7) * 32], tB = tw[(j & 63) * 4], tC0 = tw[j], tC1 = tw[j + 256];
  const f32x2 tD = tw[(j & 3) * 64], tE = tw[(j & 31) * 8];
  if (go >= 0) dep_wait(wv_, ws, go, 1u);
#pragma unroll
  for (int q = 0; q < 8; ++q) {
    const f32x4 t = __builtin_bit_cast(f32x4, ldc(rs, xin + q * 4096));
    v[q] = C2{f32x2{jb == 160 ? t.y : t.x, t.z}, f32x2{(jb == 0 || jb == 160) ? 0.f : t.y, t.w}};
  }
#define CSTAMP(i)
  CSTAMP(0)
  fft8<-1>(v);
  pass_put<1, 8>(bufA, j, v);
  __syncthreads();
  CSTAMP(1)
  pass8_load<-1>(bufA, tA, j, v);
  pass_put<8, 8>(bufB, j, v);
  __syncthreads();
  pass8_load<-1>(bufB, tB, j, v);
  pass_put<64, 8>(bufA, j, v);
  __syncthreads();
  const float sc = 1.0f / ((float)S_ * (float)D_);
  {
    f32x4 t[8];
#pragma unroll
    for (int q = 0; q < 8; ++q) t[q] = bufA[lpad(j + 256 * q)];
#pragma unroll
    for (int hh = 0; hh < 2; ++hh) {
      const f32x2 w1 = hh ? tC1 : tC0, w2 = cmul(w1, w1), w3 = cmul(w2, w1);
      C2 a[4] = {c2(t[hh]), cmul(c2(t[hh + 2]), w1), cmul(c2(t[hh + 4]), w2), cmul(c2(t[hh + 6]), w3)};
      fft4<-1>(a[0], a[1], a[2], a[3]);
#pragma unroll
      for (int q = 0; q < 4; ++q) {
        const unsigned w0 = wf[0][hh + 2 * q], w1_ = wf[1][hh + 2 * q];
        a[q] = cmul(a[q], C2{f32x2{lo_f(w0), lo_f(w1_)} * sc, f32x2{hi_f(w0), hi_f(w1_)} * sc});
      }
      fft4<1>(a[0], a[1], a[2], a[3]);
      pass_put<1, 4>(bufB, j + 256 * hh, a);
    }
  }
  __syncthreads();
  pass8_load<1>(bufB, tD, j, v);
  pass_put<4, 8>(bufA, j, v);
  __syncthreads();
  pass8_load<1>(bufA, tE, j, v);
  pass_put<32, 8>(bufB, j, v);
  __syncthreads();
  CSTAMP(2)
  {
    pass8_load<1>(bufB, tC0, j, v);
#pragma unroll
    for (int q = 0; q < 8; ++q)
      __builtin_amdgcn_raw_buffer_store_b64(v2u{pk(v[q].r.x, v[q].i.x), pk(v[q].r.y, v[q].i.y)}, rs, (int)OFF_ZT + (jb * S_ + j + 256 * q) * 8, 0, AUX_SC1);
  }
  CSTAMP(3)
}
__device__ __forceinline__ void fan_signal(int wv_, unsigned char* ws, int cnt, unsigned want, int top, unsigned wtop, int fan) {
  asm volatile("s_waitcnt vmcnt(0)" ::: "memory");
  __syncthreads();
  if (wv_ == 0) {
    const int l = lane_id();
    unsigned last = 0;
    if (l == 0) {
      last = __hip_atomic_fetch_add((gu32*)(ws + cnt), 1u, __ATOMIC_RELAXED, __HIP_MEMORY_SCOPE_AGENT) + 2u == want;
      if (last && top >= 0) last = __hip_atomic_fetch_add((gu32*)(ws + top), 1u, __ATOMIC_RELAXED, __HIP_MEMORY_SCOPE_AGENT) + 2u == wtop;
    }
    if (__builtin_amdgcn_readfirstlane(last)) __hip_atomic_store((gu32*)(ws + fan + l * 128), 1u, __ATOMIC_RELAXED, __HIP_MEMORY_SCOPE_AGENT);
  }
}
__device__ __forceinline__ void col_signal(int wv_, unsigned char* ws, int set, int jb) {
  fan_signal(wv_, ws, cnt_off(set, CNT_COL, jb & 7), (jb & 7) == 0 ? 21u : 20u, cnt_off(set, CNT_COL, 8), 8u, fan_off(set, 0, 0));
}

template <bool FINAL>
__device__ __forceinline__ void job_idft(int wv_, unsigned char* ws, int colcnt, rsrc_t rs, v4u* lds, int sg0, int sgstep, int dq, u16* out, unsigned long long* dbg) {
  const int lane = lane_id(), wv = wv_, g = lane >> 4, r = lane & 15;
  const int kt = dq * 4 + wv;
  v4u a[2][NFK];
#pragma unroll
  for (int fk = 0; fk < NFK; ++fk) {
    a[0][fk] = ldp(rs, (int)OFF_INV + ((2 * kt) * NFK + fk) * 1024 + lane * 16);
    a[1][fk] = ldp(rs, (int)OFF_INV + ((2 * kt + 1) * NFK + fk) * 1024 + lane * 16);
  }
#define ISTAMP(i)
  ISTAMP(0)
  dep_wait(wv_, ws, colcnt, 1u);
  ISTAMP(1)
#pragma unroll 1
  for (int sg = sg0; sg < NSEG; sg += sgstep) {
    const int xo = (int)OFF_XR + ((sg * 2 * KT + kt) * 64 + lane) * 16;
    {
      const int jj = lane >> 3, rp = lane & 7, row = sg * 32 + (wv & 1) * 16 + 2 * rp;
      const int src = (int)OFF_ZT + (((wv >> 1) * 80 + jj) * S_ + row) * 8;
      v4u t[10];
#pragma unroll
      for (int i = 0; i < 10; ++i) t[i] = ldc(rs, src + i * 8 * S_ * 8);
      const v4u ny = ldc(rs, (int)OFF_ZT + (160 * S_ + row) * 8);
      if (wv < 2 && jj == 0) {
        t[0].x = (t[0].x & 0xFFFFu) | (ny.x << 16);
        t[0].z = (t[0].z & 0xFFFFu) | (ny.z << 16);
      }
      v2u* dst = (v2u*)(lds + ((wv & 1) * NFK + (wv >> 1) * 10) * 64 + (jj >> 1) * 16 + 2 * rp) + (jj & 1);
#pragma unroll
      for (int i = 0; i < 10; ++i) { dst[i * 128] = v2u{t[i].x, t[i].y}; dst[i * 128 + 2] = v2u{t[i].z, t[i].w}; }
    }
    const v4u x0 = ldc(rs, xo), x1 = ldc(rs, xo + KT * 1024);
    f32x4 acc[2][2] = {{{0, 0, 0, 0}, {0, 0, 0, 0}}, {{0, 0, 0, 0}, {0, 0, 0, 0}}};
    __syncthreads();
    ISTAMP(2)
#pragma unroll
    for (int k0 = 0; k0 < NFK; k0 += 5) {
      v4u b0[5], b1[5];
#pragma unroll
      for (int d = 0; d < 5; ++d) { b0[d] = lds[(k0 + d) * 64 + lane]; b1[d] = lds[(NFK + k0 + d) * 64 + lane]; }
#pragma unroll
      for (int d = 0; d < 5; ++d) {
        acc[0][0] = mfma(a[0][k0 + d], b0[d], acc[0][0]); acc[0][1] = mfma(a[0][k0 + d], b1[d], acc[0][1]);
        acc[1][0] = mfma(a[1][k0 + d], b0[d], acc[1][0]); acc[1][1] = mfma(a[1][k0 + d], b1[d], acc[1][1]);
      }
    }
#pragma unroll
    for (int rt = 0; rt < 2; ++rt) {
      const v4u x = rt ? x1 : x0;
      v4u y;
      y.x = pk(lo_f(x.x) + bfr(acc[0][rt].x), hi_f(x.x) + bfr(acc[0][rt].y));
      y.y = pk(lo_f(x.y) + bfr(acc[0][rt].z), hi_f(x.y) + bfr(acc[0][rt].w));
      y.z = pk(lo_f(x.z) + bfr(acc[1][rt].x), hi_f(x.z) + bfr(acc[1][rt].y));
      y.w = pk(lo_f(x.w) + bfr(acc[1][rt].z), hi_f(x.w) + bfr(acc[1][rt].w));
      if (FINAL) {
        u16* op = out + (size_t)(sg * 32 + rt * 16 + r) * D_ + 32 * kt + 4 * g;
        *(v2u*)op = v2u{y.x, y.y};
        *(v2u*)(op + 16) = v2u{y.z, y.w};
      } else {
        stc(rs, xo + rt * KT * 1024, y);
        stat_put(lds, wv, rt, lane, y);
      }
    }
    ISTAMP(3)
    if (!FINAL) {
      stat_store(rs, lds, wv, lane, sg, dq);
      if (sg < 12) fan_signal(wv_, ws, cnt_off(1, CNT_IDFT, sg >> 1), 10u, cnt_off(1, CNT_COL, 9), 6u, fan_off(1, 1, 0));
      else dep_signal(wv_, ws, cnt_off(1, CNT_IDFT, sg >> 1));
    }
  }
}

constexpr int JOB_LDS = 4 * NFK * 1024;
extern __shared__ __attribute__((aligned(16))) unsigned char dyn_lds[];
constexpr int FIN_LDS = 4 * FB * 8;
#define FIN_DELAY 3
__global__ void __launch_bounds__(256, 2) k_fin(unsigned char* ws, u16* out) {
  const int wv_ = __builtin_amdgcn_readfirstlane(threadIdx.x >> 6), b = blockIdx.x;
  const rsrc_t rs = ws_rsrc(ws);
  PH(14)
  if (b < NCOL) {
    job_col(wv_, ws, rs, (f32x2*)dyn_lds, b, -1);
    col_signal(wv_, ws, 0, b);
    PH(13)
  } else {
    for (int i = 0; i < FIN_DELAY; ++i) __builtin_amdgcn_s_sleep(32);
    job_idft<true>(wv_, ws, fan_off(0, 0, b & 63), rs, (v4u*)dyn_lds, (b - NCOL) / 5, NSEG, (b - NCOL) % 5, out, nullptr);
    PH(15)
  }
}

constexpr unsigned SPIN_CAP = 1u << 16;
typedef unsigned v8u __attribute__((ext_vector_type(8)));

#define VM_LD128(dst, off, so, IMM) asm volatile("buffer_load_dwordx4 %0, %1, %2, %3 offen offset:" #IMM " sc1" : "=v"(dst) : "v"(off), "s"(rs), "s"(so) : "memory")
#define VM_LD32(dst, off, so) asm volatile("buffer_load_dword %0, %1, %2, %3 offen sc1" : "=v"(dst) : "v"(off), "s"(rs), "s"(so) : "memory")
#define VM_ST32(val, off, so) asm volatile("buffer_store_dword %0, %1, %2, %3 offen sc1" ::"v"(val), "v"(off), "s"(rs), "s"(so) : "memory")
#define VM_ST128(val, off, so) asm volatile("buffer_store_dwordx4 %0, %1, %2, %3 offen sc1\n\ts_nop 1" ::"v"(val), "v"(off), "s"(rs), "s"(so) : "memory")
#define VM_WAIT(N) asm volatile("s_waitcnt vmcnt(" #N ")" ::: "memory")
#define VM_WAIT1(N, a) asm volatile("s_waitcnt vmcnt(" #N ")" : "+v"(a)::"memory")
__device__ __forceinline__ v4u make_rs(const void* p, unsigned bytes) {
  const unsigned long long a = (unsigned long long)p;
  v4u r;
  r.x = (unsigned)__builtin_amdgcn_readfirstlane((unsigned)a);
  r.y = (unsigned)__builtin_amdgcn_readfirstlane((unsigned)(a >> 32) & 0xFFFFu);
  r.z = bytes;
  r.w = 0x00020000u;
  return r;
}
__device__ __forceinline__ unsigned rtc() { return (unsigned)__builtin_amdgcn_s_memrealtime(); }
struct Clk0 { unsigned r, c; };
__device__ __forceinline__ Clk0 clk_sample() {
  unsigned long long r, c;
  asm volatile("s_memrealtime %0\n\ts_memtime %1\n\ts_waitcnt lgkmcnt(0)" : "=s"(r), "=s"(c)::"memory");
  return Clk0{(unsigned)r, (unsigned)c};
}
__device__ __forceinline__ v4u clk_init(Clk0 a) {
  const Clk0 b = clk_sample();
  unsigned k = 1400u;
  if (b.r - a.r >= 100u) k = (unsigned)((float)(b.r - a.r) * 32768.f / (float)(b.c - a.c));
  k = (unsigned)__builtin_amdgcn_readfirstlane((int)min(max(k, 1200u), 3000u));
  asm volatile("" : "+s"(k));
  return v4u{b.r * 8u + 16u, b.c, k, 0u};
}
#define LDSBAR() asm volatile("s_waitcnt lgkmcnt(0)\n\ts_barrier" ::: "memory")
#define MEMPIN() asm volatile("" ::: "memory")
#define LDS_V4(a) (*(__attribute__((address_space(3))) v4u*)(uintptr_t)(a))
#define LDS_U32(a) (*(__attribute__((address_space(3))) unsigned*)(uintptr_t)(a))
#define LDS_V2(a) (*(__attribute__((address_space(3))) v2u*)(uintptr_t)(a))

__device__ __forceinline__ int wave_imax(int v) {
  asm volatile("s_nop 1\n\tv_max_i32_dpp %0, %0, %0 quad_perm:[1,0,3,2] row_mask:0xf bank_mask:0xf\n\t"
               "s_nop 1\n\tv_max_i32_dpp %0, %0, %0 quad_perm:[2,3,0,1] row_mask:0xf bank_mask:0xf\n\t"
               "s_nop 1\n\tv_max_i32_dpp %0, %0, %0 row_half_mirror row_mask:0xf bank_mask:0xf\n\t"
               "s_nop 1\n\tv_max_i32_dpp %0, %0, %0 row_mirror row_mask:0xf bank_mask:0xf\n\ts_nop 0"
               : "+v"(v));
  const int a = __builtin_amdgcn_readlane(v, 0), b = __builtin_amdgcn_readlane(v, 16), c = __builtin_amdgcn_readlane(v, 32),
            d = __builtin_amdgcn_readlane(v, 48);
  return max(max(a, b), max(c, d));
}

constexpr int OOB = 0x7FFFFFF0;
__device__ __forceinline__ int ladder(int x, int i) { return copy_off(((x + (i >> 1)) & 3) | ((i & 1) << 2)); }

struct RecTune { int m1, m2, dl1, rob, c2, vis, dlt, du; };

#define STAT(r)
__device__ __forceinline__ unsigned max4(v4u v) { return max(max(v.x, v.y), max(v.z, v.w)); }
template <int NAP = 6>
__device__ __forceinline__ unsigned flag_finish(const v4u rs, int off, unsigned cv) {
  for (unsigned spins = 0; __any(cv == 0xFFFFFFFFu) && spins < SPIN_CAP; ++spins) {
    __builtin_amdgcn_s_sleep(NAP);
    v4u t;
    VM_LD128(t, off, 0, 0);
    VM_WAIT1(0, t);
    cv = max4(t);
  }
  return cv;
}
struct Pool {
  unsigned long long sel, s3, ysel; int base;
  __device__ __forceinline__ int near(int s) const { return (int)((s3 >> s) & 1ull) * 16384; }
  __device__ __forceinline__ int region(int s) const { return base + s * REG + (int)((sel >> s) & 1ull) * 8192; }
};
__device__ __forceinline__ void pool_load(const unsigned char* ws, v4i (&e)[18]) {
  const int s = lane_id();
#pragma unroll
  for (int i = 0; i < 18; ++i) e[i] = ((const v4i*)(ws + OFF_SEL))[i < 16 ? (s & 60) * 4 + i : 240 + (s * 43 >> 7) * 2 + i];
}
__device__ __forceinline__ Pool pool_init(unsigned char* ws, v4i (&e)[18]) {
  v4i a = {0, 0, 0, 0};
#pragma unroll
  for (int i = 0; i < 18; ++i) asm volatile("" : "+v"(e[i]));
#pragma unroll
  for (int i = 0; i < 16; ++i) { a.x += e[i].x; a.y += (i & 4) ? -e[i].y : e[i].y; a.z += (i & 4) ? -e[i].z : e[i].z; }
  const v4i y = e[16] + e[17];
  Pool p;
  p.sel = __ballot(a.x > 0);
  p.s3 = __ballot((a.x > 0 ? a.z : a.y) > 0) ^ 0xAAAAAAAAAAAAAAAAull;
  p.ysel = __ballot(y.x + y.y + y.z + y.w > 0);
  p.base = __builtin_amdgcn_readfirstlane(pool_base(ws));
  return p;
}
constexpr int L_QT = 10240, L_KT = 12288, L_CL = 14336, L_KQ = 16384;
constexpr int L_T = 14336;
constexpr int L_M2 = L_KQ + 65536;
constexpr int REC_ST = L_M2 + 12288;
constexpr int REC_LDS = REC_ST + 4096 + 64;

#define RSTAMP(i)
#define YSTAMP
#define RST_CLOB
#define P1TRACE
#define PVTRACE
#define FINTRACE
#define P1_ASM \
   "s_mov_b32 s64, -1\n\t" "s_mov_b32 s76, 0\n\t" "s_mov_b32 s77, 0\n\t" "s_mov_b32 s71, 63\n\t" "s_mov_b32 s47, 1300\n\t" "s_mov_b32 s49, 0\n\t" \
   "s_mov_b32 s51, 0\n\t" "s_mov_b32 s52, 0\n\t" "s_mov_b32 s70, 0\n\t" "s_mov_b32 s69, 0\n\t" "v_mov_b32 v15, 0\n\t" \
   "v_cmp_gt_i32 s[20:21], 0, v126\n\t" "v_cmp_gt_i32 s[22:23], 1, v126\n\t" "v_cmp_gt_i32 s[24:25], 2, v126\n\t" "v_cmp_gt_i32 s[26:27], 3, v126\n\t" \
   "v_cmp_gt_i32 s[28:29], 16, v126\n\t" "v_cmp_gt_i32 s[30:31], 17, v126\n\t" "v_cmp_gt_i32 s[32:33], 18, v126\n\t" \
   "v_cmp_gt_i32 s[34:35], 19, v126\n\t" "s_waitcnt vmcnt(0)\n\t" "s_lshr_b64 s[72:73], %[sel], s71\n\t" "s_lshl_b32 s65, s71, 19\n\t" \
   "s_and_b32 s72, s72, 1\n\t" "s_add_u32 s65, s65, %[pool]\n\t" "s_lshl_b32 s72, s72, 13\n\t" "s_add_u32 s65, s65, s72\n\t" \
   "s_lshr_b64 s[72:73], %[s3], s71\n\t" "s_and_b32 s54, s72, 1\n\t" "s_lshl_b32 s54, s54, 14\n\t" "s_memtime s[62:63]\n\t" \
   "s_memrealtime s[60:61]\n\t" "40:\n\t" "s_cmp_eq_u32 s64, 0\n\t" "s_cbranch_scc1 95f\n\t" "96:\n\t" "s_mov_b32 m0, %[lfs]\n\t" \
   "s_cmp_gt_u32 %[wv], 1\n\t" "s_waitcnt vmcnt(3)\n\t" "s_cbranch_scc1 44f\n\t" "ds_write_b128 v127, v[16:19] offset:0\n\t" \
   "ds_write_b128 v127, v[20:23] offset:1024\n\t" "ds_write_b128 v127, v[24:27] offset:2048\n\t" "ds_write_b128 v127, v[28:31] offset:3072\n\t" \
   "s_waitcnt lgkmcnt(0)\n\t" "s_sub_u32 s74, s62, s57\n\t" "s_mul_i32 s74, s74, s58\n\t" "s_lshr_b32 s74, s74, 12\n\t" "s_add_u32 s74, s74, s56\n\t" \
   "s_lshl_b32 s60, s60, 3\n\t" "s_sub_u32 s75, s74, s60\n\t" "s_cmp_lt_u32 s75, 32\n\t" "s_cbranch_scc1 38f\n\t" "s_cmp_lt_i32 s75, 0\n\t" \
   "s_cselect_b32 s61, 0, 31\n\t" "s_add_u32 s74, s60, s61\n\t" "s_sub_i32 s75, s61, s75\n\t" "s_add_i32 s75, s75, 1\n\t" "s_ashr_i32 s75, s75, 1\n\t" \
   "s_min_i32 s75, s75, 8\n\t" "s_max_i32 s75, s75, -8\n\t" "s_mov_b32 s56, s74\n\t" "s_mov_b32 s57, s62\n\t" "s_add_u32 s58, s58, s75\n\t" "38:\n\t" \
   "s_min_u32 s72, s70, 1\n\t" "s_and_b32 s71, s74, -4\n\t" "s_or_b32 s71, s71, s72\n\t" "v_mov_b32 v111, s71\n\t" \
   "buffer_store_dword v111, v131, %[rs], s65 offen sc1\n\t" P1TRACE "s_barrier\n\t" "ds_read_b128 v[160:163], v122 offset:0\n\t" \
   "ds_read_b128 v[164:167], v122 offset:1024\n\t" "ds_read_b128 v[168:171], v122 offset:2048\n\t" "ds_read_b128 v[172:175], v122 offset:3072\n\t" \
   "ds_read_b128 v[176:179], v122 offset:4096\n\t" "ds_read_b128 v[180:183], v122 offset:5120\n\t" "ds_read_b128 v[184:187], v122 offset:6144\n\t" \
   "ds_read_b128 v[188:191], v122 offset:7168\n\t" "buffer_load_dword v139, %[rs], s65 offen sc1 lds\n\t" "s_cmp_lt_i32 s64, 1\n\t" \
   "s_cselect_b32 s72, 0x7ffffff0, 0\n\t" "s_max_i32 s71, s64, 1\n\t" "v_or_b32 v109, s72, v143\n\t" "s_lshl_b32 s71, s71, 12\n\t" \
   "v_mov_b32 v110, 1\n\t" "s_sub_u32 s71, s71, 4096\n\t" "buffer_store_dword v110, v109, %[rs], s71 offen sc1\n\t" "v_cmp_eq_u32 vcc, -1, v15\n\t" \
   "s_add_i32 s72, s64, 2\n\t" "s_add_i32 s71, s64, 3\n\t" "s_min_i32 s71, s71, 63\n\t" "s_lshl_b32 s59, s71, 12\n\t" "s_cbranch_vccnz 141f\n\t" \
   "41:\n\t" "s_min_i32 s72, s72, 63\n\t" "s_lshl_b32 s79, s72, 13\n\t" "s_add_u32 s48, s69, s47\n\t" "s_cmp_eq_u32 s70, 0\n\t" \
   "s_cselect_b32 s48, s48, -1\n\t" "s_mov_b32 s66, s65\n\t" "s_add_i32 s64, s64, 1\n\t" "s_and_b32 s71, s64, 63\n\t" \
   "s_lshr_b64 s[72:73], %[sel], s71\n\t" "s_lshl_b32 s65, s71, 19\n\t" "s_and_b32 s72, s72, 1\n\t" "s_add_u32 s65, s65, %[pool]\n\t" \
   "s_lshl_b32 s72, s72, 13\n\t" "s_add_u32 s65, s65, s72\n\t" "s_xor_b32 s67, s54, %[lad0]\n\t" "s_xor_b32 s68, s67, 0x4000\n\t" \
   "s_add_u32 s67, s67, s66\n\t" "s_add_u32 s68, s68, s66\n\t" "s_lshr_b64 s[72:73], %[s3], s71\n\t" "s_and_b32 s54, s72, 1\n\t" \
   "s_lshl_b32 s54, s54, 14\n\t" "s_cmp_eq_u32 s64, 0\n\t" "s_cselect_b32 s70, 1, 0\n\t" "s_and_b32 s55, %[wv], 1\n\t" "s_lshl_b32 s55, s55, 14\n\t" \
   "s_xor_b32 s55, s55, s54\n\t" "s_xor_b32 s50, s55, 0x4000\n\t" "s_add_u32 s55, s55, s65\n\t" "s_add_u32 s50, s50, s65\n\t" "s_waitcnt vmcnt(1)\n\t" \
   "s_branch 48f\n\t" "44:\n\t" "v_mfma_f32_16x16x32_bf16 v[32:35], v[160:163], v[16:19], 0\n\t" "s_max_i32 s51, s64, 0\n\t" \
   "s_lshl_b32 s52, s51, 16\n\t" "s_lshl_b32 s51, s51, 14\n\t" "v_mfma_f32_16x16x32_bf16 v[36:39], v[176:179], v[16:19], 0\n\t" \
   "s_cmp_lt_i32 s64, 0\n\t" "s_cselect_b32 s49, 0x7ffffff0, 0\n\t" "s_add_i32 s72, s64, 1\n\t" \
   "v_mfma_f32_16x16x32_bf16 v[32:35], v[164:167], v[20:23], v[32:35]\n\t" "s_min_i32 s72, s72, 63\n\t" "s_lshl_b32 s79, s72, 13\n\t" \
   "s_mov_b32 s59, 0\n\t" "v_mfma_f32_16x16x32_bf16 v[36:39], v[180:183], v[20:23], v[36:39]\n\t" \
   "v_mfma_f32_16x16x32_bf16 v[32:35], v[168:171], v[24:27], v[32:35]\n\t" "s_waitcnt lgkmcnt(0)\n\t" \
   "v_mfma_f32_16x16x32_bf16 v[36:39], v[184:187], v[24:27], v[36:39]\n\t" "s_sub_u32 s74, s62, s57\n\t" "s_mul_i32 s74, s74, s58\n\t" \
   "s_lshr_b32 s74, s74, 12\n\t" "v_mfma_f32_16x16x32_bf16 v[32:35], v[172:175], v[28:31], v[32:35]\n\t" "s_add_u32 s74, s74, s56\n\t" \
   "s_lshl_b32 s60, s60, 3\n\t" "s_sub_u32 s75, s74, s60\n\t" "v_mfma_f32_16x16x32_bf16 v[36:39], v[188:191], v[28:31], v[36:39]\n\t" \
   "s_cmp_lt_u32 s75, 32\n\t" "s_cbranch_scc1 39f\n\t" "s_cmp_lt_i32 s75, 0\n\t" "s_cselect_b32 s61, 0, 31\n\t" "s_add_u32 s74, s60, s61\n\t" \
   "s_sub_i32 s75, s61, s75\n\t" "s_add_i32 s75, s75, 1\n\t" "s_ashr_i32 s75, s75, 1\n\t" "s_min_i32 s75, s75, 8\n\t" "s_max_i32 s75, s75, -8\n\t" \
   "s_mov_b32 s56, s74\n\t" "s_mov_b32 s57, s62\n\t" "s_add_u32 s58, s58, s75\n\t" "39:\n\t" "s_barrier\n\t" \
   "buffer_load_dword v139, %[rs], s65 offen sc1 lds\n\t" "v_mfma_f32_16x16x32_bf16 v[40:43], v[144:147], v[16:19], 0\n\t" "s_min_u32 s72, s70, 1\n\t" \
   "s_and_b32 s71, s74, -4\n\t" "v_mfma_f32_16x16x32_bf16 v[40:43], v[148:151], v[20:23], v[40:43]\n\t" "s_or_b32 s71, s71, s72\n\t" \
   "v_mov_b32 v111, s71\n\t" "v_mfma_f32_16x16x32_bf16 v[40:43], v[152:155], v[24:27], v[40:43]\n\t" \
   "buffer_store_dword v111, v131, %[rs], s65 offen sc1\n\t" P1TRACE "v_mfma_f32_16x16x32_bf16 v[40:43], v[156:159], v[28:31], v[40:43]\n\t" \
   "v_cndmask_b32 v32, v32, v125, s[20:21]\n\t" "v_cndmask_b32 v33, v33, v125, s[22:23]\n\t" "v_cndmask_b32 v34, v34, v125, s[24:25]\n\t" \
   "v_cndmask_b32 v35, v35, v125, s[26:27]\n\t" "v_cndmask_b32 v36, v36, v125, s[28:29]\n\t" "v_cndmask_b32 v37, v37, v125, s[30:31]\n\t" \
   "v_cndmask_b32 v38, v38, v125, s[32:33]\n\t" "v_cndmask_b32 v39, v39, v125, s[34:35]\n\t" "v_max3_f32 v80, v32, v33, v34\n\t" \
   "v_max3_f32 v81, v35, v36, v37\n\t" "v_max3_f32 v82, v38, v39, v40\n\t" "v_max3_f32 v83, v41, v42, v43\n\t" "v_max3_f32 v80, v80, v81, v82\n\t" \
   "v_max_f32 v81, v80, v83\n\t" "v_max_f32 v80, v80, v83\n\t" "ds_read_b128 v[160:163], v122 offset:0\n\t" \
   "ds_read_b128 v[164:167], v122 offset:1024\n\t" "v_permlane16_swap_b32 v80, v81\n\t" "v_max_f32 v82, v80, v81\n\t" "v_max_f32 v80, v80, v81\n\t" \
   "ds_read_b128 v[168:171], v122 offset:2048\n\t" "ds_read_b128 v[172:175], v122 offset:3072\n\t" "v_permlane32_swap_b32 v80, v82\n\t" \
   "v_max_f32 v80, v80, v82\n\t" "v_mul_f32 v80, 0xbe0293ee, v80\n\t" "v_fmamk_f32 v32, v32, 0x3e0293ee, v80\n\t" \
   "v_fmamk_f32 v33, v33, 0x3e0293ee, v80\n\t" "v_fmamk_f32 v34, v34, 0x3e0293ee, v80\n\t" "v_fmamk_f32 v35, v35, 0x3e0293ee, v80\n\t" \
   "v_fmamk_f32 v36, v36, 0x3e0293ee, v80\n\t" "v_fmamk_f32 v37, v37, 0x3e0293ee, v80\n\t" "v_fmamk_f32 v38, v38, 0x3e0293ee, v80\n\t" \
   "v_fmamk_f32 v39, v39, 0x3e0293ee, v80\n\t" "v_fmamk_f32 v40, v40, 0x3e0293ee, v80\n\t" "v_fmamk_f32 v41, v41, 0x3e0293ee, v80\n\t" \
   "v_fmamk_f32 v42, v42, 0x3e0293ee, v80\n\t" "v_fmamk_f32 v43, v43, 0x3e0293ee, v80\n\t" "v_exp_f32 v32, v32\n\t" "s_add_u32 s48, s69, s47\n\t" \
   "s_cmp_eq_u32 s70, 0\n\t" "v_exp_f32 v33, v33\n\t" "s_cselect_b32 s48, s48, -1\n\t" "s_mov_b32 s66, s65\n\t" "v_exp_f32 v34, v34\n\t" \
   "s_add_i32 s64, s64, 1\n\t" "s_and_b32 s71, s64, 63\n\t" "v_exp_f32 v35, v35\n\t" "s_lshr_b64 s[72:73], %[sel], s71\n\t" \
   "s_lshl_b32 s65, s71, 19\n\t" "v_exp_f32 v36, v36\n\t" "s_and_b32 s72, s72, 1\n\t" "s_add_u32 s65, s65, %[pool]\n\t" "v_exp_f32 v37, v37\n\t" \
   "s_lshl_b32 s72, s72, 13\n\t" "s_add_u32 s65, s65, s72\n\t" "v_exp_f32 v38, v38\n\t" "s_xor_b32 s67, s54, %[lad0]\n\t" \
   "s_xor_b32 s68, s67, 0x4000\n\t" "v_exp_f32 v39, v39\n\t" "s_add_u32 s67, s67, s66\n\t" "s_add_u32 s68, s68, s66\n\t" "v_exp_f32 v40, v40\n\t" \
   "s_lshr_b64 s[72:73], %[s3], s71\n\t" "s_and_b32 s54, s72, 1\n\t" "v_exp_f32 v41, v41\n\t" "s_lshl_b32 s54, s54, 14\n\t" "s_cmp_eq_u32 s64, 0\n\t" \
   "v_exp_f32 v42, v42\n\t" "s_cselect_b32 s70, 1, 0\n\t" "s_and_b32 s55, %[wv], 1\n\t" "v_exp_f32 v43, v43\n\t" "s_lshl_b32 s55, s55, 14\n\t" \
   "s_xor_b32 s55, s55, s54\n\t" "s_xor_b32 s50, s55, 0x4000\n\t" "s_add_u32 s55, s55, s65\n\t" "s_add_u32 s50, s50, s65\n\t" \
   "v_add_f32 v80, v32, v33\n\t" "v_add_f32 v81, v34, v35\n\t" "v_add_f32 v82, v36, v37\n\t" "v_add_f32 v83, v38, v39\n\t" \
   "v_add_f32 v84, v40, v41\n\t" "v_add_f32 v85, v42, v43\n\t" "v_add_f32 v80, v80, v81\n\t" "v_add_f32 v82, v82, v83\n\t" \
   "v_add_f32 v84, v84, v85\n\t" "v_add_f32 v80, v80, v82\n\t" "v_fma_f32 v82, v80, 2.0, v84\n\t" "v_fma_f32 v80, v80, 2.0, v84\n\t" \
   "ds_read_b128 v[176:179], v122 offset:4096\n\t" "ds_read_b128 v[180:183], v122 offset:5120\n\t" "v_permlane16_swap_b32 v80, v82\n\t" \
   "v_add_f32 v81, v80, v82\n\t" "v_add_f32 v80, v80, v82\n\t" "ds_read_b128 v[184:187], v122 offset:6144\n\t" \
   "ds_read_b128 v[188:191], v122 offset:7168\n\t" "v_permlane32_swap_b32 v80, v81\n\t" "v_add_f32 v80, v80, v81\n\t" "v_rcp_f32 v90, v80\n\t" \
   "s_nop 0\n\t" "v_add_f32 v90, v90, v90\n\t" "v_pk_mul_f32 v[32:33], v[32:33], v[90:91] op_sel_hi:[1,0]\n\t" \
   "v_pk_mul_f32 v[34:35], v[34:35], v[90:91] op_sel_hi:[1,0]\n\t" "v_pk_mul_f32 v[36:37], v[36:37], v[90:91] op_sel_hi:[1,0]\n\t" \
   "v_pk_mul_f32 v[38:39], v[38:39], v[90:91] op_sel_hi:[1,0]\n\t" "v_pk_mul_f32 v[40:41], v[40:41], v[90:91] op_sel_hi:[1,0]\n\t" \
   "v_pk_mul_f32 v[42:43], v[42:43], v[90:91] op_sel_hi:[1,0]\n\t" "v_cvt_pk_bf16_f32 v44, v32, v33\n\t" "v_cvt_pk_bf16_f32 v46, v34, v35\n\t" \
   "v_cvt_pk_bf16_f32 v45, v36, v37\n\t" "v_cvt_pk_bf16_f32 v47, v38, v39\n\t" "v_cvt_pk_bf16_f32 v32, v40, v41\n\t" \
   "v_cvt_pk_bf16_f32 v33, v42, v43\n\t" "s_waitcnt vmcnt(1)\n\t" "48:\n\t" "ds_read_b64 v[108:109], v123\n\t" "v_mov_b32 v48, 0\n\t" \
   "v_mov_b32 v49, 0\n\t" "v_mov_b32 v50, 0\n\t" "v_mov_b32 v51, 0\n\t" "v_mov_b32 v52, 0\n\t" "v_mov_b32 v53, 0\n\t" "v_mov_b32 v54, 0\n\t" \
   "v_mov_b32 v55, 0\n\t" "v_mov_b32 v56, 0\n\t" "v_mov_b32 v57, 0\n\t" "v_mov_b32 v58, 0\n\t" "v_mov_b32 v59, 0\n\t" "v_mov_b32 v60, 0\n\t" \
   "v_mov_b32 v61, 0\n\t" "v_mov_b32 v62, 0\n\t" "v_mov_b32 v63, 0\n\t" "s_cmp_eq_u32 s64, 64\n\t" "s_cbranch_scc1 90f\n\t" "s_waitcnt lgkmcnt(0)\n\t" \
   "v_readfirstlane_b32 s69, v108\n\t" "v_readfirstlane_b32 s71, v109\n\t" "s_cmp_eq_u32 s69, -1\n\t" "s_cbranch_scc1 63f\n\t" \
   "s_cmp_eq_u32 s71, -1\n\t" "s_cselect_b32 s47, s47, s71\n\t" "50:\n\t" "s_add_u32 s73, s69, %[m1]\n\t" RSTAMP(5) "51:\n\t" \
   "s_memrealtime s[60:61]\n\t" "s_memtime s[62:63]\n\t" "s_waitcnt lgkmcnt(0)\n\t" "s_sub_u32 s74, s62, s57\n\t" "s_mul_i32 s74, s74, s58\n\t" \
   "s_lshr_b32 s74, s74, 12\n\t" "s_add_u32 s74, s74, s56\n\t" "s_lshl_b32 s60, s60, 3\n\t" "s_sub_u32 s75, s74, s60\n\t" "s_cmp_lt_u32 s75, 32\n\t" \
   "s_cbranch_scc1 52f\n\t" "s_cmp_lt_i32 s75, 0\n\t" "s_cselect_b32 s61, 0, 31\n\t" "s_add_u32 s74, s60, s61\n\t" "s_sub_i32 s75, s61, s75\n\t" \
   "s_add_i32 s75, s75, 1\n\t" "s_ashr_i32 s75, s75, 1\n\t" "s_min_i32 s75, s75, 8\n\t" "s_max_i32 s75, s75, -8\n\t" "s_mov_b32 s56, s74\n\t" \
   "s_mov_b32 s57, s62\n\t" "s_add_u32 s58, s58, s75\n\t" "52:\n\t" "s_sub_i32 s74, s73, s74\n\t" "s_cmp_le_i32 s74, 150\n\t" "s_cbranch_scc1 53f\n\t" \
   "s_cmp_lt_i32 s74, 260\n\t" "s_cbranch_scc1 51b\n\t" "s_sleep 2\n\t" "s_cmp_lt_i32 s74, 32000\n\t" "s_cbranch_scc1 51b\n\t" "s_mov_b32 s74, 0\n\t" \
   "53:\n\t" "s_sub_i32 s74, s74, 56\n\t" "s_max_i32 s74, s74, 0\n\t" "s_mul_i32 s74, s74, 23\n\t" "s_lshr_b32 s74, s74, 5\n\t" \
   "s_and_b32 s74, s74, -4\n\t" "s_getpc_b64 s[60:61]\n\t" "54:\n\t" "s_sub_u32 s74, 55f-54b, s74\n\t" "s_add_u32 s60, s60, s74\n\t" \
   "s_addc_u32 s61, s61, 0\n\t" "s_setpc_b64 s[60:61]\n\t" "s_nop 3\n\t" "s_nop 3\n\t" "s_nop 3\n\t" "s_nop 3\n\t" "s_nop 3\n\t" "s_nop 3\n\t" \
   "s_nop 3\n\t" "s_nop 3\n\t" "s_nop 3\n\t" "s_nop 3\n\t" "s_nop 3\n\t" "s_nop 3\n\t" "s_nop 3\n\t" "s_nop 3\n\t" "s_nop 3\n\t" "s_nop 3\n\t" \
   "s_nop 3\n\t" "s_nop 3\n\t" "s_nop 3\n\t" "s_nop 3\n\t" "s_nop 3\n\t" "s_nop 3\n\t" "s_nop 3\n\t" "s_nop 3\n\t" "55:\n\t" "20:\n\t" RSTAMP(4) \
   "buffer_load_dwordx4 v[64:67], v128, %[rs], s67 offen sc1\n\t" "buffer_load_dwordx4 v[68:71], v129, %[rs], s67 offen sc1\n\t" \
   "buffer_load_dwordx4 v[72:75], v130, %[rs], s67 offen sc1\n\t" "s_nop 7\n\t" "s_nop 7\n\t" "s_nop 7\n\t" "s_cmp_lg_u32 s70, 0\n\t" \
   "s_cbranch_scc1 27f\n\t" "buffer_load_dwordx4 v[16:19], v138, %[rs], s79 offen offset:0 sc1\n\t" \
   "buffer_load_dwordx4 v[20:23], v138, %[rs], s79 offen offset:1024 sc1\n\t" \
   "buffer_load_dwordx4 v[24:27], v138, %[rs], s79 offen offset:2048 sc1\n\t" \
   "buffer_load_dwordx4 v[28:31], v138, %[rs], s79 offen offset:3072 sc1\n\t" "buffer_load_dword v15, v140, %[rs], s59 offen sc1\n\t" \
   "v_or_b32 v108, s49, v141\n\t" "v_or_b32 v109, s49, v142\n\t" "buffer_store_dwordx4 v[44:47], v108, %[rs], s52 offen sc1\n\t" \
   "buffer_store_dwordx2 v[32:33], v109, %[rs], s51 offen sc1\n\t" "28:\n\t" RSTAMP(3) "s_waitcnt vmcnt(9)\n\t" "ds_write_b128 v112, v[64:67]\n\t" \
   "v_smfmac_f32_16x16x64_bf16 v[48:51], v[64:67], a[0:7], v134\n\t" "s_waitcnt vmcnt(8)\n\t" "ds_write_b128 v113, v[68:71]\n\t" \
   "s_waitcnt vmcnt(7)\n\t" "ds_write_b128 v114, v[72:75]\n\t" RSTAMP(7) "2:\n\t" "v_smfmac_f32_16x16x64_bf16 v[52:55], v[64:67], a[64:71], v134\n\t" \
   "v_max3_u32 v110, v64, v66, v68\n\t" "v_max3_u32 v111, v70, v72, v74\n\t" "v_smfmac_f32_16x16x64_bf16 v[48:51], v[68:71], a[8:15], v134\n\t" \
   "v_max_u32 v110, v110, v111\n\t" "v_cmp_eq_u32 vcc, -1, v110\n\t" "v_smfmac_f32_16x16x64_bf16 v[52:55], v[68:71], a[72:79], v134\n\t" \
   "s_cbranch_vccnz 8f\n\t" "v_smfmac_f32_16x16x64_bf16 v[48:51], v[72:75], a[16:23], v134\n\t" "s_waitcnt lgkmcnt(0)\n\t" "s_barrier\n\t" RSTAMP(0) \
   "ds_read_b128 v[76:79], v115\n\t" "ds_read_b128 v[80:83], v116\n\t" "v_smfmac_f32_16x16x64_bf16 v[52:55], v[72:75], a[80:87], v134\n\t" \
   "ds_read_b128 v[84:87], v117\n\t" "ds_read_b128 v[88:91], v118\n\t" "v_smfmac_f32_16x16x64_bf16 v[56:59], v[64:67], a[128:135], v134\n\t" \
   "ds_read_b128 v[92:95], v119\n\t" "ds_read_b128 v[96:99], v120\n\t" "v_smfmac_f32_16x16x64_bf16 v[60:63], v[64:67], a[192:199], v134\n\t" \
   "ds_read_b128 v[100:103], v121\n\t" "v_smfmac_f32_16x16x64_bf16 v[56:59], v[68:71], a[136:143], v134\n\t" \
   "v_smfmac_f32_16x16x64_bf16 v[60:63], v[68:71], a[200:207], v134\n\t" "s_waitcnt lgkmcnt(6)\n\t" \
   "v_smfmac_f32_16x16x64_bf16 v[48:51], v[76:79], a[24:31], v134\n\t" "v_smfmac_f32_16x16x64_bf16 v[52:55], v[76:79], a[88:95], v134\n\t" \
   "s_waitcnt lgkmcnt(5)\n\t" "v_smfmac_f32_16x16x64_bf16 v[48:51], v[80:83], a[32:39], v134\n\t" \
   "v_smfmac_f32_16x16x64_bf16 v[52:55], v[80:83], a[96:103], v134\n\t" "s_waitcnt lgkmcnt(4)\n\t" \
   "v_smfmac_f32_16x16x64_bf16 v[48:51], v[84:87], a[40:47], v134\n\t" "v_smfmac_f32_16x16x64_bf16 v[52:55], v[84:87], a[104:111], v134\n\t" \
   "s_waitcnt lgkmcnt(3)\n\t" "v_smfmac_f32_16x16x64_bf16 v[48:51], v[88:91], a[48:55], v134\n\t" \
   "v_smfmac_f32_16x16x64_bf16 v[52:55], v[88:91], a[112:119], v134\n\t" "s_waitcnt lgkmcnt(2)\n\t" \
   "v_smfmac_f32_16x16x64_bf16 v[48:51], v[92:95], a[56:63], v134\n\t" "v_smfmac_f32_16x16x64_bf16 v[52:55], v[92:95], a[120:127], v134\n\t" \
   "s_waitcnt lgkmcnt(0)\n\t" "v_smfmac_f32_16x16x64_bf16 v[48:51], v[96:99], v[192:199], v134\n\t" \
   "v_smfmac_f32_16x16x64_bf16 v[48:51], v[100:103], v[200:207], v134\n\t" "v_smfmac_f32_16x16x64_bf16 v[52:55], v[96:99], v[208:215], v134\n\t" \
   "v_smfmac_f32_16x16x64_bf16 v[52:55], v[100:103], v[216:223], v134\n\t" "v_smfmac_f32_16x16x64_bf16 v[56:59], v[72:75], a[144:151], v134\n\t" \
   "v_add_f32 v48, v48, v49\n\t" "v_add_f32 v49, v50, v51\n\t" "v_smfmac_f32_16x16x64_bf16 v[60:63], v[72:75], a[208:215], v134\n\t" \
   "v_mul_f32 v48, 0x3e0293ee, v48\n\t" "v_mul_f32 v49, 0x3e0293ee, v49\n\t" "v_smfmac_f32_16x16x64_bf16 v[56:59], v[76:79], a[152:159], v134\n\t" \
   "v_add_f32 v52, v52, v53\n\t" "v_add_f32 v53, v54, v55\n\t" "v_smfmac_f32_16x16x64_bf16 v[56:59], v[80:83], a[160:167], v134\n\t" \
   "v_mul_f32 v52, 0x3e0293ee, v52\n\t" "v_mul_f32 v53, 0x3e0293ee, v53\n\t" "v_smfmac_f32_16x16x64_bf16 v[56:59], v[84:87], a[168:175], v134\n\t" \
   "v_cvt_pk_bf16_f32 v48, v48, v49\n\t" "ds_write_b32 v135, v48 offset:10240\n\t" \
   "v_smfmac_f32_16x16x64_bf16 v[56:59], v[88:91], a[176:183], v134\n\t" "v_cvt_pk_bf16_f32 v52, v52, v53\n\t" \
   "ds_write_b32 v135, v52 offset:10496\n\t" "v_smfmac_f32_16x16x64_bf16 v[56:59], v[92:95], a[184:191], v134\n\t" \
   "v_smfmac_f32_16x16x64_bf16 v[56:59], v[96:99], v[224:231], v134\n\t" "v_smfmac_f32_16x16x64_bf16 v[56:59], v[100:103], v[232:239], v134\n\t" \
   "s_waitcnt lgkmcnt(0)\n\t" "s_barrier\n\t" "ds_read_b64_tr_b16 v[64:65], v136 offset:10240\n\t" "ds_read_b64_tr_b16 v[66:67], v136 offset:10496\n\t" \
   "v_smfmac_f32_16x16x64_bf16 v[60:63], v[76:79], a[216:223], v134\n\t" "ds_read_b64_tr_b16 v[68:69], v136 offset:10752\n\t" \
   "v_smfmac_f32_16x16x64_bf16 v[60:63], v[80:83], a[224:231], v134\n\t" "ds_read_b64_tr_b16 v[70:71], v136 offset:11008\n\t" \
   "v_smfmac_f32_16x16x64_bf16 v[60:63], v[84:87], a[232:239], v134\n\t" "ds_read_b64_tr_b16 v[72:73], v136 offset:11264\n\t" \
   "v_smfmac_f32_16x16x64_bf16 v[60:63], v[88:91], a[240:247], v134\n\t" "ds_read_b64_tr_b16 v[74:75], v136 offset:11520\n\t" \
   "v_add_f32 v56, v56, v57\n\t" "v_add_f32 v57, v58, v59\n\t" "v_smfmac_f32_16x16x64_bf16 v[60:63], v[92:95], a[248:255], v134\n\t" \
   "ds_read_b64_tr_b16 v[104:105], v136 offset:11776\n\t" "ds_read_b64_tr_b16 v[106:107], v136 offset:12032\n\t" \
   "v_smfmac_f32_16x16x64_bf16 v[60:63], v[96:99], v[240:247], v134\n\t" "v_cvt_pk_bf16_f32 v56, v56, v57\n\t" \
   "ds_write_b32 v135, v56 offset:12288\n\t" "v_smfmac_f32_16x16x64_bf16 v[60:63], v[100:103], v[248:255], v134\n\t" RSTAMP(6) \
   "s_waitcnt lgkmcnt(7)\n\t" "v_mfma_f32_16x16x32_bf16 v[48:51], v[160:163], v[64:67], 0\n\t" \
   "v_mfma_f32_16x16x32_bf16 v[52:55], v[176:179], v[64:67], 0\n\t" "s_waitcnt lgkmcnt(3)\n\t" "s_nop 0\n\t" "v_add_f32 v60, v60, v61\n\t" \
   "v_add_f32 v61, v62, v63\n\t" "v_cvt_pk_bf16_f32 v60, v60, v61\n\t" "ds_write_b32 v135, v60 offset:12544\n\t" \
   "v_mfma_f32_16x16x32_bf16 v[48:51], v[164:167], v[68:71], v[48:51]\n\t" "v_mfma_f32_16x16x32_bf16 v[52:55], v[180:183], v[68:71], v[52:55]\n\t" \
   "v_mfma_f32_16x16x32_bf16 v[48:51], v[168:171], v[72:75], v[48:51]\n\t" "v_mfma_f32_16x16x32_bf16 v[52:55], v[184:187], v[72:75], v[52:55]\n\t" \
   "s_waitcnt lgkmcnt(2)\n\t" "v_mfma_f32_16x16x32_bf16 v[48:51], v[172:175], v[104:107], v[48:51]\n\t" \
   "v_mfma_f32_16x16x32_bf16 v[52:55], v[188:191], v[104:107], v[52:55]\n\t" "s_waitcnt lgkmcnt(0)\n\t" "s_barrier\n\t" \
   "ds_read_b64_tr_b16 v[144:145], v136 offset:12288\n\t" "ds_read_b64_tr_b16 v[146:147], v136 offset:12544\n\t" \
   "ds_read_b64_tr_b16 v[148:149], v136 offset:12800\n\t" "ds_read_b64_tr_b16 v[150:151], v136 offset:13056\n\t" \
   "ds_read_b64_tr_b16 v[152:153], v136 offset:13312\n\t" "ds_read_b64_tr_b16 v[154:155], v136 offset:13568\n\t" \
   "ds_read_b64_tr_b16 v[156:157], v136 offset:13824\n\t" "ds_read_b64_tr_b16 v[158:159], v136 offset:14080\n\t" "s_cmp_eq_u32 s64, 0\n\t" \
   "s_cbranch_scc1 92f\n\t" "93:\n\t" RSTAMP(1) "v_cndmask_b32 v48, v48, v52, %[hi8]\n\t" "v_cndmask_b32 v49, v49, v53, %[hi8]\n\t" \
   "v_cndmask_b32 v50, v50, v54, %[hi8]\n\t" "v_cndmask_b32 v51, v51, v55, %[hi8]\n\t" "v_exp_f32 v80, v48\n\t" "v_exp_f32 v81, v49\n\t" \
   "s_waitcnt lgkmcnt(6)\n\t" "v_mfma_f32_16x16x32_bf16 v[76:79], v[144:147], v[64:67], 0\n\t" "v_exp_f32 v82, v50\n\t" "v_exp_f32 v83, v51\n\t" \
   "s_waitcnt lgkmcnt(4)\n\t" "v_mfma_f32_16x16x32_bf16 v[76:79], v[148:151], v[68:71], v[76:79]\n\t" "v_add_f32 v96, v80, v81\n\t" \
   "v_add_f32 v103, v82, v83\n\t" "s_waitcnt lgkmcnt(2)\n\t" "v_mfma_f32_16x16x32_bf16 v[76:79], v[152:155], v[72:75], v[76:79]\n\t" \
   "v_add_f32 v94, v96, v103\n\t" "v_add_f32 v96, v96, v103\n\t" "s_waitcnt lgkmcnt(0)\n\t" \
   "v_mfma_f32_16x16x32_bf16 v[76:79], v[156:159], v[104:107], v[76:79]\n\t" "v_permlane32_swap_b32 v96, v94\n\t" "v_add_f32 v96, v96, v94\n\t" \
   "s_nop 1\n\t" "v_add_f32_dpp v96, v96, v96 row_ror:8 row_mask:0xf bank_mask:0xf\n\t" "s_nop 2\n\t" "v_exp_f32 v88, v76\n\t" "v_exp_f32 v89, v77\n\t" \
   "v_add_f32 v103, v96, v88\n\t" "v_exp_f32 v90, v78\n\t" "v_add_f32 v103, v103, v89\n\t" "v_exp_f32 v91, v79\n\t" "v_add_f32 v103, v103, v90\n\t" \
   "v_add_f32 v97, v103, v91\n\t" "v_add_f32 v103, v103, v91\n\t" "s_nop 1\n\t" "v_permlane16_swap_b32 v103, v97\n\t" "v_add_f32 v103, v103, v97\n\t" \
   "v_rcp_f32 v100, v103\n\t" "v_add_u32 v94, 0xe0800000, v103\n\t" "v_pk_mul_f32 v[80:81], v[80:81], v[100:101] op_sel_hi:[1,0]\n\t" \
   "v_cmp_lt_u32 vcc, 0x40000000, v94\n\t" "v_pk_mul_f32 v[82:83], v[82:83], v[100:101] op_sel_hi:[1,0]\n\t" "s_cbranch_vccnz 70f\n\t" \
   "v_cvt_pk_bf16_f32 v48, v80, v81\n\t" "v_cvt_pk_bf16_f32 v49, v82, v83\n\t" "buffer_store_dwordx2 v[48:49], v132, %[rs], s55 offen sc1\n\t" \
   "v_pk_mul_f32 v[88:89], v[88:89], v[100:101] op_sel_hi:[1,0]\n\t" "v_pk_mul_f32 v[90:91], v[90:91], v[100:101] op_sel_hi:[1,0]\n\t" \
   "v_cvt_pk_bf16_f32 v52, v88, v89\n\t" "v_cvt_pk_bf16_f32 v53, v90, v91\n\t" "71:\n\t" \
   "buffer_store_dwordx2 v[52:53], v133, %[rs], s65 offen sc1\n\t" "s_memtime s[62:63]\n\t" "s_memrealtime s[60:61]\n\t" \
   "buffer_store_dwordx2 v[48:49], v132, %[rs], s50 offen sc1\n\t" RSTAMP(2) "s_branch 40b\n\t" "70:\n\t" "v_max3_f32 v94, v48, v49, v50\n\t" \
   "v_max3_f32 v102, v76, v77, v78\n\t" "v_max3_f32 v94, v94, v51, v79\n\t" "v_max_f32 v92, v94, v102\n\t" "s_nop 1\n\t" \
   "v_max_f32_dpp v102, v92, v92 row_ror:8 row_mask:0xf bank_mask:0xf\n\t" "v_max_f32_dpp v94, v92, v92 row_ror:8 row_mask:0xf bank_mask:0xf\n\t" \
   "s_nop 1\n\t" "v_permlane32_swap_b32 v102, v94\n\t" "v_max_f32 v95, v102, v94\n\t" "v_max_f32 v102, v102, v94\n\t" "s_nop 1\n\t" \
   "v_permlane16_swap_b32 v102, v95\n\t" "v_max_f32 v102, v102, v95\n\t" "v_sub_f32 v80, v48, v102\n\t" "v_sub_f32 v88, v76, v102\n\t" \
   "v_sub_f32 v81, v49, v102\n\t" "v_sub_f32 v89, v77, v102\n\t" "v_sub_f32 v82, v50, v102\n\t" "v_sub_f32 v90, v78, v102\n\t" \
   "v_sub_f32 v83, v51, v102\n\t" "v_sub_f32 v91, v79, v102\n\t" "v_exp_f32 v80, v80\n\t" "v_exp_f32 v88, v88\n\t" "v_exp_f32 v81, v81\n\t" \
   "v_exp_f32 v89, v89\n\t" "v_exp_f32 v82, v82\n\t" "v_exp_f32 v90, v90\n\t" "v_exp_f32 v83, v83\n\t" "v_exp_f32 v91, v91\n\t" "s_nop 0\n\t" \
   "v_add_f32 v96, v80, v81\n\t" "v_add_f32 v103, v82, v83\n\t" "v_add_f32 v94, v96, v103\n\t" "v_add_f32 v96, v96, v103\n\t" "s_nop 1\n\t" \
   "v_permlane32_swap_b32 v96, v94\n\t" "v_add_f32 v96, v96, v94\n\t" "s_nop 1\n\t" \
   "v_add_f32_dpp v96, v96, v96 row_ror:8 row_mask:0xf bank_mask:0xf\n\t" "v_add_f32 v103, v96, v88\n\t" "v_add_f32 v103, v103, v89\n\t" \
   "v_add_f32 v103, v103, v90\n\t" "v_add_f32 v97, v103, v91\n\t" "v_add_f32 v103, v103, v91\n\t" "s_nop 1\n\t" "v_permlane16_swap_b32 v103, v97\n\t" \
   "v_add_f32 v103, v103, v97\n\t" "v_rcp_f32 v100, v103\n\t" "s_nop 0\n\t" "v_pk_mul_f32 v[80:81], v[80:81], v[100:101] op_sel_hi:[1,0]\n\t" \
   "v_pk_mul_f32 v[82:83], v[82:83], v[100:101] op_sel_hi:[1,0]\n\t" "v_pk_mul_f32 v[88:89], v[88:89], v[100:101] op_sel_hi:[1,0]\n\t" \
   "v_pk_mul_f32 v[90:91], v[90:91], v[100:101] op_sel_hi:[1,0]\n\t" "v_cvt_pk_bf16_f32 v48, v80, v81\n\t" "v_cvt_pk_bf16_f32 v49, v82, v83\n\t" \
   "v_cvt_pk_bf16_f32 v52, v88, v89\n\t" "v_cvt_pk_bf16_f32 v53, v90, v91\n\t" "v_min_u32 v48, -2, v48\n\t" "v_min_u32 v52, -2, v52\n\t" \
   "buffer_store_dwordx2 v[48:49], v132, %[rs], s55 offen sc1\n\t" "s_branch 71b\n\t" "63:\n\t" "s_cmp_lg_u32 s64, 0\n\t" "s_cbranch_scc1 64f\n\t" \
   "s_sub_u32 s69, s56, 0x10000\n\t" "s_and_b32 s69, s69, -2\n\t" "s_branch 50b\n\t" "64:\n\t" "s_mov_b32 s69, s48\n\t" "s_cmp_lt_u32 s64, 3\n\t" \
   "s_cselect_b32 s69, -1, s69\n\t" "s_cmp_lg_u32 s69, -1\n\t" "s_cbranch_scc1 50b\n\t" "s_add_u32 s76, s76, 0x1000000\n\t" "s_mov_b32 s73, 0\n\t" \
   "65:\n\t" "s_add_u32 s73, s73, 1\n\t" "s_min_u32 s72, s73, 7\n\t" "s_mul_i32 s72, s72, 5\n\t" "s_add_u32 s72, s72, %[xs]\n\t" \
   "s_and_b32 s72, s72, 7\n\t" "s_lshl_b32 s71, %[xs], 7\n\t" "v_subrev_u32 v109, s71, v139\n\t" "s_lshl_b32 s72, s72, 7\n\t" \
   "s_add_u32 s72, s72, s66\n\t" "s_cmp_lt_u32 s73, 7\n\t" "s_cbranch_scc1 66f\n\t" "s_sleep 4\n\t" "66:\n\t" \
   "buffer_load_dword v108, v109, %[rs], s72 offen sc1\n\t" "s_waitcnt vmcnt(0)\n\t" "v_readfirstlane_b32 s69, v108\n\t" \
   "s_cmp_gt_u32 s73, 0x10000\n\t" "s_cbranch_scc1 50b\n\t" "s_cmp_eq_u32 s69, -1\n\t" "s_cbranch_scc1 65b\n\t" "s_branch 50b\n\t" "8:\n\t" \
   "s_nop 7\n\t" "s_waitcnt vmcnt(7) lgkmcnt(0)\n\t" "s_add_u32 s70, s70, 1\n\t" "s_cmp_gt_u32 s70, 2\n\t" "s_cbranch_scc1 81f\n\t" \
   "s_lshl_b32 s71, s70, 3\n\t" "s_lshl_b32 s71, 1, s71\n\t" "s_add_u32 s76, s76, s71\n\t" "81:\n\t" "v_mov_b32 v48, 0\n\t" "v_mov_b32 v49, 0\n\t" \
   "v_mov_b32 v50, 0\n\t" "v_mov_b32 v51, 0\n\t" "v_mov_b32 v52, 0\n\t" "v_mov_b32 v53, 0\n\t" "v_mov_b32 v54, 0\n\t" "v_mov_b32 v55, 0\n\t" \
   "v_mov_b32 v56, 0\n\t" "v_mov_b32 v57, 0\n\t" "v_mov_b32 v58, 0\n\t" "v_mov_b32 v59, 0\n\t" "v_mov_b32 v60, 0\n\t" "v_mov_b32 v61, 0\n\t" \
   "v_mov_b32 v62, 0\n\t" "v_mov_b32 v63, 0\n\t" "s_min_u32 s71, s70, 7\n\t" "s_lshr_b32 s67, s71, 1\n\t" "s_add_u32 s67, s67, %[xs]\n\t" \
   "s_and_b32 s67, s67, 3\n\t" "s_lshl_b32 s67, s67, 11\n\t" "s_and_b32 s72, s71, 1\n\t" "s_lshl_b32 s72, s72, 14\n\t" "s_add_u32 s67, s67, s72\n\t" \
   "s_add_u32 s67, s67, s66\n\t" "s_cmp_eq_u32 s70, 1\n\t" "s_cselect_b32 s67, s68, s67\n\t" "s_cmp_lt_u32 s70, 7\n\t" "s_cbranch_scc1 20b\n\t" \
   "s_sleep 3\n\t" "s_cmp_lt_u32 s70, 0x20000\n\t" "s_cbranch_scc1 20b\n\t" "s_mov_b32 s67, %[esc]\n\t" "s_mov_b32 s68, %[esc]\n\t" "s_branch 20b\n\t" \
   "27:\n\t" "v_mov_b32 v98, 0x7ffffff0\n\t" "buffer_store_dword v98, v98, %[rs], 0 offen\n\t" "buffer_store_dword v98, v98, %[rs], 0 offen\n\t" \
   "buffer_store_dword v98, v98, %[rs], 0 offen\n\t" "buffer_store_dword v98, v98, %[rs], 0 offen\n\t" \
   "buffer_store_dword v98, v98, %[rs], 0 offen\n\t" "buffer_store_dword v98, v98, %[rs], 0 offen\n\t" \
   "buffer_store_dword v98, v98, %[rs], 0 offen\n\t" "s_branch 28b\n\t" "141:\n\t" "s_min_i32 s71, s72, 63\n\t" "s_lshl_b32 s71, s71, 12\n\t" \
   "s_mov_b32 s74, 0\n\t" "42:\n\t" "buffer_load_dword v15, v140, %[rs], s71 offen sc1\n\t" "s_add_u32 s74, s74, 1\n\t" "s_waitcnt vmcnt(0)\n\t" \
   "v_cmp_eq_u32 vcc, -1, v15\n\t" "s_cmp_gt_u32 s74, 0x10000\n\t" "s_cbranch_scc1 41b\n\t" "s_cbranch_vccz 41b\n\t" "s_sleep 2\n\t" "s_branch 42b\n\t" \
   "95:\n\t" "s_mov_b32 s72, 1\n\t" "s_branch 197f\n\t" "97:\n\t" "buffer_load_dwordx4 v[16:19], v138, %[rs], s79 offen offset:0 sc1\n\t" \
   "buffer_load_dwordx4 v[20:23], v138, %[rs], s79 offen offset:1024 sc1\n\t" \
   "buffer_load_dwordx4 v[24:27], v138, %[rs], s79 offen offset:2048 sc1\n\t" \
   "buffer_load_dwordx4 v[28:31], v138, %[rs], s79 offen offset:3072 sc1\n\t" "buffer_load_dword v15, v140, %[rs], s59 offen sc1\n\t" \
   "v_or_b32 v108, s49, v141\n\t" "v_or_b32 v109, s49, v142\n\t" "buffer_store_dwordx4 v[44:47], v108, %[rs], s52 offen sc1\n\t" \
   "buffer_store_dwordx2 v[32:33], v109, %[rs], s51 offen sc1\n\t" "s_waitcnt vmcnt(0)\n\t" "s_branch 96b\n\t" "197:\n\t" "s_min_i32 s71, s72, 63\n\t" \
   "s_lshl_b32 s71, s71, 12\n\t" "s_mov_b32 s74, 0\n\t" "98:\n\t" "buffer_load_dword v15, v140, %[rs], s71 offen sc1\n\t" "s_add_u32 s74, s74, 1\n\t" \
   "s_waitcnt vmcnt(0)\n\t" "v_cmp_eq_u32 vcc, -1, v15\n\t" "s_cmp_gt_u32 s74, 0x10000\n\t" "s_cbranch_scc1 97b\n\t" "s_cbranch_vccz 97b\n\t" \
   "s_sleep 2\n\t" "s_branch 98b\n\t" "92:\n\t" "s_mov_b32 s72, 0\n\t" "s_branch 145f\n\t" "45:\n\t" "v_mbcnt_lo_u32_b32 v98, -1, 0\n\t" \
   "v_mbcnt_hi_u32_b32 v98, -1, v98\n\t" "v_lshlrev_b32 v98, 4, v98\n\t" "s_add_u32 s72, %[kf0], 4096\n\t" \
   "buffer_load_dwordx4 v[160:163], v98, %[rs], %[kf0] offen offset:0 sc1\n\t" \
   "buffer_load_dwordx4 v[164:167], v98, %[rs], %[kf0] offen offset:1024 sc1\n\t" \
   "buffer_load_dwordx4 v[168:171], v98, %[rs], %[kf0] offen offset:2048 sc1\n\t" \
   "buffer_load_dwordx4 v[172:175], v98, %[rs], %[kf0] offen offset:3072 sc1\n\t" \
   "buffer_load_dwordx4 v[176:179], v98, %[rs], s72 offen offset:0 sc1\n\t" "buffer_load_dwordx4 v[180:183], v98, %[rs], s72 offen offset:1024 sc1\n\t" \
   "buffer_load_dwordx4 v[184:187], v98, %[rs], s72 offen offset:2048 sc1\n\t" \
   "buffer_load_dwordx4 v[188:191], v98, %[rs], s72 offen offset:3072 sc1\n\t" "s_waitcnt vmcnt(0)\n\t" \
   "v_mfma_f32_16x16x32_bf16 v[48:51], v[160:163], v[64:67], 0\n\t" "v_mfma_f32_16x16x32_bf16 v[52:55], v[176:179], v[64:67], 0\n\t" \
   "v_mfma_f32_16x16x32_bf16 v[48:51], v[164:167], v[68:71], v[48:51]\n\t" "v_mfma_f32_16x16x32_bf16 v[52:55], v[180:183], v[68:71], v[52:55]\n\t" \
   "v_mfma_f32_16x16x32_bf16 v[48:51], v[168:171], v[72:75], v[48:51]\n\t" "v_mfma_f32_16x16x32_bf16 v[52:55], v[184:187], v[72:75], v[52:55]\n\t" \
   "v_mfma_f32_16x16x32_bf16 v[48:51], v[172:175], v[104:107], v[48:51]\n\t" "v_mfma_f32_16x16x32_bf16 v[52:55], v[188:191], v[104:107], v[52:55]\n\t" \
   "s_nop 7\n\t" "s_branch 93b\n\t" "145:\n\t" "s_min_i32 s71, s72, 63\n\t" "s_lshl_b32 s71, s71, 12\n\t" "s_mov_b32 s74, 0\n\t" "46:\n\t" \
   "buffer_load_dword v15, v140, %[rs], s71 offen sc1\n\t" "s_add_u32 s74, s74, 1\n\t" "s_waitcnt vmcnt(0)\n\t" "v_cmp_eq_u32 vcc, -1, v15\n\t" \
   "s_cmp_gt_u32 s74, 0x10000\n\t" "s_cbranch_scc1 45b\n\t" "s_cbranch_vccz 45b\n\t" "s_sleep 2\n\t" "s_branch 46b\n\t" "90:\n\t" \
   "v_or_b32 v108, s49, v141\n\t" "v_or_b32 v109, s49, v142\n\t" "buffer_store_dwordx4 v[44:47], v108, %[rs], s52 offen sc1\n\t" \
   "buffer_store_dwordx2 v[32:33], v109, %[rs], s51 offen sc1\n\t" "s_waitcnt vmcnt(0) lgkmcnt(0)"
#define P1_CLOB "v15", "v16", "v17", "v18", "v19", "v20", "v21", "v22", "v23", "v24", "v25", "v26", "v27", "v28", "v29", "v30", "v31", "v32", "v33", "v34", "v35", "v36", "v37", "v38", "v39", "v40", "v41", "v42", "v43", "v44", "v45", "v46", "v47", "v48", "v49", "v50", "v51", "v52", "v53", "v54", "v55", "v56", "v57", "v58", "v59", "v60", "v61", "v62", "v63", "v64", "v65", "v66", "v67", "v68", "v69", "v70", "v71", "v72", "v73", "v74", "v75", "v76", "v77", "v78", "v79", "v80", "v81", "v82", "v83", "v84", "v85", "v86", "v87", "v88", "v89", "v90", "v91", "v92", "v93", "v94", "v95", "v96", "v97", "v98", "v99", "v100", "v101", "v102", "v103", "v104", "v105", "v106", "v107", "v108", "v109", "v110", "v111", "v144", "v145", "v146", "v147", "v148", "v149", "v150", "v151", "v152", "v153", "v154", "v155", "v156", "v157", "v158", "v159", "v160", "v161", "v162", "v163", "v164", "v165", "v166", "v167", "v168", "v169", "v170", "v171", "v172", "v173", "v174", "v175", "v176", "v177", "v178", "v179", "v180", "v181", "v182", "v183", "v184", "v185", "v186", "v187", "v188", "v189", "v190", "v191", "s60", "s61", "s62", "s63", "s64", "s65", "s66", "s67", "s68", "s69", "s70", "s71", "s72", "s73", "s74", "s75", "s47", "s48", "s49", "s50", "s51", "s52", "s54", "s55", "s79", "s20", "s21", "s22", "s23", "s24", "s25", "s26", "s27", "s28", "s29", "s30", "s31", "s32", "s33", "s34", "s35"
#define PV_ASM \
   "s_mov_b32 s64, -1\n\t" "s_mov_b32 s76, 0\n\t" "s_mov_b32 s77, 0\n\t" "s_mov_b32 s71, 63\n\t" "s_mov_b32 s47, 1300\n\t" "s_mov_b32 s70, 0\n\t" \
   "s_mov_b32 s69, 0\n\t" "s_lshr_b64 s[72:73], %[sel], s71\n\t" "s_lshl_b32 s65, s71, 19\n\t" "s_and_b32 s72, s72, 1\n\t" \
   "s_add_u32 s65, s65, %[pool]\n\t" "s_lshl_b32 s72, s72, 13\n\t" "s_add_u32 s65, s65, s72\n\t" "s_lshr_b64 s[72:73], %[s3], s71\n\t" \
   "s_and_b32 s54, s72, 1\n\t" "s_lshl_b32 s54, s54, 14\n\t" "40:\n\t" "s_cmp_lt_i32 s64, 0\n\t" "s_cselect_b32 s72, 0x7ffffff0, 0\n\t" \
   "s_max_i32 s71, s64, 0\n\t" "v_or_b32 v109, s72, v143\n\t" "s_lshl_b32 s71, s71, 12\n\t" "v_mov_b32 v110, 1\n\t" "s_waitcnt vmcnt(0)\n\t" RSTAMP(3) \
   "s_barrier\n\t" "buffer_load_dword v108, v139, %[rs], s65 offen sc1\n\t" "buffer_store_dword v110, v109, %[rs], s71 offen sc1\n\t" PVTRACE \
   "s_add_u32 s48, s69, s47\n\t" "s_cmp_eq_u32 s70, 0\n\t" "s_cselect_b32 s48, s48, -1\n\t" "s_mov_b32 s66, s65\n\t" "s_add_i32 s64, s64, 1\n\t" \
   "s_and_b32 s71, s64, 63\n\t" "s_lshr_b64 s[72:73], %[sel], s71\n\t" "s_lshl_b32 s65, s71, 19\n\t" "s_and_b32 s72, s72, 1\n\t" \
   "s_add_u32 s65, s65, %[pool]\n\t" "s_lshl_b32 s72, s72, 13\n\t" "s_add_u32 s65, s65, s72\n\t" "s_xor_b32 s67, s54, %[lad0]\n\t" \
   "s_xor_b32 s68, s67, 0x4000\n\t" "s_add_u32 s67, s67, s66\n\t" "s_add_u32 s68, s68, s66\n\t" "s_lshr_b64 s[72:73], %[s3], s71\n\t" \
   "s_and_b32 s54, s72, 1\n\t" "s_lshl_b32 s54, s54, 14\n\t" "s_mov_b32 s70, 0\n\t" "s_lshr_b64 s[72:73], %[ysel], s71\n\t" \
   "s_mul_i32 s73, s71, 43\n\t" "s_mul_i32 s49, s71, 80\n\t" "s_and_b32 s72, s72, 1\n\t" "s_lshr_b32 s73, s73, 7\n\t" "s_add_u32 s49, s49, s72\n\t" \
   "s_lshl4_add_u32 s49, s73, s49\n\t" "s_lshl_b32 s49, s49, 13\n\t" "s_add_u32 s49, s49, %[yb]\n\t" "s_add_u32 s50, s49, 32768\n\t" \
   "s_add_u32 s51, s49, 65536\n\t" "s_add_u32 s52, s49, 98304\n\t" "s_add_u32 s53, s49, 131072\n\t" "s_cmp_eq_u32 s64, 32\n\t" "s_cbranch_scc1 70f\n\t" \
   "71:\n\t" "v_mov_b32 v48, 0\n\t" "v_mov_b32 v49, 0\n\t" "v_mov_b32 v50, 0\n\t" "v_mov_b32 v51, 0\n\t" "v_mov_b32 v52, 0\n\t" "v_mov_b32 v53, 0\n\t" \
   "v_mov_b32 v54, 0\n\t" "v_mov_b32 v55, 0\n\t" "v_mov_b32 v144, 0\n\t" "v_mov_b32 v145, 0\n\t" "v_mov_b32 v146, 0\n\t" "v_mov_b32 v147, 0\n\t" \
   "v_mov_b32 v148, 0\n\t" "v_mov_b32 v149, 0\n\t" "v_mov_b32 v150, 0\n\t" "v_mov_b32 v151, 0\n\t" "v_mov_b32 v152, 0\n\t" "v_mov_b32 v153, 0\n\t" \
   "v_mov_b32 v154, 0\n\t" "v_mov_b32 v155, 0\n\t" "v_mov_b32 v156, 0\n\t" "v_mov_b32 v157, 0\n\t" "v_mov_b32 v158, 0\n\t" "v_mov_b32 v159, 0\n\t" \
   "v_mov_b32 v160, 0\n\t" "v_mov_b32 v161, 0\n\t" "v_mov_b32 v162, 0\n\t" "v_mov_b32 v163, 0\n\t" "s_cmp_eq_u32 s64, 64\n\t" "s_cbranch_scc1 90f\n\t" \
   "s_waitcnt vmcnt(1)\n\t" "v_readlane_b32 s69, v108, 0\n\t" "v_readlane_b32 s71, v108, 1\n\t" "s_cmp_eq_u32 s69, -1\n\t" "s_cbranch_scc1 63f\n\t" \
   "s_cmp_eq_u32 s71, -1\n\t" "s_cselect_b32 s47, s47, s71\n\t" "50:\n\t" "s_add_u32 s73, s69, %[m1c]\n\t" RSTAMP(5) "51:\n\t" \
   "s_memrealtime s[60:61]\n\t" "s_memtime s[62:63]\n\t" "s_waitcnt lgkmcnt(0)\n\t" "s_sub_u32 s74, s62, s57\n\t" "s_mul_i32 s74, s74, s58\n\t" \
   "s_lshr_b32 s74, s74, 12\n\t" "s_add_u32 s74, s74, s56\n\t" "s_lshl_b32 s60, s60, 3\n\t" "s_sub_u32 s75, s74, s60\n\t" "s_cmp_lt_u32 s75, 32\n\t" \
   "s_cbranch_scc1 52f\n\t" "s_cmp_lt_i32 s75, 0\n\t" "s_cselect_b32 s61, 0, 31\n\t" "s_add_u32 s74, s60, s61\n\t" "s_sub_i32 s75, s61, s75\n\t" \
   "s_add_i32 s75, s75, 1\n\t" "s_ashr_i32 s75, s75, 1\n\t" "s_min_i32 s75, s75, 8\n\t" "s_max_i32 s75, s75, -8\n\t" "s_mov_b32 s56, s74\n\t" \
   "s_mov_b32 s57, s62\n\t" "s_add_u32 s58, s58, s75\n\t" "52:\n\t" "s_sub_i32 s74, s73, s74\n\t" "s_cmp_le_i32 s74, 150\n\t" "s_cbranch_scc1 53f\n\t" \
   "s_cmp_lt_i32 s74, 260\n\t" "s_cbranch_scc1 51b\n\t" "s_sleep 2\n\t" "s_cmp_lt_i32 s74, 32000\n\t" "s_cbranch_scc1 51b\n\t" "s_mov_b32 s74, 0\n\t" \
   "53:\n\t" "s_sub_i32 s74, s74, 56\n\t" "s_max_i32 s74, s74, 0\n\t" "s_mul_i32 s74, s74, 23\n\t" "s_lshr_b32 s74, s74, 5\n\t" \
   "s_and_b32 s74, s74, -4\n\t" "s_getpc_b64 s[60:61]\n\t" "54:\n\t" "s_sub_u32 s74, 55f-54b, s74\n\t" "s_add_u32 s60, s60, s74\n\t" \
   "s_addc_u32 s61, s61, 0\n\t" "s_setpc_b64 s[60:61]\n\t" "s_nop 3\n\t" "s_nop 3\n\t" "s_nop 3\n\t" "s_nop 3\n\t" "s_nop 3\n\t" "s_nop 3\n\t" \
   "s_nop 3\n\t" "s_nop 3\n\t" "s_nop 3\n\t" "s_nop 3\n\t" "s_nop 3\n\t" "s_nop 3\n\t" "s_nop 3\n\t" "s_nop 3\n\t" "s_nop 3\n\t" "s_nop 3\n\t" \
   "s_nop 3\n\t" "s_nop 3\n\t" "s_nop 3\n\t" "s_nop 3\n\t" "s_nop 3\n\t" "s_nop 3\n\t" "s_nop 3\n\t" "s_nop 3\n\t" "55:\n\t" "20:\n\t" RSTAMP(4) \
   "buffer_load_dwordx4 v[64:67], v128, %[rs], s67 offen sc1\n\t" "buffer_load_dwordx4 v[68:71], v129, %[rs], s67 offen sc1\n\t" \
   "buffer_load_dwordx4 v[72:75], v130, %[rs], s67 offen sc1\n\t" "s_waitcnt vmcnt(2)\n\t" "ds_write_b128 v112, v[64:67]\n\t" \
   "v_smfmac_f32_16x16x64_bf16 v[48:51], v[64:67], a[0:7], v134\n\t" "s_waitcnt vmcnt(1)\n\t" "ds_write_b128 v113, v[68:71]\n\t" \
   "s_waitcnt vmcnt(0)\n\t" "ds_write_b128 v114, v[72:75]\n\t" RSTAMP(7) "v_smfmac_f32_16x16x64_bf16 v[52:55], v[64:67], a[64:71], v134\n\t" \
   "v_max3_u32 v110, v64, v66, v68\n\t" "v_max3_u32 v111, v70, v72, v74\n\t" "v_smfmac_f32_16x16x64_bf16 v[48:51], v[68:71], a[8:15], v134\n\t" \
   "v_max_u32 v110, v110, v111\n\t" "v_cmp_eq_u32 vcc, -1, v110\n\t" "v_smfmac_f32_16x16x64_bf16 v[52:55], v[68:71], a[72:79], v134\n\t" \
   "s_cbranch_vccnz 8f\n\t" "s_waitcnt lgkmcnt(0)\n\t" "s_barrier\n\t" RSTAMP(0) "ds_read_b128 v[76:79], v115\n\t" "ds_read_b128 v[80:83], v116\n\t" \
   "ds_read_b128 v[84:87], v117\n\t" "v_smfmac_f32_16x16x64_bf16 v[48:51], v[72:75], a[16:23], v134\n\t" "ds_read_b128 v[88:91], v118\n\t" \
   "ds_read_b128 v[92:95], v119\n\t" "v_smfmac_f32_16x16x64_bf16 v[52:55], v[72:75], a[80:87], v134\n\t" "ds_read_b128 v[96:99], v120\n\t" \
   "ds_read_b128 v[100:103], v121\n\t" "s_waitcnt lgkmcnt(6)\n\t" "v_smfmac_f32_16x16x64_bf16 v[48:51], v[76:79], a[24:31], v134\n\t" \
   "v_smfmac_f32_16x16x64_bf16 v[52:55], v[76:79], a[88:95], v134\n\t" "s_waitcnt lgkmcnt(5)\n\t" \
   "v_smfmac_f32_16x16x64_bf16 v[48:51], v[80:83], a[32:39], v134\n\t" "v_smfmac_f32_16x16x64_bf16 v[52:55], v[80:83], a[96:103], v134\n\t" \
   "s_waitcnt lgkmcnt(4)\n\t" "v_smfmac_f32_16x16x64_bf16 v[48:51], v[84:87], a[40:47], v134\n\t" \
   "v_smfmac_f32_16x16x64_bf16 v[52:55], v[84:87], a[104:111], v134\n\t" "s_waitcnt lgkmcnt(3)\n\t" \
   "v_smfmac_f32_16x16x64_bf16 v[48:51], v[88:91], a[48:55], v134\n\t" "v_smfmac_f32_16x16x64_bf16 v[52:55], v[88:91], a[112:119], v134\n\t" \
   "s_waitcnt lgkmcnt(0)\n\t" "v_smfmac_f32_16x16x64_bf16 v[48:51], v[92:95], a[56:63], v134\n\t" \
   "v_smfmac_f32_16x16x64_bf16 v[48:51], v[96:99], v[192:199], v134\n\t" "v_smfmac_f32_16x16x64_bf16 v[48:51], v[100:103], v[200:207], v134\n\t" \
   "v_smfmac_f32_16x16x64_bf16 v[52:55], v[92:95], a[120:127], v134\n\t" "v_smfmac_f32_16x16x64_bf16 v[52:55], v[96:99], v[208:215], v134\n\t" \
   "v_smfmac_f32_16x16x64_bf16 v[52:55], v[100:103], v[216:223], v134\n\t" "v_add_f32 v48, v48, v49\n\t" "v_add_f32 v49, v50, v51\n\t" \
   "v_cvt_pk_bf16_f32 v48, v48, v49\n\t" "ds_write_b32 v135, v48 offset:10240\n\t" "s_nop 3\n\t" "v_add_f32 v52, v52, v53\n\t" \
   "v_add_f32 v53, v54, v55\n\t" "v_cvt_pk_bf16_f32 v52, v52, v53\n\t" "ds_write_b32 v135, v52 offset:10496\n\t" "s_waitcnt lgkmcnt(0)\n\t" \
   "s_barrier\n\t" RSTAMP(6) "ds_read_b64_tr_b16 v[16:17], v136 offset:10240\n\t" "ds_read_b64_tr_b16 v[18:19], v136 offset:10496\n\t" \
   "ds_read_b64_tr_b16 v[20:21], v136 offset:10752\n\t" "ds_read_b64_tr_b16 v[22:23], v136 offset:11008\n\t" \
   "ds_read_b64_tr_b16 v[24:25], v136 offset:11264\n\t" "ds_read_b64_tr_b16 v[26:27], v136 offset:11520\n\t" \
   "ds_read_b64_tr_b16 v[28:29], v136 offset:11776\n\t" "ds_read_b64_tr_b16 v[30:31], v136 offset:12032\n\t" "s_waitcnt lgkmcnt(6)\n\t" \
   "v_smfmac_f32_16x16x64_bf16 v[144:147], v[16:19], a[128:135], v131\n\t" "v_smfmac_f32_16x16x64_bf16 v[148:151], v[16:19], a[160:167], v131\n\t" \
   "v_smfmac_f32_16x16x64_bf16 v[152:155], v[16:19], a[192:199], v131\n\t" "v_smfmac_f32_16x16x64_bf16 v[156:159], v[16:19], a[224:231], v131\n\t" \
   "v_smfmac_f32_16x16x64_bf16 v[160:163], v[16:19], v[224:231], v131\n\t" "s_waitcnt lgkmcnt(0)\n\t" \
   "v_smfmac_f32_16x16x64_bf16 v[144:147], v[20:23], a[136:143], v131\n\t" "v_smfmac_f32_16x16x64_bf16 v[144:147], v[24:27], a[144:151], v131\n\t" \
   "v_smfmac_f32_16x16x64_bf16 v[144:147], v[28:31], a[152:159], v131\n\t" "v_smfmac_f32_16x16x64_bf16 v[148:151], v[20:23], a[168:175], v131\n\t" \
   "v_smfmac_f32_16x16x64_bf16 v[148:151], v[24:27], a[176:183], v131\n\t" "v_smfmac_f32_16x16x64_bf16 v[148:151], v[28:31], a[184:191], v131\n\t" \
   "v_smfmac_f32_16x16x64_bf16 v[152:155], v[20:23], a[200:207], v131\n\t" "v_cvt_pk_bf16_f32 v164, v144, v145\n\t" \
   "v_cvt_pk_bf16_f32 v165, v146, v147\n\t" "v_smfmac_f32_16x16x64_bf16 v[152:155], v[24:27], a[208:215], v131\n\t" "v_min_u32 v164, -2, v164\n\t" \
   "buffer_store_dwordx2 v[164:165], v132, %[rs], s49 offen sc1\n\t" "v_smfmac_f32_16x16x64_bf16 v[152:155], v[28:31], a[216:223], v131\n\t" \
   "v_smfmac_f32_16x16x64_bf16 v[156:159], v[20:23], a[232:239], v131\n\t" "v_cvt_pk_bf16_f32 v166, v148, v149\n\t" \
   "v_cvt_pk_bf16_f32 v167, v150, v151\n\t" "v_smfmac_f32_16x16x64_bf16 v[156:159], v[24:27], a[240:247], v131\n\t" "v_min_u32 v166, -2, v166\n\t" \
   "buffer_store_dwordx2 v[166:167], v132, %[rs], s50 offen sc1\n\t" "v_smfmac_f32_16x16x64_bf16 v[156:159], v[28:31], a[248:255], v131\n\t" \
   "v_smfmac_f32_16x16x64_bf16 v[160:163], v[20:23], v[232:239], v131\n\t" "v_cvt_pk_bf16_f32 v168, v152, v153\n\t" \
   "v_cvt_pk_bf16_f32 v169, v154, v155\n\t" "v_smfmac_f32_16x16x64_bf16 v[160:163], v[24:27], v[240:247], v131\n\t" "v_min_u32 v168, -2, v168\n\t" \
   "buffer_store_dwordx2 v[168:169], v132, %[rs], s51 offen sc1\n\t" "v_smfmac_f32_16x16x64_bf16 v[160:163], v[28:31], v[248:255], v131\n\t" \
   "v_cvt_pk_bf16_f32 v170, v156, v157\n\t" "v_cvt_pk_bf16_f32 v171, v158, v159\n\t" "v_min_u32 v170, -2, v170\n\t" \
   "buffer_store_dwordx2 v[170:171], v132, %[rs], s52 offen sc1\n\t" "s_nop 3\n\t" "v_cvt_pk_bf16_f32 v172, v160, v161\n\t" \
   "v_cvt_pk_bf16_f32 v173, v162, v163\n\t" "v_min_u32 v172, -2, v172\n\t" "buffer_store_dwordx2 v[172:173], v132, %[rs], s53 offen sc1\n\t" RSTAMP(2) \
   "s_branch 40b\n\t" "70:\n\t" "s_mov_b32 s71, 0\n\t" "s_lshr_b64 s[72:73], %[sel], s71\n\t" "s_lshl_b32 s45, s71, 19\n\t" "s_and_b32 s72, s72, 1\n\t" \
   "s_add_u32 s45, s45, %[pool]\n\t" "s_lshl_b32 s72, s72, 13\n\t" "s_add_u32 s45, s45, s72\n\t" "v_mov_b32 v76, -1\n\t" "v_mov_b32 v77, -1\n\t" \
   "v_mov_b32 v78, -1\n\t" "v_mov_b32 v79, -1\n\t" "s_waitcnt vmcnt(0)\n\t" "buffer_store_dwordx4 v[76:79], v133, %[rs], s45 offen sc1\n\t" \
   "buffer_store_dwordx4 v[76:79], v133, %[rs], s45 offen offset:1024 sc1\n\t" "s_waitcnt vmcnt(0)\n\t" "s_branch 71b\n\t" "63:\n\t" \
   "s_cmp_lg_u32 s64, 0\n\t" "s_cbranch_scc1 64f\n\t" "s_sub_u32 s69, s56, 0x10000\n\t" "s_and_b32 s69, s69, -2\n\t" "s_branch 50b\n\t" "64:\n\t" \
   "s_mov_b32 s69, s48\n\t" "s_cmp_lt_u32 s64, 3\n\t" "s_cselect_b32 s69, -1, s69\n\t" "s_cmp_lg_u32 s69, -1\n\t" "s_cbranch_scc1 50b\n\t" \
   "s_add_u32 s76, s76, 0x1000000\n\t" "s_mov_b32 s73, 0\n\t" "65:\n\t" "s_add_u32 s73, s73, 1\n\t" "s_sleep 6\n\t" "s_and_b32 s72, s73, 3\n\t" \
   "s_add_u32 s72, s72, 8\n\t" "s_sub_u32 s72, s72, %[xs]\n\t" "s_lshl_b32 s72, s72, 7\n\t" "s_add_u32 s72, s72, s66\n\t" \
   "buffer_load_dword v108, v139, %[rs], s72 offen sc1\n\t" "s_waitcnt vmcnt(0)\n\t" "v_readfirstlane_b32 s69, v108\n\t" \
   "s_cmp_gt_u32 s73, 0x10000\n\t" "s_cbranch_scc1 50b\n\t" "s_cmp_eq_u32 s69, -1\n\t" "s_cbranch_scc1 65b\n\t" "s_branch 50b\n\t" "8:\n\t" \
   "s_nop 7\n\t" "s_waitcnt lgkmcnt(0)\n\t" "s_add_u32 s70, s70, 1\n\t" "s_cmp_gt_u32 s70, 2\n\t" "s_cbranch_scc1 81f\n\t" "s_lshl_b32 s71, s70, 3\n\t" \
   "s_lshl_b32 s71, 1, s71\n\t" "s_add_u32 s76, s76, s71\n\t" "81:\n\t" "v_mov_b32 v48, 0\n\t" "v_mov_b32 v49, 0\n\t" "v_mov_b32 v50, 0\n\t" \
   "v_mov_b32 v51, 0\n\t" "v_mov_b32 v52, 0\n\t" "v_mov_b32 v53, 0\n\t" "v_mov_b32 v54, 0\n\t" "v_mov_b32 v55, 0\n\t" "s_add_u32 s71, s70, 1\n\t" \
   "s_min_u32 s71, s71, 7\n\t" "s_lshr_b32 s67, s71, 1\n\t" "s_add_u32 s67, s67, %[xs]\n\t" "s_and_b32 s67, s67, 3\n\t" "s_lshl_b32 s67, s67, 11\n\t" \
   "s_and_b32 s72, s71, 1\n\t" "s_lshl_b32 s72, s72, 14\n\t" "s_add_u32 s67, s67, s72\n\t" "s_add_u32 s67, s67, s66\n\t" "s_cmp_lt_u32 s70, 6\n\t" \
   "s_cbranch_scc1 20b\n\t" "s_sleep 3\n\t" "s_cmp_lt_u32 s70, 0x20000\n\t" "s_cbranch_scc1 20b\n\t" "s_mov_b32 s67, %[esc]\n\t" "s_branch 20b\n\t" \
   "90:\n\t" "s_waitcnt vmcnt(0) lgkmcnt(0)"
#define PV_CLOB "v16", "v17", "v18", "v19", "v20", "v21", "v22", "v23", "v24", "v25", "v26", "v27", "v28", "v29", "v30", "v31", "v48", "v49", "v50", "v51", "v52", "v53", "v54", "v55", "v56", "v57", "v58", "v59", "v60", "v61", "v62", "v63", "v64", "v65", "v66", "v67", "v68", "v69", "v70", "v71", "v72", "v73", "v74", "v75", "v76", "v77", "v78", "v79", "v80", "v81", "v82", "v83", "v84", "v85", "v86", "v87", "v88", "v89", "v90", "v91", "v92", "v93", "v94", "v95", "v96", "v97", "v98", "v99", "v100", "v101", "v102", "v103", "v104", "v105", "v106", "v107", "v108", "v109", "v110", "v111", "v144", "v145", "v146", "v147", "v148", "v149", "v150", "v151", "v152", "v153", "v154", "v155", "v156", "v157", "v158", "v159", "v160", "v161", "v162", "v163", "v164", "v165", "v166", "v167", "v168", "v169", "v170", "v171", "v172", "v173", "v174", "v175", "s60", "s61", "s62", "s63", "s64", "s65", "s66", "s67", "s68", "s69", "s70", "s71", "s72", "s73", "s74", "s75", "s45", "s46", "s47", "s48", "s49", "s50", "s51", "s52", "s53", "s54"
#define WAIT_ASM \
   "11:\n\t" "s_memrealtime s[60:61]\n\t" "s_memtime s[62:63]\n\t" "s_waitcnt lgkmcnt(0)\n\t" "s_sub_u32 s74, s62, s57\n\t" \
   "s_mul_i32 s74, s74, s58\n\t" "s_lshr_b32 s74, s74, 12\n\t" "s_add_u32 s74, s74, s56\n\t" "s_lshl_b32 s60, s60, 3\n\t" "s_sub_u32 s75, s74, s60\n\t" \
   "s_cmp_lt_u32 s75, 32\n\t" "s_cbranch_scc1 12f\n\t" "s_cmp_lt_i32 s75, 0\n\t" "s_cselect_b32 s61, 0, 31\n\t" "s_add_u32 s74, s60, s61\n\t" \
   "s_sub_i32 s75, s61, s75\n\t" "s_add_i32 s75, s75, 1\n\t" "s_ashr_i32 s75, s75, 1\n\t" "s_min_i32 s75, s75, 8\n\t" "s_max_i32 s75, s75, -8\n\t" \
   "s_mov_b32 s56, s74\n\t" "s_mov_b32 s57, s62\n\t" "s_add_u32 s58, s58, s75\n\t" "12:\n\t" "s_sub_i32 s74, %[ta], s74\n\t" \
   "s_cmp_le_i32 s74, 150\n\t" "s_cbranch_scc1 13f\n\t" "s_cmp_lt_i32 s74, 260\n\t" "s_cbranch_scc1 11b\n\t" "s_sleep 2\n\t" \
   "s_cmp_lt_i32 s74, 32000\n\t" "s_cbranch_scc1 11b\n\t" "s_mov_b32 s74, 0\n\t" "13:\n\t" "s_sub_i32 s74, s74, 56\n\t" "s_max_i32 s74, s74, 0\n\t" \
   "s_mul_i32 s74, s74, 23\n\t" "s_lshr_b32 s74, s74, 5\n\t" "s_and_b32 s74, s74, -4\n\t" "s_getpc_b64 s[60:61]\n\t" "14:\n\t" \
   "s_sub_u32 s74, 15f-14b, s74\n\t" "s_add_u32 s60, s60, s74\n\t" "s_addc_u32 s61, s61, 0\n\t" "s_setpc_b64 s[60:61]\n\t" "s_nop 3\n\t" "s_nop 3\n\t" \
   "s_nop 3\n\t" "s_nop 3\n\t" "s_nop 3\n\t" "s_nop 3\n\t" "s_nop 3\n\t" "s_nop 3\n\t" "s_nop 3\n\t" "s_nop 3\n\t" "s_nop 3\n\t" "s_nop 3\n\t" \
   "s_nop 3\n\t" "s_nop 3\n\t" "s_nop 3\n\t" "s_nop 3\n\t" "s_nop 3\n\t" "s_nop 3\n\t" "s_nop 3\n\t" "s_nop 3\n\t" "s_nop 3\n\t" "s_nop 3\n\t" \
   "s_nop 3\n\t" "s_nop 3\n\t" "15:\n\t" ""
#define WAIT_CLOB "s60", "s61", "s62", "s63", "s74", "s75"
#define FIN_ASM \
   "s_mov_b32 s40, 0\n\t" "s_mov_b32 s76, 0\n\t" "s_mov_b32 s77, 0\n\t" "s_sub_u32 s45, s56, 0x10000\n\t" "v_mov_b32 v13, -2\n\t" \
   "v_mov_b32 v11, -1\n\t" "30:\n\t" "s_lshr_b64 s[72:73], %[sel], s40\n\t" "s_lshl_b32 s41, s40, 19\n\t" "s_and_b32 s72, s72, 1\n\t" \
   "s_add_u32 s41, s41, %[pool]\n\t" "s_lshl_b32 s72, s72, 13\n\t" "s_add_u32 s41, s41, s72\n\t" "s_lshr_b64 s[72:73], %[s3], s40\n\t" \
   "s_and_b32 s47, s72, 1\n\t" "s_lshl_b32 s47, s47, 14\n\t" "s_mul_i32 s49, s40, 1310720\n\t" "s_add_u32 s49, s49, %[su]\n\t" \
   "s_lshr_b64 s[72:73], %[ysel], s40\n\t" "s_mul_i32 s73, s40, 43\n\t" "s_mul_i32 s50, s40, 80\n\t" "s_and_b32 s72, s72, 1\n\t" \
   "s_lshr_b32 s73, s73, 7\n\t" "s_add_u32 s50, s50, s72\n\t" "s_lshl4_add_u32 s50, s73, s50\n\t" "s_lshl_b32 s50, s50, 13\n\t" \
   "s_add_u32 s50, s50, %[yb]\n\t" "s_add_u32 s51, s50, 4096\n\t" "s_lshl_b32 s52, s40, 12\n\t" "s_add_u32 s52, s52, %[uf]\n\t" \
   "s_add_u32 s69, s45, %[tu]\n\t" "v_max3_u32 v15, v8, v9, v10\n\t" "v_max_u32 v15, v15, v11\n\t" "v_cmp_eq_u32 vcc, -1, v15\n\t" \
   "s_add_u32 s40, s40, 1\n\t" "s_cbranch_vccnz 95f\n\t" "96:\n\t" "11:\n\t" "s_memrealtime s[60:61]\n\t" "s_memtime s[62:63]\n\t" \
   "s_waitcnt lgkmcnt(0)\n\t" "s_sub_u32 s74, s62, s57\n\t" "s_mul_i32 s74, s74, s58\n\t" "s_lshr_b32 s74, s74, 12\n\t" "s_add_u32 s74, s74, s56\n\t" \
   "s_lshl_b32 s60, s60, 3\n\t" "s_sub_u32 s75, s74, s60\n\t" "s_cmp_lt_u32 s75, 32\n\t" "s_cbranch_scc1 12f\n\t" "s_cmp_lt_i32 s75, 0\n\t" \
   "s_cselect_b32 s61, 0, 31\n\t" "s_add_u32 s74, s60, s61\n\t" "s_sub_i32 s75, s61, s75\n\t" "s_add_i32 s75, s75, 1\n\t" "s_ashr_i32 s75, s75, 1\n\t" \
   "s_min_i32 s75, s75, 8\n\t" "s_max_i32 s75, s75, -8\n\t" "s_mov_b32 s56, s74\n\t" "s_mov_b32 s57, s62\n\t" "s_add_u32 s58, s58, s75\n\t" "12:\n\t" \
   "s_sub_i32 s74, s69, s74\n\t" "s_cmp_le_i32 s74, 150\n\t" "s_cbranch_scc1 13f\n\t" "s_cmp_lt_i32 s74, 260\n\t" "s_cbranch_scc1 11b\n\t" \
   "s_sleep 2\n\t" "s_cmp_lt_i32 s74, 32000\n\t" "s_cbranch_scc1 11b\n\t" "s_mov_b32 s74, 0\n\t" "13:\n\t" "s_sub_i32 s74, s74, 56\n\t" \
   "s_max_i32 s74, s74, 0\n\t" "s_mul_i32 s74, s74, 23\n\t" "s_lshr_b32 s74, s74, 5\n\t" "s_and_b32 s74, s74, -4\n\t" "s_getpc_b64 s[60:61]\n\t" \
   "14:\n\t" "s_sub_u32 s74, 15f-14b, s74\n\t" "s_add_u32 s60, s60, s74\n\t" "s_addc_u32 s61, s61, 0\n\t" "s_setpc_b64 s[60:61]\n\t" "s_nop 3\n\t" \
   "s_nop 3\n\t" "s_nop 3\n\t" "s_nop 3\n\t" "s_nop 3\n\t" "s_nop 3\n\t" "s_nop 3\n\t" "s_nop 3\n\t" "s_nop 3\n\t" "s_nop 3\n\t" "s_nop 3\n\t" \
   "s_nop 3\n\t" "s_nop 3\n\t" "s_nop 3\n\t" "s_nop 3\n\t" "s_nop 3\n\t" "s_nop 3\n\t" "s_nop 3\n\t" "s_nop 3\n\t" "s_nop 3\n\t" "s_nop 3\n\t" \
   "s_nop 3\n\t" "s_nop 3\n\t" "s_nop 3\n\t" "15:\n\t" RSTAMP(4) "buffer_load_dwordx4 a[0:3], v16, %[rs], s49 offen offset:0 sc1\n\t" "s_nop 0\n\t" \
   "buffer_load_dwordx4 a[4:7], v16, %[rs], s49 offen offset:1024 sc1\n\t" "s_nop 0\n\t" \
   "buffer_load_dwordx4 a[8:11], v16, %[rs], s49 offen offset:2048 sc1\n\t" "s_nop 0\n\t" \
   "buffer_load_dwordx4 a[12:15], v16, %[rs], s49 offen offset:3072 sc1\n\t" "s_nop 0\n\t" "s_add_u32 s49, s49, 4096\n\t" \
   "buffer_load_dwordx4 a[16:19], v16, %[rs], s49 offen offset:0 sc1\n\t" "s_nop 0\n\t" \
   "buffer_load_dwordx4 a[20:23], v16, %[rs], s49 offen offset:1024 sc1\n\t" "s_nop 0\n\t" \
   "buffer_load_dwordx4 a[24:27], v16, %[rs], s49 offen offset:2048 sc1\n\t" "s_nop 0\n\t" \
   "buffer_load_dwordx4 a[28:31], v16, %[rs], s49 offen offset:3072 sc1\n\t" "s_nop 0\n\t" "s_add_u32 s49, s49, 4096\n\t" \
   "buffer_load_dwordx4 a[32:35], v16, %[rs], s49 offen offset:0 sc1\n\t" "s_nop 0\n\t" \
   "buffer_load_dwordx4 a[36:39], v16, %[rs], s49 offen offset:1024 sc1\n\t" "s_nop 0\n\t" \
   "buffer_load_dwordx4 a[40:43], v16, %[rs], s49 offen offset:2048 sc1\n\t" "s_nop 0\n\t" \
   "buffer_load_dwordx4 a[44:47], v16, %[rs], s49 offen offset:3072 sc1\n\t" "s_nop 0\n\t" "s_add_u32 s49, s49, 4096\n\t" \
   "buffer_load_dwordx4 a[48:51], v16, %[rs], s49 offen offset:0 sc1\n\t" "s_nop 0\n\t" \
   "buffer_load_dwordx4 a[52:55], v16, %[rs], s49 offen offset:1024 sc1\n\t" "s_nop 0\n\t" \
   "buffer_load_dwordx4 a[56:59], v16, %[rs], s49 offen offset:2048 sc1\n\t" "s_nop 0\n\t" \
   "buffer_load_dwordx4 a[60:63], v16, %[rs], s49 offen offset:3072 sc1\n\t" "s_nop 0\n\t" "s_add_u32 s49, s49, 4096\n\t" \
   "buffer_load_dwordx4 a[64:67], v16, %[rs], s49 offen offset:0 sc1\n\t" "s_nop 0\n\t" \
   "buffer_load_dwordx4 a[68:71], v16, %[rs], s49 offen offset:1024 sc1\n\t" "s_nop 0\n\t" \
   "buffer_load_dwordx4 a[72:75], v16, %[rs], s49 offen offset:2048 sc1\n\t" "s_nop 0\n\t" \
   "buffer_load_dwordx4 a[76:79], v16, %[rs], s49 offen offset:3072 sc1\n\t" "s_nop 0\n\t" "s_add_u32 s49, s49, 4096\n\t" \
   "buffer_load_dwordx4 a[80:83], v16, %[rs], s49 offen offset:0 sc1\n\t" "s_nop 0\n\t" \
   "buffer_load_dwordx4 a[84:87], v16, %[rs], s49 offen offset:1024 sc1\n\t" "s_nop 0\n\t" \
   "buffer_load_dwordx4 a[88:91], v16, %[rs], s49 offen offset:2048 sc1\n\t" "s_nop 0\n\t" \
   "buffer_load_dwordx4 a[92:95], v16, %[rs], s49 offen offset:3072 sc1\n\t" "s_nop 0\n\t" "s_add_u32 s49, s49, 4096\n\t" \
   "buffer_load_dwordx4 a[96:99], v16, %[rs], s49 offen offset:0 sc1\n\t" "s_nop 0\n\t" \
   "buffer_load_dwordx4 a[100:103], v16, %[rs], s49 offen offset:1024 sc1\n\t" "s_nop 0\n\t" \
   "buffer_load_dwordx4 a[104:107], v16, %[rs], s49 offen offset:2048 sc1\n\t" "s_nop 0\n\t" \
   "buffer_load_dwordx4 a[108:111], v16, %[rs], s49 offen offset:3072 sc1\n\t" "s_nop 0\n\t" "s_add_u32 s49, s49, 4096\n\t" \
   "buffer_load_dwordx4 a[112:115], v16, %[rs], s49 offen offset:0 sc1\n\t" "s_nop 0\n\t" \
   "buffer_load_dwordx4 a[116:119], v16, %[rs], s49 offen offset:1024 sc1\n\t" "s_nop 0\n\t" \
   "buffer_load_dwordx4 a[120:123], v16, %[rs], s49 offen offset:2048 sc1\n\t" "s_nop 0\n\t" \
   "buffer_load_dwordx4 a[124:127], v16, %[rs], s49 offen offset:3072 sc1\n\t" "s_nop 0\n\t" "s_add_u32 s52, s52, 4096\n\t" \
   "buffer_load_dwordx4 v[8:11], v19, %[rs], s52 offen sc1\n\t" RSTAMP(0) "s_mov_b32 s46, 0\n\t" "s_xor_b32 s64, s47, %[lad0]\n\t" \
   "s_add_u32 s64, s64, s41\n\t" "v_xor_b32 v14, s47, v20\n\t" "v_xor_b32 v15, 0x4000, v14\n\t" "s_add_u32 s65, s64, 32768\n\t" \
   "s_add_u32 s66, s64, 65536\n\t" "s_add_u32 s67, s64, 98304\n\t" "s_add_u32 s68, s64, 131072\n\t" "s_add_u32 s69, s64, 163840\n\t" \
   "s_add_u32 s70, s64, 196608\n\t" "s_add_u32 s71, s64, 229376\n\t" "s_add_u32 s72, s64, 262144\n\t" "s_add_u32 s73, s64, 294912\n\t" \
   "s_add_u32 s53, s41, 4096\n\t" "v_mov_b32 v28, 0\n\t" "v_mov_b32 v29, 0\n\t" "v_mov_b32 v30, 0\n\t" "v_mov_b32 v31, 0\n\t" "19:\n\t" \
   "ds_read_b128 v[24:27], v23\n\t" "s_waitcnt lgkmcnt(0)\n\t" "v_readfirstlane_b32 s74, v26\n\t" "v_readfirstlane_b32 s44, v24\n\t" \
   "v_readfirstlane_b32 s45, v25\n\t" "s_sub_u32 s48, s44, 70\n\t" "s_cmp_lt_u32 s74, s40\n\t" "s_cbranch_scc1 18f\n\t" RSTAMP(7) "21:\n\t" \
   "s_memrealtime s[60:61]\n\t" "s_memtime s[62:63]\n\t" "s_waitcnt lgkmcnt(0)\n\t" "s_sub_u32 s74, s62, s57\n\t" "s_mul_i32 s74, s74, s58\n\t" \
   "s_lshr_b32 s74, s74, 12\n\t" "s_add_u32 s74, s74, s56\n\t" "s_lshl_b32 s60, s60, 3\n\t" "s_sub_u32 s75, s74, s60\n\t" "s_cmp_lt_u32 s75, 32\n\t" \
   "s_cbranch_scc1 22f\n\t" "s_cmp_lt_i32 s75, 0\n\t" "s_cselect_b32 s61, 0, 31\n\t" "s_add_u32 s74, s60, s61\n\t" "s_sub_i32 s75, s61, s75\n\t" \
   "s_add_i32 s75, s75, 1\n\t" "s_ashr_i32 s75, s75, 1\n\t" "s_min_i32 s75, s75, 8\n\t" "s_max_i32 s75, s75, -8\n\t" "s_mov_b32 s56, s74\n\t" \
   "s_mov_b32 s57, s62\n\t" "s_add_u32 s58, s58, s75\n\t" "22:\n\t" "s_sub_i32 s74, s48, s74\n\t" "s_cmp_le_i32 s74, 150\n\t" "s_cbranch_scc1 23f\n\t" \
   "s_cmp_lt_i32 s74, 260\n\t" "s_cbranch_scc1 21b\n\t" "s_sleep 2\n\t" "s_cmp_lt_i32 s74, 32000\n\t" "s_cbranch_scc1 21b\n\t" "s_mov_b32 s74, 0\n\t" \
   "23:\n\t" "s_sub_i32 s74, s74, 56\n\t" "s_max_i32 s74, s74, 0\n\t" "s_mul_i32 s74, s74, 23\n\t" "s_lshr_b32 s74, s74, 5\n\t" \
   "s_and_b32 s74, s74, -4\n\t" "s_getpc_b64 s[60:61]\n\t" "24:\n\t" "s_sub_u32 s74, 25f-24b, s74\n\t" "s_add_u32 s60, s60, s74\n\t" \
   "s_addc_u32 s61, s61, 0\n\t" "s_setpc_b64 s[60:61]\n\t" "s_nop 3\n\t" "s_nop 3\n\t" "s_nop 3\n\t" "s_nop 3\n\t" "s_nop 3\n\t" "s_nop 3\n\t" \
   "s_nop 3\n\t" "s_nop 3\n\t" "s_nop 3\n\t" "s_nop 3\n\t" "s_nop 3\n\t" "s_nop 3\n\t" "s_nop 3\n\t" "s_nop 3\n\t" "s_nop 3\n\t" "s_nop 3\n\t" \
   "s_nop 3\n\t" "s_nop 3\n\t" "s_nop 3\n\t" "s_nop 3\n\t" "s_nop 3\n\t" "s_nop 3\n\t" "s_nop 3\n\t" "s_nop 3\n\t" "25:\n\t" \
   "buffer_load_dwordx4 v[32:35], v16, %[rs], s50 offen offset:0 sc1\n\t" "buffer_load_dwordx4 v[36:39], v16, %[rs], s50 offen offset:1024 sc1\n\t" \
   "buffer_load_dwordx4 v[40:43], v16, %[rs], s50 offen offset:2048 sc1\n\t" "buffer_load_dwordx4 v[44:47], v16, %[rs], s50 offen offset:3072 sc1\n\t" \
   "buffer_load_dwordx4 v[48:51], v16, %[rs], s51 offen offset:0 sc1\n\t" "buffer_load_dwordx4 v[52:55], v16, %[rs], s51 offen offset:1024 sc1\n\t" \
   "buffer_load_dwordx4 v[56:59], v16, %[rs], s51 offen offset:2048 sc1\n\t" "buffer_load_dwordx4 v[60:63], v16, %[rs], s51 offen offset:3072 sc1\n\t" \
   "s_nop 3\n\t" "s_nop 3\n\t" "s_nop 3\n\t" "s_nop 3\n\t" "s_nop 3\n\t" "s_nop 3\n\t" "s_nop 3\n\t" "s_nop 3\n\t" RSTAMP(6) \
   "buffer_load_dwordx4 v[64:67], v16, %[rs], s64 offen offset:0 sc1\n\t" "buffer_load_dwordx4 v[68:71], v16, %[rs], s64 offen offset:1024 sc1\n\t" \
   "buffer_load_dwordx4 v[72:75], v16, %[rs], s65 offen offset:0 sc1\n\t" "buffer_load_dwordx4 v[76:79], v16, %[rs], s65 offen offset:1024 sc1\n\t" \
   "buffer_load_dwordx4 v[80:83], v16, %[rs], s66 offen offset:0 sc1\n\t" "buffer_load_dwordx4 v[84:87], v16, %[rs], s66 offen offset:1024 sc1\n\t" \
   "buffer_load_dwordx4 v[88:91], v16, %[rs], s67 offen offset:0 sc1\n\t" "buffer_load_dwordx4 v[92:95], v16, %[rs], s67 offen offset:1024 sc1\n\t" \
   "buffer_load_dwordx4 v[96:99], v16, %[rs], s68 offen offset:0 sc1\n\t" "buffer_load_dwordx4 v[100:103], v16, %[rs], s68 offen offset:1024 sc1\n\t" \
   "buffer_load_dwordx4 v[104:107], v16, %[rs], s69 offen offset:0 sc1\n\t" "buffer_load_dwordx4 v[108:111], v16, %[rs], s69 offen offset:1024 sc1\n\t" \
   "buffer_load_dwordx4 v[112:115], v16, %[rs], s70 offen offset:0 sc1\n\t" "buffer_load_dwordx4 v[116:119], v16, %[rs], s70 offen offset:1024 sc1\n\t" \
   "buffer_load_dwordx4 v[120:123], v16, %[rs], s71 offen offset:0 sc1\n\t" "buffer_load_dwordx4 v[124:127], v16, %[rs], s71 offen offset:1024 sc1\n\t" \
   "buffer_load_dwordx2 v[128:129], v17, %[rs], s72 offen offset:0 sc1\n\t" "buffer_load_dwordx2 v[130:131], v17, %[rs], s72 offen offset:128 sc1\n\t" \
   "buffer_load_dwordx2 v[132:133], v17, %[rs], s72 offen offset:1024 sc1\n\t" \
   "buffer_load_dwordx2 v[134:135], v17, %[rs], s72 offen offset:1152 sc1\n\t" "buffer_load_dwordx2 v[136:137], v17, %[rs], s73 offen offset:0 sc1\n\t" \
   "buffer_load_dwordx2 v[138:139], v17, %[rs], s73 offen offset:128 sc1\n\t" \
   "buffer_load_dwordx2 v[140:141], v17, %[rs], s73 offen offset:1024 sc1\n\t" \
   "buffer_load_dwordx2 v[142:143], v17, %[rs], s73 offen offset:1152 sc1\n\t" YSTAMP "s_waitcnt vmcnt(23)\n\t" RSTAMP(1) \
   "v_smfmac_f32_16x16x64_bf16 v[28:31], v[64:67], a[0:7], v22\n\t" "s_waitcnt vmcnt(22)\n\t" \
   "v_smfmac_f32_16x16x64_bf16 v[28:31], v[68:71], a[8:15], v22\n\t" "s_waitcnt vmcnt(21)\n\t" \
   "v_smfmac_f32_16x16x64_bf16 v[28:31], v[72:75], a[16:23], v22\n\t" "s_waitcnt vmcnt(20)\n\t" \
   "v_smfmac_f32_16x16x64_bf16 v[28:31], v[76:79], a[24:31], v22\n\t" "s_waitcnt vmcnt(19)\n\t" \
   "v_smfmac_f32_16x16x64_bf16 v[28:31], v[80:83], a[32:39], v22\n\t" "s_waitcnt vmcnt(18)\n\t" \
   "v_smfmac_f32_16x16x64_bf16 v[28:31], v[84:87], a[40:47], v22\n\t" "s_waitcnt vmcnt(17)\n\t" \
   "v_smfmac_f32_16x16x64_bf16 v[28:31], v[88:91], a[48:55], v22\n\t" "s_waitcnt vmcnt(16)\n\t" \
   "v_smfmac_f32_16x16x64_bf16 v[28:31], v[92:95], a[56:63], v22\n\t" "s_waitcnt vmcnt(15)\n\t" \
   "v_smfmac_f32_16x16x64_bf16 v[28:31], v[96:99], a[64:71], v22\n\t" "s_waitcnt vmcnt(14)\n\t" \
   "v_smfmac_f32_16x16x64_bf16 v[28:31], v[100:103], a[72:79], v22\n\t" "s_waitcnt vmcnt(13)\n\t" \
   "v_smfmac_f32_16x16x64_bf16 v[28:31], v[104:107], a[80:87], v22\n\t" "s_waitcnt vmcnt(12)\n\t" \
   "v_smfmac_f32_16x16x64_bf16 v[28:31], v[108:111], a[88:95], v22\n\t" "s_waitcnt vmcnt(11)\n\t" \
   "v_smfmac_f32_16x16x64_bf16 v[28:31], v[112:115], a[96:103], v22\n\t" "s_waitcnt vmcnt(10)\n\t" \
   "v_smfmac_f32_16x16x64_bf16 v[28:31], v[116:119], a[104:111], v22\n\t" "s_waitcnt vmcnt(9)\n\t" \
   "v_smfmac_f32_16x16x64_bf16 v[28:31], v[120:123], a[112:119], v22\n\t" "s_waitcnt vmcnt(8)\n\t" \
   "v_smfmac_f32_16x16x64_bf16 v[28:31], v[124:127], a[120:127], v22\n\t" "s_waitcnt vmcnt(6)\n\t" \
   "v_smfmac_f32_16x16x64_bf16 v[28:31], v[128:131], v[32:39], v22\n\t" "s_waitcnt vmcnt(4)\n\t" \
   "v_smfmac_f32_16x16x64_bf16 v[28:31], v[132:135], v[40:47], v22\n\t" "s_waitcnt vmcnt(2)\n\t" \
   "v_smfmac_f32_16x16x64_bf16 v[28:31], v[136:139], v[48:55], v22\n\t" "s_waitcnt vmcnt(0)\n\t" \
   "v_smfmac_f32_16x16x64_bf16 v[28:31], v[140:143], v[56:63], v22\n\t" "s_nop 7\n\t" "v_add_f32 v28, v28, v29\n\t" "v_add_f32 v30, v30, v31\n\t" \
   "v_cvt_pk_bf16_f32 v24, v28, v30\n\t" "v_cmp_u_f32 vcc, v28, v30\n\t" "s_nop 0\n\t" \
   "v_mov_b32_dpp v25, v24 quad_perm:[1,0,3,2] row_mask:0xf bank_mask:0xf\n\t" "s_cbranch_vccnz 8f\n\t" "2:\n\t" RSTAMP(2) \
   "v_perm_b32 v26, v25, v24, v18\n\t" "s_nop 1\n\t" "v_min_u32_dpp v24, v26, v13 quad_perm:[0,1,0,1] row_mask:0xf bank_mask:0xf\n\t" \
   "v_mov_b32_dpp v25, v26 quad_perm:[2,3,2,3] row_mask:0xf bank_mask:0xf\n\t" "buffer_store_dwordx2 v[24:25], v14, %[rs], s41 offen sc1\n\t" \
   "buffer_store_dwordx2 v[24:25], v14, %[rs], s53 offen sc1\n\t" "s_memtime s[62:63]\n\t" "s_memrealtime s[60:61]\n\t" \
   "buffer_store_dwordx2 v[24:25], v15, %[rs], s41 offen sc1\n\t" "buffer_store_dwordx2 v[24:25], v15, %[rs], s53 offen sc1\n\t" RSTAMP(3) \
   "s_waitcnt lgkmcnt(0)\n\t" "s_sub_u32 s74, s62, s57\n\t" "s_mul_i32 s74, s74, s58\n\t" "s_lshr_b32 s74, s74, 12\n\t" "s_add_u32 s74, s74, s56\n\t" \
   "s_lshl_b32 s60, s60, 3\n\t" "s_sub_u32 s75, s74, s60\n\t" "s_cmp_lt_u32 s75, 32\n\t" "s_cbranch_scc1 38f\n\t" "s_cmp_lt_i32 s75, 0\n\t" \
   "s_cselect_b32 s61, 0, 31\n\t" "s_add_u32 s74, s60, s61\n\t" "s_sub_i32 s75, s61, s75\n\t" "s_add_i32 s75, s75, 1\n\t" "s_ashr_i32 s75, s75, 1\n\t" \
   "s_min_i32 s75, s75, 8\n\t" "s_max_i32 s75, s75, -8\n\t" "s_mov_b32 s56, s74\n\t" "s_mov_b32 s57, s62\n\t" "s_add_u32 s58, s58, s75\n\t" "38:\n\t" \
   "s_min_u32 s75, s46, 1\n\t" "s_and_b32 s74, s74, -4\n\t" "s_or_b32 s74, s74, s75\n\t" "v_mov_b32 v26, s74\n\t" \
   "buffer_store_dword v26, v21, %[rs], s41 offen sc1\n\t" FINTRACE "v_xor_b32 v23, 32, v23\n\t" "s_cmp_lt_u32 s40, 63\n\t" "s_cbranch_scc1 30b\n\t" \
   "s_branch 90f\n\t" "18:\n\t" "s_sleep 2\n\t" "s_branch 19b\n\t" "8:\n\t" "s_mov_b32 s74, 0\n\t" "87:\n\t" "v_max3_u32 v12, v32, v34, v36\n\t" \
   "v_max3_u32 v12, v12, v38, v40\n\t" "v_max3_u32 v12, v12, v42, v44\n\t" "v_max3_u32 v12, v12, v46, v48\n\t" "v_max3_u32 v12, v12, v50, v52\n\t" \
   "v_max3_u32 v12, v12, v54, v56\n\t" "v_max3_u32 v12, v12, v58, v60\n\t" "v_max_u32 v12, v12, v62\n\t" "v_cmp_eq_u32 vcc, -1, v12\n\t" \
   "s_cmp_gt_u32 s74, 0x10000\n\t" "s_cbranch_scc1 86f\n\t" "s_cbranch_vccz 86f\n\t" "s_cmp_eq_u32 s74, 0\n\t" "s_cselect_b32 s75, 0x100, 0\n\t" \
   "s_cbranch_scc1 88f\n\t" "s_sleep 3\n\t" "88:\n\t" "buffer_load_dwordx4 v[32:35], v16, %[rs], s50 offen offset:0 sc1\n\t" \
   "buffer_load_dwordx4 v[36:39], v16, %[rs], s50 offen offset:1024 sc1\n\t" "buffer_load_dwordx4 v[40:43], v16, %[rs], s50 offen offset:2048 sc1\n\t" \
   "buffer_load_dwordx4 v[44:47], v16, %[rs], s50 offen offset:3072 sc1\n\t" "buffer_load_dwordx4 v[48:51], v16, %[rs], s51 offen offset:0 sc1\n\t" \
   "buffer_load_dwordx4 v[52:55], v16, %[rs], s51 offen offset:1024 sc1\n\t" "buffer_load_dwordx4 v[56:59], v16, %[rs], s51 offen offset:2048 sc1\n\t" \
   "buffer_load_dwordx4 v[60:63], v16, %[rs], s51 offen offset:3072 sc1\n\t" "s_add_u32 s76, s76, s75\n\t" "s_add_u32 s74, s74, 1\n\t" \
   "s_waitcnt vmcnt(0)\n\t" "s_branch 87b\n\t" "86:\n\t" "v_mov_b32 v27, 0\n\t" "v_mov_b32 v28, 0\n\t" "v_mov_b32 v29, 0\n\t" "v_mov_b32 v30, 0\n\t" \
   "v_mov_b32 v31, 0\n\t" "v_max3_u32 v27, v27, v64, v66\n\t" "v_max3_u32 v27, v27, v68, v70\n\t" "v_max3_u32 v27, v27, v72, v74\n\t" \
   "v_max3_u32 v27, v27, v76, v78\n\t" "v_max3_u32 v27, v27, v80, v82\n\t" "v_max3_u32 v27, v27, v84, v86\n\t" "v_max3_u32 v27, v27, v88, v90\n\t" \
   "v_max3_u32 v27, v27, v92, v94\n\t" "v_max3_u32 v27, v27, v96, v98\n\t" "v_max3_u32 v27, v27, v100, v102\n\t" "v_max3_u32 v27, v27, v104, v106\n\t" \
   "v_max3_u32 v27, v27, v108, v110\n\t" "v_max3_u32 v27, v27, v112, v114\n\t" "v_max3_u32 v27, v27, v116, v118\n\t" \
   "v_max3_u32 v27, v27, v120, v122\n\t" "v_max3_u32 v27, v27, v124, v126\n\t" "v_max3_u32 v27, v27, v128, v130\n\t" \
   "v_max3_u32 v27, v27, v132, v134\n\t" "v_max3_u32 v27, v27, v136, v138\n\t" "v_max3_u32 v27, v27, v140, v142\n\t" "v_cmp_eq_u32 vcc, -1, v27\n\t" \
   "s_nop 0\n\t" "s_cbranch_vccnz 85f\n\t" "v_smfmac_f32_16x16x64_bf16 v[28:31], v[64:67], a[0:7], v22\n\t" \
   "v_smfmac_f32_16x16x64_bf16 v[28:31], v[68:71], a[8:15], v22\n\t" "v_smfmac_f32_16x16x64_bf16 v[28:31], v[72:75], a[16:23], v22\n\t" \
   "v_smfmac_f32_16x16x64_bf16 v[28:31], v[76:79], a[24:31], v22\n\t" "v_smfmac_f32_16x16x64_bf16 v[28:31], v[80:83], a[32:39], v22\n\t" \
   "v_smfmac_f32_16x16x64_bf16 v[28:31], v[84:87], a[40:47], v22\n\t" "v_smfmac_f32_16x16x64_bf16 v[28:31], v[88:91], a[48:55], v22\n\t" \
   "v_smfmac_f32_16x16x64_bf16 v[28:31], v[92:95], a[56:63], v22\n\t" "v_smfmac_f32_16x16x64_bf16 v[28:31], v[96:99], a[64:71], v22\n\t" \
   "v_smfmac_f32_16x16x64_bf16 v[28:31], v[100:103], a[72:79], v22\n\t" "v_smfmac_f32_16x16x64_bf16 v[28:31], v[104:107], a[80:87], v22\n\t" \
   "v_smfmac_f32_16x16x64_bf16 v[28:31], v[108:111], a[88:95], v22\n\t" "v_smfmac_f32_16x16x64_bf16 v[28:31], v[112:115], a[96:103], v22\n\t" \
   "v_smfmac_f32_16x16x64_bf16 v[28:31], v[116:119], a[104:111], v22\n\t" "v_smfmac_f32_16x16x64_bf16 v[28:31], v[120:123], a[112:119], v22\n\t" \
   "v_smfmac_f32_16x16x64_bf16 v[28:31], v[124:127], a[120:127], v22\n\t" "v_smfmac_f32_16x16x64_bf16 v[28:31], v[128:131], v[32:39], v22\n\t" \
   "v_smfmac_f32_16x16x64_bf16 v[28:31], v[132:135], v[40:47], v22\n\t" "v_smfmac_f32_16x16x64_bf16 v[28:31], v[136:139], v[48:55], v22\n\t" \
   "v_smfmac_f32_16x16x64_bf16 v[28:31], v[140:143], v[56:63], v22\n\t" "84:\n\t" "v_cmp_eq_u32 vcc, -1, v27\n\t" "s_cmp_gt_u32 s46, 0x10000\n\t" \
   "s_cbranch_scc1 83f\n\t" "s_cbranch_vccz 83f\n\t" "85:\n\t" "s_add_u32 s46, s46, 1\n\t" "s_cmp_gt_u32 s46, 2\n\t" "s_cbranch_scc1 81f\n\t" \
   "s_lshl_b32 s74, s46, 3\n\t" "s_lshl_b32 s74, 1, s74\n\t" "s_add_u32 s77, s77, s74\n\t" "81:\n\t" "s_min_u32 s74, s46, 7\n\t" \
   "s_lshr_b32 s64, s74, 1\n\t" "s_add_u32 s64, s64, %[xs]\n\t" "s_and_b32 s64, s64, 3\n\t" "s_lshl_b32 s64, s64, 11\n\t" "s_and_b32 s75, s74, 1\n\t" \
   "s_lshl_b32 s75, s75, 14\n\t" "s_xor_b32 s75, s75, s47\n\t" "s_xor_b32 s75, s75, %[lad0]\n\t" "s_and_b32 s75, s75, 0x4000\n\t" \
   "s_add_u32 s64, s64, s75\n\t" "s_add_u32 s64, s64, s41\n\t" "s_add_u32 s65, s64, 32768\n\t" "s_add_u32 s66, s64, 65536\n\t" \
   "s_add_u32 s67, s64, 98304\n\t" "s_add_u32 s68, s64, 131072\n\t" "s_add_u32 s69, s64, 163840\n\t" "s_add_u32 s70, s64, 196608\n\t" \
   "s_add_u32 s71, s64, 229376\n\t" "s_add_u32 s72, s64, 262144\n\t" "s_add_u32 s73, s64, 294912\n\t" "v_mov_b32 v28, 0\n\t" "v_mov_b32 v29, 0\n\t" \
   "v_mov_b32 v30, 0\n\t" "v_mov_b32 v31, 0\n\t" "v_mov_b32 v27, 0\n\t" "s_cmp_lt_u32 s46, 7\n\t" "s_cbranch_scc1 82f\n\t" "s_sleep 3\n\t" "82:\n\t" \
   RSTAMP(6) "buffer_load_dwordx4 v[64:67], v16, %[rs], s64 offen offset:0 sc1\n\t" \
   "buffer_load_dwordx4 v[68:71], v16, %[rs], s64 offen offset:1024 sc1\n\t" "buffer_load_dwordx4 v[72:75], v16, %[rs], s65 offen offset:0 sc1\n\t" \
   "buffer_load_dwordx4 v[76:79], v16, %[rs], s65 offen offset:1024 sc1\n\t" "buffer_load_dwordx4 v[80:83], v16, %[rs], s66 offen offset:0 sc1\n\t" \
   "buffer_load_dwordx4 v[84:87], v16, %[rs], s66 offen offset:1024 sc1\n\t" "buffer_load_dwordx4 v[88:91], v16, %[rs], s67 offen offset:0 sc1\n\t" \
   "buffer_load_dwordx4 v[92:95], v16, %[rs], s67 offen offset:1024 sc1\n\t" "buffer_load_dwordx4 v[96:99], v16, %[rs], s68 offen offset:0 sc1\n\t" \
   "buffer_load_dwordx4 v[100:103], v16, %[rs], s68 offen offset:1024 sc1\n\t" "buffer_load_dwordx4 v[104:107], v16, %[rs], s69 offen offset:0 sc1\n\t" \
   "buffer_load_dwordx4 v[108:111], v16, %[rs], s69 offen offset:1024 sc1\n\t" "buffer_load_dwordx4 v[112:115], v16, %[rs], s70 offen offset:0 sc1\n\t" \
   "buffer_load_dwordx4 v[116:119], v16, %[rs], s70 offen offset:1024 sc1\n\t" "buffer_load_dwordx4 v[120:123], v16, %[rs], s71 offen offset:0 sc1\n\t" \
   "buffer_load_dwordx4 v[124:127], v16, %[rs], s71 offen offset:1024 sc1\n\t" "buffer_load_dwordx2 v[128:129], v17, %[rs], s72 offen offset:0 sc1\n\t" \
   "buffer_load_dwordx2 v[130:131], v17, %[rs], s72 offen offset:128 sc1\n\t" \
   "buffer_load_dwordx2 v[132:133], v17, %[rs], s72 offen offset:1024 sc1\n\t" \
   "buffer_load_dwordx2 v[134:135], v17, %[rs], s72 offen offset:1152 sc1\n\t" "buffer_load_dwordx2 v[136:137], v17, %[rs], s73 offen offset:0 sc1\n\t" \
   "buffer_load_dwordx2 v[138:139], v17, %[rs], s73 offen offset:128 sc1\n\t" \
   "buffer_load_dwordx2 v[140:141], v17, %[rs], s73 offen offset:1024 sc1\n\t" \
   "buffer_load_dwordx2 v[142:143], v17, %[rs], s73 offen offset:1152 sc1\n\t" "s_waitcnt vmcnt(23)\n\t" \
   "v_smfmac_f32_16x16x64_bf16 v[28:31], v[64:67], a[0:7], v22\n\t" "v_max3_u32 v27, v27, v64, v66\n\t" "s_waitcnt vmcnt(22)\n\t" \
   "v_smfmac_f32_16x16x64_bf16 v[28:31], v[68:71], a[8:15], v22\n\t" "v_max3_u32 v27, v27, v68, v70\n\t" "s_waitcnt vmcnt(21)\n\t" \
   "v_smfmac_f32_16x16x64_bf16 v[28:31], v[72:75], a[16:23], v22\n\t" "v_max3_u32 v27, v27, v72, v74\n\t" "s_waitcnt vmcnt(20)\n\t" \
   "v_smfmac_f32_16x16x64_bf16 v[28:31], v[76:79], a[24:31], v22\n\t" "v_max3_u32 v27, v27, v76, v78\n\t" "s_waitcnt vmcnt(19)\n\t" \
   "v_smfmac_f32_16x16x64_bf16 v[28:31], v[80:83], a[32:39], v22\n\t" "v_max3_u32 v27, v27, v80, v82\n\t" "s_waitcnt vmcnt(18)\n\t" \
   "v_smfmac_f32_16x16x64_bf16 v[28:31], v[84:87], a[40:47], v22\n\t" "v_max3_u32 v27, v27, v84, v86\n\t" "s_waitcnt vmcnt(17)\n\t" \
   "v_smfmac_f32_16x16x64_bf16 v[28:31], v[88:91], a[48:55], v22\n\t" "v_max3_u32 v27, v27, v88, v90\n\t" "s_waitcnt vmcnt(16)\n\t" \
   "v_smfmac_f32_16x16x64_bf16 v[28:31], v[92:95], a[56:63], v22\n\t" "v_max3_u32 v27, v27, v92, v94\n\t" "s_waitcnt vmcnt(15)\n\t" \
   "v_smfmac_f32_16x16x64_bf16 v[28:31], v[96:99], a[64:71], v22\n\t" "v_max3_u32 v27, v27, v96, v98\n\t" "s_waitcnt vmcnt(14)\n\t" \
   "v_smfmac_f32_16x16x64_bf16 v[28:31], v[100:103], a[72:79], v22\n\t" "v_max3_u32 v27, v27, v100, v102\n\t" "s_waitcnt vmcnt(13)\n\t" \
   "v_smfmac_f32_16x16x64_bf16 v[28:31], v[104:107], a[80:87], v22\n\t" "v_max3_u32 v27, v27, v104, v106\n\t" "s_waitcnt vmcnt(12)\n\t" \
   "v_smfmac_f32_16x16x64_bf16 v[28:31], v[108:111], a[88:95], v22\n\t" "v_max3_u32 v27, v27, v108, v110\n\t" "s_waitcnt vmcnt(11)\n\t" \
   "v_smfmac_f32_16x16x64_bf16 v[28:31], v[112:115], a[96:103], v22\n\t" "v_max3_u32 v27, v27, v112, v114\n\t" "s_waitcnt vmcnt(10)\n\t" \
   "v_smfmac_f32_16x16x64_bf16 v[28:31], v[116:119], a[104:111], v22\n\t" "v_max3_u32 v27, v27, v116, v118\n\t" "s_waitcnt vmcnt(9)\n\t" \
   "v_smfmac_f32_16x16x64_bf16 v[28:31], v[120:123], a[112:119], v22\n\t" "v_max3_u32 v27, v27, v120, v122\n\t" "s_waitcnt vmcnt(8)\n\t" \
   "v_smfmac_f32_16x16x64_bf16 v[28:31], v[124:127], a[120:127], v22\n\t" "v_max3_u32 v27, v27, v124, v126\n\t" "s_waitcnt vmcnt(6)\n\t" \
   "v_smfmac_f32_16x16x64_bf16 v[28:31], v[128:131], v[32:39], v22\n\t" "v_max3_u32 v27, v27, v128, v130\n\t" "s_waitcnt vmcnt(4)\n\t" \
   "v_smfmac_f32_16x16x64_bf16 v[28:31], v[132:135], v[40:47], v22\n\t" "v_max3_u32 v27, v27, v132, v134\n\t" "s_waitcnt vmcnt(2)\n\t" \
   "v_smfmac_f32_16x16x64_bf16 v[28:31], v[136:139], v[48:55], v22\n\t" "v_max3_u32 v27, v27, v136, v138\n\t" "s_waitcnt vmcnt(0)\n\t" \
   "v_smfmac_f32_16x16x64_bf16 v[28:31], v[140:143], v[56:63], v22\n\t" "v_max3_u32 v27, v27, v140, v142\n\t" "s_branch 84b\n\t" "83:\n\t" \
   "s_or_b32 s46, s46, 0x10000000\n\t" "s_nop 4\n\t" "v_add_f32 v28, v28, v29\n\t" "v_add_f32 v30, v30, v31\n\t" "v_cvt_pk_bf16_f32 v24, v28, v30\n\t" \
   "s_nop 1\n\t" "v_mov_b32_dpp v25, v24 quad_perm:[1,0,3,2] row_mask:0xf bank_mask:0xf\n\t" "s_branch 2b\n\t" "95:\n\t" "s_mov_b32 s74, 0\n\t" \
   "97:\n\t" "buffer_load_dwordx4 v[8:11], v19, %[rs], s52 offen sc1\n\t" "s_add_u32 s74, s74, 1\n\t" "s_waitcnt vmcnt(0)\n\t" \
   "v_max3_u32 v15, v8, v9, v10\n\t" "v_max_u32 v15, v15, v11\n\t" "v_cmp_eq_u32 vcc, -1, v15\n\t" "s_cmp_gt_u32 s74, 0x10000\n\t" \
   "s_cbranch_scc1 96b\n\t" "s_cbranch_vccz 96b\n\t" "s_sleep 2\n\t" "s_branch 97b\n\t" "90:\n\t" "s_waitcnt vmcnt(0) lgkmcnt(0)"
#define FIN_CLOB "v8", "v9", "v10", "v11", "v12", "v13", "v14", "v15", "v24", "v25", "v26", "v27", "v28", "v29", "v30", "v31", "v32", "v33", "v34", "v35", "v36", "v37", "v38", "v39", "v40", "v41", "v42", "v43", "v44", "v45", "v46", "v47", "v48", "v49", "v50", "v51", "v52", "v53", "v54", "v55", "v56", "v57", "v58", "v59", "v60", "v61", "v62", "v63", "v64", "v65", "v66", "v67", "v68", "v69", "v70", "v71", "v72", "v73", "v74", "v75", "v76", "v77", "v78", "v79", "v80", "v81", "v82", "v83", "v84", "v85", "v86", "v87", "v88", "v89", "v90", "v91", "v92", "v93", "v94", "v95", "v96", "v97", "v98", "v99", "v100", "v101", "v102", "v103", "v104", "v105", "v106", "v107", "v108", "v109", "v110", "v111", "v112", "v113", "v114", "v115", "v116", "v117", "v118", "v119", "v120", "v121", "v122", "v123", "v124", "v125", "v126", "v127", "v128", "v129", "v130", "v131", "v132", "v133", "v134", "v135", "v136", "v137", "v138", "v139", "v140", "v141", "v142", "v143", "s40", "s41", "s42", "s43", "s44", "s45", "s46", "s47", "s48", "s49", "s50", "s51", "s52", "s53", "s60", "s61", "s62", "s63", "s64", "s65", "s66", "s67", "s68", "s69", "s70", "s71", "s72", "s73", "s74", "s75", "a0", "a1", "a2", "a3", "a4", "a5", "a6", "a7", "a8", "a9", "a10", "a11", "a12", "a13", "a14", "a15", "a16", "a17", "a18", "a19", "a20", "a21", "a22", "a23", "a24", "a25", "a26", "a27", "a28", "a29", "a30", "a31", "a32", "a33", "a34", "a35", "a36", "a37", "a38", "a39", "a40", "a41", "a42", "a43", "a44", "a45", "a46", "a47", "a48", "a49", "a50", "a51", "a52", "a53", "a54", "a55", "a56", "a57", "a58", "a59", "a60", "a61", "a62", "a63", "a64", "a65", "a66", "a67", "a68", "a69", "a70", "a71", "a72", "a73", "a74", "a75", "a76", "a77", "a78", "a79", "a80", "a81", "a82", "a83", "a84", "a85", "a86", "a87", "a88", "a89", "a90", "a91", "a92", "a93", "a94", "a95", "a96", "a97", "a98", "a99", "a100", "a101", "a102", "a103", "a104", "a105", "a106", "a107", "a108", "a109", "a110", "a111", "a112", "a113", "a114", "a115", "a116", "a117", "a118", "a119", "a120", "a121", "a122", "a123", "a124", "a125", "a126", "a127"
typedef unsigned v16u __attribute__((ext_vector_type(16)));
typedef unsigned v32u __attribute__((ext_vector_type(32)));
__device__ __forceinline__ void put4(v32u& d, int i, v4u v) { d[4 * i] = v.x; d[4 * i + 1] = v.y; d[4 * i + 2] = v.z; d[4 * i + 3] = v.w; }
#define LDP_ASM \
   "s_mov_b32 s41, 0\n\t" "buffer_load_dwordx4 a[0:3], v16, %[ra], s41 offen\n\t" "s_mov_b32 s41, 131072\n\t" \
   "buffer_load_dwordx4 a[64:67], v16, %[ra], s41 offen\n\t" "s_mov_b32 s41, 524288\n\t" "buffer_load_dwordx4 a[8:11], v16, %[ra], s41 offen\n\t" \
   "s_mov_b32 s41, 655360\n\t" "buffer_load_dwordx4 a[72:75], v16, %[ra], s41 offen\n\t" "s_mov_b32 s41, 1048576\n\t" \
   "buffer_load_dwordx4 a[16:19], v16, %[ra], s41 offen\n\t" "s_mov_b32 s41, 1179648\n\t" "buffer_load_dwordx4 a[80:83], v16, %[ra], s41 offen\n\t" \
   "s_mov_b32 s41, 1572864\n\t" "buffer_load_dwordx4 a[24:27], v16, %[ra], s41 offen\n\t" "s_mov_b32 s41, 1703936\n\t" \
   "buffer_load_dwordx4 a[88:91], v16, %[ra], s41 offen\n\t" "s_mov_b32 s41, 2097152\n\t" "buffer_load_dwordx4 a[32:35], v16, %[ra], s41 offen\n\t" \
   "s_mov_b32 s41, 2228224\n\t" "buffer_load_dwordx4 a[96:99], v16, %[ra], s41 offen\n\t" "s_mov_b32 s41, 2621440\n\t" \
   "buffer_load_dwordx4 a[40:43], v16, %[ra], s41 offen\n\t" "s_mov_b32 s41, 2752512\n\t" "buffer_load_dwordx4 a[104:107], v16, %[ra], s41 offen\n\t" \
   "s_mov_b32 s41, 3145728\n\t" "buffer_load_dwordx4 a[48:51], v16, %[ra], s41 offen\n\t" "s_mov_b32 s41, 3276800\n\t" \
   "buffer_load_dwordx4 a[112:115], v16, %[ra], s41 offen\n\t" "s_mov_b32 s41, 3670016\n\t" "buffer_load_dwordx4 a[56:59], v16, %[ra], s41 offen\n\t" \
   "s_mov_b32 s41, 3801088\n\t" "buffer_load_dwordx4 a[120:123], v16, %[ra], s41 offen\n\t" "s_mov_b32 s41, 4194304\n\t" \
   "buffer_load_dwordx4 v[192:195], v16, %[ra], s41 offen\n\t" "s_mov_b32 s41, 4325376\n\t" "buffer_load_dwordx4 v[208:211], v16, %[ra], s41 offen\n\t" \
   "s_mov_b32 s41, 4718592\n\t" "buffer_load_dwordx4 v[200:203], v16, %[ra], s41 offen\n\t" "s_mov_b32 s41, 4849664\n\t" \
   "buffer_load_dwordx4 v[216:219], v16, %[ra], s41 offen\n\t" "s_mov_b32 s41, 0\n\t" "buffer_load_dwordx4 a[128:131], v16, %[rb], s41 offen\n\t" \
   "s_mov_b32 s41, 131072\n\t" "buffer_load_dwordx4 a[192:195], v16, %[rb], s41 offen\n\t" "s_mov_b32 s41, 524288\n\t" \
   "buffer_load_dwordx4 a[136:139], v16, %[rb], s41 offen\n\t" "s_mov_b32 s41, 655360\n\t" "buffer_load_dwordx4 a[200:203], v16, %[rb], s41 offen\n\t" \
   "s_mov_b32 s41, 1048576\n\t" "buffer_load_dwordx4 a[144:147], v16, %[rb], s41 offen\n\t" "s_mov_b32 s41, 1179648\n\t" \
   "buffer_load_dwordx4 a[208:211], v16, %[rb], s41 offen\n\t" "s_mov_b32 s41, 1572864\n\t" "buffer_load_dwordx4 a[152:155], v16, %[rb], s41 offen\n\t" \
   "s_mov_b32 s41, 1703936\n\t" "buffer_load_dwordx4 a[216:219], v16, %[rb], s41 offen\n\t" "s_mov_b32 s41, 2097152\n\t" \
   "buffer_load_dwordx4 a[160:163], v16, %[rb], s41 offen\n\t" "s_mov_b32 s41, 2228224\n\t" "buffer_load_dwordx4 a[224:227], v16, %[rb], s41 offen\n\t" \
   "s_mov_b32 s41, 2621440\n\t" "buffer_load_dwordx4 a[168:171], v16, %[rb], s41 offen\n\t" "s_mov_b32 s41, 2752512\n\t" \
   "buffer_load_dwordx4 a[232:235], v16, %[rb], s41 offen\n\t" "s_mov_b32 s41, 3145728\n\t" "buffer_load_dwordx4 a[176:179], v16, %[rb], s41 offen\n\t" \
   "s_mov_b32 s41, 3276800\n\t" "buffer_load_dwordx4 a[240:243], v16, %[rb], s41 offen\n\t" "s_mov_b32 s41, 3670016\n\t" \
   "buffer_load_dwordx4 a[184:187], v16, %[rb], s41 offen\n\t" "s_mov_b32 s41, 3801088\n\t" "buffer_load_dwordx4 a[248:251], v16, %[rb], s41 offen\n\t" \
   "s_mov_b32 s41, 4194304\n\t" "buffer_load_dwordx4 v[224:227], v16, %[rb], s41 offen\n\t" "s_mov_b32 s41, 4325376\n\t" \
   "buffer_load_dwordx4 v[240:243], v16, %[rb], s41 offen\n\t" "s_mov_b32 s41, 4718592\n\t" "buffer_load_dwordx4 v[232:235], v16, %[rb], s41 offen\n\t" \
   "s_mov_b32 s41, 4849664\n\t" "buffer_load_dwordx4 v[248:251], v16, %[rb], s41 offen\n\t" "s_mov_b32 s41, 262144\n\t" \
   "buffer_load_dwordx4 a[4:7], v16, %[ra], s41 offen\n\t" "s_mov_b32 s41, 393216\n\t" "buffer_load_dwordx4 a[68:71], v16, %[ra], s41 offen\n\t" \
   "s_mov_b32 s41, 786432\n\t" "buffer_load_dwordx4 a[12:15], v16, %[ra], s41 offen\n\t" "s_mov_b32 s41, 917504\n\t" \
   "buffer_load_dwordx4 a[76:79], v16, %[ra], s41 offen\n\t" "s_mov_b32 s41, 1310720\n\t" "buffer_load_dwordx4 a[20:23], v16, %[ra], s41 offen\n\t" \
   "s_mov_b32 s41, 1441792\n\t" "buffer_load_dwordx4 a[84:87], v16, %[ra], s41 offen\n\t" "s_mov_b32 s41, 1835008\n\t" \
   "buffer_load_dwordx4 a[28:31], v16, %[ra], s41 offen\n\t" "s_mov_b32 s41, 1966080\n\t" "buffer_load_dwordx4 a[92:95], v16, %[ra], s41 offen\n\t" \
   "s_mov_b32 s41, 2359296\n\t" "buffer_load_dwordx4 a[36:39], v16, %[ra], s41 offen\n\t" "s_mov_b32 s41, 2490368\n\t" \
   "buffer_load_dwordx4 a[100:103], v16, %[ra], s41 offen\n\t" "s_mov_b32 s41, 2883584\n\t" "buffer_load_dwordx4 a[44:47], v16, %[ra], s41 offen\n\t" \
   "s_mov_b32 s41, 3014656\n\t" "buffer_load_dwordx4 a[108:111], v16, %[ra], s41 offen\n\t" "s_mov_b32 s41, 3407872\n\t" \
   "buffer_load_dwordx4 a[52:55], v16, %[ra], s41 offen\n\t" "s_mov_b32 s41, 3538944\n\t" "buffer_load_dwordx4 a[116:119], v16, %[ra], s41 offen\n\t" \
   "s_mov_b32 s41, 3932160\n\t" "buffer_load_dwordx4 a[60:63], v16, %[ra], s41 offen\n\t" "s_mov_b32 s41, 4063232\n\t" \
   "buffer_load_dwordx4 a[124:127], v16, %[ra], s41 offen\n\t" "s_mov_b32 s41, 4456448\n\t" "buffer_load_dwordx4 v[196:199], v16, %[ra], s41 offen\n\t" \
   "s_mov_b32 s41, 4587520\n\t" "buffer_load_dwordx4 v[212:215], v16, %[ra], s41 offen\n\t" "s_mov_b32 s41, 4980736\n\t" \
   "buffer_load_dwordx4 v[204:207], v16, %[ra], s41 offen\n\t" "s_mov_b32 s41, 5111808\n\t" "buffer_load_dwordx4 v[220:223], v16, %[ra], s41 offen\n\t" \
   "s_waitcnt vmcnt(59)\n\t" "ds_write_b128 v20, a[0:3] offset:0\n\t" "s_mov_b32 s41, 262144\n\t" \
   "buffer_load_dwordx4 a[132:135], v16, %[rb], s41 offen\n\t" "s_waitcnt vmcnt(59)\n\t" "ds_write_b128 v20, a[64:67] offset:4096\n\t" \
   "s_mov_b32 s41, 393216\n\t" "buffer_load_dwordx4 a[196:199], v16, %[rb], s41 offen\n\t" "s_waitcnt vmcnt(59)\n\t" \
   "ds_write_b128 v20, a[8:11] offset:8192\n\t" "s_mov_b32 s41, 786432\n\t" "buffer_load_dwordx4 a[140:143], v16, %[rb], s41 offen\n\t" \
   "s_waitcnt vmcnt(59)\n\t" "ds_write_b128 v20, a[72:75] offset:12288\n\t" "s_mov_b32 s41, 917504\n\t" \
   "buffer_load_dwordx4 a[204:207], v16, %[rb], s41 offen\n\t" "s_waitcnt vmcnt(59)\n\t" "ds_write_b128 v20, a[16:19] offset:16384\n\t" \
   "s_mov_b32 s41, 1310720\n\t" "buffer_load_dwordx4 a[148:151], v16, %[rb], s41 offen\n\t" "s_waitcnt vmcnt(59)\n\t" \
   "ds_write_b128 v20, a[80:83] offset:20480\n\t" "s_mov_b32 s41, 1441792\n\t" "buffer_load_dwordx4 a[212:215], v16, %[rb], s41 offen\n\t" \
   "s_waitcnt vmcnt(59)\n\t" "ds_write_b128 v20, a[24:27] offset:24576\n\t" "s_mov_b32 s41, 1835008\n\t" \
   "buffer_load_dwordx4 a[156:159], v16, %[rb], s41 offen\n\t" "s_waitcnt vmcnt(59)\n\t" "ds_write_b128 v20, a[88:91] offset:28672\n\t" \
   "s_mov_b32 s41, 1966080\n\t" "buffer_load_dwordx4 a[220:223], v16, %[rb], s41 offen\n\t" "s_waitcnt vmcnt(59)\n\t" \
   "ds_write_b128 v20, a[32:35] offset:32768\n\t" "s_mov_b32 s41, 2359296\n\t" "buffer_load_dwordx4 a[164:167], v16, %[rb], s41 offen\n\t" \
   "s_waitcnt vmcnt(59)\n\t" "ds_write_b128 v20, a[96:99] offset:36864\n\t" "s_mov_b32 s41, 2490368\n\t" \
   "buffer_load_dwordx4 a[228:231], v16, %[rb], s41 offen\n\t" "s_waitcnt vmcnt(59)\n\t" "ds_write_b128 v20, a[40:43] offset:40960\n\t" \
   "s_mov_b32 s41, 2883584\n\t" "buffer_load_dwordx4 a[172:175], v16, %[rb], s41 offen\n\t" "s_waitcnt vmcnt(59)\n\t" \
   "ds_write_b128 v20, a[104:107] offset:45056\n\t" "s_mov_b32 s41, 3014656\n\t" "buffer_load_dwordx4 a[236:239], v16, %[rb], s41 offen\n\t" \
   "s_waitcnt vmcnt(59)\n\t" "ds_write_b128 v20, a[48:51] offset:49152\n\t" "s_mov_b32 s41, 3407872\n\t" \
   "buffer_load_dwordx4 a[180:183], v16, %[rb], s41 offen\n\t" "s_waitcnt vmcnt(59)\n\t" "ds_write_b128 v20, a[112:115] offset:53248\n\t" \
   "s_mov_b32 s41, 3538944\n\t" "buffer_load_dwordx4 a[244:247], v16, %[rb], s41 offen\n\t" "s_waitcnt vmcnt(59)\n\t" \
   "ds_write_b128 v20, a[56:59] offset:57344\n\t" "s_mov_b32 s41, 3932160\n\t" "buffer_load_dwordx4 a[188:191], v16, %[rb], s41 offen\n\t" \
   "s_waitcnt vmcnt(59)\n\t" "ds_write_b128 v20, a[120:123] offset:61440\n\t" "s_mov_b32 s41, 4063232\n\t" \
   "buffer_load_dwordx4 a[252:255], v16, %[rb], s41 offen\n\t" "s_waitcnt vmcnt(59)\n\t" "ds_write_b128 v23, v[192:195] offset:0\n\t" \
   "s_mov_b32 s41, 4456448\n\t" "buffer_load_dwordx4 v[228:231], v16, %[rb], s41 offen\n\t" "s_waitcnt vmcnt(59)\n\t" \
   "ds_write_b128 v23, v[208:211] offset:4096\n\t" "s_mov_b32 s41, 4587520\n\t" "buffer_load_dwordx4 v[244:247], v16, %[rb], s41 offen\n\t" \
   "s_waitcnt vmcnt(59)\n\t" "ds_write_b128 v23, v[200:203] offset:8192\n\t" "s_mov_b32 s41, 4980736\n\t" \
   "buffer_load_dwordx4 v[236:239], v16, %[rb], s41 offen\n\t" "s_waitcnt vmcnt(59)\n\t" "ds_write_b128 v23, v[216:219] offset:12288\n\t" \
   "s_mov_b32 s41, 5111808\n\t" "buffer_load_dwordx4 v[252:255], v16, %[rb], s41 offen\n\t" "s_waitcnt lgkmcnt(0)\n\t" "s_barrier\n\t" \
   "s_lshl_b32 s40, %[p0], 13\n\t" "v_add_u32 v24, s40, v18\n\t" "v_add_u32 v25, s40, v19\n\t" "s_add_u32 s40, s40, 8192\n\t" \
   "s_cmp_eq_u32 s40, 81920\n\t" "s_cselect_b32 s40, 0, s40\n\t" "ds_read_b64_tr_b16 a[0:1], v24 offset:0\n\t" \
   "ds_read_b64_tr_b16 a[2:3], v24 offset:4096\n\t" "ds_read_b64_tr_b16 a[64:65], v25 offset:0\n\t" "ds_read_b64_tr_b16 a[66:67], v25 offset:4096\n\t" \
   "v_add_u32 v24, s40, v18\n\t" "v_add_u32 v25, s40, v19\n\t" "s_add_u32 s40, s40, 8192\n\t" "s_cmp_eq_u32 s40, 81920\n\t" \
   "s_cselect_b32 s40, 0, s40\n\t" "ds_read_b64_tr_b16 a[8:9], v24 offset:0\n\t" "ds_read_b64_tr_b16 a[10:11], v24 offset:4096\n\t" \
   "ds_read_b64_tr_b16 a[72:73], v25 offset:0\n\t" "ds_read_b64_tr_b16 a[74:75], v25 offset:4096\n\t" "v_add_u32 v24, s40, v18\n\t" \
   "v_add_u32 v25, s40, v19\n\t" "s_add_u32 s40, s40, 8192\n\t" "s_cmp_eq_u32 s40, 81920\n\t" "s_cselect_b32 s40, 0, s40\n\t" \
   "ds_read_b64_tr_b16 a[16:17], v24 offset:0\n\t" "ds_read_b64_tr_b16 a[18:19], v24 offset:4096\n\t" "ds_read_b64_tr_b16 a[80:81], v25 offset:0\n\t" \
   "ds_read_b64_tr_b16 a[82:83], v25 offset:4096\n\t" "v_add_u32 v24, s40, v18\n\t" "v_add_u32 v25, s40, v19\n\t" "s_add_u32 s40, s40, 8192\n\t" \
   "s_cmp_eq_u32 s40, 81920\n\t" "s_cselect_b32 s40, 0, s40\n\t" "ds_read_b64_tr_b16 a[24:25], v24 offset:0\n\t" \
   "ds_read_b64_tr_b16 a[26:27], v24 offset:4096\n\t" "ds_read_b64_tr_b16 a[88:89], v25 offset:0\n\t" \
   "ds_read_b64_tr_b16 a[90:91], v25 offset:4096\n\t" "v_add_u32 v24, s40, v18\n\t" "v_add_u32 v25, s40, v19\n\t" "s_add_u32 s40, s40, 8192\n\t" \
   "s_cmp_eq_u32 s40, 81920\n\t" "s_cselect_b32 s40, 0, s40\n\t" "ds_read_b64_tr_b16 a[32:33], v24 offset:0\n\t" \
   "ds_read_b64_tr_b16 a[34:35], v24 offset:4096\n\t" "ds_read_b64_tr_b16 a[96:97], v25 offset:0\n\t" \
   "ds_read_b64_tr_b16 a[98:99], v25 offset:4096\n\t" "v_add_u32 v24, s40, v18\n\t" "v_add_u32 v25, s40, v19\n\t" "s_add_u32 s40, s40, 8192\n\t" \
   "s_cmp_eq_u32 s40, 81920\n\t" "s_cselect_b32 s40, 0, s40\n\t" "ds_read_b64_tr_b16 a[40:41], v24 offset:0\n\t" \
   "ds_read_b64_tr_b16 a[42:43], v24 offset:4096\n\t" "ds_read_b64_tr_b16 a[104:105], v25 offset:0\n\t" \
   "ds_read_b64_tr_b16 a[106:107], v25 offset:4096\n\t" "v_add_u32 v24, s40, v18\n\t" "v_add_u32 v25, s40, v19\n\t" "s_add_u32 s40, s40, 8192\n\t" \
   "s_cmp_eq_u32 s40, 81920\n\t" "s_cselect_b32 s40, 0, s40\n\t" "ds_read_b64_tr_b16 a[48:49], v24 offset:0\n\t" \
   "ds_read_b64_tr_b16 a[50:51], v24 offset:4096\n\t" "ds_read_b64_tr_b16 a[112:113], v25 offset:0\n\t" \
   "ds_read_b64_tr_b16 a[114:115], v25 offset:4096\n\t" "v_add_u32 v24, s40, v18\n\t" "v_add_u32 v25, s40, v19\n\t" "s_add_u32 s40, s40, 8192\n\t" \
   "s_cmp_eq_u32 s40, 81920\n\t" "s_cselect_b32 s40, 0, s40\n\t" "ds_read_b64_tr_b16 a[56:57], v24 offset:0\n\t" \
   "ds_read_b64_tr_b16 a[58:59], v24 offset:4096\n\t" "ds_read_b64_tr_b16 a[120:121], v25 offset:0\n\t" \
   "ds_read_b64_tr_b16 a[122:123], v25 offset:4096\n\t" "v_add_u32 v24, s40, v18\n\t" "v_add_u32 v25, s40, v19\n\t" "s_add_u32 s40, s40, 8192\n\t" \
   "s_cmp_eq_u32 s40, 81920\n\t" "s_cselect_b32 s40, 0, s40\n\t" "ds_read_b64_tr_b16 v[192:193], v24 offset:0\n\t" \
   "ds_read_b64_tr_b16 v[194:195], v24 offset:4096\n\t" "ds_read_b64_tr_b16 v[208:209], v25 offset:0\n\t" \
   "ds_read_b64_tr_b16 v[210:211], v25 offset:4096\n\t" "v_add_u32 v24, s40, v18\n\t" "v_add_u32 v25, s40, v19\n\t" "s_add_u32 s40, s40, 8192\n\t" \
   "s_cmp_eq_u32 s40, 81920\n\t" "s_cselect_b32 s40, 0, s40\n\t" "ds_read_b64_tr_b16 v[200:201], v24 offset:0\n\t" \
   "ds_read_b64_tr_b16 v[202:203], v24 offset:4096\n\t" "ds_read_b64_tr_b16 v[216:217], v25 offset:0\n\t" \
   "ds_read_b64_tr_b16 v[218:219], v25 offset:4096\n\t" "s_waitcnt vmcnt(59)\n\t" "s_waitcnt lgkmcnt(0)\n\t" "s_barrier\n\t" \
   "ds_write_b128 v20, a[128:131] offset:0\n\t" "s_waitcnt vmcnt(58)\n\t" "ds_write_b128 v20, a[192:195] offset:4096\n\t" "s_waitcnt vmcnt(57)\n\t" \
   "ds_write_b128 v20, a[136:139] offset:8192\n\t" "s_waitcnt vmcnt(56)\n\t" "ds_write_b128 v20, a[200:203] offset:12288\n\t" "s_waitcnt vmcnt(55)\n\t" \
   "ds_write_b128 v20, a[144:147] offset:16384\n\t" "s_waitcnt vmcnt(54)\n\t" "ds_write_b128 v20, a[208:211] offset:20480\n\t" \
   "s_waitcnt vmcnt(53)\n\t" "ds_write_b128 v20, a[152:155] offset:24576\n\t" "s_waitcnt vmcnt(52)\n\t" \
   "ds_write_b128 v20, a[216:219] offset:28672\n\t" "s_waitcnt vmcnt(51)\n\t" "ds_write_b128 v20, a[160:163] offset:32768\n\t" \
   "s_waitcnt vmcnt(50)\n\t" "ds_write_b128 v20, a[224:227] offset:36864\n\t" "s_waitcnt vmcnt(49)\n\t" \
   "ds_write_b128 v20, a[168:171] offset:40960\n\t" "s_waitcnt vmcnt(48)\n\t" "ds_write_b128 v20, a[232:235] offset:45056\n\t" \
   "s_waitcnt vmcnt(47)\n\t" "ds_write_b128 v20, a[176:179] offset:49152\n\t" "s_waitcnt vmcnt(46)\n\t" \
   "ds_write_b128 v20, a[240:243] offset:53248\n\t" "s_waitcnt vmcnt(45)\n\t" "ds_write_b128 v20, a[184:187] offset:57344\n\t" \
   "s_waitcnt vmcnt(44)\n\t" "ds_write_b128 v20, a[248:251] offset:61440\n\t" "s_waitcnt vmcnt(43)\n\t" "ds_write_b128 v23, v[224:227] offset:0\n\t" \
   "s_waitcnt vmcnt(42)\n\t" "ds_write_b128 v23, v[240:243] offset:4096\n\t" "s_waitcnt vmcnt(41)\n\t" "ds_write_b128 v23, v[232:235] offset:8192\n\t" \
   "s_waitcnt vmcnt(40)\n\t" "ds_write_b128 v23, v[248:251] offset:12288\n\t" "s_waitcnt lgkmcnt(0)\n\t" "s_barrier\n\t" \
   "s_lshl_b32 s40, %[p0], 13\n\t" "v_add_u32 v24, s40, v18\n\t" "v_add_u32 v25, s40, v19\n\t" "s_add_u32 s40, s40, 8192\n\t" \
   "s_cmp_eq_u32 s40, 81920\n\t" "s_cselect_b32 s40, 0, s40\n\t" "ds_read_b64_tr_b16 a[128:129], v24 offset:0\n\t" \
   "ds_read_b64_tr_b16 a[130:131], v24 offset:4096\n\t" "ds_read_b64_tr_b16 a[192:193], v25 offset:0\n\t" \
   "ds_read_b64_tr_b16 a[194:195], v25 offset:4096\n\t" "v_add_u32 v24, s40, v18\n\t" "v_add_u32 v25, s40, v19\n\t" "s_add_u32 s40, s40, 8192\n\t" \
   "s_cmp_eq_u32 s40, 81920\n\t" "s_cselect_b32 s40, 0, s40\n\t" "ds_read_b64_tr_b16 a[136:137], v24 offset:0\n\t" \
   "ds_read_b64_tr_b16 a[138:139], v24 offset:4096\n\t" "ds_read_b64_tr_b16 a[200:201], v25 offset:0\n\t" \
   "ds_read_b64_tr_b16 a[202:203], v25 offset:4096\n\t" "v_add_u32 v24, s40, v18\n\t" "v_add_u32 v25, s40, v19\n\t" "s_add_u32 s40, s40, 8192\n\t" \
   "s_cmp_eq_u32 s40, 81920\n\t" "s_cselect_b32 s40, 0, s40\n\t" "ds_read_b64_tr_b16 a[144:145], v24 offset:0\n\t" \
   "ds_read_b64_tr_b16 a[146:147], v24 offset:4096\n\t" "ds_read_b64_tr_b16 a[208:209], v25 offset:0\n\t" \
   "ds_read_b64_tr_b16 a[210:211], v25 offset:4096\n\t" "v_add_u32 v24, s40, v18\n\t" "v_add_u32 v25, s40, v19\n\t" "s_add_u32 s40, s40, 8192\n\t" \
   "s_cmp_eq_u32 s40, 81920\n\t" "s_cselect_b32 s40, 0, s40\n\t" "ds_read_b64_tr_b16 a[152:153], v24 offset:0\n\t" \
   "ds_read_b64_tr_b16 a[154:155], v24 offset:4096\n\t" "ds_read_b64_tr_b16 a[216:217], v25 offset:0\n\t" \
   "ds_read_b64_tr_b16 a[218:219], v25 offset:4096\n\t" "v_add_u32 v24, s40, v18\n\t" "v_add_u32 v25, s40, v19\n\t" "s_add_u32 s40, s40, 8192\n\t" \
   "s_cmp_eq_u32 s40, 81920\n\t" "s_cselect_b32 s40, 0, s40\n\t" "ds_read_b64_tr_b16 a[160:161], v24 offset:0\n\t" \
   "ds_read_b64_tr_b16 a[162:163], v24 offset:4096\n\t" "ds_read_b64_tr_b16 a[224:225], v25 offset:0\n\t" \
   "ds_read_b64_tr_b16 a[226:227], v25 offset:4096\n\t" "v_add_u32 v24, s40, v18\n\t" "v_add_u32 v25, s40, v19\n\t" "s_add_u32 s40, s40, 8192\n\t" \
   "s_cmp_eq_u32 s40, 81920\n\t" "s_cselect_b32 s40, 0, s40\n\t" "ds_read_b64_tr_b16 a[168:169], v24 offset:0\n\t" \
   "ds_read_b64_tr_b16 a[170:171], v24 offset:4096\n\t" "ds_read_b64_tr_b16 a[232:233], v25 offset:0\n\t" \
   "ds_read_b64_tr_b16 a[234:235], v25 offset:4096\n\t" "v_add_u32 v24, s40, v18\n\t" "v_add_u32 v25, s40, v19\n\t" "s_add_u32 s40, s40, 8192\n\t" \
   "s_cmp_eq_u32 s40, 81920\n\t" "s_cselect_b32 s40, 0, s40\n\t" "ds_read_b64_tr_b16 a[176:177], v24 offset:0\n\t" \
   "ds_read_b64_tr_b16 a[178:179], v24 offset:4096\n\t" "ds_read_b64_tr_b16 a[240:241], v25 offset:0\n\t" \
   "ds_read_b64_tr_b16 a[242:243], v25 offset:4096\n\t" "v_add_u32 v24, s40, v18\n\t" "v_add_u32 v25, s40, v19\n\t" "s_add_u32 s40, s40, 8192\n\t" \
   "s_cmp_eq_u32 s40, 81920\n\t" "s_cselect_b32 s40, 0, s40\n\t" "ds_read_b64_tr_b16 a[184:185], v24 offset:0\n\t" \
   "ds_read_b64_tr_b16 a[186:187], v24 offset:4096\n\t" "ds_read_b64_tr_b16 a[248:249], v25 offset:0\n\t" \
   "ds_read_b64_tr_b16 a[250:251], v25 offset:4096\n\t" "v_add_u32 v24, s40, v18\n\t" "v_add_u32 v25, s40, v19\n\t" "s_add_u32 s40, s40, 8192\n\t" \
   "s_cmp_eq_u32 s40, 81920\n\t" "s_cselect_b32 s40, 0, s40\n\t" "ds_read_b64_tr_b16 v[224:225], v24 offset:0\n\t" \
   "ds_read_b64_tr_b16 v[226:227], v24 offset:4096\n\t" "ds_read_b64_tr_b16 v[240:241], v25 offset:0\n\t" \
   "ds_read_b64_tr_b16 v[242:243], v25 offset:4096\n\t" "v_add_u32 v24, s40, v18\n\t" "v_add_u32 v25, s40, v19\n\t" "s_add_u32 s40, s40, 8192\n\t" \
   "s_cmp_eq_u32 s40, 81920\n\t" "s_cselect_b32 s40, 0, s40\n\t" "ds_read_b64_tr_b16 v[232:233], v24 offset:0\n\t" \
   "ds_read_b64_tr_b16 v[234:235], v24 offset:4096\n\t" "ds_read_b64_tr_b16 v[248:249], v25 offset:0\n\t" \
   "ds_read_b64_tr_b16 v[250:251], v25 offset:4096\n\t" "s_waitcnt vmcnt(39)\n\t" "s_waitcnt lgkmcnt(0)\n\t" "s_barrier\n\t" \
   "ds_write_b128 v20, a[4:7] offset:0\n\t" "s_waitcnt vmcnt(38)\n\t" "ds_write_b128 v20, a[68:71] offset:4096\n\t" "s_waitcnt vmcnt(37)\n\t" \
   "ds_write_b128 v20, a[12:15] offset:8192\n\t" "s_waitcnt vmcnt(36)\n\t" "ds_write_b128 v20, a[76:79] offset:12288\n\t" "s_waitcnt vmcnt(35)\n\t" \
   "ds_write_b128 v20, a[20:23] offset:16384\n\t" "s_waitcnt vmcnt(34)\n\t" "ds_write_b128 v20, a[84:87] offset:20480\n\t" "s_waitcnt vmcnt(33)\n\t" \
   "ds_write_b128 v20, a[28:31] offset:24576\n\t" "s_waitcnt vmcnt(32)\n\t" "ds_write_b128 v20, a[92:95] offset:28672\n\t" "s_waitcnt vmcnt(31)\n\t" \
   "ds_write_b128 v20, a[36:39] offset:32768\n\t" "s_waitcnt vmcnt(30)\n\t" "ds_write_b128 v20, a[100:103] offset:36864\n\t" "s_waitcnt vmcnt(29)\n\t" \
   "ds_write_b128 v20, a[44:47] offset:40960\n\t" "s_waitcnt vmcnt(28)\n\t" "ds_write_b128 v20, a[108:111] offset:45056\n\t" "s_waitcnt vmcnt(27)\n\t" \
   "ds_write_b128 v20, a[52:55] offset:49152\n\t" "s_waitcnt vmcnt(26)\n\t" "ds_write_b128 v20, a[116:119] offset:53248\n\t" "s_waitcnt vmcnt(25)\n\t" \
   "ds_write_b128 v20, a[60:63] offset:57344\n\t" "s_waitcnt vmcnt(24)\n\t" "ds_write_b128 v20, a[124:127] offset:61440\n\t" "s_waitcnt vmcnt(23)\n\t" \
   "ds_write_b128 v23, v[196:199] offset:0\n\t" "s_waitcnt vmcnt(22)\n\t" "ds_write_b128 v23, v[212:215] offset:4096\n\t" "s_waitcnt vmcnt(21)\n\t" \
   "ds_write_b128 v23, v[204:207] offset:8192\n\t" "s_waitcnt vmcnt(20)\n\t" "ds_write_b128 v23, v[220:223] offset:12288\n\t" \
   "s_waitcnt lgkmcnt(0)\n\t" "s_barrier\n\t" "s_lshl_b32 s40, %[p0], 13\n\t" "v_add_u32 v24, s40, v18\n\t" "v_add_u32 v25, s40, v19\n\t" \
   "s_add_u32 s40, s40, 8192\n\t" "s_cmp_eq_u32 s40, 81920\n\t" "s_cselect_b32 s40, 0, s40\n\t" "ds_read_b64_tr_b16 a[4:5], v24 offset:0\n\t" \
   "ds_read_b64_tr_b16 a[6:7], v24 offset:4096\n\t" "ds_read_b64_tr_b16 a[68:69], v25 offset:0\n\t" "ds_read_b64_tr_b16 a[70:71], v25 offset:4096\n\t" \
   "v_add_u32 v24, s40, v18\n\t" "v_add_u32 v25, s40, v19\n\t" "s_add_u32 s40, s40, 8192\n\t" "s_cmp_eq_u32 s40, 81920\n\t" \
   "s_cselect_b32 s40, 0, s40\n\t" "ds_read_b64_tr_b16 a[12:13], v24 offset:0\n\t" "ds_read_b64_tr_b16 a[14:15], v24 offset:4096\n\t" \
   "ds_read_b64_tr_b16 a[76:77], v25 offset:0\n\t" "ds_read_b64_tr_b16 a[78:79], v25 offset:4096\n\t" "v_add_u32 v24, s40, v18\n\t" \
   "v_add_u32 v25, s40, v19\n\t" "s_add_u32 s40, s40, 8192\n\t" "s_cmp_eq_u32 s40, 81920\n\t" "s_cselect_b32 s40, 0, s40\n\t" \
   "ds_read_b64_tr_b16 a[20:21], v24 offset:0\n\t" "ds_read_b64_tr_b16 a[22:23], v24 offset:4096\n\t" "ds_read_b64_tr_b16 a[84:85], v25 offset:0\n\t" \
   "ds_read_b64_tr_b16 a[86:87], v25 offset:4096\n\t" "v_add_u32 v24, s40, v18\n\t" "v_add_u32 v25, s40, v19\n\t" "s_add_u32 s40, s40, 8192\n\t" \
   "s_cmp_eq_u32 s40, 81920\n\t" "s_cselect_b32 s40, 0, s40\n\t" "ds_read_b64_tr_b16 a[28:29], v24 offset:0\n\t" \
   "ds_read_b64_tr_b16 a[30:31], v24 offset:4096\n\t" "ds_read_b64_tr_b16 a[92:93], v25 offset:0\n\t" \
   "ds_read_b64_tr_b16 a[94:95], v25 offset:4096\n\t" "v_add_u32 v24, s40, v18\n\t" "v_add_u32 v25, s40, v19\n\t" "s_add_u32 s40, s40, 8192\n\t" \
   "s_cmp_eq_u32 s40, 81920\n\t" "s_cselect_b32 s40, 0, s40\n\t" "ds_read_b64_tr_b16 a[36:37], v24 offset:0\n\t" \
   "ds_read_b64_tr_b16 a[38:39], v24 offset:4096\n\t" "ds_read_b64_tr_b16 a[100:101], v25 offset:0\n\t" \
   "ds_read_b64_tr_b16 a[102:103], v25 offset:4096\n\t" "v_add_u32 v24, s40, v18\n\t" "v_add_u32 v25, s40, v19\n\t" "s_add_u32 s40, s40, 8192\n\t" \
   "s_cmp_eq_u32 s40, 81920\n\t" "s_cselect_b32 s40, 0, s40\n\t" "ds_read_b64_tr_b16 a[44:45], v24 offset:0\n\t" \
   "ds_read_b64_tr_b16 a[46:47], v24 offset:4096\n\t" "ds_read_b64_tr_b16 a[108:109], v25 offset:0\n\t" \
   "ds_read_b64_tr_b16 a[110:111], v25 offset:4096\n\t" "v_add_u32 v24, s40, v18\n\t" "v_add_u32 v25, s40, v19\n\t" "s_add_u32 s40, s40, 8192\n\t" \
   "s_cmp_eq_u32 s40, 81920\n\t" "s_cselect_b32 s40, 0, s40\n\t" "ds_read_b64_tr_b16 a[52:53], v24 offset:0\n\t" \
   "ds_read_b64_tr_b16 a[54:55], v24 offset:4096\n\t" "ds_read_b64_tr_b16 a[116:117], v25 offset:0\n\t" \
   "ds_read_b64_tr_b16 a[118:119], v25 offset:4096\n\t" "v_add_u32 v24, s40, v18\n\t" "v_add_u32 v25, s40, v19\n\t" "s_add_u32 s40, s40, 8192\n\t" \
   "s_cmp_eq_u32 s40, 81920\n\t" "s_cselect_b32 s40, 0, s40\n\t" "ds_read_b64_tr_b16 a[60:61], v24 offset:0\n\t" \
   "ds_read_b64_tr_b16 a[62:63], v24 offset:4096\n\t" "ds_read_b64_tr_b16 a[124:125], v25 offset:0\n\t" \
   "ds_read_b64_tr_b16 a[126:127], v25 offset:4096\n\t" "v_add_u32 v24, s40, v18\n\t" "v_add_u32 v25, s40, v19\n\t" "s_add_u32 s40, s40, 8192\n\t" \
   "s_cmp_eq_u32 s40, 81920\n\t" "s_cselect_b32 s40, 0, s40\n\t" "ds_read_b64_tr_b16 v[196:197], v24 offset:0\n\t" \
   "ds_read_b64_tr_b16 v[198:199], v24 offset:4096\n\t" "ds_read_b64_tr_b16 v[212:213], v25 offset:0\n\t" \
   "ds_read_b64_tr_b16 v[214:215], v25 offset:4096\n\t" "v_add_u32 v24, s40, v18\n\t" "v_add_u32 v25, s40, v19\n\t" "s_add_u32 s40, s40, 8192\n\t" \
   "s_cmp_eq_u32 s40, 81920\n\t" "s_cselect_b32 s40, 0, s40\n\t" "ds_read_b64_tr_b16 v[204:205], v24 offset:0\n\t" \
   "ds_read_b64_tr_b16 v[206:207], v24 offset:4096\n\t" "ds_read_b64_tr_b16 v[220:221], v25 offset:0\n\t" \
   "ds_read_b64_tr_b16 v[222:223], v25 offset:4096\n\t" "s_waitcnt vmcnt(19)\n\t" "s_waitcnt lgkmcnt(0)\n\t" "s_barrier\n\t" \
   "ds_write_b128 v20, a[132:135] offset:0\n\t" "s_waitcnt vmcnt(18)\n\t" "ds_write_b128 v20, a[196:199] offset:4096\n\t" "s_waitcnt vmcnt(17)\n\t" \
   "ds_write_b128 v20, a[140:143] offset:8192\n\t" "s_waitcnt vmcnt(16)\n\t" "ds_write_b128 v20, a[204:207] offset:12288\n\t" "s_waitcnt vmcnt(15)\n\t" \
   "ds_write_b128 v20, a[148:151] offset:16384\n\t" "s_waitcnt vmcnt(14)\n\t" "ds_write_b128 v20, a[212:215] offset:20480\n\t" \
   "s_waitcnt vmcnt(13)\n\t" "ds_write_b128 v20, a[156:159] offset:24576\n\t" "s_waitcnt vmcnt(12)\n\t" \
   "ds_write_b128 v20, a[220:223] offset:28672\n\t" "s_waitcnt vmcnt(11)\n\t" "ds_write_b128 v20, a[164:167] offset:32768\n\t" \
   "s_waitcnt vmcnt(10)\n\t" "ds_write_b128 v20, a[228:231] offset:36864\n\t" "s_waitcnt vmcnt(9)\n\t" "ds_write_b128 v20, a[172:175] offset:40960\n\t" \
   "s_waitcnt vmcnt(8)\n\t" "ds_write_b128 v20, a[236:239] offset:45056\n\t" "s_waitcnt vmcnt(7)\n\t" "ds_write_b128 v20, a[180:183] offset:49152\n\t" \
   "s_waitcnt vmcnt(6)\n\t" "ds_write_b128 v20, a[244:247] offset:53248\n\t" "s_waitcnt vmcnt(5)\n\t" "ds_write_b128 v20, a[188:191] offset:57344\n\t" \
   "s_waitcnt vmcnt(4)\n\t" "ds_write_b128 v20, a[252:255] offset:61440\n\t" "s_waitcnt vmcnt(3)\n\t" "ds_write_b128 v23, v[228:231] offset:0\n\t" \
   "s_waitcnt vmcnt(2)\n\t" "ds_write_b128 v23, v[244:247] offset:4096\n\t" "s_waitcnt vmcnt(1)\n\t" "ds_write_b128 v23, v[236:239] offset:8192\n\t" \
   "s_waitcnt vmcnt(0)\n\t" "ds_write_b128 v23, v[252:255] offset:12288\n\t" "s_waitcnt lgkmcnt(0)\n\t" "s_barrier\n\t" "s_lshl_b32 s40, %[p0], 13\n\t" \
   "v_add_u32 v24, s40, v18\n\t" "v_add_u32 v25, s40, v19\n\t" "s_add_u32 s40, s40, 8192\n\t" "s_cmp_eq_u32 s40, 81920\n\t" \
   "s_cselect_b32 s40, 0, s40\n\t" "ds_read_b64_tr_b16 a[132:133], v24 offset:0\n\t" "ds_read_b64_tr_b16 a[134:135], v24 offset:4096\n\t" \
   "ds_read_b64_tr_b16 a[196:197], v25 offset:0\n\t" "ds_read_b64_tr_b16 a[198:199], v25 offset:4096\n\t" "v_add_u32 v24, s40, v18\n\t" \
   "v_add_u32 v25, s40, v19\n\t" "s_add_u32 s40, s40, 8192\n\t" "s_cmp_eq_u32 s40, 81920\n\t" "s_cselect_b32 s40, 0, s40\n\t" \
   "ds_read_b64_tr_b16 a[140:141], v24 offset:0\n\t" "ds_read_b64_tr_b16 a[142:143], v24 offset:4096\n\t" \
   "ds_read_b64_tr_b16 a[204:205], v25 offset:0\n\t" "ds_read_b64_tr_b16 a[206:207], v25 offset:4096\n\t" "v_add_u32 v24, s40, v18\n\t" \
   "v_add_u32 v25, s40, v19\n\t" "s_add_u32 s40, s40, 8192\n\t" "s_cmp_eq_u32 s40, 81920\n\t" "s_cselect_b32 s40, 0, s40\n\t" \
   "ds_read_b64_tr_b16 a[148:149], v24 offset:0\n\t" "ds_read_b64_tr_b16 a[150:151], v24 offset:4096\n\t" \
   "ds_read_b64_tr_b16 a[212:213], v25 offset:0\n\t" "ds_read_b64_tr_b16 a[214:215], v25 offset:4096\n\t" "v_add_u32 v24, s40, v18\n\t" \
   "v_add_u32 v25, s40, v19\n\t" "s_add_u32 s40, s40, 8192\n\t" "s_cmp_eq_u32 s40, 81920\n\t" "s_cselect_b32 s40, 0, s40\n\t" \
   "ds_read_b64_tr_b16 a[156:157], v24 offset:0\n\t" "ds_read_b64_tr_b16 a[158:159], v24 offset:4096\n\t" \
   "ds_read_b64_tr_b16 a[220:221], v25 offset:0\n\t" "ds_read_b64_tr_b16 a[222:223], v25 offset:4096\n\t" "v_add_u32 v24, s40, v18\n\t" \
   "v_add_u32 v25, s40, v19\n\t" "s_add_u32 s40, s40, 8192\n\t" "s_cmp_eq_u32 s40, 81920\n\t" "s_cselect_b32 s40, 0, s40\n\t" \
   "ds_read_b64_tr_b16 a[164:165], v24 offset:0\n\t" "ds_read_b64_tr_b16 a[166:167], v24 offset:4096\n\t" \
   "ds_read_b64_tr_b16 a[228:229], v25 offset:0\n\t" "ds_read_b64_tr_b16 a[230:231], v25 offset:4096\n\t" "v_add_u32 v24, s40, v18\n\t" \
   "v_add_u32 v25, s40, v19\n\t" "s_add_u32 s40, s40, 8192\n\t" "s_cmp_eq_u32 s40, 81920\n\t" "s_cselect_b32 s40, 0, s40\n\t" \
   "ds_read_b64_tr_b16 a[172:173], v24 offset:0\n\t" "ds_read_b64_tr_b16 a[174:175], v24 offset:4096\n\t" \
   "ds_read_b64_tr_b16 a[236:237], v25 offset:0\n\t" "ds_read_b64_tr_b16 a[238:239], v25 offset:4096\n\t" "v_add_u32 v24, s40, v18\n\t" \
   "v_add_u32 v25, s40, v19\n\t" "s_add_u32 s40, s40, 8192\n\t" "s_cmp_eq_u32 s40, 81920\n\t" "s_cselect_b32 s40, 0, s40\n\t" \
   "ds_read_b64_tr_b16 a[180:181], v24 offset:0\n\t" "ds_read_b64_tr_b16 a[182:183], v24 offset:4096\n\t" \
   "ds_read_b64_tr_b16 a[244:245], v25 offset:0\n\t" "ds_read_b64_tr_b16 a[246:247], v25 offset:4096\n\t" "v_add_u32 v24, s40, v18\n\t" \
   "v_add_u32 v25, s40, v19\n\t" "s_add_u32 s40, s40, 8192\n\t" "s_cmp_eq_u32 s40, 81920\n\t" "s_cselect_b32 s40, 0, s40\n\t" \
   "ds_read_b64_tr_b16 a[188:189], v24 offset:0\n\t" "ds_read_b64_tr_b16 a[190:191], v24 offset:4096\n\t" \
   "ds_read_b64_tr_b16 a[252:253], v25 offset:0\n\t" "ds_read_b64_tr_b16 a[254:255], v25 offset:4096\n\t" "v_add_u32 v24, s40, v18\n\t" \
   "v_add_u32 v25, s40, v19\n\t" "s_add_u32 s40, s40, 8192\n\t" "s_cmp_eq_u32 s40, 81920\n\t" "s_cselect_b32 s40, 0, s40\n\t" \
   "ds_read_b64_tr_b16 v[228:229], v24 offset:0\n\t" "ds_read_b64_tr_b16 v[230:231], v24 offset:4096\n\t" \
   "ds_read_b64_tr_b16 v[244:245], v25 offset:0\n\t" "ds_read_b64_tr_b16 v[246:247], v25 offset:4096\n\t" "v_add_u32 v24, s40, v18\n\t" \
   "v_add_u32 v25, s40, v19\n\t" "s_add_u32 s40, s40, 8192\n\t" "s_cmp_eq_u32 s40, 81920\n\t" "s_cselect_b32 s40, 0, s40\n\t" \
   "ds_read_b64_tr_b16 v[236:237], v24 offset:0\n\t" "ds_read_b64_tr_b16 v[238:239], v24 offset:4096\n\t" \
   "ds_read_b64_tr_b16 v[252:253], v25 offset:0\n\t" "ds_read_b64_tr_b16 v[254:255], v25 offset:4096\n\t" "s_waitcnt lgkmcnt(0)"
#define LDP_CLOB "v24", "v25", "s40", "s41"
#define LDK_ASM \
   "s_mov_b32 s41, 0\n\t" "buffer_load_dwordx4 a[0:3], v16, %[ra], s41 offen\n\t" "s_mov_b32 s41, 131072\n\t" \
   "buffer_load_dwordx4 a[4:7], v16, %[ra], s41 offen\n\t" "s_mov_b32 s41, 262144\n\t" "buffer_load_dwordx4 a[16:19], v16, %[ra], s41 offen\n\t" \
   "s_mov_b32 s41, 393216\n\t" "buffer_load_dwordx4 a[20:23], v16, %[ra], s41 offen\n\t" "s_mov_b32 s41, 524288\n\t" \
   "buffer_load_dwordx4 a[32:35], v16, %[ra], s41 offen\n\t" "s_mov_b32 s41, 655360\n\t" "buffer_load_dwordx4 a[36:39], v16, %[ra], s41 offen\n\t" \
   "s_mov_b32 s41, 786432\n\t" "buffer_load_dwordx4 a[48:51], v16, %[ra], s41 offen\n\t" "s_mov_b32 s41, 917504\n\t" \
   "buffer_load_dwordx4 a[52:55], v16, %[ra], s41 offen\n\t" "s_mov_b32 s41, 1048576\n\t" "buffer_load_dwordx4 a[64:67], v16, %[ra], s41 offen\n\t" \
   "s_mov_b32 s41, 1179648\n\t" "buffer_load_dwordx4 a[68:71], v16, %[ra], s41 offen\n\t" "s_mov_b32 s41, 0\n\t" \
   "buffer_load_dwordx4 a[8:11], v16, %[rb], s41 offen\n\t" "s_mov_b32 s41, 131072\n\t" "buffer_load_dwordx4 a[12:15], v16, %[rb], s41 offen\n\t" \
   "s_mov_b32 s41, 262144\n\t" "buffer_load_dwordx4 a[24:27], v16, %[rb], s41 offen\n\t" "s_mov_b32 s41, 393216\n\t" \
   "buffer_load_dwordx4 a[28:31], v16, %[rb], s41 offen\n\t" "s_mov_b32 s41, 524288\n\t" "buffer_load_dwordx4 a[40:43], v16, %[rb], s41 offen\n\t" \
   "s_mov_b32 s41, 655360\n\t" "buffer_load_dwordx4 a[44:47], v16, %[rb], s41 offen\n\t" "s_mov_b32 s41, 786432\n\t" \
   "buffer_load_dwordx4 a[56:59], v16, %[rb], s41 offen\n\t" "s_mov_b32 s41, 917504\n\t" "buffer_load_dwordx4 a[60:63], v16, %[rb], s41 offen\n\t" \
   "s_mov_b32 s41, 1048576\n\t" "buffer_load_dwordx4 a[72:75], v16, %[rb], s41 offen\n\t" "s_mov_b32 s41, 1179648\n\t" \
   "buffer_load_dwordx4 a[76:79], v16, %[rb], s41 offen\n\t" "s_mov_b32 s41, 1310720\n\t" "buffer_load_dwordx4 a[80:83], v16, %[ra], s41 offen\n\t" \
   "s_mov_b32 s41, 1441792\n\t" "buffer_load_dwordx4 a[84:87], v16, %[ra], s41 offen\n\t" "s_mov_b32 s41, 1572864\n\t" \
   "buffer_load_dwordx4 a[96:99], v16, %[ra], s41 offen\n\t" "s_mov_b32 s41, 1703936\n\t" "buffer_load_dwordx4 a[100:103], v16, %[ra], s41 offen\n\t" \
   "s_mov_b32 s41, 1835008\n\t" "buffer_load_dwordx4 a[112:115], v16, %[ra], s41 offen\n\t" "s_mov_b32 s41, 1966080\n\t" \
   "buffer_load_dwordx4 a[116:119], v16, %[ra], s41 offen\n\t" "s_mov_b32 s41, 2097152\n\t" "buffer_load_dwordx4 a[128:131], v16, %[ra], s41 offen\n\t" \
   "s_mov_b32 s41, 2228224\n\t" "buffer_load_dwordx4 a[132:135], v16, %[ra], s41 offen\n\t" "s_mov_b32 s41, 2359296\n\t" \
   "buffer_load_dwordx4 a[144:147], v16, %[ra], s41 offen\n\t" "s_mov_b32 s41, 2490368\n\t" "buffer_load_dwordx4 a[148:151], v16, %[ra], s41 offen\n\t" \
   "s_mov_b32 s41, 1310720\n\t" "buffer_load_dwordx4 a[88:91], v16, %[rb], s41 offen\n\t" "s_mov_b32 s41, 1441792\n\t" \
   "buffer_load_dwordx4 a[92:95], v16, %[rb], s41 offen\n\t" "s_mov_b32 s41, 1572864\n\t" "buffer_load_dwordx4 a[104:107], v16, %[rb], s41 offen\n\t" \
   "s_mov_b32 s41, 1703936\n\t" "buffer_load_dwordx4 a[108:111], v16, %[rb], s41 offen\n\t" "s_mov_b32 s41, 1835008\n\t" \
   "buffer_load_dwordx4 a[120:123], v16, %[rb], s41 offen\n\t" "s_mov_b32 s41, 1966080\n\t" "buffer_load_dwordx4 a[124:127], v16, %[rb], s41 offen\n\t" \
   "s_mov_b32 s41, 2097152\n\t" "buffer_load_dwordx4 a[136:139], v16, %[rb], s41 offen\n\t" "s_mov_b32 s41, 2228224\n\t" \
   "buffer_load_dwordx4 a[140:143], v16, %[rb], s41 offen\n\t" "s_mov_b32 s41, 2359296\n\t" "buffer_load_dwordx4 a[152:155], v16, %[rb], s41 offen\n\t" \
   "s_mov_b32 s41, 2490368\n\t" "buffer_load_dwordx4 a[156:159], v16, %[rb], s41 offen\n\t" "s_mov_b32 s41, 2621440\n\t" \
   "buffer_load_dwordx4 a[160:163], v16, %[ra], s41 offen\n\t" "s_mov_b32 s41, 2752512\n\t" "buffer_load_dwordx4 a[164:167], v16, %[ra], s41 offen\n\t" \
   "s_mov_b32 s41, 2883584\n\t" "buffer_load_dwordx4 a[176:179], v16, %[ra], s41 offen\n\t" "s_mov_b32 s41, 3014656\n\t" \
   "buffer_load_dwordx4 a[180:183], v16, %[ra], s41 offen\n\t" "s_mov_b32 s41, 3145728\n\t" "buffer_load_dwordx4 a[192:195], v16, %[ra], s41 offen\n\t" \
   "s_mov_b32 s41, 3276800\n\t" "buffer_load_dwordx4 a[196:199], v16, %[ra], s41 offen\n\t" "s_mov_b32 s41, 3407872\n\t" \
   "buffer_load_dwordx4 a[208:211], v16, %[ra], s41 offen\n\t" "s_mov_b32 s41, 3538944\n\t" "buffer_load_dwordx4 a[212:215], v16, %[ra], s41 offen\n\t" \
   "s_mov_b32 s41, 3670016\n\t" "buffer_load_dwordx4 a[224:227], v16, %[ra], s41 offen\n\t" "s_mov_b32 s41, 3801088\n\t" \
   "buffer_load_dwordx4 a[228:231], v16, %[ra], s41 offen\n\t" "s_mov_b32 s41, 2621440\n\t" "buffer_load_dwordx4 a[168:171], v16, %[rb], s41 offen\n\t" \
   "s_mov_b32 s41, 2752512\n\t" "buffer_load_dwordx4 a[172:175], v16, %[rb], s41 offen\n\t" "s_mov_b32 s41, 2883584\n\t" \
   "buffer_load_dwordx4 a[184:187], v16, %[rb], s41 offen\n\t" "s_mov_b32 s41, 3014656\n\t" "buffer_load_dwordx4 a[188:191], v16, %[rb], s41 offen\n\t" \
   "s_mov_b32 s41, 3145728\n\t" "buffer_load_dwordx4 a[200:203], v16, %[rb], s41 offen\n\t" "s_mov_b32 s41, 3276800\n\t" \
   "buffer_load_dwordx4 a[204:207], v16, %[rb], s41 offen\n\t" "s_mov_b32 s41, 3407872\n\t" "buffer_load_dwordx4 a[216:219], v16, %[rb], s41 offen\n\t" \
   "s_mov_b32 s41, 3538944\n\t" "buffer_load_dwordx4 a[220:223], v16, %[rb], s41 offen\n\t" "s_mov_b32 s41, 3670016\n\t" \
   "buffer_load_dwordx4 a[232:235], v16, %[rb], s41 offen\n\t" "s_mov_b32 s41, 3801088\n\t" "buffer_load_dwordx4 a[236:239], v16, %[rb], s41 offen\n\t" \
   "s_waitcnt vmcnt(59)\n\t" "ds_write_b128 v20, a[0:3] offset:0\n\t" "s_mov_b32 s41, 3932160\n\t" \
   "buffer_load_dwordx4 a[240:243], v16, %[ra], s41 offen\n\t" "s_waitcnt vmcnt(59)\n\t" "ds_write_b128 v20, a[4:7] offset:4096\n\t" \
   "s_mov_b32 s41, 4063232\n\t" "buffer_load_dwordx4 a[244:247], v16, %[ra], s41 offen\n\t" "s_waitcnt vmcnt(59)\n\t" \
   "ds_write_b128 v20, a[16:19] offset:8192\n\t" "s_mov_b32 s41, 4194304\n\t" "buffer_load_dwordx4 v[192:195], v16, %[ra], s41 offen\n\t" \
   "s_waitcnt vmcnt(59)\n\t" "ds_write_b128 v20, a[20:23] offset:12288\n\t" "s_mov_b32 s41, 4325376\n\t" \
   "buffer_load_dwordx4 v[196:199], v16, %[ra], s41 offen\n\t" "s_waitcnt vmcnt(59)\n\t" "ds_write_b128 v20, a[32:35] offset:16384\n\t" \
   "s_mov_b32 s41, 4456448\n\t" "buffer_load_dwordx4 v[208:211], v16, %[ra], s41 offen\n\t" "s_waitcnt vmcnt(59)\n\t" \
   "ds_write_b128 v20, a[36:39] offset:20480\n\t" "s_mov_b32 s41, 4587520\n\t" "buffer_load_dwordx4 v[212:215], v16, %[ra], s41 offen\n\t" \
   "s_waitcnt vmcnt(59)\n\t" "ds_write_b128 v20, a[48:51] offset:24576\n\t" "s_mov_b32 s41, 4718592\n\t" \
   "buffer_load_dwordx4 v[224:227], v16, %[ra], s41 offen\n\t" "s_waitcnt vmcnt(59)\n\t" "ds_write_b128 v20, a[52:55] offset:28672\n\t" \
   "s_mov_b32 s41, 4849664\n\t" "buffer_load_dwordx4 v[228:231], v16, %[ra], s41 offen\n\t" "s_waitcnt vmcnt(59)\n\t" \
   "ds_write_b128 v20, a[64:67] offset:32768\n\t" "s_mov_b32 s41, 4980736\n\t" "buffer_load_dwordx4 v[240:243], v16, %[ra], s41 offen\n\t" \
   "s_waitcnt vmcnt(59)\n\t" "ds_write_b128 v20, a[68:71] offset:36864\n\t" "s_mov_b32 s41, 5111808\n\t" \
   "buffer_load_dwordx4 v[244:247], v16, %[ra], s41 offen\n\t" "s_waitcnt lgkmcnt(0)\n\t" "s_barrier\n\t" "ds_read_b64_tr_b16 a[0:1], v18 offset:0\n\t" \
   "ds_read_b64_tr_b16 a[2:3], v18 offset:4096\n\t" "ds_read_b64_tr_b16 a[4:5], v19 offset:0\n\t" "ds_read_b64_tr_b16 a[6:7], v19 offset:4096\n\t" \
   "ds_read_b64_tr_b16 a[16:17], v18 offset:8192\n\t" "ds_read_b64_tr_b16 a[18:19], v18 offset:12288\n\t" \
   "ds_read_b64_tr_b16 a[20:21], v19 offset:8192\n\t" "ds_read_b64_tr_b16 a[22:23], v19 offset:12288\n\t" \
   "ds_read_b64_tr_b16 a[32:33], v18 offset:16384\n\t" "ds_read_b64_tr_b16 a[34:35], v18 offset:20480\n\t" \
   "ds_read_b64_tr_b16 a[36:37], v19 offset:16384\n\t" "ds_read_b64_tr_b16 a[38:39], v19 offset:20480\n\t" \
   "ds_read_b64_tr_b16 a[48:49], v18 offset:24576\n\t" "ds_read_b64_tr_b16 a[50:51], v18 offset:28672\n\t" \
   "ds_read_b64_tr_b16 a[52:53], v19 offset:24576\n\t" "ds_read_b64_tr_b16 a[54:55], v19 offset:28672\n\t" \
   "ds_read_b64_tr_b16 a[64:65], v18 offset:32768\n\t" "ds_read_b64_tr_b16 a[66:67], v18 offset:36864\n\t" \
   "ds_read_b64_tr_b16 a[68:69], v19 offset:32768\n\t" "ds_read_b64_tr_b16 a[70:71], v19 offset:36864\n\t" "s_waitcnt vmcnt(59)\n\t" \
   "s_waitcnt lgkmcnt(0)\n\t" "s_barrier\n\t" "ds_write_b128 v20, a[8:11] offset:0\n\t" "s_mov_b32 s41, 3932160\n\t" \
   "buffer_load_dwordx4 a[248:251], v16, %[rb], s41 offen\n\t" "s_waitcnt vmcnt(59)\n\t" "ds_write_b128 v20, a[12:15] offset:4096\n\t" \
   "s_mov_b32 s41, 4063232\n\t" "buffer_load_dwordx4 a[252:255], v16, %[rb], s41 offen\n\t" "s_waitcnt vmcnt(59)\n\t" \
   "ds_write_b128 v20, a[24:27] offset:8192\n\t" "s_mov_b32 s41, 4194304\n\t" "buffer_load_dwordx4 v[200:203], v16, %[rb], s41 offen\n\t" \
   "s_waitcnt vmcnt(59)\n\t" "ds_write_b128 v20, a[28:31] offset:12288\n\t" "s_mov_b32 s41, 4325376\n\t" \
   "buffer_load_dwordx4 v[204:207], v16, %[rb], s41 offen\n\t" "s_waitcnt vmcnt(59)\n\t" "ds_write_b128 v20, a[40:43] offset:16384\n\t" \
   "s_mov_b32 s41, 4456448\n\t" "buffer_load_dwordx4 v[216:219], v16, %[rb], s41 offen\n\t" "s_waitcnt vmcnt(59)\n\t" \
   "ds_write_b128 v20, a[44:47] offset:20480\n\t" "s_mov_b32 s41, 4587520\n\t" "buffer_load_dwordx4 v[220:223], v16, %[rb], s41 offen\n\t" \
   "s_waitcnt vmcnt(59)\n\t" "ds_write_b128 v20, a[56:59] offset:24576\n\t" "s_mov_b32 s41, 4718592\n\t" \
   "buffer_load_dwordx4 v[232:235], v16, %[rb], s41 offen\n\t" "s_waitcnt vmcnt(59)\n\t" "ds_write_b128 v20, a[60:63] offset:28672\n\t" \
   "s_mov_b32 s41, 4849664\n\t" "buffer_load_dwordx4 v[236:239], v16, %[rb], s41 offen\n\t" "s_waitcnt vmcnt(59)\n\t" \
   "ds_write_b128 v20, a[72:75] offset:32768\n\t" "s_mov_b32 s41, 4980736\n\t" "buffer_load_dwordx4 v[248:251], v16, %[rb], s41 offen\n\t" \
   "s_waitcnt vmcnt(59)\n\t" "ds_write_b128 v20, a[76:79] offset:36864\n\t" "s_mov_b32 s41, 5111808\n\t" \
   "buffer_load_dwordx4 v[252:255], v16, %[rb], s41 offen\n\t" "s_waitcnt lgkmcnt(0)\n\t" "s_barrier\n\t" "ds_read_b64_tr_b16 a[8:9], v18 offset:0\n\t" \
   "ds_read_b64_tr_b16 a[10:11], v18 offset:4096\n\t" "ds_read_b64_tr_b16 a[12:13], v19 offset:0\n\t" \
   "ds_read_b64_tr_b16 a[14:15], v19 offset:4096\n\t" "ds_read_b64_tr_b16 a[24:25], v18 offset:8192\n\t" \
   "ds_read_b64_tr_b16 a[26:27], v18 offset:12288\n\t" "ds_read_b64_tr_b16 a[28:29], v19 offset:8192\n\t" \
   "ds_read_b64_tr_b16 a[30:31], v19 offset:12288\n\t" "ds_read_b64_tr_b16 a[40:41], v18 offset:16384\n\t" \
   "ds_read_b64_tr_b16 a[42:43], v18 offset:20480\n\t" "ds_read_b64_tr_b16 a[44:45], v19 offset:16384\n\t" \
   "ds_read_b64_tr_b16 a[46:47], v19 offset:20480\n\t" "ds_read_b64_tr_b16 a[56:57], v18 offset:24576\n\t" \
   "ds_read_b64_tr_b16 a[58:59], v18 offset:28672\n\t" "ds_read_b64_tr_b16 a[60:61], v19 offset:24576\n\t" \
   "ds_read_b64_tr_b16 a[62:63], v19 offset:28672\n\t" "ds_read_b64_tr_b16 a[72:73], v18 offset:32768\n\t" \
   "ds_read_b64_tr_b16 a[74:75], v18 offset:36864\n\t" "ds_read_b64_tr_b16 a[76:77], v19 offset:32768\n\t" \
   "ds_read_b64_tr_b16 a[78:79], v19 offset:36864\n\t" "s_waitcnt vmcnt(59)\n\t" "s_waitcnt lgkmcnt(0)\n\t" "s_barrier\n\t" \
   "ds_write_b128 v20, a[80:83] offset:0\n\t" "s_waitcnt vmcnt(58)\n\t" "ds_write_b128 v20, a[84:87] offset:4096\n\t" "s_waitcnt vmcnt(57)\n\t" \
   "ds_write_b128 v20, a[96:99] offset:8192\n\t" "s_waitcnt vmcnt(56)\n\t" "ds_write_b128 v20, a[100:103] offset:12288\n\t" "s_waitcnt vmcnt(55)\n\t" \
   "ds_write_b128 v20, a[112:115] offset:16384\n\t" "s_waitcnt vmcnt(54)\n\t" "ds_write_b128 v20, a[116:119] offset:20480\n\t" \
   "s_waitcnt vmcnt(53)\n\t" "ds_write_b128 v20, a[128:131] offset:24576\n\t" "s_waitcnt vmcnt(52)\n\t" \
   "ds_write_b128 v20, a[132:135] offset:28672\n\t" "s_waitcnt vmcnt(51)\n\t" "ds_write_b128 v20, a[144:147] offset:32768\n\t" \
   "s_waitcnt vmcnt(50)\n\t" "ds_write_b128 v20, a[148:151] offset:36864\n\t" "s_waitcnt lgkmcnt(0)\n\t" "s_barrier\n\t" \
   "ds_read_b64_tr_b16 a[80:81], v18 offset:0\n\t" "ds_read_b64_tr_b16 a[82:83], v18 offset:4096\n\t" "ds_read_b64_tr_b16 a[84:85], v19 offset:0\n\t" \
   "ds_read_b64_tr_b16 a[86:87], v19 offset:4096\n\t" "ds_read_b64_tr_b16 a[96:97], v18 offset:8192\n\t" \
   "ds_read_b64_tr_b16 a[98:99], v18 offset:12288\n\t" "ds_read_b64_tr_b16 a[100:101], v19 offset:8192\n\t" \
   "ds_read_b64_tr_b16 a[102:103], v19 offset:12288\n\t" "ds_read_b64_tr_b16 a[112:113], v18 offset:16384\n\t" \
   "ds_read_b64_tr_b16 a[114:115], v18 offset:20480\n\t" "ds_read_b64_tr_b16 a[116:117], v19 offset:16384\n\t" \
   "ds_read_b64_tr_b16 a[118:119], v19 offset:20480\n\t" "ds_read_b64_tr_b16 a[128:129], v18 offset:24576\n\t" \
   "ds_read_b64_tr_b16 a[130:131], v18 offset:28672\n\t" "ds_read_b64_tr_b16 a[132:133], v19 offset:24576\n\t" \
   "ds_read_b64_tr_b16 a[134:135], v19 offset:28672\n\t" "ds_read_b64_tr_b16 a[144:145], v18 offset:32768\n\t" \
   "ds_read_b64_tr_b16 a[146:147], v18 offset:36864\n\t" "ds_read_b64_tr_b16 a[148:149], v19 offset:32768\n\t" \
   "ds_read_b64_tr_b16 a[150:151], v19 offset:36864\n\t" "s_waitcnt vmcnt(49)\n\t" "s_waitcnt lgkmcnt(0)\n\t" "s_barrier\n\t" \
   "ds_write_b128 v20, a[88:91] offset:0\n\t" "s_waitcnt vmcnt(48)\n\t" "ds_write_b128 v20, a[92:95] offset:4096\n\t" "s_waitcnt vmcnt(47)\n\t" \
   "ds_write_b128 v20, a[104:107] offset:8192\n\t" "s_waitcnt vmcnt(46)\n\t" "ds_write_b128 v20, a[108:111] offset:12288\n\t" "s_waitcnt vmcnt(45)\n\t" \
   "ds_write_b128 v20, a[120:123] offset:16384\n\t" "s_waitcnt vmcnt(44)\n\t" "ds_write_b128 v20, a[124:127] offset:20480\n\t" \
   "s_waitcnt vmcnt(43)\n\t" "ds_write_b128 v20, a[136:139] offset:24576\n\t" "s_waitcnt vmcnt(42)\n\t" \
   "ds_write_b128 v20, a[140:143] offset:28672\n\t" "s_waitcnt vmcnt(41)\n\t" "ds_write_b128 v20, a[152:155] offset:32768\n\t" \
   "s_waitcnt vmcnt(40)\n\t" "ds_write_b128 v20, a[156:159] offset:36864\n\t" "s_waitcnt lgkmcnt(0)\n\t" "s_barrier\n\t" \
   "ds_read_b64_tr_b16 a[88:89], v18 offset:0\n\t" "ds_read_b64_tr_b16 a[90:91], v18 offset:4096\n\t" "ds_read_b64_tr_b16 a[92:93], v19 offset:0\n\t" \
   "ds_read_b64_tr_b16 a[94:95], v19 offset:4096\n\t" "ds_read_b64_tr_b16 a[104:105], v18 offset:8192\n\t" \
   "ds_read_b64_tr_b16 a[106:107], v18 offset:12288\n\t" "ds_read_b64_tr_b16 a[108:109], v19 offset:8192\n\t" \
   "ds_read_b64_tr_b16 a[110:111], v19 offset:12288\n\t" "ds_read_b64_tr_b16 a[120:121], v18 offset:16384\n\t" \
   "ds_read_b64_tr_b16 a[122:123], v18 offset:20480\n\t" "ds_read_b64_tr_b16 a[124:125], v19 offset:16384\n\t" \
   "ds_read_b64_tr_b16 a[126:127], v19 offset:20480\n\t" "ds_read_b64_tr_b16 a[136:137], v18 offset:24576\n\t" \
   "ds_read_b64_tr_b16 a[138:139], v18 offset:28672\n\t" "ds_read_b64_tr_b16 a[140:141], v19 offset:24576\n\t" \
   "ds_read_b64_tr_b16 a[142:143], v19 offset:28672\n\t" "ds_read_b64_tr_b16 a[152:153], v18 offset:32768\n\t" \
   "ds_read_b64_tr_b16 a[154:155], v18 offset:36864\n\t" "ds_read_b64_tr_b16 a[156:157], v19 offset:32768\n\t" \
   "ds_read_b64_tr_b16 a[158:159], v19 offset:36864\n\t" "s_waitcnt vmcnt(39)\n\t" "s_waitcnt lgkmcnt(0)\n\t" "s_barrier\n\t" \
   "ds_write_b128 v20, a[160:163] offset:0\n\t" "s_waitcnt vmcnt(38)\n\t" "ds_write_b128 v20, a[164:167] offset:4096\n\t" "s_waitcnt vmcnt(37)\n\t" \
   "ds_write_b128 v20, a[176:179] offset:8192\n\t" "s_waitcnt vmcnt(36)\n\t" "ds_write_b128 v20, a[180:183] offset:12288\n\t" "s_waitcnt vmcnt(35)\n\t" \
   "ds_write_b128 v20, a[192:195] offset:16384\n\t" "s_waitcnt vmcnt(34)\n\t" "ds_write_b128 v20, a[196:199] offset:20480\n\t" \
   "s_waitcnt vmcnt(33)\n\t" "ds_write_b128 v20, a[208:211] offset:24576\n\t" "s_waitcnt vmcnt(32)\n\t" \
   "ds_write_b128 v20, a[212:215] offset:28672\n\t" "s_waitcnt vmcnt(31)\n\t" "ds_write_b128 v20, a[224:227] offset:32768\n\t" \
   "s_waitcnt vmcnt(30)\n\t" "ds_write_b128 v20, a[228:231] offset:36864\n\t" "s_waitcnt lgkmcnt(0)\n\t" "s_barrier\n\t" \
   "ds_read_b64_tr_b16 a[160:161], v18 offset:0\n\t" "ds_read_b64_tr_b16 a[162:163], v18 offset:4096\n\t" \
   "ds_read_b64_tr_b16 a[164:165], v19 offset:0\n\t" "ds_read_b64_tr_b16 a[166:167], v19 offset:4096\n\t" \
   "ds_read_b64_tr_b16 a[176:177], v18 offset:8192\n\t" "ds_read_b64_tr_b16 a[178:179], v18 offset:12288\n\t" \
   "ds_read_b64_tr_b16 a[180:181], v19 offset:8192\n\t" "ds_read_b64_tr_b16 a[182:183], v19 offset:12288\n\t" \
   "ds_read_b64_tr_b16 a[192:193], v18 offset:16384\n\t" "ds_read_b64_tr_b16 a[194:195], v18 offset:20480\n\t" \
   "ds_read_b64_tr_b16 a[196:197], v19 offset:16384\n\t" "ds_read_b64_tr_b16 a[198:199], v19 offset:20480\n\t" \
   "ds_read_b64_tr_b16 a[208:209], v18 offset:24576\n\t" "ds_read_b64_tr_b16 a[210:211], v18 offset:28672\n\t" \
   "ds_read_b64_tr_b16 a[212:213], v19 offset:24576\n\t" "ds_read_b64_tr_b16 a[214:215], v19 offset:28672\n\t" \
   "ds_read_b64_tr_b16 a[224:225], v18 offset:32768\n\t" "ds_read_b64_tr_b16 a[226:227], v18 offset:36864\n\t" \
   "ds_read_b64_tr_b16 a[228:229], v19 offset:32768\n\t" "ds_read_b64_tr_b16 a[230:231], v19 offset:36864\n\t" "s_waitcnt vmcnt(29)\n\t" \
   "s_waitcnt lgkmcnt(0)\n\t" "s_barrier\n\t" "ds_write_b128 v20, a[168:171] offset:0\n\t" "s_waitcnt vmcnt(28)\n\t" \
   "ds_write_b128 v20, a[172:175] offset:4096\n\t" "s_waitcnt vmcnt(27)\n\t" "ds_write_b128 v20, a[184:187] offset:8192\n\t" "s_waitcnt vmcnt(26)\n\t" \
   "ds_write_b128 v20, a[188:191] offset:12288\n\t" "s_waitcnt vmcnt(25)\n\t" "ds_write_b128 v20, a[200:203] offset:16384\n\t" \
   "s_waitcnt vmcnt(24)\n\t" "ds_write_b128 v20, a[204:207] offset:20480\n\t" "s_waitcnt vmcnt(23)\n\t" \
   "ds_write_b128 v20, a[216:219] offset:24576\n\t" "s_waitcnt vmcnt(22)\n\t" "ds_write_b128 v20, a[220:223] offset:28672\n\t" \
   "s_waitcnt vmcnt(21)\n\t" "ds_write_b128 v20, a[232:235] offset:32768\n\t" "s_waitcnt vmcnt(20)\n\t" \
   "ds_write_b128 v20, a[236:239] offset:36864\n\t" "s_waitcnt lgkmcnt(0)\n\t" "s_barrier\n\t" "ds_read_b64_tr_b16 a[168:169], v18 offset:0\n\t" \
   "ds_read_b64_tr_b16 a[170:171], v18 offset:4096\n\t" "ds_read_b64_tr_b16 a[172:173], v19 offset:0\n\t" \
   "ds_read_b64_tr_b16 a[174:175], v19 offset:4096\n\t" "ds_read_b64_tr_b16 a[184:185], v18 offset:8192\n\t" \
   "ds_read_b64_tr_b16 a[186:187], v18 offset:12288\n\t" "ds_read_b64_tr_b16 a[188:189], v19 offset:8192\n\t" \
   "ds_read_b64_tr_b16 a[190:191], v19 offset:12288\n\t" "ds_read_b64_tr_b16 a[200:201], v18 offset:16384\n\t" \
   "ds_read_b64_tr_b16 a[202:203], v18 offset:20480\n\t" "ds_read_b64_tr_b16 a[204:205], v19 offset:16384\n\t" \
   "ds_read_b64_tr_b16 a[206:207], v19 offset:20480\n\t" "ds_read_b64_tr_b16 a[216:217], v18 offset:24576\n\t" \
   "ds_read_b64_tr_b16 a[218:219], v18 offset:28672\n\t" "ds_read_b64_tr_b16 a[220:221], v19 offset:24576\n\t" \
   "ds_read_b64_tr_b16 a[222:223], v19 offset:28672\n\t" "ds_read_b64_tr_b16 a[232:233], v18 offset:32768\n\t" \
   "ds_read_b64_tr_b16 a[234:235], v18 offset:36864\n\t" "ds_read_b64_tr_b16 a[236:237], v19 offset:32768\n\t" \
   "ds_read_b64_tr_b16 a[238:239], v19 offset:36864\n\t" "s_waitcnt vmcnt(19)\n\t" "s_waitcnt lgkmcnt(0)\n\t" "s_barrier\n\t" \
   "ds_write_b128 v20, a[240:243] offset:0\n\t" "s_waitcnt vmcnt(18)\n\t" "ds_write_b128 v20, a[244:247] offset:4096\n\t" "s_waitcnt vmcnt(17)\n\t" \
   "ds_write_b128 v20, v[192:195] offset:8192\n\t" "s_waitcnt vmcnt(16)\n\t" "ds_write_b128 v20, v[196:199] offset:12288\n\t" "s_waitcnt vmcnt(15)\n\t" \
   "ds_write_b128 v20, v[208:211] offset:16384\n\t" "s_waitcnt vmcnt(14)\n\t" "ds_write_b128 v20, v[212:215] offset:20480\n\t" \
   "s_waitcnt vmcnt(13)\n\t" "ds_write_b128 v20, v[224:227] offset:24576\n\t" "s_waitcnt vmcnt(12)\n\t" \
   "ds_write_b128 v20, v[228:231] offset:28672\n\t" "s_waitcnt vmcnt(11)\n\t" "ds_write_b128 v20, v[240:243] offset:32768\n\t" \
   "s_waitcnt vmcnt(10)\n\t" "ds_write_b128 v20, v[244:247] offset:36864\n\t" "s_waitcnt lgkmcnt(0)\n\t" "s_barrier\n\t" \
   "ds_read_b64_tr_b16 a[240:241], v18 offset:0\n\t" "ds_read_b64_tr_b16 a[242:243], v18 offset:4096\n\t" \
   "ds_read_b64_tr_b16 a[244:245], v19 offset:0\n\t" "ds_read_b64_tr_b16 a[246:247], v19 offset:4096\n\t" \
   "ds_read_b64_tr_b16 v[192:193], v18 offset:8192\n\t" "ds_read_b64_tr_b16 v[194:195], v18 offset:12288\n\t" \
   "ds_read_b64_tr_b16 v[196:197], v19 offset:8192\n\t" "ds_read_b64_tr_b16 v[198:199], v19 offset:12288\n\t" \
   "ds_read_b64_tr_b16 v[208:209], v18 offset:16384\n\t" "ds_read_b64_tr_b16 v[210:211], v18 offset:20480\n\t" \
   "ds_read_b64_tr_b16 v[212:213], v19 offset:16384\n\t" "ds_read_b64_tr_b16 v[214:215], v19 offset:20480\n\t" \
   "ds_read_b64_tr_b16 v[224:225], v18 offset:24576\n\t" "ds_read_b64_tr_b16 v[226:227], v18 offset:28672\n\t" \
   "ds_read_b64_tr_b16 v[228:229], v19 offset:24576\n\t" "ds_read_b64_tr_b16 v[230:231], v19 offset:28672\n\t" \
   "ds_read_b64_tr_b16 v[240:241], v18 offset:32768\n\t" "ds_read_b64_tr_b16 v[242:243], v18 offset:36864\n\t" \
   "ds_read_b64_tr_b16 v[244:245], v19 offset:32768\n\t" "ds_read_b64_tr_b16 v[246:247], v19 offset:36864\n\t" "s_waitcnt vmcnt(9)\n\t" \
   "s_waitcnt lgkmcnt(0)\n\t" "s_barrier\n\t" "ds_write_b128 v20, a[248:251] offset:0\n\t" "s_waitcnt vmcnt(8)\n\t" \
   "ds_write_b128 v20, a[252:255] offset:4096\n\t" "s_waitcnt vmcnt(7)\n\t" "ds_write_b128 v20, v[200:203] offset:8192\n\t" "s_waitcnt vmcnt(6)\n\t" \
   "ds_write_b128 v20, v[204:207] offset:12288\n\t" "s_waitcnt vmcnt(5)\n\t" "ds_write_b128 v20, v[216:219] offset:16384\n\t" "s_waitcnt vmcnt(4)\n\t" \
   "ds_write_b128 v20, v[220:223] offset:20480\n\t" "s_waitcnt vmcnt(3)\n\t" "ds_write_b128 v20, v[232:235] offset:24576\n\t" "s_waitcnt vmcnt(2)\n\t" \
   "ds_write_b128 v20, v[236:239] offset:28672\n\t" "s_waitcnt vmcnt(1)\n\t" "ds_write_b128 v20, v[248:251] offset:32768\n\t" "s_waitcnt vmcnt(0)\n\t" \
   "ds_write_b128 v20, v[252:255] offset:36864\n\t" "s_waitcnt lgkmcnt(0)\n\t" "s_barrier\n\t" "ds_read_b64_tr_b16 a[248:249], v18 offset:0\n\t" \
   "ds_read_b64_tr_b16 a[250:251], v18 offset:4096\n\t" "ds_read_b64_tr_b16 a[252:253], v19 offset:0\n\t" \
   "ds_read_b64_tr_b16 a[254:255], v19 offset:4096\n\t" "ds_read_b64_tr_b16 v[200:201], v18 offset:8192\n\t" \
   "ds_read_b64_tr_b16 v[202:203], v18 offset:12288\n\t" "ds_read_b64_tr_b16 v[204:205], v19 offset:8192\n\t" \
   "ds_read_b64_tr_b16 v[206:207], v19 offset:12288\n\t" "ds_read_b64_tr_b16 v[216:217], v18 offset:16384\n\t" \
   "ds_read_b64_tr_b16 v[218:219], v18 offset:20480\n\t" "ds_read_b64_tr_b16 v[220:221], v19 offset:16384\n\t" \
   "ds_read_b64_tr_b16 v[222:223], v19 offset:20480\n\t" "ds_read_b64_tr_b16 v[232:233], v18 offset:24576\n\t" \
   "ds_read_b64_tr_b16 v[234:235], v18 offset:28672\n\t" "ds_read_b64_tr_b16 v[236:237], v19 offset:24576\n\t" \
   "ds_read_b64_tr_b16 v[238:239], v19 offset:28672\n\t" "ds_read_b64_tr_b16 v[248:249], v18 offset:32768\n\t" \
   "ds_read_b64_tr_b16 v[250:251], v18 offset:36864\n\t" "ds_read_b64_tr_b16 v[252:253], v19 offset:32768\n\t" \
   "ds_read_b64_tr_b16 v[254:255], v19 offset:36864\n\t" "s_waitcnt lgkmcnt(0)"
#define LDK_CLOB "v24", "v25", "s40", "s41"
#define LDV_ASM \
   "s_mov_b32 s41, 0\n\t" "buffer_load_dwordx4 a[0:3], v16, %[ra], s41 offen\n\t" "s_mov_b32 s41, 131072\n\t" \
   "buffer_load_dwordx4 a[4:7], v16, %[ra], s41 offen\n\t" "s_mov_b32 s41, 262144\n\t" "buffer_load_dwordx4 a[8:11], v16, %[ra], s41 offen\n\t" \
   "s_mov_b32 s41, 393216\n\t" "buffer_load_dwordx4 a[12:15], v16, %[ra], s41 offen\n\t" "s_mov_b32 s41, 524288\n\t" \
   "buffer_load_dwordx4 a[16:19], v16, %[ra], s41 offen\n\t" "s_mov_b32 s41, 655360\n\t" "buffer_load_dwordx4 a[20:23], v16, %[ra], s41 offen\n\t" \
   "s_mov_b32 s41, 786432\n\t" "buffer_load_dwordx4 a[24:27], v16, %[ra], s41 offen\n\t" "s_mov_b32 s41, 917504\n\t" \
   "buffer_load_dwordx4 a[28:31], v16, %[ra], s41 offen\n\t" "s_mov_b32 s41, 1048576\n\t" "buffer_load_dwordx4 a[32:35], v16, %[ra], s41 offen\n\t" \
   "s_mov_b32 s41, 1179648\n\t" "buffer_load_dwordx4 a[36:39], v16, %[ra], s41 offen\n\t" "s_mov_b32 s41, 1310720\n\t" \
   "buffer_load_dwordx4 a[40:43], v16, %[ra], s41 offen\n\t" "s_mov_b32 s41, 1441792\n\t" "buffer_load_dwordx4 a[44:47], v16, %[ra], s41 offen\n\t" \
   "s_mov_b32 s41, 1572864\n\t" "buffer_load_dwordx4 a[48:51], v16, %[ra], s41 offen\n\t" "s_mov_b32 s41, 1703936\n\t" \
   "buffer_load_dwordx4 a[52:55], v16, %[ra], s41 offen\n\t" "s_mov_b32 s41, 1835008\n\t" "buffer_load_dwordx4 a[56:59], v16, %[ra], s41 offen\n\t" \
   "s_mov_b32 s41, 1966080\n\t" "buffer_load_dwordx4 a[60:63], v16, %[ra], s41 offen\n\t" "s_mov_b32 s41, 2097152\n\t" \
   "buffer_load_dwordx4 a[64:67], v16, %[ra], s41 offen\n\t" "s_mov_b32 s41, 2228224\n\t" "buffer_load_dwordx4 a[68:71], v16, %[ra], s41 offen\n\t" \
   "s_mov_b32 s41, 2359296\n\t" "buffer_load_dwordx4 a[72:75], v16, %[ra], s41 offen\n\t" "s_mov_b32 s41, 2490368\n\t" \
   "buffer_load_dwordx4 a[76:79], v16, %[ra], s41 offen\n\t" "s_mov_b32 s41, 2621440\n\t" "buffer_load_dwordx4 a[80:83], v16, %[ra], s41 offen\n\t" \
   "s_mov_b32 s41, 2752512\n\t" "buffer_load_dwordx4 a[84:87], v16, %[ra], s41 offen\n\t" "s_mov_b32 s41, 2883584\n\t" \
   "buffer_load_dwordx4 a[88:91], v16, %[ra], s41 offen\n\t" "s_mov_b32 s41, 3014656\n\t" "buffer_load_dwordx4 a[92:95], v16, %[ra], s41 offen\n\t" \
   "s_mov_b32 s41, 3145728\n\t" "buffer_load_dwordx4 a[96:99], v16, %[ra], s41 offen\n\t" "s_mov_b32 s41, 3276800\n\t" \
   "buffer_load_dwordx4 a[100:103], v16, %[ra], s41 offen\n\t" "s_mov_b32 s41, 3407872\n\t" "buffer_load_dwordx4 a[104:107], v16, %[ra], s41 offen\n\t" \
   "s_mov_b32 s41, 3538944\n\t" "buffer_load_dwordx4 a[108:111], v16, %[ra], s41 offen\n\t" "s_mov_b32 s41, 3670016\n\t" \
   "buffer_load_dwordx4 a[112:115], v16, %[ra], s41 offen\n\t" "s_mov_b32 s41, 3801088\n\t" "buffer_load_dwordx4 a[116:119], v16, %[ra], s41 offen\n\t" \
   "s_mov_b32 s41, 3932160\n\t" "buffer_load_dwordx4 a[120:123], v16, %[ra], s41 offen\n\t" "s_mov_b32 s41, 4063232\n\t" \
   "buffer_load_dwordx4 a[124:127], v16, %[ra], s41 offen\n\t" "s_mov_b32 s41, 4194304\n\t" "buffer_load_dwordx4 a[128:131], v16, %[ra], s41 offen\n\t" \
   "s_mov_b32 s41, 4325376\n\t" "buffer_load_dwordx4 a[132:135], v16, %[ra], s41 offen\n\t" "s_mov_b32 s41, 4456448\n\t" \
   "buffer_load_dwordx4 a[136:139], v16, %[ra], s41 offen\n\t" "s_mov_b32 s41, 4587520\n\t" "buffer_load_dwordx4 a[140:143], v16, %[ra], s41 offen\n\t" \
   "s_mov_b32 s41, 4718592\n\t" "buffer_load_dwordx4 a[144:147], v16, %[ra], s41 offen\n\t" "s_mov_b32 s41, 4849664\n\t" \
   "buffer_load_dwordx4 a[148:151], v16, %[ra], s41 offen\n\t" "s_mov_b32 s41, 4980736\n\t" "buffer_load_dwordx4 a[152:155], v16, %[ra], s41 offen\n\t" \
   "s_mov_b32 s41, 5111808\n\t" "buffer_load_dwordx4 a[156:159], v16, %[ra], s41 offen\n\t" "s_mov_b32 s41, 0\n\t" \
   "buffer_load_dwordx4 a[160:163], v17, %[rb], s41 offen\n\t" "s_mov_b32 s41, 256\n\t" "buffer_load_dwordx4 a[176:179], v17, %[rb], s41 offen\n\t" \
   "s_mov_b32 s41, 512\n\t" "buffer_load_dwordx4 a[192:195], v17, %[rb], s41 offen\n\t" "s_mov_b32 s41, 768\n\t" \
   "buffer_load_dwordx4 a[208:211], v17, %[rb], s41 offen\n\t" "s_mov_b32 s41, 1024\n\t" "buffer_load_dwordx4 a[224:227], v17, %[rb], s41 offen\n\t" \
   "s_mov_b32 s41, 20480\n\t" "buffer_load_dwordx4 a[240:243], v17, %[rb], s41 offen\n\t" "s_mov_b32 s41, 20736\n\t" \
   "buffer_load_dwordx4 v[192:195], v17, %[rb], s41 offen\n\t" "s_mov_b32 s41, 20992\n\t" "buffer_load_dwordx4 v[208:211], v17, %[rb], s41 offen\n\t" \
   "s_mov_b32 s41, 21248\n\t" "buffer_load_dwordx4 v[224:227], v17, %[rb], s41 offen\n\t" "s_mov_b32 s41, 21504\n\t" \
   "buffer_load_dwordx4 v[240:243], v17, %[rb], s41 offen\n\t" "s_mov_b32 s41, 40960\n\t" "buffer_load_dwordx4 a[164:167], v17, %[rb], s41 offen\n\t" \
   "s_mov_b32 s41, 41216\n\t" "buffer_load_dwordx4 a[180:183], v17, %[rb], s41 offen\n\t" "s_mov_b32 s41, 41472\n\t" \
   "buffer_load_dwordx4 a[196:199], v17, %[rb], s41 offen\n\t" "s_mov_b32 s41, 41728\n\t" "buffer_load_dwordx4 a[212:215], v17, %[rb], s41 offen\n\t" \
   "s_mov_b32 s41, 41984\n\t" "buffer_load_dwordx4 a[228:231], v17, %[rb], s41 offen\n\t" "s_mov_b32 s41, 61440\n\t" \
   "buffer_load_dwordx4 a[244:247], v17, %[rb], s41 offen\n\t" "s_mov_b32 s41, 61696\n\t" "buffer_load_dwordx4 v[196:199], v17, %[rb], s41 offen\n\t" \
   "s_mov_b32 s41, 61952\n\t" "buffer_load_dwordx4 v[212:215], v17, %[rb], s41 offen\n\t" "s_mov_b32 s41, 62208\n\t" \
   "buffer_load_dwordx4 v[228:231], v17, %[rb], s41 offen\n\t" "s_mov_b32 s41, 62464\n\t" "buffer_load_dwordx4 v[244:247], v17, %[rb], s41 offen\n\t" \
   "s_waitcnt vmcnt(59)\n\t" "ds_write_b128 v20, a[0:3] offset:0\n\t" "s_mov_b32 s41, 81920\n\t" \
   "buffer_load_dwordx4 a[168:171], v17, %[rb], s41 offen\n\t" "s_waitcnt vmcnt(59)\n\t" "ds_write_b128 v20, a[4:7] offset:4096\n\t" \
   "s_mov_b32 s41, 82176\n\t" "buffer_load_dwordx4 a[184:187], v17, %[rb], s41 offen\n\t" "s_waitcnt vmcnt(59)\n\t" \
   "ds_write_b128 v20, a[8:11] offset:8192\n\t" "s_mov_b32 s41, 82432\n\t" "buffer_load_dwordx4 a[200:203], v17, %[rb], s41 offen\n\t" \
   "s_waitcnt vmcnt(59)\n\t" "ds_write_b128 v20, a[12:15] offset:12288\n\t" "s_mov_b32 s41, 82688\n\t" \
   "buffer_load_dwordx4 a[216:219], v17, %[rb], s41 offen\n\t" "s_waitcnt vmcnt(59)\n\t" "ds_write_b128 v20, a[16:19] offset:16384\n\t" \
   "s_mov_b32 s41, 82944\n\t" "buffer_load_dwordx4 a[232:235], v17, %[rb], s41 offen\n\t" "s_waitcnt vmcnt(59)\n\t" \
   "ds_write_b128 v20, a[20:23] offset:20480\n\t" "s_mov_b32 s41, 102400\n\t" "buffer_load_dwordx4 a[248:251], v17, %[rb], s41 offen\n\t" \
   "s_waitcnt vmcnt(59)\n\t" "ds_write_b128 v20, a[24:27] offset:24576\n\t" "s_mov_b32 s41, 102656\n\t" \
   "buffer_load_dwordx4 v[200:203], v17, %[rb], s41 offen\n\t" "s_waitcnt vmcnt(59)\n\t" "ds_write_b128 v20, a[28:31] offset:28672\n\t" \
   "s_mov_b32 s41, 102912\n\t" "buffer_load_dwordx4 v[216:219], v17, %[rb], s41 offen\n\t" "s_waitcnt vmcnt(59)\n\t" \
   "ds_write_b128 v20, a[32:35] offset:32768\n\t" "s_mov_b32 s41, 103168\n\t" "buffer_load_dwordx4 v[232:235], v17, %[rb], s41 offen\n\t" \
   "s_waitcnt vmcnt(59)\n\t" "ds_write_b128 v20, a[36:39] offset:36864\n\t" "s_mov_b32 s41, 103424\n\t" \
   "buffer_load_dwordx4 v[248:251], v17, %[rb], s41 offen\n\t" "s_waitcnt lgkmcnt(0)\n\t" "s_barrier\n\t" "ds_read_b64_tr_b16 a[0:1], v18 offset:0\n\t" \
   "ds_read_b64_tr_b16 a[2:3], v18 offset:4096\n\t" "ds_read_b64_tr_b16 a[4:5], v19 offset:0\n\t" "ds_read_b64_tr_b16 a[6:7], v19 offset:4096\n\t" \
   "ds_read_b64_tr_b16 a[8:9], v18 offset:8192\n\t" "ds_read_b64_tr_b16 a[10:11], v18 offset:12288\n\t" \
   "ds_read_b64_tr_b16 a[12:13], v19 offset:8192\n\t" "ds_read_b64_tr_b16 a[14:15], v19 offset:12288\n\t" \
   "ds_read_b64_tr_b16 a[16:17], v18 offset:16384\n\t" "ds_read_b64_tr_b16 a[18:19], v18 offset:20480\n\t" \
   "ds_read_b64_tr_b16 a[20:21], v19 offset:16384\n\t" "ds_read_b64_tr_b16 a[22:23], v19 offset:20480\n\t" \
   "ds_read_b64_tr_b16 a[24:25], v18 offset:24576\n\t" "ds_read_b64_tr_b16 a[26:27], v18 offset:28672\n\t" \
   "ds_read_b64_tr_b16 a[28:29], v19 offset:24576\n\t" "ds_read_b64_tr_b16 a[30:31], v19 offset:28672\n\t" \
   "ds_read_b64_tr_b16 a[32:33], v18 offset:32768\n\t" "ds_read_b64_tr_b16 a[34:35], v18 offset:36864\n\t" \
   "ds_read_b64_tr_b16 a[36:37], v19 offset:32768\n\t" "ds_read_b64_tr_b16 a[38:39], v19 offset:36864\n\t" "s_waitcnt vmcnt(59)\n\t" \
   "s_waitcnt lgkmcnt(0)\n\t" "s_barrier\n\t" "ds_write_b128 v20, a[40:43] offset:0\n\t" "s_mov_b32 s41, 122880\n\t" \
   "buffer_load_dwordx4 a[172:175], v17, %[rb], s41 offen\n\t" "s_waitcnt vmcnt(59)\n\t" "ds_write_b128 v20, a[44:47] offset:4096\n\t" \
   "s_mov_b32 s41, 123136\n\t" "buffer_load_dwordx4 a[188:191], v17, %[rb], s41 offen\n\t" "s_waitcnt vmcnt(59)\n\t" \
   "ds_write_b128 v20, a[48:51] offset:8192\n\t" "s_mov_b32 s41, 123392\n\t" "buffer_load_dwordx4 a[204:207], v17, %[rb], s41 offen\n\t" \
   "s_waitcnt vmcnt(59)\n\t" "ds_write_b128 v20, a[52:55] offset:12288\n\t" "s_mov_b32 s41, 123648\n\t" \
   "buffer_load_dwordx4 a[220:223], v17, %[rb], s41 offen\n\t" "s_waitcnt vmcnt(59)\n\t" "ds_write_b128 v20, a[56:59] offset:16384\n\t" \
   "s_mov_b32 s41, 123904\n\t" "buffer_load_dwordx4 a[236:239], v17, %[rb], s41 offen\n\t" "s_waitcnt vmcnt(59)\n\t" \
   "ds_write_b128 v20, a[60:63] offset:20480\n\t" "s_mov_b32 s41, 143360\n\t" "buffer_load_dwordx4 a[252:255], v17, %[rb], s41 offen\n\t" \
   "s_waitcnt vmcnt(59)\n\t" "ds_write_b128 v20, a[64:67] offset:24576\n\t" "s_mov_b32 s41, 143616\n\t" \
   "buffer_load_dwordx4 v[204:207], v17, %[rb], s41 offen\n\t" "s_waitcnt vmcnt(59)\n\t" "ds_write_b128 v20, a[68:71] offset:28672\n\t" \
   "s_mov_b32 s41, 143872\n\t" "buffer_load_dwordx4 v[220:223], v17, %[rb], s41 offen\n\t" "s_waitcnt vmcnt(59)\n\t" \
   "ds_write_b128 v20, a[72:75] offset:32768\n\t" "s_mov_b32 s41, 144128\n\t" "buffer_load_dwordx4 v[236:239], v17, %[rb], s41 offen\n\t" \
   "s_waitcnt vmcnt(59)\n\t" "ds_write_b128 v20, a[76:79] offset:36864\n\t" "s_mov_b32 s41, 144384\n\t" \
   "buffer_load_dwordx4 v[252:255], v17, %[rb], s41 offen\n\t" "s_waitcnt lgkmcnt(0)\n\t" "s_barrier\n\t" \
   "ds_read_b64_tr_b16 a[40:41], v18 offset:0\n\t" "ds_read_b64_tr_b16 a[42:43], v18 offset:4096\n\t" "ds_read_b64_tr_b16 a[44:45], v19 offset:0\n\t" \
   "ds_read_b64_tr_b16 a[46:47], v19 offset:4096\n\t" "ds_read_b64_tr_b16 a[48:49], v18 offset:8192\n\t" \
   "ds_read_b64_tr_b16 a[50:51], v18 offset:12288\n\t" "ds_read_b64_tr_b16 a[52:53], v19 offset:8192\n\t" \
   "ds_read_b64_tr_b16 a[54:55], v19 offset:12288\n\t" "ds_read_b64_tr_b16 a[56:57], v18 offset:16384\n\t" \
   "ds_read_b64_tr_b16 a[58:59], v18 offset:20480\n\t" "ds_read_b64_tr_b16 a[60:61], v19 offset:16384\n\t" \
   "ds_read_b64_tr_b16 a[62:63], v19 offset:20480\n\t" "ds_read_b64_tr_b16 a[64:65], v18 offset:24576\n\t" \
   "ds_read_b64_tr_b16 a[66:67], v18 offset:28672\n\t" "ds_read_b64_tr_b16 a[68:69], v19 offset:24576\n\t" \
   "ds_read_b64_tr_b16 a[70:71], v19 offset:28672\n\t" "ds_read_b64_tr_b16 a[72:73], v18 offset:32768\n\t" \
   "ds_read_b64_tr_b16 a[74:75], v18 offset:36864\n\t" "ds_read_b64_tr_b16 a[76:77], v19 offset:32768\n\t" \
   "ds_read_b64_tr_b16 a[78:79], v19 offset:36864\n\t" "s_waitcnt vmcnt(59)\n\t" "s_waitcnt lgkmcnt(0)\n\t" "s_barrier\n\t" \
   "ds_write_b128 v20, a[80:83] offset:0\n\t" "s_waitcnt vmcnt(58)\n\t" "ds_write_b128 v20, a[84:87] offset:4096\n\t" "s_waitcnt vmcnt(57)\n\t" \
   "ds_write_b128 v20, a[88:91] offset:8192\n\t" "s_waitcnt vmcnt(56)\n\t" "ds_write_b128 v20, a[92:95] offset:12288\n\t" "s_waitcnt vmcnt(55)\n\t" \
   "ds_write_b128 v20, a[96:99] offset:16384\n\t" "s_waitcnt vmcnt(54)\n\t" "ds_write_b128 v20, a[100:103] offset:20480\n\t" "s_waitcnt vmcnt(53)\n\t" \
   "ds_write_b128 v20, a[104:107] offset:24576\n\t" "s_waitcnt vmcnt(52)\n\t" "ds_write_b128 v20, a[108:111] offset:28672\n\t" \
   "s_waitcnt vmcnt(51)\n\t" "ds_write_b128 v20, a[112:115] offset:32768\n\t" "s_waitcnt vmcnt(50)\n\t" \
   "ds_write_b128 v20, a[116:119] offset:36864\n\t" "s_waitcnt lgkmcnt(0)\n\t" "s_barrier\n\t" "ds_read_b64_tr_b16 a[80:81], v18 offset:0\n\t" \
   "ds_read_b64_tr_b16 a[82:83], v18 offset:4096\n\t" "ds_read_b64_tr_b16 a[84:85], v19 offset:0\n\t" \
   "ds_read_b64_tr_b16 a[86:87], v19 offset:4096\n\t" "ds_read_b64_tr_b16 a[88:89], v18 offset:8192\n\t" \
   "ds_read_b64_tr_b16 a[90:91], v18 offset:12288\n\t" "ds_read_b64_tr_b16 a[92:93], v19 offset:8192\n\t" \
   "ds_read_b64_tr_b16 a[94:95], v19 offset:12288\n\t" "ds_read_b64_tr_b16 a[96:97], v18 offset:16384\n\t" \
   "ds_read_b64_tr_b16 a[98:99], v18 offset:20480\n\t" "ds_read_b64_tr_b16 a[100:101], v19 offset:16384\n\t" \
   "ds_read_b64_tr_b16 a[102:103], v19 offset:20480\n\t" "ds_read_b64_tr_b16 a[104:105], v18 offset:24576\n\t" \
   "ds_read_b64_tr_b16 a[106:107], v18 offset:28672\n\t" "ds_read_b64_tr_b16 a[108:109], v19 offset:24576\n\t" \
   "ds_read_b64_tr_b16 a[110:111], v19 offset:28672\n\t" "ds_read_b64_tr_b16 a[112:113], v18 offset:32768\n\t" \
   "ds_read_b64_tr_b16 a[114:115], v18 offset:36864\n\t" "ds_read_b64_tr_b16 a[116:117], v19 offset:32768\n\t" \
   "ds_read_b64_tr_b16 a[118:119], v19 offset:36864\n\t" "s_waitcnt vmcnt(49)\n\t" "s_waitcnt lgkmcnt(0)\n\t" "s_barrier\n\t" \
   "ds_write_b128 v20, a[120:123] offset:0\n\t" "s_waitcnt vmcnt(48)\n\t" "ds_write_b128 v20, a[124:127] offset:4096\n\t" "s_waitcnt vmcnt(47)\n\t" \
   "ds_write_b128 v20, a[128:131] offset:8192\n\t" "s_waitcnt vmcnt(46)\n\t" "ds_write_b128 v20, a[132:135] offset:12288\n\t" "s_waitcnt vmcnt(45)\n\t" \
   "ds_write_b128 v20, a[136:139] offset:16384\n\t" "s_waitcnt vmcnt(44)\n\t" "ds_write_b128 v20, a[140:143] offset:20480\n\t" \
   "s_waitcnt vmcnt(43)\n\t" "ds_write_b128 v20, a[144:147] offset:24576\n\t" "s_waitcnt vmcnt(42)\n\t" \
   "ds_write_b128 v20, a[148:151] offset:28672\n\t" "s_waitcnt vmcnt(41)\n\t" "ds_write_b128 v20, a[152:155] offset:32768\n\t" \
   "s_waitcnt vmcnt(40)\n\t" "ds_write_b128 v20, a[156:159] offset:36864\n\t" "s_waitcnt lgkmcnt(0)\n\t" "s_barrier\n\t" \
   "ds_read_b64_tr_b16 a[120:121], v18 offset:0\n\t" "ds_read_b64_tr_b16 a[122:123], v18 offset:4096\n\t" \
   "ds_read_b64_tr_b16 a[124:125], v19 offset:0\n\t" "ds_read_b64_tr_b16 a[126:127], v19 offset:4096\n\t" \
   "ds_read_b64_tr_b16 a[128:129], v18 offset:8192\n\t" "ds_read_b64_tr_b16 a[130:131], v18 offset:12288\n\t" \
   "ds_read_b64_tr_b16 a[132:133], v19 offset:8192\n\t" "ds_read_b64_tr_b16 a[134:135], v19 offset:12288\n\t" \
   "ds_read_b64_tr_b16 a[136:137], v18 offset:16384\n\t" "ds_read_b64_tr_b16 a[138:139], v18 offset:20480\n\t" \
   "ds_read_b64_tr_b16 a[140:141], v19 offset:16384\n\t" "ds_read_b64_tr_b16 a[142:143], v19 offset:20480\n\t" \
   "ds_read_b64_tr_b16 a[144:145], v18 offset:24576\n\t" "ds_read_b64_tr_b16 a[146:147], v18 offset:28672\n\t" \
   "ds_read_b64_tr_b16 a[148:149], v19 offset:24576\n\t" "ds_read_b64_tr_b16 a[150:151], v19 offset:28672\n\t" \
   "ds_read_b64_tr_b16 a[152:153], v18 offset:32768\n\t" "ds_read_b64_tr_b16 a[154:155], v18 offset:36864\n\t" \
   "ds_read_b64_tr_b16 a[156:157], v19 offset:32768\n\t" "ds_read_b64_tr_b16 a[158:159], v19 offset:36864\n\t" "s_waitcnt vmcnt(39)\n\t" \
   "s_waitcnt lgkmcnt(0)\n\t" "s_barrier\n\t" "ds_write_b32 v21, a160 offset:0\n\t" "ds_write_b32 v21, a161 offset:21120\n\t" \
   "ds_write_b32 v21, a162 offset:10560\n\t" "ds_write_b32 v21, a163 offset:31680\n\t" "s_waitcnt vmcnt(38)\n\t" "ds_write_b32 v21, a176 offset:64\n\t" \
   "ds_write_b32 v21, a177 offset:21184\n\t" "ds_write_b32 v21, a178 offset:10624\n\t" "ds_write_b32 v21, a179 offset:31744\n\t" \
   "s_waitcnt vmcnt(37)\n\t" "ds_write_b32 v21, a192 offset:128\n\t" "ds_write_b32 v21, a193 offset:21248\n\t" \
   "ds_write_b32 v21, a194 offset:10688\n\t" "ds_write_b32 v21, a195 offset:31808\n\t" "s_waitcnt vmcnt(36)\n\t" \
   "ds_write_b32 v21, a208 offset:192\n\t" "ds_write_b32 v21, a209 offset:21312\n\t" "ds_write_b32 v21, a210 offset:10752\n\t" \
   "ds_write_b32 v21, a211 offset:31872\n\t" "s_waitcnt vmcnt(35)\n\t" "ds_write_b32 v21, a224 offset:256\n\t" \
   "ds_write_b32 v21, a225 offset:21376\n\t" "ds_write_b32 v21, a226 offset:10816\n\t" "ds_write_b32 v21, a227 offset:31936\n\t" \
   "s_waitcnt vmcnt(34)\n\t" "ds_write_b32 v21, a240 offset:5248\n\t" "ds_write_b32 v21, a241 offset:26368\n\t" \
   "ds_write_b32 v21, a242 offset:15808\n\t" "ds_write_b32 v21, a243 offset:36928\n\t" "s_waitcnt vmcnt(33)\n\t" \
   "ds_write_b32 v21, v192 offset:5312\n\t" "ds_write_b32 v21, v193 offset:26432\n\t" "ds_write_b32 v21, v194 offset:15872\n\t" \
   "ds_write_b32 v21, v195 offset:36992\n\t" "s_waitcnt vmcnt(32)\n\t" "ds_write_b32 v21, v208 offset:5376\n\t" \
   "ds_write_b32 v21, v209 offset:26496\n\t" "ds_write_b32 v21, v210 offset:15936\n\t" "ds_write_b32 v21, v211 offset:37056\n\t" \
   "s_waitcnt vmcnt(31)\n\t" "ds_write_b32 v21, v224 offset:5440\n\t" "ds_write_b32 v21, v225 offset:26560\n\t" \
   "ds_write_b32 v21, v226 offset:16000\n\t" "ds_write_b32 v21, v227 offset:37120\n\t" "s_waitcnt vmcnt(30)\n\t" \
   "ds_write_b32 v21, v240 offset:5504\n\t" "ds_write_b32 v21, v241 offset:26624\n\t" "ds_write_b32 v21, v242 offset:16064\n\t" \
   "ds_write_b32 v21, v243 offset:37184\n\t" "s_waitcnt lgkmcnt(0)\n\t" "s_barrier\n\t" "ds_read_b64_tr_b16 a[160:161], v22 offset:0\n\t" \
   "ds_read_b64_tr_b16 a[162:163], v22 offset:5248\n\t" "ds_read_b64_tr_b16 a[176:177], v22 offset:8\n\t" \
   "ds_read_b64_tr_b16 a[178:179], v22 offset:5256\n\t" "ds_read_b64_tr_b16 a[192:193], v22 offset:16\n\t" \
   "ds_read_b64_tr_b16 a[194:195], v22 offset:5264\n\t" "ds_read_b64_tr_b16 a[208:209], v22 offset:24\n\t" \
   "ds_read_b64_tr_b16 a[210:211], v22 offset:5272\n\t" "ds_read_b64_tr_b16 a[224:225], v22 offset:32\n\t" \
   "ds_read_b64_tr_b16 a[226:227], v22 offset:5280\n\t" "ds_read_b64_tr_b16 a[240:241], v22 offset:40\n\t" \
   "ds_read_b64_tr_b16 a[242:243], v22 offset:5288\n\t" "ds_read_b64_tr_b16 v[192:193], v22 offset:48\n\t" \
   "ds_read_b64_tr_b16 v[194:195], v22 offset:5296\n\t" "ds_read_b64_tr_b16 v[208:209], v22 offset:56\n\t" \
   "ds_read_b64_tr_b16 v[210:211], v22 offset:5304\n\t" "ds_read_b64_tr_b16 v[224:225], v22 offset:64\n\t" \
   "ds_read_b64_tr_b16 v[226:227], v22 offset:5312\n\t" "ds_read_b64_tr_b16 v[240:241], v22 offset:72\n\t" \
   "ds_read_b64_tr_b16 v[242:243], v22 offset:5320\n\t" "s_waitcnt vmcnt(29)\n\t" "s_waitcnt lgkmcnt(0)\n\t" "s_barrier\n\t" \
   "ds_write_b32 v21, a164 offset:0\n\t" "ds_write_b32 v21, a165 offset:21120\n\t" "ds_write_b32 v21, a166 offset:10560\n\t" \
   "ds_write_b32 v21, a167 offset:31680\n\t" "s_waitcnt vmcnt(28)\n\t" "ds_write_b32 v21, a180 offset:64\n\t" "ds_write_b32 v21, a181 offset:21184\n\t" \
   "ds_write_b32 v21, a182 offset:10624\n\t" "ds_write_b32 v21, a183 offset:31744\n\t" "s_waitcnt vmcnt(27)\n\t" \
   "ds_write_b32 v21, a196 offset:128\n\t" "ds_write_b32 v21, a197 offset:21248\n\t" "ds_write_b32 v21, a198 offset:10688\n\t" \
   "ds_write_b32 v21, a199 offset:31808\n\t" "s_waitcnt vmcnt(26)\n\t" "ds_write_b32 v21, a212 offset:192\n\t" \
   "ds_write_b32 v21, a213 offset:21312\n\t" "ds_write_b32 v21, a214 offset:10752\n\t" "ds_write_b32 v21, a215 offset:31872\n\t" \
   "s_waitcnt vmcnt(25)\n\t" "ds_write_b32 v21, a228 offset:256\n\t" "ds_write_b32 v21, a229 offset:21376\n\t" \
   "ds_write_b32 v21, a230 offset:10816\n\t" "ds_write_b32 v21, a231 offset:31936\n\t" "s_waitcnt vmcnt(24)\n\t" \
   "ds_write_b32 v21, a244 offset:5248\n\t" "ds_write_b32 v21, a245 offset:26368\n\t" "ds_write_b32 v21, a246 offset:15808\n\t" \
   "ds_write_b32 v21, a247 offset:36928\n\t" "s_waitcnt vmcnt(23)\n\t" "ds_write_b32 v21, v196 offset:5312\n\t" \
   "ds_write_b32 v21, v197 offset:26432\n\t" "ds_write_b32 v21, v198 offset:15872\n\t" "ds_write_b32 v21, v199 offset:36992\n\t" \
   "s_waitcnt vmcnt(22)\n\t" "ds_write_b32 v21, v212 offset:5376\n\t" "ds_write_b32 v21, v213 offset:26496\n\t" \
   "ds_write_b32 v21, v214 offset:15936\n\t" "ds_write_b32 v21, v215 offset:37056\n\t" "s_waitcnt vmcnt(21)\n\t" \
   "ds_write_b32 v21, v228 offset:5440\n\t" "ds_write_b32 v21, v229 offset:26560\n\t" "ds_write_b32 v21, v230 offset:16000\n\t" \
   "ds_write_b32 v21, v231 offset:37120\n\t" "s_waitcnt vmcnt(20)\n\t" "ds_write_b32 v21, v244 offset:5504\n\t" \
   "ds_write_b32 v21, v245 offset:26624\n\t" "ds_write_b32 v21, v246 offset:16064\n\t" "ds_write_b32 v21, v247 offset:37184\n\t" \
   "s_waitcnt lgkmcnt(0)\n\t" "s_barrier\n\t" "ds_read_b64_tr_b16 a[164:165], v22 offset:0\n\t" "ds_read_b64_tr_b16 a[166:167], v22 offset:5248\n\t" \
   "ds_read_b64_tr_b16 a[180:181], v22 offset:8\n\t" "ds_read_b64_tr_b16 a[182:183], v22 offset:5256\n\t" \
   "ds_read_b64_tr_b16 a[196:197], v22 offset:16\n\t" "ds_read_b64_tr_b16 a[198:199], v22 offset:5264\n\t" \
   "ds_read_b64_tr_b16 a[212:213], v22 offset:24\n\t" "ds_read_b64_tr_b16 a[214:215], v22 offset:5272\n\t" \
   "ds_read_b64_tr_b16 a[228:229], v22 offset:32\n\t" "ds_read_b64_tr_b16 a[230:231], v22 offset:5280\n\t" \
   "ds_read_b64_tr_b16 a[244:245], v22 offset:40\n\t" "ds_read_b64_tr_b16 a[246:247], v22 offset:5288\n\t" \
   "ds_read_b64_tr_b16 v[196:197], v22 offset:48\n\t" "ds_read_b64_tr_b16 v[198:199], v22 offset:5296\n\t" \
   "ds_read_b64_tr_b16 v[212:213], v22 offset:56\n\t" "ds_read_b64_tr_b16 v[214:215], v22 offset:5304\n\t" \
   "ds_read_b64_tr_b16 v[228:229], v22 offset:64\n\t" "ds_read_b64_tr_b16 v[230:231], v22 offset:5312\n\t" \
   "ds_read_b64_tr_b16 v[244:245], v22 offset:72\n\t" "ds_read_b64_tr_b16 v[246:247], v22 offset:5320\n\t" "s_waitcnt vmcnt(19)\n\t" \
   "s_waitcnt lgkmcnt(0)\n\t" "s_barrier\n\t" "ds_write_b32 v21, a168 offset:0\n\t" "ds_write_b32 v21, a169 offset:21120\n\t" \
   "ds_write_b32 v21, a170 offset:10560\n\t" "ds_write_b32 v21, a171 offset:31680\n\t" "s_waitcnt vmcnt(18)\n\t" "ds_write_b32 v21, a184 offset:64\n\t" \
   "ds_write_b32 v21, a185 offset:21184\n\t" "ds_write_b32 v21, a186 offset:10624\n\t" "ds_write_b32 v21, a187 offset:31744\n\t" \
   "s_waitcnt vmcnt(17)\n\t" "ds_write_b32 v21, a200 offset:128\n\t" "ds_write_b32 v21, a201 offset:21248\n\t" \
   "ds_write_b32 v21, a202 offset:10688\n\t" "ds_write_b32 v21, a203 offset:31808\n\t" "s_waitcnt vmcnt(16)\n\t" \
   "ds_write_b32 v21, a216 offset:192\n\t" "ds_write_b32 v21, a217 offset:21312\n\t" "ds_write_b32 v21, a218 offset:10752\n\t" \
   "ds_write_b32 v21, a219 offset:31872\n\t" "s_waitcnt vmcnt(15)\n\t" "ds_write_b32 v21, a232 offset:256\n\t" \
   "ds_write_b32 v21, a233 offset:21376\n\t" "ds_write_b32 v21, a234 offset:10816\n\t" "ds_write_b32 v21, a235 offset:31936\n\t" \
   "s_waitcnt vmcnt(14)\n\t" "ds_write_b32 v21, a248 offset:5248\n\t" "ds_write_b32 v21, a249 offset:26368\n\t" \
   "ds_write_b32 v21, a250 offset:15808\n\t" "ds_write_b32 v21, a251 offset:36928\n\t" "s_waitcnt vmcnt(13)\n\t" \
   "ds_write_b32 v21, v200 offset:5312\n\t" "ds_write_b32 v21, v201 offset:26432\n\t" "ds_write_b32 v21, v202 offset:15872\n\t" \
   "ds_write_b32 v21, v203 offset:36992\n\t" "s_waitcnt vmcnt(12)\n\t" "ds_write_b32 v21, v216 offset:5376\n\t" \
   "ds_write_b32 v21, v217 offset:26496\n\t" "ds_write_b32 v21, v218 offset:15936\n\t" "ds_write_b32 v21, v219 offset:37056\n\t" \
   "s_waitcnt vmcnt(11)\n\t" "ds_write_b32 v21, v232 offset:5440\n\t" "ds_write_b32 v21, v233 offset:26560\n\t" \
   "ds_write_b32 v21, v234 offset:16000\n\t" "ds_write_b32 v21, v235 offset:37120\n\t" "s_waitcnt vmcnt(10)\n\t" \
   "ds_write_b32 v21, v248 offset:5504\n\t" "ds_write_b32 v21, v249 offset:26624\n\t" "ds_write_b32 v21, v250 offset:16064\n\t" \
   "ds_write_b32 v21, v251 offset:37184\n\t" "s_waitcnt lgkmcnt(0)\n\t" "s_barrier\n\t" "ds_read_b64_tr_b16 a[168:169], v22 offset:0\n\t" \
   "ds_read_b64_tr_b16 a[170:171], v22 offset:5248\n\t" "ds_read_b64_tr_b16 a[184:185], v22 offset:8\n\t" \
   "ds_read_b64_tr_b16 a[186:187], v22 offset:5256\n\t" "ds_read_b64_tr_b16 a[200:201], v22 offset:16\n\t" \
   "ds_read_b64_tr_b16 a[202:203], v22 offset:5264\n\t" "ds_read_b64_tr_b16 a[216:217], v22 offset:24\n\t" \
   "ds_read_b64_tr_b16 a[218:219], v22 offset:5272\n\t" "ds_read_b64_tr_b16 a[232:233], v22 offset:32\n\t" \
   "ds_read_b64_tr_b16 a[234:235], v22 offset:5280\n\t" "ds_read_b64_tr_b16 a[248:249], v22 offset:40\n\t" \
   "ds_read_b64_tr_b16 a[250:251], v22 offset:5288\n\t" "ds_read_b64_tr_b16 v[200:201], v22 offset:48\n\t" \
   "ds_read_b64_tr_b16 v[202:203], v22 offset:5296\n\t" "ds_read_b64_tr_b16 v[216:217], v22 offset:56\n\t" \
   "ds_read_b64_tr_b16 v[218:219], v22 offset:5304\n\t" "ds_read_b64_tr_b16 v[232:233], v22 offset:64\n\t" \
   "ds_read_b64_tr_b16 v[234:235], v22 offset:5312\n\t" "ds_read_b64_tr_b16 v[248:249], v22 offset:72\n\t" \
   "ds_read_b64_tr_b16 v[250:251], v22 offset:5320\n\t" "s_waitcnt vmcnt(9)\n\t" "s_waitcnt lgkmcnt(0)\n\t" "s_barrier\n\t" \
   "ds_write_b32 v21, a172 offset:0\n\t" "ds_write_b32 v21, a173 offset:21120\n\t" "ds_write_b32 v21, a174 offset:10560\n\t" \
   "ds_write_b32 v21, a175 offset:31680\n\t" "s_waitcnt vmcnt(8)\n\t" "ds_write_b32 v21, a188 offset:64\n\t" "ds_write_b32 v21, a189 offset:21184\n\t" \
   "ds_write_b32 v21, a190 offset:10624\n\t" "ds_write_b32 v21, a191 offset:31744\n\t" "s_waitcnt vmcnt(7)\n\t" "ds_write_b32 v21, a204 offset:128\n\t" \
   "ds_write_b32 v21, a205 offset:21248\n\t" "ds_write_b32 v21, a206 offset:10688\n\t" "ds_write_b32 v21, a207 offset:31808\n\t" \
   "s_waitcnt vmcnt(6)\n\t" "ds_write_b32 v21, a220 offset:192\n\t" "ds_write_b32 v21, a221 offset:21312\n\t" "ds_write_b32 v21, a222 offset:10752\n\t" \
   "ds_write_b32 v21, a223 offset:31872\n\t" "s_waitcnt vmcnt(5)\n\t" "ds_write_b32 v21, a236 offset:256\n\t" "ds_write_b32 v21, a237 offset:21376\n\t" \
   "ds_write_b32 v21, a238 offset:10816\n\t" "ds_write_b32 v21, a239 offset:31936\n\t" "s_waitcnt vmcnt(4)\n\t" \
   "ds_write_b32 v21, a252 offset:5248\n\t" "ds_write_b32 v21, a253 offset:26368\n\t" "ds_write_b32 v21, a254 offset:15808\n\t" \
   "ds_write_b32 v21, a255 offset:36928\n\t" "s_waitcnt vmcnt(3)\n\t" "ds_write_b32 v21, v204 offset:5312\n\t" \
   "ds_write_b32 v21, v205 offset:26432\n\t" "ds_write_b32 v21, v206 offset:15872\n\t" "ds_write_b32 v21, v207 offset:36992\n\t" \
   "s_waitcnt vmcnt(2)\n\t" "ds_write_b32 v21, v220 offset:5376\n\t" "ds_write_b32 v21, v221 offset:26496\n\t" \
   "ds_write_b32 v21, v222 offset:15936\n\t" "ds_write_b32 v21, v223 offset:37056\n\t" "s_waitcnt vmcnt(1)\n\t" \
   "ds_write_b32 v21, v236 offset:5440\n\t" "ds_write_b32 v21, v237 offset:26560\n\t" "ds_write_b32 v21, v238 offset:16000\n\t" \
   "ds_write_b32 v21, v239 offset:37120\n\t" "s_waitcnt vmcnt(0)\n\t" "ds_write_b32 v21, v252 offset:5504\n\t" \
   "ds_write_b32 v21, v253 offset:26624\n\t" "ds_write_b32 v21, v254 offset:16064\n\t" "ds_write_b32 v21, v255 offset:37184\n\t" \
   "s_waitcnt lgkmcnt(0)\n\t" "s_barrier\n\t" "ds_read_b64_tr_b16 a[172:173], v22 offset:0\n\t" "ds_read_b64_tr_b16 a[174:175], v22 offset:5248\n\t" \
   "ds_read_b64_tr_b16 a[188:189], v22 offset:8\n\t" "ds_read_b64_tr_b16 a[190:191], v22 offset:5256\n\t" \
   "ds_read_b64_tr_b16 a[204:205], v22 offset:16\n\t" "ds_read_b64_tr_b16 a[206:207], v22 offset:5264\n\t" \
   "ds_read_b64_tr_b16 a[220:221], v22 offset:24\n\t" "ds_read_b64_tr_b16 a[222:223], v22 offset:5272\n\t" \
   "ds_read_b64_tr_b16 a[236:237], v22 offset:32\n\t" "ds_read_b64_tr_b16 a[238:239], v22 offset:5280\n\t" \
   "ds_read_b64_tr_b16 a[252:253], v22 offset:40\n\t" "ds_read_b64_tr_b16 a[254:255], v22 offset:5288\n\t" \
   "ds_read_b64_tr_b16 v[204:205], v22 offset:48\n\t" "ds_read_b64_tr_b16 v[206:207], v22 offset:5296\n\t" \
   "ds_read_b64_tr_b16 v[220:221], v22 offset:56\n\t" "ds_read_b64_tr_b16 v[222:223], v22 offset:5304\n\t" \
   "ds_read_b64_tr_b16 v[236:237], v22 offset:64\n\t" "ds_read_b64_tr_b16 v[238:239], v22 offset:5312\n\t" \
   "ds_read_b64_tr_b16 v[252:253], v22 offset:72\n\t" "ds_read_b64_tr_b16 v[254:255], v22 offset:5320\n\t" "s_waitcnt lgkmcnt(0)"
#define LDV_CLOB "v24", "v25", "s40", "s41"
#define LDY_ASM \
   "s_mov_b32 s41, 0\n\t" "buffer_load_dwordx4 a[0:3], v16, %[ra], s41 offen\n\t" "s_mov_b32 s41, 131072\n\t" \
   "buffer_load_dwordx4 a[64:67], v16, %[ra], s41 offen\n\t" "s_mov_b32 s41, 524288\n\t" "buffer_load_dwordx4 a[8:11], v16, %[ra], s41 offen\n\t" \
   "s_mov_b32 s41, 655360\n\t" "buffer_load_dwordx4 a[72:75], v16, %[ra], s41 offen\n\t" "s_mov_b32 s41, 1048576\n\t" \
   "buffer_load_dwordx4 a[16:19], v16, %[ra], s41 offen\n\t" "s_mov_b32 s41, 1179648\n\t" "buffer_load_dwordx4 a[80:83], v16, %[ra], s41 offen\n\t" \
   "s_mov_b32 s41, 1572864\n\t" "buffer_load_dwordx4 a[24:27], v16, %[ra], s41 offen\n\t" "s_mov_b32 s41, 1703936\n\t" \
   "buffer_load_dwordx4 a[88:91], v16, %[ra], s41 offen\n\t" "s_mov_b32 s41, 2097152\n\t" "buffer_load_dwordx4 a[32:35], v16, %[ra], s41 offen\n\t" \
   "s_mov_b32 s41, 2228224\n\t" "buffer_load_dwordx4 a[96:99], v16, %[ra], s41 offen\n\t" "s_mov_b32 s41, 2621440\n\t" \
   "buffer_load_dwordx4 a[40:43], v16, %[ra], s41 offen\n\t" "s_mov_b32 s41, 2752512\n\t" "buffer_load_dwordx4 a[104:107], v16, %[ra], s41 offen\n\t" \
   "s_mov_b32 s41, 3145728\n\t" "buffer_load_dwordx4 a[48:51], v16, %[ra], s41 offen\n\t" "s_mov_b32 s41, 3276800\n\t" \
   "buffer_load_dwordx4 a[112:115], v16, %[ra], s41 offen\n\t" "s_mov_b32 s41, 3670016\n\t" "buffer_load_dwordx4 a[56:59], v16, %[ra], s41 offen\n\t" \
   "s_mov_b32 s41, 3801088\n\t" "buffer_load_dwordx4 a[120:123], v16, %[ra], s41 offen\n\t" "s_mov_b32 s41, 4194304\n\t" \
   "buffer_load_dwordx4 v[192:195], v16, %[ra], s41 offen\n\t" "s_mov_b32 s41, 4325376\n\t" "buffer_load_dwordx4 v[208:211], v16, %[ra], s41 offen\n\t" \
   "s_mov_b32 s41, 4718592\n\t" "buffer_load_dwordx4 v[200:203], v16, %[ra], s41 offen\n\t" "s_mov_b32 s41, 4849664\n\t" \
   "buffer_load_dwordx4 v[216:219], v16, %[ra], s41 offen\n\t" "s_mov_b32 s41, 262144\n\t" "buffer_load_dwordx4 a[4:7], v16, %[ra], s41 offen\n\t" \
   "s_mov_b32 s41, 393216\n\t" "buffer_load_dwordx4 a[68:71], v16, %[ra], s41 offen\n\t" "s_mov_b32 s41, 786432\n\t" \
   "buffer_load_dwordx4 a[12:15], v16, %[ra], s41 offen\n\t" "s_mov_b32 s41, 917504\n\t" "buffer_load_dwordx4 a[76:79], v16, %[ra], s41 offen\n\t" \
   "s_mov_b32 s41, 1310720\n\t" "buffer_load_dwordx4 a[20:23], v16, %[ra], s41 offen\n\t" "s_mov_b32 s41, 1441792\n\t" \
   "buffer_load_dwordx4 a[84:87], v16, %[ra], s41 offen\n\t" "s_mov_b32 s41, 1835008\n\t" "buffer_load_dwordx4 a[28:31], v16, %[ra], s41 offen\n\t" \
   "s_mov_b32 s41, 1966080\n\t" "buffer_load_dwordx4 a[92:95], v16, %[ra], s41 offen\n\t" "s_mov_b32 s41, 2359296\n\t" \
   "buffer_load_dwordx4 a[36:39], v16, %[ra], s41 offen\n\t" "s_mov_b32 s41, 2490368\n\t" "buffer_load_dwordx4 a[100:103], v16, %[ra], s41 offen\n\t" \
   "s_mov_b32 s41, 2883584\n\t" "buffer_load_dwordx4 a[44:47], v16, %[ra], s41 offen\n\t" "s_mov_b32 s41, 3014656\n\t" \
   "buffer_load_dwordx4 a[108:111], v16, %[ra], s41 offen\n\t" "s_mov_b32 s41, 3407872\n\t" "buffer_load_dwordx4 a[52:55], v16, %[ra], s41 offen\n\t" \
   "s_mov_b32 s41, 3538944\n\t" "buffer_load_dwordx4 a[116:119], v16, %[ra], s41 offen\n\t" "s_mov_b32 s41, 3932160\n\t" \
   "buffer_load_dwordx4 a[60:63], v16, %[ra], s41 offen\n\t" "s_mov_b32 s41, 4063232\n\t" "buffer_load_dwordx4 a[124:127], v16, %[ra], s41 offen\n\t" \
   "s_mov_b32 s41, 4456448\n\t" "buffer_load_dwordx4 v[196:199], v16, %[ra], s41 offen\n\t" "s_mov_b32 s41, 4587520\n\t" \
   "buffer_load_dwordx4 v[212:215], v16, %[ra], s41 offen\n\t" "s_mov_b32 s41, 4980736\n\t" "buffer_load_dwordx4 v[204:207], v16, %[ra], s41 offen\n\t" \
   "s_mov_b32 s41, 5111808\n\t" "buffer_load_dwordx4 v[220:223], v16, %[ra], s41 offen\n\t" "s_mov_b32 s41, 0\n\t" \
   "buffer_load_dwordx4 a[128:131], v17, %[rb], s41 offen\n\t" "s_mov_b32 s41, 256\n\t" "buffer_load_dwordx4 a[132:135], v17, %[rb], s41 offen\n\t" \
   "s_mov_b32 s41, 512\n\t" "buffer_load_dwordx4 a[160:163], v17, %[rb], s41 offen\n\t" "s_mov_b32 s41, 768\n\t" \
   "buffer_load_dwordx4 a[164:167], v17, %[rb], s41 offen\n\t" "s_mov_b32 s41, 1024\n\t" "buffer_load_dwordx4 a[192:195], v17, %[rb], s41 offen\n\t" \
   "s_mov_b32 s41, 20480\n\t" "buffer_load_dwordx4 a[196:199], v17, %[rb], s41 offen\n\t" "s_mov_b32 s41, 20736\n\t" \
   "buffer_load_dwordx4 a[224:227], v17, %[rb], s41 offen\n\t" "s_mov_b32 s41, 20992\n\t" "buffer_load_dwordx4 a[228:231], v17, %[rb], s41 offen\n\t" \
   "s_mov_b32 s41, 21248\n\t" "buffer_load_dwordx4 v[224:227], v17, %[rb], s41 offen\n\t" "s_mov_b32 s41, 21504\n\t" \
   "buffer_load_dwordx4 v[228:231], v17, %[rb], s41 offen\n\t" "s_mov_b32 s41, 40960\n\t" "buffer_load_dwordx4 a[136:139], v17, %[rb], s41 offen\n\t" \
   "s_mov_b32 s41, 41216\n\t" "buffer_load_dwordx4 a[140:143], v17, %[rb], s41 offen\n\t" "s_mov_b32 s41, 41472\n\t" \
   "buffer_load_dwordx4 a[168:171], v17, %[rb], s41 offen\n\t" "s_mov_b32 s41, 41728\n\t" "buffer_load_dwordx4 a[172:175], v17, %[rb], s41 offen\n\t" \
   "s_mov_b32 s41, 41984\n\t" "buffer_load_dwordx4 a[200:203], v17, %[rb], s41 offen\n\t" "s_mov_b32 s41, 61440\n\t" \
   "buffer_load_dwordx4 a[204:207], v17, %[rb], s41 offen\n\t" "s_mov_b32 s41, 61696\n\t" "buffer_load_dwordx4 a[232:235], v17, %[rb], s41 offen\n\t" \
   "s_mov_b32 s41, 61952\n\t" "buffer_load_dwordx4 a[236:239], v17, %[rb], s41 offen\n\t" "s_mov_b32 s41, 62208\n\t" \
   "buffer_load_dwordx4 v[232:235], v17, %[rb], s41 offen\n\t" "s_mov_b32 s41, 62464\n\t" "buffer_load_dwordx4 v[236:239], v17, %[rb], s41 offen\n\t" \
   "s_waitcnt vmcnt(59)\n\t" "ds_write_b128 v20, a[0:3] offset:0\n\t" "s_mov_b32 s41, 81920\n\t" \
   "buffer_load_dwordx4 a[144:147], v17, %[rb], s41 offen\n\t" "s_waitcnt vmcnt(59)\n\t" "ds_write_b128 v20, a[64:67] offset:4096\n\t" \
   "s_mov_b32 s41, 82176\n\t" "buffer_load_dwordx4 a[148:151], v17, %[rb], s41 offen\n\t" "s_waitcnt vmcnt(59)\n\t" \
   "ds_write_b128 v20, a[8:11] offset:8192\n\t" "s_mov_b32 s41, 82432\n\t" "buffer_load_dwordx4 a[176:179], v17, %[rb], s41 offen\n\t" \
   "s_waitcnt vmcnt(59)\n\t" "ds_write_b128 v20, a[72:75] offset:12288\n\t" "s_mov_b32 s41, 82688\n\t" \
   "buffer_load_dwordx4 a[180:183], v17, %[rb], s41 offen\n\t" "s_waitcnt vmcnt(59)\n\t" "ds_write_b128 v20, a[16:19] offset:16384\n\t" \
   "s_mov_b32 s41, 82944\n\t" "buffer_load_dwordx4 a[208:211], v17, %[rb], s41 offen\n\t" "s_waitcnt vmcnt(59)\n\t" \
   "ds_write_b128 v20, a[80:83] offset:20480\n\t" "s_mov_b32 s41, 102400\n\t" "buffer_load_dwordx4 a[212:215], v17, %[rb], s41 offen\n\t" \
   "s_waitcnt vmcnt(59)\n\t" "ds_write_b128 v20, a[24:27] offset:24576\n\t" "s_mov_b32 s41, 102656\n\t" \
   "buffer_load_dwordx4 a[240:243], v17, %[rb], s41 offen\n\t" "s_waitcnt vmcnt(59)\n\t" "ds_write_b128 v20, a[88:91] offset:28672\n\t" \
   "s_mov_b32 s41, 102912\n\t" "buffer_load_dwordx4 a[244:247], v17, %[rb], s41 offen\n\t" "s_waitcnt vmcnt(59)\n\t" \
   "ds_write_b128 v20, a[32:35] offset:32768\n\t" "s_mov_b32 s41, 103168\n\t" "buffer_load_dwordx4 v[240:243], v17, %[rb], s41 offen\n\t" \
   "s_waitcnt vmcnt(59)\n\t" "ds_write_b128 v20, a[96:99] offset:36864\n\t" "s_mov_b32 s41, 103424\n\t" \
   "buffer_load_dwordx4 v[244:247], v17, %[rb], s41 offen\n\t" "s_waitcnt vmcnt(59)\n\t" "ds_write_b128 v20, a[40:43] offset:40960\n\t" \
   "s_mov_b32 s41, 122880\n\t" "buffer_load_dwordx4 a[152:155], v17, %[rb], s41 offen\n\t" "s_waitcnt vmcnt(59)\n\t" \
   "ds_write_b128 v20, a[104:107] offset:45056\n\t" "s_mov_b32 s41, 123136\n\t" "buffer_load_dwordx4 a[156:159], v17, %[rb], s41 offen\n\t" \
   "s_waitcnt vmcnt(59)\n\t" "ds_write_b128 v20, a[48:51] offset:49152\n\t" "s_mov_b32 s41, 123392\n\t" \
   "buffer_load_dwordx4 a[184:187], v17, %[rb], s41 offen\n\t" "s_waitcnt vmcnt(59)\n\t" "ds_write_b128 v20, a[112:115] offset:53248\n\t" \
   "s_mov_b32 s41, 123648\n\t" "buffer_load_dwordx4 a[188:191], v17, %[rb], s41 offen\n\t" "s_waitcnt vmcnt(59)\n\t" \
   "ds_write_b128 v20, a[56:59] offset:57344\n\t" "s_mov_b32 s41, 123904\n\t" "buffer_load_dwordx4 a[216:219], v17, %[rb], s41 offen\n\t" \
   "s_waitcnt vmcnt(59)\n\t" "ds_write_b128 v20, a[120:123] offset:61440\n\t" "s_mov_b32 s41, 143360\n\t" \
   "buffer_load_dwordx4 a[220:223], v17, %[rb], s41 offen\n\t" "s_waitcnt vmcnt(59)\n\t" "ds_write_b128 v23, v[192:195] offset:0\n\t" \
   "s_mov_b32 s41, 143616\n\t" "buffer_load_dwordx4 a[248:251], v17, %[rb], s41 offen\n\t" "s_waitcnt vmcnt(59)\n\t" \
   "ds_write_b128 v23, v[208:211] offset:4096\n\t" "s_mov_b32 s41, 143872\n\t" "buffer_load_dwordx4 a[252:255], v17, %[rb], s41 offen\n\t" \
   "s_waitcnt vmcnt(59)\n\t" "ds_write_b128 v23, v[200:203] offset:8192\n\t" "s_mov_b32 s41, 144128\n\t" \
   "buffer_load_dwordx4 v[248:251], v17, %[rb], s41 offen\n\t" "s_waitcnt vmcnt(59)\n\t" "ds_write_b128 v23, v[216:219] offset:12288\n\t" \
   "s_mov_b32 s41, 144384\n\t" "buffer_load_dwordx4 v[252:255], v17, %[rb], s41 offen\n\t" "s_waitcnt lgkmcnt(0)\n\t" "s_barrier\n\t" \
   "s_lshl_b32 s40, %[p0], 13\n\t" "v_add_u32 v24, s40, v18\n\t" "v_add_u32 v25, s40, v19\n\t" "s_add_u32 s40, s40, 8192\n\t" \
   "s_cmp_eq_u32 s40, 81920\n\t" "s_cselect_b32 s40, 0, s40\n\t" "ds_read_b64_tr_b16 a[0:1], v24 offset:0\n\t" \
   "ds_read_b64_tr_b16 a[2:3], v24 offset:4096\n\t" "ds_read_b64_tr_b16 a[64:65], v25 offset:0\n\t" "ds_read_b64_tr_b16 a[66:67], v25 offset:4096\n\t" \
   "v_add_u32 v24, s40, v18\n\t" "v_add_u32 v25, s40, v19\n\t" "s_add_u32 s40, s40, 8192\n\t" "s_cmp_eq_u32 s40, 81920\n\t" \
   "s_cselect_b32 s40, 0, s40\n\t" "ds_read_b64_tr_b16 a[8:9], v24 offset:0\n\t" "ds_read_b64_tr_b16 a[10:11], v24 offset:4096\n\t" \
   "ds_read_b64_tr_b16 a[72:73], v25 offset:0\n\t" "ds_read_b64_tr_b16 a[74:75], v25 offset:4096\n\t" "v_add_u32 v24, s40, v18\n\t" \
   "v_add_u32 v25, s40, v19\n\t" "s_add_u32 s40, s40, 8192\n\t" "s_cmp_eq_u32 s40, 81920\n\t" "s_cselect_b32 s40, 0, s40\n\t" \
   "ds_read_b64_tr_b16 a[16:17], v24 offset:0\n\t" "ds_read_b64_tr_b16 a[18:19], v24 offset:4096\n\t" "ds_read_b64_tr_b16 a[80:81], v25 offset:0\n\t" \
   "ds_read_b64_tr_b16 a[82:83], v25 offset:4096\n\t" "v_add_u32 v24, s40, v18\n\t" "v_add_u32 v25, s40, v19\n\t" "s_add_u32 s40, s40, 8192\n\t" \
   "s_cmp_eq_u32 s40, 81920\n\t" "s_cselect_b32 s40, 0, s40\n\t" "ds_read_b64_tr_b16 a[24:25], v24 offset:0\n\t" \
   "ds_read_b64_tr_b16 a[26:27], v24 offset:4096\n\t" "ds_read_b64_tr_b16 a[88:89], v25 offset:0\n\t" \
   "ds_read_b64_tr_b16 a[90:91], v25 offset:4096\n\t" "v_add_u32 v24, s40, v18\n\t" "v_add_u32 v25, s40, v19\n\t" "s_add_u32 s40, s40, 8192\n\t" \
   "s_cmp_eq_u32 s40, 81920\n\t" "s_cselect_b32 s40, 0, s40\n\t" "ds_read_b64_tr_b16 a[32:33], v24 offset:0\n\t" \
   "ds_read_b64_tr_b16 a[34:35], v24 offset:4096\n\t" "ds_read_b64_tr_b16 a[96:97], v25 offset:0\n\t" \
   "ds_read_b64_tr_b16 a[98:99], v25 offset:4096\n\t" "v_add_u32 v24, s40, v18\n\t" "v_add_u32 v25, s40, v19\n\t" "s_add_u32 s40, s40, 8192\n\t" \
   "s_cmp_eq_u32 s40, 81920\n\t" "s_cselect_b32 s40, 0, s40\n\t" "ds_read_b64_tr_b16 a[40:41], v24 offset:0\n\t" \
   "ds_read_b64_tr_b16 a[42:43], v24 offset:4096\n\t" "ds_read_b64_tr_b16 a[104:105], v25 offset:0\n\t" \
   "ds_read_b64_tr_b16 a[106:107], v25 offset:4096\n\t" "v_add_u32 v24, s40, v18\n\t" "v_add_u32 v25, s40, v19\n\t" "s_add_u32 s40, s40, 8192\n\t" \
   "s_cmp_eq_u32 s40, 81920\n\t" "s_cselect_b32 s40, 0, s40\n\t" "ds_read_b64_tr_b16 a[48:49], v24 offset:0\n\t" \
   "ds_read_b64_tr_b16 a[50:51], v24 offset:4096\n\t" "ds_read_b64_tr_b16 a[112:113], v25 offset:0\n\t" \
   "ds_read_b64_tr_b16 a[114:115], v25 offset:4096\n\t" "v_add_u32 v24, s40, v18\n\t" "v_add_u32 v25, s40, v19\n\t" "s_add_u32 s40, s40, 8192\n\t" \
   "s_cmp_eq_u32 s40, 81920\n\t" "s_cselect_b32 s40, 0, s40\n\t" "ds_read_b64_tr_b16 a[56:57], v24 offset:0\n\t" \
   "ds_read_b64_tr_b16 a[58:59], v24 offset:4096\n\t" "ds_read_b64_tr_b16 a[120:121], v25 offset:0\n\t" \
   "ds_read_b64_tr_b16 a[122:123], v25 offset:4096\n\t" "v_add_u32 v24, s40, v18\n\t" "v_add_u32 v25, s40, v19\n\t" "s_add_u32 s40, s40, 8192\n\t" \
   "s_cmp_eq_u32 s40, 81920\n\t" "s_cselect_b32 s40, 0, s40\n\t" "ds_read_b64_tr_b16 v[192:193], v24 offset:0\n\t" \
   "ds_read_b64_tr_b16 v[194:195], v24 offset:4096\n\t" "ds_read_b64_tr_b16 v[208:209], v25 offset:0\n\t" \
   "ds_read_b64_tr_b16 v[210:211], v25 offset:4096\n\t" "v_add_u32 v24, s40, v18\n\t" "v_add_u32 v25, s40, v19\n\t" "s_add_u32 s40, s40, 8192\n\t" \
   "s_cmp_eq_u32 s40, 81920\n\t" "s_cselect_b32 s40, 0, s40\n\t" "ds_read_b64_tr_b16 v[200:201], v24 offset:0\n\t" \
   "ds_read_b64_tr_b16 v[202:203], v24 offset:4096\n\t" "ds_read_b64_tr_b16 v[216:217], v25 offset:0\n\t" \
   "ds_read_b64_tr_b16 v[218:219], v25 offset:4096\n\t" "s_waitcnt vmcnt(59)\n\t" "s_waitcnt lgkmcnt(0)\n\t" "s_barrier\n\t" \
   "ds_write_b128 v20, a[4:7] offset:0\n\t" "s_waitcnt vmcnt(58)\n\t" "ds_write_b128 v20, a[68:71] offset:4096\n\t" "s_waitcnt vmcnt(57)\n\t" \
   "ds_write_b128 v20, a[12:15] offset:8192\n\t" "s_waitcnt vmcnt(56)\n\t" "ds_write_b128 v20, a[76:79] offset:12288\n\t" "s_waitcnt vmcnt(55)\n\t" \
   "ds_write_b128 v20, a[20:23] offset:16384\n\t" "s_waitcnt vmcnt(54)\n\t" "ds_write_b128 v20, a[84:87] offset:20480\n\t" "s_waitcnt vmcnt(53)\n\t" \
   "ds_write_b128 v20, a[28:31] offset:24576\n\t" "s_waitcnt vmcnt(52)\n\t" "ds_write_b128 v20, a[92:95] offset:28672\n\t" "s_waitcnt vmcnt(51)\n\t" \
   "ds_write_b128 v20, a[36:39] offset:32768\n\t" "s_waitcnt vmcnt(50)\n\t" "ds_write_b128 v20, a[100:103] offset:36864\n\t" "s_waitcnt vmcnt(49)\n\t" \
   "ds_write_b128 v20, a[44:47] offset:40960\n\t" "s_waitcnt vmcnt(48)\n\t" "ds_write_b128 v20, a[108:111] offset:45056\n\t" "s_waitcnt vmcnt(47)\n\t" \
   "ds_write_b128 v20, a[52:55] offset:49152\n\t" "s_waitcnt vmcnt(46)\n\t" "ds_write_b128 v20, a[116:119] offset:53248\n\t" "s_waitcnt vmcnt(45)\n\t" \
   "ds_write_b128 v20, a[60:63] offset:57344\n\t" "s_waitcnt vmcnt(44)\n\t" "ds_write_b128 v20, a[124:127] offset:61440\n\t" "s_waitcnt vmcnt(43)\n\t" \
   "ds_write_b128 v23, v[196:199] offset:0\n\t" "s_waitcnt vmcnt(42)\n\t" "ds_write_b128 v23, v[212:215] offset:4096\n\t" "s_waitcnt vmcnt(41)\n\t" \
   "ds_write_b128 v23, v[204:207] offset:8192\n\t" "s_waitcnt vmcnt(40)\n\t" "ds_write_b128 v23, v[220:223] offset:12288\n\t" \
   "s_waitcnt lgkmcnt(0)\n\t" "s_barrier\n\t" "s_lshl_b32 s40, %[p0], 13\n\t" "v_add_u32 v24, s40, v18\n\t" "v_add_u32 v25, s40, v19\n\t" \
   "s_add_u32 s40, s40, 8192\n\t" "s_cmp_eq_u32 s40, 81920\n\t" "s_cselect_b32 s40, 0, s40\n\t" "ds_read_b64_tr_b16 a[4:5], v24 offset:0\n\t" \
   "ds_read_b64_tr_b16 a[6:7], v24 offset:4096\n\t" "ds_read_b64_tr_b16 a[68:69], v25 offset:0\n\t" "ds_read_b64_tr_b16 a[70:71], v25 offset:4096\n\t" \
   "v_add_u32 v24, s40, v18\n\t" "v_add_u32 v25, s40, v19\n\t" "s_add_u32 s40, s40, 8192\n\t" "s_cmp_eq_u32 s40, 81920\n\t" \
   "s_cselect_b32 s40, 0, s40\n\t" "ds_read_b64_tr_b16 a[12:13], v24 offset:0\n\t" "ds_read_b64_tr_b16 a[14:15], v24 offset:4096\n\t" \
   "ds_read_b64_tr_b16 a[76:77], v25 offset:0\n\t" "ds_read_b64_tr_b16 a[78:79], v25 offset:4096\n\t" "v_add_u32 v24, s40, v18\n\t" \
   "v_add_u32 v25, s40, v19\n\t" "s_add_u32 s40, s40, 8192\n\t" "s_cmp_eq_u32 s40, 81920\n\t" "s_cselect_b32 s40, 0, s40\n\t" \
   "ds_read_b64_tr_b16 a[20:21], v24 offset:0\n\t" "ds_read_b64_tr_b16 a[22:23], v24 offset:4096\n\t" "ds_read_b64_tr_b16 a[84:85], v25 offset:0\n\t" \
   "ds_read_b64_tr_b16 a[86:87], v25 offset:4096\n\t" "v_add_u32 v24, s40, v18\n\t" "v_add_u32 v25, s40, v19\n\t" "s_add_u32 s40, s40, 8192\n\t" \
   "s_cmp_eq_u32 s40, 81920\n\t" "s_cselect_b32 s40, 0, s40\n\t" "ds_read_b64_tr_b16 a[28:29], v24 offset:0\n\t" \
   "ds_read_b64_tr_b16 a[30:31], v24 offset:4096\n\t" "ds_read_b64_tr_b16 a[92:93], v25 offset:0\n\t" \
   "ds_read_b64_tr_b16 a[94:95], v25 offset:4096\n\t" "v_add_u32 v24, s40, v18\n\t" "v_add_u32 v25, s40, v19\n\t" "s_add_u32 s40, s40, 8192\n\t" \
   "s_cmp_eq_u32 s40, 81920\n\t" "s_cselect_b32 s40, 0, s40\n\t" "ds_read_b64_tr_b16 a[36:37], v24 offset:0\n\t" \
   "ds_read_b64_tr_b16 a[38:39], v24 offset:4096\n\t" "ds_read_b64_tr_b16 a[100:101], v25 offset:0\n\t" \
   "ds_read_b64_tr_b16 a[102:103], v25 offset:4096\n\t" "v_add_u32 v24, s40, v18\n\t" "v_add_u32 v25, s40, v19\n\t" "s_add_u32 s40, s40, 8192\n\t" \
   "s_cmp_eq_u32 s40, 81920\n\t" "s_cselect_b32 s40, 0, s40\n\t" "ds_read_b64_tr_b16 a[44:45], v24 offset:0\n\t" \
   "ds_read_b64_tr_b16 a[46:47], v24 offset:4096\n\t" "ds_read_b64_tr_b16 a[108:109], v25 offset:0\n\t" \
   "ds_read_b64_tr_b16 a[110:111], v25 offset:4096\n\t" "v_add_u32 v24, s40, v18\n\t" "v_add_u32 v25, s40, v19\n\t" "s_add_u32 s40, s40, 8192\n\t" \
   "s_cmp_eq_u32 s40, 81920\n\t" "s_cselect_b32 s40, 0, s40\n\t" "ds_read_b64_tr_b16 a[52:53], v24 offset:0\n\t" \
   "ds_read_b64_tr_b16 a[54:55], v24 offset:4096\n\t" "ds_read_b64_tr_b16 a[116:117], v25 offset:0\n\t" \
   "ds_read_b64_tr_b16 a[118:119], v25 offset:4096\n\t" "v_add_u32 v24, s40, v18\n\t" "v_add_u32 v25, s40, v19\n\t" "s_add_u32 s40, s40, 8192\n\t" \
   "s_cmp_eq_u32 s40, 81920\n\t" "s_cselect_b32 s40, 0, s40\n\t" "ds_read_b64_tr_b16 a[60:61], v24 offset:0\n\t" \
   "ds_read_b64_tr_b16 a[62:63], v24 offset:4096\n\t" "ds_read_b64_tr_b16 a[124:125], v25 offset:0\n\t" \
   "ds_read_b64_tr_b16 a[126:127], v25 offset:4096\n\t" "v_add_u32 v24, s40, v18\n\t" "v_add_u32 v25, s40, v19\n\t" "s_add_u32 s40, s40, 8192\n\t" \
   "s_cmp_eq_u32 s40, 81920\n\t" "s_cselect_b32 s40, 0, s40\n\t" "ds_read_b64_tr_b16 v[196:197], v24 offset:0\n\t" \
   "ds_read_b64_tr_b16 v[198:199], v24 offset:4096\n\t" "ds_read_b64_tr_b16 v[212:213], v25 offset:0\n\t" \
   "ds_read_b64_tr_b16 v[214:215], v25 offset:4096\n\t" "v_add_u32 v24, s40, v18\n\t" "v_add_u32 v25, s40, v19\n\t" "s_add_u32 s40, s40, 8192\n\t" \
   "s_cmp_eq_u32 s40, 81920\n\t" "s_cselect_b32 s40, 0, s40\n\t" "ds_read_b64_tr_b16 v[204:205], v24 offset:0\n\t" \
   "ds_read_b64_tr_b16 v[206:207], v24 offset:4096\n\t" "ds_read_b64_tr_b16 v[220:221], v25 offset:0\n\t" \
   "ds_read_b64_tr_b16 v[222:223], v25 offset:4096\n\t" "s_waitcnt vmcnt(39)\n\t" "s_waitcnt lgkmcnt(0)\n\t" "s_barrier\n\t" \
   "ds_write_b32 v21, a128 offset:0\n\t" "ds_write_b32 v21, a129 offset:21120\n\t" "ds_write_b32 v21, a130 offset:10560\n\t" \
   "ds_write_b32 v21, a131 offset:31680\n\t" "s_waitcnt vmcnt(38)\n\t" "ds_write_b32 v21, a132 offset:64\n\t" "ds_write_b32 v21, a133 offset:21184\n\t" \
   "ds_write_b32 v21, a134 offset:10624\n\t" "ds_write_b32 v21, a135 offset:31744\n\t" "s_waitcnt vmcnt(37)\n\t" \
   "ds_write_b32 v21, a160 offset:128\n\t" "ds_write_b32 v21, a161 offset:21248\n\t" "ds_write_b32 v21, a162 offset:10688\n\t" \
   "ds_write_b32 v21, a163 offset:31808\n\t" "s_waitcnt vmcnt(36)\n\t" "ds_write_b32 v21, a164 offset:192\n\t" \
   "ds_write_b32 v21, a165 offset:21312\n\t" "ds_write_b32 v21, a166 offset:10752\n\t" "ds_write_b32 v21, a167 offset:31872\n\t" \
   "s_waitcnt vmcnt(35)\n\t" "ds_write_b32 v21, a192 offset:256\n\t" "ds_write_b32 v21, a193 offset:21376\n\t" \
   "ds_write_b32 v21, a194 offset:10816\n\t" "ds_write_b32 v21, a195 offset:31936\n\t" "s_waitcnt vmcnt(34)\n\t" \
   "ds_write_b32 v21, a196 offset:5248\n\t" "ds_write_b32 v21, a197 offset:26368\n\t" "ds_write_b32 v21, a198 offset:15808\n\t" \
   "ds_write_b32 v21, a199 offset:36928\n\t" "s_waitcnt vmcnt(33)\n\t" "ds_write_b32 v21, a224 offset:5312\n\t" \
   "ds_write_b32 v21, a225 offset:26432\n\t" "ds_write_b32 v21, a226 offset:15872\n\t" "ds_write_b32 v21, a227 offset:36992\n\t" \
   "s_waitcnt vmcnt(32)\n\t" "ds_write_b32 v21, a228 offset:5376\n\t" "ds_write_b32 v21, a229 offset:26496\n\t" \
   "ds_write_b32 v21, a230 offset:15936\n\t" "ds_write_b32 v21, a231 offset:37056\n\t" "s_waitcnt vmcnt(31)\n\t" \
   "ds_write_b32 v21, v224 offset:5440\n\t" "ds_write_b32 v21, v225 offset:26560\n\t" "ds_write_b32 v21, v226 offset:16000\n\t" \
   "ds_write_b32 v21, v227 offset:37120\n\t" "s_waitcnt vmcnt(30)\n\t" "ds_write_b32 v21, v228 offset:5504\n\t" \
   "ds_write_b32 v21, v229 offset:26624\n\t" "ds_write_b32 v21, v230 offset:16064\n\t" "ds_write_b32 v21, v231 offset:37184\n\t" \
   "s_waitcnt lgkmcnt(0)\n\t" "s_barrier\n\t" "ds_read_b64_tr_b16 a[128:129], v22 offset:0\n\t" "ds_read_b64_tr_b16 a[130:131], v22 offset:656\n\t" \
   "ds_read_b64_tr_b16 a[132:133], v22 offset:2624\n\t" "ds_read_b64_tr_b16 a[134:135], v22 offset:3280\n\t" \
   "ds_read_b64_tr_b16 a[160:161], v22 offset:16\n\t" "ds_read_b64_tr_b16 a[162:163], v22 offset:672\n\t" \
   "ds_read_b64_tr_b16 a[164:165], v22 offset:2640\n\t" "ds_read_b64_tr_b16 a[166:167], v22 offset:3296\n\t" \
   "ds_read_b64_tr_b16 a[192:193], v22 offset:32\n\t" "ds_read_b64_tr_b16 a[194:195], v22 offset:688\n\t" \
   "ds_read_b64_tr_b16 a[196:197], v22 offset:2656\n\t" "ds_read_b64_tr_b16 a[198:199], v22 offset:3312\n\t" \
   "ds_read_b64_tr_b16 a[224:225], v22 offset:48\n\t" "ds_read_b64_tr_b16 a[226:227], v22 offset:704\n\t" \
   "ds_read_b64_tr_b16 a[228:229], v22 offset:2672\n\t" "ds_read_b64_tr_b16 a[230:231], v22 offset:3328\n\t" \
   "ds_read_b64_tr_b16 v[224:225], v22 offset:64\n\t" "ds_read_b64_tr_b16 v[226:227], v22 offset:720\n\t" \
   "ds_read_b64_tr_b16 v[228:229], v22 offset:2688\n\t" "ds_read_b64_tr_b16 v[230:231], v22 offset:3344\n\t" "s_waitcnt vmcnt(29)\n\t" \
   "s_waitcnt lgkmcnt(0)\n\t" "s_barrier\n\t" "ds_write_b32 v21, a136 offset:0\n\t" "ds_write_b32 v21, a137 offset:21120\n\t" \
   "ds_write_b32 v21, a138 offset:10560\n\t" "ds_write_b32 v21, a139 offset:31680\n\t" "s_waitcnt vmcnt(28)\n\t" "ds_write_b32 v21, a140 offset:64\n\t" \
   "ds_write_b32 v21, a141 offset:21184\n\t" "ds_write_b32 v21, a142 offset:10624\n\t" "ds_write_b32 v21, a143 offset:31744\n\t" \
   "s_waitcnt vmcnt(27)\n\t" "ds_write_b32 v21, a168 offset:128\n\t" "ds_write_b32 v21, a169 offset:21248\n\t" \
   "ds_write_b32 v21, a170 offset:10688\n\t" "ds_write_b32 v21, a171 offset:31808\n\t" "s_waitcnt vmcnt(26)\n\t" \
   "ds_write_b32 v21, a172 offset:192\n\t" "ds_write_b32 v21, a173 offset:21312\n\t" "ds_write_b32 v21, a174 offset:10752\n\t" \
   "ds_write_b32 v21, a175 offset:31872\n\t" "s_waitcnt vmcnt(25)\n\t" "ds_write_b32 v21, a200 offset:256\n\t" \
   "ds_write_b32 v21, a201 offset:21376\n\t" "ds_write_b32 v21, a202 offset:10816\n\t" "ds_write_b32 v21, a203 offset:31936\n\t" \
   "s_waitcnt vmcnt(24)\n\t" "ds_write_b32 v21, a204 offset:5248\n\t" "ds_write_b32 v21, a205 offset:26368\n\t" \
   "ds_write_b32 v21, a206 offset:15808\n\t" "ds_write_b32 v21, a207 offset:36928\n\t" "s_waitcnt vmcnt(23)\n\t" \
   "ds_write_b32 v21, a232 offset:5312\n\t" "ds_write_b32 v21, a233 offset:26432\n\t" "ds_write_b32 v21, a234 offset:15872\n\t" \
   "ds_write_b32 v21, a235 offset:36992\n\t" "s_waitcnt vmcnt(22)\n\t" "ds_write_b32 v21, a236 offset:5376\n\t" \
   "ds_write_b32 v21, a237 offset:26496\n\t" "ds_write_b32 v21, a238 offset:15936\n\t" "ds_write_b32 v21, a239 offset:37056\n\t" \
   "s_waitcnt vmcnt(21)\n\t" "ds_write_b32 v21, v232 offset:5440\n\t" "ds_write_b32 v21, v233 offset:26560\n\t" \
   "ds_write_b32 v21, v234 offset:16000\n\t" "ds_write_b32 v21, v235 offset:37120\n\t" "s_waitcnt vmcnt(20)\n\t" \
   "ds_write_b32 v21, v236 offset:5504\n\t" "ds_write_b32 v21, v237 offset:26624\n\t" "ds_write_b32 v21, v238 offset:16064\n\t" \
   "ds_write_b32 v21, v239 offset:37184\n\t" "s_waitcnt lgkmcnt(0)\n\t" "s_barrier\n\t" "ds_read_b64_tr_b16 a[136:137], v22 offset:0\n\t" \
   "ds_read_b64_tr_b16 a[138:139], v22 offset:656\n\t" "ds_read_b64_tr_b16 a[140:141], v22 offset:2624\n\t" \
   "ds_read_b64_tr_b16 a[142:143], v22 offset:3280\n\t" "ds_read_b64_tr_b16 a[168:169], v22 offset:16\n\t" \
   "ds_read_b64_tr_b16 a[170:171], v22 offset:672\n\t" "ds_read_b64_tr_b16 a[172:173], v22 offset:2640\n\t" \
   "ds_read_b64_tr_b16 a[174:175], v22 offset:3296\n\t" "ds_read_b64_tr_b16 a[200:201], v22 offset:32\n\t" \
   "ds_read_b64_tr_b16 a[202:203], v22 offset:688\n\t" "ds_read_b64_tr_b16 a[204:205], v22 offset:2656\n\t" \
   "ds_read_b64_tr_b16 a[206:207], v22 offset:3312\n\t" "ds_read_b64_tr_b16 a[232:233], v22 offset:48\n\t" \
   "ds_read_b64_tr_b16 a[234:235], v22 offset:704\n\t" "ds_read_b64_tr_b16 a[236:237], v22 offset:2672\n\t" \
   "ds_read_b64_tr_b16 a[238:239], v22 offset:3328\n\t" "ds_read_b64_tr_b16 v[232:233], v22 offset:64\n\t" \
   "ds_read_b64_tr_b16 v[234:235], v22 offset:720\n\t" "ds_read_b64_tr_b16 v[236:237], v22 offset:2688\n\t" \
   "ds_read_b64_tr_b16 v[238:239], v22 offset:3344\n\t" "s_waitcnt vmcnt(19)\n\t" "s_waitcnt lgkmcnt(0)\n\t" "s_barrier\n\t" \
   "ds_write_b32 v21, a144 offset:0\n\t" "ds_write_b32 v21, a145 offset:21120\n\t" "ds_write_b32 v21, a146 offset:10560\n\t" \
   "ds_write_b32 v21, a147 offset:31680\n\t" "s_waitcnt vmcnt(18)\n\t" "ds_write_b32 v21, a148 offset:64\n\t" "ds_write_b32 v21, a149 offset:21184\n\t" \
   "ds_write_b32 v21, a150 offset:10624\n\t" "ds_write_b32 v21, a151 offset:31744\n\t" "s_waitcnt vmcnt(17)\n\t" \
   "ds_write_b32 v21, a176 offset:128\n\t" "ds_write_b32 v21, a177 offset:21248\n\t" "ds_write_b32 v21, a178 offset:10688\n\t" \
   "ds_write_b32 v21, a179 offset:31808\n\t" "s_waitcnt vmcnt(16)\n\t" "ds_write_b32 v21, a180 offset:192\n\t" \
   "ds_write_b32 v21, a181 offset:21312\n\t" "ds_write_b32 v21, a182 offset:10752\n\t" "ds_write_b32 v21, a183 offset:31872\n\t" \
   "s_waitcnt vmcnt(15)\n\t" "ds_write_b32 v21, a208 offset:256\n\t" "ds_write_b32 v21, a209 offset:21376\n\t" \
   "ds_write_b32 v21, a210 offset:10816\n\t" "ds_write_b32 v21, a211 offset:31936\n\t" "s_waitcnt vmcnt(14)\n\t" \
   "ds_write_b32 v21, a212 offset:5248\n\t" "ds_write_b32 v21, a213 offset:26368\n\t" "ds_write_b32 v21, a214 offset:15808\n\t" \
   "ds_write_b32 v21, a215 offset:36928\n\t" "s_waitcnt vmcnt(13)\n\t" "ds_write_b32 v21, a240 offset:5312\n\t" \
   "ds_write_b32 v21, a241 offset:26432\n\t" "ds_write_b32 v21, a242 offset:15872\n\t" "ds_write_b32 v21, a243 offset:36992\n\t" \
   "s_waitcnt vmcnt(12)\n\t" "ds_write_b32 v21, a244 offset:5376\n\t" "ds_write_b32 v21, a245 offset:26496\n\t" \
   "ds_write_b32 v21, a246 offset:15936\n\t" "ds_write_b32 v21, a247 offset:37056\n\t" "s_waitcnt vmcnt(11)\n\t" \
   "ds_write_b32 v21, v240 offset:5440\n\t" "ds_write_b32 v21, v241 offset:26560\n\t" "ds_write_b32 v21, v242 offset:16000\n\t" \
   "ds_write_b32 v21, v243 offset:37120\n\t" "s_waitcnt vmcnt(10)\n\t" "ds_write_b32 v21, v244 offset:5504\n\t" \
   "ds_write_b32 v21, v245 offset:26624\n\t" "ds_write_b32 v21, v246 offset:16064\n\t" "ds_write_b32 v21, v247 offset:37184\n\t" \
   "s_waitcnt lgkmcnt(0)\n\t" "s_barrier\n\t" "ds_read_b64_tr_b16 a[144:145], v22 offset:0\n\t" "ds_read_b64_tr_b16 a[146:147], v22 offset:656\n\t" \
   "ds_read_b64_tr_b16 a[148:149], v22 offset:2624\n\t" "ds_read_b64_tr_b16 a[150:151], v22 offset:3280\n\t" \
   "ds_read_b64_tr_b16 a[176:177], v22 offset:16\n\t" "ds_read_b64_tr_b16 a[178:179], v22 offset:672\n\t" \
   "ds_read_b64_tr_b16 a[180:181], v22 offset:2640\n\t" "ds_read_b64_tr_b16 a[182:183], v22 offset:3296\n\t" \
   "ds_read_b64_tr_b16 a[208:209], v22 offset:32\n\t" "ds_read_b64_tr_b16 a[210:211], v22 offset:688\n\t" \
   "ds_read_b64_tr_b16 a[212:213], v22 offset:2656\n\t" "ds_read_b64_tr_b16 a[214:215], v22 offset:3312\n\t" \
   "ds_read_b64_tr_b16 a[240:241], v22 offset:48\n\t" "ds_read_b64_tr_b16 a[242:243], v22 offset:704\n\t" \
   "ds_read_b64_tr_b16 a[244:245], v22 offset:2672\n\t" "ds_read_b64_tr_b16 a[246:247], v22 offset:3328\n\t" \
   "ds_read_b64_tr_b16 v[240:241], v22 offset:64\n\t" "ds_read_b64_tr_b16 v[242:243], v22 offset:720\n\t" \
   "ds_read_b64_tr_b16 v[244:245], v22 offset:2688\n\t" "ds_read_b64_tr_b16 v[246:247], v22 offset:3344\n\t" "s_waitcnt vmcnt(9)\n\t" \
   "s_waitcnt lgkmcnt(0)\n\t" "s_barrier\n\t" "ds_write_b32 v21, a152 offset:0\n\t" "ds_write_b32 v21, a153 offset:21120\n\t" \
   "ds_write_b32 v21, a154 offset:10560\n\t" "ds_write_b32 v21, a155 offset:31680\n\t" "s_waitcnt vmcnt(8)\n\t" "ds_write_b32 v21, a156 offset:64\n\t" \
   "ds_write_b32 v21, a157 offset:21184\n\t" "ds_write_b32 v21, a158 offset:10624\n\t" "ds_write_b32 v21, a159 offset:31744\n\t" \
   "s_waitcnt vmcnt(7)\n\t" "ds_write_b32 v21, a184 offset:128\n\t" "ds_write_b32 v21, a185 offset:21248\n\t" "ds_write_b32 v21, a186 offset:10688\n\t" \
   "ds_write_b32 v21, a187 offset:31808\n\t" "s_waitcnt vmcnt(6)\n\t" "ds_write_b32 v21, a188 offset:192\n\t" "ds_write_b32 v21, a189 offset:21312\n\t" \
   "ds_write_b32 v21, a190 offset:10752\n\t" "ds_write_b32 v21, a191 offset:31872\n\t" "s_waitcnt vmcnt(5)\n\t" "ds_write_b32 v21, a216 offset:256\n\t" \
   "ds_write_b32 v21, a217 offset:21376\n\t" "ds_write_b32 v21, a218 offset:10816\n\t" "ds_write_b32 v21, a219 offset:31936\n\t" \
   "s_waitcnt vmcnt(4)\n\t" "ds_write_b32 v21, a220 offset:5248\n\t" "ds_write_b32 v21, a221 offset:26368\n\t" \
   "ds_write_b32 v21, a222 offset:15808\n\t" "ds_write_b32 v21, a223 offset:36928\n\t" "s_waitcnt vmcnt(3)\n\t" \
   "ds_write_b32 v21, a248 offset:5312\n\t" "ds_write_b32 v21, a249 offset:26432\n\t" "ds_write_b32 v21, a250 offset:15872\n\t" \
   "ds_write_b32 v21, a251 offset:36992\n\t" "s_waitcnt vmcnt(2)\n\t" "ds_write_b32 v21, a252 offset:5376\n\t" \
   "ds_write_b32 v21, a253 offset:26496\n\t" "ds_write_b32 v21, a254 offset:15936\n\t" "ds_write_b32 v21, a255 offset:37056\n\t" \
   "s_waitcnt vmcnt(1)\n\t" "ds_write_b32 v21, v248 offset:5440\n\t" "ds_write_b32 v21, v249 offset:26560\n\t" \
   "ds_write_b32 v21, v250 offset:16000\n\t" "ds_write_b32 v21, v251 offset:37120\n\t" "s_waitcnt vmcnt(0)\n\t" \
   "ds_write_b32 v21, v252 offset:5504\n\t" "ds_write_b32 v21, v253 offset:26624\n\t" "ds_write_b32 v21, v254 offset:16064\n\t" \
   "ds_write_b32 v21, v255 offset:37184\n\t" "s_waitcnt lgkmcnt(0)\n\t" "s_barrier\n\t" "ds_read_b64_tr_b16 a[152:153], v22 offset:0\n\t" \
   "ds_read_b64_tr_b16 a[154:155], v22 offset:656\n\t" "ds_read_b64_tr_b16 a[156:157], v22 offset:2624\n\t" \
   "ds_read_b64_tr_b16 a[158:159], v22 offset:3280\n\t" "ds_read_b64_tr_b16 a[184:185], v22 offset:16\n\t" \
   "ds_read_b64_tr_b16 a[186:187], v22 offset:672\n\t" "ds_read_b64_tr_b16 a[188:189], v22 offset:2640\n\t" \
   "ds_read_b64_tr_b16 a[190:191], v22 offset:3296\n\t" "ds_read_b64_tr_b16 a[216:217], v22 offset:32\n\t" \
   "ds_read_b64_tr_b16 a[218:219], v22 offset:688\n\t" "ds_read_b64_tr_b16 a[220:221], v22 offset:2656\n\t" \
   "ds_read_b64_tr_b16 a[222:223], v22 offset:3312\n\t" "ds_read_b64_tr_b16 a[248:249], v22 offset:48\n\t" \
   "ds_read_b64_tr_b16 a[250:251], v22 offset:704\n\t" "ds_read_b64_tr_b16 a[252:253], v22 offset:2672\n\t" \
   "ds_read_b64_tr_b16 a[254:255], v22 offset:3328\n\t" "ds_read_b64_tr_b16 v[248:249], v22 offset:64\n\t" \
   "ds_read_b64_tr_b16 v[250:251], v22 offset:720\n\t" "ds_read_b64_tr_b16 v[252:253], v22 offset:2688\n\t" \
   "ds_read_b64_tr_b16 v[254:255], v22 offset:3344\n\t" "s_waitcnt lgkmcnt(0)"
#define LDY_CLOB "v24", "v25", "s40", "s41"
__device__ __forceinline__ void load_rows(v32u (&w)[10], int kind, const u16* A, const u16* B, int h, int wv, unsigned slot) {
  const int l = lane_id(), i = l & 15, g = l >> 4;
  const v4u ra = make_rs(A, 640u * 8192u), rb = make_rs(B, 640u * 8192u);
  v8u ad = (unsigned)l;
  const int wr = 4 * wv + g, rr = 4 * g + (i >> 2);
  ad[0] = wr * 8192 + h * 256 + i * 16;
  ad[1] = (h * 128 + wr) * 1280 + i * 16;
  ad[2] = slot + rr * 256 + ((2 * wv) ^ (rr & 7)) * 32 + (i & 3) * 8;
  ad[3] = ad[2] ^ 32u;
  ad[4] = slot + wr * 256 + ((i >> 1) ^ (wr & 7)) * 32 + (i & 1) * 16;
  ad[5] = slot + wr * 328 + ((i >> 2) * 2 + (i & 1)) * 8 + ((i >> 1) & 1) * 4;
  ad[6] = slot + (kind == 3 ? (16 * (g & 1) + 4 * (g >> 1) + ((i >> 2) & 1)) * 328 + (i >> 3) * 8 : rr * 328) + (i & 3) * 10560 + wv * 80;
  ad[7] = ad[4] + 65536u;
#define LD_OPS                                                                                                                            \
  : "={a[0:31]}"(w[0]), "={a[32:63]}"(w[1]), "={a[64:95]}"(w[2]), "={a[96:127]}"(w[3]), "={a[128:159]}"(w[4]), "={a[160:191]}"(w[5]),     \
    "={a[192:223]}"(w[6]), "={a[224:255]}"(w[7]), "={v[192:223]}"(w[8]), "={v[224:255]}"(w[9])                                            \
  : "{v[16:23]}"(ad), [ra] "s"(ra), [rb] "s"(rb), [p0] "s"(wv < 3 ? 3 * wv : 8)                                                          \
  : LDP_CLOB, "scc", "memory"
  if (kind == 0) asm volatile(LDP_ASM LD_OPS);
  else if (kind == 1) asm volatile(LDK_ASM LD_OPS);
  else if (kind == 2) asm volatile(LDV_ASM LD_OPS);
  else asm volatile(LDY_ASM LD_OPS);
}
#define W_OPS "{a[0:31]}"(w[0]), "{a[32:63]}"(w[1]), "{a[64:95]}"(w[2]), "{a[96:127]}"(w[3]), "{a[128:159]}"(w[4]), "{a[160:191]}"(w[5]), \
              "{a[192:223]}"(w[6]), "{a[224:255]}"(w[7]), "{v[192:223]}"(w[8]), "{v[224:255]}"(w[9])

__device__ __forceinline__ int mem_lane(int lane) { return lane * 16; }
__device__ __forceinline__ int pair_of(int g) { return (g & 1) * 2 + (g >> 1); }

__device__ __forceinline__ void rec_p1(int wv_, const v32u (&w)[10], unsigned char* ws, const Pool& pool, int layer, int h, int xs, RecTune tn, unsigned long long* stamps) {
  const Clk0 c0 = clk_sample();
  const unsigned lds0 = (unsigned)(uintptr_t)(__attribute__((address_space(3))) unsigned char*)dyn_lds;
  const int lane = lane_id(), wv = wv_, g = lane >> 4, r = lane & 15;
  const v4u rs = make_rs(ws, (unsigned)OFF_END);
  const int p0 = wv < 3 ? 3 * wv : 8;
  v16u ad, la = (unsigned)lane;
  const int kqo = (int)(wv < 2 ? OFF_KF : OFF_QF) + ((h * RT + (wv & 1)) * 4 * 64 + lane) * 16;
  const int progo = (wv == 0 && lane == 0) ? prog_off(layer, 0, h) : OOB;
  const unsigned lfs = lds0 + L_CL + wv * 512;
  {
    const int p2 = wv < 3 ? p0 + 2 : 0;
    ad[0] = W_MEM + (p0 >> 1) * WIN + (p0 & 1) * 1024 + mem_lane(lane);
    ad[1] = W_MEM + ((p0 + 1) >> 1) * WIN + ((p0 + 1) & 1) * 1024 + mem_lane(lane);
    ad[2] = W_MEM + (p2 >> 1) * WIN + (p2 & 1) * 1024 + mem_lane(lane);
    ad[3] = lane == 0 ? W_MISC + h * 64 + wv * 4 : OOB;
    ad[4] = copy_off(wv) + (h >> 2) * WIN + (h & 3) * 512 + (g >> 1) * 256 + (2 * (r & 7) + (r >> 3)) * 16 + (g & 1) * 8;
    ad[5] = g < 2 ? copy_off(wv + 4 * (r >> 3)) + (8 + (h >> 4)) * WIN + ((h >> 3) & 1) * 1024 + (((h >> 1) & 3) * 16 + (h & 1) * 8 + (r & 7)) * 16 + g * 8 : OOB;
    ad[6] = (r & 1) ? 0xEEEEu : 0x4444u;
    ad[7] = lds0 + (32 * wv + r) * 16 + g * 4;
    ad[8] = lds0 + (4 * g + (r >> 2)) * 16 + (r & 1) * 8;
    ad[9] = lds0 + L_M2 + wv * 3072 + lane * 16;
    ad[10] = kqo;
    ad[11] = M_TM + xs * 128 + (lane & 1) * 12;
    ad[12] = kqf_off(layer, 0, h) + (lane & 3) * 4;
    ad[13] = wv > 1 ? (int)OFF_PXF + ((h * 2 + (wv & 1)) * 64 + lane) * 16 : OOB;
    ad[14] = (g < 2 && wv > 1) ? (int)OFF_PXM + (((h >> 2) * 2 + (wv & 1)) * 64 + (h & 3) * 16 + r) * 16 + g * 8 : OOB;
    ad[15] = progo;
#pragma unroll
    for (int i = 0; i < 10; ++i) { int p = p0 + i; p = p >= 10 ? p - 10 : p; la[i] = lds0 + p * 1024 + lane * 16; }
    la[10] = lds0 + L_KQ + lane * 16;
    la[11] = lfs;
    la[12] = lds0 + REC_ST + wv * 8;
    asm volatile("v_mov_b32 %0, 0xd028965f" : "=v"(la[13]));
    la[14] = 16 * (wv & 1) + r - 4 * g;
    la[15] = lds0 + L_KQ + (wv & 1) * 4096 + lane * 16;
  }
  __builtin_amdgcn_s_waitcnt(0x0F70);
  if (layer) __builtin_amdgcn_s_sleep(64);
  v4u clk = clk_init(c0);
  unsigned long long stat;
  asm volatile(P1_ASM
               : "=&{s[76:77]}"(stat), "+{s[56:59]}"(clk)
               : W_OPS, "{v[128:143]}"(ad), "{v[112:127]}"(la), [rs] "s"(rs), [sel] "s"(pool.sel), [pool] "s"(pool.base), [xs] "s"(xs),
                 [lad0] "s"(ladder(xs, xs & 1)), [s3] "s"(pool.s3), [m1] "s"(tn.m1), [l2s] "s"(lds0 + L_M2 + wv * 3072),
                 [hi8] "s"(0xFF00FF00FF00FF00ull), [lfs] "s"(lfs), [wv] "s"(wv), [kf0] "s"((int)OFF_KF + h * RT * 4096),
                 [esc] "s"((int)OFF_FWD - W_MEM)
               : P1_CLOB RST_CLOB, "vcc", "scc", "m0", "memory");
  LDSBAR();
  {
    const unsigned one = 1u;
    const int so = (NSEG - 1) * 4096;
    VM_ST32(one, progo, so);
    VM_WAIT(0);
  }
}

__device__ __forceinline__ void rec_pv(int wv_, const v32u (&w)[10], unsigned char* ws, const Pool& pool, int layer, int h, int xs, RecTune tn, unsigned long long* stamps) {
  const Clk0 c0 = clk_sample();
  const unsigned lds0 = (unsigned)(uintptr_t)(__attribute__((address_space(3))) unsigned char*)dyn_lds;
  const int lane = lane_id(), wv = wv_, g = lane >> 4, r = lane & 15;
  const v4u rs = make_rs(ws, (unsigned)OFF_END);
  const int p0 = wv < 3 ? 3 * wv : 8, p2 = wv < 3 ? p0 + 2 : 0;
  v16u ad = (unsigned)lane, la = (unsigned)lane;
  ad[0] = W_MEM + (p0 >> 1) * WIN + (p0 & 1) * 1024 + mem_lane(lane);
  ad[1] = W_MEM + ((p0 + 1) >> 1) * WIN + ((p0 + 1) & 1) * 1024 + mem_lane(lane);
  ad[2] = W_MEM + (p2 >> 1) * WIN + (p2 & 1) * 1024 + mem_lane(lane);
  ad[3] = (r & 8) ? 0xEEEEu : 0x4444u;
  ad[4] = (wv * 10 + (g >> 1)) * 16384 + h * 256 + r * 16 + (g & 1) * 8;
  ad[5] = h * 16384 + wv * 2048 + lane * 16;
  ad[6] = (r & 1) ? 0xEEEEu : 0x4444u;
  ad[7] = lds0 + (32 * wv + r) * 16 + pair_of(g) * 4;
  ad[8] = lds0 + (4 * g + (r >> 2)) * 16 + (r & 1) * 8;
  ad[11] = M_TM + (4 + xs) * 128 + (lane & 1) * 12;
  ad[15] = (wv == 0 && lane == 0) ? prog_off(layer, 0, 32 + h) : OOB;
#pragma unroll
  for (int i = 0; i < 10; ++i) { int p = p0 + i; p = p >= 10 ? p - 10 : p; la[i] = lds0 + p * 1024 + lane * 16; }
  la[12] = lds0 + REC_ST + wv * 8;
  __builtin_amdgcn_s_waitcnt(0x0F70);
  if (layer) __builtin_amdgcn_s_sleep(64);
  v4u clk = clk_init(c0);
  unsigned long long stat;
  asm volatile(PV_ASM
               : "=&{s[76:77]}"(stat), "+{s[56:59]}"(clk)
               : W_OPS, "{v[128:143]}"(ad), "{v[112:127]}"(la), [rs] "s"(rs), [sel] "s"(pool.sel), [pool] "s"(pool.base), [xs] "s"(xs),
                 [lad0] "s"(ladder(xs, xs & 1)), [s3] "s"(pool.s3), [m1c] "s"(tn.m1 + tn.c2), [esc] "s"((int)OFF_FWD - W_MEM),
                 [ysel] "s"(pool.ysel), [yb] "s"(pool.base + P_Y)
               : PV_CLOB RST_CLOB, "vcc", "scc", "memory");
}

#define TSTART 4400
#define TD1 1000u
#define TR2 470u
#define TEMA 1
#define TEMB 2
#define TBUP 48
#define TBDN 6
#define TLATE 520
#define TSEC 50
__device__ __forceinline__ unsigned track_dur(unsigned e, int m, int rob, int s) {
  const int lo = (int)e - (s < 8 ? 64 : (rob & 255)), hi = (int)e + (rob >> 8);
  if (s >= 8) m = (int)e + ((m - (int)e + (m > (int)e ? (1 << TEMA) - 1 : 0)) >> (m > (int)e ? TEMA : TEMB));
  return (unsigned)min(max(m, lo), hi);
}
#define TTS 200
#define TTU 240
__device__ __forceinline__ void rec_sched(unsigned char* ws, const Pool& pool, int layer, int c16, int xs, RecTune tn, unsigned long long* stamps) {
  const Clk0 c0 = clk_sample();
  const unsigned lds0 = (unsigned)(uintptr_t)(__attribute__((address_space(3))) unsigned char*)dyn_lds;
  const int lane = lane_id();
  const v4u rs = make_rs(ws, (unsigned)OFF_END);
  unsigned long long stat = 0;
  const int sm = M_STAMP + min(lane, 39) * 64, sp = W_MISC + (lane & 31) * 64;
  const int yo = M_TM + xs * 128, oc = (c16 == 0 && lane < 16) ? M_TM + lane * 128 : OOB;
  const unsigned cv = flag_finish<2>(rs, kqf_off(layer, 0, 32 + (lane & 31)), 0xFFFFFFFFu);
  v4u clk = clk_init(c0);
  PH(3 + layer * 6)
  unsigned r2a = TR2, d1a = TD1, dd = TD1 + tn.m2, tp = 0, tm = 0, anchor = clk.x - dd;
  unsigned rr = (unsigned)min(max(wave_imax((int)(cv * 8u - clk.x)) + TSTART, 300), 4000);
  int bonus = 0;
  bool clean = false;
#pragma unroll 1
  for (int s = 0; s < NSEG - 1; ++s) {
    const int rgm = pool.region((s + NSEG - 1) & (NSEG - 1)), rg = pool.region(s);
    const unsigned slot = lds0 + L_T + (s & 1) * 32, ao = anchor;
    unsigned b2 = 1u;
    v4u b1 = {1u, 1u, 1u, 1u};
    if (s > 0) {
      asm volatile(WAIT_ASM : "+{s[56:59]}"(clk) : [ta] "s"(tm + TTS) : WAIT_CLOB, "scc", "memory");
      VM_LD32(b2, sm, rgm);
      VM_LD128(b1, sp, rgm, 0);
      asm volatile("s_waitcnt vmcnt(0)" : "+v"(b2), "+v"(b1)::"memory");
      anchor = tm;
      if (__builtin_expect(__any(b2 == 0xFFFFFFFFu || (int)(b2 - (tm + tn.m1 - tn.vis)) > 0), 0)) {
        STAT(3);
        for (unsigned spins = 0; __any(b2 == 0xFFFFFFFFu) && spins < SPIN_CAP; ++spins) {
          __builtin_amdgcn_s_sleep(2);
          VM_LD32(b2, sm, rgm);
          VM_WAIT1(0, b2);
        }
        const int d = wave_imax((int)(b2 - tm)) + tn.vis - tn.m1;
        anchor = tm + (d <= 0 ? 0 : (d <= tn.dlt ? tn.dlt + TSEC : d + TLATE));
      }
    }
    const unsigned tpo = tp, tmo = tm;
    tp = anchor + dd;
    tm = nosent(tp + rr);
    LDS_V4(slot) = v4u{tp, tm, (unsigned)(s + 1), anchor};
    if (anchor != tmo && s > 2) bonus = TBUP; else bonus = max(bonus - TBDN, 0);
    dd = d1a + tn.m2 + bonus;
    rr = r2a + bonus;
    {
      const v4u wq = v4u{tm, dd, rr, dd + rr};
      VM_ST128(wq, oc, rg);
    }
    if (s > 1 && !__any((b2 & 1u) != 0u)) r2a = track_dur(r2a, wave_imax((int)(b2 - tpo)), tn.rob, s);
    if (s > 1 && clean && !__any(((b1.x | b1.y | b1.z | b1.w) & 1u) != 0u))
      d1a = track_dur(d1a, wave_imax(max(max((int)(b1.x - ao), (int)(b1.y - ao)), max((int)(b1.z - ao), (int)(b1.w - ao)))), tn.rob, s);
    clean = anchor == tmo;
    while ((int)(tp + 40u - rtc() * 8u) > 0 && (int)(tp - rtc() * 8u) < 32000) __builtin_amdgcn_s_sleep(1);
    v4u lw;
    VM_LD128(lw, yo, rg, 0);
    VM_WAIT1(0, lw);
    const unsigned l0 = (unsigned)__builtin_amdgcn_readfirstlane(lw.x), l1 = (unsigned)__builtin_amdgcn_readfirstlane(lw.y),
                   l2 = (unsigned)__builtin_amdgcn_readfirstlane(lw.z);
    if (max(max(l0, l1), l2) != 0xFFFFFFFFu) { tm = l0; dd = l1; rr = l2; }
  }
}

__device__ __forceinline__ void rec_fin(unsigned char* ws, const Pool& pool, int layer, int c16, int xs, RecTune tn, unsigned long long* stamps) {
  const Clk0 c0 = clk_sample();
  const unsigned lds0 = (unsigned)(uintptr_t)(__attribute__((address_space(3))) unsigned char*)dyn_lds;
  const int lane = lane_id(), g = lane >> 4, r = lane & 15;
  const v4u rs = make_rs(ws, (unsigned)OFF_END);
  v8u ad;
  ad[0] = lane * 16;
  ad[1] = g * 256 + r * 8;
  ad[2] = (r & 1) ? 0x03020706u : 0x05040100u;
  ad[3] = (lane & 31) * 64;
  ad[4] = W_MEM + copy_off(((r >> 1) & 1) * 5) + (c16 >> 3) * WIN + (c16 & 7) * 256 + (2 * (2 * g + (r & 1)) + (r >> 3)) * 16 + ((r >> 2) & 1) * 8;
  ad[5] = lane == 0 ? M_STAMP + c16 * 64 : OOB;
  ad[6] = (r & 1) ? 0xEEEEu : 0x4444u;
  ad[7] = lds0 + L_T;
  __builtin_amdgcn_s_sleep(64);
  v4u clk = clk_init(c0);
  unsigned long long stat;
  asm volatile(FIN_ASM
               : "=&{s[76:77]}"(stat), "+{s[56:59]}"(clk), "+{v[16:23]}"(ad)
               : [rs] "s"(rs), [sel] "s"(pool.sel), [pool] "s"(pool.base), [xs] "s"(xs), [lad0] "s"(ladder(xs, xs & 1)), [s3] "s"(pool.s3),
                 [su] "s"((int)OFF_UF + c16 * 32768), [yb] "s"(pool.base + P_Y + c16 * 16384), [ysel] "s"(pool.ysel), [tu] "s"(TTU),
                 [stl] "s"(lds0 + REC_ST - 32), [uf] "s"(kqf_off(layer, 0, 32))
               : FIN_CLOB RST_CLOB, "vcc", "scc", "memory");
}

constexpr int PJ_BUF = 43 * 1024, PJ_X = 16384, PJ_LDS = PJ_X + 3 * PJ_BUF;
#define KQ_ASM \
   "s_mov_b32 s44, %[rdy]\n\t" "s_mov_b32 s45, 0\n\t" "s_mov_b32 s46, 0\n\t" "s_cmp_eq_u32 s44, -1\n\t" "s_cbranch_scc1 68f\n\t" \
   "s_mul_i32 s52, s45, 1536\n\t" "s_mul_i32 s51, s45, 40960\n\t" "s_add_u32 m0, s46, %[ldx]\n\t" "s_mov_b32 s53, s46\n\t" \
   "buffer_load_dwordx4 v16, %[rs], s51 offen offset:0 sc1 lds\n\t" "buffer_load_dwordx4 v16, %[rs], s51 offen offset:1024 sc1 lds\n\t" \
   "buffer_load_dwordx4 v16, %[rs], s51 offen offset:2048 sc1 lds\n\t" "buffer_load_dwordx4 v16, %[rs], s51 offen offset:3072 sc1 lds\n\t" \
   "s_add_u32 m0, m0, 4096\n\t" "s_add_u32 s51, s51, 4096\n\t" "buffer_load_dwordx4 v16, %[rs], s51 offen offset:0 sc1 lds\n\t" \
   "buffer_load_dwordx4 v16, %[rs], s51 offen offset:1024 sc1 lds\n\t" "buffer_load_dwordx4 v16, %[rs], s51 offen offset:2048 sc1 lds\n\t" \
   "buffer_load_dwordx4 v16, %[rs], s51 offen offset:3072 sc1 lds\n\t" "s_add_u32 m0, m0, 4096\n\t" "s_add_u32 s51, s51, 4096\n\t" \
   "buffer_load_dwordx4 v16, %[rs], s51 offen offset:0 sc1 lds\n\t" "buffer_load_dwordx4 v16, %[rs], s51 offen offset:1024 sc1 lds\n\t" \
   "s_add_u32 m0, s53, %[ld1]\n\t" "s_nop 0\n\t" "buffer_load_dwordx4 v17, %[rs], s52 offen sc1 lds\n\t" "s_mov_b32 s45, 1\n\t" \
   "s_mov_b32 s46, 44032\n\t" "s_mul_i32 s52, s45, 1536\n\t" "s_mul_i32 s51, s45, 40960\n\t" "s_add_u32 m0, s46, %[ldx]\n\t" "s_mov_b32 s53, s46\n\t" \
   "buffer_load_dwordx4 v16, %[rs], s51 offen offset:0 sc1 lds\n\t" "buffer_load_dwordx4 v16, %[rs], s51 offen offset:1024 sc1 lds\n\t" \
   "buffer_load_dwordx4 v16, %[rs], s51 offen offset:2048 sc1 lds\n\t" "buffer_load_dwordx4 v16, %[rs], s51 offen offset:3072 sc1 lds\n\t" \
   "s_add_u32 m0, m0, 4096\n\t" "s_add_u32 s51, s51, 4096\n\t" "buffer_load_dwordx4 v16, %[rs], s51 offen offset:0 sc1 lds\n\t" \
   "buffer_load_dwordx4 v16, %[rs], s51 offen offset:1024 sc1 lds\n\t" "buffer_load_dwordx4 v16, %[rs], s51 offen offset:2048 sc1 lds\n\t" \
   "buffer_load_dwordx4 v16, %[rs], s51 offen offset:3072 sc1 lds\n\t" "s_add_u32 m0, m0, 4096\n\t" "s_add_u32 s51, s51, 4096\n\t" \
   "buffer_load_dwordx4 v16, %[rs], s51 offen offset:0 sc1 lds\n\t" "buffer_load_dwordx4 v16, %[rs], s51 offen offset:1024 sc1 lds\n\t" \
   "s_add_u32 m0, s53, %[ld1]\n\t" "s_nop 0\n\t" "buffer_load_dwordx4 v17, %[rs], s52 offen sc1 lds\n\t" "68:\n\t" "v_mov_b32 v80, 0x3f803f80\n\t" \
   "v_mov_b32 v81, 0x3f803f80\n\t" "v_mov_b32 v82, 0x3f803f80\n\t" "v_mov_b32 v83, 0x3f803f80\n\t" "v_mov_b32 v31, 1\n\t" "s_nop 1\n\t" \
   "v_mfma_f32_16x16x32_bf16 v[32:35], a[0:3], v[80:83], 0\n\t" "v_mfma_f32_16x16x32_bf16 v[36:39], a[4:7], v[80:83], 0\n\t" \
   "v_mfma_f32_16x16x32_bf16 v[40:43], a[8:11], v[80:83], 0\n\t" "v_mfma_f32_16x16x32_bf16 v[44:47], a[12:15], v[80:83], 0\n\t" \
   "v_mfma_f32_16x16x32_bf16 v[32:35], a[16:19], v[80:83], v[32:35]\n\t" "v_mfma_f32_16x16x32_bf16 v[36:39], a[20:23], v[80:83], v[36:39]\n\t" \
   "v_mfma_f32_16x16x32_bf16 v[40:43], a[24:27], v[80:83], v[40:43]\n\t" "v_mfma_f32_16x16x32_bf16 v[44:47], a[28:31], v[80:83], v[44:47]\n\t" \
   "v_mfma_f32_16x16x32_bf16 v[32:35], a[32:35], v[80:83], v[32:35]\n\t" "v_mfma_f32_16x16x32_bf16 v[36:39], a[36:39], v[80:83], v[36:39]\n\t" \
   "v_mfma_f32_16x16x32_bf16 v[40:43], a[40:43], v[80:83], v[40:43]\n\t" "v_mfma_f32_16x16x32_bf16 v[44:47], a[44:47], v[80:83], v[44:47]\n\t" \
   "v_mfma_f32_16x16x32_bf16 v[32:35], a[48:51], v[80:83], v[32:35]\n\t" "v_mfma_f32_16x16x32_bf16 v[36:39], a[52:55], v[80:83], v[36:39]\n\t" \
   "v_mfma_f32_16x16x32_bf16 v[40:43], a[56:59], v[80:83], v[40:43]\n\t" "v_mfma_f32_16x16x32_bf16 v[44:47], a[60:63], v[80:83], v[44:47]\n\t" \
   "v_mfma_f32_16x16x32_bf16 v[32:35], a[64:67], v[80:83], v[32:35]\n\t" "v_mfma_f32_16x16x32_bf16 v[36:39], a[68:71], v[80:83], v[36:39]\n\t" \
   "v_mfma_f32_16x16x32_bf16 v[40:43], a[72:75], v[80:83], v[40:43]\n\t" "v_mfma_f32_16x16x32_bf16 v[44:47], a[76:79], v[80:83], v[44:47]\n\t" \
   "v_mfma_f32_16x16x32_bf16 v[32:35], a[80:83], v[80:83], v[32:35]\n\t" "v_mfma_f32_16x16x32_bf16 v[36:39], a[84:87], v[80:83], v[36:39]\n\t" \
   "v_mfma_f32_16x16x32_bf16 v[40:43], a[88:91], v[80:83], v[40:43]\n\t" "v_mfma_f32_16x16x32_bf16 v[44:47], a[92:95], v[80:83], v[44:47]\n\t" \
   "v_mfma_f32_16x16x32_bf16 v[32:35], a[96:99], v[80:83], v[32:35]\n\t" "v_mfma_f32_16x16x32_bf16 v[36:39], a[100:103], v[80:83], v[36:39]\n\t" \
   "v_mfma_f32_16x16x32_bf16 v[40:43], a[104:107], v[80:83], v[40:43]\n\t" "v_mfma_f32_16x16x32_bf16 v[44:47], a[108:111], v[80:83], v[44:47]\n\t" \
   "v_mfma_f32_16x16x32_bf16 v[32:35], a[112:115], v[80:83], v[32:35]\n\t" "v_mfma_f32_16x16x32_bf16 v[36:39], a[116:119], v[80:83], v[36:39]\n\t" \
   "v_mfma_f32_16x16x32_bf16 v[40:43], a[120:123], v[80:83], v[40:43]\n\t" "v_mfma_f32_16x16x32_bf16 v[44:47], a[124:127], v[80:83], v[44:47]\n\t" \
   "v_mfma_f32_16x16x32_bf16 v[32:35], a[128:131], v[80:83], v[32:35]\n\t" "v_mfma_f32_16x16x32_bf16 v[36:39], a[132:135], v[80:83], v[36:39]\n\t" \
   "v_mfma_f32_16x16x32_bf16 v[40:43], a[136:139], v[80:83], v[40:43]\n\t" "v_mfma_f32_16x16x32_bf16 v[44:47], a[140:143], v[80:83], v[44:47]\n\t" \
   "v_mfma_f32_16x16x32_bf16 v[32:35], a[144:147], v[80:83], v[32:35]\n\t" "v_mfma_f32_16x16x32_bf16 v[36:39], a[148:151], v[80:83], v[36:39]\n\t" \
   "v_mfma_f32_16x16x32_bf16 v[40:43], a[152:155], v[80:83], v[40:43]\n\t" "v_mfma_f32_16x16x32_bf16 v[44:47], a[156:159], v[80:83], v[44:47]\n\t" \
   "v_mfma_f32_16x16x32_bf16 v[32:35], a[160:163], v[80:83], v[32:35]\n\t" "v_mfma_f32_16x16x32_bf16 v[36:39], a[164:167], v[80:83], v[36:39]\n\t" \
   "v_mfma_f32_16x16x32_bf16 v[40:43], a[168:171], v[80:83], v[40:43]\n\t" "v_mfma_f32_16x16x32_bf16 v[44:47], a[172:175], v[80:83], v[44:47]\n\t" \
   "v_mfma_f32_16x16x32_bf16 v[32:35], a[176:179], v[80:83], v[32:35]\n\t" "v_mfma_f32_16x16x32_bf16 v[36:39], a[180:183], v[80:83], v[36:39]\n\t" \
   "v_mfma_f32_16x16x32_bf16 v[40:43], a[184:187], v[80:83], v[40:43]\n\t" "v_mfma_f32_16x16x32_bf16 v[44:47], a[188:191], v[80:83], v[44:47]\n\t" \
   "v_mfma_f32_16x16x32_bf16 v[32:35], a[192:195], v[80:83], v[32:35]\n\t" "v_mfma_f32_16x16x32_bf16 v[36:39], a[196:199], v[80:83], v[36:39]\n\t" \
   "v_mfma_f32_16x16x32_bf16 v[40:43], a[200:203], v[80:83], v[40:43]\n\t" "v_mfma_f32_16x16x32_bf16 v[44:47], a[204:207], v[80:83], v[44:47]\n\t" \
   "v_mfma_f32_16x16x32_bf16 v[32:35], a[208:211], v[80:83], v[32:35]\n\t" "v_mfma_f32_16x16x32_bf16 v[36:39], a[212:215], v[80:83], v[36:39]\n\t" \
   "v_mfma_f32_16x16x32_bf16 v[40:43], a[216:219], v[80:83], v[40:43]\n\t" "v_mfma_f32_16x16x32_bf16 v[44:47], a[220:223], v[80:83], v[44:47]\n\t" \
   "v_mfma_f32_16x16x32_bf16 v[32:35], a[224:227], v[80:83], v[32:35]\n\t" "v_mfma_f32_16x16x32_bf16 v[36:39], a[228:231], v[80:83], v[36:39]\n\t" \
   "v_mfma_f32_16x16x32_bf16 v[40:43], a[232:235], v[80:83], v[40:43]\n\t" "v_mfma_f32_16x16x32_bf16 v[44:47], a[236:239], v[80:83], v[44:47]\n\t" \
   "v_mfma_f32_16x16x32_bf16 v[32:35], a[240:243], v[80:83], v[32:35]\n\t" "v_mfma_f32_16x16x32_bf16 v[36:39], a[244:247], v[80:83], v[36:39]\n\t" \
   "v_mfma_f32_16x16x32_bf16 v[40:43], a[248:251], v[80:83], v[40:43]\n\t" "v_mfma_f32_16x16x32_bf16 v[44:47], a[252:255], v[80:83], v[44:47]\n\t" \
   "v_mfma_f32_16x16x32_bf16 v[32:35], v[192:195], v[80:83], v[32:35]\n\t" "v_mfma_f32_16x16x32_bf16 v[36:39], v[196:199], v[80:83], v[36:39]\n\t" \
   "v_mfma_f32_16x16x32_bf16 v[40:43], v[200:203], v[80:83], v[40:43]\n\t" "v_mfma_f32_16x16x32_bf16 v[44:47], v[204:207], v[80:83], v[44:47]\n\t" \
   "v_mfma_f32_16x16x32_bf16 v[32:35], v[208:211], v[80:83], v[32:35]\n\t" "v_mfma_f32_16x16x32_bf16 v[36:39], v[212:215], v[80:83], v[36:39]\n\t" \
   "v_mfma_f32_16x16x32_bf16 v[40:43], v[216:219], v[80:83], v[40:43]\n\t" "v_mfma_f32_16x16x32_bf16 v[44:47], v[220:223], v[80:83], v[44:47]\n\t" \
   "v_mfma_f32_16x16x32_bf16 v[32:35], v[224:227], v[80:83], v[32:35]\n\t" "v_mfma_f32_16x16x32_bf16 v[36:39], v[228:231], v[80:83], v[36:39]\n\t" \
   "v_mfma_f32_16x16x32_bf16 v[40:43], v[232:235], v[80:83], v[40:43]\n\t" "v_mfma_f32_16x16x32_bf16 v[44:47], v[236:239], v[80:83], v[44:47]\n\t" \
   "v_mfma_f32_16x16x32_bf16 v[32:35], v[240:243], v[80:83], v[32:35]\n\t" "v_mfma_f32_16x16x32_bf16 v[36:39], v[244:247], v[80:83], v[36:39]\n\t" \
   "v_mfma_f32_16x16x32_bf16 v[40:43], v[248:251], v[80:83], v[40:43]\n\t" "v_mfma_f32_16x16x32_bf16 v[44:47], v[252:255], v[80:83], v[44:47]\n\t" \
   "s_mov_b32 s40, 0\n\t" "s_mov_b32 s41, 0\n\t" "s_mov_b32 s42, 0\n\t" "s_mov_b32 s43, 44032\n\t" "v_mov_b32 v28, v22\n\t" "v_mov_b32 v29, v23\n\t" \
   "s_mov_b32 s45, 2\n\t" "s_mov_b32 s46, 88064\n\t" "s_waitcnt vmcnt(11)\n\t" "s_barrier\n\t" "s_lshr_b32 s48, s45, 1\n\t" \
   "s_bitcmp1_b32 s44, s48\n\t" "s_cbranch_scc0 172f\n\t" "72:\n\t" "s_mul_i32 s52, s45, 1536\n\t" "s_mul_i32 s51, s45, 40960\n\t" \
   "s_add_u32 m0, s46, %[ldx]\n\t" "s_mov_b32 s53, s46\n\t" "buffer_load_dwordx4 v16, %[rs], s51 offen offset:0 sc1 lds\n\t" \
   "ds_read_b128 v[80:83], v28 offset:0\n\t" "ds_read_b128 v[84:87], v28 offset:1024\n\t" "ds_read_b128 v[88:91], v28 offset:2048\n\t" \
   "ds_read_b128 v[92:95], v28 offset:3072\n\t" "1:\n\t" "v_mov_b32 v26, v28\n\t" "v_mov_b32 v27, v29\n\t" "s_waitcnt lgkmcnt(2)\n\t" \
   "v_mfma_f32_16x16x32_bf16 v[48:51], a[0:3], v[80:83], 0\n\t" "ds_read_b128 v[112:115], v27 offset:0\n\t" \
   "v_mfma_f32_16x16x32_bf16 v[52:55], a[4:7], v[80:83], 0\n\t" "ds_read_b128 v[96:99], v26 offset:4096\n\t" \
   "v_mfma_f32_16x16x32_bf16 v[56:59], a[8:11], v[80:83], 0\n\t" "ds_read_b128 v[116:119], v27 offset:16\n\t" \
   "v_mfma_f32_16x16x32_bf16 v[60:63], a[12:15], v[80:83], 0\n\t" "v_fmac_f32 v64, v32, v12\n\t" "v_fmac_f32 v65, v33, v12\n\t" \
   "v_mfma_f32_16x16x32_bf16 v[48:51], a[16:19], v[84:87], v[48:51]\n\t" "v_fmac_f32 v66, v34, v12\n\t" "v_fmac_f32 v67, v35, v12\n\t" \
   "v_mfma_f32_16x16x32_bf16 v[52:55], a[20:23], v[84:87], v[52:55]\n\t" "ds_read_b128 v[100:103], v26 offset:5120\n\t" \
   "v_mfma_f32_16x16x32_bf16 v[56:59], a[24:27], v[84:87], v[56:59]\n\t" "v_fmac_f32 v68, v36, v12\n\t" "v_fmac_f32 v69, v37, v12\n\t" \
   "v_mfma_f32_16x16x32_bf16 v[60:63], a[28:31], v[84:87], v[60:63]\n\t" "ds_read_b64 v[120:121], v27 offset:32\n\t" "s_waitcnt lgkmcnt(5)\n\t" \
   "v_mfma_f32_16x16x32_bf16 v[48:51], a[32:35], v[88:91], v[48:51]\n\t" "ds_read_b128 v[122:125], v27 offset:768\n\t" \
   "v_mfma_f32_16x16x32_bf16 v[52:55], a[36:39], v[88:91], v[52:55]\n\t" "ds_read_b128 v[104:107], v26 offset:6144\n\t" \
   "ds_read_b128 v[126:129], v27 offset:784\n\t" "v_mfma_f32_16x16x32_bf16 v[56:59], a[40:43], v[88:91], v[56:59]\n\t" \
   "ds_read_b64 v[130:131], v27 offset:800\n\t" "v_mfma_f32_16x16x32_bf16 v[60:63], a[44:47], v[88:91], v[60:63]\n\t" "v_fmac_f32 v70, v38, v12\n\t" \
   "v_fmac_f32 v71, v39, v12\n\t" "v_mfma_f32_16x16x32_bf16 v[48:51], a[48:51], v[92:95], v[48:51]\n\t" "v_fmac_f32 v72, v40, v12\n\t" \
   "v_fmac_f32 v73, v41, v12\n\t" "v_mfma_f32_16x16x32_bf16 v[52:55], a[52:55], v[92:95], v[52:55]\n\t" "ds_read_b128 v[108:111], v26 offset:7168\n\t" \
   "v_mfma_f32_16x16x32_bf16 v[56:59], a[56:59], v[92:95], v[56:59]\n\t" "v_fmac_f32 v74, v42, v12\n\t" "v_fmac_f32 v75, v43, v12\n\t" \
   "v_mfma_f32_16x16x32_bf16 v[60:63], a[60:63], v[92:95], v[60:63]\n\t" "v_fmac_f32 v76, v44, v12\n\t" "s_waitcnt lgkmcnt(6)\n\t" \
   "v_mfma_f32_16x16x32_bf16 v[48:51], a[64:67], v[96:99], v[48:51]\n\t" "v_fmac_f32 v77, v45, v12\n\t" "v_fmac_f32 v78, v46, v12\n\t" \
   "v_mfma_f32_16x16x32_bf16 v[52:55], a[68:71], v[96:99], v[52:55]\n\t" "ds_read_b128 v[80:83], v26 offset:8192\n\t" \
   "v_mfma_f32_16x16x32_bf16 v[56:59], a[72:75], v[96:99], v[56:59]\n\t" "v_fmac_f32 v79, v47, v12\n\t" "v_mul_f32 v64, v64, v132\n\t" \
   "v_mfma_f32_16x16x32_bf16 v[60:63], a[76:79], v[96:99], v[60:63]\n\t" "v_mul_f32 v65, v65, v132\n\t" "v_mul_f32 v66, v66, v132\n\t" \
   "v_mfma_f32_16x16x32_bf16 v[48:51], a[80:83], v[100:103], v[48:51]\n\t" "v_mul_f32 v67, v67, v132\n\t" "v_mul_f32 v68, v68, v132\n\t" \
   "v_mfma_f32_16x16x32_bf16 v[52:55], a[84:87], v[100:103], v[52:55]\n\t" "ds_read_b128 v[84:87], v26 offset:9216\n\t" \
   "v_mfma_f32_16x16x32_bf16 v[56:59], a[88:91], v[100:103], v[56:59]\n\t" "v_mul_f32 v69, v69, v132\n\t" "v_mul_f32 v70, v70, v132\n\t" \
   "v_mfma_f32_16x16x32_bf16 v[60:63], a[92:95], v[100:103], v[60:63]\n\t" "v_mul_f32 v71, v71, v132\n\t" "s_waitcnt lgkmcnt(2)\n\t" \
   "v_mfma_f32_16x16x32_bf16 v[48:51], a[96:99], v[104:107], v[48:51]\n\t" "v_mul_f32 v72, v72, v132\n\t" "v_mul_f32 v73, v73, v132\n\t" \
   "v_mfma_f32_16x16x32_bf16 v[52:55], a[100:103], v[104:107], v[52:55]\n\t" "ds_read_b128 v[88:91], v26 offset:10240\n\t" \
   "v_mfma_f32_16x16x32_bf16 v[56:59], a[104:107], v[104:107], v[56:59]\n\t" "v_mul_f32 v74, v74, v132\n\t" "v_mul_f32 v75, v75, v132\n\t" \
   "v_mfma_f32_16x16x32_bf16 v[60:63], a[108:111], v[104:107], v[60:63]\n\t" "v_mul_f32 v76, v76, v132\n\t" "v_mul_f32 v77, v77, v132\n\t" \
   "v_mfma_f32_16x16x32_bf16 v[48:51], a[112:115], v[108:111], v[48:51]\n\t" "v_mul_f32 v78, v78, v132\n\t" "v_mul_f32 v79, v79, v132\n\t" \
   "v_mfma_f32_16x16x32_bf16 v[52:55], a[116:119], v[108:111], v[52:55]\n\t" "ds_read_b128 v[92:95], v26 offset:11264\n\t" \
   "v_mfma_f32_16x16x32_bf16 v[56:59], a[120:123], v[108:111], v[56:59]\n\t" "v_cvt_pk_bf16_f32 v136, v64, v65\n\t" \
   "v_cvt_pk_bf16_f32 v137, v66, v67\n\t" "v_mfma_f32_16x16x32_bf16 v[60:63], a[124:127], v[108:111], v[60:63]\n\t" \
   "v_cvt_pk_bf16_f32 v138, v68, v69\n\t" "s_waitcnt lgkmcnt(2)\n\t" "v_mfma_f32_16x16x32_bf16 v[48:51], a[128:131], v[80:83], v[48:51]\n\t" \
   "v_cvt_pk_bf16_f32 v139, v70, v71\n\t" "v_cvt_pk_bf16_f32 v140, v72, v73\n\t" \
   "v_mfma_f32_16x16x32_bf16 v[52:55], a[132:135], v[80:83], v[52:55]\n\t" "ds_read_b128 v[96:99], v26 offset:12288\n\t" \
   "v_mfma_f32_16x16x32_bf16 v[56:59], a[136:139], v[80:83], v[56:59]\n\t" "v_cvt_pk_bf16_f32 v141, v74, v75\n\t" \
   "v_cvt_pk_bf16_f32 v142, v76, v77\n\t" "v_mfma_f32_16x16x32_bf16 v[60:63], a[140:143], v[80:83], v[60:63]\n\t" \
   "v_cvt_pk_bf16_f32 v143, v78, v79\n\t" "s_lshl_b32 s47, s41, 13\n\t" "v_mfma_f32_16x16x32_bf16 v[48:51], a[144:147], v[84:87], v[48:51]\n\t" \
   "s_add_i32 s47, s47, -4096\n\t" "s_add_u32 s54, s41, 1\n\t" "v_mfma_f32_16x16x32_bf16 v[52:55], a[148:151], v[84:87], v[52:55]\n\t" \
   "ds_read_b128 v[100:103], v26 offset:13312\n\t" "v_mfma_f32_16x16x32_bf16 v[56:59], a[152:155], v[84:87], v[56:59]\n\t" "s_max_i32 s47, s47, 0\n\t" \
   "s_lshr_b64 s[60:61], %[sel], s54\n\t" "v_mfma_f32_16x16x32_bf16 v[60:63], a[156:159], v[84:87], v[60:63]\n\t" "s_cmp_eq_u32 s40, 0\n\t" \
   "s_cselect_b32 s48, 0x7ffffff0, 0\n\t" "s_lshl_b32 s55, s54, 19\n\t" "s_waitcnt lgkmcnt(2)\n\t" \
   "v_mfma_f32_16x16x32_bf16 v[48:51], a[160:163], v[88:91], v[48:51]\n\t" "s_and_b32 s60, s60, 1\n\t" "v_or_b32 v30, s48, v21\n\t" \
   "s_add_u32 s55, s55, %[pool]\n\t" "v_mfma_f32_16x16x32_bf16 v[52:55], a[164:167], v[88:91], v[52:55]\n\t" \
   "ds_read_b128 v[104:107], v26 offset:14336\n\t" "s_add_u32 s48, s47, %[qf]\n\t" "s_lshl_b32 s60, s60, 13\n\t" \
   "buffer_store_dwordx4 v[136:139], v30, %[rs], s48 offen sc1\n\t" "v_mfma_f32_16x16x32_bf16 v[56:59], a[168:171], v[88:91], v[56:59]\n\t" \
   "s_add_u32 s55, s55, s60\n\t" "s_add_u32 s48, s47, %[kf]\n\t" "s_bitcmp1_b64 %[skip], s41\n\t" "s_cselect_b32 s58, 0, %[size]\n\t" \
   "v_mfma_f32_16x16x32_bf16 v[60:63], a[172:175], v[88:91], v[60:63]\n\t" "buffer_store_dwordx4 v[140:143], v30, %[rs], s48 offen sc1\n\t" \
   "v_mfma_f32_16x16x32_bf16 v[48:51], a[176:179], v[92:95], v[48:51]\n\t" "v_add_f32 v112, v112, v114\n\t" \
   "v_mfma_f32_16x16x32_bf16 v[52:55], a[180:183], v[92:95], v[52:55]\n\t" "ds_read_b128 v[108:111], v26 offset:15360\n\t" \
   "v_mfma_f32_16x16x32_bf16 v[56:59], a[184:187], v[92:95], v[56:59]\n\t" "v_add_f32 v113, v113, v115\n\t" "v_add_f32 v116, v116, v118\n\t" \
   "v_mfma_f32_16x16x32_bf16 v[60:63], a[188:191], v[92:95], v[60:63]\n\t" \
   "buffer_store_dwordx4 v[156:159], v152, s[56:59], s55 offen offset:0 sc1\n\t" "s_waitcnt lgkmcnt(2)\n\t" \
   "v_mfma_f32_16x16x32_bf16 v[48:51], a[192:195], v[96:99], v[48:51]\n\t" "v_add_f32 v117, v117, v119\n\t" "v_add_f32 v112, v112, v120\n\t" \
   "v_mfma_f32_16x16x32_bf16 v[52:55], a[196:199], v[96:99], v[52:55]\n\t" "ds_read_b128 v[80:83], v26 offset:16384\n\t" \
   "v_mfma_f32_16x16x32_bf16 v[56:59], a[200:203], v[96:99], v[56:59]\n\t" "v_add_f32 v113, v113, v121\n\t" "v_add_f32 v112, v112, v116\n\t" \
   "v_mfma_f32_16x16x32_bf16 v[60:63], a[204:207], v[96:99], v[60:63]\n\t" "v_add_f32 v113, v113, v117\n\t" "v_mul_f32 v112, 0xbacccccd, v112\n\t" \
   "v_mfma_f32_16x16x32_bf16 v[48:51], a[208:211], v[100:103], v[48:51]\n\t" \
   "buffer_store_dwordx4 v[156:159], v152, s[56:59], s55 offen offset:1024 sc1\n\t" \
   "v_mfma_f32_16x16x32_bf16 v[52:55], a[212:215], v[100:103], v[52:55]\n\t" "ds_read_b128 v[84:87], v26 offset:17408\n\t" \
   "v_mfma_f32_16x16x32_bf16 v[56:59], a[216:219], v[100:103], v[56:59]\n\t" "v_mul_f32 v113, 0x3acccccd, v113\n\t" \
   "v_fma_f32 v113, -v112, v112, v113\n\t" "v_mfma_f32_16x16x32_bf16 v[60:63], a[220:223], v[100:103], v[60:63]\n\t" "v_max_f32 v113, 0, v113\n\t" \
   "s_waitcnt lgkmcnt(2)\n\t" "v_mfma_f32_16x16x32_bf16 v[48:51], a[224:227], v[104:107], v[48:51]\n\t" "v_cvt_pk_bf16_f32 v113, v113, v112\n\t" \
   "v_and_b32 v8, 0xffff0000, v113\n\t" "v_mfma_f32_16x16x32_bf16 v[52:55], a[228:231], v[104:107], v[52:55]\n\t" \
   "ds_read_b128 v[88:91], v26 offset:18432\n\t" "v_mfma_f32_16x16x32_bf16 v[56:59], a[232:235], v[104:107], v[56:59]\n\t" \
   "v_lshlrev_b32 v113, 16, v113\n\t" "v_add_f32 v113, 0x3727c5ac, v113\n\t" "v_mfma_f32_16x16x32_bf16 v[60:63], a[236:239], v[104:107], v[60:63]\n\t" \
   "v_cvt_pk_bf16_f32 v113, v113, v113\n\t" "v_lshlrev_b32 v113, 16, v113\n\t" \
   "v_mfma_f32_16x16x32_bf16 v[48:51], a[240:243], v[108:111], v[48:51]\n\t" "v_sqrt_f32 v113, v113\n\t" \
   "v_mfma_f32_16x16x32_bf16 v[52:55], a[244:247], v[108:111], v[52:55]\n\t" "ds_read_b128 v[92:95], v26 offset:19456\n\t" \
   "v_mfma_f32_16x16x32_bf16 v[56:59], a[248:251], v[108:111], v[56:59]\n\t" "buffer_load_dwordx4 v16, %[rs], s51 offen offset:1024 sc1 lds\n\t" \
   "v_mfma_f32_16x16x32_bf16 v[60:63], a[252:255], v[108:111], v[60:63]\n\t" "s_lshr_b64 s[60:61], %[ysel], s54\n\t" "s_waitcnt lgkmcnt(2)\n\t" \
   "v_mfma_f32_16x16x32_bf16 v[48:51], v[192:195], v[80:83], v[48:51]\n\t" "s_nop 0\n\t" "s_mul_i32 s61, s54, 43\n\t" \
   "v_mfma_f32_16x16x32_bf16 v[52:55], v[196:199], v[80:83], v[52:55]\n\t" "ds_read_b128 v[96:99], v26 offset:20480\n\t" \
   "v_mfma_f32_16x16x32_bf16 v[56:59], v[200:203], v[80:83], v[56:59]\n\t" "v_add_f32 v122, v122, v124\n\t" \
   "v_mfma_f32_16x16x32_bf16 v[60:63], v[204:207], v[80:83], v[60:63]\n\t" "v_cvt_pk_bf16_f32 v113, v113, v113\n\t" "v_add_f32 v123, v123, v125\n\t" \
   "v_mfma_f32_16x16x32_bf16 v[48:51], v[208:211], v[84:87], v[48:51]\n\t" "s_mul_i32 s55, s54, 80\n\t" "v_add_f32 v126, v126, v128\n\t" \
   "v_mfma_f32_16x16x32_bf16 v[52:55], v[212:215], v[84:87], v[52:55]\n\t" "ds_read_b128 v[100:103], v26 offset:21504\n\t" \
   "v_mfma_f32_16x16x32_bf16 v[56:59], v[216:219], v[84:87], v[56:59]\n\t" "buffer_load_dwordx4 v16, %[rs], s51 offen offset:2048 sc1 lds\n\t" \
   "v_mfma_f32_16x16x32_bf16 v[60:63], v[220:223], v[84:87], v[60:63]\n\t" "v_lshlrev_b32 v113, 16, v113\n\t" "s_waitcnt lgkmcnt(2)\n\t" \
   "v_mfma_f32_16x16x32_bf16 v[48:51], v[224:227], v[88:91], v[48:51]\n\t" "s_and_b32 s60, s60, 1\n\t" "v_add_f32 v127, v127, v129\n\t" \
   "v_mfma_f32_16x16x32_bf16 v[52:55], v[228:231], v[88:91], v[52:55]\n\t" "ds_read_b128 v[104:107], v26 offset:22528\n\t" \
   "v_mfma_f32_16x16x32_bf16 v[56:59], v[232:235], v[88:91], v[56:59]\n\t" "v_rcp_f32 v10, v113\n\t" \
   "v_mfma_f32_16x16x32_bf16 v[60:63], v[236:239], v[88:91], v[60:63]\n\t" "s_lshr_b32 s61, s61, 7\n\t" "v_add_f32 v122, v122, v130\n\t" \
   "v_mfma_f32_16x16x32_bf16 v[48:51], v[240:243], v[92:95], v[48:51]\n\t" "buffer_load_dwordx4 v16, %[rs], s51 offen offset:3072 sc1 lds\n\t" \
   "s_add_u32 m0, m0, 4096\n\t" "s_add_u32 s51, s51, 4096\n\t" "v_mfma_f32_16x16x32_bf16 v[52:55], v[244:247], v[92:95], v[52:55]\n\t" \
   "ds_read_b128 v[108:111], v26 offset:23552\n\t" "v_mfma_f32_16x16x32_bf16 v[56:59], v[248:251], v[92:95], v[56:59]\n\t" \
   "s_add_u32 s55, s55, s60\n\t" "v_add_f32 v123, v123, v131\n\t" "v_mfma_f32_16x16x32_bf16 v[60:63], v[252:255], v[92:95], v[60:63]\n\t" \
   "s_lshl4_add_u32 s55, s61, s55\n\t" "s_waitcnt lgkmcnt(2)\n\t" "v_mfma_f32_16x16x32_bf16 v[64:67], a[0:3], v[96:99], 0\n\t" \
   "v_add_f32 v122, v122, v126\n\t" "v_add_f32 v123, v123, v127\n\t" "v_mfma_f32_16x16x32_bf16 v[68:71], a[4:7], v[96:99], 0\n\t" \
   "ds_read_b128 v[80:83], v26 offset:24576\n\t" "v_mfma_f32_16x16x32_bf16 v[72:75], a[8:11], v[96:99], 0\n\t" "s_lshl_b32 s55, s55, 13\n\t" \
   "v_mul_f32 v122, 0xbacccccd, v122\n\t" "v_mfma_f32_16x16x32_bf16 v[76:79], a[12:15], v[96:99], 0\n\t" "v_fmac_f32 v48, v32, v8\n\t" \
   "v_fmac_f32 v49, v33, v8\n\t" "v_mfma_f32_16x16x32_bf16 v[64:67], a[16:19], v[100:103], v[64:67]\n\t" "v_fmac_f32 v50, v34, v8\n\t" \
   "v_fmac_f32 v51, v35, v8\n\t" "v_mfma_f32_16x16x32_bf16 v[68:71], a[20:23], v[100:103], v[68:71]\n\t" "ds_read_b128 v[84:87], v26 offset:25600\n\t" \
   "v_mfma_f32_16x16x32_bf16 v[72:75], a[24:27], v[100:103], v[72:75]\n\t" "s_cmp_lt_i32 s40, 1\n\t" "s_cselect_b32 s49, 0x7ffffff0, 0\n\t" \
   "v_mfma_f32_16x16x32_bf16 v[76:79], a[28:31], v[100:103], v[76:79]\n\t" "v_or_b32 v9, s49, v20\n\t" "s_waitcnt lgkmcnt(2)\n\t" \
   "v_mfma_f32_16x16x32_bf16 v[64:67], a[32:35], v[104:107], v[64:67]\n\t" "s_sub_i32 s49, s41, 1\n\t" "s_max_i32 s49, s49, 0\n\t" \
   "buffer_load_dwordx4 v16, %[rs], s51 offen offset:0 sc1 lds\n\t" "v_mfma_f32_16x16x32_bf16 v[68:71], a[36:39], v[104:107], v[68:71]\n\t" \
   "ds_read_b128 v[88:91], v26 offset:26624\n\t" "v_mfma_f32_16x16x32_bf16 v[72:75], a[40:43], v[104:107], v[72:75]\n\t" "s_lshl_b32 s49, s49, 12\n\t" \
   "s_waitcnt vmcnt(6)\n\t" "v_mfma_f32_16x16x32_bf16 v[76:79], a[44:47], v[104:107], v[76:79]\n\t" \
   "buffer_store_dword v31, v9, %[rs], s49 offen sc1\n\t" "v_mfma_f32_16x16x32_bf16 v[64:67], a[48:51], v[108:111], v[64:67]\n\t" \
   "s_add_u32 s55, s55, %[yb]\n\t" "v_fmac_f32 v52, v36, v8\n\t" "v_mfma_f32_16x16x32_bf16 v[68:71], a[52:55], v[108:111], v[68:71]\n\t" \
   "ds_read_b128 v[92:95], v26 offset:27648\n\t" "s_cmp_eq_u32 s41, 63\n\t" "s_cselect_b32 s58, 0, %[size]\n\t" \
   "v_mfma_f32_16x16x32_bf16 v[72:75], a[56:59], v[108:111], v[72:75]\n\t" "v_fmac_f32 v53, v37, v8\n\t" "v_fmac_f32 v54, v38, v8\n\t" \
   "v_mfma_f32_16x16x32_bf16 v[76:79], a[60:63], v[108:111], v[76:79]\n\t" "v_fmac_f32 v55, v39, v8\n\t" "s_waitcnt lgkmcnt(2)\n\t" \
   "v_mfma_f32_16x16x32_bf16 v[64:67], a[64:67], v[80:83], v[64:67]\n\t" "buffer_load_dwordx4 v16, %[rs], s51 offen offset:1024 sc1 lds\n\t" \
   "v_mfma_f32_16x16x32_bf16 v[68:71], a[68:71], v[80:83], v[68:71]\n\t" "ds_read_b128 v[96:99], v26 offset:28672\n\t" \
   "v_mfma_f32_16x16x32_bf16 v[72:75], a[72:75], v[80:83], v[72:75]\n\t" "buffer_store_dwordx4 v[156:159], v153, s[56:59], s55 offen sc1\n\t" \
   "v_mfma_f32_16x16x32_bf16 v[76:79], a[76:79], v[80:83], v[76:79]\n\t" "v_fmac_f32 v56, v40, v8\n\t" "v_fmac_f32 v57, v41, v8\n\t" \
   "v_mfma_f32_16x16x32_bf16 v[64:67], a[80:83], v[84:87], v[64:67]\n\t" "v_fmac_f32 v58, v42, v8\n\t" "v_fmac_f32 v59, v43, v8\n\t" \
   "v_mfma_f32_16x16x32_bf16 v[68:71], a[84:87], v[84:87], v[68:71]\n\t" "ds_read_b128 v[100:103], v26 offset:29696\n\t" \
   "v_mfma_f32_16x16x32_bf16 v[72:75], a[88:91], v[84:87], v[72:75]\n\t" "v_fmac_f32 v60, v44, v8\n\t" "v_fmac_f32 v61, v45, v8\n\t" \
   "v_mfma_f32_16x16x32_bf16 v[76:79], a[92:95], v[84:87], v[76:79]\n\t" "v_fmac_f32 v62, v46, v8\n\t" "s_waitcnt lgkmcnt(2)\n\t" \
   "v_mfma_f32_16x16x32_bf16 v[64:67], a[96:99], v[88:91], v[64:67]\n\t" "buffer_load_dwordx4 v16, %[rs], s51 offen offset:2048 sc1 lds\n\t" \
   "v_mfma_f32_16x16x32_bf16 v[68:71], a[100:103], v[88:91], v[68:71]\n\t" "ds_read_b128 v[104:107], v26 offset:30720\n\t" \
   "v_mfma_f32_16x16x32_bf16 v[72:75], a[104:107], v[88:91], v[72:75]\n\t" "buffer_store_dwordx4 v[156:159], v154, s[56:59], s55 offen sc1\n\t" \
   "v_mfma_f32_16x16x32_bf16 v[76:79], a[108:111], v[88:91], v[76:79]\n\t" "v_fmac_f32 v63, v47, v8\n\t" "v_mul_f32 v48, v48, v10\n\t" \
   "v_mfma_f32_16x16x32_bf16 v[64:67], a[112:115], v[92:95], v[64:67]\n\t" "v_mul_f32 v49, v49, v10\n\t" "v_mul_f32 v50, v50, v10\n\t" \
   "v_mfma_f32_16x16x32_bf16 v[68:71], a[116:119], v[92:95], v[68:71]\n\t" "ds_read_b128 v[108:111], v26 offset:31744\n\t" \
   "v_mfma_f32_16x16x32_bf16 v[72:75], a[120:123], v[92:95], v[72:75]\n\t" "v_mul_f32 v51, v51, v10\n\t" "v_mul_f32 v52, v52, v10\n\t" \
   "v_mfma_f32_16x16x32_bf16 v[76:79], a[124:127], v[92:95], v[76:79]\n\t" "v_mul_f32 v53, v53, v10\n\t" "s_waitcnt lgkmcnt(2)\n\t" \
   "v_mfma_f32_16x16x32_bf16 v[64:67], a[128:131], v[96:99], v[64:67]\n\t" "buffer_load_dwordx4 v16, %[rs], s51 offen offset:3072 sc1 lds\n\t" \
   "s_add_u32 m0, m0, 4096\n\t" "s_add_u32 s51, s51, 4096\n\t" "v_mfma_f32_16x16x32_bf16 v[68:71], a[132:135], v[96:99], v[68:71]\n\t" \
   "ds_read_b128 v[80:83], v26 offset:32768\n\t" "v_mfma_f32_16x16x32_bf16 v[72:75], a[136:139], v[96:99], v[72:75]\n\t" \
   "buffer_store_dwordx4 v[156:159], v155, s[56:59], s55 offen sc1\n\t" "v_mfma_f32_16x16x32_bf16 v[76:79], a[140:143], v[96:99], v[76:79]\n\t" \
   "v_mul_f32 v54, v54, v10\n\t" "v_mul_f32 v55, v55, v10\n\t" "v_mfma_f32_16x16x32_bf16 v[64:67], a[144:147], v[100:103], v[64:67]\n\t" \
   "v_mul_f32 v56, v56, v10\n\t" "v_mul_f32 v57, v57, v10\n\t" "v_mfma_f32_16x16x32_bf16 v[68:71], a[148:151], v[100:103], v[68:71]\n\t" \
   "ds_read_b128 v[84:87], v26 offset:33792\n\t" "v_mfma_f32_16x16x32_bf16 v[72:75], a[152:155], v[100:103], v[72:75]\n\t" \
   "v_mul_f32 v58, v58, v10\n\t" "v_mul_f32 v59, v59, v10\n\t" "v_mfma_f32_16x16x32_bf16 v[76:79], a[156:159], v[100:103], v[76:79]\n\t" \
   "v_mul_f32 v60, v60, v10\n\t" "s_waitcnt lgkmcnt(2)\n\t" "v_mfma_f32_16x16x32_bf16 v[64:67], a[160:163], v[104:107], v[64:67]\n\t" \
   "buffer_load_dwordx4 v16, %[rs], s51 offen offset:0 sc1 lds\n\t" "v_mfma_f32_16x16x32_bf16 v[68:71], a[164:167], v[104:107], v[68:71]\n\t" \
   "ds_read_b128 v[88:91], v26 offset:34816\n\t" "v_mfma_f32_16x16x32_bf16 v[72:75], a[168:171], v[104:107], v[72:75]\n\t" \
   "v_mul_f32 v61, v61, v10\n\t" "v_mul_f32 v62, v62, v10\n\t" "v_mfma_f32_16x16x32_bf16 v[76:79], a[172:175], v[104:107], v[76:79]\n\t" \
   "v_mul_f32 v63, v63, v10\n\t" "v_mul_f32 v123, 0x3acccccd, v123\n\t" "v_mfma_f32_16x16x32_bf16 v[64:67], a[176:179], v[108:111], v[64:67]\n\t" \
   "v_cvt_pk_bf16_f32 v136, v48, v49\n\t" "v_fma_f32 v123, -v122, v122, v123\n\t" \
   "v_mfma_f32_16x16x32_bf16 v[68:71], a[180:183], v[108:111], v[68:71]\n\t" "ds_read_b128 v[92:95], v26 offset:35840\n\t" \
   "v_mfma_f32_16x16x32_bf16 v[72:75], a[184:187], v[108:111], v[72:75]\n\t" "v_cvt_pk_bf16_f32 v137, v50, v51\n\t" "v_max_f32 v123, 0, v123\n\t" \
   "v_mfma_f32_16x16x32_bf16 v[76:79], a[188:191], v[108:111], v[76:79]\n\t" "v_cvt_pk_bf16_f32 v138, v52, v53\n\t" "s_waitcnt lgkmcnt(2)\n\t" \
   "v_mfma_f32_16x16x32_bf16 v[64:67], a[192:195], v[80:83], v[64:67]\n\t" "v_cvt_pk_bf16_f32 v123, v123, v122\n\t" \
   "v_cvt_pk_bf16_f32 v139, v54, v55\n\t" "buffer_load_dwordx4 v16, %[rs], s51 offen offset:1024 sc1 lds\n\t" "s_add_u32 m0, s53, %[ld1]\n\t" \
   "s_nop 0\n\t" "v_mfma_f32_16x16x32_bf16 v[68:71], a[196:199], v[80:83], v[68:71]\n\t" "ds_read_b128 v[96:99], v26 offset:36864\n\t" \
   "v_mfma_f32_16x16x32_bf16 v[72:75], a[200:203], v[80:83], v[72:75]\n\t" "v_and_b32 v12, 0xffff0000, v123\n\t" "v_cvt_pk_bf16_f32 v140, v56, v57\n\t" \
   "v_mfma_f32_16x16x32_bf16 v[76:79], a[204:207], v[80:83], v[76:79]\n\t" "v_lshlrev_b32 v123, 16, v123\n\t" "v_cvt_pk_bf16_f32 v141, v58, v59\n\t" \
   "v_mfma_f32_16x16x32_bf16 v[64:67], a[208:211], v[84:87], v[64:67]\n\t" "v_add_f32 v123, 0x3727c5ac, v123\n\t" \
   "v_cvt_pk_bf16_f32 v142, v60, v61\n\t" "v_mfma_f32_16x16x32_bf16 v[68:71], a[212:215], v[84:87], v[68:71]\n\t" \
   "ds_read_b128 v[100:103], v26 offset:37888\n\t" "v_mfma_f32_16x16x32_bf16 v[72:75], a[216:219], v[84:87], v[72:75]\n\t" \
   "v_cvt_pk_bf16_f32 v123, v123, v123\n\t" "v_cvt_pk_bf16_f32 v143, v62, v63\n\t" \
   "v_mfma_f32_16x16x32_bf16 v[76:79], a[220:223], v[84:87], v[76:79]\n\t" "v_lshlrev_b32 v123, 16, v123\n\t" "s_waitcnt lgkmcnt(2)\n\t" \
   "v_mfma_f32_16x16x32_bf16 v[64:67], a[224:227], v[88:91], v[64:67]\n\t" "s_lshl_b32 s47, s41, 13\n\t" "v_sqrt_f32 v123, v123\n\t" \
   "s_add_i32 s47, s47, 0\n\t" "v_mfma_f32_16x16x32_bf16 v[68:71], a[228:231], v[88:91], v[68:71]\n\t" "ds_read_b128 v[104:107], v26 offset:38912\n\t" \
   "buffer_load_dwordx4 v17, %[rs], s52 offen sc1 lds\n\t" "v_mfma_f32_16x16x32_bf16 v[72:75], a[232:235], v[88:91], v[72:75]\n\t" \
   "s_add_u32 s48, s47, %[qf]\n\t" "s_nop 0\n\t" "buffer_store_dwordx4 v[136:139], v21, %[rs], s48 offen sc1\n\t" \
   "v_mfma_f32_16x16x32_bf16 v[76:79], a[236:239], v[88:91], v[76:79]\n\t" "v_cvt_pk_bf16_f32 v123, v123, v123\n\t" "v_lshlrev_b32 v123, 16, v123\n\t" \
   "s_add_u32 s48, s47, %[kf]\n\t" "v_rcp_f32 v132, v123\n\t" "v_mfma_f32_16x16x32_bf16 v[64:67], a[240:243], v[92:95], v[64:67]\n\t" \
   "buffer_store_dwordx4 v[140:143], v21, %[rs], s48 offen sc1\n\t" "v_add_u32 v28, s43, v22\n\t" "v_add_u32 v29, s43, v23\n\t" \
   "v_mfma_f32_16x16x32_bf16 v[68:71], a[244:247], v[92:95], v[68:71]\n\t" "ds_read_b128 v[108:111], v26 offset:39936\n\t" \
   "v_mfma_f32_16x16x32_bf16 v[72:75], a[248:251], v[92:95], v[72:75]\n\t" "v_mfma_f32_16x16x32_bf16 v[76:79], a[252:255], v[92:95], v[76:79]\n\t" \
   "s_waitcnt vmcnt(18) lgkmcnt(0)\n\t" "s_barrier\n\t" "v_mfma_f32_16x16x32_bf16 v[64:67], v[192:195], v[96:99], v[64:67]\n\t" \
   "s_add_u32 s45, s40, 3\n\t" "s_min_u32 s45, s45, 63\n\t" "v_mfma_f32_16x16x32_bf16 v[68:71], v[196:199], v[96:99], v[68:71]\n\t" \
   "ds_read_b128 v[80:83], v28 offset:0\n\t" "v_mfma_f32_16x16x32_bf16 v[72:75], v[200:203], v[96:99], v[72:75]\n\t" "s_mov_b32 s46, s42\n\t" \
   "v_mfma_f32_16x16x32_bf16 v[76:79], v[204:207], v[96:99], v[76:79]\n\t" "s_lshr_b32 s48, s45, 1\n\t" "s_bitcmp1_b32 s44, s48\n\t" \
   "s_cbranch_scc0 174f\n\t" "74:\n\t" "v_mfma_f32_16x16x32_bf16 v[64:67], v[208:211], v[100:103], v[64:67]\n\t" "s_mul_i32 s52, s45, 1536\n\t" \
   "s_mul_i32 s51, s45, 40960\n\t" "s_add_u32 m0, s46, %[ldx]\n\t" "s_mov_b32 s53, s46\n\t" \
   "v_mfma_f32_16x16x32_bf16 v[68:71], v[212:215], v[100:103], v[68:71]\n\t" "ds_read_b128 v[84:87], v28 offset:1024\n\t" \
   "v_mfma_f32_16x16x32_bf16 v[72:75], v[216:219], v[100:103], v[72:75]\n\t" "buffer_load_dwordx4 v16, %[rs], s51 offen offset:0 sc1 lds\n\t" \
   "v_mfma_f32_16x16x32_bf16 v[76:79], v[220:223], v[100:103], v[76:79]\n\t" "s_mov_b32 s42, s43\n\t" \
   "v_mfma_f32_16x16x32_bf16 v[64:67], v[224:227], v[104:107], v[64:67]\n\t" "s_add_u32 s43, s43, 44032\n\t" \
   "v_mfma_f32_16x16x32_bf16 v[68:71], v[228:231], v[104:107], v[68:71]\n\t" "ds_read_b128 v[88:91], v28 offset:2048\n\t" \
   "v_mfma_f32_16x16x32_bf16 v[72:75], v[232:235], v[104:107], v[72:75]\n\t" "s_cmp_eq_u32 s43, 132096\n\t" "s_cselect_b32 s43, 0, s43\n\t" \
   "v_mfma_f32_16x16x32_bf16 v[76:79], v[236:239], v[104:107], v[76:79]\n\t" "s_add_u32 s41, s41, 1\n\t" "s_add_u32 s40, s40, 1\n\t" \
   "v_mfma_f32_16x16x32_bf16 v[64:67], v[240:243], v[108:111], v[64:67]\n\t" "v_mfma_f32_16x16x32_bf16 v[68:71], v[244:247], v[108:111], v[68:71]\n\t" \
   "ds_read_b128 v[92:95], v28 offset:3072\n\t" "v_mfma_f32_16x16x32_bf16 v[72:75], v[248:251], v[108:111], v[72:75]\n\t" \
   "v_mfma_f32_16x16x32_bf16 v[76:79], v[252:255], v[108:111], v[76:79]\n\t" "s_cmp_lt_u32 s40, 64\n\t" "s_cbranch_scc1 1b\n\t" "s_nop 7\n\t" \
   "v_fmac_f32 v64, v32, v12\n\t" "v_fmac_f32 v65, v33, v12\n\t" "v_fmac_f32 v66, v34, v12\n\t" "v_fmac_f32 v67, v35, v12\n\t" \
   "v_fmac_f32 v68, v36, v12\n\t" "v_fmac_f32 v69, v37, v12\n\t" "v_fmac_f32 v70, v38, v12\n\t" "v_fmac_f32 v71, v39, v12\n\t" \
   "v_fmac_f32 v72, v40, v12\n\t" "v_fmac_f32 v73, v41, v12\n\t" "v_fmac_f32 v74, v42, v12\n\t" "v_fmac_f32 v75, v43, v12\n\t" \
   "v_fmac_f32 v76, v44, v12\n\t" "v_fmac_f32 v77, v45, v12\n\t" "v_fmac_f32 v78, v46, v12\n\t" "v_fmac_f32 v79, v47, v12\n\t" \
   "v_mul_f32 v64, v64, v132\n\t" "v_mul_f32 v65, v65, v132\n\t" "v_mul_f32 v66, v66, v132\n\t" "v_mul_f32 v67, v67, v132\n\t" \
   "v_mul_f32 v68, v68, v132\n\t" "v_mul_f32 v69, v69, v132\n\t" "v_mul_f32 v70, v70, v132\n\t" "v_mul_f32 v71, v71, v132\n\t" \
   "v_mul_f32 v72, v72, v132\n\t" "v_mul_f32 v73, v73, v132\n\t" "v_mul_f32 v74, v74, v132\n\t" "v_mul_f32 v75, v75, v132\n\t" \
   "v_mul_f32 v76, v76, v132\n\t" "v_mul_f32 v77, v77, v132\n\t" "v_mul_f32 v78, v78, v132\n\t" "v_mul_f32 v79, v79, v132\n\t" \
   "v_cvt_pk_bf16_f32 v136, v64, v65\n\t" "v_cvt_pk_bf16_f32 v137, v66, v67\n\t" "v_cvt_pk_bf16_f32 v138, v68, v69\n\t" \
   "v_cvt_pk_bf16_f32 v139, v70, v71\n\t" "v_cvt_pk_bf16_f32 v140, v72, v73\n\t" "v_cvt_pk_bf16_f32 v141, v74, v75\n\t" \
   "v_cvt_pk_bf16_f32 v142, v76, v77\n\t" "v_cvt_pk_bf16_f32 v143, v78, v79\n\t" "s_lshl_b32 s47, s41, 13\n\t" "s_add_i32 s47, s47, -4096\n\t" \
   "s_max_i32 s47, s47, 0\n\t" "s_cmp_eq_u32 s40, 0\n\t" "s_cselect_b32 s48, 0x7ffffff0, 0\n\t" "v_or_b32 v30, s48, v21\n\t" \
   "s_add_u32 s48, s47, %[qf]\n\t" "buffer_store_dwordx4 v[136:139], v30, %[rs], s48 offen sc1\n\t" "s_add_u32 s48, s47, %[kf]\n\t" \
   "buffer_store_dwordx4 v[140:143], v30, %[rs], s48 offen sc1\n\t" "s_waitcnt vmcnt(0) lgkmcnt(0)\n\t" "s_cmp_lt_i32 s40, 1\n\t" \
   "s_cselect_b32 s47, 0x7ffffff0, 0\n\t" "v_or_b32 v30, s47, v20\n\t" "s_sub_i32 s47, s41, 1\n\t" "s_max_i32 s47, s47, 0\n\t" \
   "s_lshl_b32 s47, s47, 12\n\t" "buffer_store_dword v31, v30, %[rs], s47 offen sc1\n\t" "s_branch 9f\n\t" "172:\n\t" "s_mov_b32 s47, 0\n\t" \
   "buffer_load_dword v30, v19, %[rs], 0 offen sc1\n\t" "s_waitcnt vmcnt(0)\n\t" "v_add_u32 v30, 1, v30\n\t" "v_cmp_lt_u32 vcc, 9, v30\n\t" \
   "s_mov_b32 s44, vcc_lo\n\t" "s_bitcmp1_b32 s44, s48\n\t" "s_cbranch_scc1 72b\n\t" "s_bitset1_b32 s44, s48\n\t" "s_lshl_b32 s48, s48, 7\n\t" \
   "s_add_u32 s48, s48, %[cnt]\n\t" "73:\n\t" "s_sleep 10\n\t" "buffer_load_dword v30, off, %[rs], s48 sc1\n\t" "s_add_u32 s47, s47, 1\n\t" \
   "s_waitcnt vmcnt(0)\n\t" "v_add_u32 v30, 1, v30\n\t" "v_cmp_lt_u32 vcc, 9, v30\n\t" "s_cmp_gt_u32 s47, 0x20000\n\t" "s_cbranch_scc1 72b\n\t" \
   "s_cbranch_vccz 73b\n\t" "s_branch 72b\n\t" "174:\n\t" "s_mov_b32 s47, 0\n\t" "buffer_load_dword v30, v19, %[rs], 0 offen sc1\n\t" \
   "s_waitcnt vmcnt(0)\n\t" "v_add_u32 v30, 1, v30\n\t" "v_cmp_lt_u32 vcc, 9, v30\n\t" "s_mov_b32 s44, vcc_lo\n\t" "s_bitcmp1_b32 s44, s48\n\t" \
   "s_cbranch_scc1 74b\n\t" "s_bitset1_b32 s44, s48\n\t" "s_lshl_b32 s48, s48, 7\n\t" "s_add_u32 s48, s48, %[cnt]\n\t" "75:\n\t" "s_sleep 10\n\t" \
   "buffer_load_dword v30, off, %[rs], s48 sc1\n\t" "s_add_u32 s47, s47, 1\n\t" "s_waitcnt vmcnt(0)\n\t" "v_add_u32 v30, 1, v30\n\t" \
   "v_cmp_lt_u32 vcc, 9, v30\n\t" "s_cmp_gt_u32 s47, 0x20000\n\t" "s_cbranch_scc1 74b\n\t" "s_cbranch_vccz 75b\n\t" "s_branch 74b\n\t" "9:\n\t" \
   "s_waitcnt vmcnt(0)"
#define KQ_CLOB "v8", "v9", "v10", "v11", "v12", "v13", "v14", "v15", "v32", "v33", "v34", "v35", "v36", "v37", "v38", "v39", "v40", "v41", "v42", "v43", "v44", "v45", "v46", "v47", "v48", "v49", "v50", "v51", "v52", "v53", "v54", "v55", "v56", "v57", "v58", "v59", "v60", "v61", "v62", "v63", "v64", "v65", "v66", "v67", "v68", "v69", "v70", "v71", "v72", "v73", "v74", "v75", "v76", "v77", "v78", "v79", "v80", "v81", "v82", "v83", "v84", "v85", "v86", "v87", "v88", "v89", "v90", "v91", "v92", "v93", "v94", "v95", "v96", "v97", "v98", "v99", "v100", "v101", "v102", "v103", "v104", "v105", "v106", "v107", "v108", "v109", "v110", "v111", "v112", "v113", "v114", "v115", "v116", "v117", "v118", "v119", "v120", "v121", "v122", "v123", "v124", "v125", "v126", "v127", "v128", "v129", "v130", "v131", "v132", "v133", "v134", "v135", "v136", "v137", "v138", "v139", "v140", "v141", "v142", "v143", "v144", "v145", "v146", "v147", "v148", "v149", "v150", "v151", "s40", "s41", "s42", "s43", "s44", "s45", "s46", "s47", "s48", "s49", "s50", "s51", "s52", "s53", "s54", "s55", "s60", "s61"
#define VU_ASM \
   "s_mov_b32 s44, %[rdy]\n\t" "s_mov_b32 s45, 0\n\t" "s_mov_b32 s46, 0\n\t" "s_cmp_eq_u32 s44, -1\n\t" "s_cbranch_scc1 68f\n\t" \
   "s_mul_i32 s52, s45, 1536\n\t" "s_mul_i32 s51, s45, 40960\n\t" "s_add_u32 m0, s46, %[ldx]\n\t" "s_mov_b32 s53, s46\n\t" \
   "buffer_load_dwordx4 v16, %[rs], s51 offen offset:0 sc1 lds\n\t" "buffer_load_dwordx4 v16, %[rs], s51 offen offset:1024 sc1 lds\n\t" \
   "buffer_load_dwordx4 v16, %[rs], s51 offen offset:2048 sc1 lds\n\t" "buffer_load_dwordx4 v16, %[rs], s51 offen offset:3072 sc1 lds\n\t" \
   "s_add_u32 m0, m0, 4096\n\t" "s_add_u32 s51, s51, 4096\n\t" "buffer_load_dwordx4 v16, %[rs], s51 offen offset:0 sc1 lds\n\t" \
   "buffer_load_dwordx4 v16, %[rs], s51 offen offset:1024 sc1 lds\n\t" "buffer_load_dwordx4 v16, %[rs], s51 offen offset:2048 sc1 lds\n\t" \
   "buffer_load_dwordx4 v16, %[rs], s51 offen offset:3072 sc1 lds\n\t" "s_add_u32 m0, m0, 4096\n\t" "s_add_u32 s51, s51, 4096\n\t" \
   "buffer_load_dwordx4 v16, %[rs], s51 offen offset:0 sc1 lds\n\t" "buffer_load_dwordx4 v16, %[rs], s51 offen offset:1024 sc1 lds\n\t" \
   "s_add_u32 m0, s53, %[ld1]\n\t" "s_nop 0\n\t" "buffer_load_dwordx4 v17, %[rs], s52 offen sc1 lds\n\t" "s_mov_b32 s45, 1\n\t" \
   "s_mov_b32 s46, 44032\n\t" "s_mul_i32 s52, s45, 1536\n\t" "s_mul_i32 s51, s45, 40960\n\t" "s_add_u32 m0, s46, %[ldx]\n\t" "s_mov_b32 s53, s46\n\t" \
   "buffer_load_dwordx4 v16, %[rs], s51 offen offset:0 sc1 lds\n\t" "buffer_load_dwordx4 v16, %[rs], s51 offen offset:1024 sc1 lds\n\t" \
   "buffer_load_dwordx4 v16, %[rs], s51 offen offset:2048 sc1 lds\n\t" "buffer_load_dwordx4 v16, %[rs], s51 offen offset:3072 sc1 lds\n\t" \
   "s_add_u32 m0, m0, 4096\n\t" "s_add_u32 s51, s51, 4096\n\t" "buffer_load_dwordx4 v16, %[rs], s51 offen offset:0 sc1 lds\n\t" \
   "buffer_load_dwordx4 v16, %[rs], s51 offen offset:1024 sc1 lds\n\t" "buffer_load_dwordx4 v16, %[rs], s51 offen offset:2048 sc1 lds\n\t" \
   "buffer_load_dwordx4 v16, %[rs], s51 offen offset:3072 sc1 lds\n\t" "s_add_u32 m0, m0, 4096\n\t" "s_add_u32 s51, s51, 4096\n\t" \
   "buffer_load_dwordx4 v16, %[rs], s51 offen offset:0 sc1 lds\n\t" "buffer_load_dwordx4 v16, %[rs], s51 offen offset:1024 sc1 lds\n\t" \
   "s_add_u32 m0, s53, %[ld1]\n\t" "s_nop 0\n\t" "buffer_load_dwordx4 v17, %[rs], s52 offen sc1 lds\n\t" "68:\n\t" "v_mov_b32 v80, 0x3f803f80\n\t" \
   "v_mov_b32 v81, 0x3f803f80\n\t" "v_mov_b32 v82, 0x3f803f80\n\t" "v_mov_b32 v83, 0x3f803f80\n\t" "v_mov_b32 v31, 1\n\t" "s_nop 1\n\t" \
   "v_mfma_f32_16x16x32_bf16 v[32:35], a[0:3], v[80:83], 0\n\t" "v_mfma_f32_16x16x32_bf16 v[36:39], a[4:7], v[80:83], 0\n\t" \
   "v_mfma_f32_16x16x32_bf16 v[32:35], a[8:11], v[80:83], v[32:35]\n\t" "v_mfma_f32_16x16x32_bf16 v[36:39], a[12:15], v[80:83], v[36:39]\n\t" \
   "v_mfma_f32_16x16x32_bf16 v[32:35], a[16:19], v[80:83], v[32:35]\n\t" "v_mfma_f32_16x16x32_bf16 v[36:39], a[20:23], v[80:83], v[36:39]\n\t" \
   "v_mfma_f32_16x16x32_bf16 v[32:35], a[24:27], v[80:83], v[32:35]\n\t" "v_mfma_f32_16x16x32_bf16 v[36:39], a[28:31], v[80:83], v[36:39]\n\t" \
   "v_mfma_f32_16x16x32_bf16 v[32:35], a[32:35], v[80:83], v[32:35]\n\t" "v_mfma_f32_16x16x32_bf16 v[36:39], a[36:39], v[80:83], v[36:39]\n\t" \
   "v_mfma_f32_16x16x32_bf16 v[32:35], a[40:43], v[80:83], v[32:35]\n\t" "v_mfma_f32_16x16x32_bf16 v[36:39], a[44:47], v[80:83], v[36:39]\n\t" \
   "v_mfma_f32_16x16x32_bf16 v[32:35], a[48:51], v[80:83], v[32:35]\n\t" "v_mfma_f32_16x16x32_bf16 v[36:39], a[52:55], v[80:83], v[36:39]\n\t" \
   "v_mfma_f32_16x16x32_bf16 v[32:35], a[56:59], v[80:83], v[32:35]\n\t" "v_mfma_f32_16x16x32_bf16 v[36:39], a[60:63], v[80:83], v[36:39]\n\t" \
   "v_mfma_f32_16x16x32_bf16 v[32:35], a[64:67], v[80:83], v[32:35]\n\t" "v_mfma_f32_16x16x32_bf16 v[36:39], a[68:71], v[80:83], v[36:39]\n\t" \
   "v_mfma_f32_16x16x32_bf16 v[32:35], a[72:75], v[80:83], v[32:35]\n\t" "v_mfma_f32_16x16x32_bf16 v[36:39], a[76:79], v[80:83], v[36:39]\n\t" \
   "v_mfma_f32_16x16x32_bf16 v[32:35], a[80:83], v[80:83], v[32:35]\n\t" "v_mfma_f32_16x16x32_bf16 v[36:39], a[84:87], v[80:83], v[36:39]\n\t" \
   "v_mfma_f32_16x16x32_bf16 v[32:35], a[88:91], v[80:83], v[32:35]\n\t" "v_mfma_f32_16x16x32_bf16 v[36:39], a[92:95], v[80:83], v[36:39]\n\t" \
   "v_mfma_f32_16x16x32_bf16 v[32:35], a[96:99], v[80:83], v[32:35]\n\t" "v_mfma_f32_16x16x32_bf16 v[36:39], a[100:103], v[80:83], v[36:39]\n\t" \
   "v_mfma_f32_16x16x32_bf16 v[32:35], a[104:107], v[80:83], v[32:35]\n\t" "v_mfma_f32_16x16x32_bf16 v[36:39], a[108:111], v[80:83], v[36:39]\n\t" \
   "v_mfma_f32_16x16x32_bf16 v[32:35], a[112:115], v[80:83], v[32:35]\n\t" "v_mfma_f32_16x16x32_bf16 v[36:39], a[116:119], v[80:83], v[36:39]\n\t" \
   "v_mfma_f32_16x16x32_bf16 v[32:35], a[120:123], v[80:83], v[32:35]\n\t" "v_mfma_f32_16x16x32_bf16 v[36:39], a[124:127], v[80:83], v[36:39]\n\t" \
   "v_mfma_f32_16x16x32_bf16 v[32:35], a[128:131], v[80:83], v[32:35]\n\t" "v_mfma_f32_16x16x32_bf16 v[36:39], a[132:135], v[80:83], v[36:39]\n\t" \
   "v_mfma_f32_16x16x32_bf16 v[32:35], a[136:139], v[80:83], v[32:35]\n\t" "v_mfma_f32_16x16x32_bf16 v[36:39], a[140:143], v[80:83], v[36:39]\n\t" \
   "v_mfma_f32_16x16x32_bf16 v[32:35], a[144:147], v[80:83], v[32:35]\n\t" "v_mfma_f32_16x16x32_bf16 v[36:39], a[148:151], v[80:83], v[36:39]\n\t" \
   "v_mfma_f32_16x16x32_bf16 v[32:35], a[152:155], v[80:83], v[32:35]\n\t" "v_mfma_f32_16x16x32_bf16 v[36:39], a[156:159], v[80:83], v[36:39]\n\t" \
   "s_mov_b32 s40, 0\n\t" "s_mov_b32 s41, 0\n\t" "s_mov_b32 s42, 0\n\t" "s_mov_b32 s43, 44032\n\t" "v_mov_b32 v28, v22\n\t" "v_mov_b32 v29, v23\n\t" \
   "s_mov_b32 s45, 2\n\t" "s_mov_b32 s46, 88064\n\t" "s_waitcnt vmcnt(11)\n\t" "s_barrier\n\t" "s_lshr_b32 s48, s45, 1\n\t" \
   "s_bitcmp1_b32 s44, s48\n\t" "s_cbranch_scc0 172f\n\t" "72:\n\t" "s_mul_i32 s52, s45, 1536\n\t" "s_mul_i32 s51, s45, 40960\n\t" \
   "s_add_u32 m0, s46, %[ldx]\n\t" "s_mov_b32 s53, s46\n\t" "buffer_load_dwordx4 v16, %[rs], s51 offen offset:0 sc1 lds\n\t" \
   "ds_read_b128 v[80:83], v28 offset:0\n\t" "ds_read_b128 v[84:87], v28 offset:1024\n\t" "ds_read_b128 v[88:91], v28 offset:2048\n\t" \
   "ds_read_b128 v[92:95], v28 offset:3072\n\t" "ds_read_b128 v[96:99], v28 offset:4096\n\t" "ds_read_b128 v[100:103], v28 offset:5120\n\t" \
   "ds_read_b128 v[104:107], v28 offset:6144\n\t" "v_mov_b32 v14, v24\n\t" "v_mov_b32 v15, v25\n\t" "v_mov_b32 v18, 0x7ffffff0\n\t" \
   "s_mov_b32 s49, 0\n\t" "v_mov_b32 v26, v28\n\t" "v_mov_b32 v27, v29\n\t" "s_waitcnt lgkmcnt(5)\n\t" \
   "v_mfma_f32_16x16x32_bf16 v[48:51], a[0:3], v[80:83], 0\n\t" "ds_read_b128 v[112:115], v27 offset:0\n\t" \
   "v_mfma_f32_16x16x32_bf16 v[52:55], a[4:7], v[80:83], 0\n\t" "ds_read_b128 v[108:111], v26 offset:7168\n\t" \
   "v_mfma_f32_16x16x32_bf16 v[48:51], a[8:11], v[84:87], v[48:51]\n\t" "ds_read_b128 v[116:119], v27 offset:16\n\t" \
   "v_mfma_f32_16x16x32_bf16 v[52:55], a[12:15], v[84:87], v[52:55]\n\t" "ds_read_b128 v[80:83], v26 offset:8192\n\t" "s_waitcnt lgkmcnt(7)\n\t" \
   "v_mfma_f32_16x16x32_bf16 v[48:51], a[16:19], v[88:91], v[48:51]\n\t" "ds_read_b64 v[120:121], v27 offset:32\n\t" \
   "v_mfma_f32_16x16x32_bf16 v[52:55], a[20:23], v[88:91], v[52:55]\n\t" "ds_read_b128 v[84:87], v26 offset:9216\n\t" \
   "v_mfma_f32_16x16x32_bf16 v[48:51], a[24:27], v[92:95], v[48:51]\n\t" "ds_read_b128 v[122:125], v27 offset:768\n\t" \
   "v_mfma_f32_16x16x32_bf16 v[52:55], a[28:31], v[92:95], v[52:55]\n\t" "ds_read_b128 v[88:91], v26 offset:10240\n\t" "s_waitcnt lgkmcnt(9)\n\t" \
   "v_mfma_f32_16x16x32_bf16 v[48:51], a[32:35], v[96:99], v[48:51]\n\t" "buffer_load_dwordx4 v16, %[rs], s51 offen offset:1024 sc1 lds\n\t" \
   "v_mfma_f32_16x16x32_bf16 v[52:55], a[36:39], v[96:99], v[52:55]\n\t" "ds_read_b128 v[92:95], v26 offset:11264\n\t" \
   "v_mfma_f32_16x16x32_bf16 v[48:51], a[40:43], v[100:103], v[48:51]\n\t" "ds_read_b128 v[126:129], v27 offset:784\n\t" \
   "v_mfma_f32_16x16x32_bf16 v[52:55], a[44:47], v[100:103], v[52:55]\n\t" "ds_read_b128 v[96:99], v26 offset:12288\n\t" "s_waitcnt lgkmcnt(9)\n\t" \
   "v_mfma_f32_16x16x32_bf16 v[48:51], a[48:51], v[104:107], v[48:51]\n\t" "ds_read_b64 v[130:131], v27 offset:800\n\t" \
   "v_mfma_f32_16x16x32_bf16 v[52:55], a[52:55], v[104:107], v[52:55]\n\t" "ds_read_b128 v[100:103], v26 offset:13312\n\t" \
   "v_mfma_f32_16x16x32_bf16 v[48:51], a[56:59], v[108:111], v[48:51]\n\t" "s_waitcnt lgkmcnt(8)\n\t" "v_add_f32 v112, v112, v114\n\t" \
   "v_mfma_f32_16x16x32_bf16 v[52:55], a[60:63], v[108:111], v[52:55]\n\t" "ds_read_b128 v[104:107], v26 offset:14336\n\t" "s_waitcnt lgkmcnt(8)\n\t" \
   "v_mfma_f32_16x16x32_bf16 v[48:51], a[64:67], v[80:83], v[48:51]\n\t" "buffer_load_dwordx4 v16, %[rs], s51 offen offset:2048 sc1 lds\n\t" \
   "v_mfma_f32_16x16x32_bf16 v[52:55], a[68:71], v[80:83], v[52:55]\n\t" "ds_read_b128 v[108:111], v26 offset:15360\n\t" \
   "v_mfma_f32_16x16x32_bf16 v[48:51], a[72:75], v[84:87], v[48:51]\n\t" "v_add_f32 v113, v113, v115\n\t" "v_add_f32 v116, v116, v118\n\t" \
   "v_mfma_f32_16x16x32_bf16 v[52:55], a[76:79], v[84:87], v[52:55]\n\t" "ds_read_b128 v[80:83], v26 offset:16384\n\t" "s_waitcnt lgkmcnt(7)\n\t" \
   "v_mfma_f32_16x16x32_bf16 v[48:51], a[80:83], v[88:91], v[48:51]\n\t" "v_add_f32 v117, v117, v119\n\t" "v_add_f32 v112, v112, v120\n\t" \
   "v_mfma_f32_16x16x32_bf16 v[52:55], a[84:87], v[88:91], v[52:55]\n\t" "ds_read_b128 v[84:87], v26 offset:17408\n\t" \
   "v_mfma_f32_16x16x32_bf16 v[48:51], a[88:91], v[92:95], v[48:51]\n\t" "v_add_f32 v113, v113, v121\n\t" "v_add_f32 v112, v112, v116\n\t" \
   "v_mfma_f32_16x16x32_bf16 v[52:55], a[92:95], v[92:95], v[52:55]\n\t" "ds_read_b128 v[88:91], v26 offset:18432\n\t" \
   "buffer_load_dwordx4 v16, %[rs], s51 offen offset:3072 sc1 lds\n\t" "s_add_u32 m0, m0, 4096\n\t" "s_add_u32 s51, s51, 4096\n\t" \
   "s_waitcnt lgkmcnt(5)\n\t" "v_mfma_f32_16x16x32_bf16 v[48:51], a[96:99], v[96:99], v[48:51]\n\t" "v_add_f32 v113, v113, v117\n\t" \
   "v_mul_f32 v112, 0xbacccccd, v112\n\t" "v_mfma_f32_16x16x32_bf16 v[52:55], a[100:103], v[96:99], v[52:55]\n\t" \
   "ds_read_b128 v[92:95], v26 offset:19456\n\t" "v_mfma_f32_16x16x32_bf16 v[48:51], a[104:107], v[100:103], v[48:51]\n\t" \
   "v_mul_f32 v113, 0x3acccccd, v113\n\t" "v_fma_f32 v113, -v112, v112, v113\n\t" \
   "v_mfma_f32_16x16x32_bf16 v[52:55], a[108:111], v[100:103], v[52:55]\n\t" "ds_read_b128 v[96:99], v26 offset:20480\n\t" "s_waitcnt lgkmcnt(5)\n\t" \
   "v_mfma_f32_16x16x32_bf16 v[48:51], a[112:115], v[104:107], v[48:51]\n\t" "v_max_f32 v113, 0, v113\n\t" "v_cvt_pk_bf16_f32 v113, v113, v112\n\t" \
   "v_mfma_f32_16x16x32_bf16 v[52:55], a[116:119], v[104:107], v[52:55]\n\t" "ds_read_b128 v[100:103], v26 offset:21504\n\t" \
   "v_mfma_f32_16x16x32_bf16 v[48:51], a[120:123], v[108:111], v[48:51]\n\t" "buffer_load_dwordx4 v16, %[rs], s51 offen offset:0 sc1 lds\n\t" \
   "v_mfma_f32_16x16x32_bf16 v[52:55], a[124:127], v[108:111], v[52:55]\n\t" "ds_read_b128 v[104:107], v26 offset:22528\n\t" "s_waitcnt lgkmcnt(5)\n\t" \
   "v_mfma_f32_16x16x32_bf16 v[48:51], a[128:131], v[80:83], v[48:51]\n\t" "v_and_b32 v8, 0xffff0000, v113\n\t" "v_lshlrev_b32 v113, 16, v113\n\t" \
   "v_mfma_f32_16x16x32_bf16 v[52:55], a[132:135], v[80:83], v[52:55]\n\t" "ds_read_b128 v[108:111], v26 offset:23552\n\t" \
   "v_mfma_f32_16x16x32_bf16 v[48:51], a[136:139], v[84:87], v[48:51]\n\t" "v_add_f32 v113, 0x3727c5ac, v113\n\t" \
   "v_cvt_pk_bf16_f32 v113, v113, v113\n\t" "v_mfma_f32_16x16x32_bf16 v[52:55], a[140:143], v[84:87], v[52:55]\n\t" \
   "ds_read_b128 v[80:83], v26 offset:24576\n\t" "s_waitcnt lgkmcnt(5)\n\t" "v_mfma_f32_16x16x32_bf16 v[48:51], a[144:147], v[88:91], v[48:51]\n\t" \
   "v_lshlrev_b32 v113, 16, v113\n\t" "v_add_u32 v28, s43, v22\n\t" "v_mfma_f32_16x16x32_bf16 v[52:55], a[148:151], v[88:91], v[52:55]\n\t" \
   "ds_read_b128 v[84:87], v26 offset:25600\n\t" "buffer_load_dwordx4 v16, %[rs], s51 offen offset:1024 sc1 lds\n\t" \
   "v_mfma_f32_16x16x32_bf16 v[48:51], a[152:155], v[92:95], v[48:51]\n\t" "v_sqrt_f32 v113, v113\n\t" \
   "v_mfma_f32_16x16x32_bf16 v[52:55], a[156:159], v[92:95], v[52:55]\n\t" "ds_read_b128 v[88:91], v26 offset:26624\n\t" "s_nop 0\n\t" \
   "s_waitcnt lgkmcnt(5)\n\t" "v_mfma_f32_16x16x32_bf16 v[56:59], a[0:3], v[96:99], 0\n\t" "v_cvt_pk_bf16_f32 v113, v113, v113\n\t" \
   "v_lshlrev_b32 v113, 16, v113\n\t" "v_mfma_f32_16x16x32_bf16 v[60:63], a[4:7], v[96:99], 0\n\t" "ds_read_b128 v[92:95], v26 offset:27648\n\t" \
   "v_rcp_f32 v10, v113\n\t" "v_mfma_f32_16x16x32_bf16 v[56:59], a[8:11], v[100:103], v[56:59]\n\t" "v_add_f32 v122, v122, v124\n\t" \
   "v_mfma_f32_16x16x32_bf16 v[60:63], a[12:15], v[100:103], v[60:63]\n\t" "ds_read_b128 v[96:99], v26 offset:28672\n\t" "s_waitcnt lgkmcnt(5)\n\t" \
   "v_mfma_f32_16x16x32_bf16 v[56:59], a[16:19], v[104:107], v[56:59]\n\t" "buffer_load_dwordx4 v16, %[rs], s51 offen offset:2048 sc1 lds\n\t" \
   "v_mfma_f32_16x16x32_bf16 v[60:63], a[20:23], v[104:107], v[60:63]\n\t" "ds_read_b128 v[100:103], v26 offset:29696\n\t" \
   "v_mfma_f32_16x16x32_bf16 v[56:59], a[24:27], v[108:111], v[56:59]\n\t" "v_add_f32 v123, v123, v125\n\t" "v_add_f32 v126, v126, v128\n\t" \
   "v_mfma_f32_16x16x32_bf16 v[60:63], a[28:31], v[108:111], v[60:63]\n\t" "ds_read_b128 v[104:107], v26 offset:30720\n\t" "s_waitcnt lgkmcnt(5)\n\t" \
   "v_mfma_f32_16x16x32_bf16 v[56:59], a[32:35], v[80:83], v[56:59]\n\t" "v_fmac_f32 v48, v32, v8\n\t" "v_add_f32 v127, v127, v129\n\t" \
   "v_mfma_f32_16x16x32_bf16 v[60:63], a[36:39], v[80:83], v[60:63]\n\t" "ds_read_b128 v[108:111], v26 offset:31744\n\t" \
   "v_mfma_f32_16x16x32_bf16 v[56:59], a[40:43], v[84:87], v[56:59]\n\t" "v_fmac_f32 v49, v33, v8\n\t" "v_add_f32 v122, v122, v130\n\t" \
   "v_mfma_f32_16x16x32_bf16 v[60:63], a[44:47], v[84:87], v[60:63]\n\t" "ds_read_b128 v[80:83], v26 offset:32768\n\t" \
   "buffer_load_dwordx4 v16, %[rs], s51 offen offset:3072 sc1 lds\n\t" "s_add_u32 m0, m0, 4096\n\t" "s_add_u32 s51, s51, 4096\n\t" \
   "s_waitcnt lgkmcnt(5)\n\t" "v_mfma_f32_16x16x32_bf16 v[56:59], a[48:51], v[88:91], v[56:59]\n\t" "v_fmac_f32 v50, v34, v8\n\t" \
   "v_add_f32 v123, v123, v131\n\t" "v_mfma_f32_16x16x32_bf16 v[60:63], a[52:55], v[88:91], v[60:63]\n\t" "ds_read_b128 v[84:87], v26 offset:33792\n\t" \
   "v_mfma_f32_16x16x32_bf16 v[56:59], a[56:59], v[92:95], v[56:59]\n\t" "v_fmac_f32 v51, v35, v8\n\t" "v_add_f32 v122, v122, v126\n\t" \
   "v_mfma_f32_16x16x32_bf16 v[60:63], a[60:63], v[92:95], v[60:63]\n\t" "ds_read_b128 v[88:91], v26 offset:34816\n\t" "s_waitcnt lgkmcnt(5)\n\t" \
   "v_mfma_f32_16x16x32_bf16 v[56:59], a[64:67], v[96:99], v[56:59]\n\t" "v_fmac_f32 v52, v36, v8\n\t" "v_add_f32 v123, v123, v127\n\t" \
   "v_mfma_f32_16x16x32_bf16 v[60:63], a[68:71], v[96:99], v[60:63]\n\t" "ds_read_b128 v[92:95], v26 offset:35840\n\t" \
   "v_mfma_f32_16x16x32_bf16 v[56:59], a[72:75], v[100:103], v[56:59]\n\t" "buffer_load_dwordx4 v16, %[rs], s51 offen offset:0 sc1 lds\n\t" \
   "v_mfma_f32_16x16x32_bf16 v[60:63], a[76:79], v[100:103], v[60:63]\n\t" "ds_read_b128 v[96:99], v26 offset:36864\n\t" "s_waitcnt lgkmcnt(5)\n\t" \
   "v_mfma_f32_16x16x32_bf16 v[56:59], a[80:83], v[104:107], v[56:59]\n\t" "v_fmac_f32 v53, v37, v8\n\t" "v_mul_f32 v122, 0xbacccccd, v122\n\t" \
   "v_mfma_f32_16x16x32_bf16 v[60:63], a[84:87], v[104:107], v[60:63]\n\t" "ds_read_b128 v[100:103], v26 offset:37888\n\t" \
   "v_mfma_f32_16x16x32_bf16 v[56:59], a[88:91], v[108:111], v[56:59]\n\t" "v_fmac_f32 v54, v38, v8\n\t" "v_mul_f32 v123, 0x3acccccd, v123\n\t" \
   "v_mfma_f32_16x16x32_bf16 v[60:63], a[92:95], v[108:111], v[60:63]\n\t" "ds_read_b128 v[104:107], v26 offset:38912\n\t" "s_waitcnt lgkmcnt(5)\n\t" \
   "v_mfma_f32_16x16x32_bf16 v[56:59], a[96:99], v[80:83], v[56:59]\n\t" "v_fmac_f32 v55, v39, v8\n\t" "v_fma_f32 v123, -v122, v122, v123\n\t" \
   "v_mfma_f32_16x16x32_bf16 v[60:63], a[100:103], v[80:83], v[60:63]\n\t" "ds_read_b128 v[108:111], v26 offset:39936\n\t" \
   "buffer_load_dwordx4 v16, %[rs], s51 offen offset:1024 sc1 lds\n\t" "s_add_u32 m0, s53, %[ld1]\n\t" "s_nop 0\n\t" \
   "v_mfma_f32_16x16x32_bf16 v[56:59], a[104:107], v[84:87], v[56:59]\n\t" "v_mul_f32 v48, v48, v10\n\t" "v_max_f32 v123, 0, v123\n\t" \
   "v_mfma_f32_16x16x32_bf16 v[60:63], a[108:111], v[84:87], v[60:63]\n\t" "v_mul_f32 v49, v49, v10\n\t" "s_waitcnt lgkmcnt(4)\n\t" \
   "v_mfma_f32_16x16x32_bf16 v[56:59], a[112:115], v[88:91], v[56:59]\n\t" "v_cvt_pk_bf16_f32 v123, v123, v122\n\t" "v_mul_f32 v50, v50, v10\n\t" \
   "v_mfma_f32_16x16x32_bf16 v[60:63], a[116:119], v[88:91], v[60:63]\n\t" "v_and_b32 v12, 0xffff0000, v123\n\t" "v_mul_f32 v51, v51, v10\n\t" \
   "v_mfma_f32_16x16x32_bf16 v[56:59], a[120:123], v[92:95], v[56:59]\n\t" "v_lshlrev_b32 v123, 16, v123\n\t" "v_mul_f32 v52, v52, v10\n\t" \
   "v_mfma_f32_16x16x32_bf16 v[60:63], a[124:127], v[92:95], v[60:63]\n\t" "v_add_f32 v123, 0x3727c5ac, v123\n\t" "s_waitcnt lgkmcnt(2)\n\t" \
   "v_mfma_f32_16x16x32_bf16 v[56:59], a[128:131], v[96:99], v[56:59]\n\t" "v_mul_f32 v53, v53, v10\n\t" "v_cvt_pk_bf16_f32 v123, v123, v123\n\t" \
   "buffer_load_dwordx4 v17, %[rs], s52 offen sc1 lds\n\t" "v_mfma_f32_16x16x32_bf16 v[60:63], a[132:135], v[96:99], v[60:63]\n\t" \
   "v_mul_f32 v54, v54, v10\n\t" "v_lshlrev_b32 v123, 16, v123\n\t" "v_mfma_f32_16x16x32_bf16 v[56:59], a[136:139], v[100:103], v[56:59]\n\t" \
   "v_mul_f32 v55, v55, v10\n\t" "v_cvt_pk_bf16_f32 v136, v48, v49\n\t" "v_mfma_f32_16x16x32_bf16 v[60:63], a[140:143], v[100:103], v[60:63]\n\t" \
   "v_sqrt_f32 v123, v123\n\t" "s_waitcnt lgkmcnt(0)\n\t" "v_mfma_f32_16x16x32_bf16 v[56:59], a[144:147], v[104:107], v[56:59]\n\t" \
   "v_cvt_pk_bf16_f32 v137, v50, v51\n\t" "s_nop 0\n\t" "v_mfma_f32_16x16x32_bf16 v[60:63], a[148:151], v[104:107], v[60:63]\n\t" \
   "v_cvt_pk_bf16_f32 v138, v52, v53\n\t" "v_cvt_pk_bf16_f32 v123, v123, v123\n\t" \
   "v_mfma_f32_16x16x32_bf16 v[56:59], a[152:155], v[108:111], v[56:59]\n\t" "v_cvt_pk_bf16_f32 v139, v54, v55\n\t" "v_lshlrev_b32 v123, 16, v123\n\t" \
   "ds_write_b128 v14, v[136:139] offset:0\n\t" "v_mfma_f32_16x16x32_bf16 v[60:63], a[156:159], v[108:111], v[60:63]\n\t" "v_rcp_f32 v132, v123\n\t" \
   "v_add_u32 v29, s43, v23\n\t" "s_memrealtime s[54:55]\n\t" "s_nop 6\n\t" "v_fmac_f32 v56, v32, v12\n\t" "v_fmac_f32 v57, v33, v12\n\t" \
   "v_fmac_f32 v58, v34, v12\n\t" "v_fmac_f32 v59, v35, v12\n\t" "v_fmac_f32 v60, v36, v12\n\t" "v_fmac_f32 v61, v37, v12\n\t" \
   "v_fmac_f32 v62, v38, v12\n\t" "v_fmac_f32 v63, v39, v12\n\t" "v_mul_f32 v56, v56, v132\n\t" "v_mul_f32 v57, v57, v132\n\t" \
   "v_mul_f32 v58, v58, v132\n\t" "v_mul_f32 v59, v59, v132\n\t" "v_mul_f32 v60, v60, v132\n\t" "v_mul_f32 v61, v61, v132\n\t" \
   "v_mul_f32 v62, v62, v132\n\t" "v_mul_f32 v63, v63, v132\n\t" "v_cvt_pk_bf16_f32 v140, v56, v57\n\t" "v_cvt_pk_bf16_f32 v141, v58, v59\n\t" \
   "v_cvt_pk_bf16_f32 v142, v60, v61\n\t" "v_cvt_pk_bf16_f32 v143, v62, v63\n\t" "ds_write_b128 v14, v[140:143] offset:4096\n\t" \
   "s_waitcnt vmcnt(11) lgkmcnt(0)\n\t" "s_barrier\n\t" "ds_read_b128 v[80:83], v28 offset:0\n\t" "ds_read_b128 v[84:87], v28 offset:1024\n\t" \
   "ds_read_b128 v[88:91], v28 offset:2048\n\t" "ds_read_b128 v[92:95], v28 offset:3072\n\t" "ds_read_b128 v[96:99], v28 offset:4096\n\t" \
   "ds_read_b128 v[100:103], v28 offset:5120\n\t" "ds_read_b128 v[104:107], v28 offset:6144\n\t" "s_add_u32 s45, s40, 3\n\t" \
   "s_min_u32 s45, s45, 63\n\t" "s_mov_b32 s46, s42\n\t" "s_lshr_b32 s48, s45, 1\n\t" "s_bitcmp1_b32 s44, s48\n\t" "s_cbranch_scc0 176f\n\t" "76:\n\t" \
   "s_mul_i32 s52, s45, 1536\n\t" "s_mul_i32 s51, s45, 40960\n\t" "s_add_u32 m0, s46, %[ldx]\n\t" "s_mov_b32 s53, s46\n\t" \
   "buffer_load_dwordx4 v16, %[rs], s51 offen offset:0 sc1 lds\n\t" "s_mov_b32 s42, s43\n\t" "s_add_u32 s43, s43, 44032\n\t" \
   "s_cmp_eq_u32 s43, 132096\n\t" "s_cselect_b32 s43, 0, s43\n\t" "s_add_u32 s41, s41, 1\n\t" "s_add_u32 s40, s40, 1\n\t" \
   "v_xor_b32 v14, 0x2000, v14\n\t" "v_xor_b32 v15, 0x2000, v15\n\t" "v_mov_b32 v31, s54\n\t" "v_min_u32 v31, -2, v31\n\t" "1:\n\t" \
   "v_mov_b32 v26, v28\n\t" "v_mov_b32 v27, v29\n\t" "s_waitcnt lgkmcnt(5)\n\t" "v_mfma_f32_16x16x32_bf16 v[48:51], a[0:3], v[80:83], 0\n\t" \
   "ds_read_b128 v[152:155], v15 offset:0\n\t" "ds_read_b128 v[168:171], v15 offset:4096\n\t" \
   "v_mfma_f32_16x16x32_bf16 v[52:55], a[4:7], v[80:83], 0\n\t" "ds_read_b128 v[108:111], v26 offset:7168\n\t" \
   "ds_read_b128 v[156:159], v15 offset:1024\n\t" "v_mfma_f32_16x16x32_bf16 v[48:51], a[8:11], v[84:87], v[48:51]\n\t" \
   "ds_read_b128 v[172:175], v15 offset:5120\n\t" "v_mfma_f32_16x16x32_bf16 v[52:55], a[12:15], v[84:87], v[52:55]\n\t" \
   "ds_read_b128 v[80:83], v26 offset:8192\n\t" "ds_read_b128 v[160:163], v15 offset:2048\n\t" "s_waitcnt lgkmcnt(10)\n\t" \
   "v_mfma_f32_16x16x32_bf16 v[48:51], a[16:19], v[88:91], v[48:51]\n\t" "ds_read_b128 v[176:179], v15 offset:6144\n\t" \
   "v_mfma_f32_16x16x32_bf16 v[52:55], a[20:23], v[88:91], v[52:55]\n\t" "ds_read_b128 v[84:87], v26 offset:9216\n\t" \
   "ds_read_b128 v[164:167], v15 offset:3072\n\t" "v_mfma_f32_16x16x32_bf16 v[48:51], a[24:27], v[92:95], v[48:51]\n\t" \
   "ds_read_b128 v[180:183], v15 offset:7168\n\t" "v_mfma_f32_16x16x32_bf16 v[52:55], a[28:31], v[92:95], v[52:55]\n\t" \
   "ds_read_b128 v[88:91], v26 offset:10240\n\t" "s_waitcnt lgkmcnt(13)\n\t" "v_mfma_f32_16x16x32_bf16 v[48:51], a[32:35], v[96:99], v[48:51]\n\t" \
   "v_cvt_pk_bf16_f32 v148, v72, v73\n\t" "v_cvt_pk_bf16_f32 v150, v74, v75\n\t" "v_mfma_f32_16x16x32_bf16 v[52:55], a[36:39], v[96:99], v[52:55]\n\t" \
   "ds_read_b128 v[92:95], v26 offset:11264\n\t" "s_waitcnt lgkmcnt(8)\n\t" "v_mfma_f32_16x16x32_bf16 v[64:67], v[152:155], a[160:163], 0\n\t" \
   "v_cvt_pk_bf16_f32 v149, v76, v77\n\t" "v_cvt_pk_bf16_f32 v151, v78, v79\n\t" \
   "v_mfma_f32_16x16x32_bf16 v[48:51], a[40:43], v[100:103], v[48:51]\n\t" "s_add_u32 s50, s49, 294912\n\t" \
   "v_mfma_f32_16x16x32_bf16 v[52:55], a[44:47], v[100:103], v[52:55]\n\t" "ds_read_b128 v[96:99], v26 offset:12288\n\t" \
   "v_mfma_f32_16x16x32_bf16 v[68:71], v[168:171], a[160:163], 0\n\t" "v_mfma_f32_16x16x32_bf16 v[48:51], a[48:51], v[104:107], v[48:51]\n\t" \
   "buffer_store_dwordx4 v[148:151], v18, %[rs], s50 offen sc1\n\t" "v_mfma_f32_16x16x32_bf16 v[52:55], a[52:55], v[104:107], v[52:55]\n\t" \
   "ds_read_b128 v[100:103], v26 offset:13312\n\t" "s_sub_i32 s49, s41, 1\n\t" \
   "v_mfma_f32_16x16x32_bf16 v[64:67], v[156:159], a[164:167], v[64:67]\n\t" "s_mul_i32 s49, s49, 1310720\n\t" \
   "v_mfma_f32_16x16x32_bf16 v[48:51], a[56:59], v[108:111], v[48:51]\n\t" "ds_read_b128 v[112:115], v27 offset:0\n\t" \
   "v_mfma_f32_16x16x32_bf16 v[52:55], a[60:63], v[108:111], v[52:55]\n\t" "ds_read_b128 v[104:107], v26 offset:14336\n\t" \
   "v_mfma_f32_16x16x32_bf16 v[68:71], v[172:175], a[164:167], v[68:71]\n\t" "s_waitcnt lgkmcnt(8)\n\t" \
   "v_mfma_f32_16x16x32_bf16 v[48:51], a[64:67], v[80:83], v[48:51]\n\t" "ds_read_b128 v[116:119], v27 offset:16\n\t" \
   "v_mfma_f32_16x16x32_bf16 v[52:55], a[68:71], v[80:83], v[52:55]\n\t" "ds_read_b128 v[108:111], v26 offset:15360\n\t" \
   "ds_read_b64 v[120:121], v27 offset:32\n\t" "s_waitcnt lgkmcnt(9)\n\t" "v_mfma_f32_16x16x32_bf16 v[64:67], v[160:163], a[168:171], v[64:67]\n\t" \
   "ds_read_b128 v[122:125], v27 offset:768\n\t" "v_mfma_f32_16x16x32_bf16 v[48:51], a[72:75], v[84:87], v[48:51]\n\t" \
   "ds_read_b128 v[126:129], v27 offset:784\n\t" "v_mfma_f32_16x16x32_bf16 v[52:55], a[76:79], v[84:87], v[52:55]\n\t" \
   "ds_read_b128 v[80:83], v26 offset:16384\n\t" "ds_read_b64 v[130:131], v27 offset:800\n\t" \
   "v_mfma_f32_16x16x32_bf16 v[68:71], v[176:179], a[168:171], v[68:71]\n\t" "v_add_u32 v28, s43, v22\n\t" "s_waitcnt lgkmcnt(11)\n\t" \
   "v_mfma_f32_16x16x32_bf16 v[48:51], a[80:83], v[88:91], v[48:51]\n\t" "buffer_load_dwordx4 v16, %[rs], s51 offen offset:1024 sc1 lds\n\t" \
   "v_mfma_f32_16x16x32_bf16 v[52:55], a[84:87], v[88:91], v[52:55]\n\t" "ds_read_b128 v[84:87], v26 offset:17408\n\t" \
   "v_mfma_f32_16x16x32_bf16 v[48:51], a[88:91], v[92:95], v[48:51]\n\t" "s_waitcnt lgkmcnt(5)\n\t" "v_add_f32 v112, v112, v114\n\t" \
   "v_mfma_f32_16x16x32_bf16 v[64:67], v[164:167], a[172:175], v[64:67]\n\t" "v_add_f32 v113, v113, v115\n\t" "v_add_f32 v116, v116, v118\n\t" \
   "v_mfma_f32_16x16x32_bf16 v[52:55], a[92:95], v[92:95], v[52:55]\n\t" "ds_read_b128 v[88:91], v26 offset:18432\n\t" \
   "v_mfma_f32_16x16x32_bf16 v[48:51], a[96:99], v[96:99], v[48:51]\n\t" "v_add_f32 v117, v117, v119\n\t" "v_add_f32 v112, v112, v120\n\t" \
   "v_mfma_f32_16x16x32_bf16 v[68:71], v[180:183], a[172:175], v[68:71]\n\t" "v_add_f32 v113, v113, v121\n\t" "v_add_f32 v112, v112, v116\n\t" \
   "v_mfma_f32_16x16x32_bf16 v[52:55], a[100:103], v[96:99], v[52:55]\n\t" "ds_read_b128 v[92:95], v26 offset:19456\n\t" \
   "v_mfma_f32_16x16x32_bf16 v[48:51], a[104:107], v[100:103], v[48:51]\n\t" "v_add_f32 v113, v113, v117\n\t" "v_mul_f32 v112, 0xbacccccd, v112\n\t" \
   "v_mfma_f32_16x16x32_bf16 v[72:75], v[152:155], a[176:179], 0\n\t" "v_cvt_pk_bf16_f32 v144, v64, v65\n\t" "v_mul_f32 v113, 0x3acccccd, v113\n\t" \
   "v_mfma_f32_16x16x32_bf16 v[52:55], a[108:111], v[100:103], v[52:55]\n\t" "ds_read_b128 v[96:99], v26 offset:20480\n\t" \
   "buffer_load_dwordx4 v16, %[rs], s51 offen offset:2048 sc1 lds\n\t" "v_mfma_f32_16x16x32_bf16 v[48:51], a[112:115], v[104:107], v[48:51]\n\t" \
   "v_cvt_pk_bf16_f32 v146, v66, v67\n\t" "v_cvt_pk_bf16_f32 v145, v68, v69\n\t" "v_mfma_f32_16x16x32_bf16 v[76:79], v[168:171], a[176:179], 0\n\t" \
   "v_fma_f32 v113, -v112, v112, v113\n\t" "v_cvt_pk_bf16_f32 v147, v70, v71\n\t" \
   "v_mfma_f32_16x16x32_bf16 v[52:55], a[116:119], v[104:107], v[52:55]\n\t" "ds_read_b128 v[100:103], v26 offset:21504\n\t" \
   "v_mfma_f32_16x16x32_bf16 v[48:51], a[120:123], v[108:111], v[48:51]\n\t" "s_add_u32 s50, s49, 0\n\t" "v_max_f32 v113, 0, v113\n\t" \
   "v_mfma_f32_16x16x32_bf16 v[72:75], v[156:159], a[180:183], v[72:75]\n\t" "buffer_store_dwordx4 v[144:147], v21, %[rs], s50 offen sc1\n\t" \
   "v_mfma_f32_16x16x32_bf16 v[52:55], a[124:127], v[108:111], v[52:55]\n\t" "ds_read_b128 v[104:107], v26 offset:22528\n\t" "s_waitcnt lgkmcnt(5)\n\t" \
   "v_mfma_f32_16x16x32_bf16 v[48:51], a[128:131], v[80:83], v[48:51]\n\t" "v_cvt_pk_bf16_f32 v113, v113, v112\n\t" \
   "v_and_b32 v8, 0xffff0000, v113\n\t" "v_mfma_f32_16x16x32_bf16 v[52:55], a[132:135], v[80:83], v[52:55]\n\t" \
   "ds_read_b128 v[108:111], v26 offset:23552\n\t" "v_mfma_f32_16x16x32_bf16 v[76:79], v[172:175], a[180:183], v[76:79]\n\t" \
   "v_lshlrev_b32 v113, 16, v113\n\t" "v_add_f32 v113, 0x3727c5ac, v113\n\t" "v_mfma_f32_16x16x32_bf16 v[48:51], a[136:139], v[84:87], v[48:51]\n\t" \
   "buffer_load_dwordx4 v16, %[rs], s51 offen offset:3072 sc1 lds\n\t" "s_add_u32 m0, m0, 4096\n\t" "s_add_u32 s51, s51, 4096\n\t" \
   "v_mfma_f32_16x16x32_bf16 v[52:55], a[140:143], v[84:87], v[52:55]\n\t" "ds_read_b128 v[80:83], v26 offset:24576\n\t" \
   "v_mfma_f32_16x16x32_bf16 v[72:75], v[160:163], a[184:187], v[72:75]\n\t" "v_cvt_pk_bf16_f32 v113, v113, v113\n\t" "s_waitcnt lgkmcnt(5)\n\t" \
   "v_mfma_f32_16x16x32_bf16 v[48:51], a[144:147], v[88:91], v[48:51]\n\t" "v_lshlrev_b32 v113, 16, v113\n\t" "v_add_u32 v29, s43, v23\n\t" \
   "v_mfma_f32_16x16x32_bf16 v[52:55], a[148:151], v[88:91], v[52:55]\n\t" "ds_read_b128 v[84:87], v26 offset:25600\n\t" \
   "v_mfma_f32_16x16x32_bf16 v[76:79], v[176:179], a[184:187], v[76:79]\n\t" "v_sqrt_f32 v113, v113\n\t" \
   "v_mfma_f32_16x16x32_bf16 v[48:51], a[152:155], v[92:95], v[48:51]\n\t" "s_nop 0\n\t" "v_cvt_pk_bf16_f32 v113, v113, v113\n\t" \
   "v_mfma_f32_16x16x32_bf16 v[52:55], a[156:159], v[92:95], v[52:55]\n\t" "ds_read_b128 v[88:91], v26 offset:26624\n\t" \
   "v_mfma_f32_16x16x32_bf16 v[72:75], v[164:167], a[188:191], v[72:75]\n\t" "v_lshlrev_b32 v113, 16, v113\n\t" "v_rcp_f32 v10, v113\n\t" \
   "s_waitcnt lgkmcnt(5)\n\t" "v_mfma_f32_16x16x32_bf16 v[56:59], a[0:3], v[96:99], 0\n\t" \
   "buffer_load_dwordx4 v16, %[rs], s51 offen offset:0 sc1 lds\n\t" "v_mfma_f32_16x16x32_bf16 v[60:63], a[4:7], v[96:99], 0\n\t" \
   "ds_read_b128 v[92:95], v26 offset:27648\n\t" "v_mfma_f32_16x16x32_bf16 v[76:79], v[180:183], a[188:191], v[76:79]\n\t" \
   "v_add_f32 v122, v122, v124\n\t" "v_mfma_f32_16x16x32_bf16 v[56:59], a[8:11], v[100:103], v[56:59]\n\t" "v_add_f32 v123, v123, v125\n\t" \
   "v_add_f32 v126, v126, v128\n\t" "v_mfma_f32_16x16x32_bf16 v[60:63], a[12:15], v[100:103], v[60:63]\n\t" \
   "ds_read_b128 v[96:99], v26 offset:28672\n\t" "s_waitcnt lgkmcnt(5)\n\t" "v_mfma_f32_16x16x32_bf16 v[56:59], a[16:19], v[104:107], v[56:59]\n\t" \
   "v_cvt_pk_bf16_f32 v148, v72, v73\n\t" "v_cvt_pk_bf16_f32 v150, v74, v75\n\t" "v_mfma_f32_16x16x32_bf16 v[64:67], v[152:155], a[192:195], 0\n\t" \
   "v_cvt_pk_bf16_f32 v149, v76, v77\n\t" "v_cvt_pk_bf16_f32 v151, v78, v79\n\t" \
   "v_mfma_f32_16x16x32_bf16 v[60:63], a[20:23], v[104:107], v[60:63]\n\t" "ds_read_b128 v[100:103], v26 offset:29696\n\t" \
   "v_mfma_f32_16x16x32_bf16 v[56:59], a[24:27], v[108:111], v[56:59]\n\t" "s_add_u32 s50, s49, 32768\n\t" "v_fmac_f32 v48, v32, v8\n\t" \
   "v_mfma_f32_16x16x32_bf16 v[68:71], v[168:171], a[192:195], 0\n\t" "buffer_store_dwordx4 v[148:151], v21, %[rs], s50 offen sc1\n\t" \
   "v_mfma_f32_16x16x32_bf16 v[60:63], a[28:31], v[108:111], v[60:63]\n\t" "ds_read_b128 v[104:107], v26 offset:30720\n\t" "s_waitcnt lgkmcnt(5)\n\t" \
   "v_mfma_f32_16x16x32_bf16 v[56:59], a[32:35], v[80:83], v[56:59]\n\t" "buffer_load_dwordx4 v16, %[rs], s51 offen offset:1024 sc1 lds\n\t" \
   "v_mfma_f32_16x16x32_bf16 v[64:67], v[156:159], a[196:199], v[64:67]\n\t" "v_add_f32 v127, v127, v129\n\t" "v_fmac_f32 v49, v33, v8\n\t" \
   "v_mfma_f32_16x16x32_bf16 v[60:63], a[36:39], v[80:83], v[60:63]\n\t" "ds_read_b128 v[108:111], v26 offset:31744\n\t" \
   "v_mfma_f32_16x16x32_bf16 v[56:59], a[40:43], v[84:87], v[56:59]\n\t" "v_add_f32 v122, v122, v130\n\t" "v_fmac_f32 v50, v34, v8\n\t" \
   "v_mfma_f32_16x16x32_bf16 v[68:71], v[172:175], a[196:199], v[68:71]\n\t" "v_add_f32 v123, v123, v131\n\t" "v_fmac_f32 v51, v35, v8\n\t" \
   "v_mfma_f32_16x16x32_bf16 v[60:63], a[44:47], v[84:87], v[60:63]\n\t" "ds_read_b128 v[80:83], v26 offset:32768\n\t" "s_waitcnt lgkmcnt(5)\n\t" \
   "v_mfma_f32_16x16x32_bf16 v[56:59], a[48:51], v[88:91], v[56:59]\n\t" "v_add_f32 v122, v122, v126\n\t" "v_fmac_f32 v52, v36, v8\n\t" \
   "v_mfma_f32_16x16x32_bf16 v[64:67], v[160:163], a[200:203], v[64:67]\n\t" "v_add_f32 v123, v123, v127\n\t" "v_fmac_f32 v53, v37, v8\n\t" \
   "v_mfma_f32_16x16x32_bf16 v[60:63], a[52:55], v[88:91], v[60:63]\n\t" "ds_read_b128 v[84:87], v26 offset:33792\n\t" \
   "v_mfma_f32_16x16x32_bf16 v[56:59], a[56:59], v[92:95], v[56:59]\n\t" "s_cmp_lt_i32 s40, 2\n\t" "s_cselect_b32 s47, 0x7ffffff0, 0\n\t" \
   "v_mfma_f32_16x16x32_bf16 v[68:71], v[176:179], a[200:203], v[68:71]\n\t" "v_or_b32 v30, s47, v20\n\t" "s_sub_i32 s47, s41, 2\n\t" \
   "buffer_load_dwordx4 v16, %[rs], s51 offen offset:2048 sc1 lds\n\t" "v_mfma_f32_16x16x32_bf16 v[60:63], a[60:63], v[92:95], v[60:63]\n\t" \
   "ds_read_b128 v[88:91], v26 offset:34816\n\t" "s_max_i32 s47, s47, 0\n\t" "s_waitcnt lgkmcnt(5)\n\t" \
   "v_mfma_f32_16x16x32_bf16 v[56:59], a[64:67], v[96:99], v[56:59]\n\t" "s_lshl_b32 s47, s47, 12\n\t" "s_waitcnt vmcnt(8)\n\t" \
   "v_mfma_f32_16x16x32_bf16 v[60:63], a[68:71], v[96:99], v[60:63]\n\t" "ds_read_b128 v[92:95], v26 offset:35840\n\t" \
   "buffer_store_dword v31, v30, %[rs], s47 offen sc1\n\t" "v_mfma_f32_16x16x32_bf16 v[64:67], v[164:167], a[204:207], v[64:67]\n\t" \
   "v_fmac_f32 v54, v38, v8\n\t" "v_mul_f32 v122, 0xbacccccd, v122\n\t" "v_mfma_f32_16x16x32_bf16 v[56:59], a[72:75], v[100:103], v[56:59]\n\t" \
   "v_mul_f32 v123, 0x3acccccd, v123\n\t" "v_fmac_f32 v55, v39, v8\n\t" "v_mfma_f32_16x16x32_bf16 v[60:63], a[76:79], v[100:103], v[60:63]\n\t" \
   "ds_read_b128 v[96:99], v26 offset:36864\n\t" "v_mfma_f32_16x16x32_bf16 v[68:71], v[180:183], a[204:207], v[68:71]\n\t" \
   "v_fma_f32 v123, -v122, v122, v123\n\t" "s_waitcnt lgkmcnt(5)\n\t" "v_mfma_f32_16x16x32_bf16 v[56:59], a[80:83], v[104:107], v[56:59]\n\t" \
   "v_mul_f32 v48, v48, v10\n\t" "v_max_f32 v123, 0, v123\n\t" "v_mfma_f32_16x16x32_bf16 v[60:63], a[84:87], v[104:107], v[60:63]\n\t" \
   "ds_read_b128 v[100:103], v26 offset:37888\n\t" "v_mfma_f32_16x16x32_bf16 v[72:75], v[152:155], a[208:211], 0\n\t" \
   "buffer_load_dwordx4 v16, %[rs], s51 offen offset:3072 sc1 lds\n\t" "s_add_u32 m0, m0, 4096\n\t" "s_add_u32 s51, s51, 4096\n\t" \
   "v_mfma_f32_16x16x32_bf16 v[56:59], a[88:91], v[108:111], v[56:59]\n\t" "v_cvt_pk_bf16_f32 v144, v64, v65\n\t" \
   "v_cvt_pk_bf16_f32 v146, v66, v67\n\t" "v_mfma_f32_16x16x32_bf16 v[60:63], a[92:95], v[108:111], v[60:63]\n\t" \
   "ds_read_b128 v[104:107], v26 offset:38912\n\t" "v_mfma_f32_16x16x32_bf16 v[76:79], v[168:171], a[208:211], 0\n\t" \
   "v_cvt_pk_bf16_f32 v145, v68, v69\n\t" "s_waitcnt lgkmcnt(5)\n\t" "v_mfma_f32_16x16x32_bf16 v[56:59], a[96:99], v[80:83], v[56:59]\n\t" \
   "v_cvt_pk_bf16_f32 v147, v70, v71\n\t" "s_add_u32 s50, s49, 65536\n\t" "v_mfma_f32_16x16x32_bf16 v[60:63], a[100:103], v[80:83], v[60:63]\n\t" \
   "ds_read_b128 v[108:111], v26 offset:39936\n\t" "v_mfma_f32_16x16x32_bf16 v[72:75], v[156:159], a[212:215], v[72:75]\n\t" \
   "buffer_store_dwordx4 v[144:147], v21, %[rs], s50 offen sc1\n\t" "v_mfma_f32_16x16x32_bf16 v[56:59], a[104:107], v[84:87], v[56:59]\n\t" \
   "v_mul_f32 v49, v49, v10\n\t" "v_mul_f32 v50, v50, v10\n\t" "v_mfma_f32_16x16x32_bf16 v[60:63], a[108:111], v[84:87], v[60:63]\n\t" \
   "v_cvt_pk_bf16_f32 v123, v123, v122\n\t" "s_waitcnt lgkmcnt(4)\n\t" "v_mfma_f32_16x16x32_bf16 v[56:59], a[112:115], v[88:91], v[56:59]\n\t" \
   "buffer_load_dwordx4 v16, %[rs], s51 offen offset:0 sc1 lds\n\t" "v_mfma_f32_16x16x32_bf16 v[76:79], v[172:175], a[212:215], v[76:79]\n\t" \
   "v_mul_f32 v51, v51, v10\n\t" "v_and_b32 v12, 0xffff0000, v123\n\t" "v_mfma_f32_16x16x32_bf16 v[60:63], a[116:119], v[88:91], v[60:63]\n\t" \
   "v_mul_f32 v52, v52, v10\n\t" "v_lshlrev_b32 v123, 16, v123\n\t" "v_mfma_f32_16x16x32_bf16 v[56:59], a[120:123], v[92:95], v[56:59]\n\t" \
   "v_mul_f32 v53, v53, v10\n\t" "v_add_f32 v123, 0x3727c5ac, v123\n\t" "v_mfma_f32_16x16x32_bf16 v[72:75], v[160:163], a[216:219], v[72:75]\n\t" \
   "v_mul_f32 v54, v54, v10\n\t" "v_cvt_pk_bf16_f32 v123, v123, v123\n\t" "v_mfma_f32_16x16x32_bf16 v[60:63], a[124:127], v[92:95], v[60:63]\n\t" \
   "v_mul_f32 v55, v55, v10\n\t" "s_waitcnt lgkmcnt(2)\n\t" "v_mfma_f32_16x16x32_bf16 v[56:59], a[128:131], v[96:99], v[56:59]\n\t" \
   "v_lshlrev_b32 v123, 16, v123\n\t" "v_cvt_pk_bf16_f32 v136, v48, v49\n\t" "v_mfma_f32_16x16x32_bf16 v[76:79], v[176:179], a[216:219], v[76:79]\n\t" \
   "v_sqrt_f32 v123, v123\n\t" "v_mfma_f32_16x16x32_bf16 v[60:63], a[132:135], v[96:99], v[60:63]\n\t" "v_cvt_pk_bf16_f32 v137, v50, v51\n\t" \
   "s_nop 0\n\t" "v_mfma_f32_16x16x32_bf16 v[56:59], a[136:139], v[100:103], v[56:59]\n\t" "v_cvt_pk_bf16_f32 v138, v52, v53\n\t" \
   "v_cvt_pk_bf16_f32 v123, v123, v123\n\t" "v_mfma_f32_16x16x32_bf16 v[72:75], v[164:167], a[220:223], v[72:75]\n\t" \
   "buffer_load_dwordx4 v16, %[rs], s51 offen offset:1024 sc1 lds\n\t" "s_add_u32 m0, s53, %[ld1]\n\t" "s_nop 0\n\t" \
   "v_mfma_f32_16x16x32_bf16 v[60:63], a[140:143], v[100:103], v[60:63]\n\t" "v_cvt_pk_bf16_f32 v139, v54, v55\n\t" "s_waitcnt lgkmcnt(0)\n\t" \
   "v_mfma_f32_16x16x32_bf16 v[56:59], a[144:147], v[104:107], v[56:59]\n\t" "v_lshlrev_b32 v123, 16, v123\n\t" \
   "v_mfma_f32_16x16x32_bf16 v[76:79], v[180:183], a[220:223], v[76:79]\n\t" "ds_write_b128 v14, v[136:139] offset:0\n\t" \
   "v_mfma_f32_16x16x32_bf16 v[60:63], a[148:151], v[104:107], v[60:63]\n\t" "v_rcp_f32 v132, v123\n\t" \
   "v_mfma_f32_16x16x32_bf16 v[56:59], a[152:155], v[108:111], v[56:59]\n\t" "buffer_load_dwordx4 v17, %[rs], s52 offen sc1 lds\n\t" \
   "v_mfma_f32_16x16x32_bf16 v[60:63], a[156:159], v[108:111], v[60:63]\n\t" "v_cvt_pk_bf16_f32 v148, v72, v73\n\t" \
   "v_cvt_pk_bf16_f32 v150, v74, v75\n\t" "v_mfma_f32_16x16x32_bf16 v[64:67], v[152:155], a[224:227], 0\n\t" "v_cvt_pk_bf16_f32 v149, v76, v77\n\t" \
   "v_cvt_pk_bf16_f32 v151, v78, v79\n\t" "v_mfma_f32_16x16x32_bf16 v[68:71], v[168:171], a[224:227], 0\n\t" "s_add_u32 s50, s49, 98304\n\t" \
   "v_mfma_f32_16x16x32_bf16 v[64:67], v[156:159], a[228:231], v[64:67]\n\t" "v_fmac_f32 v56, v32, v12\n\t" "v_fmac_f32 v57, v33, v12\n\t" \
   "v_mfma_f32_16x16x32_bf16 v[68:71], v[172:175], a[228:231], v[68:71]\n\t" "v_fmac_f32 v58, v34, v12\n\t" "v_fmac_f32 v59, v35, v12\n\t" \
   "v_mfma_f32_16x16x32_bf16 v[64:67], v[160:163], a[232:235], v[64:67]\n\t" "v_fmac_f32 v60, v36, v12\n\t" "v_fmac_f32 v61, v37, v12\n\t" \
   "v_mfma_f32_16x16x32_bf16 v[68:71], v[176:179], a[232:235], v[68:71]\n\t" "v_fmac_f32 v62, v38, v12\n\t" "v_fmac_f32 v63, v39, v12\n\t" \
   "buffer_store_dwordx4 v[148:151], v21, %[rs], s50 offen sc1\n\t" "v_mfma_f32_16x16x32_bf16 v[64:67], v[164:167], a[236:239], v[64:67]\n\t" \
   "v_mul_f32 v56, v56, v132\n\t" "v_mul_f32 v57, v57, v132\n\t" "v_mfma_f32_16x16x32_bf16 v[68:71], v[180:183], a[236:239], v[68:71]\n\t" \
   "v_mul_f32 v58, v58, v132\n\t" "v_mul_f32 v59, v59, v132\n\t" "v_mfma_f32_16x16x32_bf16 v[72:75], v[152:155], a[240:243], 0\n\t" \
   "v_mul_f32 v60, v60, v132\n\t" "v_mul_f32 v61, v61, v132\n\t" "v_mfma_f32_16x16x32_bf16 v[76:79], v[168:171], a[240:243], 0\n\t" \
   "v_mul_f32 v62, v62, v132\n\t" "v_mul_f32 v63, v63, v132\n\t" "v_mfma_f32_16x16x32_bf16 v[72:75], v[156:159], a[244:247], v[72:75]\n\t" \
   "v_cvt_pk_bf16_f32 v144, v64, v65\n\t" "v_cvt_pk_bf16_f32 v146, v66, v67\n\t" \
   "v_mfma_f32_16x16x32_bf16 v[76:79], v[172:175], a[244:247], v[76:79]\n\t" "v_cvt_pk_bf16_f32 v145, v68, v69\n\t" \
   "v_cvt_pk_bf16_f32 v140, v56, v57\n\t" "v_mfma_f32_16x16x32_bf16 v[72:75], v[160:163], a[248:251], v[72:75]\n\t" \
   "v_cvt_pk_bf16_f32 v147, v70, v71\n\t" "v_cvt_pk_bf16_f32 v141, v58, v59\n\t" \
   "v_mfma_f32_16x16x32_bf16 v[76:79], v[176:179], a[248:251], v[76:79]\n\t" "s_add_u32 s50, s49, 131072\n\t" "v_cvt_pk_bf16_f32 v142, v60, v61\n\t" \
   "v_mfma_f32_16x16x32_bf16 v[72:75], v[164:167], a[252:255], v[72:75]\n\t" "v_cvt_pk_bf16_f32 v143, v62, v63\n\t" \
   "v_mfma_f32_16x16x32_bf16 v[76:79], v[180:183], a[252:255], v[76:79]\n\t" "buffer_store_dwordx4 v[144:147], v21, %[rs], s50 offen sc1\n\t" \
   "v_mfma_f32_16x16x32_bf16 v[64:67], v[152:155], v[192:195], 0\n\t" "ds_write_b128 v14, v[140:143] offset:4096\n\t" \
   "v_mfma_f32_16x16x32_bf16 v[68:71], v[168:171], v[192:195], 0\n\t" "v_mfma_f32_16x16x32_bf16 v[64:67], v[156:159], v[196:199], v[64:67]\n\t" \
   "v_cvt_pk_bf16_f32 v148, v72, v73\n\t" "v_cvt_pk_bf16_f32 v150, v74, v75\n\t" \
   "v_mfma_f32_16x16x32_bf16 v[68:71], v[172:175], v[196:199], v[68:71]\n\t" "v_cvt_pk_bf16_f32 v149, v76, v77\n\t" \
   "v_cvt_pk_bf16_f32 v151, v78, v79\n\t" "s_waitcnt vmcnt(16) lgkmcnt(0)\n\t" "s_barrier\n\t" \
   "v_mfma_f32_16x16x32_bf16 v[64:67], v[160:163], v[200:203], v[64:67]\n\t" "s_add_u32 s45, s40, 3\n\t" "s_min_u32 s45, s45, 63\n\t" \
   "v_mfma_f32_16x16x32_bf16 v[68:71], v[176:179], v[200:203], v[68:71]\n\t" "s_add_u32 s50, s49, 163840\n\t" "s_mov_b32 s46, s42\n\t" \
   "v_mfma_f32_16x16x32_bf16 v[64:67], v[164:167], v[204:207], v[64:67]\n\t" "s_lshr_b32 s48, s45, 1\n\t" "s_bitcmp1_b32 s44, s48\n\t" \
   "s_cbranch_scc0 174f\n\t" "74:\n\t" "v_mfma_f32_16x16x32_bf16 v[68:71], v[180:183], v[204:207], v[68:71]\n\t" \
   "buffer_store_dwordx4 v[148:151], v21, %[rs], s50 offen sc1\n\t" "v_mfma_f32_16x16x32_bf16 v[72:75], v[152:155], v[208:211], 0\n\t" \
   "s_mul_i32 s52, s45, 1536\n\t" "s_mul_i32 s51, s45, 40960\n\t" "s_add_u32 m0, s46, %[ldx]\n\t" "s_mov_b32 s53, s46\n\t" \
   "v_mfma_f32_16x16x32_bf16 v[76:79], v[168:171], v[208:211], 0\n\t" "buffer_load_dwordx4 v16, %[rs], s51 offen offset:0 sc1 lds\n\t" \
   "v_mfma_f32_16x16x32_bf16 v[72:75], v[156:159], v[212:215], v[72:75]\n\t" "v_cvt_pk_bf16_f32 v144, v64, v65\n\t" \
   "v_cvt_pk_bf16_f32 v146, v66, v67\n\t" "v_mfma_f32_16x16x32_bf16 v[76:79], v[172:175], v[212:215], v[76:79]\n\t" \
   "v_cvt_pk_bf16_f32 v145, v68, v69\n\t" "v_cvt_pk_bf16_f32 v147, v70, v71\n\t" \
   "v_mfma_f32_16x16x32_bf16 v[72:75], v[160:163], v[216:219], v[72:75]\n\t" "ds_read_b128 v[80:83], v28 offset:0\n\t" \
   "v_mfma_f32_16x16x32_bf16 v[76:79], v[176:179], v[216:219], v[76:79]\n\t" "s_add_u32 s50, s49, 196608\n\t" "s_mov_b32 s42, s43\n\t" \
   "v_mfma_f32_16x16x32_bf16 v[72:75], v[164:167], v[220:223], v[72:75]\n\t" "ds_read_b128 v[84:87], v28 offset:1024\n\t" \
   "v_mfma_f32_16x16x32_bf16 v[76:79], v[180:183], v[220:223], v[76:79]\n\t" "buffer_store_dwordx4 v[144:147], v21, %[rs], s50 offen sc1\n\t" \
   "v_mfma_f32_16x16x32_bf16 v[64:67], v[152:155], v[224:227], 0\n\t" "ds_read_b128 v[88:91], v28 offset:2048\n\t" \
   "v_mfma_f32_16x16x32_bf16 v[68:71], v[168:171], v[224:227], 0\n\t" "s_add_u32 s43, s43, 44032\n\t" \
   "v_mfma_f32_16x16x32_bf16 v[64:67], v[156:159], v[228:231], v[64:67]\n\t" "v_cvt_pk_bf16_f32 v148, v72, v73\n\t" \
   "v_cvt_pk_bf16_f32 v150, v74, v75\n\t" "v_mfma_f32_16x16x32_bf16 v[68:71], v[172:175], v[228:231], v[68:71]\n\t" \
   "v_cvt_pk_bf16_f32 v149, v76, v77\n\t" "v_cvt_pk_bf16_f32 v151, v78, v79\n\t" \
   "v_mfma_f32_16x16x32_bf16 v[64:67], v[160:163], v[232:235], v[64:67]\n\t" "s_cmp_eq_u32 s43, 132096\n\t" "s_cselect_b32 s43, 0, s43\n\t" \
   "v_mfma_f32_16x16x32_bf16 v[68:71], v[176:179], v[232:235], v[68:71]\n\t" "ds_read_b128 v[92:95], v28 offset:3072\n\t" \
   "v_mfma_f32_16x16x32_bf16 v[64:67], v[164:167], v[236:239], v[64:67]\n\t" "s_add_u32 s50, s49, 229376\n\t" "s_add_u32 s41, s41, 1\n\t" \
   "v_mfma_f32_16x16x32_bf16 v[68:71], v[180:183], v[236:239], v[68:71]\n\t" "buffer_store_dwordx4 v[148:151], v21, %[rs], s50 offen sc1\n\t" \
   "v_mfma_f32_16x16x32_bf16 v[72:75], v[152:155], v[240:243], 0\n\t" "ds_read_b128 v[96:99], v28 offset:4096\n\t" \
   "v_mfma_f32_16x16x32_bf16 v[76:79], v[168:171], v[240:243], 0\n\t" "s_add_u32 s40, s40, 1\n\t" "v_xor_b32 v14, 0x2000, v14\n\t" \
   "v_mfma_f32_16x16x32_bf16 v[72:75], v[156:159], v[244:247], v[72:75]\n\t" "v_cvt_pk_bf16_f32 v144, v64, v65\n\t" \
   "v_cvt_pk_bf16_f32 v146, v66, v67\n\t" "v_mfma_f32_16x16x32_bf16 v[76:79], v[172:175], v[244:247], v[76:79]\n\t" \
   "v_cvt_pk_bf16_f32 v145, v68, v69\n\t" "v_cvt_pk_bf16_f32 v147, v70, v71\n\t" \
   "v_mfma_f32_16x16x32_bf16 v[72:75], v[160:163], v[248:251], v[72:75]\n\t" "ds_read_b128 v[100:103], v28 offset:5120\n\t" \
   "v_mfma_f32_16x16x32_bf16 v[76:79], v[176:179], v[248:251], v[76:79]\n\t" "s_add_u32 s50, s49, 262144\n\t" "v_xor_b32 v15, 0x2000, v15\n\t" \
   "v_mfma_f32_16x16x32_bf16 v[72:75], v[164:167], v[252:255], v[72:75]\n\t" "ds_read_b128 v[104:107], v28 offset:6144\n\t" \
   "buffer_store_dwordx4 v[144:147], v21, %[rs], s50 offen sc1\n\t" "v_mfma_f32_16x16x32_bf16 v[76:79], v[180:183], v[252:255], v[76:79]\n\t" \
   "v_mov_b32 v18, v21\n\t" "s_cmp_lt_u32 s40, 64\n\t" "s_cbranch_scc1 1b\n\t" "s_nop 7\n\t" "s_waitcnt lgkmcnt(0)\n\t" \
   "v_cvt_pk_bf16_f32 v148, v72, v73\n\t" "v_cvt_pk_bf16_f32 v150, v74, v75\n\t" "v_cvt_pk_bf16_f32 v149, v76, v77\n\t" \
   "v_cvt_pk_bf16_f32 v151, v78, v79\n\t" "s_add_u32 s50, s49, 294912\n\t" "buffer_store_dwordx4 v[148:151], v21, %[rs], s50 offen sc1\n\t" \
   "s_sub_i32 s49, s41, 1\n\t" "s_mul_i32 s49, s49, 1310720\n\t" "ds_read_b128 v[152:155], v15 offset:0\n\t" \
   "ds_read_b128 v[168:171], v15 offset:4096\n\t" "ds_read_b128 v[156:159], v15 offset:1024\n\t" "ds_read_b128 v[172:175], v15 offset:5120\n\t" \
   "ds_read_b128 v[160:163], v15 offset:2048\n\t" "ds_read_b128 v[176:179], v15 offset:6144\n\t" "ds_read_b128 v[164:167], v15 offset:3072\n\t" \
   "ds_read_b128 v[180:183], v15 offset:7168\n\t" "s_waitcnt lgkmcnt(4)\n\t" "v_mfma_f32_16x16x32_bf16 v[64:67], v[152:155], a[160:163], 0\n\t" \
   "v_mfma_f32_16x16x32_bf16 v[68:71], v[168:171], a[160:163], 0\n\t" "v_mfma_f32_16x16x32_bf16 v[64:67], v[156:159], a[164:167], v[64:67]\n\t" \
   "v_mfma_f32_16x16x32_bf16 v[68:71], v[172:175], a[164:167], v[68:71]\n\t" "s_waitcnt lgkmcnt(0)\n\t" \
   "v_mfma_f32_16x16x32_bf16 v[64:67], v[160:163], a[168:171], v[64:67]\n\t" "v_mfma_f32_16x16x32_bf16 v[68:71], v[176:179], a[168:171], v[68:71]\n\t" \
   "v_mfma_f32_16x16x32_bf16 v[64:67], v[164:167], a[172:175], v[64:67]\n\t" "v_mfma_f32_16x16x32_bf16 v[68:71], v[180:183], a[172:175], v[68:71]\n\t" \
   "v_mfma_f32_16x16x32_bf16 v[72:75], v[152:155], a[176:179], 0\n\t" "v_mfma_f32_16x16x32_bf16 v[76:79], v[168:171], a[176:179], 0\n\t" \
   "v_mfma_f32_16x16x32_bf16 v[72:75], v[156:159], a[180:183], v[72:75]\n\t" "v_cvt_pk_bf16_f32 v144, v64, v65\n\t" \
   "v_cvt_pk_bf16_f32 v146, v66, v67\n\t" "v_mfma_f32_16x16x32_bf16 v[76:79], v[172:175], a[180:183], v[76:79]\n\t" \
   "v_cvt_pk_bf16_f32 v145, v68, v69\n\t" "v_cvt_pk_bf16_f32 v147, v70, v71\n\t" \
   "v_mfma_f32_16x16x32_bf16 v[72:75], v[160:163], a[184:187], v[72:75]\n\t" "s_add_u32 s50, s49, 0\n\t" \
   "v_mfma_f32_16x16x32_bf16 v[76:79], v[176:179], a[184:187], v[76:79]\n\t" "buffer_store_dwordx4 v[144:147], v21, %[rs], s50 offen sc1\n\t" \
   "v_mfma_f32_16x16x32_bf16 v[72:75], v[164:167], a[188:191], v[72:75]\n\t" "v_mfma_f32_16x16x32_bf16 v[76:79], v[180:183], a[188:191], v[76:79]\n\t" \
   "v_mfma_f32_16x16x32_bf16 v[64:67], v[152:155], a[192:195], 0\n\t" "v_mfma_f32_16x16x32_bf16 v[68:71], v[168:171], a[192:195], 0\n\t" \
   "v_mfma_f32_16x16x32_bf16 v[64:67], v[156:159], a[196:199], v[64:67]\n\t" "v_cvt_pk_bf16_f32 v148, v72, v73\n\t" \
   "v_cvt_pk_bf16_f32 v150, v74, v75\n\t" "v_mfma_f32_16x16x32_bf16 v[68:71], v[172:175], a[196:199], v[68:71]\n\t" \
   "v_cvt_pk_bf16_f32 v149, v76, v77\n\t" "v_cvt_pk_bf16_f32 v151, v78, v79\n\t" \
   "v_mfma_f32_16x16x32_bf16 v[64:67], v[160:163], a[200:203], v[64:67]\n\t" "s_add_u32 s50, s49, 32768\n\t" \
   "v_mfma_f32_16x16x32_bf16 v[68:71], v[176:179], a[200:203], v[68:71]\n\t" "buffer_store_dwordx4 v[148:151], v21, %[rs], s50 offen sc1\n\t" \
   "v_mfma_f32_16x16x32_bf16 v[64:67], v[164:167], a[204:207], v[64:67]\n\t" "v_mfma_f32_16x16x32_bf16 v[68:71], v[180:183], a[204:207], v[68:71]\n\t" \
   "v_mfma_f32_16x16x32_bf16 v[72:75], v[152:155], a[208:211], 0\n\t" "v_mfma_f32_16x16x32_bf16 v[76:79], v[168:171], a[208:211], 0\n\t" \
   "v_mfma_f32_16x16x32_bf16 v[72:75], v[156:159], a[212:215], v[72:75]\n\t" "v_cvt_pk_bf16_f32 v144, v64, v65\n\t" \
   "v_cvt_pk_bf16_f32 v146, v66, v67\n\t" "v_mfma_f32_16x16x32_bf16 v[76:79], v[172:175], a[212:215], v[76:79]\n\t" \
   "v_cvt_pk_bf16_f32 v145, v68, v69\n\t" "v_cvt_pk_bf16_f32 v147, v70, v71\n\t" \
   "v_mfma_f32_16x16x32_bf16 v[72:75], v[160:163], a[216:219], v[72:75]\n\t" "s_add_u32 s50, s49, 65536\n\t" \
   "v_mfma_f32_16x16x32_bf16 v[76:79], v[176:179], a[216:219], v[76:79]\n\t" "buffer_store_dwordx4 v[144:147], v21, %[rs], s50 offen sc1\n\t" \
   "v_mfma_f32_16x16x32_bf16 v[72:75], v[164:167], a[220:223], v[72:75]\n\t" "v_mfma_f32_16x16x32_bf16 v[76:79], v[180:183], a[220:223], v[76:79]\n\t" \
   "v_mfma_f32_16x16x32_bf16 v[64:67], v[152:155], a[224:227], 0\n\t" "v_mfma_f32_16x16x32_bf16 v[68:71], v[168:171], a[224:227], 0\n\t" \
   "v_mfma_f32_16x16x32_bf16 v[64:67], v[156:159], a[228:231], v[64:67]\n\t" "v_cvt_pk_bf16_f32 v148, v72, v73\n\t" \
   "v_cvt_pk_bf16_f32 v150, v74, v75\n\t" "v_mfma_f32_16x16x32_bf16 v[68:71], v[172:175], a[228:231], v[68:71]\n\t" \
   "v_cvt_pk_bf16_f32 v149, v76, v77\n\t" "v_cvt_pk_bf16_f32 v151, v78, v79\n\t" \
   "v_mfma_f32_16x16x32_bf16 v[64:67], v[160:163], a[232:235], v[64:67]\n\t" "s_add_u32 s50, s49, 98304\n\t" \
   "v_mfma_f32_16x16x32_bf16 v[68:71], v[176:179], a[232:235], v[68:71]\n\t" "buffer_store_dwordx4 v[148:151], v21, %[rs], s50 offen sc1\n\t" \
   "v_mfma_f32_16x16x32_bf16 v[64:67], v[164:167], a[236:239], v[64:67]\n\t" "v_mfma_f32_16x16x32_bf16 v[68:71], v[180:183], a[236:239], v[68:71]\n\t" \
   "v_mfma_f32_16x16x32_bf16 v[72:75], v[152:155], a[240:243], 0\n\t" "v_mfma_f32_16x16x32_bf16 v[76:79], v[168:171], a[240:243], 0\n\t" \
   "v_mfma_f32_16x16x32_bf16 v[72:75], v[156:159], a[244:247], v[72:75]\n\t" "v_cvt_pk_bf16_f32 v144, v64, v65\n\t" \
   "v_cvt_pk_bf16_f32 v146, v66, v67\n\t" "v_mfma_f32_16x16x32_bf16 v[76:79], v[172:175], a[244:247], v[76:79]\n\t" \
   "v_cvt_pk_bf16_f32 v145, v68, v69\n\t" "v_cvt_pk_bf16_f32 v147, v70, v71\n\t" \
   "v_mfma_f32_16x16x32_bf16 v[72:75], v[160:163], a[248:251], v[72:75]\n\t" "s_add_u32 s50, s49, 131072\n\t" \
   "v_mfma_f32_16x16x32_bf16 v[76:79], v[176:179], a[248:251], v[76:79]\n\t" "buffer_store_dwordx4 v[144:147], v21, %[rs], s50 offen sc1\n\t" \
   "v_mfma_f32_16x16x32_bf16 v[72:75], v[164:167], a[252:255], v[72:75]\n\t" "v_mfma_f32_16x16x32_bf16 v[76:79], v[180:183], a[252:255], v[76:79]\n\t" \
   "v_mfma_f32_16x16x32_bf16 v[64:67], v[152:155], v[192:195], 0\n\t" "v_mfma_f32_16x16x32_bf16 v[68:71], v[168:171], v[192:195], 0\n\t" \
   "v_mfma_f32_16x16x32_bf16 v[64:67], v[156:159], v[196:199], v[64:67]\n\t" "v_cvt_pk_bf16_f32 v148, v72, v73\n\t" \
   "v_cvt_pk_bf16_f32 v150, v74, v75\n\t" "v_mfma_f32_16x16x32_bf16 v[68:71], v[172:175], v[196:199], v[68:71]\n\t" \
   "v_cvt_pk_bf16_f32 v149, v76, v77\n\t" "v_cvt_pk_bf16_f32 v151, v78, v79\n\t" \
   "v_mfma_f32_16x16x32_bf16 v[64:67], v[160:163], v[200:203], v[64:67]\n\t" "s_add_u32 s50, s49, 163840\n\t" \
   "v_mfma_f32_16x16x32_bf16 v[68:71], v[176:179], v[200:203], v[68:71]\n\t" "buffer_store_dwordx4 v[148:151], v21, %[rs], s50 offen sc1\n\t" \
   "v_mfma_f32_16x16x32_bf16 v[64:67], v[164:167], v[204:207], v[64:67]\n\t" "v_mfma_f32_16x16x32_bf16 v[68:71], v[180:183], v[204:207], v[68:71]\n\t" \
   "v_mfma_f32_16x16x32_bf16 v[72:75], v[152:155], v[208:211], 0\n\t" "v_mfma_f32_16x16x32_bf16 v[76:79], v[168:171], v[208:211], 0\n\t" \
   "v_mfma_f32_16x16x32_bf16 v[72:75], v[156:159], v[212:215], v[72:75]\n\t" "v_cvt_pk_bf16_f32 v144, v64, v65\n\t" \
   "v_cvt_pk_bf16_f32 v146, v66, v67\n\t" "v_mfma_f32_16x16x32_bf16 v[76:79], v[172:175], v[212:215], v[76:79]\n\t" \
   "v_cvt_pk_bf16_f32 v145, v68, v69\n\t" "v_cvt_pk_bf16_f32 v147, v70, v71\n\t" \
   "v_mfma_f32_16x16x32_bf16 v[72:75], v[160:163], v[216:219], v[72:75]\n\t" "s_add_u32 s50, s49, 196608\n\t" \
   "v_mfma_f32_16x16x32_bf16 v[76:79], v[176:179], v[216:219], v[76:79]\n\t" "buffer_store_dwordx4 v[144:147], v21, %[rs], s50 offen sc1\n\t" \
   "v_mfma_f32_16x16x32_bf16 v[72:75], v[164:167], v[220:223], v[72:75]\n\t" "v_mfma_f32_16x16x32_bf16 v[76:79], v[180:183], v[220:223], v[76:79]\n\t" \
   "v_mfma_f32_16x16x32_bf16 v[64:67], v[152:155], v[224:227], 0\n\t" "v_mfma_f32_16x16x32_bf16 v[68:71], v[168:171], v[224:227], 0\n\t" \
   "v_mfma_f32_16x16x32_bf16 v[64:67], v[156:159], v[228:231], v[64:67]\n\t" "v_cvt_pk_bf16_f32 v148, v72, v73\n\t" \
   "v_cvt_pk_bf16_f32 v150, v74, v75\n\t" "v_mfma_f32_16x16x32_bf16 v[68:71], v[172:175], v[228:231], v[68:71]\n\t" \
   "v_cvt_pk_bf16_f32 v149, v76, v77\n\t" "v_cvt_pk_bf16_f32 v151, v78, v79\n\t" \
   "v_mfma_f32_16x16x32_bf16 v[64:67], v[160:163], v[232:235], v[64:67]\n\t" "s_add_u32 s50, s49, 229376\n\t" \
   "v_mfma_f32_16x16x32_bf16 v[68:71], v[176:179], v[232:235], v[68:71]\n\t" "buffer_store_dwordx4 v[148:151], v21, %[rs], s50 offen sc1\n\t" \
   "v_mfma_f32_16x16x32_bf16 v[64:67], v[164:167], v[236:239], v[64:67]\n\t" "v_mfma_f32_16x16x32_bf16 v[68:71], v[180:183], v[236:239], v[68:71]\n\t" \
   "v_mfma_f32_16x16x32_bf16 v[72:75], v[152:155], v[240:243], 0\n\t" "v_mfma_f32_16x16x32_bf16 v[76:79], v[168:171], v[240:243], 0\n\t" \
   "v_mfma_f32_16x16x32_bf16 v[72:75], v[156:159], v[244:247], v[72:75]\n\t" "v_cvt_pk_bf16_f32 v144, v64, v65\n\t" \
   "v_cvt_pk_bf16_f32 v146, v66, v67\n\t" "v_mfma_f32_16x16x32_bf16 v[76:79], v[172:175], v[244:247], v[76:79]\n\t" \
   "v_cvt_pk_bf16_f32 v145, v68, v69\n\t" "v_cvt_pk_bf16_f32 v147, v70, v71\n\t" \
   "v_mfma_f32_16x16x32_bf16 v[72:75], v[160:163], v[248:251], v[72:75]\n\t" "s_add_u32 s50, s49, 262144\n\t" \
   "v_mfma_f32_16x16x32_bf16 v[76:79], v[176:179], v[248:251], v[76:79]\n\t" "buffer_store_dwordx4 v[144:147], v21, %[rs], s50 offen sc1\n\t" \
   "v_mfma_f32_16x16x32_bf16 v[72:75], v[164:167], v[252:255], v[72:75]\n\t" "v_mfma_f32_16x16x32_bf16 v[76:79], v[180:183], v[252:255], v[76:79]\n\t" \
   "s_nop 7\n\t" "v_cvt_pk_bf16_f32 v148, v72, v73\n\t" "v_cvt_pk_bf16_f32 v150, v74, v75\n\t" "v_cvt_pk_bf16_f32 v149, v76, v77\n\t" \
   "v_cvt_pk_bf16_f32 v151, v78, v79\n\t" "s_add_u32 s50, s49, 294912\n\t" "buffer_store_dwordx4 v[148:151], v21, %[rs], s50 offen sc1\n\t" \
   "s_waitcnt vmcnt(0)\n\t" "s_cmp_lt_i32 s40, 2\n\t" "s_cselect_b32 s47, 0x7ffffff0, 0\n\t" "v_or_b32 v30, s47, v20\n\t" "s_sub_i32 s47, s41, 2\n\t" \
   "s_max_i32 s47, s47, 0\n\t" "s_lshl_b32 s47, s47, 12\n\t" "buffer_store_dword v31, v30, %[rs], s47 offen sc1\n\t" "s_cmp_lt_i32 s40, 1\n\t" \
   "s_cselect_b32 s47, 0x7ffffff0, 0\n\t" "v_or_b32 v30, s47, v20\n\t" "s_sub_i32 s47, s41, 1\n\t" "s_max_i32 s47, s47, 0\n\t" \
   "s_lshl_b32 s47, s47, 12\n\t" "buffer_store_dword v31, v30, %[rs], s47 offen sc1\n\t" "s_branch 9f\n\t" "172:\n\t" "s_mov_b32 s47, 0\n\t" \
   "buffer_load_dword v30, v19, %[rs], 0 offen sc1\n\t" "s_waitcnt vmcnt(0)\n\t" "v_add_u32 v30, 1, v30\n\t" "v_cmp_lt_u32 vcc, 9, v30\n\t" \
   "s_mov_b32 s44, vcc_lo\n\t" "s_bitcmp1_b32 s44, s48\n\t" "s_cbranch_scc1 72b\n\t" "s_bitset1_b32 s44, s48\n\t" "s_lshl_b32 s48, s48, 7\n\t" \
   "s_add_u32 s48, s48, %[cnt]\n\t" "73:\n\t" "s_sleep 10\n\t" "buffer_load_dword v30, off, %[rs], s48 sc1\n\t" "s_add_u32 s47, s47, 1\n\t" \
   "s_waitcnt vmcnt(0)\n\t" "v_add_u32 v30, 1, v30\n\t" "v_cmp_lt_u32 vcc, 9, v30\n\t" "s_cmp_gt_u32 s47, 0x20000\n\t" "s_cbranch_scc1 72b\n\t" \
   "s_cbranch_vccz 73b\n\t" "s_branch 72b\n\t" "176:\n\t" "s_mov_b32 s47, 0\n\t" "buffer_load_dword v30, v19, %[rs], 0 offen sc1\n\t" \
   "s_waitcnt vmcnt(0)\n\t" "v_add_u32 v30, 1, v30\n\t" "v_cmp_lt_u32 vcc, 9, v30\n\t" "s_mov_b32 s44, vcc_lo\n\t" "s_bitcmp1_b32 s44, s48\n\t" \
   "s_cbranch_scc1 76b\n\t" "s_bitset1_b32 s44, s48\n\t" "s_lshl_b32 s48, s48, 7\n\t" "s_add_u32 s48, s48, %[cnt]\n\t" "77:\n\t" "s_sleep 10\n\t" \
   "buffer_load_dword v30, off, %[rs], s48 sc1\n\t" "s_add_u32 s47, s47, 1\n\t" "s_waitcnt vmcnt(0)\n\t" "v_add_u32 v30, 1, v30\n\t" \
   "v_cmp_lt_u32 vcc, 9, v30\n\t" "s_cmp_gt_u32 s47, 0x20000\n\t" "s_cbranch_scc1 76b\n\t" "s_cbranch_vccz 77b\n\t" "s_branch 76b\n\t" "174:\n\t" \
   "s_mov_b32 s47, 0\n\t" "buffer_load_dword v30, v19, %[rs], 0 offen sc1\n\t" "s_waitcnt vmcnt(0)\n\t" "v_add_u32 v30, 1, v30\n\t" \
   "v_cmp_lt_u32 vcc, 9, v30\n\t" "s_mov_b32 s44, vcc_lo\n\t" "s_bitcmp1_b32 s44, s48\n\t" "s_cbranch_scc1 74b\n\t" "s_bitset1_b32 s44, s48\n\t" \
   "s_lshl_b32 s48, s48, 7\n\t" "s_add_u32 s48, s48, %[cnt]\n\t" "75:\n\t" "s_sleep 10\n\t" "buffer_load_dword v30, off, %[rs], s48 sc1\n\t" \
   "s_add_u32 s47, s47, 1\n\t" "s_waitcnt vmcnt(0)\n\t" "v_add_u32 v30, 1, v30\n\t" "v_cmp_lt_u32 vcc, 9, v30\n\t" "s_cmp_gt_u32 s47, 0x20000\n\t" \
   "s_cbranch_scc1 74b\n\t" "s_cbranch_vccz 75b\n\t" "s_branch 74b\n\t" "9:\n\t" "s_waitcnt vmcnt(0)"
#define VU_CLOB "v8", "v9", "v10", "v11", "v12", "v13", "v14", "v15", "v32", "v33", "v34", "v35", "v36", "v37", "v38", "v39", "v40", "v41", "v42", "v43", "v44", "v45", "v46", "v47", "v48", "v49", "v50", "v51", "v52", "v53", "v54", "v55", "v56", "v57", "v58", "v59", "v60", "v61", "v62", "v63", "v64", "v65", "v66", "v67", "v68", "v69", "v70", "v71", "v72", "v73", "v74", "v75", "v76", "v77", "v78", "v79", "v80", "v81", "v82", "v83", "v84", "v85", "v86", "v87", "v88", "v89", "v90", "v91", "v92", "v93", "v94", "v95", "v96", "v97", "v98", "v99", "v100", "v101", "v102", "v103", "v104", "v105", "v106", "v107", "v108", "v109", "v110", "v111", "v112", "v113", "v114", "v115", "v116", "v117", "v118", "v119", "v120", "v121", "v122", "v123", "v124", "v125", "v126", "v127", "v128", "v129", "v130", "v131", "v132", "v133", "v134", "v135", "v136", "v137", "v138", "v139", "v140", "v141", "v142", "v143", "v144", "v145", "v146", "v147", "v148", "v149", "v150", "v151", "v152", "v153", "v154", "v155", "v156", "v157", "v158", "v159", "v160", "v161", "v162", "v163", "v164", "v165", "v166", "v167", "v168", "v169", "v170", "v171", "v172", "v173", "v174", "v175", "v176", "v177", "v178", "v179", "v180", "v181", "v182", "v183", "s40", "s41", "s42", "s43", "s44", "s45", "s46", "s47", "s48", "s49", "s50", "s51", "s52", "s53", "s54", "s55"
#define DF_ASM \
   "v_mov_b32 v80, 0x3f803f80\n\t" "v_mov_b32 v81, 0x3f803f80\n\t" "v_mov_b32 v82, 0x3f803f80\n\t" "v_mov_b32 v83, 0x3f803f80\n\t" \
   "s_mov_b32 s45, %[set]\n\t" "s_mov_b32 s46, 0\n\t" "v_mfma_f32_16x16x32_bf16 v[32:35], a[0:3], v[80:83], 0\n\t" \
   "v_mfma_f32_16x16x32_bf16 v[32:35], a[4:7], v[80:83], v[32:35]\n\t" "v_mfma_f32_16x16x32_bf16 v[32:35], a[8:11], v[80:83], v[32:35]\n\t" \
   "v_mfma_f32_16x16x32_bf16 v[32:35], a[12:15], v[80:83], v[32:35]\n\t" "v_mfma_f32_16x16x32_bf16 v[32:35], a[16:19], v[80:83], v[32:35]\n\t" \
   "v_mfma_f32_16x16x32_bf16 v[32:35], a[20:23], v[80:83], v[32:35]\n\t" "v_mfma_f32_16x16x32_bf16 v[32:35], a[24:27], v[80:83], v[32:35]\n\t" \
   "v_mfma_f32_16x16x32_bf16 v[32:35], a[28:31], v[80:83], v[32:35]\n\t" "v_mfma_f32_16x16x32_bf16 v[32:35], a[32:35], v[80:83], v[32:35]\n\t" \
   "v_mfma_f32_16x16x32_bf16 v[32:35], a[36:39], v[80:83], v[32:35]\n\t" "v_mfma_f32_16x16x32_bf16 v[32:35], a[40:43], v[80:83], v[32:35]\n\t" \
   "v_mfma_f32_16x16x32_bf16 v[32:35], a[44:47], v[80:83], v[32:35]\n\t" "v_mfma_f32_16x16x32_bf16 v[32:35], a[48:51], v[80:83], v[32:35]\n\t" \
   "v_mfma_f32_16x16x32_bf16 v[32:35], a[52:55], v[80:83], v[32:35]\n\t" "v_mfma_f32_16x16x32_bf16 v[32:35], a[56:59], v[80:83], v[32:35]\n\t" \
   "v_mfma_f32_16x16x32_bf16 v[32:35], a[60:63], v[80:83], v[32:35]\n\t" "v_mfma_f32_16x16x32_bf16 v[32:35], a[64:67], v[80:83], v[32:35]\n\t" \
   "v_mfma_f32_16x16x32_bf16 v[32:35], a[68:71], v[80:83], v[32:35]\n\t" "v_mfma_f32_16x16x32_bf16 v[32:35], a[72:75], v[80:83], v[32:35]\n\t" \
   "v_mfma_f32_16x16x32_bf16 v[32:35], a[76:79], v[80:83], v[32:35]\n\t" "v_mfma_f32_16x16x32_bf16 v[36:39], a[80:83], v[80:83], 0\n\t" \
   "v_mfma_f32_16x16x32_bf16 v[36:39], a[84:87], v[80:83], v[36:39]\n\t" "v_mfma_f32_16x16x32_bf16 v[36:39], a[88:91], v[80:83], v[36:39]\n\t" \
   "v_mfma_f32_16x16x32_bf16 v[36:39], a[92:95], v[80:83], v[36:39]\n\t" "v_mfma_f32_16x16x32_bf16 v[36:39], a[96:99], v[80:83], v[36:39]\n\t" \
   "v_mfma_f32_16x16x32_bf16 v[36:39], a[100:103], v[80:83], v[36:39]\n\t" "v_mfma_f32_16x16x32_bf16 v[36:39], a[104:107], v[80:83], v[36:39]\n\t" \
   "v_mfma_f32_16x16x32_bf16 v[36:39], a[108:111], v[80:83], v[36:39]\n\t" "v_mfma_f32_16x16x32_bf16 v[36:39], a[112:115], v[80:83], v[36:39]\n\t" \
   "v_mfma_f32_16x16x32_bf16 v[36:39], a[116:119], v[80:83], v[36:39]\n\t" "v_mfma_f32_16x16x32_bf16 v[36:39], a[120:123], v[80:83], v[36:39]\n\t" \
   "v_mfma_f32_16x16x32_bf16 v[36:39], a[124:127], v[80:83], v[36:39]\n\t" "v_mfma_f32_16x16x32_bf16 v[36:39], a[128:131], v[80:83], v[36:39]\n\t" \
   "v_mfma_f32_16x16x32_bf16 v[36:39], a[132:135], v[80:83], v[36:39]\n\t" "v_mfma_f32_16x16x32_bf16 v[36:39], a[136:139], v[80:83], v[36:39]\n\t" \
   "v_mfma_f32_16x16x32_bf16 v[36:39], a[140:143], v[80:83], v[36:39]\n\t" "v_mfma_f32_16x16x32_bf16 v[36:39], a[144:147], v[80:83], v[36:39]\n\t" \
   "v_mfma_f32_16x16x32_bf16 v[36:39], a[148:151], v[80:83], v[36:39]\n\t" "v_mfma_f32_16x16x32_bf16 v[36:39], a[152:155], v[80:83], v[36:39]\n\t" \
   "v_mfma_f32_16x16x32_bf16 v[36:39], a[156:159], v[80:83], v[36:39]\n\t" "1:\n\t" "s_lshl_b32 s48, s45, 7\n\t" "s_mov_b32 s47, 0\n\t" \
   "s_cmp_lg_u32 %[ldx], %[ld0]\n\t" "s_cbranch_scc1 3f\n\t" "2:\n\t" "buffer_load_dword v30, v19, %[rs], s48 offen sc1\n\t" \
   "s_add_u32 s47, s47, 1\n\t" "s_waitcnt vmcnt(0)\n\t" "v_readfirstlane_b32 s44, v30\n\t" "s_add_u32 s44, s44, 1\n\t" "s_cmp_ge_u32 s44, 5\n\t" \
   "s_cbranch_scc1 3f\n\t" "s_cmp_gt_u32 s47, 0x20000\n\t" "s_cbranch_scc1 3f\n\t" "s_sleep 3\n\t" "s_cmp_lg_u32 s44, 0\n\t" "s_cbranch_scc1 2b\n\t" \
   "s_sleep 8\n\t" "s_branch 2b\n\t" "3:\n\t" "s_barrier\n\t" "s_mul_i32 s52, s45, 1536\n\t" "s_mul_i32 s51, s45, 40960\n\t" \
   "s_add_u32 m0, s46, %[ldx]\n\t" "s_mov_b32 s53, s46\n\t" "buffer_load_dwordx4 v16, %[rs], s51 offen offset:0 sc1 lds\n\t" \
   "buffer_load_dwordx4 v16, %[rs], s51 offen offset:1024 sc1 lds\n\t" "buffer_load_dwordx4 v16, %[rs], s51 offen offset:2048 sc1 lds\n\t" \
   "buffer_load_dwordx4 v16, %[rs], s51 offen offset:3072 sc1 lds\n\t" "s_add_u32 m0, m0, 4096\n\t" "s_add_u32 s51, s51, 4096\n\t" \
   "buffer_load_dwordx4 v16, %[rs], s51 offen offset:0 sc1 lds\n\t" "buffer_load_dwordx4 v16, %[rs], s51 offen offset:1024 sc1 lds\n\t" \
   "buffer_load_dwordx4 v16, %[rs], s51 offen offset:2048 sc1 lds\n\t" "buffer_load_dwordx4 v16, %[rs], s51 offen offset:3072 sc1 lds\n\t" \
   "s_add_u32 m0, m0, 4096\n\t" "s_add_u32 s51, s51, 4096\n\t" "buffer_load_dwordx4 v16, %[rs], s51 offen offset:0 sc1 lds\n\t" \
   "buffer_load_dwordx4 v16, %[rs], s51 offen offset:1024 sc1 lds\n\t" "s_add_u32 m0, s53, %[ld1]\n\t" "s_nop 0\n\t" \
   "buffer_load_dwordx4 v17, %[rs], s52 offen sc1 lds\n\t" "v_add_u32 v26, s46, v22\n\t" "v_add_u32 v27, s46, v23\n\t" "s_waitcnt vmcnt(0)\n\t" \
   "s_barrier\n\t" "ds_read_b128 v[80:83], v26 offset:0\n\t" "ds_read_b128 v[84:87], v26 offset:1024\n\t" "ds_read_b128 v[88:91], v26 offset:2048\n\t" \
   "ds_read_b128 v[92:95], v26 offset:3072\n\t" "ds_read_b128 v[96:99], v26 offset:4096\n\t" "ds_read_b128 v[100:103], v26 offset:5120\n\t" \
   "ds_read_b128 v[104:107], v26 offset:6144\n\t" "s_waitcnt lgkmcnt(5)\n\t" "v_mfma_f32_16x16x32_bf16 v[52:55], a[80:83], v[80:83], 0\n\t" \
   "ds_read_b128 v[112:115], v27 offset:0\n\t" "v_mfma_f32_16x16x32_bf16 v[48:51], a[0:3], v[80:83], 0\n\t" \
   "ds_read_b128 v[108:111], v26 offset:7168\n\t" "ds_read_b128 v[116:119], v27 offset:16\n\t" \
   "v_mfma_f32_16x16x32_bf16 v[52:55], a[84:87], v[84:87], v[52:55]\n\t" "ds_read_b64 v[120:121], v27 offset:32\n\t" \
   "v_mfma_f32_16x16x32_bf16 v[48:51], a[4:7], v[84:87], v[48:51]\n\t" "ds_read_b128 v[80:83], v26 offset:8192\n\t" "s_waitcnt lgkmcnt(8)\n\t" \
   "v_mfma_f32_16x16x32_bf16 v[52:55], a[88:91], v[88:91], v[52:55]\n\t" "ds_read_b128 v[122:125], v27 offset:768\n\t" \
   "v_mfma_f32_16x16x32_bf16 v[48:51], a[8:11], v[88:91], v[48:51]\n\t" "ds_read_b128 v[84:87], v26 offset:9216\n\t" \
   "ds_read_b128 v[126:129], v27 offset:784\n\t" "v_mfma_f32_16x16x32_bf16 v[52:55], a[92:95], v[92:95], v[52:55]\n\t" \
   "v_mfma_f32_16x16x32_bf16 v[48:51], a[12:15], v[92:95], v[48:51]\n\t" "ds_read_b128 v[88:91], v26 offset:10240\n\t" \
   "ds_read_b64 v[130:131], v27 offset:800\n\t" "s_waitcnt lgkmcnt(11)\n\t" "v_mfma_f32_16x16x32_bf16 v[52:55], a[96:99], v[96:99], v[52:55]\n\t" \
   "s_lshl_b32 s48, s45, 9\n\t" "v_mfma_f32_16x16x32_bf16 v[48:51], a[16:19], v[96:99], v[48:51]\n\t" "ds_read_b128 v[92:95], v26 offset:11264\n\t" \
   "s_add_u32 s47, s48, 131072\n\t" "v_mfma_f32_16x16x32_bf16 v[52:55], a[100:103], v[100:103], v[52:55]\n\t" \
   "v_mfma_f32_16x16x32_bf16 v[48:51], a[20:23], v[100:103], v[48:51]\n\t" "ds_read_b128 v[96:99], v26 offset:12288\n\t" "s_waitcnt lgkmcnt(8)\n\t" \
   "v_add_f32 v112, v112, v114\n\t" "v_mfma_f32_16x16x32_bf16 v[52:55], a[104:107], v[104:107], v[52:55]\n\t" "v_add_f32 v113, v113, v115\n\t" \
   "v_mfma_f32_16x16x32_bf16 v[48:51], a[24:27], v[104:107], v[48:51]\n\t" "ds_read_b128 v[100:103], v26 offset:13312\n\t" \
   "v_mfma_f32_16x16x32_bf16 v[52:55], a[108:111], v[108:111], v[52:55]\n\t" "v_add_f32 v116, v116, v118\n\t" \
   "v_mfma_f32_16x16x32_bf16 v[48:51], a[28:31], v[108:111], v[48:51]\n\t" "ds_read_b128 v[104:107], v26 offset:14336\n\t" \
   "v_add_f32 v117, v117, v119\n\t" "s_waitcnt lgkmcnt(7)\n\t" "v_mfma_f32_16x16x32_bf16 v[52:55], a[112:115], v[80:83], v[52:55]\n\t" \
   "v_mfma_f32_16x16x32_bf16 v[48:51], a[32:35], v[80:83], v[48:51]\n\t" "ds_read_b128 v[108:111], v26 offset:15360\n\t" \
   "v_add_f32 v112, v112, v120\n\t" "v_mfma_f32_16x16x32_bf16 v[52:55], a[116:119], v[84:87], v[52:55]\n\t" "v_add_f32 v113, v113, v121\n\t" \
   "v_mfma_f32_16x16x32_bf16 v[48:51], a[36:39], v[84:87], v[48:51]\n\t" "ds_read_b128 v[80:83], v26 offset:16384\n\t" "v_add_f32 v112, v112, v116\n\t" \
   "s_waitcnt lgkmcnt(5)\n\t" "v_mfma_f32_16x16x32_bf16 v[52:55], a[120:123], v[88:91], v[52:55]\n\t" \
   "v_mfma_f32_16x16x32_bf16 v[48:51], a[40:43], v[88:91], v[48:51]\n\t" "ds_read_b128 v[84:87], v26 offset:17408\n\t" "v_add_f32 v113, v113, v117\n\t" \
   "v_mfma_f32_16x16x32_bf16 v[52:55], a[124:127], v[92:95], v[52:55]\n\t" "v_mul_f32 v112, 0xbacccccd, v112\n\t" \
   "v_mfma_f32_16x16x32_bf16 v[48:51], a[44:47], v[92:95], v[48:51]\n\t" "ds_read_b128 v[88:91], v26 offset:18432\n\t" "s_waitcnt lgkmcnt(5)\n\t" \
   "v_mfma_f32_16x16x32_bf16 v[52:55], a[128:131], v[96:99], v[52:55]\n\t" "v_mul_f32 v113, 0x3acccccd, v113\n\t" \
   "v_mfma_f32_16x16x32_bf16 v[48:51], a[48:51], v[96:99], v[48:51]\n\t" "ds_read_b128 v[92:95], v26 offset:19456\n\t" \
   "v_fma_f32 v113, -v112, v112, v113\n\t" "v_mfma_f32_16x16x32_bf16 v[52:55], a[132:135], v[100:103], v[52:55]\n\t" "v_max_f32 v113, 0, v113\n\t" \
   "v_mfma_f32_16x16x32_bf16 v[48:51], a[52:55], v[100:103], v[48:51]\n\t" "ds_read_b128 v[96:99], v26 offset:20480\n\t" "s_waitcnt lgkmcnt(5)\n\t" \
   "v_mfma_f32_16x16x32_bf16 v[52:55], a[136:139], v[104:107], v[52:55]\n\t" "v_cvt_pk_bf16_f32 v113, v113, v112\n\t" \
   "v_mfma_f32_16x16x32_bf16 v[48:51], a[56:59], v[104:107], v[48:51]\n\t" "ds_read_b128 v[100:103], v26 offset:21504\n\t" \
   "v_and_b32 v8, 0xffff0000, v113\n\t" "v_mfma_f32_16x16x32_bf16 v[52:55], a[140:143], v[108:111], v[52:55]\n\t" \
   "v_mfma_f32_16x16x32_bf16 v[48:51], a[60:63], v[108:111], v[48:51]\n\t" "ds_read_b128 v[104:107], v26 offset:22528\n\t" \
   "v_lshlrev_b32 v113, 16, v113\n\t" "s_waitcnt lgkmcnt(5)\n\t" "v_mfma_f32_16x16x32_bf16 v[52:55], a[144:147], v[80:83], v[52:55]\n\t" \
   "v_add_f32 v113, 0x3727c5ac, v113\n\t" "v_mfma_f32_16x16x32_bf16 v[48:51], a[64:67], v[80:83], v[48:51]\n\t" \
   "ds_read_b128 v[108:111], v26 offset:23552\n\t" "v_mfma_f32_16x16x32_bf16 v[52:55], a[148:151], v[84:87], v[52:55]\n\t" \
   "v_cvt_pk_bf16_f32 v113, v113, v113\n\t" "v_mfma_f32_16x16x32_bf16 v[48:51], a[68:71], v[84:87], v[48:51]\n\t" \
   "ds_read_b128 v[80:83], v26 offset:24576\n\t" "v_lshlrev_b32 v113, 16, v113\n\t" "s_waitcnt lgkmcnt(5)\n\t" \
   "v_mfma_f32_16x16x32_bf16 v[52:55], a[152:155], v[88:91], v[52:55]\n\t" "v_sqrt_f32 v113, v113\n\t" \
   "v_mfma_f32_16x16x32_bf16 v[48:51], a[72:75], v[88:91], v[48:51]\n\t" "ds_read_b128 v[84:87], v26 offset:25600\n\t" \
   "v_mfma_f32_16x16x32_bf16 v[52:55], a[156:159], v[92:95], v[52:55]\n\t" "s_nop 0\n\t" \
   "v_mfma_f32_16x16x32_bf16 v[48:51], a[76:79], v[92:95], v[48:51]\n\t" "ds_read_b128 v[88:91], v26 offset:26624\n\t" \
   "v_cvt_pk_bf16_f32 v113, v113, v113\n\t" "s_waitcnt lgkmcnt(5)\n\t" "v_mfma_f32_16x16x32_bf16 v[60:63], a[80:83], v[96:99], 0\n\t" \
   "v_mfma_f32_16x16x32_bf16 v[56:59], a[0:3], v[96:99], 0\n\t" "ds_read_b128 v[92:95], v26 offset:27648\n\t" "v_lshlrev_b32 v113, 16, v113\n\t" \
   "v_mfma_f32_16x16x32_bf16 v[60:63], a[84:87], v[100:103], v[60:63]\n\t" "v_rcp_f32 v10, v113\n\t" \
   "v_mfma_f32_16x16x32_bf16 v[56:59], a[4:7], v[100:103], v[56:59]\n\t" "ds_read_b128 v[96:99], v26 offset:28672\n\t" "s_waitcnt lgkmcnt(5)\n\t" \
   "v_mfma_f32_16x16x32_bf16 v[60:63], a[88:91], v[104:107], v[60:63]\n\t" "v_fmac_f32 v48, v32, v8\n\t" "v_fmac_f32 v49, v33, v8\n\t" \
   "v_mfma_f32_16x16x32_bf16 v[56:59], a[8:11], v[104:107], v[56:59]\n\t" "ds_read_b128 v[100:103], v26 offset:29696\n\t" "v_fmac_f32 v50, v34, v8\n\t" \
   "v_mfma_f32_16x16x32_bf16 v[60:63], a[92:95], v[108:111], v[60:63]\n\t" "v_fmac_f32 v51, v35, v8\n\t" \
   "v_mfma_f32_16x16x32_bf16 v[56:59], a[12:15], v[108:111], v[56:59]\n\t" "ds_read_b128 v[104:107], v26 offset:30720\n\t" \
   "v_fmac_f32 v52, v36, v8\n\t" "s_waitcnt lgkmcnt(5)\n\t" "v_mfma_f32_16x16x32_bf16 v[60:63], a[96:99], v[80:83], v[60:63]\n\t" \
   "v_fmac_f32 v53, v37, v8\n\t" "v_mfma_f32_16x16x32_bf16 v[56:59], a[16:19], v[80:83], v[56:59]\n\t" "ds_read_b128 v[108:111], v26 offset:31744\n\t" \
   "v_fmac_f32 v54, v38, v8\n\t" "v_mfma_f32_16x16x32_bf16 v[60:63], a[100:103], v[84:87], v[60:63]\n\t" "v_fmac_f32 v55, v39, v8\n\t" \
   "v_mfma_f32_16x16x32_bf16 v[56:59], a[20:23], v[84:87], v[56:59]\n\t" "ds_read_b128 v[80:83], v26 offset:32768\n\t" "v_mul_f32 v48, v48, v10\n\t" \
   "v_mul_f32 v49, v49, v10\n\t" "s_waitcnt lgkmcnt(5)\n\t" "v_mfma_f32_16x16x32_bf16 v[60:63], a[104:107], v[88:91], v[60:63]\n\t" \
   "v_mul_f32 v50, v50, v10\n\t" "v_mfma_f32_16x16x32_bf16 v[56:59], a[24:27], v[88:91], v[56:59]\n\t" "ds_read_b128 v[84:87], v26 offset:33792\n\t" \
   "v_mul_f32 v51, v51, v10\n\t" "v_mfma_f32_16x16x32_bf16 v[60:63], a[108:111], v[92:95], v[60:63]\n\t" "v_mul_f32 v52, v52, v10\n\t" \
   "v_mfma_f32_16x16x32_bf16 v[56:59], a[28:31], v[92:95], v[56:59]\n\t" "ds_read_b128 v[88:91], v26 offset:34816\n\t" "v_mul_f32 v53, v53, v10\n\t" \
   "s_waitcnt lgkmcnt(5)\n\t" "v_mfma_f32_16x16x32_bf16 v[60:63], a[112:115], v[96:99], v[60:63]\n\t" "v_mul_f32 v54, v54, v10\n\t" \
   "v_mfma_f32_16x16x32_bf16 v[56:59], a[32:35], v[96:99], v[56:59]\n\t" "ds_read_b128 v[92:95], v26 offset:35840\n\t" "v_mul_f32 v55, v55, v10\n\t" \
   "v_mfma_f32_16x16x32_bf16 v[60:63], a[116:119], v[100:103], v[60:63]\n\t" "buffer_store_dwordx4 v[48:51], v21, %[rs], s48 offen offset:0 sc1\n\t" \
   "buffer_store_dwordx4 v[52:55], v21, %[rs], s47 offen offset:0 sc1\n\t" "v_mfma_f32_16x16x32_bf16 v[56:59], a[36:39], v[100:103], v[56:59]\n\t" \
   "ds_read_b128 v[96:99], v26 offset:36864\n\t" "v_add_f32 v122, v122, v124\n\t" "s_waitcnt lgkmcnt(5)\n\t" \
   "v_mfma_f32_16x16x32_bf16 v[60:63], a[120:123], v[104:107], v[60:63]\n\t" "v_add_f32 v123, v123, v125\n\t" \
   "v_mfma_f32_16x16x32_bf16 v[56:59], a[40:43], v[104:107], v[56:59]\n\t" "ds_read_b128 v[100:103], v26 offset:37888\n\t" \
   "v_add_f32 v126, v126, v128\n\t" "v_mfma_f32_16x16x32_bf16 v[60:63], a[124:127], v[108:111], v[60:63]\n\t" "v_add_f32 v127, v127, v129\n\t" \
   "v_mfma_f32_16x16x32_bf16 v[56:59], a[44:47], v[108:111], v[56:59]\n\t" "ds_read_b128 v[104:107], v26 offset:38912\n\t" \
   "v_add_f32 v122, v122, v130\n\t" "s_waitcnt lgkmcnt(5)\n\t" "v_mfma_f32_16x16x32_bf16 v[60:63], a[128:131], v[80:83], v[60:63]\n\t" \
   "v_add_f32 v123, v123, v131\n\t" "v_mfma_f32_16x16x32_bf16 v[56:59], a[48:51], v[80:83], v[56:59]\n\t" \
   "ds_read_b128 v[108:111], v26 offset:39936\n\t" "v_add_f32 v122, v122, v126\n\t" "v_add_f32 v123, v123, v127\n\t" \
   "v_mfma_f32_16x16x32_bf16 v[60:63], a[132:135], v[84:87], v[60:63]\n\t" "v_mul_f32 v122, 0xbacccccd, v122\n\t" \
   "v_mfma_f32_16x16x32_bf16 v[56:59], a[52:55], v[84:87], v[56:59]\n\t" "v_mul_f32 v123, 0x3acccccd, v123\n\t" "s_waitcnt lgkmcnt(4)\n\t" \
   "v_mfma_f32_16x16x32_bf16 v[60:63], a[136:139], v[88:91], v[60:63]\n\t" "v_fma_f32 v123, -v122, v122, v123\n\t" \
   "v_mfma_f32_16x16x32_bf16 v[56:59], a[56:59], v[88:91], v[56:59]\n\t" "v_max_f32 v123, 0, v123\n\t" \
   "v_mfma_f32_16x16x32_bf16 v[60:63], a[140:143], v[92:95], v[60:63]\n\t" "v_cvt_pk_bf16_f32 v123, v123, v122\n\t" \
   "v_mfma_f32_16x16x32_bf16 v[56:59], a[60:63], v[92:95], v[56:59]\n\t" "v_and_b32 v12, 0xffff0000, v123\n\t" "s_waitcnt lgkmcnt(2)\n\t" \
   "v_mfma_f32_16x16x32_bf16 v[60:63], a[144:147], v[96:99], v[60:63]\n\t" "v_lshlrev_b32 v123, 16, v123\n\t" "v_add_f32 v123, 0x3727c5ac, v123\n\t" \
   "v_mfma_f32_16x16x32_bf16 v[56:59], a[64:67], v[96:99], v[56:59]\n\t" "v_cvt_pk_bf16_f32 v123, v123, v123\n\t" \
   "v_mfma_f32_16x16x32_bf16 v[60:63], a[148:151], v[100:103], v[60:63]\n\t" "v_lshlrev_b32 v123, 16, v123\n\t" \
   "v_mfma_f32_16x16x32_bf16 v[56:59], a[68:71], v[100:103], v[56:59]\n\t" "v_sqrt_f32 v123, v123\n\t" "s_waitcnt lgkmcnt(0)\n\t" \
   "v_mfma_f32_16x16x32_bf16 v[60:63], a[152:155], v[104:107], v[60:63]\n\t" "s_nop 0\n\t" \
   "v_mfma_f32_16x16x32_bf16 v[56:59], a[72:75], v[104:107], v[56:59]\n\t" "v_cvt_pk_bf16_f32 v123, v123, v123\n\t" \
   "v_mfma_f32_16x16x32_bf16 v[60:63], a[156:159], v[108:111], v[60:63]\n\t" "v_lshlrev_b32 v123, 16, v123\n\t" \
   "v_mfma_f32_16x16x32_bf16 v[56:59], a[76:79], v[108:111], v[56:59]\n\t" "v_rcp_f32 v132, v123\n\t" "s_xor_b32 s46, s46, 44032\n\t" \
   "s_add_u32 s45, s45, 4\n\t" "s_nop 5\n\t" "v_fmac_f32 v56, v32, v12\n\t" "v_fmac_f32 v57, v33, v12\n\t" "v_fmac_f32 v58, v34, v12\n\t" \
   "v_fmac_f32 v59, v35, v12\n\t" "v_fmac_f32 v60, v36, v12\n\t" "v_fmac_f32 v61, v37, v12\n\t" "v_fmac_f32 v62, v38, v12\n\t" \
   "v_fmac_f32 v63, v39, v12\n\t" "v_mul_f32 v56, v56, v132\n\t" "v_mul_f32 v57, v57, v132\n\t" "v_mul_f32 v58, v58, v132\n\t" \
   "v_mul_f32 v59, v59, v132\n\t" "v_mul_f32 v60, v60, v132\n\t" "v_mul_f32 v61, v61, v132\n\t" "v_mul_f32 v62, v62, v132\n\t" \
   "v_mul_f32 v63, v63, v132\n\t" "buffer_store_dwordx4 v[56:59], v21, %[rs], s48 offen offset:256 sc1\n\t" \
   "buffer_store_dwordx4 v[60:63], v21, %[rs], s47 offen offset:256 sc1\n\t" "s_cmp_lt_u32 s45, 64\n\t" "s_cbranch_scc1 1b\n\t" \
   "s_waitcnt vmcnt(0) lgkmcnt(0)"
#define DF_CLOB "v8", "v9", "v10", "v11", "v12", "v13", "v14", "v15", "v32", "v33", "v34", "v35", "v36", "v37", "v38", "v39", "v40", "v41", "v42", "v43", "v44", "v45", "v46", "v47", "v48", "v49", "v50", "v51", "v52", "v53", "v54", "v55", "v56", "v57", "v58", "v59", "v60", "v61", "v62", "v63", "v64", "v65", "v66", "v67", "v68", "v69", "v70", "v71", "v72", "v73", "v74", "v75", "v76", "v77", "v78", "v79", "v80", "v81", "v82", "v83", "v84", "v85", "v86", "v87", "v88", "v89", "v90", "v91", "v92", "v93", "v94", "v95", "v96", "v97", "v98", "v99", "v100", "v101", "v102", "v103", "v104", "v105", "v106", "v107", "v108", "v109", "v110", "v111", "v112", "v113", "v114", "v115", "v116", "v117", "v118", "v119", "v120", "v121", "v122", "v123", "v124", "v125", "v126", "v127", "v128", "v129", "v130", "v131", "v132", "v133", "v134", "v135", "s40", "s41", "s42", "s43", "s44", "s45", "s46", "s47", "s48", "s49", "s50", "s51", "s52", "s53"
#define PRE_ASM \
   "s_mov_b32 s45, 0\n\t" "s_mov_b32 s46, 0\n\t" "s_mul_i32 s52, s45, 1536\n\t" "s_mul_i32 s51, s45, 40960\n\t" "s_add_u32 m0, s46, %[ldx]\n\t" \
   "s_mov_b32 s53, s46\n\t" "buffer_load_dwordx4 v16, %[rs], s51 offen offset:0 sc1 lds\n\t" \
   "buffer_load_dwordx4 v16, %[rs], s51 offen offset:1024 sc1 lds\n\t" "buffer_load_dwordx4 v16, %[rs], s51 offen offset:2048 sc1 lds\n\t" \
   "buffer_load_dwordx4 v16, %[rs], s51 offen offset:3072 sc1 lds\n\t" "s_add_u32 m0, m0, 4096\n\t" "s_add_u32 s51, s51, 4096\n\t" \
   "buffer_load_dwordx4 v16, %[rs], s51 offen offset:0 sc1 lds\n\t" "buffer_load_dwordx4 v16, %[rs], s51 offen offset:1024 sc1 lds\n\t" \
   "buffer_load_dwordx4 v16, %[rs], s51 offen offset:2048 sc1 lds\n\t" "buffer_load_dwordx4 v16, %[rs], s51 offen offset:3072 sc1 lds\n\t" \
   "s_add_u32 m0, m0, 4096\n\t" "s_add_u32 s51, s51, 4096\n\t" "buffer_load_dwordx4 v16, %[rs], s51 offen offset:0 sc1 lds\n\t" \
   "buffer_load_dwordx4 v16, %[rs], s51 offen offset:1024 sc1 lds\n\t" "s_add_u32 m0, s53, %[ld1]\n\t" "s_nop 0\n\t" \
   "buffer_load_dwordx4 v17, %[rs], s52 offen sc1 lds\n\t" "s_mov_b32 s45, 1\n\t" "s_mov_b32 s46, 44032\n\t" "s_mul_i32 s52, s45, 1536\n\t" \
   "s_mul_i32 s51, s45, 40960\n\t" "s_add_u32 m0, s46, %[ldx]\n\t" "s_mov_b32 s53, s46\n\t" \
   "buffer_load_dwordx4 v16, %[rs], s51 offen offset:0 sc1 lds\n\t" "buffer_load_dwordx4 v16, %[rs], s51 offen offset:1024 sc1 lds\n\t" \
   "buffer_load_dwordx4 v16, %[rs], s51 offen offset:2048 sc1 lds\n\t" "buffer_load_dwordx4 v16, %[rs], s51 offen offset:3072 sc1 lds\n\t" \
   "s_add_u32 m0, m0, 4096\n\t" "s_add_u32 s51, s51, 4096\n\t" "buffer_load_dwordx4 v16, %[rs], s51 offen offset:0 sc1 lds\n\t" \
   "buffer_load_dwordx4 v16, %[rs], s51 offen offset:1024 sc1 lds\n\t" "buffer_load_dwordx4 v16, %[rs], s51 offen offset:2048 sc1 lds\n\t" \
   "buffer_load_dwordx4 v16, %[rs], s51 offen offset:3072 sc1 lds\n\t" "s_add_u32 m0, m0, 4096\n\t" "s_add_u32 s51, s51, 4096\n\t" \
   "buffer_load_dwordx4 v16, %[rs], s51 offen offset:0 sc1 lds\n\t" "buffer_load_dwordx4 v16, %[rs], s51 offen offset:1024 sc1 lds\n\t" \
   "s_add_u32 m0, s53, %[ld1]\n\t" "s_nop 0\n\t" "buffer_load_dwordx4 v17, %[rs], s52 offen sc1 lds\n\t" ""
#define PRE_CLOB "s45", "s46", "s47", "s48", "s49", "s50", "s51", "s52", "s53"
__device__ __forceinline__ void pre_proj(int wv, rsrc_t rb) {
  const int lane = lane_id();
  const unsigned lds0 = (unsigned)(uintptr_t)(__attribute__((address_space(3))) unsigned char*)dyn_lds;
  asm volatile(PRE_ASM ::"{v16}"((int)OFF_XR + wv * 10240 + lane * 16), "{v17}"(wv * 64 + lane < 96 ? (int)OFF_ST + wv * 1024 + lane * 16 : OOB), [rs] "s"(rb),
               [ldx] "s"(lds0 + PJ_X + wv * 10240), [ld1] "s"(lds0 + PJ_X + 40960 + (wv < 2 ? wv : 2) * 1024) : PRE_CLOB, "scc", "m0", "memory");
}
template <bool VU>
__device__ __forceinline__ void job_proj(int wv_, const v32u (&w)[10], unsigned char* ws, const Pool& pool, int layer, int h) {
  const unsigned lds0 = (unsigned)(uintptr_t)(__attribute__((address_space(3))) unsigned char*)dyn_lds;
  const int lane = lane_id(), wv = wv_, g = lane >> 4, r = lane & 15;
  const v4u rs = make_rs(ws, (unsigned)OFF_END);
  v16u ad = (unsigned)lane;
  ad[0] = (int)OFF_XR + wv * 10240 + lane * 16;
  ad[1] = wv * 64 + lane < 96 ? (int)OFF_ST + wv * 1024 + lane * 16 : OOB;
  ad[3] = cnt_off(layer, CNT_IDFT, lane & 31);
  ad[4] = lane == 0 ? kqf_off(layer, 0, (VU ? 32 : 0) + h) + wv * 4 : OOB;
  ad[5] = (VU ? (int)OFF_UF + (wv * 320 + h) * 1024 : (h * RT * 4 + wv) * 1024) + lane * 16;
  ad[6] = lds0 + PJ_X + lane * 16;
  ad[7] = lds0 + PJ_X + 40960 + r * 48;
  ad[8] = lds0 + wv * 1024 + lane * 16;
  ad[9] = lds0 + 8192 + lane * 16;
  v4u rf = rs;
  v8u fo;
  if constexpr (!VU) {
    fo[0] = h * 16384 + wv * 2048 + lane * 16;
#pragma unroll
    for (int k = 0; k < 3; ++k) { const int u = h * 10 + wv + 4 * k; fo[1 + k] = wv + 4 * k < 10 ? (u >> 3) * 16384 + (u & 7) * 1024 + lane * 16 : OOB; }
    asm volatile("v_mov_b32 %0, -1" : "=v"(fo[4]));
    fo[5] = fo[6] = fo[7] = fo[4];
  }
  if (layer > 0) dep_wait(wv_, ws, fan_off(1, 1, (VU ? 32 : 0) + h), 1u);
  PH(2 + layer * 6)
#define PJ_OPS                                                                                                                      \
  : W_OPS, [rs] "s"(rs), [rdy] "s"((layer - 1) | 63), [ldx] "s"(lds0 + PJ_X + wv * 10240),                        \
    [ld1] "s"(lds0 + PJ_X + 40960 + (wv < 2 ? wv : 2) * 1024), [qf] "s"((int)OFF_QF), [kf] "s"((int)OFF_KF), [cnt] "s"(cnt_off(layer, CNT_IDFT, 0))
  if constexpr (VU) asm volatile(VU_ASM : "+{v[16:31]}"(ad) PJ_OPS : VU_CLOB, "vcc", "scc", "m0", "memory");
  else asm volatile(KQ_ASM : "+{v[16:31]}"(ad), "+&{s[56:59]}"(rf) PJ_OPS, "{v[152:159]}"(fo), [sel] "s"(pool.sel), [ysel] "s"(pool.ysel), [pool] "s"(pool.base),
                    [yb] "s"(pool.base + P_Y), [size] "s"((unsigned)OFF_END), [skip] "s"(h >= 20 && h < 30 ? 3ull << 62 : 2ull << 62)
                    : KQ_CLOB, "vcc", "scc", "m0", "memory");
  LDSBAR();
}

__device__ __forceinline__ void job_dft(int wv_, unsigned char* ws, int layer, int fg, int set) {
  const unsigned lds0 = (unsigned)(uintptr_t)(__attribute__((address_space(3))) unsigned char*)dyn_lds;
  const int lane = lane_id(), wv = wv_, g = lane >> 4, r = lane & 15;
  const v4u rs = make_rs(ws, (unsigned)OFF_END);
  const rsrc_t rb = ws_rsrc(ws);
  v32u w[5];
#pragma unroll
  for (int i = 0; i < 40; ++i) put4(w[i >> 3], i & 7, ldp(rb, (int)OFF_FWD + ((fg * 8 + wv * 2) * KT + i) * 1024 + lane * 16));
  v16u ad = (unsigned)lane;
  ad[0] = (int)OFF_XR + wv * 10240 + lane * 16;
  ad[1] = wv * 64 + lane < 96 ? (int)OFF_ST + wv * 1024 + lane * 16 : OOB;
  ad[3] = cnt_off(layer, CNT_OUT, 0);
  ad[5] = (int)OFF_X1T + (((fg * 8 + wv * 2) * 4 + g) * S_ + r) * 16;
  ad[6] = lds0 + lane * 16;
  ad[7] = lds0 + 40960 + r * 48;
  asm volatile(DF_ASM
               : "+{v[16:31]}"(ad)
               : "{a[0:31]}"(w[0]), "{a[32:63]}"(w[1]), "{a[64:95]}"(w[2]), "{a[96:127]}"(w[3]), "{a[128:159]}"(w[4]), [rs] "s"(rs), [set] "s"(set),
                 [ldx] "s"(lds0 + wv * 10240), [ld0] "s"(lds0), [ld1] "s"(lds0 + 40960 + (wv < 2 ? wv : 2) * 1024)
               : DF_CLOB, "vcc", "scc", "m0", "memory");
}

constexpr int NOUT = 44;
constexpr int MAIN_LDS = PJ_LDS;
static_assert(MAIN_LDS >= JOB_LDS && MAIN_LDS >= REC_LDS && MAIN_LDS <= 160 * 1024, "LDS");
__device__ __forceinline__ int take_role(int wv_, unsigned char* ws) {
  int* slot = (int*)dyn_lds;
  if (TIDX == 0) {
    const int x = (int)xcc_id();
    const int first = x;
    int role = 0;
#pragma unroll 1
    for (int i = 0; i < 8; ++i) {
      const int pl = first ^ i;
      const unsigned t = __hip_atomic_fetch_add((gu32*)(ws + tick_off(0, pl)), 1u, __ATOMIC_RELAXED, __HIP_MEMORY_SCOPE_AGENT) + 1u;
      if (t < 32u) { role = pl * 32 + (int)t; break; }
    }
    *slot = role;
  }
  __syncthreads();
  const int role = __builtin_amdgcn_readfirstlane(*slot);
  __syncthreads();
  return role;
}
#define TROB (24 * 256 + 16)
#define TC2 30
#define TVIS 32
#define TDLT 170
#define TM1 88
#define TM2 40
__global__ void __launch_bounds__(256, 1) k_main(unsigned char* ws, const u16* Wq, const u16* Wk, const u16* Wv, const u16* Wo, long nlayer
                                                ) {
  v4u* smem = (v4u*)dyn_lds;
  const int wv_ = __builtin_amdgcn_readfirstlane(threadIdx.x >> 6);
  const RecTune tn = {TM1, TM2, 4, TROB, TC2, TVIS, TDLT, 1};
  unsigned long long* dbg = nullptr;
#define RSTAMP_(i)
  const rsrc_t rs = ws_rsrc(ws);
  PH(1)
  v4i cal[18];
  pool_load(ws, cal);
  const int tk = take_role(wv_, ws), pl = tk >> 5, tt = tk & 31;
  Pool pool = pool_init(ws, cal);
  const int role = pl < 4 ? (tt < 16 ? (tt & 8) * 4 + pl * 8 + (tt & 7) : (tt < 26 ? 64 + pl * 10 + tt - 16 : 104 + pl * 6 + tt - 26)) : tk;
  if (role >= 104 && role < 128) return;
  if (role >= 64 && role < 104) {
#pragma unroll 1
    for (int layer = 0; layer < nlayer; ++layer) {
      if (layer > 0) {
        job_col(wv_, ws, rs, (f32x2*)dyn_lds, role + 57, fan_off(0, 1, (role + 57) & 63));
        col_signal(wv_, ws, 1, role + 57);
        PH(7)
      }
      ((unsigned*)(dyn_lds + L_T))[lane_id() & 15] = 0u;
      __syncthreads();
      if (wv_ == 3) rec_fin(ws, pool, layer, role - 64, pl, tn, dbg);
      else if (wv_ == 0) rec_sched(ws, pool, layer, role - 64, pl, tn, dbg);
      __syncthreads();
    }
    return;
  }
  if (tt < 16) {
    v32u w[10];
    const int hd = (pl - 4) * 8 + (tt & 7);
    const unsigned lds0 = (unsigned)(uintptr_t)(__attribute__((address_space(3))) unsigned char*)dyn_lds;
    if (role < 64) {
      const bool pv = role >= 32;
      load_rows(w, pv ? 3 : 0, pv ? Wv : Wq, pv ? Wo : Wk, role & 31, wv_, lds0);
      LDSBAR();
    } else {
      pre_proj(wv_, rs);
      const bool vu = (tt & 8) != 0;
      load_rows(w, vu ? 2 : 1, vu ? Wv : Wq, vu ? Wo : Wk, hd, wv_, lds0 + PJ_X + 2 * PJ_BUF);
    }
#pragma unroll 1
    for (int layer = 0; layer < nlayer; ++layer) {
      int ro = role, xs = pl, wq = wv_, h2 = hd | (tt & 8) << 8;
      asm volatile("" : "+s"(ro), "+s"(xs), "+s"(wq), "+s"(h2), "+s"(ws), "+s"(pool.base));
      if (layer > 0) {
        const int jb = ro < 64 ? ro + 57 : (xs - 4) * 16 + (ro & 31);
        if (ro < 64 || jb < 57) {
          __syncthreads();
          job_col(wq, ws, ws_rsrc(ws), (f32x2*)dyn_lds, jb, fan_off(0, 1, jb & 63));
          col_signal(wq, ws, 1, jb);
          RSTAMP_(5)
          PH(7)
        }
      }
      if (ro < 32) { rec_p1(wq, w, ws, pool, layer, ro, xs, tn, dbg); PH(4 + layer * 6) }
      else if (ro < 64) rec_pv(wq, w, ws, pool, layer, ro - 32, xs, tn, dbg);
      else {
        if (h2 >> 8) job_proj<true>(wq, w, ws, pool, layer, h2 & 31);
        else job_proj<false>(wq, w, ws, pool, layer, h2 & 31);
        RSTAMP_(2)
      }
    }
    return;
  }
#pragma unroll 1
  for (int layer = 0; layer < nlayer; ++layer) {
    if (layer > 0 && tt < 31) {
      const int i = tt - 16;
      job_idft<false>(wv_, ws, fan_off(1, 0, (pl - 4) * 16 + i), rs, smem, (i / 5) * 4 + pl - 4, 12, i % 5, nullptr, dbg + 60000 + role * 8);
    }
    RSTAMP_(2)
    __syncthreads();
    if (tt >= 27) {
      job_dft(wv_, ws, layer, tt - 27, pl - 4);
      if (layer == 0) fan_signal(wv_, ws, cnt_off(0, CNT_IDFT, 0), 20u, -1, 0u, fan_off(0, 1, 0));
    } else {
#pragma unroll 1
      for (int j = (pl - 4) * 11 + tt - 16; j < 320; j += NOUT) {
        flags32_wait(wv_, ws, kqf_off(layer, j / 5, 32));
        job_out(wv_, ws, rs, smem, pool.ysel, layer, j / 5, j % 5);
        dep_signal(wv_, ws, cnt_off(layer, CNT_OUT, j / 5));
      }
    }
    RSTAMP_(3)
    PH((tt >= 27 ? 6 : 5) + layer * 6)
  }
}

extern "C" void kernel_launch(void* const* d_in, const int* in_sizes, int n_in, void* d_out, int out_size, void* d_ws, size_t ws_size,
                              hipStream_t stream) {
  const u16* x = (const u16*)d_in[0];
  const u16* mem0 = (const u16*)d_in[1];
  const u16* Wq = (const u16*)d_in[2];
  const u16* Wk = (const u16*)d_in[3];
  const u16* Wv = (const u16*)d_in[4];
  const u16* Wo = (const u16*)d_in[5];
  const u16* wr = (const u16*)d_in[8];
  const u16* wi = (const u16*)d_in[9];
  unsigned char* ws = (unsigned char*)d_ws;
  u16* out = (u16*)d_out;
  (void)hipFuncSetAttribute((const void*)k_main, hipFuncAttributeMaxDynamicSharedMemorySize, MAIN_LDS);
  (void)hipFuncSetAttribute((const void*)k_fin, hipFuncAttributeMaxDynamicSharedMemorySize, FIN_LDS);
  k_prep<<<PB_TOTAL, 256, 0, stream>>>(x, mem0, Wq, Wk, Wv, Wo, wr, wi, ws);
  k_main<<<256, 256, MAIN_LDS, stream>>>(ws, Wq, Wk, Wv, Wo, 2);
  k_fin<<<NCOL + 320, 256, FIN_LDS, stream>>>(ws, out);
}
```

```cpp
#include <hip/hip_runtime.h>
#include <hip/hip_bf16.h>
#include <stdint.h>

typedef unsigned v4u __attribute__((ext_vector_type(4)));
typedef unsigned v2u __attribute__((ext_vector_type(2)));
typedef float f32x4 __attribute__((ext_vector_type(4)));
typedef float f32x2 __attribute__((ext_vector_type(2)));
typedef __bf16 bf16x8 __attribute__((ext_vector_type(8)));
typedef __bf16 bf16x2 __attribute__((ext_vector_type(2)));
typedef unsigned short u16;

constexpr int S_ = 2048, D_ = 640, NSEG = 64, KT = 20, RT = 128;
constexpr int NFT = 40;
constexpr int NFK = 20;
constexpr int WIN = 32768, REG = 16 * WIN;
constexpr int W_MEM = 10 * WIN, W_MISC = 15 * WIN;
__host__ __device__ constexpr int copy_off(int c) { return (c & 3) * 2048 + (c >> 2) * 16384; }
constexpr int M_STAMP = W_MISC + copy_off(4), M_TM = M_STAMP + 40 * 64, M_CAL = W_MISC + copy_off(3);
constexpr int PAGE = 4 * REG, NYP = 22, P_Y = NSEG * REG;
constexpr size_t SZ_POOL = (size_t)P_Y + (size_t)(NYP + 1) * PAGE;

constexpr size_t OFF_CNT = 0;
constexpr int CNT_IDFT = 0, CNT_COL = 1, CNT_OUT = 2;
constexpr size_t OFF_PROG = OFF_CNT + (size_t)4 * 32 * 128;
constexpr size_t OFF_KQF = OFF_PROG + (size_t)NSEG * 4096;
constexpr size_t OFF_TICK = OFF_KQF + (size_t)NSEG * 4096;
constexpr size_t OFF_FAN = OFF_TICK + (size_t)16 * 128;
constexpr size_t HO_LAYER = OFF_FAN + (size_t)2 * 64 * 128;
constexpr size_t OFF_HO = SZ_POOL;
constexpr size_t OFF_SEL = OFF_HO + 2 * HO_LAYER;
__host__ __device__ constexpr int prog_off(int layer, int rb, int slot) { return (int)(OFF_HO + layer * HO_LAYER + OFF_PROG) + (rb * 64 + slot) * 64; }
__host__ __device__ constexpr int kqf_off(int layer, int sg, int h) { return (int)(OFF_HO + layer * HO_LAYER + OFF_KQF) + (sg * 64 + h) * 64; }
__host__ __device__ constexpr int cnt_off(int layer, int kind, int rb) { return (int)(OFF_HO + layer * HO_LAYER + OFF_CNT) + (kind * 32 + rb) * 128; }
__host__ __device__ constexpr int tick_off(int layer, int i) { return (int)(OFF_HO + layer * HO_LAYER + OFF_TICK) + i * 128; }
__host__ __device__ constexpr int fan_off(int layer, int set, int i) { return (int)(OFF_HO + layer * HO_LAYER + OFF_FAN) + (set * 64 + i) * 128; }
constexpr size_t OFF_XR = (OFF_SEL + 8192 + 4095) / 4096 * 4096;
constexpr size_t OFF_ST = OFF_XR + (size_t)RT * KT * 1024;
constexpr size_t OFF_QF = OFF_ST + (size_t)S_ * 48;
constexpr size_t OFF_KF = OFF_QF + (size_t)32 * RT * 4 * 1024;
constexpr size_t OFF_UF = OFF_KF + (size_t)32 * RT * 4 * 1024;
constexpr size_t OFF_PXF = OFF_UF + (size_t)NSEG * 80 * 16 * 1024;
constexpr size_t OFF_PXM = OFF_PXF + (size_t)NSEG * 32 * 2 * 1024;
constexpr size_t OFF_FWD = OFF_PXM + (size_t)NSEG * 8 * 2 * 1024;
constexpr size_t OFF_INV = OFF_FWD + (size_t)NFT * KT * 1024;
constexpr size_t OFF_X1T = OFF_INV + (size_t)40 * NFK * 1024;
constexpr size_t OFF_ZT = OFF_X1T + (size_t)NFT * 16 * S_ * 4;
constexpr size_t OFF_WT = OFF_ZT + (size_t)NFK * 32 * S_ * 4;
constexpr size_t OFF_TW = OFF_WT + (size_t)321 * S_ * 4;
constexpr size_t OFF_DBG = OFF_TW + (size_t)2048 * 8;
constexpr size_t OFF_END = OFF_DBG + (size_t)1048576;
static_assert(OFF_END <= ((size_t)256 << 20), "workspace too large");

constexpr int AUX_SC1 = 16;
#define PH(k)

__device__ __forceinline__ f32x4 mfma(v4u a, v4u b, f32x4 c) {
  return __builtin_amdgcn_mfma_f32_16x16x32_bf16(__builtin_bit_cast(bf16x8, a), __builtin_bit_cast(bf16x8, b), c, 0, 0, 0);
}
__device__ __forceinline__ unsigned pk(float a, float b) {
  f32x2 v = {a, b};
  return __builtin_bit_cast(unsigned, __builtin_convertvector(v, bf16x2));
}
__device__ __forceinline__ float lo_f(unsigned u) { return __builtin_bit_cast(float, u << 16); }
__device__ __forceinline__ float hi_f(unsigned u) { return __builtin_bit_cast(float, u & 0xFFFF0000u); }
__device__ __forceinline__ float bfr(float x) { return lo_f(pk(x, x)); }
__device__ __forceinline__ v4u pk8(f32x4 a, f32x4 b) { return v4u{pk(a.x, a.y), pk(a.z, a.w), pk(b.x, b.y), pk(b.z, b.w)}; }
__device__ __forceinline__ int sigma(int g, int jj) { return 16 * (jj >> 2) + 4 * g + (jj & 3); }
__device__ __forceinline__ int colmap(int c8, int jj) {
  const int p = c8 >> 3, ga = (c8 >> 1) & 3, half = c8 & 1;
  return 32 * (2 * p + (ga >> 1)) + 16 * ((jj >> 1) & 1) + 4 * (2 * (ga & 1) + (jj >> 2)) + 2 * half + (jj & 1);
}
__device__ __forceinline__ int pool_base(const void* ws) { return (int)((0u - (unsigned)(uintptr_t)ws) & (unsigned)(PAGE - 1)); }
__device__ __forceinline__ unsigned xcc_id() { return (unsigned)__builtin_amdgcn_s_getreg(20 | (3 << 11)) & 7u; }
__device__ __forceinline__ float shx(float v, int m) { return __shfl_xor(v, m); }
__device__ __forceinline__ unsigned nosent(unsigned x) { return x < 0xFFFFFFFEu ? x : 0xFFFFFFFEu; }

__device__ __forceinline__ f32x2 frag_stat(v4u c) {
  const float v[8] = {lo_f(c.x), hi_f(c.x), lo_f(c.y), hi_f(c.y), lo_f(c.z), hi_f(c.z), lo_f(c.w), hi_f(c.w)};
  float q = 0.f;
#pragma unroll
  for (int j = 0; j < 8; ++j) q = __builtin_fmaf(v[j], v[j], q);
  return f32x2{((v[0] + v[1]) + (v[2] + v[3])) + ((v[4] + v[5]) + (v[6] + v[7])), q};
}
__device__ __forceinline__ f32x2 rows_sum(f32x2 a) {
  a.x += shx(a.x, 16); a.y += shx(a.y, 16); a.x += shx(a.x, 32); a.y += shx(a.y, 32);
  return a;
}

__device__ __forceinline__ void dft_frag(unsigned char* ws, bool fwd, int fi, int lane) {
  const int g = lane >> 4, r = lane & 15;
  float hi[8];
#pragma unroll
  for (int jj = 0; jj < 8; ++jj) {
    int f, d;
    if (fwd) { const int kt = fi % KT, ft = fi / KT; f = 16 * ft + r; d = 32 * kt + sigma(g, jj); }
    else { const int fk = fi % NFK, dt = fi / NFK; d = 16 * dt + r; f = 32 * fk + 8 * g + jj; }
    const int k = f == 1 ? 320 : f >> 1;
    const int idx = (k * d) % D_;
    const float a = (float)idx * (2.0f / D_);
    float v = ((f & 1) && f != 1) ? -sinpif(a) : cospif(a);
    if (!fwd && f > 1) v *= 2.f;
    hi[jj] = v;
  }
  v4u* dst = (v4u*)(ws + (fwd ? OFF_FWD : OFF_INV));
  dst[(size_t)fi * 64 + lane] = v4u{pk(hi[0], hi[1]), pk(hi[2], hi[3]), pk(hi[4], hi[5]), pk(hi[6], hi[7])};
}

constexpr int PB_XR = RT * KT / 16;
constexpr int PB_FWD = 2 * NFT * KT / 4;
constexpr int PB_WT = 32 * 6 + 8;
constexpr int PB_HO = (int)((2 * HO_LAYER + 32767) / 32768);
constexpr int PB_CAL = 4 * NSEG + 8 * NYP;
constexpr int PB_FILL = 2 * 16, PB_YF = 20;
constexpr int PB_TOTAL = PB_CAL + PB_XR + PB_FWD + PB_WT + PB_FILL + PB_YF + PB_HO;
#define PB_LIST {PB_CAL, 0, PB_XR, PB_FWD, PB_WT, PB_FILL, PB_YF, PB_HO}
static_assert(HO_LAYER % 16 == 0, "fill granularity");
template <int AUX>
__device__ __forceinline__ void fill_pool(unsigned char* ws, int rg, int half, int tid, int q0 = 0, int q1 = 64) {
  const __amdgpu_buffer_rsrc_t rs = __builtin_amdgcn_make_buffer_rsrc(ws, 0, (int)SZ_POOL, 0x00020000);
  const int base = pool_base(ws) + rg * REG + half * 8192 + tid * 16;
#pragma unroll 8
  for (int q = q0; q < q1; ++q) {
    const bool skip = (rg == NSEG - 1 && q >= 40 && q < 60) || ((q | 2) == 63 && tid >= 128);
    __builtin_amdgcn_raw_buffer_store_b128(v4u{0xFFFFFFFFu, 0xFFFFFFFFu, 0xFFFFFFFFu, 0xFFFFFFFFu}, rs,
                                           skip ? 0x7FFFFFF0 : base + (q >> 1) * 16384 + (q & 1) * 4096, 0, AUX);
  }
}
__device__ __forceinline__ void fill_mem0(unsigned char* ws, const u16* __restrict__ mem0, int w, int tid) {
  const int c = tid & 127, c8 = (w * 8 + (c >> 4)) * 2 + (c & 1), row = (c >> 1) & 7;
  unsigned t[4];
#pragma unroll
  for (int k = 0; k < 4; ++k) t[k] = nosent(*(const unsigned*)(mem0 + (size_t)row * D_ + colmap(c8, 2 * k)));
  v4u* dst = (v4u*)(ws + pool_base(ws) + (size_t)(NSEG - 1) * REG + W_MEM + w * WIN);
#pragma unroll
  for (int q = 0; q < 8; ++q) dst[q * 256 + tid] = v4u{t[0], t[1], t[2], t[3]};
}
typedef int v4i __attribute__((ext_vector_type(4)));
__device__ __forceinline__ v4i calibrate(unsigned char* ws, unsigned* lds, int off) {
  const __amdgpu_buffer_rsrc_t rs = __builtin_amdgcn_make_buffer_rsrc(ws, 0, (int)SZ_POOL, 0x00020000);
  const int base = pool_base(ws) + off, tid = threadIdx.x, wv = __builtin_amdgcn_readfirstlane(tid >> 6);
  __builtin_amdgcn_raw_buffer_store_b128(v4u{1u, 2u, 3u, 4u}, rs, base + wv * 128 + ((tid >> 3) & 3) * 8192 + (tid & 7) * 16, 0, AUX_SC1);
  const unsigned char* p = ws + base;
  unsigned t0, t1, t2, t3, t4;
  asm volatile("s_load_dword %0, %5, 0x180\n\ts_load_dword %1, %5, 0x2180\n\ts_load_dword %2, %5, 0x4180\n\ts_load_dword %3, %5, 0x6180\n\t"
               "s_waitcnt vmcnt(0) lgkmcnt(0)\n\ts_memtime s[40:41]\n\ts_waitcnt lgkmcnt(0)\n\ts_mov_b32 %0, s40\n\t"
               "s_load_dword %1, %6, 0\n\ts_waitcnt lgkmcnt(0)\n\ts_memtime s[40:41]\n\ts_waitcnt lgkmcnt(0)\n\ts_mov_b32 %1, s40\n\t"
               "s_load_dword %2, %6, 0x2000\n\ts_waitcnt lgkmcnt(0)\n\ts_memtime s[40:41]\n\ts_waitcnt lgkmcnt(0)\n\ts_mov_b32 %2, s40\n\t"
               "s_load_dword %3, %6, 0x4000\n\ts_waitcnt lgkmcnt(0)\n\ts_memtime s[40:41]\n\ts_waitcnt lgkmcnt(0)\n\ts_mov_b32 %3, s40\n\t"
               "s_load_dword %4, %6, 0x6000\n\ts_waitcnt lgkmcnt(0)\n\ts_memtime s[40:41]\n\ts_waitcnt lgkmcnt(0)\n\ts_mov_b32 %4, s40"
               : "=&s"(t0), "=&s"(t1), "=&s"(t2), "=&s"(t3), "=&s"(t4) : "s"(p), "s"(p + wv * 128) : "s40", "s41", "memory");
  *(v4u*)(lds + wv * 4) = v4u{t1 - t0, t2 - t1, t3 - t2, t4 - t3};
  __syncthreads();
  unsigned best[4];
#pragma unroll
  for (int c = 0; c < 4; ++c) best[c] = min(min(lds[c], lds[4 + c]), lds[8 + c]);
  const int x = (int)xcc_id(), sg = x < 4 ? 1 - 2 * (x & 1) : 0;
  const int d = min(max((int)(best[0] + best[2]) - (int)(best[1] + best[3]), -400), 400);
  return v4i{x < 4 ? d : -d, sg * min(max((int)best[0] - (int)best[2], -80), 80), sg * min(max((int)best[1] - (int)best[3], -80), 80), 0};
}
__device__ __forceinline__ int y_region(unsigned long long sel, int s) { return (s * 80 + (s * 43 >> 7) * 16 + (int)((sel >> s) & 1ull)) * 8192; }

__global__ void __launch_bounds__(256) k_prep(const u16* __restrict__ x,
                                              const u16* __restrict__ mem0, const u16* __restrict__ Wq,
                                              const u16* __restrict__ Wk, const u16* __restrict__ Wv, const u16* __restrict__ Wo,
                                              const u16* __restrict__ wr, const u16* __restrict__ wi, unsigned char* __restrict__ ws) {
  __shared__ __attribute__((aligned(16))) unsigned t32[64 * 65];
  int b = blockIdx.x;
  const int tid = threadIdx.x, lane = tid & 63, wv = tid >> 6, g = lane >> 4, r = lane & 15;
  if (b < PB_CAL) {
    PH(0)
    const int c = b - 4 * NSEG;
    const v4i sel = calibrate(ws, t32, (c < 0 ? (b >> 2) * REG : P_Y + (c >> 3) * PAGE + 3 * REG - (c & 4) * (WIN / 4)) + M_CAL + (b & 3) * 512);
    if (tid == 0) { if (c < 0) ((v4i*)(ws + OFF_SEL))[b] = sel; else ((int*)(ws + OFF_SEL + 4096))[c] = sel.x; }
    if (b < 5) fill_mem0(ws, mem0, b, tid);
    return;
  }
  b -= PB_CAL;
  if (b < PB_XR) {
    f32x2 st = {0.f, 0.f};
#pragma unroll
    for (int q = 0; q < 4; ++q) {
      const int f = b * 16 + wv * 4 + q, kt = f % KT, rt = f / KT;
      const u16* xr = x + (size_t)(rt * 16 + r) * D_ + 32 * kt + 4 * g;
      const v2u c0 = *(const v2u*)xr, c1 = *(const v2u*)(xr + 16);
      const v4u c = v4u{c0.x, c0.y, c1.x, c1.y};
      ((v4u*)(ws + OFF_XR))[(size_t)f * 64 + lane] = c;
      st += frag_stat(c);
    }
    st = rows_sum(st);
    const int f4 = b * 4 + wv;
    if (g == 0) ((f32x2*)(ws + OFF_ST))[((f4 / 5) * 16 + r) * 6 + f4 % 5] = st;
    return;
  }
  b -= PB_XR;
  if (b < PB_FWD) { dft_frag(ws, b < PB_FWD / 2, (b * 4 + wv) % (NFT * KT), lane); return; }
  b -= PB_FWD;
  if (b < PB_WT) {
    if (b >= 192) {
      const int i = (b - 192) * 256 + tid;
      const float a = (float)i * (2.0f / 2048.f);
      ((f32x2*)(ws + OFF_TW))[i] = f32x2{cospif(a), -sinpif(a)};
      return;
    }
    const int m0 = (b & 31) * 64, k0 = (b >> 5) * 64;
#pragma unroll
    for (int q = 0; q < 16; ++q) {
      const int m = q * 4 + wv, k = k0 + lane;
      if (k < 321) t32[m * 65 + lane] = (unsigned)wr[(size_t)(m0 + m) * 321 + k] | ((unsigned)wi[(size_t)(m0 + m) * 321 + k] << 16);
    }
    __syncthreads();
#pragma unroll
    for (int q = 0; q < 16; ++q) {
      const int k = q * 4 + wv;
      if (k0 + k < 321) ((unsigned*)(ws + OFF_WT))[(size_t)(k0 + k) * S_ + m0 + lane] = t32[lane * 65 + k];
    }
    return;
  }
  b -= PB_WT;
  if (b < PB_FILL) { fill_pool<0>(ws, ((b >> 4) + 63) & 63, (b >> 3) & 1, tid, (b & 7) * 8, (b & 7) * 8 + 8); return; }
  b -= PB_FILL;
  if (b < PB_YF) {
#pragma unroll
    for (int q = 0; q < 8; ++q) ((v4u*)(ws + pool_base(ws) + P_Y))[b * 2048 + q * 256 + tid] = v4u{0xFFFFFFFFu, 0xFFFFFFFFu, 0xFFFFFFFFu, 0xFFFFFFFFu};
    return;
  }
  b -= PB_YF;
#pragma unroll
  for (int q = 0; q < 8; ++q) {
    const int i = b * 2048 + q * 256 + tid;
    if ((size_t)i * 16 < 2 * HO_LAYER) ((v4u*)(ws + OFF_HO))[i] = v4u{0xFFFFFFFFu, 0xFFFFFFFFu, 0xFFFFFFFFu, 0xFFFFFFFFu};
  }
}

__device__ __forceinline__ int lane_id() {
  int l;
  asm volatile("v_mbcnt_lo_u32_b32 %0, -1, 0\n\tv_mbcnt_hi_u32_b32 %0, -1, %0" : "=v"(l));
  return l;
}
#define TIDX (wv_ * 64 + lane_id())
typedef __amdgpu_buffer_rsrc_t rsrc_t;
__device__ __forceinline__ rsrc_t ws_rsrc(unsigned char* ws) { return __builtin_amdgcn_make_buffer_rsrc(ws, 0, (int)OFF_END, 0x00020000); }
__device__ __forceinline__ v4u ldc(rsrc_t rs, int off) { return __builtin_amdgcn_raw_buffer_load_b128(rs, off, 0, AUX_SC1); }
__device__ __forceinline__ v4u ldp(rsrc_t rs, int off) { return __builtin_amdgcn_raw_buffer_load_b128(rs, off, 0, 0); }
__device__ __forceinline__ void stc(rsrc_t rs, int off, v4u v) { __builtin_amdgcn_raw_buffer_store_b128(v, rs, off, 0, AUX_SC1); }

typedef __attribute__((address_space(1))) unsigned gu32;
__device__ __forceinline__ void dep_wait(int wv_, unsigned char* ws, int off, unsigned want) {
  if (TIDX == 0) {
    gu32* p = (gu32*)(ws + off);
    for (unsigned spins = 0; spins < (1u << 17); ++spins) {
      if (__hip_atomic_load(p, __ATOMIC_RELAXED, __HIP_MEMORY_SCOPE_AGENT) + 1u >= want) break;
      __builtin_amdgcn_s_sleep(8);
    }
  }
  __syncthreads();
}
__device__ __forceinline__ void prog_prewait(int wv_, unsigned char* ws, int off) {
  if (TIDX == 0) {
    gu32* p = (gu32*)(ws + off);
    for (unsigned spins = 0; spins < (1u << 16); ++spins) {
      if (__hip_atomic_load(p, __ATOMIC_RELAXED, __HIP_MEMORY_SCOPE_AGENT) != 0xFFFFFFFFu) break;
      __builtin_amdgcn_s_sleep(48);
    }
  }
}
__device__ __forceinline__ void prog_wait(int wv_, unsigned char* ws, int off) {
  if (wv_ == 0) {
    const int l = lane_id();
    gu32* p0 = (gu32*)(ws + off + l * 64);
    for (unsigned spins = 0; spins < (1u << 17); ++spins) {
      const unsigned a = __hip_atomic_load(p0, __ATOMIC_RELAXED, __HIP_MEMORY_SCOPE_AGENT);
      if (!__any(a == 0xFFFFFFFFu)) break;
      __builtin_amdgcn_s_sleep(8);
    }
  }
  __syncthreads();
}
__device__ __forceinline__ void flags32_wait(int wv_, unsigned char* ws, int off) {
  if (wv_ == 0) {
    const int l = lane_id();
    __attribute__((address_space(1))) unsigned long long* p0 = (__attribute__((address_space(1))) unsigned long long*)(ws + off + (l & 31) * 64 + (l >> 5) * 8);
    for (unsigned spins = 0; spins < (1u << 17); ++spins) {
      const unsigned long long v = __hip_atomic_load(p0, __ATOMIC_RELAXED, __HIP_MEMORY_SCOPE_AGENT);
      if (!__any(max((unsigned)v, (unsigned)(v >> 32)) == 0xFFFFFFFFu)) break;
      __builtin_amdgcn_s_sleep(16);
    }
  }
  __syncthreads();
}
__device__ __forceinline__ void dep_signal(int wv_, unsigned char* ws, int off, unsigned inc = 1u) {
  asm volatile("s_waitcnt vmcnt(0)" ::: "memory");
  __syncthreads();
  if (TIDX == 0) __hip_atomic_fetch_add((gu32*)(ws + off), inc, __ATOMIC_RELAXED, __HIP_MEMORY_SCOPE_AGENT);
}

__device__ __forceinline__ void stat_put(v4u* lds, int wv, int rt, int lane, v4u y) {
  const f32x2 st = rows_sum(frag_stat(y));
  if (lane < 16) ((f32x2*)(lds + 5120))[(wv * 2 + rt) * 16 + lane] = st;
}
__device__ __forceinline__ void stat_store(rsrc_t rs, v4u* lds, int wv, int lane, int sg, int cg) {
  __syncthreads();
  if (wv == 0) {
    const f32x2* sp = (const f32x2*)(lds + 5120) + (lane & 31);
    const f32x2 st = (sp[0] + sp[32]) + (sp[64] + sp[96]);
    __builtin_amdgcn_raw_buffer_store_b64(__builtin_bit_cast(v2u, st), rs, lane < 32 ? (int)OFF_ST + ((sg * 32 + lane) * 6 + cg) * 8 : 0x7FFFFFF0, 0, AUX_SC1);
  }
}

__device__ __forceinline__ void job_out(int wv_, unsigned char* ws, rsrc_t rs, v4u* lds, unsigned long long ysel, int layer, int s, int cg) {
  const int lane = lane_id(), wv = wv_, g = lane >> 4, r = lane & 15;
  const int kt = cg * 4 + wv;
  const int c16 = 2 * kt + (r >> 3), ci = (g * 16 + (r & 4) * 2 + (r & 3)) * 16;
  const int ubase = (int)OFF_UF + (s * 40 + c16) * 32768 + ci;
  const int ybase = pool_base(ws) + P_Y + y_region(ysel, s) + c16 * 16384 + ci;
  v4u z[40][2];
#pragma unroll
  for (int ks = 0; ks < 32; ++ks) { z[ks][0] = ldc(rs, ubase + ks * 1024); z[ks][1] = ldc(rs, ubase + ks * 1024 + 64); }
  const int xo = (int)OFF_XR + ((2 * s * KT + kt) * 64 + lane) * 16;
  const v4u x0 = ldc(rs, xo), x1 = ldc(rs, xo + KT * 1024);
  if (s > 1) prog_prewait(wv_, ws, prog_off(layer, s - 2, (s + cg * 7) & 31));
#define OSTAMP(i)
  OSTAMP(4)
  prog_wait(wv_, ws, prog_off(layer, s, 0));
  OSTAMP(0)
  {
    const unsigned l0 = (unsigned)(uintptr_t)(__attribute__((address_space(3))) v4u*)lds;
    const int vo = lane * 16;
#pragma unroll
    for (int q = 0; q < 5; ++q) {
      const unsigned d = l0 + (q < 4 ? wv * 16384 + q * 4096 : 65536 + wv * 4096);
      const int so = q < 4 ? (int)OFF_PXF + s * 65536 + wv * 16384 + q * 4096 : (int)OFF_PXM + s * 16384 + wv * 4096;
      asm volatile("s_mov_b32 m0, %0\n\ts_nop 0\n\tbuffer_load_dwordx4 %1, %2, %3 offen sc1 lds\n\t"
                   "buffer_load_dwordx4 %1, %2, %3 offen offset:1024 sc1 lds\n\tbuffer_load_dwordx4 %1, %2, %3 offen offset:2048 sc1 lds\n\t"
                   "buffer_load_dwordx4 %1, %2, %3 offen offset:3072 sc1 lds" ::"s"(d), "v"(vo), "s"(rs), "s"(so) : "m0", "memory");
    }
#pragma unroll
    for (int hq = 0; hq < 8; ++hq) { z[32 + hq][0] = ldc(rs, ybase + hq * 1024); z[32 + hq][1] = ldc(rs, ybase + hq * 1024 + 64); }
  }
  f32x4 acc[2][2] = {{{0, 0, 0, 0}, {0, 0, 0, 0}}, {{0, 0, 0, 0}, {0, 0, 0, 0}}};
  asm volatile("s_waitcnt vmcnt(16)\n\ts_barrier" ::: "memory");
  OSTAMP(1)
  v4u pf[2][8];
#pragma unroll
  for (int i = 0; i < 8; ++i) pf[0][i] = lds[i * 64 + lane];
#pragma unroll
  for (int b = 0; b < 10; ++b) {
    if (b < 9) {
#pragma unroll
      for (int i = 0; i < 8; ++i) pf[(b + 1) & 1][i] = lds[((b + 1) * 8 + i) * 64 + lane];
    }
    __builtin_amdgcn_sched_barrier(0);
#pragma unroll
    for (int i = 0; i < 8; ++i) {
      const v4u z0 = z[b * 4 + (i >> 1)][0], z1 = z[b * 4 + (i >> 1)][1];
      acc[0][i & 1] = mfma(b < 8 ? z0 : z0.xzyw, pf[b & 1][i], acc[0][i & 1]);
      acc[1][i & 1] = mfma(b < 8 ? z1 : z1.xzyw, pf[b & 1][i], acc[1][i & 1]);
    }
    __builtin_amdgcn_sched_barrier(0);
  }
#pragma unroll
  for (int qt = 0; qt < 2; ++qt) {
    const v4u x = qt ? x1 : x0;
    const auto s0 = __builtin_amdgcn_permlane16_swap(pk(acc[0][qt].x, acc[0][qt].y), pk(acc[1][qt].x, acc[1][qt].y), false, false);
    const auto s1 = __builtin_amdgcn_permlane16_swap(pk(acc[0][qt].z, acc[0][qt].w), pk(acc[1][qt].z, acc[1][qt].w), false, false);
    v4u y;
    y.x = pk(lo_f(x.x) + lo_f(s0[0]), hi_f(x.x) + hi_f(s0[0]));
    y.y = pk(lo_f(x.y) + lo_f(s0[1]), hi_f(x.y) + hi_f(s0[1]));
    y.z = pk(lo_f(x.z) + lo_f(s1[0]), hi_f(x.z) + hi_f(s1[0]));
    y.w = pk(lo_f(x.w) + lo_f(s1[1]), hi_f(x.w) + hi_f(s1[1]));
    stc(rs, xo + qt * KT * 1024, y);
    stat_put(lds, wv, qt, lane, y);
  }
  OSTAMP(2)
  stat_store(rs, lds, wv, lane, s, cg);
}

__device__ __forceinline__ f32x2 cmul(f32x2 a, f32x2 b) { return f32x2{a.x * b.x - a.y * b.y, a.x * b.y + a.y * b.x}; }
struct C2 { f32x2 r, i; };
__device__ __forceinline__ C2 operator+(C2 a, C2 b) { return C2{a.r + b.r, a.i + b.i}; }
__device__ __forceinline__ C2 operator-(C2 a, C2 b) { return C2{a.r - b.r, a.i - b.i}; }
__device__ __forceinline__ C2 cmul(C2 a, f32x2 w) {
  return C2{__builtin_elementwise_fma(a.r, f32x2{w.x, w.x}, -(a.i * f32x2{w.y, w.y})), __builtin_elementwise_fma(a.r, f32x2{w.y, w.y}, a.i * f32x2{w.x, w.x})};
}
__device__ __forceinline__ C2 cmul(C2 a, C2 w) { return C2{__builtin_elementwise_fma(a.r, w.r, -(a.i * w.i)), __builtin_elementwise_fma(a.r, w.i, a.i * w.r)}; }
template <int SGN> __device__ __forceinline__ C2 addrot(C2 a, C2 b) { return SGN < 0 ? C2{a.r + b.i, a.i - b.r} : C2{a.r - b.i, a.i + b.r}; }
template <int SGN> __device__ __forceinline__ void fft4(C2& a0, C2& a1, C2& a2, C2& a3) {
  const C2 t0 = a0 + a2, t1 = a0 - a2, t2 = a1 + a3, t3 = a1 - a3;
  a0 = t0 + t2; a1 = addrot<SGN>(t1, t3); a2 = t0 - t2; a3 = addrot<-SGN>(t1, t3);
}
template <int SGN> __device__ __forceinline__ void fft8(C2* v) {
  C2 e0 = v[0], e1 = v[2], e2 = v[4], e3 = v[6], o0 = v[1], o1 = v[3], o2 = v[5], o3 = v[7];
  fft4<SGN>(e0, e1, e2, e3);
  fft4<SGN>(o0, o1, o2, o3);
  const f32x2 h = {0.70710678118654752f, 0.70710678118654752f}, nh = -h;
  const C2 u1 = addrot<SGN>(o1, o1), u3 = addrot<SGN>(o3, o3);
  v[0] = e0 + o0; v[4] = e0 - o0;
  v[1] = C2{__builtin_elementwise_fma(u1.r, h, e1.r), __builtin_elementwise_fma(u1.i, h, e1.i)};
  v[5] = C2{__builtin_elementwise_fma(u1.r, nh, e1.r), __builtin_elementwise_fma(u1.i, nh, e1.i)};
  v[2] = addrot<SGN>(e2, o2); v[6] = addrot<-SGN>(e2, o2);
  v[3] = C2{__builtin_elementwise_fma(u3.i, SGN < 0 ? h : nh, e3.r), __builtin_elementwise_fma(u3.r, SGN < 0 ? nh : h, e3.i)};
  v[7] = C2{__builtin_elementwise_fma(u3.i, SGN < 0 ? nh : h, e3.r), __builtin_elementwise_fma(u3.r, SGN < 0 ? h : nh, e3.i)};
}
__device__ __forceinline__ int lpad(int i) { return i + (i >> 3); }
__device__ __forceinline__ C2 c2(f32x4 t) { return C2{f32x2{t.x, t.y}, f32x2{t.z, t.w}}; }
template <int SGN> __device__ __forceinline__ void pass8_load(const f32x4* in, f32x2 w1, int j, C2* v) {
  f32x4 t[8];
#pragma unroll
  for (int q = 0; q < 8; ++q) t[q] = in[lpad(j + 256 * q)];
  if (SGN > 0) w1.y = -w1.y;
  f32x2 w[8];
  w[1] = w1; w[2] = cmul(w1, w1); w[3] = cmul(w[2], w1); w[4] = cmul(w[2], w[2]); w[5] = cmul(w[4], w1); w[6] = cmul(w[3], w[3]); w[7] = cmul(w[4], w[3]);
  v[0] = c2(t[0]);
#pragma unroll
  for (int q = 1; q < 8; ++q) v[q] = cmul(c2(t[q]), w[q]);
  fft8<SGN>(v);
}
template <int NS, int R> __device__ __forceinline__ void pass_put(f32x4* out, int j, const C2* v) {
  const int k = j & (NS - 1);
  const int base = (j - k) * R + k;
#pragma unroll
  for (int q = 0; q < R; ++q) out[lpad(base + q * NS)] = f32x4{v[q].r.x, v[q].r.y, v[q].i.x, v[q].i.y};
}

constexpr int NCOL = 161, FB = 2048 + 256;
__device__ __forceinline__ void job_col(int wv_, unsigned char* ws, rsrc_t rs, f32x2* lds, int jb, int go) {
  const int j = wv_ * 64 + lane_id();
  f32x4 *bufA = (f32x4*)lds, *bufB = bufA + FB;
  const int xin = (int)OFF_X1T + ((jb == 160 ? 0 : jb) * S_ + j) * 16;
  const f32x2* tw = (const f32x2*)(ws + OFF_TW);
  C2 v[8];
  unsigned wf[2][8];
#pragma unroll
  for (int c = 0; c < 2; ++c) {
    const unsigned* wt = (const unsigned*)(ws + OFF_WT) + (size_t)(jb == 160 ? 320 : 2 * jb + c) * S_;
#pragma unroll
    for (int q = 0; q < 8; ++q) wf[c][q] = wt[j + 256 * q];
  }
  const f32x2 tA = tw[(j & 7) * 32], tB = tw[(j & 63) * 4], tC0 = tw[j], tC1 = tw[j + 256];
  const f32x2 tD = tw[(j & 3) * 64], tE = tw[(j & 31) * 8];
  if (go >= 0) dep_wait(wv_, ws, go, 1u);
#pragma unroll
  for (int q = 0; q < 8; ++q) {
    const f32x4 t = __builtin_bit_cast(f32x4, ldc(rs, xin + q * 4096));
    v[q] = C2{f32x2{jb == 160 ? t.y : t.x, t.z}, f32x2{(jb == 0 || jb == 160) ? 0.f : t.y, t.w}};
  }
#define CSTAMP(i)
  CSTAMP(0)
  fft8<-1>(v);
  pass_put<1, 8>(bufA, j, v);
  __syncthreads();
  CSTAMP(1)
  pass8_load<-1>(bufA, tA, j, v);
  pass_put<8, 8>(bufB, j, v);
  __syncthreads();
  pass8_load<-1>(bufB, tB, j, v);
  pass_put<64, 8>(bufA, j, v);
  __syncthreads();
  const float sc = 1.0f / ((float)S_ * (float)D_);
  {
    f32x4 t[8];
#pragma unroll
    for (int q = 0; q < 8; ++q) t[q] = bufA[lpad(j + 256 * q)];
#pragma unroll
    for (int hh = 0; hh < 2; ++hh) {
      const f32x2 w1 = hh ? tC1 : tC0, w2 = cmul(w1, w1), w3 = cmul(w2, w1);
      C2 a[4] = {c2(t[hh]), cmul(c2(t[hh + 2]), w1), cmul(c2(t[hh + 4]), w2), cmul(c2(t[hh + 6]), w3)};
      fft4<-1>(a[0], a[1], a[2], a[3]);
#pragma unroll
      for (int q = 0; q < 4; ++q) {
        const unsigned w0 = wf[0][hh + 2 * q], w1_ = wf[1][hh + 2 * q];
        a[q] = cmul(a[q], C2{f32x2{lo_f(w0), lo_f(w1_)} * sc, f32x2{hi_f(w0), hi_f(w1_)} * sc});
      }
      fft4<1>(a[0], a[1], a[2], a[3]);
      pass_put<1, 4>(bufB, j + 256 * hh, a);
    }
  }
  __syncthreads();
  pass8_load<1>(bufB, tD, j, v);
  pass_put<4, 8>(bufA, j, v);
  __syncthreads();
  pass8_load<1>(bufA, tE, j, v);
  pass_put<32, 8>(bufB, j, v);
  __syncthreads();
  CSTAMP(2)
  {
    pass8_load<1>(bufB, tC0, j, v);
#pragma unroll
    for (int q = 0; q < 8; ++q)
      __builtin_amdgcn_raw_buffer_store_b64(v2u{pk(v[q].r.x, v[q].i.x), pk(v[q].r.y, v[q].i.y)}, rs, (int)OFF_ZT + (jb * S_ + j + 256 * q) * 8, 0, AUX_SC1);
  }
  CSTAMP(3)
}
__device__ __forceinline__ void fan_signal(int wv_, unsigned char* ws, int cnt, unsigned want, int top, unsigned wtop, int fan) {
  asm volatile("s_waitcnt vmcnt(0)" ::: "memory");
  __syncthreads();
  if (wv_ == 0) {
    const int l = lane_id();
    unsigned last = 0;
    if (l == 0) {
      last = __hip_atomic_fetch_add((gu32*)(ws + cnt), 1u, __ATOMIC_RELAXED, __HIP_MEMORY_SCOPE_AGENT) + 2u == want;
      if (last && top >= 0) last = __hip_atomic_fetch_add((gu32*)(ws + top), 1u, __ATOMIC_RELAXED, __HIP_MEMORY_SCOPE_AGENT) + 2u == wtop;
    }
    if (__builtin_amdgcn_readfirstlane(last)) __hip_atomic_store((gu32*)(ws + fan + l * 128), 1u, __ATOMIC_RELAXED, __HIP_MEMORY_SCOPE_AGENT);
  }
}
__device__ __forceinline__ void col_signal(int wv_, unsigned char* ws, int set, int jb) {
  fan_signal(wv_, ws, cnt_off(set, CNT_COL, jb & 7), (jb & 7) == 0 ? 21u : 20u, cnt_off(set, CNT_COL, 8), 8u, fan_off(set, 0, 0));
}

template <bool FINAL>
__device__ __forceinline__ void job_idft(int wv_, unsigned char* ws, int colcnt, rsrc_t rs, v4u* lds, int sg0, int sgstep, int dq, u16* out, unsigned long long* dbg) {
  const int lane = lane_id(), wv = wv_, g = lane >> 4, r = lane & 15;
  const int kt = dq * 4 + wv;
  v4u a[2][NFK];
#pragma unroll
  for (int fk = 0; fk < NFK; ++fk) {
    a[0][fk] = ldp(rs, (int)OFF_INV + ((2 * kt) * NFK + fk) * 1024 + lane * 16);
    a[1][fk] = ldp(rs, (int)OFF_INV + ((2 * kt + 1) * NFK + fk) * 1024 + lane * 16);
  }
#define ISTAMP(i)
  ISTAMP(0)
  dep_wait(wv_, ws, colcnt, 1u);
  ISTAMP(1)
#pragma unroll 1
  for (int sg = sg0; sg < NSEG; sg += sgstep) {
    const int xo = (int)OFF_XR + ((sg * 2 * KT + kt) * 64 + lane) * 16;
    {
      const int jj = lane >> 3, rp = lane & 7, row = sg * 32 + (wv & 1) * 16 + 2 * rp;
      const int src = (int)OFF_ZT + (((wv >> 1) * 80 + jj) * S_ + row) * 8;
      v4u t[10];
#pragma unroll
      for (int i = 0; i < 10; ++i) t[i] = ldc(rs, src + i * 8 * S_ * 8);
      const v4u ny = ldc(rs, (int)OFF_ZT + (160 * S_ + row) * 8);
      if (wv < 2 && jj == 0) {
        t[0].x = (t[0].x & 0xFFFFu) | (ny.x << 16);
        t[0].z = (t[0].z & 0xFFFFu) | (ny.z << 16);
      }
      v2u* dst = (v2u*)(lds + ((wv & 1) * NFK + (wv >> 1) * 10) * 64 + (jj >> 1) * 16 + 2 * rp) + (jj & 1);
#pragma unroll
      for (int i = 0; i < 10; ++i) { dst[i * 128] = v2u{t[i].x, t[i].y}; dst[i * 128 + 2] = v2u{t[i].z, t[i].w}; }
    }
    const v4u x0 = ldc(rs, xo), x1 = ldc(rs, xo + KT * 1024);
    f32x4 acc[2][2] = {{{0, 0, 0, 0}, {0, 0, 0, 0}}, {{0, 0, 0, 0}, {0, 0, 0, 0}}};
    __syncthreads();
    ISTAMP(2)
#pragma unroll
    for (int k0 = 0; k0 < NFK; k0 += 5) {
      v4u b0[5], b1[5];
#pragma unroll
      for (int d = 0; d < 5; ++d) { b0[d] = lds[(k0 + d) * 64 + lane]; b1[d] = lds[(NFK + k0 + d) * 64 + lane]; }
#pragma unroll
      for (int d = 0; d < 5; ++d) {
        acc[0][0] = mfma(a[0][k0 + d], b0[d], acc[0][0]); acc[0][1] = mfma(a[0][k0 + d], b1[d], acc[0][1]);
        acc[1][0] = mfma(a[1][k0 + d], b0[d], acc[1][0]); acc[1][1] = mfma(a[1][k0 + d], b1[d], acc[1][1]);
      }
    }
#pragma unroll
    for (int rt = 0; rt < 2; ++rt) {
      const v4u x = rt ? x1 : x0;
      v4u y;
      y.x = pk(lo_f(x.x) + bfr(acc[0][rt].x), hi_f(x.x) + bfr(acc[0][rt].y));
      y.y = pk(lo_f(x.y) + bfr(acc[0][rt].z), hi_f(x.y) + bfr(acc[0][rt].w));
      y.z = pk(lo_f(x.z) + bfr(acc[1][rt].x), hi_f(x.z) + bfr(acc[1][rt].y));
      y.w = pk(lo_f(x.w) + bfr(acc[1][rt].z), hi_f(x.w) + bfr(acc[1][rt].w));
      if (FINAL) {
        u16* op = out + (size_t)(sg * 32 + rt * 16 + r) * D_ + 32 * kt + 4 * g;
        *(v2u*)op = v2u{y.x, y.y};
        *(v2u*)(op + 16) = v2u{y.z, y.w};
      } else {
        stc(rs, xo + rt * KT * 1024, y);
        stat_put(lds, wv, rt, lane, y);
      }
    }
    ISTAMP(3)
    if (!FINAL) {
      stat_store(rs, lds, wv, lane, sg, dq);
      if (sg < 12) fan_signal(wv_, ws, cnt_off(1, CNT_IDFT, sg >> 1), 10u, cnt_off(1, CNT_COL, 9), 6u, fan_off(1, 1, 0));
      else dep_signal(wv_, ws, cnt_off(1, CNT_IDFT, sg >> 1));
    }
  }
}

constexpr int JOB_LDS = 4 * NFK * 1024;
extern __shared__ __attribute__((aligned(16))) unsigned char dyn_lds[];
constexpr int FIN_LDS = 4 * FB * 8;
#define FIN_DELAY 3
__global__ void __launch_bounds__(256, 2) k_fin(unsigned char* ws, u16* out) {
  const int wv_ = __builtin_amdgcn_readfirstlane(threadIdx.x >> 6), b = blockIdx.x;
  const rsrc_t rs = ws_rsrc(ws);
  PH(14)
  if (b < NCOL) {
    job_col(wv_, ws, rs, (f32x2*)dyn_lds, b, -1);
    col_signal(wv_, ws, 0, b);
    PH(13)
  } else {
    for (int i = 0; i < FIN_DELAY; ++i) __builtin_amdgcn_s_sleep(32);
    job_idft<true>(wv_, ws, fan_off(0, 0, b & 63), rs, (v4u*)dyn_lds, (b - NCOL) / 5, NSEG, (b - NCOL) % 5, out, nullptr);
    PH(15)
  }
}

constexpr unsigned SPIN_CAP = 1u << 16;
typedef unsigned v8u __attribute__((ext_vector_type(8)));

#define VM_LD128(dst, off, so, IMM) asm volatile("buffer_load_dwordx4 %0, %1, %2, %3 offen offset:" #IMM " sc1" : "=v"(dst) : "v"(off), "s"(rs), "s"(so) : "memory")
#define VM_LD32(dst, off, so) asm volatile("buffer_load_dword %0, %1, %2, %3 offen sc1" : "=v"(dst) : "v"(off), "s"(rs), "s"(so) : "memory")
#define VM_ST32(val, off, so) asm volatile("buffer_store_dword %0, %1, %2, %3 offen sc1" ::"v"(val), "v"(off), "s"(rs), "s"(so) : "memory")
#define VM_ST128(val, off, so) asm volatile("buffer_store_dwordx4 %0, %1, %2, %3 offen sc1\n\ts_nop 1" ::"v"(val), "v"(off), "s"(rs), "s"(so) : "memory")
#define VM_WAIT(N) asm volatile("s_waitcnt vmcnt(" #N ")" ::: "memory")
#define VM_WAIT1(N, a) asm volatile("s_waitcnt vmcnt(" #N ")" : "+v"(a)::"memory")
__device__ __forceinline__ v4u make_rs(const void* p, unsigned bytes) {
  const unsigned long long a = (unsigned long long)p;
  v4u r;
  r.x = (unsigned)__builtin_amdgcn_readfirstlane((unsigned)a);
  r.y = (unsigned)__builtin_amdgcn_readfirstlane((unsigned)(a >> 32) & 0xFFFFu);
  r.z = bytes;
  r.w = 0x00020000u;
  return r;
}
__device__ __forceinline__ unsigned rtc() { return (unsigned)__builtin_amdgcn_s_memrealtime(); }
struct Clk0 { unsigned r, c; };
__device__ __forceinline__ Clk0 clk_sample() {
  unsigned long long r, c;
  asm volatile("s_memrealtime %0\n\ts_memtime %1\n\ts_waitcnt lgkmcnt(0)" : "=s"(r), "=s"(c)::"memory");
  return Clk0{(unsigned)r, (unsigned)c};
}
__device__ __forceinline__ v4u clk_init(Clk0 a) {
  const Clk0 b = clk_sample();
  unsigned k = 1400u;
  if (b.r - a.r >= 100u) k = (unsigned)((float)(b.r - a.r) * 32768.f / (float)(b.c - a.c));
  k = (unsigned)__builtin_amdgcn_readfirstlane((int)min(max(k, 1200u), 3000u));
  asm volatile("" : "+s"(k));
  return v4u{b.r * 8u + 16u, b.c, k, 0u};
}
#define LDSBAR() asm volatile("s_waitcnt lgkmcnt(0)\n\ts_barrier" ::: "memory")
#define MEMPIN() asm volatile("" ::: "memory")
#define LDS_V4(a) (*(__attribute__((address_space(3))) v4u*)(uintptr_t)(a))
#define LDS_U32(a) (*(__attribute__((address_space(3))) unsigned*)(uintptr_t)(a))
#define LDS_V2(a) (*(__attribute__((address_space(3))) v2u*)(uintptr_t)(a))

__device__ __forceinline__ int wave_imax(int v) {
  asm volatile("s_nop 1\n\tv_max_i32_dpp %0, %0, %0 quad_perm:[1,0,3,2] row_mask:0xf bank_mask:0xf\n\t"
               "s_nop 1\n\tv_max_i32_dpp %0, %0, %0 quad_perm:[2,3,0,1] row_mask:0xf bank_mask:0xf\n\t"
               "s_nop 1\n\tv_max_i32_dpp %0, %0, %0 row_half_mirror row_mask:0xf bank_mask:0xf\n\t"
               "s_nop 1\n\tv_max_i32_dpp %0, %0, %0 row_mirror row_mask:0xf bank_mask:0xf\n\ts_nop 0"
               : "+v"(v));
  const int a = __builtin_amdgcn_readlane(v, 0), b = __builtin_amdgcn_readlane(v, 16), c = __builtin_amdgcn_readlane(v, 32),
            d = __builtin_amdgcn_readlane(v, 48);
  return max(max(a, b), max(c, d));
}

constexpr int OOB = 0x7FFFFFF0;
__device__ __forceinline__ int ladder(int x, int i) { return copy_off(((x + (i >> 1)) & 3) | ((i & 1) << 2)); }

struct RecTune { int m1, m2, dl1, rob, c2, vis, dlt, du; };

#define STAT(r)
__device__ __forceinline__ unsigned max4(v4u v) { return max(max(v.x, v.y), max(v.z, v.w)); }
template <int NAP = 6>
__device__ __forceinline__ unsigned flag_finish(const v4u rs, int off, unsigned cv) {
  for (unsigned spins = 0; __any(cv == 0xFFFFFFFFu) && spins < SPIN_CAP; ++spins) {
    __builtin_amdgcn_s_sleep(NAP);
    v4u t;
    VM_LD128(t, off, 0, 0);
    VM_WAIT1(0, t);
    cv = max4(t);
  }
  return cv;
}
struct Pool {
  unsigned long long sel, s3, ysel; int base;
  __device__ __forceinline__ int near(int s) const { return (int)((s3 >> s) & 1ull) * 16384; }
  __device__ __forceinline__ int region(int s) const { return base + s * REG + (int)((sel >> s) & 1ull) * 8192; }
};
__device__ __forceinline__ void pool_load(const unsigned char* ws, v4i (&e)[18]) {
  const int s = lane_id();
#pragma unroll
  for (int i = 0; i < 18; ++i) e[i] = ((const v4i*)(ws + OFF_SEL))[i < 16 ? (s & 60) * 4 + i : 240 + (s * 43 >> 7) * 2 + i];
}
__device__ __forceinline__ Pool pool_init(unsigned char* ws, v4i (&e)[18]) {
  v4i a = {0, 0, 0, 0};
#pragma unroll
  for (int i = 0; i < 18; ++i) asm volatile("" : "+v"(e[i]));
#pragma unroll
  for (int i = 0; i < 16; ++i) { a.x += e[i].x; a.y += (i & 4) ? -e[i].y : e[i].y; a.z += (i & 4) ? -e[i].z : e[i].z; }
  const v4i y = e[16] + e[17];
  int yx = 2 * (y.x + y.y + y.z + y.w), b = a.x > 0 ? a.z : a.y;
  const int s = lane_id(), fx = (s & 4) ? -1 : 1, fy = ((s * 43 >> 7) & 1) ? -1 : 1;
  int g = a.x * fx + yx * fy, m = abs(a.x) + abs(yx), bs = (a.x * fx > 0 ? b : 0), bt = b - bs;
#pragma unroll
  for (int k = 1; k < 64; k <<= 1) { g += __shfl_xor(g, k); m += __shfl_xor(m, k); bs += __shfl_xor(bs, k); bt += __shfl_xor(bt, k); }
  if (abs(g) * 8 > m * 7) {
    const bool odd = (a.x * fx > 0) != (g > 0);
    if (!odd || abs(a.x) < 2000) { a.x = g * fx; b = g > 0 ? bs : bt; }
    if (abs(yx) < 2000) yx = g * fy;
  }
  Pool p;
  p.sel = __ballot(a.x > 0);
  p.s3 = __ballot(b > 0) ^ 0xAAAAAAAAAAAAAAAAull;
  p.ysel = __ballot(yx > 0);
  p.base = __builtin_amdgcn_readfirstlane(pool_base(ws));
  return p;
}
constexpr int L_QT = 10240, L_KT = 12288, L_CL = 14336, L_KQ = 16384;
constexpr int L_T = 14336;
constexpr int L_M2 = L_KQ + 65536;
constexpr int REC_ST = L_M2 + 12288;
constexpr int REC_LDS = REC_ST + 4096 + 64;

#define RSTAMP(i)
#define YSTAMP(n)
#define RST_CLOB
#define P1TRACE
#define PVTRACE
#define FINTRACE
#define P1_ASM \
   "s_mov_b32 s64, -1\n\t" "s_mov_b32 s76, 0\n\t" "s_mov_b32 s77, 0\n\t" "s_mov_b32 s71, 63\n\t" "s_mov_b32 s47, 1300\n\t" "s_mov_b32 s49, 0\n\t" \
   "s_mov_b32 s51, 0\n\t" "s_mov_b32 s52, 0\n\t" "s_mov_b32 s70, 0\n\t" "s_mov_b32 s69, 0\n\t" "v_cmp_gt_i32 s[20:21], 0, v126\n\t" \
   "v_cmp_gt_i32 s[22:23], 1, v126\n\t" "v_cmp_gt_i32 s[24:25], 2, v126\n\t" "v_cmp_gt_i32 s[26:27], 3, v126\n\t" "v_cmp_gt_i32 s[28:29], 16, v126\n\t" \
   "v_cmp_gt_i32 s[30:31], 17, v126\n\t" "v_cmp_gt_i32 s[32:33], 18, v126\n\t" "v_cmp_gt_i32 s[34:35], 19, v126\n\t" "s_waitcnt vmcnt(0)\n\t" \
   "s_lshr_b64 s[72:73], %[sel], s71\n\t" "s_lshl_b32 s65, s71, 19\n\t" "s_and_b32 s72, s72, 1\n\t" "s_add_u32 s65, s65, %[pool]\n\t" \
   "s_lshl_b32 s72, s72, 13\n\t" "s_add_u32 s65, s65, s72\n\t" "s_lshr_b64 s[72:73], %[s3], s71\n\t" "s_and_b32 s54, s72, 1\n\t" \
   "s_lshl_b32 s54, s54, 14\n\t" "s_memtime s[62:63]\n\t" "s_memrealtime s[60:61]\n\t" "40:\n\t" "s_cmp_eq_u32 s64, 0\n\t" "s_cbranch_scc1 95f\n\t" \
   "96:\n\t" "s_add_i32 s72, s64, 2\n\t" "s_min_i32 s72, s72, 63\n\t" "s_lshl_b32 s59, s72, 12\n\t" "s_lshl_b32 s79, s72, 13\n\t" \
   "buffer_load_dword v108, v139, %[rs], s65 offen sc1\n\t" "s_cmp_gt_u32 %[wv], 1\n\t" "s_waitcnt vmcnt(4)\n\t" "s_cbranch_scc1 44f\n\t" \
   "buffer_load_dword v15, v140, %[rs], s59 offen sc1\n\t" "ds_write_b128 v127, v[16:19] offset:0\n\t" "ds_write_b128 v127, v[20:23] offset:1024\n\t" \
   "ds_write_b128 v127, v[24:27] offset:2048\n\t" "ds_write_b128 v127, v[28:31] offset:3072\n\t" "s_waitcnt lgkmcnt(0)\n\t" \
   "s_sub_u32 s74, s62, s57\n\t" "s_mul_i32 s74, s74, s58\n\t" "s_lshr_b32 s74, s74, 12\n\t" "s_add_u32 s74, s74, s56\n\t" "s_lshl_b32 s60, s60, 3\n\t" \
   "s_sub_u32 s75, s74, s60\n\t" "s_cmp_lt_u32 s75, 32\n\t" "s_cbranch_scc1 38f\n\t" "s_cmp_lt_i32 s75, 0\n\t" "s_cselect_b32 s61, 0, 31\n\t" \
   "s_add_u32 s74, s60, s61\n\t" "s_sub_i32 s75, s61, s75\n\t" "s_add_i32 s75, s75, 1\n\t" "s_ashr_i32 s75, s75, 1\n\t" "s_min_i32 s75, s75, 8\n\t" \
   "s_max_i32 s75, s75, -8\n\t" "s_mov_b32 s56, s74\n\t" "s_mov_b32 s57, s62\n\t" "s_add_u32 s58, s58, s75\n\t" "38:\n\t" "s_min_u32 s72, s70, 1\n\t" \
   "s_and_b32 s71, s74, -4\n\t" "s_or_b32 s71, s71, s72\n\t" "v_mov_b32 v111, s71\n\t" "buffer_store_dword v111, v131, %[rs], s65 offen sc1\n\t" \
   P1TRACE "s_barrier\n\t" "ds_read_b128 v[160:163], v122 offset:0\n\t" "ds_read_b128 v[164:167], v122 offset:1024\n\t" \
   "ds_read_b128 v[168:171], v122 offset:2048\n\t" "ds_read_b128 v[172:175], v122 offset:3072\n\t" "ds_read_b128 v[176:179], v122 offset:4096\n\t" \
   "ds_read_b128 v[180:183], v122 offset:5120\n\t" "ds_read_b128 v[184:187], v122 offset:6144\n\t" "ds_read_b128 v[188:191], v122 offset:7168\n\t" \
   "s_cmp_lt_i32 s64, 1\n\t" "s_cselect_b32 s72, 0x7ffffff0, 0\n\t" "s_max_i32 s71, s64, 1\n\t" "v_or_b32 v109, s72, v143\n\t" \
   "s_lshl_b32 s71, s71, 12\n\t" "v_mov_b32 v110, 1\n\t" "s_sub_u32 s71, s71, 4096\n\t" "buffer_store_dword v110, v109, %[rs], s71 offen sc1\n\t" \
   "s_add_u32 s48, s69, s47\n\t" "s_cmp_eq_u32 s70, 0\n\t" "s_cselect_b32 s48, s48, -1\n\t" "s_mov_b32 s66, s65\n\t" "s_add_i32 s64, s64, 1\n\t" \
   "s_and_b32 s71, s64, 63\n\t" "s_lshr_b64 s[72:73], %[sel], s71\n\t" "s_lshl_b32 s65, s71, 19\n\t" "s_and_b32 s72, s72, 1\n\t" \
   "s_add_u32 s65, s65, %[pool]\n\t" "s_lshl_b32 s72, s72, 13\n\t" "s_add_u32 s65, s65, s72\n\t" "s_xor_b32 s67, s54, %[lad0]\n\t" \
   "s_xor_b32 s68, s67, 0x4000\n\t" "s_add_u32 s67, s67, s66\n\t" "s_add_u32 s68, s68, s66\n\t" "s_lshr_b64 s[72:73], %[s3], s71\n\t" \
   "s_and_b32 s54, s72, 1\n\t" "s_lshl_b32 s54, s54, 14\n\t" "s_cmp_eq_u32 s64, 0\n\t" "s_cselect_b32 s70, 1, 0\n\t" "s_and_b32 s55, %[wv], 1\n\t" \
   "s_lshl_b32 s55, s55, 14\n\t" "s_xor_b32 s55, s55, s54\n\t" "s_xor_b32 s50, s55, 0x4000\n\t" "s_add_u32 s55, s55, s65\n\t" \
   "s_add_u32 s50, s50, s65\n\t" "v_mov_b32 v48, 0\n\t" "v_mov_b32 v49, 0\n\t" "v_mov_b32 v50, 0\n\t" "v_mov_b32 v51, 0\n\t" "v_mov_b32 v52, 0\n\t" \
   "v_mov_b32 v53, 0\n\t" "v_mov_b32 v54, 0\n\t" "v_mov_b32 v55, 0\n\t" "v_mov_b32 v56, 0\n\t" "v_mov_b32 v57, 0\n\t" "v_mov_b32 v58, 0\n\t" \
   "v_mov_b32 v59, 0\n\t" "v_mov_b32 v60, 0\n\t" "v_mov_b32 v61, 0\n\t" "v_mov_b32 v62, 0\n\t" "v_mov_b32 v63, 0\n\t" "s_waitcnt vmcnt(2)\n\t" \
   "v_cmp_eq_u32 vcc, -1, v15\n\t" "s_nop 0\n\t" "s_cbranch_vccnz 141f\n\t" "41:\n\t" "s_branch 48f\n\t" "44:\n\t" \
   "v_mfma_f32_16x16x32_bf16 v[32:35], v[160:163], v[16:19], 0\n\t" "s_max_i32 s51, s64, 0\n\t" "s_lshl_b32 s52, s51, 16\n\t" \
   "s_lshl_b32 s51, s51, 14\n\t" "v_mfma_f32_16x16x32_bf16 v[36:39], v[176:179], v[16:19], 0\n\t" "s_cmp_lt_i32 s64, 0\n\t" \
   "s_cselect_b32 s49, 0x7ffffff0, 0\n\t" "s_add_i32 s72, s64, 1\n\t" "v_mfma_f32_16x16x32_bf16 v[32:35], v[164:167], v[20:23], v[32:35]\n\t" \
   "s_min_i32 s72, s72, 63\n\t" "s_lshl_b32 s79, s72, 13\n\t" "v_mfma_f32_16x16x32_bf16 v[36:39], v[180:183], v[20:23], v[36:39]\n\t" \
   "v_mfma_f32_16x16x32_bf16 v[32:35], v[168:171], v[24:27], v[32:35]\n\t" "s_waitcnt lgkmcnt(0)\n\t" \
   "v_mfma_f32_16x16x32_bf16 v[36:39], v[184:187], v[24:27], v[36:39]\n\t" "s_sub_u32 s74, s62, s57\n\t" "s_mul_i32 s74, s74, s58\n\t" \
   "s_lshr_b32 s74, s74, 12\n\t" "v_mfma_f32_16x16x32_bf16 v[32:35], v[172:175], v[28:31], v[32:35]\n\t" "s_add_u32 s74, s74, s56\n\t" \
   "s_lshl_b32 s60, s60, 3\n\t" "s_sub_u32 s75, s74, s60\n\t" "v_mfma_f32_16x16x32_bf16 v[36:39], v[188:191], v[28:31], v[36:39]\n\t" \
   "s_cmp_lt_u32 s75, 32\n\t" "s_cbranch_scc1 39f\n\t" "s_cmp_lt_i32 s75, 0\n\t" "s_cselect_b32 s61, 0, 31\n\t" "s_add_u32 s74, s60, s61\n\t" \
   "s_sub_i32 s75, s61, s75\n\t" "s_add_i32 s75, s75, 1\n\t" "s_ashr_i32 s75, s75, 1\n\t" "s_min_i32 s75, s75, 8\n\t" "s_max_i32 s75, s75, -8\n\t" \
   "s_mov_b32 s56, s74\n\t" "s_mov_b32 s57, s62\n\t" "s_add_u32 s58, s58, s75\n\t" "39:\n\t" "s_barrier\n\t" \
   "v_mfma_f32_16x16x32_bf16 v[40:43], v[144:147], v[16:19], 0\n\t" "s_min_u32 s72, s70, 1\n\t" "s_and_b32 s71, s74, -4\n\t" \
   "s_or_b32 s71, s71, s72\n\t" "v_mov_b32 v111, s71\n\t" "v_mfma_f32_16x16x32_bf16 v[40:43], v[148:151], v[20:23], v[40:43]\n\t" \
   "buffer_store_dword v111, v131, %[rs], s65 offen sc1\n\t" P1TRACE "v_mov_b32 v48, 0\n\t" "v_mov_b32 v49, 0\n\t" \
   "v_mfma_f32_16x16x32_bf16 v[40:43], v[152:155], v[24:27], v[40:43]\n\t" "v_mov_b32 v50, 0\n\t" "v_mov_b32 v51, 0\n\t" "v_mov_b32 v52, 0\n\t" \
   "v_mov_b32 v53, 0\n\t" "v_mfma_f32_16x16x32_bf16 v[40:43], v[156:159], v[28:31], v[40:43]\n\t" "v_mov_b32 v54, 0\n\t" "v_mov_b32 v55, 0\n\t" \
   "v_cndmask_b32 v32, v32, v125, s[20:21]\n\t" "v_cndmask_b32 v33, v33, v125, s[22:23]\n\t" "v_cndmask_b32 v34, v34, v125, s[24:25]\n\t" \
   "v_cndmask_b32 v35, v35, v125, s[26:27]\n\t" "v_cndmask_b32 v36, v36, v125, s[28:29]\n\t" "v_cndmask_b32 v37, v37, v125, s[30:31]\n\t" \
   "v_cndmask_b32 v38, v38, v125, s[32:33]\n\t" "v_cndmask_b32 v39, v39, v125, s[34:35]\n\t" "v_exp_f32 v64, v32\n\t" "s_add_u32 s48, s69, s47\n\t" \
   "s_cmp_eq_u32 s70, 0\n\t" "s_cselect_b32 s48, s48, -1\n\t" "v_exp_f32 v65, v33\n\t" "s_mov_b32 s66, s65\n\t" "s_add_i32 s64, s64, 1\n\t" \
   "s_and_b32 s71, s64, 63\n\t" "v_exp_f32 v66, v34\n\t" "s_lshr_b64 s[72:73], %[sel], s71\n\t" "s_lshl_b32 s65, s71, 19\n\t" \
   "s_and_b32 s72, s72, 1\n\t" "v_exp_f32 v67, v35\n\t" "s_add_u32 s65, s65, %[pool]\n\t" "s_lshl_b32 s72, s72, 13\n\t" "s_add_u32 s65, s65, s72\n\t" \
   "v_exp_f32 v68, v36\n\t" "s_xor_b32 s67, s54, %[lad0]\n\t" "s_xor_b32 s68, s67, 0x4000\n\t" "s_add_u32 s67, s67, s66\n\t" "v_exp_f32 v69, v37\n\t" \
   "s_add_u32 s68, s68, s66\n\t" "s_lshr_b64 s[72:73], %[s3], s71\n\t" "s_and_b32 s54, s72, 1\n\t" "v_exp_f32 v70, v38\n\t" \
   "s_lshl_b32 s54, s54, 14\n\t" "s_cmp_eq_u32 s64, 0\n\t" "s_cselect_b32 s70, 1, 0\n\t" "v_exp_f32 v71, v39\n\t" "s_and_b32 s55, %[wv], 1\n\t" \
   "s_lshl_b32 s55, s55, 14\n\t" "s_xor_b32 s55, s55, s54\n\t" "v_exp_f32 v72, v40\n\t" "s_xor_b32 s50, s55, 0x4000\n\t" "s_add_u32 s55, s55, s65\n\t" \
   "s_add_u32 s50, s50, s65\n\t" "v_exp_f32 v73, v41\n\t" "v_mov_b32 v56, 0\n\t" "v_mov_b32 v57, 0\n\t" "v_mov_b32 v58, 0\n\t" "v_exp_f32 v74, v42\n\t" \
   "v_mov_b32 v59, 0\n\t" "v_mov_b32 v60, 0\n\t" "v_mov_b32 v61, 0\n\t" "v_exp_f32 v75, v43\n\t" "v_mov_b32 v62, 0\n\t" "v_mov_b32 v63, 0\n\t" \
   "v_add_f32 v80, v64, v65\n\t" "v_add_f32 v81, v66, v67\n\t" "v_add_f32 v82, v68, v69\n\t" "v_add_f32 v83, v70, v71\n\t" \
   "v_add_f32 v84, v72, v73\n\t" "v_add_f32 v85, v74, v75\n\t" "v_add_f32 v80, v80, v81\n\t" "v_add_f32 v82, v82, v83\n\t" \
   "v_add_f32 v84, v84, v85\n\t" "v_add_f32 v80, v80, v82\n\t" "v_fma_f32 v82, v80, 2.0, v84\n\t" "v_fma_f32 v80, v80, 2.0, v84\n\t" \
   "ds_read_b128 v[160:163], v122 offset:0\n\t" "ds_read_b128 v[164:167], v122 offset:1024\n\t" "v_permlane16_swap_b32 v80, v82\n\t" \
   "v_add_f32 v81, v80, v82\n\t" "v_add_f32 v80, v80, v82\n\t" "ds_read_b128 v[168:171], v122 offset:2048\n\t" \
   "ds_read_b128 v[172:175], v122 offset:3072\n\t" "v_permlane32_swap_b32 v80, v81\n\t" "v_add_f32 v80, v80, v81\n\t" "v_rcp_f32 v90, v80\n\t" \
   "v_add_u32 v81, 0xe0800000, v80\n\t" "v_add_f32 v90, v90, v90\n\t" "v_cmp_lt_u32 vcc, 0x40000000, v81\n\t" \
   "ds_read_b128 v[176:179], v122 offset:4096\n\t" "ds_read_b128 v[180:183], v122 offset:5120\n\t" "ds_read_b128 v[184:187], v122 offset:6144\n\t" \
   "ds_read_b128 v[188:191], v122 offset:7168\n\t" "s_cbranch_vccnz 74f\n\t" "75:\n\t" "v_pk_mul_f32 v[64:65], v[64:65], v[90:91] op_sel_hi:[1,0]\n\t" \
   "v_pk_mul_f32 v[66:67], v[66:67], v[90:91] op_sel_hi:[1,0]\n\t" "v_pk_mul_f32 v[68:69], v[68:69], v[90:91] op_sel_hi:[1,0]\n\t" \
   "v_pk_mul_f32 v[70:71], v[70:71], v[90:91] op_sel_hi:[1,0]\n\t" "v_pk_mul_f32 v[72:73], v[72:73], v[90:91] op_sel_hi:[1,0]\n\t" \
   "v_pk_mul_f32 v[74:75], v[74:75], v[90:91] op_sel_hi:[1,0]\n\t" "v_cvt_pk_bf16_f32 v44, v64, v65\n\t" "v_cvt_pk_bf16_f32 v46, v66, v67\n\t" \
   "v_cvt_pk_bf16_f32 v45, v68, v69\n\t" "v_cvt_pk_bf16_f32 v47, v70, v71\n\t" "v_cvt_pk_bf16_f32 v32, v72, v73\n\t" \
   "v_cvt_pk_bf16_f32 v33, v74, v75\n\t" "s_waitcnt vmcnt(1)\n\t" "48:\n\t" "v_readlane_b32 s69, v108, 0\n\t" "v_readlane_b32 s71, v108, 1\n\t" \
   "s_cmp_eq_u32 s64, 64\n\t" "s_cbranch_scc1 90f\n\t" "s_cmp_eq_u32 s69, -1\n\t" "s_cbranch_scc1 63f\n\t" "s_cmp_eq_u32 s71, -1\n\t" \
   "s_cselect_b32 s47, s47, s71\n\t" "50:\n\t" "s_add_u32 s73, s69, %[m1]\n\t" RSTAMP(5) "51:\n\t" "s_memrealtime s[60:61]\n\t" \
   "s_memtime s[62:63]\n\t" "s_waitcnt lgkmcnt(0)\n\t" "s_sub_u32 s74, s62, s57\n\t" "s_mul_i32 s74, s74, s58\n\t" "s_lshr_b32 s74, s74, 12\n\t" \
   "s_add_u32 s74, s74, s56\n\t" "s_lshl_b32 s60, s60, 3\n\t" "s_sub_u32 s75, s74, s60\n\t" "s_cmp_lt_u32 s75, 32\n\t" "s_cbranch_scc1 52f\n\t" \
   "s_cmp_lt_i32 s75, 0\n\t" "s_cselect_b32 s61, 0, 31\n\t" "s_add_u32 s74, s60, s61\n\t" "s_sub_i32 s75, s61, s75\n\t" "s_add_i32 s75, s75, 1\n\t" \
   "s_ashr_i32 s75, s75, 1\n\t" "s_min_i32 s75, s75, 8\n\t" "s_max_i32 s75, s75, -8\n\t" "s_mov_b32 s56, s74\n\t" "s_mov_b32 s57, s62\n\t" \
   "s_add_u32 s58, s58, s75\n\t" "52:\n\t" "s_sub_i32 s74, s73, s74\n\t" "s_cmp_le_i32 s74, 150\n\t" "s_cbranch_scc1 53f\n\t" \
   "s_cmp_lt_i32 s74, 260\n\t" "s_cbranch_scc1 51b\n\t" "s_sleep 2\n\t" "s_cmp_lt_i32 s74, 32000\n\t" "s_cbranch_scc1 51b\n\t" "s_mov_b32 s74, 0\n\t" \
   "53:\n\t" "s_sub_i32 s74, s74, 56\n\t" "s_max_i32 s74, s74, 0\n\t" "s_mul_i32 s74, s74, 23\n\t" "s_lshr_b32 s74, s74, 5\n\t" \
   "s_and_b32 s74, s74, -4\n\t" "s_getpc_b64 s[60:61]\n\t" "54:\n\t" "s_sub_u32 s74, 55f-54b, s74\n\t" "s_add_u32 s60, s60, s74\n\t" \
   "s_addc_u32 s61, s61, 0\n\t" "s_setpc_b64 s[60:61]\n\t" "s_nop 3\n\t" "s_nop 3\n\t" "s_nop 3\n\t" "s_nop 3\n\t" "s_nop 3\n\t" "s_nop 3\n\t" \
   "s_nop 3\n\t" "s_nop 3\n\t" "s_nop 3\n\t" "s_nop 3\n\t" "s_nop 3\n\t" "s_nop 3\n\t" "s_nop 3\n\t" "s_nop 3\n\t" "s_nop 3\n\t" "s_nop 3\n\t" \
   "s_nop 3\n\t" "s_nop 3\n\t" "s_nop 3\n\t" "s_nop 3\n\t" "s_nop 3\n\t" "s_nop 3\n\t" "s_nop 3\n\t" "s_nop 3\n\t" "55:\n\t" "20:\n\t" RSTAMP(4) \
   "buffer_load_dwordx4 v[64:67], v128, %[rs], s67 offen sc1\n\t" "buffer_load_dwordx4 v[68:71], v129, %[rs], s67 offen sc1\n\t" \
   "buffer_load_dwordx4 v[72:75], v130, %[rs], s67 offen sc1\n\t" "s_nop 7\n\t" "s_nop 7\n\t" "s_nop 7\n\t" "s_cmp_lg_u32 s70, 0\n\t" \
   "s_cbranch_scc1 27f\n\t" "buffer_load_dwordx4 v[16:19], v138, %[rs], s79 offen offset:0 sc1\n\t" \
   "buffer_load_dwordx4 v[20:23], v138, %[rs], s79 offen offset:1024 sc1\n\t" \
   "buffer_load_dwordx4 v[24:27], v138, %[rs], s79 offen offset:2048 sc1\n\t" \
   "buffer_load_dwordx4 v[28:31], v138, %[rs], s79 offen offset:3072 sc1\n\t" "v_or_b32 v108, s49, v141\n\t" "v_or_b32 v109, s49, v142\n\t" \
   "buffer_store_dwordx4 v[44:47], v108, %[rs], s52 offen sc1\n\t" "buffer_store_dwordx2 v[32:33], v109, %[rs], s51 offen sc1\n\t" "28:\n\t" RSTAMP(3) \
   "s_waitcnt vmcnt(8)\n\t" "ds_write_b128 v112, v[64:67]\n\t" "v_smfmac_f32_16x16x64_bf16 v[48:51], v[64:67], a[0:7], v134\n\t" \
   "s_waitcnt vmcnt(7)\n\t" "ds_write_b128 v113, v[68:71]\n\t" "s_waitcnt vmcnt(6)\n\t" "ds_write_b128 v114, v[72:75]\n\t" RSTAMP(7) "2:\n\t" \
   "v_smfmac_f32_16x16x64_bf16 v[52:55], v[64:67], a[64:71], v134\n\t" "v_max3_u32 v110, v64, v66, v68\n\t" "v_max3_u32 v111, v70, v72, v74\n\t" \
   "v_smfmac_f32_16x16x64_bf16 v[48:51], v[68:71], a[8:15], v134\n\t" "v_max_u32 v110, v110, v111\n\t" "v_cmp_eq_u32 vcc, -1, v110\n\t" \
   "v_smfmac_f32_16x16x64_bf16 v[52:55], v[68:71], a[72:79], v134\n\t" "s_cbranch_vccnz 8f\n\t" \
   "v_smfmac_f32_16x16x64_bf16 v[48:51], v[72:75], a[16:23], v134\n\t" "s_waitcnt lgkmcnt(0)\n\t" "s_barrier\n\t" RSTAMP(0) \
   "ds_read_b128 v[76:79], v115\n\t" "ds_read_b128 v[80:83], v116\n\t" "v_smfmac_f32_16x16x64_bf16 v[52:55], v[72:75], a[80:87], v134\n\t" \
   "ds_read_b128 v[84:87], v117\n\t" "ds_read_b128 v[88:91], v118\n\t" "v_smfmac_f32_16x16x64_bf16 v[56:59], v[64:67], a[128:135], v134\n\t" \
   "ds_read_b128 v[92:95], v119\n\t" "ds_read_b128 v[96:99], v120\n\t" "v_smfmac_f32_16x16x64_bf16 v[60:63], v[64:67], a[192:199], v134\n\t" \
   "ds_read_b128 v[100:103], v121\n\t" "v_smfmac_f32_16x16x64_bf16 v[56:59], v[68:71], a[136:143], v134\n\t" \
   "v_smfmac_f32_16x16x64_bf16 v[60:63], v[68:71], a[200:207], v134\n\t" "s_waitcnt lgkmcnt(6)\n\t" \
   "v_smfmac_f32_16x16x64_bf16 v[48:51], v[76:79], a[24:31], v134\n\t" "v_smfmac_f32_16x16x64_bf16 v[52:55], v[76:79], a[88:95], v134\n\t" \
   "s_waitcnt lgkmcnt(5)\n\t" "v_smfmac_f32_16x16x64_bf16 v[48:51], v[80:83], a[32:39], v134\n\t" \
   "v_smfmac_f32_16x16x64_bf16 v[52:55], v[80:83], a[96:103], v134\n\t" "s_waitcnt lgkmcnt(4)\n\t" \
   "v_smfmac_f32_16x16x64_bf16 v[48:51], v[84:87], a[40:47], v134\n\t" "v_smfmac_f32_16x16x64_bf16 v[52:55], v[84:87], a[104:111], v134\n\t" \
   "s_waitcnt lgkmcnt(3)\n\t" "v_smfmac_f32_16x16x64_bf16 v[48:51], v[88:91], a[48:55], v134\n\t" \
   "v_smfmac_f32_16x16x64_bf16 v[52:55], v[88:91], a[112:119], v134\n\t" "s_waitcnt lgkmcnt(2)\n\t" \
   "v_smfmac_f32_16x16x64_bf16 v[48:51], v[92:95], a[56:63], v134\n\t" "v_smfmac_f32_16x16x64_bf16 v[52:55], v[92:95], a[120:127], v134\n\t" \
   "s_waitcnt lgkmcnt(0)\n\t" "v_smfmac_f32_16x16x64_bf16 v[48:51], v[96:99], v[192:199], v134\n\t" \
   "v_smfmac_f32_16x16x64_bf16 v[48:51], v[100:103], v[200:207], v134\n\t" "v_smfmac_f32_16x16x64_bf16 v[52:55], v[96:99], v[208:215], v134\n\t" \
   "v_smfmac_f32_16x16x64_bf16 v[52:55], v[100:103], v[216:223], v134\n\t" "v_smfmac_f32_16x16x64_bf16 v[56:59], v[72:75], a[144:151], v134\n\t" \
   "v_add_f32 v48, v48, v49\n\t" "v_add_f32 v49, v50, v51\n\t" "v_smfmac_f32_16x16x64_bf16 v[60:63], v[72:75], a[208:215], v134\n\t" \
   "v_mul_f32 v48, 0x3e0293ee, v48\n\t" "v_mul_f32 v49, 0x3e0293ee, v49\n\t" "v_smfmac_f32_16x16x64_bf16 v[56:59], v[76:79], a[152:159], v134\n\t" \
   "v_add_f32 v52, v52, v53\n\t" "v_add_f32 v53, v54, v55\n\t" "v_smfmac_f32_16x16x64_bf16 v[56:59], v[80:83], a[160:167], v134\n\t" \
   "v_mul_f32 v52, 0x3e0293ee, v52\n\t" "v_mul_f32 v53, 0x3e0293ee, v53\n\t" "v_smfmac_f32_16x16x64_bf16 v[56:59], v[84:87], a[168:175], v134\n\t" \
   "v_cvt_pk_bf16_f32 v48, v48, v49\n\t" "ds_write_b32 v135, v48 offset:10240\n\t" \
   "v_smfmac_f32_16x16x64_bf16 v[56:59], v[88:91], a[176:183], v134\n\t" "v_cvt_pk_bf16_f32 v52, v52, v53\n\t" \
   "ds_write_b32 v135, v52 offset:10496\n\t" "v_smfmac_f32_16x16x64_bf16 v[56:59], v[92:95], a[184:191], v134\n\t" \
   "v_smfmac_f32_16x16x64_bf16 v[56:59], v[96:99], v[224:231], v134\n\t" "v_smfmac_f32_16x16x64_bf16 v[56:59], v[100:103], v[232:239], v134\n\t" \
   "s_waitcnt lgkmcnt(0)\n\t" "s_barrier\n\t" "ds_read_b64_tr_b16 v[64:65], v136 offset:10240\n\t" "ds_read_b64_tr_b16 v[66:67], v136 offset:10496\n\t" \
   "v_smfmac_f32_16x16x64_bf16 v[60:63], v[76:79], a[216:223], v134\n\t" "ds_read_b64_tr_b16 v[68:69], v136 offset:10752\n\t" \
   "v_smfmac_f32_16x16x64_bf16 v[60:63], v[80:83], a[224:231], v134\n\t" "ds_read_b64_tr_b16 v[70:71], v136 offset:11008\n\t" \
   "v_smfmac_f32_16x16x64_bf16 v[60:63], v[84:87], a[232:239], v134\n\t" "ds_read_b64_tr_b16 v[72:73], v136 offset:11264\n\t" \
   "v_smfmac_f32_16x16x64_bf16 v[60:63], v[88:91], a[240:247], v134\n\t" "ds_read_b64_tr_b16 v[74:75], v136 offset:11520\n\t" \
   "v_add_f32 v56, v56, v57\n\t" "v_add_f32 v57, v58, v59\n\t" "v_smfmac_f32_16x16x64_bf16 v[60:63], v[92:95], a[248:255], v134\n\t" \
   "ds_read_b64_tr_b16 v[104:105], v136 offset:11776\n\t" "ds_read_b64_tr_b16 v[106:107], v136 offset:12032\n\t" \
   "v_smfmac_f32_16x16x64_bf16 v[60:63], v[96:99], v[240:247], v134\n\t" "v_cvt_pk_bf16_f32 v56, v56, v57\n\t" \
   "ds_write_b32 v135, v56 offset:12288\n\t" "v_smfmac_f32_16x16x64_bf16 v[60:63], v[100:103], v[248:255], v134\n\t" RSTAMP(6) \
   "s_waitcnt lgkmcnt(7)\n\t" "v_mfma_f32_16x16x32_bf16 v[48:51], v[160:163], v[64:67], 0\n\t" \
   "v_mfma_f32_16x16x32_bf16 v[52:55], v[176:179], v[64:67], 0\n\t" "s_waitcnt lgkmcnt(3)\n\t" "s_nop 0\n\t" "v_add_f32 v60, v60, v61\n\t" \
   "v_add_f32 v61, v62, v63\n\t" "v_cvt_pk_bf16_f32 v60, v60, v61\n\t" "ds_write_b32 v135, v60 offset:12544\n\t" \
   "v_mfma_f32_16x16x32_bf16 v[48:51], v[164:167], v[68:71], v[48:51]\n\t" "v_mfma_f32_16x16x32_bf16 v[52:55], v[180:183], v[68:71], v[52:55]\n\t" \
   "v_mfma_f32_16x16x32_bf16 v[48:51], v[168:171], v[72:75], v[48:51]\n\t" "v_mfma_f32_16x16x32_bf16 v[52:55], v[184:187], v[72:75], v[52:55]\n\t" \
   "s_waitcnt lgkmcnt(2)\n\t" "v_mfma_f32_16x16x32_bf16 v[48:51], v[172:175], v[104:107], v[48:51]\n\t" \
   "v_mfma_f32_16x16x32_bf16 v[52:55], v[188:191], v[104:107], v[52:55]\n\t" "s_waitcnt lgkmcnt(0)\n\t" "s_barrier\n\t" \
   "ds_read_b64_tr_b16 v[144:145], v136 offset:12288\n\t" "ds_read_b64_tr_b16 v[146:147], v136 offset:12544\n\t" \
   "ds_read_b64_tr_b16 v[148:149], v136 offset:12800\n\t" "ds_read_b64_tr_b16 v[150:151], v136 offset:13056\n\t" \
   "ds_read_b64_tr_b16 v[152:153], v136 offset:13312\n\t" "ds_read_b64_tr_b16 v[154:155], v136 offset:13568\n\t" \
   "ds_read_b64_tr_b16 v[156:157], v136 offset:13824\n\t" "ds_read_b64_tr_b16 v[158:159], v136 offset:14080\n\t" "s_cmp_eq_u32 s64, 0\n\t" \
   "s_cbranch_scc1 92f\n\t" "93:\n\t" RSTAMP(1) "v_cndmask_b32 v48, v48, v52, %[hi8]\n\t" "v_cndmask_b32 v49, v49, v53, %[hi8]\n\t" \
   "v_cndmask_b32 v50, v50, v54, %[hi8]\n\t" "v_cndmask_b32 v51, v51, v55, %[hi8]\n\t" "v_exp_f32 v80, v48\n\t" "v_exp_f32 v81, v49\n\t" \
   "s_waitcnt lgkmcnt(6)\n\t" "v_mfma_f32_16x16x32_bf16 v[76:79], v[144:147], v[64:67], 0\n\t" "v_exp_f32 v82, v50\n\t" "v_exp_f32 v83, v51\n\t" \
   "s_waitcnt lgkmcnt(4)\n\t" "v_mfma_f32_16x16x32_bf16 v[76:79], v[148:151], v[68:71], v[76:79]\n\t" "v_add_f32 v96, v80, v81\n\t" \
   "v_add_f32 v103, v82, v83\n\t" "s_waitcnt lgkmcnt(2)\n\t" "v_mfma_f32_16x16x32_bf16 v[76:79], v[152:155], v[72:75], v[76:79]\n\t" \
   "v_add_f32 v94, v96, v103\n\t" "v_add_f32 v96, v96, v103\n\t" "s_waitcnt lgkmcnt(0)\n\t" \
   "v_mfma_f32_16x16x32_bf16 v[76:79], v[156:159], v[104:107], v[76:79]\n\t" "v_permlane32_swap_b32 v96, v94\n\t" "v_add_f32 v96, v96, v94\n\t" \
   "s_nop 1\n\t" "v_add_f32_dpp v96, v96, v96 row_ror:8 row_mask:0xf bank_mask:0xf\n\t" "s_nop 2\n\t" "v_exp_f32 v88, v76\n\t" "v_exp_f32 v89, v77\n\t" \
   "v_add_f32 v103, v96, v88\n\t" "v_exp_f32 v90, v78\n\t" "v_add_f32 v103, v103, v89\n\t" "v_exp_f32 v91, v79\n\t" "v_add_f32 v103, v103, v90\n\t" \
   "v_add_f32 v97, v103, v91\n\t" "v_add_f32 v103, v103, v91\n\t" "s_nop 1\n\t" "v_permlane16_swap_b32 v103, v97\n\t" "v_add_f32 v103, v103, v97\n\t" \
   "v_rcp_f32 v100, v103\n\t" "v_add_u32 v94, 0xe0800000, v103\n\t" "v_pk_mul_f32 v[80:81], v[80:81], v[100:101] op_sel_hi:[1,0]\n\t" \
   "v_cmp_lt_u32 vcc, 0x40000000, v94\n\t" "v_pk_mul_f32 v[82:83], v[82:83], v[100:101] op_sel_hi:[1,0]\n\t" "s_cbranch_vccnz 70f\n\t" \
   "v_cvt_pk_bf16_f32 v48, v80, v81\n\t" "v_cvt_pk_bf16_f32 v49, v82, v83\n\t" "buffer_store_dwordx2 v[48:49], v132, %[rs], s55 offen sc1\n\t" \
   "v_pk_mul_f32 v[88:89], v[88:89], v[100:101] op_sel_hi:[1,0]\n\t" "v_pk_mul_f32 v[90:91], v[90:91], v[100:101] op_sel_hi:[1,0]\n\t" \
   "v_cvt_pk_bf16_f32 v52, v88, v89\n\t" "v_cvt_pk_bf16_f32 v53, v90, v91\n\t" "71:\n\t" \
   "buffer_store_dwordx2 v[52:53], v133, %[rs], s65 offen sc1\n\t" "s_memtime s[62:63]\n\t" "s_memrealtime s[60:61]\n\t" \
   "buffer_store_dwordx2 v[48:49], v132, %[rs], s50 offen sc1\n\t" RSTAMP(2) "s_branch 40b\n\t" "70:\n\t" "v_max3_f32 v94, v48, v49, v50\n\t" \
   "v_max3_f32 v102, v76, v77, v78\n\t" "v_max3_f32 v94, v94, v51, v79\n\t" "v_max_f32 v92, v94, v102\n\t" "s_nop 1\n\t" \
   "v_max_f32_dpp v102, v92, v92 row_ror:8 row_mask:0xf bank_mask:0xf\n\t" "v_max_f32_dpp v94, v92, v92 row_ror:8 row_mask:0xf bank_mask:0xf\n\t" \
   "s_nop 1\n\t" "v_permlane32_swap_b32 v102, v94\n\t" "v_max_f32 v95, v102, v94\n\t" "v_max_f32 v102, v102, v94\n\t" "s_nop 1\n\t" \
   "v_permlane16_swap_b32 v102, v95\n\t" "v_max_f32 v102, v102, v95\n\t" "v_sub_f32 v80, v48, v102\n\t" "v_sub_f32 v88, v76, v102\n\t" \
   "v_sub_f32 v81, v49, v102\n\t" "v_sub_f32 v89, v77, v102\n\t" "v_sub_f32 v82, v50, v102\n\t" "v_sub_f32 v90, v78, v102\n\t" \
   "v_sub_f32 v83, v51, v102\n\t" "v_sub_f32 v91, v79, v102\n\t" "v_exp_f32 v80, v80\n\t" "v_exp_f32 v88, v88\n\t" "v_exp_f32 v81, v81\n\t" \
   "v_exp_f32 v89, v89\n\t" "v_exp_f32 v82, v82\n\t" "v_exp_f32 v90, v90\n\t" "v_exp_f32 v83, v83\n\t" "v_exp_f32 v91, v91\n\t" "s_nop 0\n\t" \
   "v_add_f32 v96, v80, v81\n\t" "v_add_f32 v103, v82, v83\n\t" "v_add_f32 v94, v96, v103\n\t" "v_add_f32 v96, v96, v103\n\t" "s_nop 1\n\t" \
   "v_permlane32_swap_b32 v96, v94\n\t" "v_add_f32 v96, v96, v94\n\t" "s_nop 1\n\t" \
   "v_add_f32_dpp v96, v96, v96 row_ror:8 row_mask:0xf bank_mask:0xf\n\t" "v_add_f32 v103, v96, v88\n\t" "v_add_f32 v103, v103, v89\n\t" \
   "v_add_f32 v103, v103, v90\n\t" "v_add_f32 v97, v103, v91\n\t" "v_add_f32 v103, v103, v91\n\t" "s_nop 1\n\t" "v_permlane16_swap_b32 v103, v97\n\t" \
   "v_add_f32 v103, v103, v97\n\t" "v_rcp_f32 v100, v103\n\t" "s_nop 0\n\t" "v_pk_mul_f32 v[80:81], v[80:81], v[100:101] op_sel_hi:[1,0]\n\t" \
   "v_pk_mul_f32 v[82:83], v[82:83], v[100:101] op_sel_hi:[1,0]\n\t" "v_pk_mul_f32 v[88:89], v[88:89], v[100:101] op_sel_hi:[1,0]\n\t" \
   "v_pk_mul_f32 v[90:91], v[90:91], v[100:101] op_sel_hi:[1,0]\n\t" "v_cvt_pk_bf16_f32 v48, v80, v81\n\t" "v_cvt_pk_bf16_f32 v49, v82, v83\n\t" \
   "v_cvt_pk_bf16_f32 v52, v88, v89\n\t" "v_cvt_pk_bf16_f32 v53, v90, v91\n\t" "v_min_u32 v48, -2, v48\n\t" "v_min_u32 v52, -2, v52\n\t" \
   "buffer_store_dwordx2 v[48:49], v132, %[rs], s55 offen sc1\n\t" "s_branch 71b\n\t" "74:\n\t" "v_max3_f32 v80, v32, v33, v34\n\t" \
   "v_max3_f32 v81, v35, v36, v37\n\t" "v_max3_f32 v82, v38, v39, v40\n\t" "v_max3_f32 v83, v41, v42, v43\n\t" "v_max3_f32 v80, v80, v81, v82\n\t" \
   "v_max_f32 v81, v80, v83\n\t" "v_max_f32 v80, v80, v83\n\t" "s_nop 1\n\t" "v_permlane16_swap_b32 v80, v81\n\t" "v_max_f32 v82, v80, v81\n\t" \
   "v_max_f32 v80, v80, v81\n\t" "s_nop 1\n\t" "v_permlane32_swap_b32 v80, v82\n\t" "v_max_f32 v80, v80, v82\n\t" "v_sub_f32 v64, v32, v80\n\t" \
   "v_sub_f32 v65, v33, v80\n\t" "v_sub_f32 v66, v34, v80\n\t" "v_sub_f32 v67, v35, v80\n\t" "v_sub_f32 v68, v36, v80\n\t" \
   "v_sub_f32 v69, v37, v80\n\t" "v_sub_f32 v70, v38, v80\n\t" "v_sub_f32 v71, v39, v80\n\t" "v_sub_f32 v72, v40, v80\n\t" \
   "v_sub_f32 v73, v41, v80\n\t" "v_sub_f32 v74, v42, v80\n\t" "v_sub_f32 v75, v43, v80\n\t" "v_exp_f32 v64, v64\n\t" "v_exp_f32 v65, v65\n\t" \
   "v_exp_f32 v66, v66\n\t" "v_exp_f32 v67, v67\n\t" "v_exp_f32 v68, v68\n\t" "v_exp_f32 v69, v69\n\t" "v_exp_f32 v70, v70\n\t" \
   "v_exp_f32 v71, v71\n\t" "v_exp_f32 v72, v72\n\t" "v_exp_f32 v73, v73\n\t" "v_exp_f32 v74, v74\n\t" "v_exp_f32 v75, v75\n\t" "s_nop 0\n\t" \
   "v_add_f32 v80, v64, v65\n\t" "v_add_f32 v81, v66, v67\n\t" "v_add_f32 v82, v68, v69\n\t" "v_add_f32 v83, v70, v71\n\t" \
   "v_add_f32 v84, v72, v73\n\t" "v_add_f32 v85, v74, v75\n\t" "v_add_f32 v80, v80, v81\n\t" "v_add_f32 v82, v82, v83\n\t" \
   "v_add_f32 v84, v84, v85\n\t" "v_add_f32 v80, v80, v82\n\t" "v_fma_f32 v82, v80, 2.0, v84\n\t" "v_fma_f32 v80, v80, 2.0, v84\n\t" "s_nop 0\n\t" \
   "s_nop 0\n\t" "v_permlane16_swap_b32 v80, v82\n\t" "v_add_f32 v81, v80, v82\n\t" "v_add_f32 v80, v80, v82\n\t" "s_nop 0\n\t" "s_nop 0\n\t" \
   "v_permlane32_swap_b32 v80, v81\n\t" "v_add_f32 v80, v80, v81\n\t" "v_rcp_f32 v90, v80\n\t" "s_nop 0\n\t" "v_add_f32 v90, v90, v90\n\t" \
   "s_branch 75b\n\t" "63:\n\t" "s_cmp_lg_u32 s64, 0\n\t" "s_cbranch_scc1 64f\n\t" "s_sub_u32 s69, s56, 0x10000\n\t" "s_and_b32 s69, s69, -2\n\t" \
   "s_branch 50b\n\t" "64:\n\t" "s_mov_b32 s69, s48\n\t" "s_cmp_lt_u32 s64, 3\n\t" "s_cselect_b32 s69, -1, s69\n\t" "s_cmp_lg_u32 s69, -1\n\t" \
   "s_cbranch_scc1 50b\n\t" "s_add_u32 s76, s76, 0x1000000\n\t" "s_mov_b32 s73, 0\n\t" "65:\n\t" "s_add_u32 s73, s73, 1\n\t" \
   "s_min_u32 s72, s73, 7\n\t" "s_mul_i32 s72, s72, 5\n\t" "s_add_u32 s72, s72, %[xs]\n\t" "s_and_b32 s72, s72, 7\n\t" "s_lshl_b32 s71, %[xs], 7\n\t" \
   "v_subrev_u32 v109, s71, v139\n\t" "s_lshl_b32 s72, s72, 7\n\t" "s_add_u32 s72, s72, s66\n\t" "s_cmp_lt_u32 s73, 7\n\t" "s_cbranch_scc1 66f\n\t" \
   "s_sleep 4\n\t" "66:\n\t" "buffer_load_dword v108, v109, %[rs], s72 offen sc1\n\t" "s_waitcnt vmcnt(0)\n\t" "v_readfirstlane_b32 s69, v108\n\t" \
   "s_cmp_gt_u32 s73, 0x10000\n\t" "s_cbranch_scc1 50b\n\t" "s_cmp_eq_u32 s69, -1\n\t" "s_cbranch_scc1 65b\n\t" "s_branch 50b\n\t" "8:\n\t" \
   "s_nop 7\n\t" "s_waitcnt vmcnt(6) lgkmcnt(0)\n\t" "s_add_u32 s70, s70, 1\n\t" "s_cmp_gt_u32 s70, 2\n\t" "s_cbranch_scc1 81f\n\t" \
   "s_lshl_b32 s71, s70, 3\n\t" "s_lshl_b32 s71, 1, s71\n\t" "s_add_u32 s76, s76, s71\n\t" "81:\n\t" "v_mov_b32 v48, 0\n\t" "v_mov_b32 v49, 0\n\t" \
   "v_mov_b32 v50, 0\n\t" "v_mov_b32 v51, 0\n\t" "v_mov_b32 v52, 0\n\t" "v_mov_b32 v53, 0\n\t" "v_mov_b32 v54, 0\n\t" "v_mov_b32 v55, 0\n\t" \
   "v_mov_b32 v56, 0\n\t" "v_mov_b32 v57, 0\n\t" "v_mov_b32 v58, 0\n\t" "v_mov_b32 v59, 0\n\t" "v_mov_b32 v60, 0\n\t" "v_mov_b32 v61, 0\n\t" \
   "v_mov_b32 v62, 0\n\t" "v_mov_b32 v63, 0\n\t" "s_min_u32 s71, s70, 7\n\t" "s_lshr_b32 s67, s71, 1\n\t" "s_add_u32 s67, s67, %[xs]\n\t" \
   "s_and_b32 s67, s67, 3\n\t" "s_lshl_b32 s67, s67, 11\n\t" "s_and_b32 s72, s71, 1\n\t" "s_lshl_b32 s72, s72, 14\n\t" "s_add_u32 s67, s67, s72\n\t" \
   "s_add_u32 s67, s67, s66\n\t" "s_cmp_eq_u32 s70, 1\n\t" "s_cselect_b32 s67, s68, s67\n\t" "s_cmp_lt_u32 s70, 7\n\t" "s_cbranch_scc1 20b\n\t" \
   "s_sleep 3\n\t" "s_cmp_lt_u32 s70, 0x20000\n\t" "s_cbranch_scc1 20b\n\t" "s_mov_b32 s67, %[esc]\n\t" "s_mov_b32 s68, %[esc]\n\t" "s_branch 20b\n\t" \
   "27:\n\t" "v_mov_b32 v98, 0x7ffffff0\n\t" "buffer_store_dword v98, v98, %[rs], 0 offen\n\t" "buffer_store_dword v98, v98, %[rs], 0 offen\n\t" \
   "buffer_store_dword v98, v98, %[rs], 0 offen\n\t" "buffer_store_dword v98, v98, %[rs], 0 offen\n\t" \
   "buffer_store_dword v98, v98, %[rs], 0 offen\n\t" "buffer_store_dword v98, v98, %[rs], 0 offen\n\t" "s_branch 28b\n\t" "141:\n\t" \
   "s_mov_b32 s74, 0\n\t" "s_cmp_eq_u32 s64, 0\n\t" "s_cbranch_scc1 41b\n\t" "42:\n\t" "buffer_load_dword v15, v140, %[rs], s59 offen sc1\n\t" \
   "s_add_u32 s74, s74, 1\n\t" "s_waitcnt vmcnt(0)\n\t" "v_cmp_eq_u32 vcc, -1, v15\n\t" "s_cmp_gt_u32 s74, 0x10000\n\t" "s_cbranch_scc1 41b\n\t" \
   "s_cbranch_vccz 41b\n\t" "s_sleep 2\n\t" "s_branch 42b\n\t" "95:\n\t" "s_movk_i32 s59, 0x1000\n\t" "s_branch 197f\n\t" "97:\n\t" \
   "buffer_load_dwordx4 v[16:19], v138, %[rs], s79 offen offset:0 sc1\n\t" "buffer_load_dwordx4 v[20:23], v138, %[rs], s79 offen offset:1024 sc1\n\t" \
   "buffer_load_dwordx4 v[24:27], v138, %[rs], s79 offen offset:2048 sc1\n\t" \
   "buffer_load_dwordx4 v[28:31], v138, %[rs], s79 offen offset:3072 sc1\n\t" "v_or_b32 v108, s49, v141\n\t" "v_or_b32 v109, s49, v142\n\t" \
   "buffer_store_dwordx4 v[44:47], v108, %[rs], s52 offen sc1\n\t" "buffer_store_dwordx2 v[32:33], v109, %[rs], s51 offen sc1\n\t" \
   "s_waitcnt vmcnt(0)\n\t" "s_branch 96b\n\t" "197:\n\t" "s_mov_b32 s74, 0\n\t" "98:\n\t" "buffer_load_dword v15, v140, %[rs], s59 offen sc1\n\t" \
   "s_add_u32 s74, s74, 1\n\t" "s_waitcnt vmcnt(0)\n\t" "v_cmp_eq_u32 vcc, -1, v15\n\t" "s_cmp_gt_u32 s74, 0x10000\n\t" "s_cbranch_scc1 97b\n\t" \
   "s_cbranch_vccz 97b\n\t" "s_sleep 2\n\t" "s_branch 98b\n\t" "92:\n\t" "s_mov_b32 s59, 0\n\t" "s_branch 145f\n\t" "45:\n\t" \
   "v_mbcnt_lo_u32_b32 v98, -1, 0\n\t" "v_mbcnt_hi_u32_b32 v98, -1, v98\n\t" "v_lshlrev_b32 v98, 4, v98\n\t" "s_add_u32 s72, %[kf0], 4096\n\t" \
   "buffer_load_dwordx4 v[160:163], v98, %[rs], %[kf0] offen offset:0 sc1\n\t" \
   "buffer_load_dwordx4 v[164:167], v98, %[rs], %[kf0] offen offset:1024 sc1\n\t" \
   "buffer_load_dwordx4 v[168:171], v98, %[rs], %[kf0] offen offset:2048 sc1\n\t" \
   "buffer_load_dwordx4 v[172:175], v98, %[rs], %[kf0] offen offset:3072 sc1\n\t" \
   "buffer_load_dwordx4 v[176:179], v98, %[rs], s72 offen offset:0 sc1\n\t" "buffer_load_dwordx4 v[180:183], v98, %[rs], s72 offen offset:1024 sc1\n\t" \
   "buffer_load_dwordx4 v[184:187], v98, %[rs], s72 offen offset:2048 sc1\n\t" \
   "buffer_load_dwordx4 v[188:191], v98, %[rs], s72 offen offset:3072 sc1\n\t" "s_waitcnt vmcnt(0)\n\t" \
   "v_mfma_f32_16x16x32_bf16 v[48:51], v[160:163], v[64:67], 0\n\t" "v_mfma_f32_16x16x32_bf16 v[52:55], v[176:179], v[64:67], 0\n\t" \
   "v_mfma_f32_16x16x32_bf16 v[48:51], v[164:167], v[68:71], v[48:51]\n\t" "v_mfma_f32_16x16x32_bf16 v[52:55], v[180:183], v[68:71], v[52:55]\n\t" \
   "v_mfma_f32_16x16x32_bf16 v[48:51], v[168:171], v[72:75], v[48:51]\n\t" "v_mfma_f32_16x16x32_bf16 v[52:55], v[184:187], v[72:75], v[52:55]\n\t" \
   "v_mfma_f32_16x16x32_bf16 v[48:51], v[172:175], v[104:107], v[48:51]\n\t" "v_mfma_f32_16x16x32_bf16 v[52:55], v[188:191], v[104:107], v[52:55]\n\t" \
   "s_nop 7\n\t" "s_branch 93b\n\t" "145:\n\t" "s_mov_b32 s74, 0\n\t" "46:\n\t" "buffer_load_dword v15, v140, %[rs], s59 offen sc1\n\t" \
   "s_add_u32 s74, s74, 1\n\t" "s_waitcnt vmcnt(0)\n\t" "v_cmp_eq_u32 vcc, -1, v15\n\t" "s_cmp_gt_u32 s74, 0x10000\n\t" "s_cbranch_scc1 45b\n\t" \
   "s_cbranch_vccz 45b\n\t" "s_sleep 2\n\t" "s_branch 46b\n\t" "90:\n\t" "v_or_b32 v108, s49, v141\n\t" "v_or_b32 v109, s49, v142\n\t" \
   "buffer_store_dwordx4 v[44:47], v108, %[rs], s52 offen sc1\n\t" "buffer_store_dwordx2 v[32:33], v109, %[rs], s51 offen sc1\n\t" \
   "s_waitcnt vmcnt(0) lgkmcnt(0)"
#define P1_CLOB "v15", "v16", "v17", "v18", "v19", "v20", "v21", "v22", "v23", "v24", "v25", "v26", "v27", "v28", "v29", "v30", "v31", "v32", "v33", "v34", "v35", "v36", "v37", "v38", "v39", "v40", "v41", "v42", "v43", "v44", "v45", "v46", "v47", "v48", "v49", "v50", "v51", "v52", "v53", "v54", "v55", "v56", "v57", "v58", "v59", "v60", "v61", "v62", "v63", "v64", "v65", "v66", "v67", "v68", "v69", "v70", "v71", "v72", "v73", "v74", "v75", "v76", "v77", "v78", "v79", "v80", "v81", "v82", "v83", "v84", "v85", "v86", "v87", "v88", "v89", "v90", "v91", "v92", "v93", "v94", "v95", "v96", "v97", "v98", "v99", "v100", "v101", "v102", "v103", "v104", "v105", "v106", "v107", "v108", "v109", "v110", "v111", "v144", "v145", "v146", "v147", "v148", "v149", "v150", "v151", "v152", "v153", "v154", "v155", "v156", "v157", "v158", "v159", "v160", "v161", "v162", "v163", "v164", "v165", "v166", "v167", "v168", "v169", "v170", "v171", "v172", "v173", "v174", "v175", "v176", "v177", "v178", "v179", "v180", "v181", "v182", "v183", "v184", "v185", "v186", "v187", "v188", "v189", "v190", "v191", "s60", "s61", "s62", "s63", "s64", "s65", "s66", "s67", "s68", "s69", "s70", "s71", "s72", "s73", "s74", "s75", "s47", "s48", "s49", "s50", "s51", "s52", "s54", "s55", "s79", "s20", "s21", "s22", "s23", "s24", "s25", "s26", "s27", "s28", "s29", "s30", "s31", "s32", "s33", "s34", "s35"
#define PV_ASM \
   "s_mov_b32 s64, -1\n\t" "s_mov_b32 s76, 0\n\t" "s_mov_b32 s77, 0\n\t" "s_mov_b32 s71, 63\n\t" "s_mov_b32 s47, 1300\n\t" "s_mov_b32 s70, 0\n\t" \
   "s_mov_b32 s69, 0\n\t" "s_lshr_b64 s[72:73], %[sel], s71\n\t" "s_lshl_b32 s65, s71, 19\n\t" "s_and_b32 s72, s72, 1\n\t" \
   "s_add_u32 s65, s65, %[pool]\n\t" "s_lshl_b32 s72, s72, 13\n\t" "s_add_u32 s65, s65, s72\n\t" "s_lshr_b64 s[72:73], %[s3], s71\n\t" \
   "s_and_b32 s54, s72, 1\n\t" "s_lshl_b32 s54, s54, 14\n\t" "40:\n\t" "s_cmp_lt_i32 s64, 0\n\t" "s_cselect_b32 s72, 0x7ffffff0, 0\n\t" \
   "s_max_i32 s71, s64, 0\n\t" "v_or_b32 v109, s72, v143\n\t" "s_lshl_b32 s71, s71, 12\n\t" "v_mov_b32 v110, 1\n\t" "s_cmp_eq_u32 s64, 63\n\t" \
   "s_cbranch_scc1 70f\n\t" "71:\n\t" "s_waitcnt vmcnt(0)\n\t" RSTAMP(3) "s_barrier\n\t" "buffer_load_dword v108, v139, %[rs], s65 offen sc1\n\t" \
   "buffer_store_dword v110, v109, %[rs], s71 offen sc1\n\t" PVTRACE "s_add_u32 s48, s69, s47\n\t" "s_cmp_eq_u32 s70, 0\n\t" \
   "s_cselect_b32 s48, s48, -1\n\t" "s_mov_b32 s66, s65\n\t" "s_add_i32 s64, s64, 1\n\t" "s_and_b32 s71, s64, 63\n\t" \
   "s_lshr_b64 s[72:73], %[sel], s71\n\t" "s_lshl_b32 s65, s71, 19\n\t" "s_and_b32 s72, s72, 1\n\t" "s_add_u32 s65, s65, %[pool]\n\t" \
   "s_lshl_b32 s72, s72, 13\n\t" "s_add_u32 s65, s65, s72\n\t" "s_xor_b32 s67, s54, %[lad0]\n\t" "s_xor_b32 s68, s67, 0x4000\n\t" \
   "s_add_u32 s67, s67, s66\n\t" "s_add_u32 s68, s68, s66\n\t" "s_lshr_b64 s[72:73], %[s3], s71\n\t" "s_and_b32 s54, s72, 1\n\t" \
   "s_lshl_b32 s54, s54, 14\n\t" "s_mov_b32 s70, 0\n\t" "s_lshr_b64 s[72:73], %[ysel], s71\n\t" "s_mul_i32 s73, s71, 43\n\t" \
   "s_mul_i32 s49, s71, 80\n\t" "s_and_b32 s72, s72, 1\n\t" "s_lshr_b32 s73, s73, 7\n\t" "s_add_u32 s49, s49, s72\n\t" \
   "s_lshl4_add_u32 s49, s73, s49\n\t" "s_lshl_b32 s49, s49, 13\n\t" "s_add_u32 s49, s49, %[yb]\n\t" "s_add_u32 s50, s49, 32768\n\t" \
   "s_add_u32 s51, s49, 65536\n\t" "s_add_u32 s52, s49, 98304\n\t" "s_add_u32 s53, s49, 131072\n\t" "v_mov_b32 v48, 0\n\t" "v_mov_b32 v49, 0\n\t" \
   "v_mov_b32 v50, 0\n\t" "v_mov_b32 v51, 0\n\t" "v_mov_b32 v52, 0\n\t" "v_mov_b32 v53, 0\n\t" "v_mov_b32 v54, 0\n\t" "v_mov_b32 v55, 0\n\t" \
   "v_mov_b32 v144, 0\n\t" "v_mov_b32 v145, 0\n\t" "v_mov_b32 v146, 0\n\t" "v_mov_b32 v147, 0\n\t" "v_mov_b32 v148, 0\n\t" "v_mov_b32 v149, 0\n\t" \
   "v_mov_b32 v150, 0\n\t" "v_mov_b32 v151, 0\n\t" "v_mov_b32 v152, 0\n\t" "v_mov_b32 v153, 0\n\t" "v_mov_b32 v154, 0\n\t" "v_mov_b32 v155, 0\n\t" \
   "v_mov_b32 v156, 0\n\t" "v_mov_b32 v157, 0\n\t" "v_mov_b32 v158, 0\n\t" "v_mov_b32 v159, 0\n\t" "v_mov_b32 v160, 0\n\t" "v_mov_b32 v161, 0\n\t" \
   "v_mov_b32 v162, 0\n\t" "v_mov_b32 v163, 0\n\t" "s_cmp_eq_u32 s64, 64\n\t" "s_cbranch_scc1 90f\n\t" "s_waitcnt vmcnt(1)\n\t" \
   "v_readlane_b32 s69, v108, 0\n\t" "v_readlane_b32 s71, v108, 1\n\t" "s_cmp_eq_u32 s69, -1\n\t" "s_cbranch_scc1 63f\n\t" "s_cmp_eq_u32 s71, -1\n\t" \
   "s_cselect_b32 s47, s47, s71\n\t" "50:\n\t" "s_add_u32 s73, s69, %[m1c]\n\t" RSTAMP(5) "51:\n\t" "s_memrealtime s[60:61]\n\t" \
   "s_memtime s[62:63]\n\t" "s_waitcnt lgkmcnt(0)\n\t" "s_sub_u32 s74, s62, s57\n\t" "s_mul_i32 s74, s74, s58\n\t" "s_lshr_b32 s74, s74, 12\n\t" \
   "s_add_u32 s74, s74, s56\n\t" "s_lshl_b32 s60, s60, 3\n\t" "s_sub_u32 s75, s74, s60\n\t" "s_cmp_lt_u32 s75, 32\n\t" "s_cbranch_scc1 52f\n\t" \
   "s_cmp_lt_i32 s75, 0\n\t" "s_cselect_b32 s61, 0, 31\n\t" "s_add_u32 s74, s60, s61\n\t" "s_sub_i32 s75, s61, s75\n\t" "s_add_i32 s75, s75, 1\n\t" \
   "s_ashr_i32 s75, s75, 1\n\t" "s_min_i32 s75, s75, 8\n\t" "s_max_i32 s75, s75, -8\n\t" "s_mov_b32 s56, s74\n\t" "s_mov_b32 s57, s62\n\t" \
   "s_add_u32 s58, s58, s75\n\t" "52:\n\t" "s_sub_i32 s74, s73, s74\n\t" "s_cmp_le_i32 s74, 150\n\t" "s_cbranch_scc1 53f\n\t" \
   "s_cmp_lt_i32 s74, 260\n\t" "s_cbranch_scc1 51b\n\t" "s_sleep 2\n\t" "s_cmp_lt_i32 s74, 32000\n\t" "s_cbranch_scc1 51b\n\t" "s_mov_b32 s74, 0\n\t" \
   "53:\n\t" "s_sub_i32 s74, s74, 56\n\t" "s_max_i32 s74, s74, 0\n\t" "s_mul_i32 s74, s74, 23\n\t" "s_lshr_b32 s74, s74, 5\n\t" \
   "s_and_b32 s74, s74, -4\n\t" "s_getpc_b64 s[60:61]\n\t" "54:\n\t" "s_sub_u32 s74, 55f-54b, s74\n\t" "s_add_u32 s60, s60, s74\n\t" \
   "s_addc_u32 s61, s61, 0\n\t" "s_setpc_b64 s[60:61]\n\t" "s_nop 3\n\t" "s_nop 3\n\t" "s_nop 3\n\t" "s_nop 3\n\t" "s_nop 3\n\t" "s_nop 3\n\t" \
   "s_nop 3\n\t" "s_nop 3\n\t" "s_nop 3\n\t" "s_nop 3\n\t" "s_nop 3\n\t" "s_nop 3\n\t" "s_nop 3\n\t" "s_nop 3\n\t" "s_nop 3\n\t" "s_nop 3\n\t" \
   "s_nop 3\n\t" "s_nop 3\n\t" "s_nop 3\n\t" "s_nop 3\n\t" "s_nop 3\n\t" "s_nop 3\n\t" "s_nop 3\n\t" "s_nop 3\n\t" "55:\n\t" "20:\n\t" RSTAMP(4) \
   "buffer_load_dwordx4 v[64:67], v128, %[rs], s67 offen sc1\n\t" "buffer_load_dwordx4 v[68:71], v129, %[rs], s67 offen sc1\n\t" \
   "buffer_load_dwordx4 v[72:75], v130, %[rs], s67 offen sc1\n\t" "s_waitcnt vmcnt(2)\n\t" "ds_write_b128 v112, v[64:67]\n\t" \
   "v_smfmac_f32_16x16x64_bf16 v[48:51], v[64:67], a[0:7], v134\n\t" "s_waitcnt vmcnt(1)\n\t" "ds_write_b128 v113, v[68:71]\n\t" \
   "s_waitcnt vmcnt(0)\n\t" "ds_write_b128 v114, v[72:75]\n\t" RSTAMP(7) "v_smfmac_f32_16x16x64_bf16 v[52:55], v[64:67], a[64:71], v134\n\t" \
   "v_max3_u32 v110, v64, v66, v68\n\t" "v_max3_u32 v111, v70, v72, v74\n\t" "v_smfmac_f32_16x16x64_bf16 v[48:51], v[68:71], a[8:15], v134\n\t" \
   "v_max_u32 v110, v110, v111\n\t" "v_cmp_eq_u32 vcc, -1, v110\n\t" "v_smfmac_f32_16x16x64_bf16 v[52:55], v[68:71], a[72:79], v134\n\t" \
   "s_cbranch_vccnz 8f\n\t" "s_waitcnt lgkmcnt(0)\n\t" "s_barrier\n\t" RSTAMP(0) "ds_read_b128 v[76:79], v115\n\t" "ds_read_b128 v[80:83], v116\n\t" \
   "ds_read_b128 v[84:87], v117\n\t" "v_smfmac_f32_16x16x64_bf16 v[48:51], v[72:75], a[16:23], v134\n\t" "ds_read_b128 v[88:91], v118\n\t" \
   "ds_read_b128 v[92:95], v119\n\t" "v_smfmac_f32_16x16x64_bf16 v[52:55], v[72:75], a[80:87], v134\n\t" "ds_read_b128 v[96:99], v120\n\t" \
   "ds_read_b128 v[100:103], v121\n\t" "s_waitcnt lgkmcnt(6)\n\t" "v_smfmac_f32_16x16x64_bf16 v[48:51], v[76:79], a[24:31], v134\n\t" \
   "v_smfmac_f32_16x16x64_bf16 v[52:55], v[76:79], a[88:95], v134\n\t" "s_waitcnt lgkmcnt(5)\n\t" \
   "v_smfmac_f32_16x16x64_bf16 v[48:51], v[80:83], a[32:39], v134\n\t" "v_smfmac_f32_16x16x64_bf16 v[52:55], v[80:83], a[96:103], v134\n\t" \
   "s_waitcnt lgkmcnt(4)\n\t" "v_smfmac_f32_16x16x64_bf16 v[48:51], v[84:87], a[40:47], v134\n\t" \
   "v_smfmac_f32_16x16x64_bf16 v[52:55], v[84:87], a[104:111], v134\n\t" "s_waitcnt lgkmcnt(3)\n\t" \
   "v_smfmac_f32_16x16x64_bf16 v[48:51], v[88:91], a[48:55], v134\n\t" "v_smfmac_f32_16x16x64_bf16 v[52:55], v[88:91], a[112:119], v134\n\t" \
   "s_waitcnt lgkmcnt(0)\n\t" "v_smfmac_f32_16x16x64_bf16 v[48:51], v[92:95], a[56:63], v134\n\t" \
   "v_smfmac_f32_16x16x64_bf16 v[48:51], v[96:99], v[192:199], v134\n\t" "v_smfmac_f32_16x16x64_bf16 v[48:51], v[100:103], v[200:207], v134\n\t" \
   "v_smfmac_f32_16x16x64_bf16 v[52:55], v[92:95], a[120:127], v134\n\t" "v_smfmac_f32_16x16x64_bf16 v[52:55], v[96:99], v[208:215], v134\n\t" \
   "v_smfmac_f32_16x16x64_bf16 v[52:55], v[100:103], v[216:223], v134\n\t" "v_add_f32 v48, v48, v49\n\t" "v_add_f32 v49, v50, v51\n\t" \
   "v_cvt_pk_bf16_f32 v48, v48, v49\n\t" "ds_write_b32 v135, v48 offset:10240\n\t" "s_nop 3\n\t" "v_add_f32 v52, v52, v53\n\t" \
   "v_add_f32 v53, v54, v55\n\t" "v_cvt_pk_bf16_f32 v52, v52, v53\n\t" "ds_write_b32 v135, v52 offset:10496\n\t" "s_waitcnt lgkmcnt(0)\n\t" \
   "s_barrier\n\t" RSTAMP(6) "ds_read_b64_tr_b16 v[16:17], v136 offset:10240\n\t" "ds_read_b64_tr_b16 v[18:19], v136 offset:10496\n\t" \
   "ds_read_b64_tr_b16 v[20:21], v136 offset:10752\n\t" "ds_read_b64_tr_b16 v[22:23], v136 offset:11008\n\t" \
   "ds_read_b64_tr_b16 v[24:25], v136 offset:11264\n\t" "ds_read_b64_tr_b16 v[26:27], v136 offset:11520\n\t" \
   "ds_read_b64_tr_b16 v[28:29], v136 offset:11776\n\t" "ds_read_b64_tr_b16 v[30:31], v136 offset:12032\n\t" "s_waitcnt lgkmcnt(6)\n\t" \
   "v_smfmac_f32_16x16x64_bf16 v[144:147], v[16:19], a[128:135], v131\n\t" "v_smfmac_f32_16x16x64_bf16 v[148:151], v[16:19], a[160:167], v131\n\t" \
   "v_smfmac_f32_16x16x64_bf16 v[152:155], v[16:19], a[192:199], v131\n\t" "v_smfmac_f32_16x16x64_bf16 v[156:159], v[16:19], a[224:231], v131\n\t" \
   "v_smfmac_f32_16x16x64_bf16 v[160:163], v[16:19], v[224:231], v131\n\t" "s_waitcnt lgkmcnt(0)\n\t" \
   "v_smfmac_f32_16x16x64_bf16 v[144:147], v[20:23], a[136:143], v131\n\t" "v_smfmac_f32_16x16x64_bf16 v[144:147], v[24:27], a[144:151], v131\n\t" \
   "v_smfmac_f32_16x16x64_bf16 v[144:147], v[28:31], a[152:159], v131\n\t" "v_smfmac_f32_16x16x64_bf16 v[148:151], v[20:23], a[168:175], v131\n\t" \
   "v_smfmac_f32_16x16x64_bf16 v[148:151], v[24:27], a[176:183], v131\n\t" "v_smfmac_f32_16x16x64_bf16 v[148:151], v[28:31], a[184:191], v131\n\t" \
   "v_smfmac_f32_16x16x64_bf16 v[152:155], v[20:23], a[200:207], v131\n\t" "v_cvt_pk_bf16_f32 v164, v144, v145\n\t" \
   "v_cvt_pk_bf16_f32 v165, v146, v147\n\t" "v_smfmac_f32_16x16x64_bf16 v[152:155], v[24:27], a[208:215], v131\n\t" "v_min_u32 v164, -2, v164\n\t" \
   "buffer_store_dwordx2 v[164:165], v132, %[rs], s49 offen sc1\n\t" "v_smfmac_f32_16x16x64_bf16 v[152:155], v[28:31], a[216:223], v131\n\t" \
   "v_smfmac_f32_16x16x64_bf16 v[156:159], v[20:23], a[232:239], v131\n\t" "v_cvt_pk_bf16_f32 v166, v148, v149\n\t" \
   "v_cvt_pk_bf16_f32 v167, v150, v151\n\t" "v_smfmac_f32_16x16x64_bf16 v[156:159], v[24:27], a[240:247], v131\n\t" "v_min_u32 v166, -2, v166\n\t" \
   "buffer_store_dwordx2 v[166:167], v132, %[rs], s50 offen sc1\n\t" "v_smfmac_f32_16x16x64_bf16 v[156:159], v[28:31], a[248:255], v131\n\t" \
   "v_smfmac_f32_16x16x64_bf16 v[160:163], v[20:23], v[232:239], v131\n\t" "v_cvt_pk_bf16_f32 v168, v152, v153\n\t" \
   "v_cvt_pk_bf16_f32 v169, v154, v155\n\t" "v_smfmac_f32_16x16x64_bf16 v[160:163], v[24:27], v[240:247], v131\n\t" "v_min_u32 v168, -2, v168\n\t" \
   "buffer_store_dwordx2 v[168:169], v132, %[rs], s51 offen sc1\n\t" "v_smfmac_f32_16x16x64_bf16 v[160:163], v[28:31], v[248:255], v131\n\t" \
   "v_cvt_pk_bf16_f32 v170, v156, v157\n\t" "v_cvt_pk_bf16_f32 v171, v158, v159\n\t" "v_min_u32 v170, -2, v170\n\t" \
   "buffer_store_dwordx2 v[170:171], v132, %[rs], s52 offen sc1\n\t" "s_nop 3\n\t" "v_cvt_pk_bf16_f32 v172, v160, v161\n\t" \
   "v_cvt_pk_bf16_f32 v173, v162, v163\n\t" "v_min_u32 v172, -2, v172\n\t" "buffer_store_dwordx2 v[172:173], v132, %[rs], s53 offen sc1\n\t" RSTAMP(2) \
   "s_branch 40b\n\t" "70:\n\t" "s_mov_b32 s46, 0\n\t" "s_lshr_b64 s[72:73], %[sel], s46\n\t" "s_lshl_b32 s45, s46, 19\n\t" "s_and_b32 s72, s72, 1\n\t" \
   "s_add_u32 s45, s45, %[pool]\n\t" "s_lshl_b32 s72, s72, 13\n\t" "s_add_u32 s45, s45, s72\n\t" "v_mov_b32 v76, -1\n\t" "v_mov_b32 v77, -1\n\t" \
   "v_mov_b32 v78, -1\n\t" "v_mov_b32 v79, -1\n\t" "buffer_store_dwordx4 v[76:79], v133, %[rs], s45 offen sc1\n\t" \
   "buffer_store_dwordx4 v[76:79], v133, %[rs], s45 offen offset:1024 sc1\n\t" "s_branch 71b\n\t" "63:\n\t" "s_cmp_lg_u32 s64, 0\n\t" \
   "s_cbranch_scc1 64f\n\t" "s_sub_u32 s69, s56, 0x10000\n\t" "s_and_b32 s69, s69, -2\n\t" "s_branch 50b\n\t" "64:\n\t" "s_mov_b32 s69, s48\n\t" \
   "s_cmp_lt_u32 s64, 3\n\t" "s_cselect_b32 s69, -1, s69\n\t" "s_cmp_lg_u32 s69, -1\n\t" "s_cbranch_scc1 50b\n\t" "s_add_u32 s76, s76, 0x1000000\n\t" \
   "s_mov_b32 s73, 0\n\t" "65:\n\t" "s_add_u32 s73, s73, 1\n\t" "s_sleep 6\n\t" "s_and_b32 s72, s73, 3\n\t" "s_add_u32 s72, s72, 8\n\t" \
   "s_sub_u32 s72, s72, %[xs]\n\t" "s_lshl_b32 s72, s72, 7\n\t" "s_add_u32 s72, s72, s66\n\t" "buffer_load_dword v108, v139, %[rs], s72 offen sc1\n\t" \
   "s_waitcnt vmcnt(0)\n\t" "v_readfirstlane_b32 s69, v108\n\t" "s_cmp_gt_u32 s73, 0x10000\n\t" "s_cbranch_scc1 50b\n\t" "s_cmp_eq_u32 s69, -1\n\t" \
   "s_cbranch_scc1 65b\n\t" "s_branch 50b\n\t" "8:\n\t" "s_nop 7\n\t" "s_waitcnt lgkmcnt(0)\n\t" "s_add_u32 s70, s70, 1\n\t" "s_cmp_gt_u32 s70, 2\n\t" \
   "s_cbranch_scc1 81f\n\t" "s_lshl_b32 s71, s70, 3\n\t" "s_lshl_b32 s71, 1, s71\n\t" "s_add_u32 s76, s76, s71\n\t" "81:\n\t" "v_mov_b32 v48, 0\n\t" \
   "v_mov_b32 v49, 0\n\t" "v_mov_b32 v50, 0\n\t" "v_mov_b32 v51, 0\n\t" "v_mov_b32 v52, 0\n\t" "v_mov_b32 v53, 0\n\t" "v_mov_b32 v54, 0\n\t" \
   "v_mov_b32 v55, 0\n\t" "s_add_u32 s71, s70, 1\n\t" "s_min_u32 s71, s71, 7\n\t" "s_lshr_b32 s67, s71, 1\n\t" "s_add_u32 s67, s67, %[xs]\n\t" \
   "s_and_b32 s67, s67, 3\n\t" "s_lshl_b32 s67, s67, 11\n\t" "s_and_b32 s72, s71, 1\n\t" "s_lshl_b32 s72, s72, 14\n\t" "s_add_u32 s67, s67, s72\n\t" \
   "s_add_u32 s67, s67, s66\n\t" "s_cmp_lt_u32 s70, 6\n\t" "s_cbranch_scc1 20b\n\t" "s_sleep 3\n\t" "s_cmp_lt_u32 s70, 0x20000\n\t" \
   "s_cbranch_scc1 20b\n\t" "s_mov_b32 s67, %[esc]\n\t" "s_branch 20b\n\t" "90:\n\t" "s_waitcnt vmcnt(0) lgkmcnt(0)"
#define PV_CLOB "v16", "v17", "v18", "v19", "v20", "v21", "v22", "v23", "v24", "v25", "v26", "v27", "v28", "v29", "v30", "v31", "v48", "v49", "v50", "v51", "v52", "v53", "v54", "v55", "v56", "v57", "v58", "v59", "v60", "v61", "v62", "v63", "v64", "v65", "v66", "v67", "v68", "v69", "v70", "v71", "v72", "v73", "v74", "v75", "v76", "v77", "v78", "v79", "v80", "v81", "v82", "v83", "v84", "v85", "v86", "v87", "v88", "v89", "v90", "v91", "v92", "v93", "v94", "v95", "v96", "v97", "v98", "v99", "v100", "v101", "v102", "v103", "v104", "v105", "v106", "v107", "v108", "v109", "v110", "v111", "v144", "v145", "v146", "v147", "v148", "v149", "v150", "v151", "v152", "v153", "v154", "v155", "v156", "v157", "v158", "v159", "v160", "v161", "v162", "v163", "v164", "v165", "v166", "v167", "v168", "v169", "v170", "v171", "v172", "v173", "v174", "v175", "s60", "s61", "s62", "s63", "s64", "s65", "s66", "s67", "s68", "s69", "s70", "s71", "s72", "s73", "s74", "s75", "s45", "s46", "s47", "s48", "s49", "s50", "s51", "s52", "s53", "s54"
#define WAIT_ASM \
   "11:\n\t" "s_memrealtime s[60:61]\n\t" "s_memtime s[62:63]\n\t" "s_waitcnt lgkmcnt(0)\n\t" "s_sub_u32 s74, s62, s57\n\t" \
   "s_mul_i32 s74, s74, s58\n\t" "s_lshr_b32 s74, s74, 12\n\t" "s_add_u32 s74, s74, s56\n\t" "s_lshl_b32 s60, s60, 3\n\t" "s_sub_u32 s75, s74, s60\n\t" \
   "s_cmp_lt_u32 s75, 32\n\t" "s_cbranch_scc1 12f\n\t" "s_cmp_lt_i32 s75, 0\n\t" "s_cselect_b32 s61, 0, 31\n\t" "s_add_u32 s74, s60, s61\n\t" \
   "s_sub_i32 s75, s61, s75\n\t" "s_add_i32 s75, s75, 1\n\t" "s_ashr_i32 s75, s75, 1\n\t" "s_min_i32 s75, s75, 8\n\t" "s_max_i32 s75, s75, -8\n\t" \
   "s_mov_b32 s56, s74\n\t" "s_mov_b32 s57, s62\n\t" "s_add_u32 s58, s58, s75\n\t" "12:\n\t" "s_sub_i32 s74, %[ta], s74\n\t" \
   "s_cmp_le_i32 s74, 150\n\t" "s_cbranch_scc1 13f\n\t" "s_cmp_lt_i32 s74, 260\n\t" "s_cbranch_scc1 11b\n\t" "s_sleep 2\n\t" \
   "s_cmp_lt_i32 s74, 32000\n\t" "s_cbranch_scc1 11b\n\t" "s_mov_b32 s74, 0\n\t" "13:\n\t" "s_sub_i32 s74, s74, 56\n\t" "s_max_i32 s74, s74, 0\n\t" \
   "s_mul_i32 s74, s74, 23\n\t" "s_lshr_b32 s74, s74, 5\n\t" "s_and_b32 s74, s74, -4\n\t" "s_getpc_b64 s[60:61]\n\t" "14:\n\t" \
   "s_sub_u32 s74, 15f-14b, s74\n\t" "s_add_u32 s60, s60, s74\n\t" "s_addc_u32 s61, s61, 0\n\t" "s_setpc_b64 s[60:61]\n\t" "s_nop 3\n\t" "s_nop 3\n\t" \
   "s_nop 3\n\t" "s_nop 3\n\t" "s_nop 3\n\t" "s_nop 3\n\t" "s_nop 3\n\t" "s_nop 3\n\t" "s_nop 3\n\t" "s_nop 3\n\t" "s_nop 3\n\t" "s_nop 3\n\t" \
   "s_nop 3\n\t" "s_nop 3\n\t" "s_nop 3\n\t" "s_nop 3\n\t" "s_nop 3\n\t" "s_nop 3\n\t" "s_nop 3\n\t" "s_nop 3\n\t" "s_nop 3\n\t" "s_nop 3\n\t" \
   "s_nop 3\n\t" "s_nop 3\n\t" "15:\n\t" ""
#define WAIT_CLOB "s60", "s61", "s62", "s63", "s74", "s75"
#define FIN_ASM \
   "s_mov_b32 s40, 0\n\t" "s_mov_b32 s76, 0\n\t" "s_mov_b32 s77, 0\n\t" "s_sub_u32 s45, s56, 0x10000\n\t" "v_mov_b32 v13, -2\n\t" \
   "v_mov_b32 v11, -1\n\t" "30:\n\t" "s_lshr_b64 s[72:73], %[sel], s40\n\t" "s_lshl_b32 s41, s40, 19\n\t" "s_and_b32 s72, s72, 1\n\t" \
   "s_add_u32 s41, s41, %[pool]\n\t" "s_lshl_b32 s72, s72, 13\n\t" "s_add_u32 s41, s41, s72\n\t" "s_lshr_b64 s[72:73], %[s3], s40\n\t" \
   "s_and_b32 s47, s72, 1\n\t" "s_lshl_b32 s47, s47, 14\n\t" "s_mul_i32 s49, s40, 1310720\n\t" "s_add_u32 s49, s49, %[su]\n\t" \
   "s_lshr_b64 s[72:73], %[ysel], s40\n\t" "s_mul_i32 s73, s40, 43\n\t" "s_mul_i32 s50, s40, 80\n\t" "s_and_b32 s72, s72, 1\n\t" \
   "s_lshr_b32 s73, s73, 7\n\t" "s_add_u32 s50, s50, s72\n\t" "s_lshl4_add_u32 s50, s73, s50\n\t" "s_lshl_b32 s50, s50, 13\n\t" \
   "s_add_u32 s50, s50, %[yb]\n\t" "s_add_u32 s51, s50, 4096\n\t" "s_lshl_b32 s52, s40, 12\n\t" "s_add_u32 s52, s52, %[uf]\n\t" \
   "s_add_u32 s69, s45, %[tu]\n\t" "v_max3_u32 v15, v8, v9, v10\n\t" "v_max_u32 v15, v15, v11\n\t" "v_cmp_eq_u32 vcc, -1, v15\n\t" \
   "s_add_u32 s40, s40, 1\n\t" "s_cbranch_vccnz 95f\n\t" "96:\n\t" "11:\n\t" "s_memrealtime s[60:61]\n\t" "s_memtime s[62:63]\n\t" \
   "s_waitcnt lgkmcnt(0)\n\t" "s_sub_u32 s74, s62, s57\n\t" "s_mul_i32 s74, s74, s58\n\t" "s_lshr_b32 s74, s74, 12\n\t" "s_add_u32 s74, s74, s56\n\t" \
   "s_lshl_b32 s60, s60, 3\n\t" "s_sub_u32 s75, s74, s60\n\t" "s_cmp_lt_u32 s75, 32\n\t" "s_cbranch_scc1 12f\n\t" "s_cmp_lt_i32 s75, 0\n\t" \
   "s_cselect_b32 s61, 0, 31\n\t" "s_add_u32 s74, s60, s61\n\t" "s_sub_i32 s75, s61, s75\n\t" "s_add_i32 s75, s75, 1\n\t" "s_ashr_i32 s75, s75, 1\n\t" \
   "s_min_i32 s75, s75, 8\n\t" "s_max_i32 s75, s75, -8\n\t" "s_mov_b32 s56, s74\n\t" "s_mov_b32 s57, s62\n\t" "s_add_u32 s58, s58, s75\n\t" "12:\n\t" \
   "s_sub_i32 s74, s69, s74\n\t" "s_cmp_le_i32 s74, 150\n\t" "s_cbranch_scc1 13f\n\t" "s_cmp_lt_i32 s74, 260\n\t" "s_cbranch_scc1 11b\n\t" \
   "s_sleep 2\n\t" "s_cmp_lt_i32 s74, 32000\n\t" "s_cbranch_scc1 11b\n\t" "s_mov_b32 s74, 0\n\t" "13:\n\t" "s_sub_i32 s74, s74, 56\n\t" \
   "s_max_i32 s74, s74, 0\n\t" "s_mul_i32 s74, s74, 23\n\t" "s_lshr_b32 s74, s74, 5\n\t" "s_and_b32 s74, s74, -4\n\t" "s_getpc_b64 s[60:61]\n\t" \
   "14:\n\t" "s_sub_u32 s74, 15f-14b, s74\n\t" "s_add_u32 s60, s60, s74\n\t" "s_addc_u32 s61, s61, 0\n\t" "s_setpc_b64 s[60:61]\n\t" "s_nop 3\n\t" \
   "s_nop 3\n\t" "s_nop 3\n\t" "s_nop 3\n\t" "s_nop 3\n\t" "s_nop 3\n\t" "s_nop 3\n\t" "s_nop 3\n\t" "s_nop 3\n\t" "s_nop 3\n\t" "s_nop 3\n\t" \
   "s_nop 3\n\t" "s_nop 3\n\t" "s_nop 3\n\t" "s_nop 3\n\t" "s_nop 3\n\t" "s_nop 3\n\t" "s_nop 3\n\t" "s_nop 3\n\t" "s_nop 3\n\t" "s_nop 3\n\t" \
   "s_nop 3\n\t" "s_nop 3\n\t" "s_nop 3\n\t" "15:\n\t" RSTAMP(4) "buffer_load_dwordx4 a[0:3], v16, %[rs], s49 offen offset:0 sc1\n\t" "s_nop 0\n\t" \
   "buffer_load_dwordx4 a[4:7], v16, %[rs], s49 offen offset:1024 sc1\n\t" "s_nop 0\n\t" \
   "buffer_load_dwordx4 a[8:11], v16, %[rs], s49 offen offset:2048 sc1\n\t" "s_nop 0\n\t" \
   "buffer_load_dwordx4 a[12:15], v16, %[rs], s49 offen offset:3072 sc1\n\t" "s_nop 0\n\t" "s_add_u32 s49, s49, 4096\n\t" \
   "buffer_load_dwordx4 a[16:19], v16, %[rs], s49 offen offset:0 sc1\n\t" "s_nop 0\n\t" \
   "buffer_load_dwordx4 a[20:23], v16, %[rs], s49 offen offset:1024 sc1\n\t" "s_nop 0\n\t" \
   "buffer_load_dwordx4 a[24:27], v16, %[rs], s49 offen offset:2048 sc1\n\t" "s_nop 0\n\t" \
   "buffer_load_dwordx4 a[28:31], v16, %[rs], s49 offen offset:3072 sc1\n\t" "s_nop 0\n\t" "s_add_u32 s49, s49, 4096\n\t" \
   "buffer_load_dwordx4 a[32:35], v16, %[rs], s49 offen offset:0 sc1\n\t" "s_nop 0\n\t" \
   "buffer_load_dwordx4 a[36:39], v16, %[rs], s49 offen offset:1024 sc1\n\t" "s_nop 0\n\t" \
   "buffer_load_dwordx4 a[40:43], v16, %[rs], s49 offen offset:2048 sc1\n\t" "s_nop 0\n\t" \
   "buffer_load_dwordx4 a[44:47], v16, %[rs], s49 offen offset:3072 sc1\n\t" "s_nop 0\n\t" "s_add_u32 s49, s49, 4096\n\t" \
   "buffer_load_dwordx4 a[48:51], v16, %[rs], s49 offen offset:0 sc1\n\t" "s_nop 0\n\t" \
   "buffer_load_dwordx4 a[52:55], v16, %[rs], s49 offen offset:1024 sc1\n\t" "s_nop 0\n\t" \
   "buffer_load_dwordx4 a[56:59], v16, %[rs], s49 offen offset:2048 sc1\n\t" "s_nop 0\n\t" \
   "buffer_load_dwordx4 a[60:63], v16, %[rs], s49 offen offset:3072 sc1\n\t" "s_nop 0\n\t" "s_add_u32 s49, s49, 4096\n\t" \
   "buffer_load_dwordx4 a[64:67], v16, %[rs], s49 offen offset:0 sc1\n\t" "s_nop 0\n\t" \
   "buffer_load_dwordx4 a[68:71], v16, %[rs], s49 offen offset:1024 sc1\n\t" "s_nop 0\n\t" \
   "buffer_load_dwordx4 a[72:75], v16, %[rs], s49 offen offset:2048 sc1\n\t" "s_nop 0\n\t" \
   "buffer_load_dwordx4 a[76:79], v16, %[rs], s49 offen offset:3072 sc1\n\t" "s_nop 0\n\t" "s_add_u32 s49, s49, 4096\n\t" \
   "buffer_load_dwordx4 a[80:83], v16, %[rs], s49 offen offset:0 sc1\n\t" "s_nop 0\n\t" \
   "buffer_load_dwordx4 a[84:87], v16, %[rs], s49 offen offset:1024 sc1\n\t" "s_nop 0\n\t" \
   "buffer_load_dwordx4 a[88:91], v16, %[rs], s49 offen offset:2048 sc1\n\t" "s_nop 0\n\t" \
   "buffer_load_dwordx4 a[92:95], v16, %[rs], s49 offen offset:3072 sc1\n\t" "s_nop 0\n\t" "s_add_u32 s49, s49, 4096\n\t" \
   "buffer_load_dwordx4 a[96:99], v16, %[rs], s49 offen offset:0 sc1\n\t" "s_nop 0\n\t" \
   "buffer_load_dwordx4 a[100:103], v16, %[rs], s49 offen offset:1024 sc1\n\t" "s_nop 0\n\t" \
   "buffer_load_dwordx4 a[104:107], v16, %[rs], s49 offen offset:2048 sc1\n\t" "s_nop 0\n\t" \
   "buffer_load_dwordx4 a[108:111], v16, %[rs], s49 offen offset:3072 sc1\n\t" "s_nop 0\n\t" "s_add_u32 s49, s49, 4096\n\t" \
   "buffer_load_dwordx4 a[112:115], v16, %[rs], s49 offen offset:0 sc1\n\t" "s_nop 0\n\t" \
   "buffer_load_dwordx4 a[116:119], v16, %[rs], s49 offen offset:1024 sc1\n\t" "s_nop 0\n\t" \
   "buffer_load_dwordx4 a[120:123], v16, %[rs], s49 offen offset:2048 sc1\n\t" "s_nop 0\n\t" \
   "buffer_load_dwordx4 a[124:127], v16, %[rs], s49 offen offset:3072 sc1\n\t" "s_nop 0\n\t" "s_add_u32 s52, s52, 4096\n\t" \
   "buffer_load_dwordx4 v[8:11], v19, %[rs], s52 offen sc1\n\t" RSTAMP(0) "s_mov_b32 s46, 0\n\t" "s_xor_b32 s64, s47, %[lad0]\n\t" \
   "s_and_b32 s65, s64, 0x4000\n\t" "s_xor_b32 s64, s64, s65\n\t" "v_or_b32 v224, s65, v16\n\t" "v_or_b32 v225, s65, v17\n\t" \
   "v_xor_b32 v226, 0x4000, v224\n\t" "v_xor_b32 v227, 0x4000, v225\n\t" "s_add_u32 s64, s64, s41\n\t" "v_xor_b32 v14, s47, v20\n\t" \
   "v_xor_b32 v15, 0x4000, v14\n\t" "s_add_u32 s65, s64, 32768\n\t" "s_add_u32 s66, s64, 65536\n\t" "s_add_u32 s67, s64, 98304\n\t" \
   "s_add_u32 s68, s64, 131072\n\t" "s_add_u32 s69, s64, 163840\n\t" "s_add_u32 s70, s64, 196608\n\t" "s_add_u32 s71, s64, 229376\n\t" \
   "s_add_u32 s72, s64, 262144\n\t" "s_add_u32 s73, s64, 294912\n\t" "s_add_u32 s53, s41, 4096\n\t" "v_mov_b32 v28, 0\n\t" "v_mov_b32 v29, 0\n\t" \
   "v_mov_b32 v30, 0\n\t" "v_mov_b32 v31, 0\n\t" "19:\n\t" "ds_read_b128 v[24:27], v23\n\t" "s_waitcnt lgkmcnt(0)\n\t" \
   "v_readfirstlane_b32 s74, v26\n\t" "v_readfirstlane_b32 s44, v24\n\t" "v_readfirstlane_b32 s45, v25\n\t" "s_sub_u32 s48, s44, 110\n\t" \
   "s_cmp_lt_u32 s74, s40\n\t" "s_cbranch_scc1 18f\n\t" RSTAMP(7) "21:\n\t" "s_memrealtime s[60:61]\n\t" "s_memtime s[62:63]\n\t" \
   "s_waitcnt lgkmcnt(0)\n\t" "s_sub_u32 s74, s62, s57\n\t" "s_mul_i32 s74, s74, s58\n\t" "s_lshr_b32 s74, s74, 12\n\t" "s_add_u32 s74, s74, s56\n\t" \
   "s_lshl_b32 s60, s60, 3\n\t" "s_sub_u32 s75, s74, s60\n\t" "s_cmp_lt_u32 s75, 32\n\t" "s_cbranch_scc1 22f\n\t" "s_cmp_lt_i32 s75, 0\n\t" \
   "s_cselect_b32 s61, 0, 31\n\t" "s_add_u32 s74, s60, s61\n\t" "s_sub_i32 s75, s61, s75\n\t" "s_add_i32 s75, s75, 1\n\t" "s_ashr_i32 s75, s75, 1\n\t" \
   "s_min_i32 s75, s75, 8\n\t" "s_max_i32 s75, s75, -8\n\t" "s_mov_b32 s56, s74\n\t" "s_mov_b32 s57, s62\n\t" "s_add_u32 s58, s58, s75\n\t" "22:\n\t" \
   "s_sub_i32 s74, s48, s74\n\t" "s_cmp_le_i32 s74, 150\n\t" "s_cbranch_scc1 23f\n\t" "s_cmp_lt_i32 s74, 260\n\t" "s_cbranch_scc1 21b\n\t" \
   "s_sleep 2\n\t" "s_cmp_lt_i32 s74, 32000\n\t" "s_cbranch_scc1 21b\n\t" "s_mov_b32 s74, 0\n\t" "23:\n\t" "s_sub_i32 s74, s74, 56\n\t" \
   "s_max_i32 s74, s74, 0\n\t" "s_mul_i32 s74, s74, 23\n\t" "s_lshr_b32 s74, s74, 5\n\t" "s_and_b32 s74, s74, -4\n\t" "s_getpc_b64 s[60:61]\n\t" \
   "24:\n\t" "s_sub_u32 s74, 25f-24b, s74\n\t" "s_add_u32 s60, s60, s74\n\t" "s_addc_u32 s61, s61, 0\n\t" "s_setpc_b64 s[60:61]\n\t" "s_nop 3\n\t" \
   "s_nop 3\n\t" "s_nop 3\n\t" "s_nop 3\n\t" "s_nop 3\n\t" "s_nop 3\n\t" "s_nop 3\n\t" "s_nop 3\n\t" "s_nop 3\n\t" "s_nop 3\n\t" "s_nop 3\n\t" \
   "s_nop 3\n\t" "s_nop 3\n\t" "s_nop 3\n\t" "s_nop 3\n\t" "s_nop 3\n\t" "s_nop 3\n\t" "s_nop 3\n\t" "s_nop 3\n\t" "s_nop 3\n\t" "s_nop 3\n\t" \
   "s_nop 3\n\t" "s_nop 3\n\t" "s_nop 3\n\t" "25:\n\t" "buffer_load_dwordx4 v[32:35], v16, %[rs], s50 offen offset:0 sc1\n\t" \
   "buffer_load_dwordx4 v[36:39], v16, %[rs], s50 offen offset:1024 sc1\n\t" "buffer_load_dwordx4 v[40:43], v16, %[rs], s50 offen offset:2048 sc1\n\t" \
   "buffer_load_dwordx4 v[44:47], v16, %[rs], s50 offen offset:3072 sc1\n\t" "buffer_load_dwordx4 v[48:51], v16, %[rs], s51 offen offset:0 sc1\n\t" \
   "buffer_load_dwordx4 v[52:55], v16, %[rs], s51 offen offset:1024 sc1\n\t" "buffer_load_dwordx4 v[56:59], v16, %[rs], s51 offen offset:2048 sc1\n\t" \
   "buffer_load_dwordx4 v[60:63], v16, %[rs], s51 offen offset:3072 sc1\n\t" "s_nop 3\n\t" "s_nop 3\n\t" "s_nop 3\n\t" "s_nop 3\n\t" "s_nop 3\n\t" \
   "s_nop 3\n\t" "s_nop 3\n\t" "s_nop 3\n\t" "s_nop 3\n\t" "s_nop 3\n\t" "s_nop 3\n\t" "s_nop 3\n\t" "s_nop 3\n\t" "s_nop 3\n\t" "s_nop 3\n\t" \
   "s_nop 3\n\t" RSTAMP(6) "buffer_load_dwordx4 v[64:67], v224, %[rs], s64 offen offset:0 sc1\n\t" \
   "buffer_load_dwordx4 v[68:71], v224, %[rs], s64 offen offset:1024 sc1\n\t" "buffer_load_dwordx4 v[72:75], v224, %[rs], s65 offen offset:0 sc1\n\t" \
   "buffer_load_dwordx4 v[76:79], v224, %[rs], s65 offen offset:1024 sc1\n\t" "buffer_load_dwordx4 v[80:83], v224, %[rs], s66 offen offset:0 sc1\n\t" \
   "buffer_load_dwordx4 v[84:87], v224, %[rs], s66 offen offset:1024 sc1\n\t" "buffer_load_dwordx4 v[88:91], v224, %[rs], s67 offen offset:0 sc1\n\t" \
   "buffer_load_dwordx4 v[92:95], v224, %[rs], s67 offen offset:1024 sc1\n\t" "buffer_load_dwordx4 v[96:99], v224, %[rs], s68 offen offset:0 sc1\n\t" \
   "buffer_load_dwordx4 v[100:103], v224, %[rs], s68 offen offset:1024 sc1\n\t" \
   "buffer_load_dwordx4 v[104:107], v224, %[rs], s69 offen offset:0 sc1\n\t" \
   "buffer_load_dwordx4 v[108:111], v224, %[rs], s69 offen offset:1024 sc1\n\t" \
   "buffer_load_dwordx4 v[112:115], v224, %[rs], s70 offen offset:0 sc1\n\t" \
   "buffer_load_dwordx4 v[116:119], v224, %[rs], s70 offen offset:1024 sc1\n\t" \
   "buffer_load_dwordx4 v[120:123], v224, %[rs], s71 offen offset:0 sc1\n\t" \
   "buffer_load_dwordx4 v[124:127], v224, %[rs], s71 offen offset:1024 sc1\n\t" \
   "buffer_load_dwordx2 v[128:129], v225, %[rs], s72 offen offset:0 sc1\n\t" \
   "buffer_load_dwordx2 v[130:131], v225, %[rs], s72 offen offset:128 sc1\n\t" \
   "buffer_load_dwordx2 v[132:133], v225, %[rs], s72 offen offset:1024 sc1\n\t" \
   "buffer_load_dwordx2 v[134:135], v225, %[rs], s72 offen offset:1152 sc1\n\t" \
   "buffer_load_dwordx2 v[136:137], v225, %[rs], s73 offen offset:0 sc1\n\t" \
   "buffer_load_dwordx2 v[138:139], v225, %[rs], s73 offen offset:128 sc1\n\t" \
   "buffer_load_dwordx2 v[140:141], v225, %[rs], s73 offen offset:1024 sc1\n\t" \
   "buffer_load_dwordx2 v[142:143], v225, %[rs], s73 offen offset:1152 sc1\n\t" YSTAMP(24) "s_waitcnt vmcnt(23)\n\t" RSTAMP(1) \
   "v_smfmac_f32_16x16x64_bf16 v[28:31], v[64:67], a[0:7], v22\n\t" "s_waitcnt vmcnt(22)\n\t" \
   "v_smfmac_f32_16x16x64_bf16 v[28:31], v[68:71], a[8:15], v22\n\t" "s_waitcnt vmcnt(21)\n\t" \
   "v_smfmac_f32_16x16x64_bf16 v[28:31], v[72:75], a[16:23], v22\n\t" "s_waitcnt vmcnt(20)\n\t" \
   "v_smfmac_f32_16x16x64_bf16 v[28:31], v[76:79], a[24:31], v22\n\t" "s_waitcnt vmcnt(19)\n\t" \
   "v_smfmac_f32_16x16x64_bf16 v[28:31], v[80:83], a[32:39], v22\n\t" "s_waitcnt vmcnt(18)\n\t" \
   "v_smfmac_f32_16x16x64_bf16 v[28:31], v[84:87], a[40:47], v22\n\t" "s_waitcnt vmcnt(17)\n\t" \
   "v_smfmac_f32_16x16x64_bf16 v[28:31], v[88:91], a[48:55], v22\n\t" "s_waitcnt vmcnt(16)\n\t" \
   "v_smfmac_f32_16x16x64_bf16 v[28:31], v[92:95], a[56:63], v22\n\t" "s_waitcnt vmcnt(15)\n\t" \
   "v_smfmac_f32_16x16x64_bf16 v[28:31], v[96:99], a[64:71], v22\n\t" "s_waitcnt vmcnt(14)\n\t" \
   "v_smfmac_f32_16x16x64_bf16 v[28:31], v[100:103], a[72:79], v22\n\t" "s_waitcnt vmcnt(13)\n\t" \
   "v_smfmac_f32_16x16x64_bf16 v[28:31], v[104:107], a[80:87], v22\n\t" "s_waitcnt vmcnt(12)\n\t" \
   "v_smfmac_f32_16x16x64_bf16 v[28:31], v[108:111], a[88:95], v22\n\t" "s_waitcnt vmcnt(11)\n\t" \
   "v_smfmac_f32_16x16x64_bf16 v[28:31], v[112:115], a[96:103], v22\n\t" "s_waitcnt vmcnt(10)\n\t" \
   "v_smfmac_f32_16x16x64_bf16 v[28:31], v[116:119], a[104:111], v22\n\t" "s_waitcnt vmcnt(9)\n\t" \
   "v_smfmac_f32_16x16x64_bf16 v[28:31], v[120:123], a[112:119], v22\n\t" "s_waitcnt vmcnt(8)\n\t" \
   "v_smfmac_f32_16x16x64_bf16 v[28:31], v[124:127], a[120:127], v22\n\t" "s_waitcnt vmcnt(6)\n\t" \
   "v_smfmac_f32_16x16x64_bf16 v[28:31], v[128:131], v[32:39], v22\n\t" "s_waitcnt vmcnt(4)\n\t" \
   "v_smfmac_f32_16x16x64_bf16 v[28:31], v[132:135], v[40:47], v22\n\t" "s_waitcnt vmcnt(2)\n\t" \
   "v_smfmac_f32_16x16x64_bf16 v[28:31], v[136:139], v[48:55], v22\n\t" "s_waitcnt vmcnt(0)\n\t" \
   "v_smfmac_f32_16x16x64_bf16 v[28:31], v[140:143], v[56:63], v22\n\t" "s_nop 7\n\t" "v_add_f32 v28, v28, v29\n\t" "v_add_f32 v30, v30, v31\n\t" \
   "v_cvt_pk_bf16_f32 v24, v28, v30\n\t" "v_cmp_u_f32 vcc, v28, v30\n\t" "s_nop 0\n\t" \
   "v_mov_b32_dpp v25, v24 quad_perm:[1,0,3,2] row_mask:0xf bank_mask:0xf\n\t" "s_cbranch_vccnz 8f\n\t" "2:\n\t" RSTAMP(2) \
   "v_perm_b32 v26, v25, v24, v18\n\t" "s_nop 1\n\t" "v_min_u32_dpp v24, v26, v13 quad_perm:[0,1,0,1] row_mask:0xf bank_mask:0xf\n\t" \
   "v_mov_b32_dpp v25, v26 quad_perm:[2,3,2,3] row_mask:0xf bank_mask:0xf\n\t" "buffer_store_dwordx2 v[24:25], v14, %[rs], s41 offen sc1\n\t" \
   "buffer_store_dwordx2 v[24:25], v14, %[rs], s53 offen sc1\n\t" "s_memtime s[62:63]\n\t" "s_memrealtime s[60:61]\n\t" \
   "buffer_store_dwordx2 v[24:25], v15, %[rs], s41 offen sc1\n\t" "buffer_store_dwordx2 v[24:25], v15, %[rs], s53 offen sc1\n\t" RSTAMP(3) \
   "s_waitcnt lgkmcnt(0)\n\t" "s_sub_u32 s74, s62, s57\n\t" "s_mul_i32 s74, s74, s58\n\t" "s_lshr_b32 s74, s74, 12\n\t" "s_add_u32 s74, s74, s56\n\t" \
   "s_lshl_b32 s60, s60, 3\n\t" "s_sub_u32 s75, s74, s60\n\t" "s_cmp_lt_u32 s75, 32\n\t" "s_cbranch_scc1 38f\n\t" "s_cmp_lt_i32 s75, 0\n\t" \
   "s_cselect_b32 s61, 0, 31\n\t" "s_add_u32 s74, s60, s61\n\t" "s_sub_i32 s75, s61, s75\n\t" "s_add_i32 s75, s75, 1\n\t" "s_ashr_i32 s75, s75, 1\n\t" \
   "s_min_i32 s75, s75, 8\n\t" "s_max_i32 s75, s75, -8\n\t" "s_mov_b32 s56, s74\n\t" "s_mov_b32 s57, s62\n\t" "s_add_u32 s58, s58, s75\n\t" "38:\n\t" \
   "s_min_u32 s75, s46, 1\n\t" "s_and_b32 s74, s74, -4\n\t" "s_or_b32 s74, s74, s75\n\t" "v_mov_b32 v26, s74\n\t" \
   "buffer_store_dword v26, v21, %[rs], s41 offen sc1\n\t" FINTRACE "v_xor_b32 v23, 32, v23\n\t" "s_cmp_lt_u32 s40, 63\n\t" "s_cbranch_scc1 30b\n\t" \
   "s_branch 90f\n\t" "18:\n\t" "s_sleep 2\n\t" "s_branch 19b\n\t" "8:\n\t" "s_mov_b32 s74, 0\n\t" "87:\n\t" "v_max3_u32 v12, v32, v34, v36\n\t" \
   "v_max3_u32 v12, v12, v38, v40\n\t" "v_max3_u32 v12, v12, v42, v44\n\t" "v_max3_u32 v12, v12, v46, v48\n\t" "v_max3_u32 v12, v12, v50, v52\n\t" \
   "v_max3_u32 v12, v12, v54, v56\n\t" "v_max3_u32 v12, v12, v58, v60\n\t" "v_max_u32 v12, v12, v62\n\t" "v_cmp_eq_u32 vcc, -1, v12\n\t" \
   "s_cmp_gt_u32 s74, 0x10000\n\t" "s_cbranch_scc1 86f\n\t" "s_cbranch_vccz 86f\n\t" "s_cmp_eq_u32 s74, 0\n\t" "s_cselect_b32 s75, 0x100, 0\n\t" \
   "s_cbranch_scc1 88f\n\t" "s_sleep 3\n\t" "88:\n\t" "buffer_load_dwordx4 v[32:35], v16, %[rs], s50 offen offset:0 sc1\n\t" \
   "buffer_load_dwordx4 v[36:39], v16, %[rs], s50 offen offset:1024 sc1\n\t" "buffer_load_dwordx4 v[40:43], v16, %[rs], s50 offen offset:2048 sc1\n\t" \
   "buffer_load_dwordx4 v[44:47], v16, %[rs], s50 offen offset:3072 sc1\n\t" "buffer_load_dwordx4 v[48:51], v16, %[rs], s51 offen offset:0 sc1\n\t" \
   "buffer_load_dwordx4 v[52:55], v16, %[rs], s51 offen offset:1024 sc1\n\t" "buffer_load_dwordx4 v[56:59], v16, %[rs], s51 offen offset:2048 sc1\n\t" \
   "buffer_load_dwordx4 v[60:63], v16, %[rs], s51 offen offset:3072 sc1\n\t" "s_add_u32 s76, s76, s75\n\t" "s_add_u32 s74, s74, 1\n\t" \
   "s_waitcnt vmcnt(0)\n\t" "s_branch 87b\n\t" "86:\n\t" "v_mov_b32 v27, 0\n\t" "v_mov_b32 v28, 0\n\t" "v_mov_b32 v29, 0\n\t" "v_mov_b32 v30, 0\n\t" \
   "v_mov_b32 v31, 0\n\t" "v_max3_u32 v27, v27, v64, v66\n\t" "v_max3_u32 v27, v27, v68, v70\n\t" "v_max3_u32 v27, v27, v72, v74\n\t" \
   "v_max3_u32 v27, v27, v76, v78\n\t" "v_max3_u32 v27, v27, v80, v82\n\t" "v_max3_u32 v27, v27, v84, v86\n\t" "v_max3_u32 v27, v27, v88, v90\n\t" \
   "v_max3_u32 v27, v27, v92, v94\n\t" "v_max3_u32 v27, v27, v96, v98\n\t" "v_max3_u32 v27, v27, v100, v102\n\t" "v_max3_u32 v27, v27, v104, v106\n\t" \
   "v_max3_u32 v27, v27, v108, v110\n\t" "v_max3_u32 v27, v27, v112, v114\n\t" "v_max3_u32 v27, v27, v116, v118\n\t" \
   "v_max3_u32 v27, v27, v120, v122\n\t" "v_max3_u32 v27, v27, v124, v126\n\t" "v_max3_u32 v27, v27, v128, v130\n\t" \
   "v_max3_u32 v27, v27, v132, v134\n\t" "v_max3_u32 v27, v27, v136, v138\n\t" "v_max3_u32 v27, v27, v140, v142\n\t" "v_cmp_eq_u32 vcc, -1, v27\n\t" \
   "s_nop 0\n\t" "s_cbranch_vccnz 85f\n\t" "v_smfmac_f32_16x16x64_bf16 v[28:31], v[64:67], a[0:7], v22\n\t" \
   "v_smfmac_f32_16x16x64_bf16 v[28:31], v[68:71], a[8:15], v22\n\t" "v_smfmac_f32_16x16x64_bf16 v[28:31], v[72:75], a[16:23], v22\n\t" \
   "v_smfmac_f32_16x16x64_bf16 v[28:31], v[76:79], a[24:31], v22\n\t" "v_smfmac_f32_16x16x64_bf16 v[28:31], v[80:83], a[32:39], v22\n\t" \
   "v_smfmac_f32_16x16x64_bf16 v[28:31], v[84:87], a[40:47], v22\n\t" "v_smfmac_f32_16x16x64_bf16 v[28:31], v[88:91], a[48:55], v22\n\t" \
   "v_smfmac_f32_16x16x64_bf16 v[28:31], v[92:95], a[56:63], v22\n\t" "v_smfmac_f32_16x16x64_bf16 v[28:31], v[96:99], a[64:71], v22\n\t" \
   "v_smfmac_f32_16x16x64_bf16 v[28:31], v[100:103], a[72:79], v22\n\t" "v_smfmac_f32_16x16x64_bf16 v[28:31], v[104:107], a[80:87], v22\n\t" \
   "v_smfmac_f32_16x16x64_bf16 v[28:31], v[108:111], a[88:95], v22\n\t" "v_smfmac_f32_16x16x64_bf16 v[28:31], v[112:115], a[96:103], v22\n\t" \
   "v_smfmac_f32_16x16x64_bf16 v[28:31], v[116:119], a[104:111], v22\n\t" "v_smfmac_f32_16x16x64_bf16 v[28:31], v[120:123], a[112:119], v22\n\t" \
   "v_smfmac_f32_16x16x64_bf16 v[28:31], v[124:127], a[120:127], v22\n\t" "v_smfmac_f32_16x16x64_bf16 v[28:31], v[128:131], v[32:39], v22\n\t" \
   "v_smfmac_f32_16x16x64_bf16 v[28:31], v[132:135], v[40:47], v22\n\t" "v_smfmac_f32_16x16x64_bf16 v[28:31], v[136:139], v[48:55], v22\n\t" \
   "v_smfmac_f32_16x16x64_bf16 v[28:31], v[140:143], v[56:63], v22\n\t" "84:\n\t" "v_cmp_eq_u32 vcc, -1, v27\n\t" "s_cmp_gt_u32 s46, 0x10000\n\t" \
   "s_cbranch_scc1 83f\n\t" "s_cbranch_vccz 83f\n\t" "85:\n\t" "s_add_u32 s46, s46, 1\n\t" "s_cmp_gt_u32 s46, 2\n\t" "s_cbranch_scc1 81f\n\t" \
   "s_lshl_b32 s74, s46, 3\n\t" "s_lshl_b32 s74, 1, s74\n\t" "s_add_u32 s77, s77, s74\n\t" "81:\n\t" "s_min_u32 s74, s46, 7\n\t" \
   "s_lshr_b32 s64, s74, 1\n\t" "s_add_u32 s64, s64, %[xs]\n\t" "s_and_b32 s64, s64, 3\n\t" "s_lshl_b32 s64, s64, 11\n\t" "s_and_b32 s75, s74, 1\n\t" \
   "s_lshl_b32 s75, s75, 14\n\t" "s_xor_b32 s75, s75, s47\n\t" "s_xor_b32 s75, s75, %[lad0]\n\t" "s_and_b32 s75, s75, 0x4000\n\t" \
   "s_add_u32 s64, s64, s75\n\t" "s_add_u32 s64, s64, s41\n\t" "s_add_u32 s65, s64, 32768\n\t" "s_add_u32 s66, s64, 65536\n\t" \
   "s_add_u32 s67, s64, 98304\n\t" "s_add_u32 s68, s64, 131072\n\t" "s_add_u32 s69, s64, 163840\n\t" "s_add_u32 s70, s64, 196608\n\t" \
   "s_add_u32 s71, s64, 229376\n\t" "s_add_u32 s72, s64, 262144\n\t" "s_add_u32 s73, s64, 294912\n\t" "v_mov_b32 v28, 0\n\t" "v_mov_b32 v29, 0\n\t" \
   "v_mov_b32 v30, 0\n\t" "v_mov_b32 v31, 0\n\t" "v_mov_b32 v27, 0\n\t" "s_cmp_lt_u32 s46, 7\n\t" "s_cbranch_scc1 82f\n\t" "s_sleep 3\n\t" "82:\n\t" \
   RSTAMP(6) "buffer_load_dwordx4 v[64:67], v16, %[rs], s64 offen offset:0 sc1\n\t" \
   "buffer_load_dwordx4 v[68:71], v16, %[rs], s64 offen offset:1024 sc1\n\t" "buffer_load_dwordx4 v[72:75], v16, %[rs], s65 offen offset:0 sc1\n\t" \
   "buffer_load_dwordx4 v[76:79], v16, %[rs], s65 offen offset:1024 sc1\n\t" "buffer_load_dwordx4 v[80:83], v16, %[rs], s66 offen offset:0 sc1\n\t" \
   "buffer_load_dwordx4 v[84:87], v16, %[rs], s66 offen offset:1024 sc1\n\t" "buffer_load_dwordx4 v[88:91], v16, %[rs], s67 offen offset:0 sc1\n\t" \
   "buffer_load_dwordx4 v[92:95], v16, %[rs], s67 offen offset:1024 sc1\n\t" "buffer_load_dwordx4 v[96:99], v16, %[rs], s68 offen offset:0 sc1\n\t" \
   "buffer_load_dwordx4 v[100:103], v16, %[rs], s68 offen offset:1024 sc1\n\t" "buffer_load_dwordx4 v[104:107], v16, %[rs], s69 offen offset:0 sc1\n\t" \
   "buffer_load_dwordx4 v[108:111], v16, %[rs], s69 offen offset:1024 sc1\n\t" "buffer_load_dwordx4 v[112:115], v16, %[rs], s70 offen offset:0 sc1\n\t" \
   "buffer_load_dwordx4 v[116:119], v16, %[rs], s70 offen offset:1024 sc1\n\t" "buffer_load_dwordx4 v[120:123], v16, %[rs], s71 offen offset:0 sc1\n\t" \
   "buffer_load_dwordx4 v[124:127], v16, %[rs], s71 offen offset:1024 sc1\n\t" "buffer_load_dwordx2 v[128:129], v17, %[rs], s72 offen offset:0 sc1\n\t" \
   "buffer_load_dwordx2 v[130:131], v17, %[rs], s72 offen offset:128 sc1\n\t" \
   "buffer_load_dwordx2 v[132:133], v17, %[rs], s72 offen offset:1024 sc1\n\t" \
   "buffer_load_dwordx2 v[134:135], v17, %[rs], s72 offen offset:1152 sc1\n\t" "buffer_load_dwordx2 v[136:137], v17, %[rs], s73 offen offset:0 sc1\n\t" \
   "buffer_load_dwordx2 v[138:139], v17, %[rs], s73 offen offset:128 sc1\n\t" \
   "buffer_load_dwordx2 v[140:141], v17, %[rs], s73 offen offset:1024 sc1\n\t" \
   "buffer_load_dwordx2 v[142:143], v17, %[rs], s73 offen offset:1152 sc1\n\t" "s_waitcnt vmcnt(23)\n\t" \
   "v_smfmac_f32_16x16x64_bf16 v[28:31], v[64:67], a[0:7], v22\n\t" "v_max3_u32 v27, v27, v64, v66\n\t" "s_waitcnt vmcnt(22)\n\t" \
   "v_smfmac_f32_16x16x64_bf16 v[28:31], v[68:71], a[8:15], v22\n\t" "v_max3_u32 v27, v27, v68, v70\n\t" "s_waitcnt vmcnt(21)\n\t" \
   "v_smfmac_f32_16x16x64_bf16 v[28:31], v[72:75], a[16:23], v22\n\t" "v_max3_u32 v27, v27, v72, v74\n\t" "s_waitcnt vmcnt(20)\n\t" \
   "v_smfmac_f32_16x16x64_bf16 v[28:31], v[76:79], a[24:31], v22\n\t" "v_max3_u32 v27, v27, v76, v78\n\t" "s_waitcnt vmcnt(19)\n\t" \
   "v_smfmac_f32_16x16x64_bf16 v[28:31], v[80:83], a[32:39], v22\n\t" "v_max3_u32 v27, v27, v80, v82\n\t" "s_waitcnt vmcnt(18)\n\t" \
   "v_smfmac_f32_16x16x64_bf16 v[28:31], v[84:87], a[40:47], v22\n\t" "v_max3_u32 v27, v27, v84, v86\n\t" "s_waitcnt vmcnt(17)\n\t" \
   "v_smfmac_f32_16x16x64_bf16 v[28:31], v[88:91], a[48:55], v22\n\t" "v_max3_u32 v27, v27, v88, v90\n\t" "s_waitcnt vmcnt(16)\n\t" \
   "v_smfmac_f32_16x16x64_bf16 v[28:31], v[92:95], a[56:63], v22\n\t" "v_max3_u32 v27, v27, v92, v94\n\t" "s_waitcnt vmcnt(15)\n\t" \
   "v_smfmac_f32_16x16x64_bf16 v[28:31], v[96:99], a[64:71], v22\n\t" "v_max3_u32 v27, v27, v96, v98\n\t" "s_waitcnt vmcnt(14)\n\t" \
   "v_smfmac_f32_16x16x64_bf16 v[28:31], v[100:103], a[72:79], v22\n\t" "v_max3_u32 v27, v27, v100, v102\n\t" "s_waitcnt vmcnt(13)\n\t" \
   "v_smfmac_f32_16x16x64_bf16 v[28:31], v[104:107], a[80:87], v22\n\t" "v_max3_u32 v27, v27, v104, v106\n\t" "s_waitcnt vmcnt(12)\n\t" \
   "v_smfmac_f32_16x16x64_bf16 v[28:31], v[108:111], a[88:95], v22\n\t" "v_max3_u32 v27, v27, v108, v110\n\t" "s_waitcnt vmcnt(11)\n\t" \
   "v_smfmac_f32_16x16x64_bf16 v[28:31], v[112:115], a[96:103], v22\n\t" "v_max3_u32 v27, v27, v112, v114\n\t" "s_waitcnt vmcnt(10)\n\t" \
   "v_smfmac_f32_16x16x64_bf16 v[28:31], v[116:119], a[104:111], v22\n\t" "v_max3_u32 v27, v27, v116, v118\n\t" "s_waitcnt vmcnt(9)\n\t" \
   "v_smfmac_f32_16x16x64_bf16 v[28:31], v[120:123], a[112:119], v22\n\t" "v_max3_u32 v27, v27, v120, v122\n\t" "s_waitcnt vmcnt(8)\n\t" \
   "v_smfmac_f32_16x16x64_bf16 v[28:31], v[124:127], a[120:127], v22\n\t" "v_max3_u32 v27, v27, v124, v126\n\t" "s_waitcnt vmcnt(6)\n\t" \
   "v_smfmac_f32_16x16x64_bf16 v[28:31], v[128:131], v[32:39], v22\n\t" "v_max3_u32 v27, v27, v128, v130\n\t" "s_waitcnt vmcnt(4)\n\t" \
   "v_smfmac_f32_16x16x64_bf16 v[28:31], v[132:135], v[40:47], v22\n\t" "v_max3_u32 v27, v27, v132, v134\n\t" "s_waitcnt vmcnt(2)\n\t" \
   "v_smfmac_f32_16x16x64_bf16 v[28:31], v[136:139], v[48:55], v22\n\t" "v_max3_u32 v27, v27, v136, v138\n\t" "s_waitcnt vmcnt(0)\n\t" \
   "v_smfmac_f32_16x16x64_bf16 v[28:31], v[140:143], v[56:63], v22\n\t" "v_max3_u32 v27, v27, v140, v142\n\t" "s_branch 84b\n\t" "83:\n\t" \
   "s_or_b32 s46, s46, 0x10000000\n\t" "s_nop 4\n\t" "v_add_f32 v28, v28, v29\n\t" "v_add_f32 v30, v30, v31\n\t" "v_cvt_pk_bf16_f32 v24, v28, v30\n\t" \
   "s_nop 1\n\t" "v_mov_b32_dpp v25, v24 quad_perm:[1,0,3,2] row_mask:0xf bank_mask:0xf\n\t" "s_branch 2b\n\t" "95:\n\t" "s_mov_b32 s74, 0\n\t" \
   "97:\n\t" "buffer_load_dwordx4 v[8:11], v19, %[rs], s52 offen sc1\n\t" "s_add_u32 s74, s74, 1\n\t" "s_waitcnt vmcnt(0)\n\t" \
   "v_max3_u32 v15, v8, v9, v10\n\t" "v_max_u32 v15, v15, v11\n\t" "v_cmp_eq_u32 vcc, -1, v15\n\t" "s_cmp_gt_u32 s74, 0x10000\n\t" \
   "s_cbranch_scc1 96b\n\t" "s_cbranch_vccz 96b\n\t" "s_sleep 2\n\t" "s_branch 97b\n\t" "90:\n\t" "s_waitcnt vmcnt(0) lgkmcnt(0)"
#define FIN_CLOB "v8", "v9", "v10", "v11", "v12", "v13", "v14", "v15", "v24", "v25", "v26", "v27", "v28", "v29", "v30", "v31", "v32", "v33", "v34", "v35", "v36", "v37", "v38", "v39", "v40", "v41", "v42", "v43", "v44", "v45", "v46", "v47", "v48", "v49", "v50", "v51", "v52", "v53", "v54", "v55", "v56", "v57", "v58", "v59", "v60", "v61", "v62", "v63", "v64", "v65", "v66", "v67", "v68", "v69", "v70", "v71", "v72", "v73", "v74", "v75", "v76", "v77", "v78", "v79", "v80", "v81", "v82", "v83", "v84", "v85", "v86", "v87", "v88", "v89", "v90", "v91", "v92", "v93", "v94", "v95", "v96", "v97", "v98", "v99", "v100", "v101", "v102", "v103", "v104", "v105", "v106", "v107", "v108", "v109", "v110", "v111", "v112", "v113", "v114", "v115", "v116", "v117", "v118", "v119", "v120", "v121", "v122", "v123", "v124", "v125", "v126", "v127", "v128", "v129", "v130", "v131", "v132", "v133", "v134", "v135", "v136", "v137", "v138", "v139", "v140", "v141", "v142", "v143", "v144", "v145", "v146", "v147", "v148", "v149", "v150", "v151", "v152", "v153", "v154", "v155", "v156", "v157", "v158", "v159", "v160", "v161", "v162", "v163", "v164", "v165", "v166", "v167", "v168", "v169", "v170", "v171", "v172", "v173", "v174", "v175", "v176", "v177", "v178", "v179", "v180", "v181", "v182", "v183", "v184", "v185", "v186", "v187", "v188", "v189", "v190", "v191", "v192", "v193", "v194", "v195", "v196", "v197", "v198", "v199", "v200", "v201", "v202", "v203", "v204", "v205", "v206", "v207", "v208", "v209", "v210", "v211", "v212", "v213", "v214", "v215", "v216", "v217", "v218", "v219", "v220", "v221", "v222", "v223", "v224", "v225", "v226", "v227", "s40", "s41", "s42", "s43", "s44", "s45", "s46", "s47", "s48", "s49", "s50", "s51", "s52", "s53", "s60", "s61", "s62", "s63", "s64", "s65", "s66", "s67", "s68", "s69", "s70", "s71", "s72", "s73", "s74", "s75", "a0", "a1", "a2", "a3", "a4", "a5", "a6", "a7", "a8", "a9", "a10", "a11", "a12", "a13", "a14", "a15", "a16", "a17", "a18", "a19", "a20", "a21", "a22", "a23", "a24", "a25", "a26", "a27", "a28", "a29", "a30", "a31", "a32", "a33", "a34", "a35", "a36", "a37", "a38", "a39", "a40", "a41", "a42", "a43", "a44", "a45", "a46", "a47", "a48", "a49", "a50", "a51", "a52", "a53", "a54", "a55", "a56", "a57", "a58", "a59", "a60", "a61", "a62", "a63", "a64", "a65", "a66", "a67", "a68", "a69", "a70", "a71", "a72", "a73", "a74", "a75", "a76", "a77", "a78", "a79", "a80", "a81", "a82", "a83", "a84", "a85", "a86", "a87", "a88", "a89", "a90", "a91", "a92", "a93", "a94", "a95", "a96", "a97", "a98", "a99", "a100", "a101", "a102", "a103", "a104", "a105", "a106", "a107", "a108", "a109", "a110", "a111", "a112", "a113", "a114", "a115", "a116", "a117", "a118", "a119", "a120", "a121", "a122", "a123", "a124", "a125", "a126", "a127"
typedef unsigned v16u __attribute__((ext_vector_type(16)));
typedef unsigned v32u __attribute__((ext_vector_type(32)));
__device__ __forceinline__ void put4(v32u& d, int i, v4u v) { d[4 * i] = v.x; d[4 * i + 1] = v.y; d[4 * i + 2] = v.z; d[4 * i + 3] = v.w; }
#define LDP_ASM \
   "s_mov_b32 s41, 0\n\t" "buffer_load_dwordx4 a[0:3], v16, %[ra], s41 offen\n\t" "s_mov_b32 s41, 131072\n\t" \
   "buffer_load_dwordx4 a[64:67], v16, %[ra], s41 offen\n\t" "s_mov_b32 s41, 524288\n\t" "buffer_load_dwordx4 a[8:11], v16, %[ra], s41 offen\n\t" \
   "s_mov_b32 s41, 655360\n\t" "buffer_load_dwordx4 a[72:75], v16, %[ra], s41 offen\n\t" "s_mov_b32 s41, 1048576\n\t" \
   "buffer_load_dwordx4 a[16:19], v16, %[ra], s41 offen\n\t" "s_mov_b32 s41, 1179648\n\t" "buffer_load_dwordx4 a[80:83], v16, %[ra], s41 offen\n\t" \
   "s_mov_b32 s41, 1572864\n\t" "buffer_load_dwordx4 a[24:27], v16, %[ra], s41 offen\n\t" "s_mov_b32 s41, 1703936\n\t" \
   "buffer_load_dwordx4 a[88:91], v16, %[ra], s41 offen\n\t" "s_mov_b32 s41, 2097152\n\t" "buffer_load_dwordx4 a[32:35], v16, %[ra], s41 offen\n\t" \
   "s_mov_b32 s41, 2228224\n\t" "buffer_load_dwordx4 a[96:99], v16, %[ra], s41 offen\n\t" "s_mov_b32 s41, 2621440\n\t" \
   "buffer_load_dwordx4 a[40:43], v16, %[ra], s41 offen\n\t" "s_mov_b32 s41, 2752512\n\t" "buffer_load_dwordx4 a[104:107], v16, %[ra], s41 offen\n\t" \
   "s_mov_b32 s41, 3145728\n\t" "buffer_load_dwordx4 a[48:51], v16, %[ra], s41 offen\n\t" "s_mov_b32 s41, 3276800\n\t" \
   "buffer_load_dwordx4 a[112:115], v16, %[ra], s41 offen\n\t" "s_mov_b32 s41, 3670016\n\t" "buffer_load_dwordx4 a[56:59], v16, %[ra], s41 offen\n\t" \
   "s_mov_b32 s41, 3801088\n\t" "buffer_load_dwordx4 a[120:123], v16, %[ra], s41 offen\n\t" "s_mov_b32 s41, 4194304\n\t" \
   "buffer_load_dwordx4 v[192:195], v16, %[ra], s41 offen\n\t" "s_mov_b32 s41, 4325376\n\t" "buffer_load_dwordx4 v[208:211], v16, %[ra], s41 offen\n\t" \
   "s_mov_b32 s41, 4718592\n\t" "buffer_load_dwordx4 v[200:203], v16, %[ra], s41 offen\n\t" "s_mov_b32 s41, 4849664\n\t" \
   "buffer_load_dwordx4 v[216:219], v16, %[ra], s41 offen\n\t" "s_mov_b32 s41, 0\n\t" "buffer_load_dwordx4 a[128:131], v16, %[rb], s41 offen\n\t" \
   "s_mov_b32 s41, 131072\n\t" "buffer_load_dwordx4 a[192:195], v16, %[rb], s41 offen\n\t" "s_mov_b32 s41, 524288\n\t" \
   "buffer_load_dwordx4 a[136:139], v16, %[rb], s41 offen\n\t" "s_mov_b32 s41, 655360\n\t" "buffer_load_dwordx4 a[200:203], v16, %[rb], s41 offen\n\t" \
   "s_mov_b32 s41, 1048576\n\t" "buffer_load_dwordx4 a[144:147], v16, %[rb], s41 offen\n\t" "s_mov_b32 s41, 1179648\n\t" \
   "buffer_load_dwordx4 a[208:211], v16, %[rb], s41 offen\n\t" "s_mov_b32 s41, 1572864\n\t" "buffer_load_dwordx4 a[152:155], v16, %[rb], s41 offen\n\t" \
   "s_mov_b32 s41, 1703936\n\t" "buffer_load_dwordx4 a[216:219], v16, %[rb], s41 offen\n\t" "s_mov_b32 s41, 2097152\n\t" \
   "buffer_load_dwordx4 a[160:163], v16, %[rb], s41 offen\n\t" "s_mov_b32 s41, 2228224\n\t" "buffer_load_dwordx4 a[224:227], v16, %[rb], s41 offen\n\t" \
   "s_mov_b32 s41, 2621440\n\t" "buffer_load_dwordx4 a[168:171], v16, %[rb], s41 offen\n\t" "s_mov_b32 s41, 2752512\n\t" \
   "buffer_load_dwordx4 a[232:235], v16, %[rb], s41 offen\n\t" "s_mov_b32 s41, 3145728\n\t" "buffer_load_dwordx4 a[176:179], v16, %[rb], s41 offen\n\t" \
   "s_mov_b32 s41, 3276800\n\t" "buffer_load_dwordx4 a[240:243], v16, %[rb], s41 offen\n\t" "s_mov_b32 s41, 3670016\n\t" \
   "buffer_load_dwordx4 a[184:187], v16, %[rb], s41 offen\n\t" "s_mov_b32 s41, 3801088\n\t" "buffer_load_dwordx4 a[248:251], v16, %[rb], s41 offen\n\t" \
   "s_mov_b32 s41, 4194304\n\t" "buffer_load_dwordx4 v[224:227], v16, %[rb], s41 offen\n\t" "s_mov_b32 s41, 4325376\n\t" \
   "buffer_load_dwordx4 v[240:243], v16, %[rb], s41 offen\n\t" "s_mov_b32 s41, 4718592\n\t" "buffer_load_dwordx4 v[232:235], v16, %[rb], s41 offen\n\t" \
   "s_mov_b32 s41, 4849664\n\t" "buffer_load_dwordx4 v[248:251], v16, %[rb], s41 offen\n\t" "s_mov_b32 s41, 262144\n\t" \
   "buffer_load_dwordx4 a[4:7], v16, %[ra], s41 offen\n\t" "s_mov_b32 s41, 393216\n\t" "buffer_load_dwordx4 a[68:71], v16, %[ra], s41 offen\n\t" \
   "s_mov_b32 s41, 786432\n\t" "buffer_load_dwordx4 a[12:15], v16, %[ra], s41 offen\n\t" "s_mov_b32 s41, 917504\n\t" \
   "buffer_load_dwordx4 a[76:79], v16, %[ra], s41 offen\n\t" "s_mov_b32 s41, 1310720\n\t" "buffer_load_dwordx4 a[20:23], v16, %[ra], s41 offen\n\t" \
   "s_mov_b32 s41, 1441792\n\t" "buffer_load_dwordx4 a[84:87], v16, %[ra], s41 offen\n\t" "s_mov_b32 s41, 1835008\n\t" \
   "buffer_load_dwordx4 a[28:31], v16, %[ra], s41 offen\n\t" "s_mov_b32 s41, 1966080\n\t" "buffer_load_dwordx4 a[92:95], v16, %[ra], s41 offen\n\t" \
   "s_mov_b32 s41, 2359296\n\t" "buffer_load_dwordx4 a[36:39], v16, %[ra], s41 offen\n\t" "s_mov_b32 s41, 2490368\n\t" \
   "buffer_load_dwordx4 a[100:103], v16, %[ra], s41 offen\n\t" "s_mov_b32 s41, 2883584\n\t" "buffer_load_dwordx4 a[44:47], v16, %[ra], s41 offen\n\t" \
   "s_mov_b32 s41, 3014656\n\t" "buffer_load_dwordx4 a[108:111], v16, %[ra], s41 offen\n\t" "s_mov_b32 s41, 3407872\n\t" \
   "buffer_load_dwordx4 a[52:55], v16, %[ra], s41 offen\n\t" "s_mov_b32 s41, 3538944\n\t" "buffer_load_dwordx4 a[116:119], v16, %[ra], s41 offen\n\t" \
   "s_mov_b32 s41, 3932160\n\t" "buffer_load_dwordx4 a[60:63], v16, %[ra], s41 offen\n\t" "s_mov_b32 s41, 4063232\n\t" \
   "buffer_load_dwordx4 a[124:127], v16, %[ra], s41 offen\n\t" "s_mov_b32 s41, 4456448\n\t" "buffer_load_dwordx4 v[196:199], v16, %[ra], s41 offen\n\t" \
   "s_mov_b32 s41, 4587520\n\t" "buffer_load_dwordx4 v[212:215], v16, %[ra], s41 offen\n\t" "s_mov_b32 s41, 4980736\n\t" \
   "buffer_load_dwordx4 v[204:207], v16, %[ra], s41 offen\n\t" "s_mov_b32 s41, 5111808\n\t" "buffer_load_dwordx4 v[220:223], v16, %[ra], s41 offen\n\t" \
   "s_waitcnt vmcnt(59)\n\t" "ds_write_b128 v20, a[0:3] offset:0\n\t" "s_mov_b32 s41, 262144\n\t" \
   "buffer_load_dwordx4 a[132:135], v16, %[rb], s41 offen\n\t" "s_waitcnt vmcnt(59)\n\t" "ds_write_b128 v20, a[64:67] offset:4096\n\t" \
   "s_mov_b32 s41, 393216\n\t" "buffer_load_dwordx4 a[196:199], v16, %[rb], s41 offen\n\t" "s_waitcnt vmcnt(59)\n\t" \
   "ds_write_b128 v20, a[8:11] offset:8192\n\t" "s_mov_b32 s41, 786432\n\t" "buffer_load_dwordx4 a[140:143], v16, %[rb], s41 offen\n\t" \
   "s_waitcnt vmcnt(59)\n\t" "ds_write_b128 v20, a[72:75] offset:12288\n\t" "s_mov_b32 s41, 917504\n\t" \
   "buffer_load_dwordx4 a[204:207], v16, %[rb], s41 offen\n\t" "s_waitcnt vmcnt(59)\n\t" "ds_write_b128 v20, a[16:19] offset:16384\n\t" \
   "s_mov_b32 s41, 1310720\n\t" "buffer_load_dwordx4 a[148:151], v16, %[rb], s41 offen\n\t" "s_waitcnt vmcnt(59)\n\t" \
   "ds_write_b128 v20, a[80:83] offset:20480\n\t" "s_mov_b32 s41, 1441792\n\t" "buffer_load_dwordx4 a[212:215], v16, %[rb], s41 offen\n\t" \
   "s_waitcnt vmcnt(59)\n\t" "ds_write_b128 v20, a[24:27] offset:24576\n\t" "s_mov_b32 s41, 1835008\n\t" \
   "buffer_load_dwordx4 a[156:159], v16, %[rb], s41 offen\n\t" "s_waitcnt vmcnt(59)\n\t" "ds_write_b128 v20, a[88:91] offset:28672\n\t" \
   "s_mov_b32 s41, 1966080\n\t" "buffer_load_dwordx4 a[220:223], v16, %[rb], s41 offen\n\t" "s_waitcnt vmcnt(59)\n\t" \
   "ds_write_b128 v20, a[32:35] offset:32768\n\t" "s_mov_b32 s41, 2359296\n\t" "buffer_load_dwordx4 a[164:167], v16, %[rb], s41 offen\n\t" \
   "s_waitcnt vmcnt(59)\n\t" "ds_write_b128 v20, a[96:99] offset:36864\n\t" "s_mov_b32 s41, 2490368\n\t" \
   "buffer_load_dwordx4 a[228:231], v16, %[rb], s41 offen\n\t" "s_waitcnt vmcnt(59)\n\t" "ds_write_b128 v20, a[40:43] offset:40960\n\t" \
   "s_mov_b32 s41, 2883584\n\t" "buffer_load_dwordx4 a[172:175], v16, %[rb], s41 offen\n\t" "s_waitcnt vmcnt(59)\n\t" \
   "ds_write_b128 v20, a[104:107] offset:45056\n\t" "s_mov_b32 s41, 3014656\n\t" "buffer_load_dwordx4 a[236:239], v16, %[rb], s41 offen\n\t" \
   "s_waitcnt vmcnt(59)\n\t" "ds_write_b128 v20, a[48:51] offset:49152\n\t" "s_mov_b32 s41, 3407872\n\t" \
   "buffer_load_dwordx4 a[180:183], v16, %[rb], s41 offen\n\t" "s_waitcnt vmcnt(59)\n\t" "ds_write_b128 v20, a[112:115] offset:53248\n\t" \
   "s_mov_b32 s41, 3538944\n\t" "buffer_load_dwordx4 a[244:247], v16, %[rb], s41 offen\n\t" "s_waitcnt vmcnt(59)\n\t" \
   "ds_write_b128 v20, a[56:59] offset:57344\n\t" "s_mov_b32 s41, 3932160\n\t" "buffer_load_dwordx4 a[188:191], v16, %[rb], s41 offen\n\t" \
   "s_waitcnt vmcnt(59)\n\t" "ds_write_b128 v20, a[120:123] offset:61440\n\t" "s_mov_b32 s41, 4063232\n\t" \
   "buffer_load_dwordx4 a[252:255], v16, %[rb], s41 offen\n\t" "s_waitcnt vmcnt(59)\n\t" "ds_write_b128 v23, v[192:195] offset:0\n\t" \
   "s_mov_b32 s41, 4456448\n\t" "buffer_load_dwordx4 v[228:231], v16, %[rb], s41 offen\n\t" "s_waitcnt vmcnt(59)\n\t" \
   "ds_write_b128 v23, v[208:211] offset:4096\n\t" "s_mov_b32 s41, 4587520\n\t" "buffer_load_dwordx4 v[244:247], v16, %[rb], s41 offen\n\t" \
   "s_waitcnt vmcnt(59)\n\t" "ds_write_b128 v23, v[200:203] offset:8192\n\t" "s_mov_b32 s41, 4980736\n\t" \
   "buffer_load_dwordx4 v[236:239], v16, %[rb], s41 offen\n\t" "s_waitcnt vmcnt(59)\n\t" "ds_write_b128 v23, v[216:219] offset:12288\n\t" \
   "s_mov_b32 s41, 5111808\n\t" "buffer_load_dwordx4 v[252:255], v16, %[rb], s41 offen\n\t" "s_waitcnt lgkmcnt(0)\n\t" "s_barrier\n\t" \
   "s_lshl_b32 s40, %[p0], 13\n\t" "v_add_u32 v24, s40, v18\n\t" "v_add_u32 v25, s40, v19\n\t" "s_add_u32 s40, s40, 8192\n\t" \
   "s_cmp_eq_u32 s40, 81920\n\t" "s_cselect_b32 s40, 0, s40\n\t" "ds_read_b64_tr_b16 a[0:1], v24 offset:0\n\t" \
   "ds_read_b64_tr_b16 a[2:3], v24 offset:4096\n\t" "ds_read_b64_tr_b16 a[64:65], v25 offset:0\n\t" "ds_read_b64_tr_b16 a[66:67], v25 offset:4096\n\t" \
   "v_add_u32 v24, s40, v18\n\t" "v_add_u32 v25, s40, v19\n\t" "s_add_u32 s40, s40, 8192\n\t" "s_cmp_eq_u32 s40, 81920\n\t" \
   "s_cselect_b32 s40, 0, s40\n\t" "ds_read_b64_tr_b16 a[8:9], v24 offset:0\n\t" "ds_read_b64_tr_b16 a[10:11], v24 offset:4096\n\t" \
   "ds_read_b64_tr_b16 a[72:73], v25 offset:0\n\t" "ds_read_b64_tr_b16 a[74:75], v25 offset:4096\n\t" "v_add_u32 v24, s40, v18\n\t" \
   "v_add_u32 v25, s40, v19\n\t" "s_add_u32 s40, s40, 8192\n\t" "s_cmp_eq_u32 s40, 81920\n\t" "s_cselect_b32 s40, 0, s40\n\t" \
   "ds_read_b64_tr_b16 a[16:17], v24 offset:0\n\t" "ds_read_b64_tr_b16 a[18:19], v24 offset:4096\n\t" "ds_read_b64_tr_b16 a[80:81], v25 offset:0\n\t" \
   "ds_read_b64_tr_b16 a[82:83], v25 offset:4096\n\t" "v_add_u32 v24, s40, v18\n\t" "v_add_u32 v25, s40, v19\n\t" "s_add_u32 s40, s40, 8192\n\t" \
   "s_cmp_eq_u32 s40, 81920\n\t" "s_cselect_b32 s40, 0, s40\n\t" "ds_read_b64_tr_b16 a[24:25], v24 offset:0\n\t" \
   "ds_read_b64_tr_b16 a[26:27], v24 offset:4096\n\t" "ds_read_b64_tr_b16 a[88:89], v25 offset:0\n\t" \
   "ds_read_b64_tr_b16 a[90:91], v25 offset:4096\n\t" "v_add_u32 v24, s40, v18\n\t" "v_add_u32 v25, s40, v19\n\t" "s_add_u32 s40, s40, 8192\n\t" \
   "s_cmp_eq_u32 s40, 81920\n\t" "s_cselect_b32 s40, 0, s40\n\t" "ds_read_b64_tr_b16 a[32:33], v24 offset:0\n\t" \
   "ds_read_b64_tr_b16 a[34:35], v24 offset:4096\n\t" "ds_read_b64_tr_b16 a[96:97], v25 offset:0\n\t" \
   "ds_read_b64_tr_b16 a[98:99], v25 offset:4096\n\t" "v_add_u32 v24, s40, v18\n\t" "v_add_u32 v25, s40, v19\n\t" "s_add_u32 s40, s40, 8192\n\t" \
   "s_cmp_eq_u32 s40, 81920\n\t" "s_cselect_b32 s40, 0, s40\n\t" "ds_read_b64_tr_b16 a[40:41], v24 offset:0\n\t" \
   "ds_read_b64_tr_b16 a[42:43], v24 offset:4096\n\t" "ds_read_b64_tr_b16 a[104:105], v25 offset:0\n\t" \
   "ds_read_b64_tr_b16 a[106:107], v25 offset:4096\n\t" "v_add_u32 v24, s40, v18\n\t" "v_add_u32 v25, s40, v19\n\t" "s_add_u32 s40, s40, 8192\n\t" \
   "s_cmp_eq_u32 s40, 81920\n\t" "s_cselect_b32 s40, 0, s40\n\t" "ds_read_b64_tr_b16 a[48:49], v24 offset:0\n\t" \
   "ds_read_b64_tr_b16 a[50:51], v24 offset:4096\n\t" "ds_read_b64_tr_b16 a[112:113], v25 offset:0\n\t" \
   "ds_read_b64_tr_b16 a[114:115], v25 offset:4096\n\t" "v_add_u32 v24, s40, v18\n\t" "v_add_u32 v25, s40, v19\n\t" "s_add_u32 s40, s40, 8192\n\t" \
   "s_cmp_eq_u32 s40, 81920\n\t" "s_cselect_b32 s40, 0, s40\n\t" "ds_read_b64_tr_b16 a[56:57], v24 offset:0\n\t" \
   "ds_read_b64_tr_b16 a[58:59], v24 offset:4096\n\t" "ds_read_b64_tr_b16 a[120:121], v25 offset:0\n\t" \
   "ds_read_b64_tr_b16 a[122:123], v25 offset:4096\n\t" "v_add_u32 v24, s40, v18\n\t" "v_add_u32 v25, s40, v19\n\t" "s_add_u32 s40, s40, 8192\n\t" \
   "s_cmp_eq_u32 s40, 81920\n\t" "s_cselect_b32 s40, 0, s40\n\t" "ds_read_b64_tr_b16 v[192:193], v24 offset:0\n\t" \
   "ds_read_b64_tr_b16 v[194:195], v24 offset:4096\n\t" "ds_read_b64_tr_b16 v[208:209], v25 offset:0\n\t" \
   "ds_read_b64_tr_b16 v[210:211], v25 offset:4096\n\t" "v_add_u32 v24, s40, v18\n\t" "v_add_u32 v25, s40, v19\n\t" "s_add_u32 s40, s40, 8192\n\t" \
   "s_cmp_eq_u32 s40, 81920\n\t" "s_cselect_b32 s40, 0, s40\n\t" "ds_read_b64_tr_b16 v[200:201], v24 offset:0\n\t" \
   "ds_read_b64_tr_b16 v[202:203], v24 offset:4096\n\t" "ds_read_b64_tr_b16 v[216:217], v25 offset:0\n\t" \
   "ds_read_b64_tr_b16 v[218:219], v25 offset:4096\n\t" "s_waitcnt vmcnt(59)\n\t" "s_waitcnt lgkmcnt(0)\n\t" "s_barrier\n\t" \
   "ds_write_b128 v20, a[128:131] offset:0\n\t" "s_waitcnt vmcnt(58)\n\t" "ds_write_b128 v20, a[192:195] offset:4096\n\t" "s_waitcnt vmcnt(57)\n\t" \
   "ds_write_b128 v20, a[136:139] offset:8192\n\t" "s_waitcnt vmcnt(56)\n\t" "ds_write_b128 v20, a[200:203] offset:12288\n\t" "s_waitcnt vmcnt(55)\n\t" \
   "ds_write_b128 v20, a[144:147] offset:16384\n\t" "s_waitcnt vmcnt(54)\n\t" "ds_write_b128 v20, a[208:211] offset:20480\n\t" \
   "s_waitcnt vmcnt(53)\n\t" "ds_write_b128 v20, a[152:155] offset:24576\n\t" "s_waitcnt vmcnt(52)\n\t" \
   "ds_write_b128 v20, a[216:219] offset:28672\n\t" "s_waitcnt vmcnt(51)\n\t" "ds_write_b128 v20, a[160:163] offset:32768\n\t" \
   "s_waitcnt vmcnt(50)\n\t" "ds_write_b128 v20, a[224:227] offset:36864\n\t" "s_waitcnt vmcnt(49)\n\t" \
   "ds_write_b128 v20, a[168:171] offset:40960\n\t" "s_waitcnt vmcnt(48)\n\t" "ds_write_b128 v20, a[232:235] offset:45056\n\t" \
   "s_waitcnt vmcnt(47)\n\t" "ds_write_b128 v20, a[176:179] offset:49152\n\t" "s_waitcnt vmcnt(46)\n\t" \
   "ds_write_b128 v20, a[240:243] offset:53248\n\t" "s_waitcnt vmcnt(45)\n\t" "ds_write_b128 v20, a[184:187] offset:57344\n\t" \
   "s_waitcnt vmcnt(44)\n\t" "ds_write_b128 v20, a[248:251] offset:61440\n\t" "s_waitcnt vmcnt(43)\n\t" "ds_write_b128 v23, v[224:227] offset:0\n\t" \
   "s_waitcnt vmcnt(42)\n\t" "ds_write_b128 v23, v[240:243] offset:4096\n\t" "s_waitcnt vmcnt(41)\n\t" "ds_write_b128 v23, v[232:235] offset:8192\n\t" \
   "s_waitcnt vmcnt(40)\n\t" "ds_write_b128 v23, v[248:251] offset:12288\n\t" "s_waitcnt lgkmcnt(0)\n\t" "s_barrier\n\t" \
   "s_lshl_b32 s40, %[p0], 13\n\t" "v_add_u32 v24, s40, v18\n\t" "v_add_u32 v25, s40, v19\n\t" "s_add_u32 s40, s40, 8192\n\t" \
   "s_cmp_eq_u32 s40, 81920\n\t" "s_cselect_b32 s40, 0, s40\n\t" "ds_read_b64_tr_b16 a[128:129], v24 offset:0\n\t" \
   "ds_read_b64_tr_b16 a[130:131], v24 offset:4096\n\t" "ds_read_b64_tr_b16 a[192:193], v25 offset:0\n\t" \
   "ds_read_b64_tr_b16 a[194:195], v25 offset:4096\n\t" "v_add_u32 v24, s40, v18\n\t" "v_add_u32 v25, s40, v19\n\t" "s_add_u32 s40, s40, 8192\n\t" \
   "s_cmp_eq_u32 s40, 81920\n\t" "s_cselect_b32 s40, 0, s40\n\t" "ds_read_b64_tr_b16 a[136:137], v24 offset:0\n\t" \
   "ds_read_b64_tr_b16 a[138:139], v24 offset:4096\n\t" "ds_read_b64_tr_b16 a[200:201], v25 offset:0\n\t" \
   "ds_read_b64_tr_b16 a[202:203], v25 offset:4096\n\t" "v_add_u32 v24, s40, v18\n\t" "v_add_u32 v25, s40, v19\n\t" "s_add_u32 s40, s40, 8192\n\t" \
   "s_cmp_eq_u32 s40, 81920\n\t" "s_cselect_b32 s40, 0, s40\n\t" "ds_read_b64_tr_b16 a[144:145], v24 offset:0\n\t" \
   "ds_read_b64_tr_b16 a[146:147], v24 offset:4096\n\t" "ds_read_b64_tr_b16 a[208:209], v25 offset:0\n\t" \
   "ds_read_b64_tr_b16 a[210:211], v25 offset:4096\n\t" "v_add_u32 v24, s40, v18\n\t" "v_add_u32 v25, s40, v19\n\t" "s_add_u32 s40, s40, 8192\n\t" \
   "s_cmp_eq_u32 s40, 81920\n\t" "s_cselect_b32 s40, 0, s40\n\t" "ds_read_b64_tr_b16 a[152:153], v24 offset:0\n\t" \
   "ds_read_b64_tr_b16 a[154:155], v24 offset:4096\n\t" "ds_read_b64_tr_b16 a[216:217], v25 offset:0\n\t" \
   "ds_read_b64_tr_b16 a[218:219], v25 offset:4096\n\t" "v_add_u32 v24, s40, v18\n\t" "v_add_u32 v25, s40, v19\n\t" "s_add_u32 s40, s40, 8192\n\t" \
   "s_cmp_eq_u32 s40, 81920\n\t" "s_cselect_b32 s40, 0, s40\n\t" "ds_read_b64_tr_b16 a[160:161], v24 offset:0\n\t" \
   "ds_read_b64_tr_b16 a[162:163], v24 offset:4096\n\t" "ds_read_b64_tr_b16 a[224:225], v25 offset:0\n\t" \
   "ds_read_b64_tr_b16 a[226:227], v25 offset:4096\n\t" "v_add_u32 v24, s40, v18\n\t" "v_add_u32 v25, s40, v19\n\t" "s_add_u32 s40, s40, 8192\n\t" \
   "s_cmp_eq_u32 s40, 81920\n\t" "s_cselect_b32 s40, 0, s40\n\t" "ds_read_b64_tr_b16 a[168:169], v24 offset:0\n\t" \
   "ds_read_b64_tr_b16 a[170:171], v24 offset:4096\n\t" "ds_read_b64_tr_b16 a[232:233], v25 offset:0\n\t" \
   "ds_read_b64_tr_b16 a[234:235], v25 offset:4096\n\t" "v_add_u32 v24, s40, v18\n\t" "v_add_u32 v25, s40, v19\n\t" "s_add_u32 s40, s40, 8192\n\t" \
   "s_cmp_eq_u32 s40, 81920\n\t" "s_cselect_b32 s40, 0, s40\n\t" "ds_read_b64_tr_b16 a[176:177], v24 offset:0\n\t" \
   "ds_read_b64_tr_b16 a[178:179], v24 offset:4096\n\t" "ds_read_b64_tr_b16 a[240:241], v25 offset:0\n\t" \
   "ds_read_b64_tr_b16 a[242:243], v25 offset:4096\n\t" "v_add_u32 v24, s40, v18\n\t" "v_add_u32 v25, s40, v19\n\t" "s_add_u32 s40, s40, 8192\n\t" \
   "s_cmp_eq_u32 s40, 81920\n\t" "s_cselect_b32 s40, 0, s40\n\t" "ds_read_b64_tr_b16 a[184:185], v24 offset:0\n\t" \
   "ds_read_b64_tr_b16 a[186:187], v24 offset:4096\n\t" "ds_read_b64_tr_b16 a[248:249], v25 offset:0\n\t" \
   "ds_read_b64_tr_b16 a[250:251], v25 offset:4096\n\t" "v_add_u32 v24, s40, v18\n\t" "v_add_u32 v25, s40, v19\n\t" "s_add_u32 s40, s40, 8192\n\t" \
   "s_cmp_eq_u32 s40, 81920\n\t" "s_cselect_b32 s40, 0, s40\n\t" "ds_read_b64_tr_b16 v[224:225], v24 offset:0\n\t" \
   "ds_read_b64_tr_b16 v[226:227], v24 offset:4096\n\t" "ds_read_b64_tr_b16 v[240:241], v25 offset:0\n\t" \
   "ds_read_b64_tr_b16 v[242:243], v25 offset:4096\n\t" "v_add_u32 v24, s40, v18\n\t" "v_add_u32 v25, s40, v19\n\t" "s_add_u32 s40, s40, 8192\n\t" \
   "s_cmp_eq_u32 s40, 81920\n\t" "s_cselect_b32 s40, 0, s40\n\t" "ds_read_b64_tr_b16 v[232:233], v24 offset:0\n\t" \
   "ds_read_b64_tr_b16 v[234:235], v24 offset:4096\n\t" "ds_read_b64_tr_b16 v[248:249], v25 offset:0\n\t" \
   "ds_read_b64_tr_b16 v[250:251], v25 offset:4096\n\t" "s_waitcnt vmcnt(39)\n\t" "s_waitcnt lgkmcnt(0)\n\t" "s_barrier\n\t" \
   "ds_write_b128 v20, a[4:7] offset:0\n\t" "s_waitcnt vmcnt(38)\n\t" "ds_write_b128 v20, a[68:71] offset:4096\n\t" "s_waitcnt vmcnt(37)\n\t" \
   "ds_write_b128 v20, a[12:15] offset:8192\n\t" "s_waitcnt vmcnt(36)\n\t" "ds_write_b128 v20, a[76:79] offset:12288\n\t" "s_waitcnt vmcnt(35)\n\t" \
   "ds_write_b128 v20, a[20:23] offset:16384\n\t" "s_waitcnt vmcnt(34)\n\t" "ds_write_b128 v20, a[84:87] offset:20480\n\t" "s_waitcnt vmcnt(33)\n\t" \
   "ds_write_b128 v20, a[28:31] offset:24576\n\t" "s_waitcnt vmcnt(32)\n\t" "ds_write_b128 v20, a[92:95] offset:28672\n\t" "s_waitcnt vmcnt(31)\n\t" \
   "ds_write_b128 v20, a[36:39] offset:32768\n\t" "s_waitcnt vmcnt(30)\n\t" "ds_write_b128 v20, a[100:103] offset:36864\n\t" "s_waitcnt vmcnt(29)\n\t" \
   "ds_write_b128 v20, a[44:47] offset:40960\n\t" "s_waitcnt vmcnt(28)\n\t" "ds_write_b128 v20, a[108:111] offset:45056\n\t" "s_waitcnt vmcnt(27)\n\t" \
   "ds_write_b128 v20, a[52:55] offset:49152\n\t" "s_waitcnt vmcnt(26)\n\t" "ds_write_b128 v20, a[116:119] offset:53248\n\t" "s_waitcnt vmcnt(25)\n\t" \
   "ds_write_b128 v20, a[60:63] offset:57344\n\t" "s_waitcnt vmcnt(24)\n\t" "ds_write_b128 v20, a[124:127] offset:61440\n\t" "s_waitcnt vmcnt(23)\n\t" \
   "ds_write_b128 v23, v[196:199] offset:0\n\t" "s_waitcnt vmcnt(22)\n\t" "ds_write_b128 v23, v[212:215] offset:4096\n\t" "s_waitcnt vmcnt(21)\n\t" \
   "ds_write_b128 v23, v[204:207] offset:8192\n\t" "s_waitcnt vmcnt(20)\n\t" "ds_write_b128 v23, v[220:223] offset:12288\n\t" \
   "s_waitcnt lgkmcnt(0)\n\t" "s_barrier\n\t" "s_lshl_b32 s40, %[p0], 13\n\t" "v_add_u32 v24, s40, v18\n\t" "v_add_u32 v25, s40, v19\n\t" \
   "s_add_u32 s40, s40, 8192\n\t" "s_cmp_eq_u32 s40, 81920\n\t" "s_cselect_b32 s40, 0, s40\n\t" "ds_read_b64_tr_b16 a[4:5], v24 offset:0\n\t" \
   "ds_read_b64_tr_b16 a[6:7], v24 offset:4096\n\t" "ds_read_b64_tr_b16 a[68:69], v25 offset:0\n\t" "ds_read_b64_tr_b16 a[70:71], v25 offset:4096\n\t" \
   "v_add_u32 v24, s40, v18\n\t" "v_add_u32 v25, s40, v19\n\t" "s_add_u32 s40, s40, 8192\n\t" "s_cmp_eq_u32 s40, 81920\n\t" \
   "s_cselect_b32 s40, 0, s40\n\t" "ds_read_b64_tr_b16 a[12:13], v24 offset:0\n\t" "ds_read_b64_tr_b16 a[14:15], v24 offset:4096\n\t" \
   "ds_read_b64_tr_b16 a[76:77], v25 offset:0\n\t" "ds_read_b64_tr_b16 a[78:79], v25 offset:4096\n\t" "v_add_u32 v24, s40, v18\n\t" \
   "v_add_u32 v25, s40, v19\n\t" "s_add_u32 s40, s40, 8192\n\t" "s_cmp_eq_u32 s40, 81920\n\t" "s_cselect_b32 s40, 0, s40\n\t" \
   "ds_read_b64_tr_b16 a[20:21], v24 offset:0\n\t" "ds_read_b64_tr_b16 a[22:23], v24 offset:4096\n\t" "ds_read_b64_tr_b16 a[84:85], v25 offset:0\n\t" \
   "ds_read_b64_tr_b16 a[86:87], v25 offset:4096\n\t" "v_add_u32 v24, s40, v18\n\t" "v_add_u32 v25, s40, v19\n\t" "s_add_u32 s40, s40, 8192\n\t" \
   "s_cmp_eq_u32 s40, 81920\n\t" "s_cselect_b32 s40, 0, s40\n\t" "ds_read_b64_tr_b16 a[28:29], v24 offset:0\n\t" \
   "ds_read_b64_tr_b16 a[30:31], v24 offset:4096\n\t" "ds_read_b64_tr_b16 a[92:93], v25 offset:0\n\t" \
   "ds_read_b64_tr_b16 a[94:95], v25 offset:4096\n\t" "v_add_u32 v24, s40, v18\n\t" "v_add_u32 v25, s40, v19\n\t" "s_add_u32 s40, s40, 8192\n\t" \
   "s_cmp_eq_u32 s40, 81920\n\t" "s_cselect_b32 s40, 0, s40\n\t" "ds_read_b64_tr_b16 a[36:37], v24 offset:0\n\t" \
   "ds_read_b64_tr_b16 a[38:39], v24 offset:4096\n\t" "ds_read_b64_tr_b16 a[100:101], v25 offset:0\n\t" \
   "ds_read_b64_tr_b16 a[102:103], v25 offset:4096\n\t" "v_add_u32 v24, s40, v18\n\t" "v_add_u32 v25, s40, v19\n\t" "s_add_u32 s40, s40, 8192\n\t" \
   "s_cmp_eq_u32 s40, 81920\n\t" "s_cselect_b32 s40, 0, s40\n\t" "ds_read_b64_tr_b16 a[44:45], v24 offset:0\n\t" \
   "ds_read_b64_tr_b16 a[46:47], v24 offset:4096\n\t" "ds_read_b64_tr_b16 a[108:109], v25 offset:0\n\t" \
   "ds_read_b64_tr_b16 a[110:111], v25 offset:4096\n\t" "v_add_u32 v24, s40, v18\n\t" "v_add_u32 v25, s40, v19\n\t" "s_add_u32 s40, s40, 8192\n\t" \
   "s_cmp_eq_u32 s40, 81920\n\t" "s_cselect_b32 s40, 0, s40\n\t" "ds_read_b64_tr_b16 a[52:53], v24 offset:0\n\t" \
   "ds_read_b64_tr_b16 a[54:55], v24 offset:4096\n\t" "ds_read_b64_tr_b16 a[116:117], v25 offset:0\n\t" \
   "ds_read_b64_tr_b16 a[118:119], v25 offset:4096\n\t" "v_add_u32 v24, s40, v18\n\t" "v_add_u32 v25, s40, v19\n\t" "s_add_u32 s40, s40, 8192\n\t" \
   "s_cmp_eq_u32 s40, 81920\n\t" "s_cselect_b32 s40, 0, s40\n\t" "ds_read_b64_tr_b16 a[60:61], v24 offset:0\n\t" \
   "ds_read_b64_tr_b16 a[62:63], v24 offset:4096\n\t" "ds_read_b64_tr_b16 a[124:125], v25 offset:0\n\t" \
   "ds_read_b64_tr_b16 a[126:127], v25 offset:4096\n\t" "v_add_u32 v24, s40, v18\n\t" "v_add_u32 v25, s40, v19\n\t" "s_add_u32 s40, s40, 8192\n\t" \
   "s_cmp_eq_u32 s40, 81920\n\t" "s_cselect_b32 s40, 0, s40\n\t" "ds_read_b64_tr_b16 v[196:197], v24 offset:0\n\t" \
   "ds_read_b64_tr_b16 v[198:199], v24 offset:4096\n\t" "ds_read_b64_tr_b16 v[212:213], v25 offset:0\n\t" \
   "ds_read_b64_tr_b16 v[214:215], v25 offset:4096\n\t" "v_add_u32 v24, s40, v18\n\t" "v_add_u32 v25, s40, v19\n\t" "s_add_u32 s40, s40, 8192\n\t" \
   "s_cmp_eq_u32 s40, 81920\n\t" "s_cselect_b32 s40, 0, s40\n\t" "ds_read_b64_tr_b16 v[204:205], v24 offset:0\n\t" \
   "ds_read_b64_tr_b16 v[206:207], v24 offset:4096\n\t" "ds_read_b64_tr_b16 v[220:221], v25 offset:0\n\t" \
   "ds_read_b64_tr_b16 v[222:223], v25 offset:4096\n\t" "s_waitcnt vmcnt(19)\n\t" "s_waitcnt lgkmcnt(0)\n\t" "s_barrier\n\t" \
   "ds_write_b128 v20, a[132:135] offset:0\n\t" "s_waitcnt vmcnt(18)\n\t" "ds_write_b128 v20, a[196:199] offset:4096\n\t" "s_waitcnt vmcnt(17)\n\t" \
   "ds_write_b128 v20, a[140:143] offset:8192\n\t" "s_waitcnt vmcnt(16)\n\t" "ds_write_b128 v20, a[204:207] offset:12288\n\t" "s_waitcnt vmcnt(15)\n\t" \
   "ds_write_b128 v20, a[148:151] offset:16384\n\t" "s_waitcnt vmcnt(14)\n\t" "ds_write_b128 v20, a[212:215] offset:20480\n\t" \
   "s_waitcnt vmcnt(13)\n\t" "ds_write_b128 v20, a[156:159] offset:24576\n\t" "s_waitcnt vmcnt(12)\n\t" \
   "ds_write_b128 v20, a[220:223] offset:28672\n\t" "s_waitcnt vmcnt(11)\n\t" "ds_write_b128 v20, a[164:167] offset:32768\n\t" \
   "s_waitcnt vmcnt(10)\n\t" "ds_write_b128 v20, a[228:231] offset:36864\n\t" "s_waitcnt vmcnt(9)\n\t" "ds_write_b128 v20, a[172:175] offset:40960\n\t" \
   "s_waitcnt vmcnt(8)\n\t" "ds_write_b128 v20, a[236:239] offset:45056\n\t" "s_waitcnt vmcnt(7)\n\t" "ds_write_b128 v20, a[180:183] offset:49152\n\t" \
   "s_waitcnt vmcnt(6)\n\t" "ds_write_b128 v20, a[244:247] offset:53248\n\t" "s_waitcnt vmcnt(5)\n\t" "ds_write_b128 v20, a[188:191] offset:57344\n\t" \
   "s_waitcnt vmcnt(4)\n\t" "ds_write_b128 v20, a[252:255] offset:61440\n\t" "s_waitcnt vmcnt(3)\n\t" "ds_write_b128 v23, v[228:231] offset:0\n\t" \
   "s_waitcnt vmcnt(2)\n\t" "ds_write_b128 v23, v[244:247] offset:4096\n\t" "s_waitcnt vmcnt(1)\n\t" "ds_write_b128 v23, v[236:239] offset:8192\n\t" \
   "s_waitcnt vmcnt(0)\n\t" "ds_write_b128 v23, v[252:255] offset:12288\n\t" "s_waitcnt lgkmcnt(0)\n\t" "s_barrier\n\t" "s_lshl_b32 s40, %[p0], 13\n\t" \
   "v_add_u32 v24, s40, v18\n\t" "v_add_u32 v25, s40, v19\n\t" "s_add_u32 s40, s40, 8192\n\t" "s_cmp_eq_u32 s40, 81920\n\t" \
   "s_cselect_b32 s40, 0, s40\n\t" "ds_read_b64_tr_b16 a[132:133], v24 offset:0\n\t" "ds_read_b64_tr_b16 a[134:135], v24 offset:4096\n\t" \
   "ds_read_b64_tr_b16 a[196:197], v25 offset:0\n\t" "ds_read_b64_tr_b16 a[198:199], v25 offset:4096\n\t" "v_add_u32 v24, s40, v18\n\t" \
   "v_add_u32 v25, s40, v19\n\t" "s_add_u32 s40, s40, 8192\n\t" "s_cmp_eq_u32 s40, 81920\n\t" "s_cselect_b32 s40, 0, s40\n\t" \
   "ds_read_b64_tr_b16 a[140:141], v24 offset:0\n\t" "ds_read_b64_tr_b16 a[142:143], v24 offset:4096\n\t" \
   "ds_read_b64_tr_b16 a[204:205], v25 offset:0\n\t" "ds_read_b64_tr_b16 a[206:207], v25 offset:4096\n\t" "v_add_u32 v24, s40, v18\n\t" \
   "v_add_u32 v25, s40, v19\n\t" "s_add_u32 s40, s40, 8192\n\t" "s_cmp_eq_u32 s40, 81920\n\t" "s_cselect_b32 s40, 0, s40\n\t" \
   "ds_read_b64_tr_b16 a[148:149], v24 offset:0\n\t" "ds_read_b64_tr_b16 a[150:151], v24 offset:4096\n\t" \
   "ds_read_b64_tr_b16 a[212:213], v25 offset:0\n\t" "ds_read_b64_tr_b16 a[214:215], v25 offset:4096\n\t" "v_add_u32 v24, s40, v18\n\t" \
   "v_add_u32 v25, s40, v19\n\t" "s_add_u32 s40, s40, 8192\n\t" "s_cmp_eq_u32 s40, 81920\n\t" "s_cselect_b32 s40, 0, s40\n\t" \
   "ds_read_b64_tr_b16 a[156:157], v24 offset:0\n\t" "ds_read_b64_tr_b16 a[158:159], v24 offset:4096\n\t" \
   "ds_read_b64_tr_b16 a[220:221], v25 offset:0\n\t" "ds_read_b64_tr_b16 a[222:223], v25 offset:4096\n\t" "v_add_u32 v24, s40, v18\n\t" \
   "v_add_u32 v25, s40, v19\n\t" "s_add_u32 s40, s40, 8192\n\t" "s_cmp_eq_u32 s40, 81920\n\t" "s_cselect_b32 s40, 0, s40\n\t" \
   "ds_read_b64_tr_b16 a[164:165], v24 offset:0\n\t" "ds_read_b64_tr_b16 a[166:167], v24 offset:4096\n\t" \
   "ds_read_b64_tr_b16 a[228:229], v25 offset:0\n\t" "ds_read_b64_tr_b16 a[230:231], v25 offset:4096\n\t" "v_add_u32 v24, s40, v18\n\t" \
   "v_add_u32 v25, s40, v19\n\t" "s_add_u32 s40, s40, 8192\n\t" "s_cmp_eq_u32 s40, 81920\n\t" "s_cselect_b32 s40, 0, s40\n\t" \
   "ds_read_b64_tr_b16 a[172:173], v24 offset:0\n\t" "ds_read_b64_tr_b16 a[174:175], v24 offset:4096\n\t" \
   "ds_read_b64_tr_b16 a[236:237], v25 offset:0\n\t" "ds_read_b64_tr_b16 a[238:239], v25 offset:4096\n\t" "v_add_u32 v24, s40, v18\n\t" \
   "v_add_u32 v25, s40, v19\n\t" "s_add_u32 s40, s40, 8192\n\t" "s_cmp_eq_u32 s40, 81920\n\t" "s_cselect_b32 s40, 0, s40\n\t" \
   "ds_read_b64_tr_b16 a[180:181], v24 offset:0\n\t" "ds_read_b64_tr_b16 a[182:183], v24 offset:4096\n\t" \
   "ds_read_b64_tr_b16 a[244:245], v25 offset:0\n\t" "ds_read_b64_tr_b16 a[246:247], v25 offset:4096\n\t" "v_add_u32 v24, s40, v18\n\t" \
   "v_add_u32 v25, s40, v19\n\t" "s_add_u32 s40, s40, 8192\n\t" "s_cmp_eq_u32 s40, 81920\n\t" "s_cselect_b32 s40, 0, s40\n\t" \
   "ds_read_b64_tr_b16 a[188:189], v24 offset:0\n\t" "ds_read_b64_tr_b16 a[190:191], v24 offset:4096\n\t" \
   "ds_read_b64_tr_b16 a[252:253], v25 offset:0\n\t" "ds_read_b64_tr_b16 a[254:255], v25 offset:4096\n\t" "v_add_u32 v24, s40, v18\n\t" \
   "v_add_u32 v25, s40, v19\n\t" "s_add_u32 s40, s40, 8192\n\t" "s_cmp_eq_u32 s40, 81920\n\t" "s_cselect_b32 s40, 0, s40\n\t" \
   "ds_read_b64_tr_b16 v[228:229], v24 offset:0\n\t" "ds_read_b64_tr_b16 v[230:231], v24 offset:4096\n\t" \
   "ds_read_b64_tr_b16 v[244:245], v25 offset:0\n\t" "ds_read_b64_tr_b16 v[246:247], v25 offset:4096\n\t" "v_add_u32 v24, s40, v18\n\t" \
   "v_add_u32 v25, s40, v19\n\t" "s_add_u32 s40, s40, 8192\n\t" "s_cmp_eq_u32 s40, 81920\n\t" "s_cselect_b32 s40, 0, s40\n\t" \
   "ds_read_b64_tr_b16 v[236:237], v24 offset:0\n\t" "ds_read_b64_tr_b16 v[238:239], v24 offset:4096\n\t" \
   "ds_read_b64_tr_b16 v[252:253], v25 offset:0\n\t" "ds_read_b64_tr_b16 v[254:255], v25 offset:4096\n\t" "s_waitcnt lgkmcnt(0)"
#define LDP_CLOB "v24", "v25", "s40", "s41"
#define LDK_ASM \
   "s_mov_b32 s41, 0\n\t" "buffer_load_dwordx4 a[0:3], v16, %[ra], s41 offen\n\t" "s_mov_b32 s41, 131072\n\t" \
   "buffer_load_dwordx4 a[4:7], v16, %[ra], s41 offen\n\t" "s_mov_b32 s41, 262144\n\t" "buffer_load_dwordx4 a[16:19], v16, %[ra], s41 offen\n\t" \
   "s_mov_b32 s41, 393216\n\t" "buffer_load_dwordx4 a[20:23], v16, %[ra], s41 offen\n\t" "s_mov_b32 s41, 524288\n\t" \
   "buffer_load_dwordx4 a[32:35], v16, %[ra], s41 offen\n\t" "s_mov_b32 s41, 655360\n\t" "buffer_load_dwordx4 a[36:39], v16, %[ra], s41 offen\n\t" \
   "s_mov_b32 s41, 786432\n\t" "buffer_load_dwordx4 a[48:51], v16, %[ra], s41 offen\n\t" "s_mov_b32 s41, 917504\n\t" \
   "buffer_load_dwordx4 a[52:55], v16, %[ra], s41 offen\n\t" "s_mov_b32 s41, 1048576\n\t" "buffer_load_dwordx4 a[64:67], v16, %[ra], s41 offen\n\t" \
   "s_mov_b32 s41, 1179648\n\t" "buffer_load_dwordx4 a[68:71], v16, %[ra], s41 offen\n\t" "s_mov_b32 s41, 0\n\t" \
   "buffer_load_dwordx4 a[8:11], v16, %[rb], s41 offen\n\t" "s_mov_b32 s41, 131072\n\t" "buffer_load_dwordx4 a[12:15], v16, %[rb], s41 offen\n\t" \
   "s_mov_b32 s41, 262144\n\t" "buffer_load_dwordx4 a[24:27], v16, %[rb], s41 offen\n\t" "s_mov_b32 s41, 393216\n\t" \
   "buffer_load_dwordx4 a[28:31], v16, %[rb], s41 offen\n\t" "s_mov_b32 s41, 524288\n\t" "buffer_load_dwordx4 a[40:43], v16, %[rb], s41 offen\n\t" \
   "s_mov_b32 s41, 655360\n\t" "buffer_load_dwordx4 a[44:47], v16, %[rb], s41 offen\n\t" "s_mov_b32 s41, 786432\n\t" \
   "buffer_load_dwordx4 a[56:59], v16, %[rb], s41 offen\n\t" "s_mov_b32 s41, 917504\n\t" "buffer_load_dwordx4 a[60:63], v16, %[rb], s41 offen\n\t" \
   "s_mov_b32 s41, 1048576\n\t" "buffer_load_dwordx4 a[72:75], v16, %[rb], s41 offen\n\t" "s_mov_b32 s41, 1179648\n\t" \
   "buffer_load_dwordx4 a[76:79], v16, %[rb], s41 offen\n\t" "s_mov_b32 s41, 1310720\n\t" "buffer_load_dwordx4 a[80:83], v16, %[ra], s41 offen\n\t" \
   "s_mov_b32 s41, 1441792\n\t" "buffer_load_dwordx4 a[84:87], v16, %[ra], s41 offen\n\t" "s_mov_b32 s41, 1572864\n\t" \
   "buffer_load_dwordx4 a[96:99], v16, %[ra], s41 offen\n\t" "s_mov_b32 s41, 1703936\n\t" "buffer_load_dwordx4 a[100:103], v16, %[ra], s41 offen\n\t" \
   "s_mov_b32 s41, 1835008\n\t" "buffer_load_dwordx4 a[112:115], v16, %[ra], s41 offen\n\t" "s_mov_b32 s41, 1966080\n\t" \
   "buffer_load_dwordx4 a[116:119], v16, %[ra], s41 offen\n\t" "s_mov_b32 s41, 2097152\n\t" "buffer_load_dwordx4 a[128:131], v16, %[ra], s41 offen\n\t" \
   "s_mov_b32 s41, 2228224\n\t" "buffer_load_dwordx4 a[132:135], v16, %[ra], s41 offen\n\t" "s_mov_b32 s41, 2359296\n\t" \
   "buffer_load_dwordx4 a[144:147], v16, %[ra], s41 offen\n\t" "s_mov_b32 s41, 2490368\n\t" "buffer_load_dwordx4 a[148:151], v16, %[ra], s41 offen\n\t" \
   "s_mov_b32 s41, 1310720\n\t" "buffer_load_dwordx4 a[88:91], v16, %[rb], s41 offen\n\t" "s_mov_b32 s41, 1441792\n\t" \
   "buffer_load_dwordx4 a[92:95], v16, %[rb], s41 offen\n\t" "s_mov_b32 s41, 1572864\n\t" "buffer_load_dwordx4 a[104:107], v16, %[rb], s41 offen\n\t" \
   "s_mov_b32 s41, 1703936\n\t" "buffer_load_dwordx4 a[108:111], v16, %[rb], s41 offen\n\t" "s_mov_b32 s41, 1835008\n\t" \
   "buffer_load_dwordx4 a[120:123], v16, %[rb], s41 offen\n\t" "s_mov_b32 s41, 1966080\n\t" "buffer_load_dwordx4 a[124:127], v16, %[rb], s41 offen\n\t" \
   "s_mov_b32 s41, 2097152\n\t" "buffer_load_dwordx4 a[136:139], v16, %[rb], s41 offen\n\t" "s_mov_b32 s41, 2228224\n\t" \
   "buffer_load_dwordx4 a[140:143], v16, %[rb], s41 offen\n\t" "s_mov_b32 s41, 2359296\n\t" "buffer_load_dwordx4 a[152:155], v16, %[rb], s41 offen\n\t" \
   "s_mov_b32 s41, 2490368\n\t" "buffer_load_dwordx4 a[156:159], v16, %[rb], s41 offen\n\t" "s_mov_b32 s41, 2621440\n\t" \
   "buffer_load_dwordx4 a[160:163], v16, %[ra], s41 offen\n\t" "s_mov_b32 s41, 2752512\n\t" "buffer_load_dwordx4 a[164:167], v16, %[ra], s41 offen\n\t" \
   "s_mov_b32 s41, 2883584\n\t" "buffer_load_dwordx4 a[176:179], v16, %[ra], s41 offen\n\t" "s_mov_b32 s41, 3014656\n\t" \
   "buffer_load_dwordx4 a[180:183], v16, %[ra], s41 offen\n\t" "s_mov_b32 s41, 3145728\n\t" "buffer_load_dwordx4 a[192:195], v16, %[ra], s41 offen\n\t" \
   "s_mov_b32 s41, 3276800\n\t" "buffer_load_dwordx4 a[196:199], v16, %[ra], s41 offen\n\t" "s_mov_b32 s41, 3407872\n\t" \
   "buffer_load_dwordx4 a[208:211], v16, %[ra], s41 offen\n\t" "s_mov_b32 s41, 3538944\n\t" "buffer_load_dwordx4 a[212:215], v16, %[ra], s41 offen\n\t" \
   "s_mov_b32 s41, 3670016\n\t" "buffer_load_dwordx4 a[224:227], v16, %[ra], s41 offen\n\t" "s_mov_b32 s41, 3801088\n\t" \
   "buffer_load_dwordx4 a[228:231], v16, %[ra], s41 offen\n\t" "s_mov_b32 s41, 2621440\n\t" "buffer_load_dwordx4 a[168:171], v16, %[rb], s41 offen\n\t" \
   "s_mov_b32 s41, 2752512\n\t" "buffer_load_dwordx4 a[172:175], v16, %[rb], s41 offen\n\t" "s_mov_b32 s41, 2883584\n\t" \
   "buffer_load_dwordx4 a[184:187], v16, %[rb], s41 offen\n\t" "s_mov_b32 s41, 3014656\n\t" "buffer_load_dwordx4 a[188:191], v16, %[rb], s41 offen\n\t" \
   "s_mov_b32 s41, 3145728\n\t" "buffer_load_dwordx4 a[200:203], v16, %[rb], s41 offen\n\t" "s_mov_b32 s41, 3276800\n\t" \
   "buffer_load_dwordx4 a[204:207], v16, %[rb], s41 offen\n\t" "s_mov_b32 s41, 3407872\n\t" "buffer_load_dwordx4 a[216:219], v16, %[rb], s41 offen\n\t" \
   "s_mov_b32 s41, 3538944\n\t" "buffer_load_dwordx4 a[220:223], v16, %[rb], s41 offen\n\t" "s_mov_b32 s41, 3670016\n\t" \
   "buffer_load_dwordx4 a[232:235], v16, %[rb], s41 offen\n\t" "s_mov_b32 s41, 3801088\n\t" "buffer_load_dwordx4 a[236:239], v16, %[rb], s41 offen\n\t" \
   "s_waitcnt vmcnt(59)\n\t" "ds_write_b128 v20, a[0:3] offset:0\n\t" "s_mov_b32 s41, 3932160\n\t" \
   "buffer_load_dwordx4 a[240:243], v16, %[ra], s41 offen\n\t" "s_waitcnt vmcnt(59)\n\t" "ds_write_b128 v20, a[4:7] offset:4096\n\t" \
   "s_mov_b32 s41, 4063232\n\t" "buffer_load_dwordx4 a[244:247], v16, %[ra], s41 offen\n\t" "s_waitcnt vmcnt(59)\n\t" \
   "ds_write_b128 v20, a[16:19] offset:8192\n\t" "s_mov_b32 s41, 4194304\n\t" "buffer_load_dwordx4 v[192:195], v16, %[ra], s41 offen\n\t" \
   "s_waitcnt vmcnt(59)\n\t" "ds_write_b128 v20, a[20:23] offset:12288\n\t" "s_mov_b32 s41, 4325376\n\t" \
   "buffer_load_dwordx4 v[196:199], v16, %[ra], s41 offen\n\t" "s_waitcnt vmcnt(59)\n\t" "ds_write_b128 v20, a[32:35] offset:16384\n\t" \
   "s_mov_b32 s41, 4456448\n\t" "buffer_load_dwordx4 v[208:211], v16, %[ra], s41 offen\n\t" "s_waitcnt vmcnt(59)\n\t" \
   "ds_write_b128 v20, a[36:39] offset:20480\n\t" "s_mov_b32 s41, 4587520\n\t" "buffer_load_dwordx4 v[212:215], v16, %[ra], s41 offen\n\t" \
   "s_waitcnt vmcnt(59)\n\t" "ds_write_b128 v20, a[48:51] offset:24576\n\t" "s_mov_b32 s41, 4718592\n\t" \
   "buffer_load_dwordx4 v[224:227], v16, %[ra], s41 offen\n\t" "s_waitcnt vmcnt(59)\n\t" "ds_write_b128 v20, a[52:55] offset:28672\n\t" \
   "s_mov_b32 s41, 4849664\n\t" "buffer_load_dwordx4 v[228:231], v16, %[ra], s41 offen\n\t" "s_waitcnt vmcnt(59)\n\t" \
   "ds_write_b128 v20, a[64:67] offset:32768\n\t" "s_mov_b32 s41, 4980736\n\t" "buffer_load_dwordx4 v[240:243], v16, %[ra], s41 offen\n\t" \
   "s_waitcnt vmcnt(59)\n\t" "ds_write_b128 v20, a[68:71] offset:36864\n\t" "s_mov_b32 s41, 5111808\n\t" \
   "buffer_load_dwordx4 v[244:247], v16, %[ra], s41 offen\n\t" "s_waitcnt lgkmcnt(0)\n\t" "s_barrier\n\t" "ds_read_b64_tr_b16 a[0:1], v18 offset:0\n\t" \
   "ds_read_b64_tr_b16 a[2:3], v18 offset:4096\n\t" "ds_read_b64_tr_b16 a[4:5], v19 offset:0\n\t" "ds_read_b64_tr_b16 a[6:7], v19 offset:4096\n\t" \
   "ds_read_b64_tr_b16 a[16:17], v18 offset:8192\n\t" "ds_read_b64_tr_b16 a[18:19], v18 offset:12288\n\t" \
   "ds_read_b64_tr_b16 a[20:21], v19 offset:8192\n\t" "ds_read_b64_tr_b16 a[22:23], v19 offset:12288\n\t" \
   "ds_read_b64_tr_b16 a[32:33], v18 offset:16384\n\t" "ds_read_b64_tr_b16 a[34:35], v18 offset:20480\n\t" \
   "ds_read_b64_tr_b16 a[36:37], v19 offset:16384\n\t" "ds_read_b64_tr_b16 a[38:39], v19 offset:20480\n\t" \
   "ds_read_b64_tr_b16 a[48:49], v18 offset:24576\n\t" "ds_read_b64_tr_b16 a[50:51], v18 offset:28672\n\t" \
   "ds_read_b64_tr_b16 a[52:53], v19 offset:24576\n\t" "ds_read_b64_tr_b16 a[54:55], v19 offset:28672\n\t" \
   "ds_read_b64_tr_b16 a[64:65], v18 offset:32768\n\t" "ds_read_b64_tr_b16 a[66:67], v18 offset:36864\n\t" \
   "ds_read_b64_tr_b16 a[68:69], v19 offset:32768\n\t" "ds_read_b64_tr_b16 a[70:71], v19 offset:36864\n\t" "s_waitcnt vmcnt(59)\n\t" \
   "s_waitcnt lgkmcnt(0)\n\t" "s_barrier\n\t" "ds_write_b128 v20, a[8:11] offset:0\n\t" "s_mov_b32 s41, 3932160\n\t" \
   "buffer_load_dwordx4 a[248:251], v16, %[rb], s41 offen\n\t" "s_waitcnt vmcnt(59)\n\t" "ds_write_b128 v20, a[12:15] offset:4096\n\t" \
   "s_mov_b32 s41, 4063232\n\t" "buffer_load_dwordx4 a[252:255], v16, %[rb], s41 offen\n\t" "s_waitcnt vmcnt(59)\n\t" \
   "ds_write_b128 v20, a[24:27] offset:8192\n\t" "s_mov_b32 s41, 4194304\n\t" "buffer_load_dwordx4 v[200:203], v16, %[rb], s41 offen\n\t" \
   "s_waitcnt vmcnt(59)\n\t" "ds_write_b128 v20, a[28:31] offset:12288\n\t" "s_mov_b32 s41, 4325376\n\t" \
   "buffer_load_dwordx4 v[204:207], v16, %[rb], s41 offen\n\t" "s_waitcnt vmcnt(59)\n\t" "ds_write_b128 v20, a[40:43] offset:16384\n\t" \
   "s_mov_b32 s41, 4456448\n\t" "buffer_load_dwordx4 v[216:219], v16, %[rb], s41 offen\n\t" "s_waitcnt vmcnt(59)\n\t" \
   "ds_write_b128 v20, a[44:47] offset:20480\n\t" "s_mov_b32 s41, 4587520\n\t" "buffer_load_dwordx4 v[220:223], v16, %[rb], s41 offen\n\t" \
   "s_waitcnt vmcnt(59)\n\t" "ds_write_b128 v20, a[56:59] offset:24576\n\t" "s_mov_b32 s41, 4718592\n\t" \
   "buffer_load_dwordx4 v[232:235], v16, %[rb], s41 offen\n\t" "s_waitcnt vmcnt(59)\n\t" "ds_write_b128 v20, a[60:63] offset:28672\n\t" \
   "s_mov_b32 s41, 4849664\n\t" "buffer_load_dwordx4 v[236:239], v16, %[rb], s41 offen\n\t" "s_waitcnt vmcnt(59)\n\t" \
   "ds_write_b128 v20, a[72:75] offset:32768\n\t" "s_mov_b32 s41, 4980736\n\t" "buffer_load_dwordx4 v[248:251], v16, %[rb], s41 offen\n\t" \
   "s_waitcnt vmcnt(59)\n\t" "ds_write_b128 v20, a[76:79] offset:36864\n\t" "s_mov_b32 s41, 5111808\n\t" \
   "buffer_load_dwordx4 v[252:255], v16, %[rb], s41 offen\n\t" "s_waitcnt lgkmcnt(0)\n\t" "s_barrier\n\t" "ds_read_b64_tr_b16 a[8:9], v18 offset:0\n\t" \
   "ds_read_b64_tr_b16 a[10:11], v18 offset:4096\n\t" "ds_read_b64_tr_b16 a[12:13], v19 offset:0\n\t" \
   "ds_read_b64_tr_b16 a[14:15], v19 offset:4096\n\t" "ds_read_b64_tr_b16 a[24:25], v18 offset:8192\n\t" \
   "ds_read_b64_tr_b16 a[26:27], v18 offset:12288\n\t" "ds_read_b64_tr_b16 a[28:29], v19 offset:8192\n\t" \
   "ds_read_b64_tr_b16 a[30:31], v19 offset:12288\n\t" "ds_read_b64_tr_b16 a[40:41], v18 offset:16384\n\t" \
   "ds_read_b64_tr_b16 a[42:43], v18 offset:20480\n\t" "ds_read_b64_tr_b16 a[44:45], v19 offset:16384\n\t" \
   "ds_read_b64_tr_b16 a[46:47], v19 offset:20480\n\t" "ds_read_b64_tr_b16 a[56:57], v18 offset:24576\n\t" \
   "ds_read_b64_tr_b16 a[58:59], v18 offset:28672\n\t" "ds_read_b64_tr_b16 a[60:61], v19 offset:24576\n\t" \
   "ds_read_b64_tr_b16 a[62:63], v19 offset:28672\n\t" "ds_read_b64_tr_b16 a[72:73], v18 offset:32768\n\t" \
   "ds_read_b64_tr_b16 a[74:75], v18 offset:36864\n\t" "ds_read_b64_tr_b16 a[76:77], v19 offset:32768\n\t" \
   "ds_read_b64_tr_b16 a[78:79], v19 offset:36864\n\t" "s_waitcnt vmcnt(59)\n\t" "s_waitcnt lgkmcnt(0)\n\t" "s_barrier\n\t" \
   "ds_write_b128 v20, a[80:83] offset:0\n\t" "s_waitcnt vmcnt(58)\n\t" "ds_write_b128 v20, a[84:87] offset:4096\n\t" "s_waitcnt vmcnt(57)\n\t" \
   "ds_write_b128 v20, a[96:99] offset:8192\n\t" "s_waitcnt vmcnt(56)\n\t" "ds_write_b128 v20, a[100:103] offset:12288\n\t" "s_waitcnt vmcnt(55)\n\t" \
   "ds_write_b128 v20, a[112:115] offset:16384\n\t" "s_waitcnt vmcnt(54)\n\t" "ds_write_b128 v20, a[116:119] offset:20480\n\t" \
   "s_waitcnt vmcnt(53)\n\t" "ds_write_b128 v20, a[128:131] offset:24576\n\t" "s_waitcnt vmcnt(52)\n\t" \
   "ds_write_b128 v20, a[132:135] offset:28672\n\t" "s_waitcnt vmcnt(51)\n\t" "ds_write_b128 v20, a[144:147] offset:32768\n\t" \
   "s_waitcnt vmcnt(50)\n\t" "ds_write_b128 v20, a[148:151] offset:36864\n\t" "s_waitcnt lgkmcnt(0)\n\t" "s_barrier\n\t" \
   "ds_read_b64_tr_b16 a[80:81], v18 offset:0\n\t" "ds_read_b64_tr_b16 a[82:83], v18 offset:4096\n\t" "ds_read_b64_tr_b16 a[84:85], v19 offset:0\n\t" \
   "ds_read_b64_tr_b16 a[86:87], v19 offset:4096\n\t" "ds_read_b64_tr_b16 a[96:97], v18 offset:8192\n\t" \
   "ds_read_b64_tr_b16 a[98:99], v18 offset:12288\n\t" "ds_read_b64_tr_b16 a[100:101], v19 offset:8192\n\t" \
   "ds_read_b64_tr_b16 a[102:103], v19 offset:12288\n\t" "ds_read_b64_tr_b16 a[112:113], v18 offset:16384\n\t" \
   "ds_read_b64_tr_b16 a[114:115], v18 offset:20480\n\t" "ds_read_b64_tr_b16 a[116:117], v19 offset:16384\n\t" \
   "ds_read_b64_tr_b16 a[118:119], v19 offset:20480\n\t" "ds_read_b64_tr_b16 a[128:129], v18 offset:24576\n\t" \
   "ds_read_b64_tr_b16 a[130:131], v18 offset:28672\n\t" "ds_read_b64_tr_b16 a[132:133], v19 offset:24576\n\t" \
   "ds_read_b64_tr_b16 a[134:135], v19 offset:28672\n\t" "ds_read_b64_tr_b16 a[144:145], v18 offset:32768\n\t" \
   "ds_read_b64_tr_b16 a[146:147], v18 offset:36864\n\t" "ds_read_b64_tr_b16 a[148:149], v19 offset:32768\n\t" \
   "ds_read_b64_tr_b16 a[150:151], v19 offset:36864\n\t" "s_waitcnt vmcnt(49)\n\t" "s_waitcnt lgkmcnt(0)\n\t" "s_barrier\n\t" \
   "ds_write_b128 v20, a[88:91] offset:0\n\t" "s_waitcnt vmcnt(48)\n\t" "ds_write_b128 v20, a[92:95] offset:4096\n\t" "s_waitcnt vmcnt(47)\n\t" \
   "ds_write_b128 v20, a[104:107] offset:8192\n\t" "s_waitcnt vmcnt(46)\n\t" "ds_write_b128 v20, a[108:111] offset:12288\n\t" "s_waitcnt vmcnt(45)\n\t" \
   "ds_write_b128 v20, a[120:123] offset:16384\n\t" "s_waitcnt vmcnt(44)\n\t" "ds_write_b128 v20, a[124:127] offset:20480\n\t" \
   "s_waitcnt vmcnt(43)\n\t" "ds_write_b128 v20, a[136:139] offset:24576\n\t" "s_waitcnt vmcnt(42)\n\t" \
   "ds_write_b128 v20, a[140:143] offset:28672\n\t" "s_waitcnt vmcnt(41)\n\t" "ds_write_b128 v20, a[152:155] offset:32768\n\t" \
   "s_waitcnt vmcnt(40)\n\t" "ds_write_b128 v20, a[156:159] offset:36864\n\t" "s_waitcnt lgkmcnt(0)\n\t" "s_barrier\n\t" \
   "ds_read_b64_tr_b16 a[88:89], v18 offset:0\n\t" "ds_read_b64_tr_b16 a[90:91], v18 offset:4096\n\t" "ds_read_b64_tr_b16 a[92:93], v19 offset:0\n\t" \
   "ds_read_b64_tr_b16 a[94:95], v19 offset:4096\n\t" "ds_read_b64_tr_b16 a[104:105], v18 offset:8192\n\t" \
   "ds_read_b64_tr_b16 a[106:107], v18 offset:12288\n\t" "ds_read_b64_tr_b16 a[108:109], v19 offset:8192\n\t" \
   "ds_read_b64_tr_b16 a[110:111], v19 offset:12288\n\t" "ds_read_b64_tr_b16 a[120:121], v18 offset:16384\n\t" \
   "ds_read_b64_tr_b16 a[122:123], v18 offset:20480\n\t" "ds_read_b64_tr_b16 a[124:125], v19 offset:16384\n\t" \
   "ds_read_b64_tr_b16 a[126:127], v19 offset:20480\n\t" "ds_read_b64_tr_b16 a[136:137], v18 offset:24576\n\t" \
   "ds_read_b64_tr_b16 a[138:139], v18 offset:28672\n\t" "ds_read_b64_tr_b16 a[140:141], v19 offset:24576\n\t" \
   "ds_read_b64_tr_b16 a[142:143], v19 offset:28672\n\t" "ds_read_b64_tr_b16 a[152:153], v18 offset:32768\n\t" \
   "ds_read_b64_tr_b16 a[154:155], v18 offset:36864\n\t" "ds_read_b64_tr_b16 a[156:157], v19 offset:32768\n\t" \
   "ds_read_b64_tr_b16 a[158:159], v19 offset:36864\n\t" "s_waitcnt vmcnt(39)\n\t" "s_waitcnt lgkmcnt(0)\n\t" "s_barrier\n\t" \
   "ds_write_b128 v20, a[160:163] offset:0\n\t" "s_waitcnt vmcnt(38)\n\t" "ds_write_b128 v20, a[164:167] offset:4096\n\t" "s_waitcnt vmcnt(37)\n\t" \
   "ds_write_b128 v20, a[176:179] offset:8192\n\t" "s_waitcnt vmcnt(36)\n\t" "ds_write_b128 v20, a[180:183] offset:12288\n\t" "s_waitcnt vmcnt(35)\n\t" \
   "ds_write_b128 v20, a[192:195] offset:16384\n\t" "s_waitcnt vmcnt(34)\n\t" "ds_write_b128 v20, a[196:199] offset:20480\n\t" \
   "s_waitcnt vmcnt(33)\n\t" "ds_write_b128 v20, a[208:211] offset:24576\n\t" "s_waitcnt vmcnt(32)\n\t" \
   "ds_write_b128 v20, a[212:215] offset:28672\n\t" "s_waitcnt vmcnt(31)\n\t" "ds_write_b128 v20, a[224:227] offset:32768\n\t" \
   "s_waitcnt vmcnt(30)\n\t" "ds_write_b128 v20, a[228:231] offset:36864\n\t" "s_waitcnt lgkmcnt(0)\n\t" "s_barrier\n\t" \
   "ds_read_b64_tr_b16 a[160:161], v18 offset:0\n\t" "ds_read_b64_tr_b16 a[162:163], v18 offset:4096\n\t" \
   "ds_read_b64_tr_b16 a[164:165], v19 offset:0\n\t" "ds_read_b64_tr_b16 a[166:167], v19 offset:4096\n\t" \
   "ds_read_b64_tr_b16 a[176:177], v18 offset:8192\n\t" "ds_read_b64_tr_b16 a[178:179], v18 offset:12288\n\t" \
   "ds_read_b64_tr_b16 a[180:181], v19 offset:8192\n\t" "ds_read_b64_tr_b16 a[182:183], v19 offset:12288\n\t" \
   "ds_read_b64_tr_b16 a[192:193], v18 offset:16384\n\t" "ds_read_b64_tr_b16 a[194:195], v18 offset:20480\n\t" \
   "ds_read_b64_tr_b16 a[196:197], v19 offset:16384\n\t" "ds_read_b64_tr_b16 a[198:199], v19 offset:20480\n\t" \
   "ds_read_b64_tr_b16 a[208:209], v18 offset:24576\n\t" "ds_read_b64_tr_b16 a[210:211], v18 offset:28672\n\t" \
   "ds_read_b64_tr_b16 a[212:213], v19 offset:24576\n\t" "ds_read_b64_tr_b16 a[214:215], v19 offset:28672\n\t" \
   "ds_read_b64_tr_b16 a[224:225], v18 offset:32768\n\t" "ds_read_b64_tr_b16 a[226:227], v18 offset:36864\n\t" \
   "ds_read_b64_tr_b16 a[228:229], v19 offset:32768\n\t" "ds_read_b64_tr_b16 a[230:231], v19 offset:36864\n\t" "s_waitcnt vmcnt(29)\n\t" \
   "s_waitcnt lgkmcnt(0)\n\t" "s_barrier\n\t" "ds_write_b128 v20, a[168:171] offset:0\n\t" "s_waitcnt vmcnt(28)\n\t" \
   "ds_write_b128 v20, a[172:175] offset:4096\n\t" "s_waitcnt vmcnt(27)\n\t" "ds_write_b128 v20, a[184:187] offset:8192\n\t" "s_waitcnt vmcnt(26)\n\t" \
   "ds_write_b128 v20, a[188:191] offset:12288\n\t" "s_waitcnt vmcnt(25)\n\t" "ds_write_b128 v20, a[200:203] offset:16384\n\t" \
   "s_waitcnt vmcnt(24)\n\t" "ds_write_b128 v20, a[204:207] offset:20480\n\t" "s_waitcnt vmcnt(23)\n\t" \
   "ds_write_b128 v20, a[216:219] offset:24576\n\t" "s_waitcnt vmcnt(22)\n\t" "ds_write_b128 v20, a[220:223] offset:28672\n\t" \
   "s_waitcnt vmcnt(21)\n\t" "ds_write_b128 v20, a[232:235] offset:32768\n\t" "s_waitcnt vmcnt(20)\n\t" \
   "ds_write_b128 v20, a[236:239] offset:36864\n\t" "s_waitcnt lgkmcnt(0)\n\t" "s_barrier\n\t" "ds_read_b64_tr_b16 a[168:169], v18 offset:0\n\t" \
   "ds_read_b64_tr_b16 a[170:171], v18 offset:4096\n\t" "ds_read_b64_tr_b16 a[172:173], v19 offset:0\n\t" \
   "ds_read_b64_tr_b16 a[174:175], v19 offset:4096\n\t" "ds_read_b64_tr_b16 a[184:185], v18 offset:8192\n\t" \
   "ds_read_b64_tr_b16 a[186:187], v18 offset:12288\n\t" "ds_read_b64_tr_b16 a[188:189], v19 offset:8192\n\t" \
   "ds_read_b64_tr_b16 a[190:191], v19 offset:12288\n\t" "ds_read_b64_tr_b16 a[200:201], v18 offset:16384\n\t" \
   "ds_read_b64_tr_b16 a[202:203], v18 offset:20480\n\t" "ds_read_b64_tr_b16 a[204:205], v19 offset:16384\n\t" \
   "ds_read_b64_tr_b16 a[206:207], v19 offset:20480\n\t" "ds_read_b64_tr_b16 a[216:217], v18 offset:24576\n\t" \
   "ds_read_b64_tr_b16 a[218:219], v18 offset:28672\n\t" "ds_read_b64_tr_b16 a[220:221], v19 offset:24576\n\t" \
   "ds_read_b64_tr_b16 a[222:223], v19 offset:28672\n\t" "ds_read_b64_tr_b16 a[232:233], v18 offset:32768\n\t" \
   "ds_read_b64_tr_b16 a[234:235], v18 offset:36864\n\t" "ds_read_b64_tr_b16 a[236:237], v19 offset:32768\n\t" \
   "ds_read_b64_tr_b16 a[238:239], v19 offset:36864\n\t" "s_waitcnt vmcnt(19)\n\t" "s_waitcnt lgkmcnt(0)\n\t" "s_barrier\n\t" \
   "ds_write_b128 v20, a[240:243] offset:0\n\t" "s_waitcnt vmcnt(18)\n\t" "ds_write_b128 v20, a[244:247] offset:4096\n\t" "s_waitcnt vmcnt(17)\n\t" \
   "ds_write_b128 v20, v[192:195] offset:8192\n\t" "s_waitcnt vmcnt(16)\n\t" "ds_write_b128 v20, v[196:199] offset:12288\n\t" "s_waitcnt vmcnt(15)\n\t" \
   "ds_write_b128 v20, v[208:211] offset:16384\n\t" "s_waitcnt vmcnt(14)\n\t" "ds_write_b128 v20, v[212:215] offset:20480\n\t" \
   "s_waitcnt vmcnt(13)\n\t" "ds_write_b128 v20, v[224:227] offset:24576\n\t" "s_waitcnt vmcnt(12)\n\t" \
   "ds_write_b128 v20, v[228:231] offset:28672\n\t" "s_waitcnt vmcnt(11)\n\t" "ds_write_b128 v20, v[240:243] offset:32768\n\t" \
   "s_waitcnt vmcnt(10)\n\t" "ds_write_b128 v20, v[244:247] offset:36864\n\t" "s_waitcnt lgkmcnt(0)\n\t" "s_barrier\n\t" \
   "ds_read_b64_tr_b16 a[240:241], v18 offset:0\n\t" "ds_read_b64_tr_b16 a[242:243], v18 offset:4096\n\t" \
   "ds_read_b64_tr_b16 a[244:245], v19 offset:0\n\t" "ds_read_b64_tr_b16 a[246:247], v19 offset:4096\n\t" \
   "ds_read_b64_tr_b16 v[192:193], v18 offset:8192\n\t" "ds_read_b64_tr_b16 v[194:195], v18 offset:12288\n\t" \
   "ds_read_b64_tr_b16 v[196:197], v19 offset:8192\n\t" "ds_read_b64_tr_b16 v[198:199], v19 offset:12288\n\t" \
   "ds_read_b64_tr_b16 v[208:209], v18 offset:16384\n\t" "ds_read_b64_tr_b16 v[210:211], v18 offset:20480\n\t" \
   "ds_read_b64_tr_b16 v[212:213], v19 offset:16384\n\t" "ds_read_b64_tr_b16 v[214:215], v19 offset:20480\n\t" \
   "ds_read_b64_tr_b16 v[224:225], v18 offset:24576\n\t" "ds_read_b64_tr_b16 v[226:227], v18 offset:28672\n\t" \
   "ds_read_b64_tr_b16 v[228:229], v19 offset:24576\n\t" "ds_read_b64_tr_b16 v[230:231], v19 offset:28672\n\t" \
   "ds_read_b64_tr_b16 v[240:241], v18 offset:32768\n\t" "ds_read_b64_tr_b16 v[242:243], v18 offset:36864\n\t" \
   "ds_read_b64_tr_b16 v[244:245], v19 offset:32768\n\t" "ds_read_b64_tr_b16 v[246:247], v19 offset:36864\n\t" "s_waitcnt vmcnt(9)\n\t" \
   "s_waitcnt lgkmcnt(0)\n\t" "s_barrier\n\t" "ds_write_b128 v20, a[248:251] offset:0\n\t" "s_waitcnt vmcnt(8)\n\t" \
   "ds_write_b128 v20, a[252:255] offset:4096\n\t" "s_waitcnt vmcnt(7)\n\t" "ds_write_b128 v20, v[200:203] offset:8192\n\t" "s_waitcnt vmcnt(6)\n\t" \
   "ds_write_b128 v20, v[204:207] offset:12288\n\t" "s_waitcnt vmcnt(5)\n\t" "ds_write_b128 v20, v[216:219] offset:16384\n\t" "s_waitcnt vmcnt(4)\n\t" \
   "ds_write_b128 v20, v[220:223] offset:20480\n\t" "s_waitcnt vmcnt(3)\n\t" "ds_write_b128 v20, v[232:235] offset:24576\n\t" "s_waitcnt vmcnt(2)\n\t" \
   "ds_write_b128 v20, v[236:239] offset:28672\n\t" "s_waitcnt vmcnt(1)\n\t" "ds_write_b128 v20, v[248:251] offset:32768\n\t" "s_waitcnt vmcnt(0)\n\t" \
   "ds_write_b128 v20, v[252:255] offset:36864\n\t" "s_waitcnt lgkmcnt(0)\n\t" "s_barrier\n\t" "ds_read_b64_tr_b16 a[248:249], v18 offset:0\n\t" \
   "ds_read_b64_tr_b16 a[250:251], v18 offset:4096\n\t" "ds_read_b64_tr_b16 a[252:253], v19 offset:0\n\t" \
   "ds_read_b64_tr_b16 a[254:255], v19 offset:4096\n\t" "ds_read_b64_tr_b16 v[200:201], v18 offset:8192\n\t" \
   "ds_read_b64_tr_b16 v[202:203], v18 offset:12288\n\t" "ds_read_b64_tr_b16 v[204:205], v19 offset:8192\n\t" \
   "ds_read_b64_tr_b16 v[206:207], v19 offset:12288\n\t" "ds_read_b64_tr_b16 v[216:217], v18 offset:16384\n\t" \
   "ds_read_b64_tr_b16 v[218:219], v18 offset:20480\n\t" "ds_read_b64_tr_b16 v[220:221], v19 offset:16384\n\t" \
   "ds_read_b64_tr_b16 v[222:223], v19 offset:20480\n\t" "ds_read_b64_tr_b16 v[232:233], v18 offset:24576\n\t" \
   "ds_read_b64_tr_b16 v[234:235], v18 offset:28672\n\t" "ds_read_b64_tr_b16 v[236:237], v19 offset:24576\n\t" \
   "ds_read_b64_tr_b16 v[238:239], v19 offset:28672\n\t" "ds_read_b64_tr_b16 v[248:249], v18 offset:32768\n\t" \
   "ds_read_b64_tr_b16 v[250:251], v18 offset:36864\n\t" "ds_read_b64_tr_b16 v[252:253], v19 offset:32768\n\t" \
   "ds_read_b64_tr_b16 v[254:255], v19 offset:36864\n\t" "s_waitcnt lgkmcnt(0)"
#define LDK_CLOB "v24", "v25", "s40", "s41"
#define LDV_ASM \
   "s_mov_b32 s41, 0\n\t" "buffer_load_dwordx4 a[0:3], v16, %[ra], s41 offen\n\t" "s_mov_b32 s41, 131072\n\t" \
   "buffer_load_dwordx4 a[4:7], v16, %[ra], s41 offen\n\t" "s_mov_b32 s41, 262144\n\t" "buffer_load_dwordx4 a[8:11], v16, %[ra], s41 offen\n\t" \
   "s_mov_b32 s41, 393216\n\t" "buffer_load_dwordx4 a[12:15], v16, %[ra], s41 offen\n\t" "s_mov_b32 s41, 524288\n\t" \
   "buffer_load_dwordx4 a[16:19], v16, %[ra], s41 offen\n\t" "s_mov_b32 s41, 655360\n\t" "buffer_load_dwordx4 a[20:23], v16, %[ra], s41 offen\n\t" \
   "s_mov_b32 s41, 786432\n\t" "buffer_load_dwordx4 a[24:27], v16, %[ra], s41 offen\n\t" "s_mov_b32 s41, 917504\n\t" \
   "buffer_load_dwordx4 a[28:31], v16, %[ra], s41 offen\n\t" "s_mov_b32 s41, 1048576\n\t" "buffer_load_dwordx4 a[32:35], v16, %[ra], s41 offen\n\t" \
   "s_mov_b32 s41, 1179648\n\t" "buffer_load_dwordx4 a[36:39], v16, %[ra], s41 offen\n\t" "s_mov_b32 s41, 1310720\n\t" \
   "buffer_load_dwordx4 a[40:43], v16, %[ra], s41 offen\n\t" "s_mov_b32 s41, 1441792\n\t" "buffer_load_dwordx4 a[44:47], v16, %[ra], s41 offen\n\t" \
   "s_mov_b32 s41, 1572864\n\t" "buffer_load_dwordx4 a[48:51], v16, %[ra], s41 offen\n\t" "s_mov_b32 s41, 1703936\n\t" \
   "buffer_load_dwordx4 a[52:55], v16, %[ra], s41 offen\n\t" "s_mov_b32 s41, 1835008\n\t" "buffer_load_dwordx4 a[56:59], v16, %[ra], s41 offen\n\t" \
   "s_mov_b32 s41, 1966080\n\t" "buffer_load_dwordx4 a[60:63], v16, %[ra], s41 offen\n\t" "s_mov_b32 s41, 2097152\n\t" \
   "buffer_load_dwordx4 a[64:67], v16, %[ra], s41 offen\n\t" "s_mov_b32 s41, 2228224\n\t" "buffer_load_dwordx4 a[68:71], v16, %[ra], s41 offen\n\t" \
   "s_mov_b32 s41, 2359296\n\t" "buffer_load_dwordx4 a[72:75], v16, %[ra], s41 offen\n\t" "s_mov_b32 s41, 2490368\n\t" \
   "buffer_load_dwordx4 a[76:79], v16, %[ra], s41 offen\n\t" "s_mov_b32 s41, 2621440\n\t" "buffer_load_dwordx4 a[80:83], v16, %[ra], s41 offen\n\t" \
   "s_mov_b32 s41, 2752512\n\t" "buffer_load_dwordx4 a[84:87], v16, %[ra], s41 offen\n\t" "s_mov_b32 s41, 2883584\n\t" \
   "buffer_load_dwordx4 a[88:91], v16, %[ra], s41 offen\n\t" "s_mov_b32 s41, 3014656\n\t" "buffer_load_dwordx4 a[92:95], v16, %[ra], s41 offen\n\t" \
   "s_mov_b32 s41, 3145728\n\t" "buffer_load_dwordx4 a[96:99], v16, %[ra], s41 offen\n\t" "s_mov_b32 s41, 3276800\n\t" \
   "buffer_load_dwordx4 a[100:103], v16, %[ra], s41 offen\n\t" "s_mov_b32 s41, 3407872\n\t" "buffer_load_dwordx4 a[104:107], v16, %[ra], s41 offen\n\t" \
   "s_mov_b32 s41, 3538944\n\t" "buffer_load_dwordx4 a[108:111], v16, %[ra], s41 offen\n\t" "s_mov_b32 s41, 3670016\n\t" \
   "buffer_load_dwordx4 a[112:115], v16, %[ra], s41 offen\n\t" "s_mov_b32 s41, 3801088\n\t" "buffer_load_dwordx4 a[116:119], v16, %[ra], s41 offen\n\t" \
   "s_mov_b32 s41, 3932160\n\t" "buffer_load_dwordx4 a[120:123], v16, %[ra], s41 offen\n\t" "s_mov_b32 s41, 4063232\n\t" \
   "buffer_load_dwordx4 a[124:127], v16, %[ra], s41 offen\n\t" "s_mov_b32 s41, 4194304\n\t" "buffer_load_dwordx4 a[128:131], v16, %[ra], s41 offen\n\t" \
   "s_mov_b32 s41, 4325376\n\t" "buffer_load_dwordx4 a[132:135], v16, %[ra], s41 offen\n\t" "s_mov_b32 s41, 4456448\n\t" \
   "buffer_load_dwordx4 a[136:139], v16, %[ra], s41 offen\n\t" "s_mov_b32 s41, 4587520\n\t" "buffer_load_dwordx4 a[140:143], v16, %[ra], s41 offen\n\t" \
   "s_mov_b32 s41, 4718592\n\t" "buffer_load_dwordx4 a[144:147], v16, %[ra], s41 offen\n\t" "s_mov_b32 s41, 4849664\n\t" \
   "buffer_load_dwordx4 a[148:151], v16, %[ra], s41 offen\n\t" "s_mov_b32 s41, 4980736\n\t" "buffer_load_dwordx4 a[152:155], v16, %[ra], s41 offen\n\t" \
   "s_mov_b32 s41, 5111808\n\t" "buffer_load_dwordx4 a[156:159], v16, %[ra], s41 offen\n\t" "s_mov_b32 s41, 0\n\t" \
   "buffer_load_dwordx4 a[160:163], v17, %[rb], s41 offen\n\t" "s_mov_b32 s41, 256\n\t" "buffer_load_dwordx4 a[176:179], v17, %[rb], s41 offen\n\t" \
   "s_mov_b32 s41, 512\n\t" "buffer_load_dwordx4 a[192:195], v17, %[rb], s41 offen\n\t" "s_mov_b32 s41, 768\n\t" \
   "buffer_load_dwordx4 a[208:211], v17, %[rb], s41 offen\n\t" "s_mov_b32 s41, 1024\n\t" "buffer_load_dwordx4 a[224:227], v17, %[rb], s41 offen\n\t" \
   "s_mov_b32 s41, 20480\n\t" "buffer_load_dwordx4 a[240:243], v17, %[rb], s41 offen\n\t" "s_mov_b32 s41, 20736\n\t" \
   "buffer_load_dwordx4 v[192:195], v17, %[rb], s41 offen\n\t" "s_mov_b32 s41, 20992\n\t" "buffer_load_dwordx4 v[208:211], v17, %[rb], s41 offen\n\t" \
   "s_mov_b32 s41, 21248\n\t" "buffer_load_dwordx4 v[224:227], v17, %[rb], s41 offen\n\t" "s_mov_b32 s41, 21504\n\t" \
   "buffer_load_dwordx4 v[240:243], v17, %[rb], s41 offen\n\t" "s_mov_b32 s41, 40960\n\t" "buffer_load_dwordx4 a[164:167], v17, %[rb], s41 offen\n\t" \
   "s_mov_b32 s41, 41216\n\t" "buffer_load_dwordx4 a[180:183], v17, %[rb], s41 offen\n\t" "s_mov_b32 s41, 41472\n\t" \
   "buffer_load_dwordx4 a[196:199], v17, %[rb], s41 offen\n\t" "s_mov_b32 s41, 41728\n\t" "buffer_load_dwordx4 a[212:215], v17, %[rb], s41 offen\n\t" \
   "s_mov_b32 s41, 41984\n\t" "buffer_load_dwordx4 a[228:231], v17, %[rb], s41 offen\n\t" "s_mov_b32 s41, 61440\n\t" \
   "buffer_load_dwordx4 a[244:247], v17, %[rb], s41 offen\n\t" "s_mov_b32 s41, 61696\n\t" "buffer_load_dwordx4 v[196:199], v17, %[rb], s41 offen\n\t" \
   "s_mov_b32 s41, 61952\n\t" "buffer_load_dwordx4 v[212:215], v17, %[rb], s41 offen\n\t" "s_mov_b32 s41, 62208\n\t" \
   "buffer_load_dwordx4 v[228:231], v17, %[rb], s41 offen\n\t" "s_mov_b32 s41, 62464\n\t" "buffer_load_dwordx4 v[244:247], v17, %[rb], s41 offen\n\t" \
   "s_waitcnt vmcnt(59)\n\t" "ds_write_b128 v20, a[0:3] offset:0\n\t" "s_mov_b32 s41, 81920\n\t" \
   "buffer_load_dwordx4 a[168:171], v17, %[rb], s41 offen\n\t" "s_waitcnt vmcnt(59)\n\t" "ds_write_b128 v20, a[4:7] offset:4096\n\t" \
   "s_mov_b32 s41, 82176\n\t" "buffer_load_dwordx4 a[184:187], v17, %[rb], s41 offen\n\t" "s_waitcnt vmcnt(59)\n\t" \
   "ds_write_b128 v20, a[8:11] offset:8192\n\t" "s_mov_b32 s41, 82432\n\t" "buffer_load_dwordx4 a[200:203], v17, %[rb], s41 offen\n\t" \
   "s_waitcnt vmcnt(59)\n\t" "ds_write_b128 v20, a[12:15] offset:12288\n\t" "s_mov_b32 s41, 82688\n\t" \
   "buffer_load_dwordx4 a[216:219], v17, %[rb], s41 offen\n\t" "s_waitcnt vmcnt(59)\n\t" "ds_write_b128 v20, a[16:19] offset:16384\n\t" \
   "s_mov_b32 s41, 82944\n\t" "buffer_load_dwordx4 a[232:235], v17, %[rb], s41 offen\n\t" "s_waitcnt vmcnt(59)\n\t" \
   "ds_write_b128 v20, a[20:23] offset:20480\n\t" "s_mov_b32 s41, 102400\n\t" "buffer_load_dwordx4 a[248:251], v17, %[rb], s41 offen\n\t" \
   "s_waitcnt vmcnt(59)\n\t" "ds_write_b128 v20, a[24:27] offset:24576\n\t" "s_mov_b32 s41, 102656\n\t" \
   "buffer_load_dwordx4 v[200:203], v17, %[rb], s41 offen\n\t" "s_waitcnt vmcnt(59)\n\t" "ds_write_b128 v20, a[28:31] offset:28672\n\t" \
   "s_mov_b32 s41, 102912\n\t" "buffer_load_dwordx4 v[216:219], v17, %[rb], s41 offen\n\t" "s_waitcnt vmcnt(59)\n\t" \
   "ds_write_b128 v20, a[32:35] offset:32768\n\t" "s_mov_b32 s41, 103168\n\t" "buffer_load_dwordx4 v[232:235], v17, %[rb], s41 offen\n\t" \
   "s_waitcnt vmcnt(59)\n\t" "ds_write_b128 v20, a[36:39] offset:36864\n\t" "s_mov_b32 s41, 103424\n\t" \
   "buffer_load_dwordx4 v[248:251], v17, %[rb], s41 offen\n\t" "s_waitcnt lgkmcnt(0)\n\t" "s_barrier\n\t" "ds_read_b64_tr_b16 a[0:1], v18 offset:0\n\t" \
   "ds_read_b64_tr_b16 a[2:3], v18 offset:4096\n\t" "ds_read_b64_tr_b16 a[4:5], v19 offset:0\n\t" "ds_read_b64_tr_b16 a[6:7], v19 offset:4096\n\t" \
   "ds_read_b64_tr_b16 a[8:9], v18 offset:8192\n\t" "ds_read_b64_tr_b16 a[10:11], v18 offset:12288\n\t" \
   "ds_read_b64_tr_b16 a[12:13], v19 offset:8192\n\t" "ds_read_b64_tr_b16 a[14:15], v19 offset:12288\n\t" \
   "ds_read_b64_tr_b16 a[16:17], v18 offset:16384\n\t" "ds_read_b64_tr_b16 a[18:19], v18 offset:20480\n\t" \
   "ds_read_b64_tr_b16 a[20:21], v19 offset:16384\n\t" "ds_read_b64_tr_b16 a[22:23], v19 offset:20480\n\t" \
   "ds_read_b64_tr_b16 a[24:25], v18 offset:24576\n\t" "ds_read_b64_tr_b16 a[26:27], v18 offset:28672\n\t" \
   "ds_read_b64_tr_b16 a[28:29], v19 offset:24576\n\t" "ds_read_b64_tr_b16 a[30:31], v19 offset:28672\n\t" \
   "ds_read_b64_tr_b16 a[32:33], v18 offset:32768\n\t" "ds_read_b64_tr_b16 a[34:35], v18 offset:36864\n\t" \
   "ds_read_b64_tr_b16 a[36:37], v19 offset:32768\n\t" "ds_read_b64_tr_b16 a[38:39], v19 offset:36864\n\t" "s_waitcnt vmcnt(59)\n\t" \
   "s_waitcnt lgkmcnt(0)\n\t" "s_barrier\n\t" "ds_write_b128 v20, a[40:43] offset:0\n\t" "s_mov_b32 s41, 122880\n\t" \
   "buffer_load_dwordx4 a[172:175], v17, %[rb], s41 offen\n\t" "s_waitcnt vmcnt(59)\n\t" "ds_write_b128 v20, a[44:47] offset:4096\n\t" \
   "s_mov_b32 s41, 123136\n\t" "buffer_load_dwordx4 a[188:191], v17, %[rb], s41 offen\n\t" "s_waitcnt vmcnt(59)\n\t" \
   "ds_write_b128 v20, a[48:51] offset:8192\n\t" "s_mov_b32 s41, 123392\n\t" "buffer_load_dwordx4 a[204:207], v17, %[rb], s41 offen\n\t" \
   "s_waitcnt vmcnt(59)\n\t" "ds_write_b128 v20, a[52:55] offset:12288\n\t" "s_mov_b32 s41, 123648\n\t" \
   "buffer_load_dwordx4 a[220:223], v17, %[rb], s41 offen\n\t" "s_waitcnt vmcnt(59)\n\t" "ds_write_b128 v20, a[56:59] offset:16384\n\t" \
   "s_mov_b32 s41, 123904\n\t" "buffer_load_dwordx4 a[236:239], v17, %[rb], s41 offen\n\t" "s_waitcnt vmcnt(59)\n\t" \
   "ds_write_b128 v20, a[60:63] offset:20480\n\t" "s_mov_b32 s41, 143360\n\t" "buffer_load_dwordx4 a[252:255], v17, %[rb], s41 offen\n\t" \
   "s_waitcnt vmcnt(59)\n\t" "ds_write_b128 v20, a[64:67] offset:24576\n\t" "s_mov_b32 s41, 143616\n\t" \
   "buffer_load_dwordx4 v[204:207], v17, %[rb], s41 offen\n\t" "s_waitcnt vmcnt(59)\n\t" "ds_write_b128 v20, a[68:71] offset:28672\n\t" \
   "s_mov_b32 s41, 143872\n\t" "buffer_load_dwordx4 v[220:223], v17, %[rb], s41 offen\n\t" "s_waitcnt vmcnt(59)\n\t" \
   "ds_write_b128 v20, a[72:75] offset:32768\n\t" "s_mov_b32 s41, 144128\n\t" "buffer_load_dwordx4 v[236:239], v17, %[rb], s41 offen\n\t" \
   "s_waitcnt vmcnt(59)\n\t" "ds_write_b128 v20, a[76:79] offset:36864\n\t" "s_mov_b32 s41, 144384\n\t" \
   "buffer_load_dwordx4 v[252:255], v17, %[rb], s41 offen\n\t" "s_waitcnt lgkmcnt(0)\n\t" "s_barrier\n\t" \
   "ds_read_b64_tr_b16 a[40:41], v18 offset:0\n\t" "ds_read_b64_tr_b16 a[42:43], v18 offset:4096\n\t" "ds_read_b64_tr_b16 a[44:45], v19 offset:0\n\t" \
   "ds_read_b64_tr_b16 a[46:47], v19 offset:4096\n\t" "ds_read_b64_tr_b16 a[48:49], v18 offset:8192\n\t" \
   "ds_read_b64_tr_b16 a[50:51], v18 offset:12288\n\t" "ds_read_b64_tr_b16 a[52:53], v19 offset:8192\n\t" \
   "ds_read_b64_tr_b16 a[54:55], v19 offset:12288\n\t" "ds_read_b64_tr_b16 a[56:57], v18 offset:16384\n\t" \
   "ds_read_b64_tr_b16 a[58:59], v18 offset:20480\n\t" "ds_read_b64_tr_b16 a[60:61], v19 offset:16384\n\t" \
   "ds_read_b64_tr_b16 a[62:63], v19 offset:20480\n\t" "ds_read_b64_tr_b16 a[64:65], v18 offset:24576\n\t" \
   "ds_read_b64_tr_b16 a[66:67], v18 offset:28672\n\t" "ds_read_b64_tr_b16 a[68:69], v19 offset:24576\n\t" \
   "ds_read_b64_tr_b16 a[70:71], v19 offset:28672\n\t" "ds_read_b64_tr_b16 a[72:73], v18 offset:32768\n\t" \
   "ds_read_b64_tr_b16 a[74:75], v18 offset:36864\n\t" "ds_read_b64_tr_b16 a[76:77], v19 offset:32768\n\t" \
   "ds_read_b64_tr_b16 a[78:79], v19 offset:36864\n\t" "s_waitcnt vmcnt(59)\n\t" "s_waitcnt lgkmcnt(0)\n\t" "s_barrier\n\t" \
   "ds_write_b128 v20, a[80:83] offset:0\n\t" "s_waitcnt vmcnt(58)\n\t" "ds_write_b128 v20, a[84:87] offset:4096\n\t" "s_waitcnt vmcnt(57)\n\t" \
   "ds_write_b128 v20, a[88:91] offset:8192\n\t" "s_waitcnt vmcnt(56)\n\t" "ds_write_b128 v20, a[92:95] offset:12288\n\t" "s_waitcnt vmcnt(55)\n\t" \
   "ds_write_b128 v20, a[96:99] offset:16384\n\t" "s_waitcnt vmcnt(54)\n\t" "ds_write_b128 v20, a[100:103] offset:20480\n\t" "s_waitcnt vmcnt(53)\n\t" \
   "ds_write_b128 v20, a[104:107] offset:24576\n\t" "s_waitcnt vmcnt(52)\n\t" "ds_write_b128 v20, a[108:111] offset:28672\n\t" \
   "s_waitcnt vmcnt(51)\n\t" "ds_write_b128 v20, a[112:115] offset:32768\n\t" "s_waitcnt vmcnt(50)\n\t" \
   "ds_write_b128 v20, a[116:119] offset:36864\n\t" "s_waitcnt lgkmcnt(0)\n\t" "s_barrier\n\t" "ds_read_b64_tr_b16 a[80:81], v18 offset:0\n\t" \
   "ds_read_b64_tr_b16 a[82:83], v18 offset:4096\n\t" "ds_read_b64_tr_b16 a[84:85], v19 offset:0\n\t" \
   "ds_read_b64_tr_b16 a[86:87], v19 offset:4096\n\t" "ds_read_b64_tr_b16 a[88:89], v18 offset:8192\n\t" \
   "ds_read_b64_tr_b16 a[90:91], v18 offset:12288\n\t" "ds_read_b64_tr_b16 a[92:93], v19 offset:8192\n\t" \
   "ds_read_b64_tr_b16 a[94:95], v19 offset:12288\n\t" "ds_read_b64_tr_b16 a[96:97], v18 offset:16384\n\t" \
   "ds_read_b64_tr_b16 a[98:99], v18 offset:20480\n\t" "ds_read_b64_tr_b16 a[100:101], v19 offset:16384\n\t" \
   "ds_read_b64_tr_b16 a[102:103], v19 offset:20480\n\t" "ds_read_b64_tr_b16 a[104:105], v18 offset:24576\n\t" \
   "ds_read_b64_tr_b16 a[106:107], v18 offset:28672\n\t" "ds_read_b64_tr_b16 a[108:109], v19 offset:24576\n\t" \
   "ds_read_b64_tr_b16 a[110:111], v19 offset:28672\n\t" "ds_read_b64_tr_b16 a[112:113], v18 offset:32768\n\t" \
   "ds_read_b64_tr_b16 a[114:115], v18 offset:36864\n\t" "ds_read_b64_tr_b16 a[116:117], v19 offset:32768\n\t" \
   "ds_read_b64_tr_b16 a[118:119], v19 offset:36864\n\t" "s_waitcnt vmcnt(49)\n\t" "s_waitcnt lgkmcnt(0)\n\t" "s_barrier\n\t" \
   "ds_write_b128 v20, a[120:123] offset:0\n\t" "s_waitcnt vmcnt(48)\n\t" "ds_write_b128 v20, a[124:127] offset:4096\n\t" "s_waitcnt vmcnt(47)\n\t" \
   "ds_write_b128 v20, a[128:131] offset:8192\n\t" "s_waitcnt vmcnt(46)\n\t" "ds_write_b128 v20, a[132:135] offset:12288\n\t" "s_waitcnt vmcnt(45)\n\t" \
   "ds_write_b128 v20, a[136:139] offset:16384\n\t" "s_waitcnt vmcnt(44)\n\t" "ds_write_b128 v20, a[140:143] offset:20480\n\t" \
   "s_waitcnt vmcnt(43)\n\t" "ds_write_b128 v20, a[144:147] offset:24576\n\t" "s_waitcnt vmcnt(42)\n\t" \
   "ds_write_b128 v20, a[148:151] offset:28672\n\t" "s_waitcnt vmcnt(41)\n\t" "ds_write_b128 v20, a[152:155] offset:32768\n\t" \
   "s_waitcnt vmcnt(40)\n\t" "ds_write_b128 v20, a[156:159] offset:36864\n\t" "s_waitcnt lgkmcnt(0)\n\t" "s_barrier\n\t" \
   "ds_read_b64_tr_b16 a[120:121], v18 offset:0\n\t" "ds_read_b64_tr_b16 a[122:123], v18 offset:4096\n\t" \
   "ds_read_b64_tr_b16 a[124:125], v19 offset:0\n\t" "ds_read_b64_tr_b16 a[126:127], v19 offset:4096\n\t" \
   "ds_read_b64_tr_b16 a[128:129], v18 offset:8192\n\t" "ds_read_b64_tr_b16 a[130:131], v18 offset:12288\n\t" \
   "ds_read_b64_tr_b16 a[132:133], v19 offset:8192\n\t" "ds_read_b64_tr_b16 a[134:135], v19 offset:12288\n\t" \
   "ds_read_b64_tr_b16 a[136:137], v18 offset:16384\n\t" "ds_read_b64_tr_b16 a[138:139], v18 offset:20480\n\t" \
   "ds_read_b64_tr_b16 a[140:141], v19 offset:16384\n\t" "ds_read_b64_tr_b16 a[142:143], v19 offset:20480\n\t" \
   "ds_read_b64_tr_b16 a[144:145], v18 offset:24576\n\t" "ds_read_b64_tr_b16 a[146:147], v18 offset:28672\n\t" \
   "ds_read_b64_tr_b16 a[148:149], v19 offset:24576\n\t" "ds_read_b64_tr_b16 a[150:151], v19 offset:28672\n\t" \
   "ds_read_b64_tr_b16 a[152:153], v18 offset:32768\n\t" "ds_read_b64_tr_b16 a[154:155], v18 offset:36864\n\t" \
   "ds_read_b64_tr_b16 a[156:157], v19 offset:32768\n\t" "ds_read_b64_tr_b16 a[158:159], v19 offset:36864\n\t" "s_waitcnt vmcnt(39)\n\t" \
   "s_waitcnt lgkmcnt(0)\n\t" "s_barrier\n\t" "ds_write_b32 v21, a160 offset:0\n\t" "ds_write_b32 v21, a161 offset:21120\n\t" \
   "ds_write_b32 v21, a162 offset:10560\n\t" "ds_write_b32 v21, a163 offset:31680\n\t" "s_waitcnt vmcnt(38)\n\t" "ds_write_b32 v21, a176 offset:64\n\t" \
   "ds_write_b32 v21, a177 offset:21184\n\t" "ds_write_b32 v21, a178 offset:10624\n\t" "ds_write_b32 v21, a179 offset:31744\n\t" \
   "s_waitcnt vmcnt(37)\n\t" "ds_write_b32 v21, a192 offset:128\n\t" "ds_write_b32 v21, a193 offset:21248\n\t" \
   "ds_write_b32 v21, a194 offset:10688\n\t" "ds_write_b32 v21, a195 offset:31808\n\t" "s_waitcnt vmcnt(36)\n\t" \
   "ds_write_b32 v21, a208 offset:192\n\t" "ds_write_b32 v21, a209 offset:21312\n\t" "ds_write_b32 v21, a210 offset:10752\n\t" \
   "ds_write_b32 v21, a211 offset:31872\n\t" "s_waitcnt vmcnt(35)\n\t" "ds_write_b32 v21, a224 offset:256\n\t" \
   "ds_write_b32 v21, a225 offset:21376\n\t" "ds_write_b32 v21, a226 offset:10816\n\t" "ds_write_b32 v21, a227 offset:31936\n\t" \
   "s_waitcnt vmcnt(34)\n\t" "ds_write_b32 v21, a240 offset:5248\n\t" "ds_write_b32 v21, a241 offset:26368\n\t" \
   "ds_write_b32 v21, a242 offset:15808\n\t" "ds_write_b32 v21, a243 offset:36928\n\t" "s_waitcnt vmcnt(33)\n\t" \
   "ds_write_b32 v21, v192 offset:5312\n\t" "ds_write_b32 v21, v193 offset:26432\n\t" "ds_write_b32 v21, v194 offset:15872\n\t" \
   "ds_write_b32 v21, v195 offset:36992\n\t" "s_waitcnt vmcnt(32)\n\t" "ds_write_b32 v21, v208 offset:5376\n\t" \
   "ds_write_b32 v21, v209 offset:26496\n\t" "ds_write_b32 v21, v210 offset:15936\n\t" "ds_write_b32 v21, v211 offset:37056\n\t" \
   "s_waitcnt vmcnt(31)\n\t" "ds_write_b32 v21, v224 offset:5440\n\t" "ds_write_b32 v21, v225 offset:26560\n\t" \
   "ds_write_b32 v21, v226 offset:16000\n\t" "ds_write_b32 v21, v227 offset:37120\n\t" "s_waitcnt vmcnt(30)\n\t" \
   "ds_write_b32 v21, v240 offset:5504\n\t" "ds_write_b32 v21, v241 offset:26624\n\t" "ds_write_b32 v21, v242 offset:16064\n\t" \
   "ds_write_b32 v21, v243 offset:37184\n\t" "s_waitcnt lgkmcnt(0)\n\t" "s_barrier\n\t" "ds_read_b64_tr_b16 a[160:161], v22 offset:0\n\t" \
   "ds_read_b64_tr_b16 a[162:163], v22 offset:5248\n\t" "ds_read_b64_tr_b16 a[176:177], v22 offset:8\n\t" \
   "ds_read_b64_tr_b16 a[178:179], v22 offset:5256\n\t" "ds_read_b64_tr_b16 a[192:193], v22 offset:16\n\t" \
   "ds_read_b64_tr_b16 a[194:195], v22 offset:5264\n\t" "ds_read_b64_tr_b16 a[208:209], v22 offset:24\n\t" \
   "ds_read_b64_tr_b16 a[210:211], v22 offset:5272\n\t" "ds_read_b64_tr_b16 a[224:225], v22 offset:32\n\t" \
   "ds_read_b64_tr_b16 a[226:227], v22 offset:5280\n\t" "ds_read_b64_tr_b16 a[240:241], v22 offset:40\n\t" \
   "ds_read_b64_tr_b16 a[242:243], v22 offset:5288\n\t" "ds_read_b64_tr_b16 v[192:193], v22 offset:48\n\t" \
   "ds_read_b64_tr_b16 v[194:195], v22 offset:5296\n\t" "ds_read_b64_tr_b16 v[208:209], v22 offset:56\n\t" \
   "ds_read_b64_tr_b16 v[210:211], v22 offset:5304\n\t" "ds_read_b64_tr_b16 v[224:225], v22 offset:64\n\t" \
   "ds_read_b64_tr_b16 v[226:227], v22 offset:5312\n\t" "ds_read_b64_tr_b16 v[240:241], v22 offset:72\n\t" \
   "ds_read_b64_tr_b16 v[242:243], v22 offset:5320\n\t" "s_waitcnt vmcnt(29)\n\t" "s_waitcnt lgkmcnt(0)\n\t" "s_barrier\n\t" \
   "ds_write_b32 v21, a164 offset:0\n\t" "ds_write_b32 v21, a165 offset:21120\n\t" "ds_write_b32 v21, a166 offset:10560\n\t" \
   "ds_write_b32 v21, a167 offset:31680\n\t" "s_waitcnt vmcnt(28)\n\t" "ds_write_b32 v21, a180 offset:64\n\t" "ds_write_b32 v21, a181 offset:21184\n\t" \
   "ds_write_b32 v21, a182 offset:10624\n\t" "ds_write_b32 v21, a183 offset:31744\n\t" "s_waitcnt vmcnt(27)\n\t" \
   "ds_write_b32 v21, a196 offset:128\n\t" "ds_write_b32 v21, a197 offset:21248\n\t" "ds_write_b32 v21, a198 offset:10688\n\t" \
   "ds_write_b32 v21, a199 offset:31808\n\t" "s_waitcnt vmcnt(26)\n\t" "ds_write_b32 v21, a212 offset:192\n\t" \
   "ds_write_b32 v21, a213 offset:21312\n\t" "ds_write_b32 v21, a214 offset:10752\n\t" "ds_write_b32 v21, a215 offset:31872\n\t" \
   "s_waitcnt vmcnt(25)\n\t" "ds_write_b32 v21, a228 offset:256\n\t" "ds_write_b32 v21, a229 offset:21376\n\t" \
   "ds_write_b32 v21, a230 offset:10816\n\t" "ds_write_b32 v21, a231 offset:31936\n\t" "s_waitcnt vmcnt(24)\n\t" \
   "ds_write_b32 v21, a244 offset:5248\n\t" "ds_write_b32 v21, a245 offset:26368\n\t" "ds_write_b32 v21, a246 offset:15808\n\t" \
   "ds_write_b32 v21, a247 offset:36928\n\t" "s_waitcnt vmcnt(23)\n\t" "ds_write_b32 v21, v196 offset:5312\n\t" \
   "ds_write_b32 v21, v197 offset:26432\n\t" "ds_write_b32 v21, v198 offset:15872\n\t" "ds_write_b32 v21, v199 offset:36992\n\t" \
   "s_waitcnt vmcnt(22)\n\t" "ds_write_b32 v21, v212 offset:5376\n\t" "ds_write_b32 v21, v213 offset:26496\n\t" \
   "ds_write_b32 v21, v214 offset:15936\n\t" "ds_write_b32 v21, v215 offset:37056\n\t" "s_waitcnt vmcnt(21)\n\t" \
   "ds_write_b32 v21, v228 offset:5440\n\t" "ds_write_b32 v21, v229 offset:26560\n\t" "ds_write_b32 v21, v230 offset:16000\n\t" \
   "ds_write_b32 v21, v231 offset:37120\n\t" "s_waitcnt vmcnt(20)\n\t" "ds_write_b32 v21, v244 offset:5504\n\t" \
   "ds_write_b32 v21, v245 offset:26624\n\t" "ds_write_b32 v21, v246 offset:16064\n\t" "ds_write_b32 v21, v247 offset:37184\n\t" \
   "s_waitcnt lgkmcnt(0)\n\t" "s_barrier\n\t" "ds_read_b64_tr_b16 a[164:165], v22 offset:0\n\t" "ds_read_b64_tr_b16 a[166:167], v22 offset:5248\n\t" \
   "ds_read_b64_tr_b16 a[180:181], v22 offset:8\n\t" "ds_read_b64_tr_b16 a[182:183], v22 offset:5256\n\t" \
   "ds_read_b64_tr_b16 a[196:197], v22 offset:16\n\t" "ds_read_b64_tr_b16 a[198:199], v22 offset:5264\n\t" \
   "ds_read_b64_tr_b16 a[212:213], v22 offset:24\n\t" "ds_read_b64_tr_b16 a[214:215], v22 offset:5272\n\t" \
   "ds_read_b64_tr_b16 a[228:229], v22 offset:32\n\t" "ds_read_b64_tr_b16 a[230:231], v22 offset:5280\n\t" \
   "ds_read_b64_tr_b16 a[244:245], v22 offset:40\n\t" "ds_read_b64_tr_b16 a[246:247], v22 offset:5288\n\t" \
   "ds_read_b64_tr_b16 v[196:197], v22 offset:48\n\t" "ds_read_b64_tr_b16 v[198:199], v22 offset:5296\n\t" \
   "ds_read_b64_tr_b16 v[212:213], v22 offset:56\n\t" "ds_read_b64_tr_b16 v[214:215], v22 offset:5304\n\t" \
   "ds_read_b64_tr_b16 v[228:229], v22 offset:64\n\t" "ds_read_b64_tr_b16 v[230:231], v22 offset:5312\n\t" \
   "ds_read_b64_tr_b16 v[244:245], v22 offset:72\n\t" "ds_read_b64_tr_b16 v[246:247], v22 offset:5320\n\t" "s_waitcnt vmcnt(19)\n\t" \
   "s_waitcnt lgkmcnt(0)\n\t" "s_barrier\n\t" "ds_write_b32 v21, a168 offset:0\n\t" "ds_write_b32 v21, a169 offset:21120\n\t" \
   "ds_write_b32 v21, a170 offset:10560\n\t" "ds_write_b32 v21, a171 offset:31680\n\t" "s_waitcnt vmcnt(18)\n\t" "ds_write_b32 v21, a184 offset:64\n\t" \
   "ds_write_b32 v21, a185 offset:21184\n\t" "ds_write_b32 v21, a186 offset:10624\n\t" "ds_write_b32 v21, a187 offset:31744\n\t" \
   "s_waitcnt vmcnt(17)\n\t" "ds_write_b32 v21, a200 offset:128\n\t" "ds_write_b32 v21, a201 offset:21248\n\t" \
   "ds_write_b32 v21, a202 offset:10688\n\t" "ds_write_b32 v21, a203 offset:31808\n\t" "s_waitcnt vmcnt(16)\n\t" \
   "ds_write_b32 v21, a216 offset:192\n\t" "ds_write_b32 v21, a217 offset:21312\n\t" "ds_write_b32 v21, a218 offset:10752\n\t" \
   "ds_write_b32 v21, a219 offset:31872\n\t" "s_waitcnt vmcnt(15)\n\t" "ds_write_b32 v21, a232 offset:256\n\t" \
   "ds_write_b32 v21, a233 offset:21376\n\t" "ds_write_b32 v21, a234 offset:10816\n\t" "ds_write_b32 v21, a235 offset:31936\n\t" \
   "s_waitcnt vmcnt(14)\n\t" "ds_write_b32 v21, a248 offset:5248\n\t" "ds_write_b32 v21, a249 offset:26368\n\t" \
   "ds_write_b32 v21, a250 offset:15808\n\t" "ds_write_b32 v21, a251 offset:36928\n\t" "s_waitcnt vmcnt(13)\n\t" \
   "ds_write_b32 v21, v200 offset:5312\n\t" "ds_write_b32 v21, v201 offset:26432\n\t" "ds_write_b32 v21, v202 offset:15872\n\t" \
   "ds_write_b32 v21, v203 offset:36992\n\t" "s_waitcnt vmcnt(12)\n\t" "ds_write_b32 v21, v216 offset:5376\n\t" \
   "ds_write_b32 v21, v217 offset:26496\n\t" "ds_write_b32 v21, v218 offset:15936\n\t" "ds_write_b32 v21, v219 offset:37056\n\t" \
   "s_waitcnt vmcnt(11)\n\t" "ds_write_b32 v21, v232 offset:5440\n\t" "ds_write_b32 v21, v233 offset:26560\n\t" \
   "ds_write_b32 v21, v234 offset:16000\n\t" "ds_write_b32 v21, v235 offset:37120\n\t" "s_waitcnt vmcnt(10)\n\t" \
   "ds_write_b32 v21, v248 offset:5504\n\t" "ds_write_b32 v21, v249 offset:26624\n\t" "ds_write_b32 v21, v250 offset:16064\n\t" \
   "ds_write_b32 v21, v251 offset:37184\n\t" "s_waitcnt lgkmcnt(0)\n\t" "s_barrier\n\t" "ds_read_b64_tr_b16 a[168:169], v22 offset:0\n\t" \
   "ds_read_b64_tr_b16 a[170:171], v22 offset:5248\n\t" "ds_read_b64_tr_b16 a[184:185], v22 offset:8\n\t" \
   "ds_read_b64_tr_b16 a[186:187], v22 offset:5256\n\t" "ds_read_b64_tr_b16 a[200:201], v22 offset:16\n\t" \
   "ds_read_b64_tr_b16 a[202:203], v22 offset:5264\n\t" "ds_read_b64_tr_b16 a[216:217], v22 offset:24\n\t" \
   "ds_read_b64_tr_b16 a[218:219], v22 offset:5272\n\t" "ds_read_b64_tr_b16 a[232:233], v22 offset:32\n\t" \
   "ds_read_b64_tr_b16 a[234:235], v22 offset:5280\n\t" "ds_read_b64_tr_b16 a[248:249], v22 offset:40\n\t" \
   "ds_read_b64_tr_b16 a[250:251], v22 offset:5288\n\t" "ds_read_b64_tr_b16 v[200:201], v22 offset:48\n\t" \
   "ds_read_b64_tr_b16 v[202:203], v22 offset:5296\n\t" "ds_read_b64_tr_b16 v[216:217], v22 offset:56\n\t" \
   "ds_read_b64_tr_b16 v[218:219], v22 offset:5304\n\t" "ds_read_b64_tr_b16 v[232:233], v22 offset:64\n\t" \
   "ds_read_b64_tr_b16 v[234:235], v22 offset:5312\n\t" "ds_read_b64_tr_b16 v[248:249], v22 offset:72\n\t" \
   "ds_read_b64_tr_b16 v[250:251], v22 offset:5320\n\t" "s_waitcnt vmcnt(9)\n\t" "s_waitcnt lgkmcnt(0)\n\t" "s_barrier\n\t" \
   "ds_write_b32 v21, a172 offset:0\n\t" "ds_write_b32 v21, a173 offset:21120\n\t" "ds_write_b32 v21, a174 offset:10560\n\t" \
   "ds_write_b32 v21, a175 offset:31680\n\t" "s_waitcnt vmcnt(8)\n\t" "ds_write_b32 v21, a188 offset:64\n\t" "ds_write_b32 v21, a189 offset:21184\n\t" \
   "ds_write_b32 v21, a190 offset:10624\n\t" "ds_write_b32 v21, a191 offset:31744\n\t" "s_waitcnt vmcnt(7)\n\t" "ds_write_b32 v21, a204 offset:128\n\t" \
   "ds_write_b32 v21, a205 offset:21248\n\t" "ds_write_b32 v21, a206 offset:10688\n\t" "ds_write_b32 v21, a207 offset:31808\n\t" \
   "s_waitcnt vmcnt(6)\n\t" "ds_write_b32 v21, a220 offset:192\n\t" "ds_write_b32 v21, a221 offset:21312\n\t" "ds_write_b32 v21, a222 offset:10752\n\t" \
   "ds_write_b32 v21, a223 offset:31872\n\t" "s_waitcnt vmcnt(5)\n\t" "ds_write_b32 v21, a236 offset:256\n\t" "ds_write_b32 v21, a237 offset:21376\n\t" \
   "ds_write_b32 v21, a238 offset:10816\n\t" "ds_write_b32 v21, a239 offset:31936\n\t" "s_waitcnt vmcnt(4)\n\t" \
   "ds_write_b32 v21, a252 offset:5248\n\t" "ds_write_b32 v21, a253 offset:26368\n\t" "ds_write_b32 v21, a254 offset:15808\n\t" \
   "ds_write_b32 v21, a255 offset:36928\n\t" "s_waitcnt vmcnt(3)\n\t" "ds_write_b32 v21, v204 offset:5312\n\t" \
   "ds_write_b32 v21, v205 offset:26432\n\t" "ds_write_b32 v21, v206 offset:15872\n\t" "ds_write_b32 v21, v207 offset:36992\n\t" \
   "s_waitcnt vmcnt(2)\n\t" "ds_write_b32 v21, v220 offset:5376\n\t" "ds_write_b32 v21, v221 offset:26496\n\t" \
   "ds_write_b32 v21, v222 offset:15936\n\t" "ds_write_b32 v21, v223 offset:37056\n\t" "s_waitcnt vmcnt(1)\n\t" \
   "ds_write_b32 v21, v236 offset:5440\n\t" "ds_write_b32 v21, v237 offset:26560\n\t" "ds_write_b32 v21, v238 offset:16000\n\t" \
   "ds_write_b32 v21, v239 offset:37120\n\t" "s_waitcnt vmcnt(0)\n\t" "ds_write_b32 v21, v252 offset:5504\n\t" \
   "ds_write_b32 v21, v253 offset:26624\n\t" "ds_write_b32 v21, v254 offset:16064\n\t" "ds_write_b32 v21, v255 offset:37184\n\t" \
   "s_waitcnt lgkmcnt(0)\n\t" "s_barrier\n\t" "ds_read_b64_tr_b16 a[172:173], v22 offset:0\n\t" "ds_read_b64_tr_b16 a[174:175], v22 offset:5248\n\t" \
   "ds_read_b64_tr_b16 a[188:189], v22 offset:8\n\t" "ds_read_b64_tr_b16 a[190:191], v22 offset:5256\n\t" \
   "ds_read_b64_tr_b16 a[204:205], v22 offset:16\n\t" "ds_read_b64_tr_b16 a[206:207], v22 offset:5264\n\t" \
   "ds_read_b64_tr_b16 a[220:221], v22 offset:24\n\t" "ds_read_b64_tr_b16 a[222:223], v22 offset:5272\n\t" \
   "ds_read_b64_tr_b16 a[236:237], v22 offset:32\n\t" "ds_read_b64_tr_b16 a[238:239], v22 offset:5280\n\t" \
   "ds_read_b64_tr_b16 a[252:253], v22 offset:40\n\t" "ds_read_b64_tr_b16 a[254:255], v22 offset:5288\n\t" \
   "ds_read_b64_tr_b16 v[204:205], v22 offset:48\n\t" "ds_read_b64_tr_b16 v[206:207], v22 offset:5296\n\t" \
   "ds_read_b64_tr_b16 v[220:221], v22 offset:56\n\t" "ds_read_b64_tr_b16 v[222:223], v22 offset:5304\n\t" \
   "ds_read_b64_tr_b16 v[236:237], v22 offset:64\n\t" "ds_read_b64_tr_b16 v[238:239], v22 offset:5312\n\t" \
   "ds_read_b64_tr_b16 v[252:253], v22 offset:72\n\t" "ds_read_b64_tr_b16 v[254:255], v22 offset:5320\n\t" "s_waitcnt lgkmcnt(0)"
#define LDV_CLOB "v24", "v25", "s40", "s41"
#define LDY_ASM \
   "s_mov_b32 s41, 0\n\t" "buffer_load_dwordx4 a[0:3], v16, %[ra], s41 offen\n\t" "s_mov_b32 s41, 131072\n\t" \
   "buffer_load_dwordx4 a[64:67], v16, %[ra], s41 offen\n\t" "s_mov_b32 s41, 524288\n\t" "buffer_load_dwordx4 a[8:11], v16, %[ra], s41 offen\n\t" \
   "s_mov_b32 s41, 655360\n\t" "buffer_load_dwordx4 a[72:75], v16, %[ra], s41 offen\n\t" "s_mov_b32 s41, 1048576\n\t" \
   "buffer_load_dwordx4 a[16:19], v16, %[ra], s41 offen\n\t" "s_mov_b32 s41, 1179648\n\t" "buffer_load_dwordx4 a[80:83], v16, %[ra], s41 offen\n\t" \
   "s_mov_b32 s41, 1572864\n\t" "buffer_load_dwordx4 a[24:27], v16, %[ra], s41 offen\n\t" "s_mov_b32 s41, 1703936\n\t" \
   "buffer_load_dwordx4 a[88:91], v16, %[ra], s41 offen\n\t" "s_mov_b32 s41, 2097152\n\t" "buffer_load_dwordx4 a[32:35], v16, %[ra], s41 offen\n\t" \
   "s_mov_b32 s41, 2228224\n\t" "buffer_load_dwordx4 a[96:99], v16, %[ra], s41 offen\n\t" "s_mov_b32 s41, 2621440\n\t" \
   "buffer_load_dwordx4 a[40:43], v16, %[ra], s41 offen\n\t" "s_mov_b32 s41, 2752512\n\t" "buffer_load_dwordx4 a[104:107], v16, %[ra], s41 offen\n\t" \
   "s_mov_b32 s41, 3145728\n\t" "buffer_load_dwordx4 a[48:51], v16, %[ra], s41 offen\n\t" "s_mov_b32 s41, 3276800\n\t" \
   "buffer_load_dwordx4 a[112:115], v16, %[ra], s41 offen\n\t" "s_mov_b32 s41, 3670016\n\t" "buffer_load_dwordx4 a[56:59], v16, %[ra], s41 offen\n\t" \
   "s_mov_b32 s41, 3801088\n\t" "buffer_load_dwordx4 a[120:123], v16, %[ra], s41 offen\n\t" "s_mov_b32 s41, 4194304\n\t" \
   "buffer_load_dwordx4 v[192:195], v16, %[ra], s41 offen\n\t" "s_mov_b32 s41, 4325376\n\t" "buffer_load_dwordx4 v[208:211], v16, %[ra], s41 offen\n\t" \
   "s_mov_b32 s41, 4718592\n\t" "buffer_load_dwordx4 v[200:203], v16, %[ra], s41 offen\n\t" "s_mov_b32 s41, 4849664\n\t" \
   "buffer_load_dwordx4 v[216:219], v16, %[ra], s41 offen\n\t" "s_mov_b32 s41, 262144\n\t" "buffer_load_dwordx4 a[4:7], v16, %[ra], s41 offen\n\t" \
   "s_mov_b32 s41, 393216\n\t" "buffer_load_dwordx4 a[68:71], v16, %[ra], s41 offen\n\t" "s_mov_b32 s41, 786432\n\t" \
   "buffer_load_dwordx4 a[12:15], v16, %[ra], s41 offen\n\t" "s_mov_b32 s41, 917504\n\t" "buffer_load_dwordx4 a[76:79], v16, %[ra], s41 offen\n\t" \
   "s_mov_b32 s41, 1310720\n\t" "buffer_load_dwordx4 a[20:23], v16, %[ra], s41 offen\n\t" "s_mov_b32 s41, 1441792\n\t" \
   "buffer_load_dwordx4 a[84:87], v16, %[ra], s41 offen\n\t" "s_mov_b32 s41, 1835008\n\t" "buffer_load_dwordx4 a[28:31], v16, %[ra], s41 offen\n\t" \
   "s_mov_b32 s41, 1966080\n\t" "buffer_load_dwordx4 a[92:95], v16, %[ra], s41 offen\n\t" "s_mov_b32 s41, 2359296\n\t" \
   "buffer_load_dwordx4 a[36:39], v16, %[ra], s41 offen\n\t" "s_mov_b32 s41, 2490368\n\t" "buffer_load_dwordx4 a[100:103], v16, %[ra], s41 offen\n\t" \
   "s_mov_b32 s41, 2883584\n\t" "buffer_load_dwordx4 a[44:47], v16, %[ra], s41 offen\n\t" "s_mov_b32 s41, 3014656\n\t" \
   "buffer_load_dwordx4 a[108:111], v16, %[ra], s41 offen\n\t" "s_mov_b32 s41, 3407872\n\t" "buffer_load_dwordx4 a[52:55], v16, %[ra], s41 offen\n\t" \
   "s_mov_b32 s41, 3538944\n\t" "buffer_load_dwordx4 a[116:119], v16, %[ra], s41 offen\n\t" "s_mov_b32 s41, 3932160\n\t" \
   "buffer_load_dwordx4 a[60:63], v16, %[ra], s41 offen\n\t" "s_mov_b32 s41, 4063232\n\t" "buffer_load_dwordx4 a[124:127], v16, %[ra], s41 offen\n\t" \
   "s_mov_b32 s41, 4456448\n\t" "buffer_load_dwordx4 v[196:199], v16, %[ra], s41 offen\n\t" "s_mov_b32 s41, 4587520\n\t" \
   "buffer_load_dwordx4 v[212:215], v16, %[ra], s41 offen\n\t" "s_mov_b32 s41, 4980736\n\t" "buffer_load_dwordx4 v[204:207], v16, %[ra], s41 offen\n\t" \
   "s_mov_b32 s41, 5111808\n\t" "buffer_load_dwordx4 v[220:223], v16, %[ra], s41 offen\n\t" "s_mov_b32 s41, 0\n\t" \
   "buffer_load_dwordx4 a[128:131], v17, %[rb], s41 offen\n\t" "s_mov_b32 s41, 256\n\t" "buffer_load_dwordx4 a[132:135], v17, %[rb], s41 offen\n\t" \
   "s_mov_b32 s41, 512\n\t" "buffer_load_dwordx4 a[160:163], v17, %[rb], s41 offen\n\t" "s_mov_b32 s41, 768\n\t" \
   "buffer_load_dwordx4 a[164:167], v17, %[rb], s41 offen\n\t" "s_mov_b32 s41, 1024\n\t" "buffer_load_dwordx4 a[192:195], v17, %[rb], s41 offen\n\t" \
   "s_mov_b32 s41, 20480\n\t" "buffer_load_dwordx4 a[196:199], v17, %[rb], s41 offen\n\t" "s_mov_b32 s41, 20736\n\t" \
   "buffer_load_dwordx4 a[224:227], v17, %[rb], s41 offen\n\t" "s_mov_b32 s41, 20992\n\t" "buffer_load_dwordx4 a[228:231], v17, %[rb], s41 offen\n\t" \
   "s_mov_b32 s41, 21248\n\t" "buffer_load_dwordx4 v[224:227], v17, %[rb], s41 offen\n\t" "s_mov_b32 s41, 21504\n\t" \
   "buffer_load_dwordx4 v[228:231], v17, %[rb], s41 offen\n\t" "s_mov_b32 s41, 40960\n\t" "buffer_load_dwordx4 a[136:139], v17, %[rb], s41 offen\n\t" \
   "s_mov_b32 s41, 41216\n\t" "buffer_load_dwordx4 a[140:143], v17, %[rb], s41 offen\n\t" "s_mov_b32 s41, 41472\n\t" \
   "buffer_load_dwordx4 a[168:171], v17, %[rb], s41 offen\n\t" "s_mov_b32 s41, 41728\n\t" "buffer_load_dwordx4 a[172:175], v17, %[rb], s41 offen\n\t" \
   "s_mov_b32 s41, 41984\n\t" "buffer_load_dwordx4 a[200:203], v17, %[rb], s41 offen\n\t" "s_mov_b32 s41, 61440\n\t" \
   "buffer_load_dwordx4 a[204:207], v17, %[rb], s41 offen\n\t" "s_mov_b32 s41, 61696\n\t" "buffer_load_dwordx4 a[232:235], v17, %[rb], s41 offen\n\t" \
   "s_mov_b32 s41, 61952\n\t" "buffer_load_dwordx4 a[236:239], v17, %[rb], s41 offen\n\t" "s_mov_b32 s41, 62208\n\t" \
   "buffer_load_dwordx4 v[232:235], v17, %[rb], s41 offen\n\t" "s_mov_b32 s41, 62464\n\t" "buffer_load_dwordx4 v[236:239], v17, %[rb], s41 offen\n\t" \
   "s_waitcnt vmcnt(59)\n\t" "ds_write_b128 v20, a[0:3] offset:0\n\t" "s_mov_b32 s41, 81920\n\t" \
   "buffer_load_dwordx4 a[144:147], v17, %[rb], s41 offen\n\t" "s_waitcnt vmcnt(59)\n\t" "ds_write_b128 v20, a[64:67] offset:4096\n\t" \
   "s_mov_b32 s41, 82176\n\t" "buffer_load_dwordx4 a[148:151], v17, %[rb], s41 offen\n\t" "s_waitcnt vmcnt(59)\n\t" \
   "ds_write_b128 v20, a[8:11] offset:8192\n\t" "s_mov_b32 s41, 82432\n\t" "buffer_load_dwordx4 a[176:179], v17, %[rb], s41 offen\n\t" \
   "s_waitcnt vmcnt(59)\n\t" "ds_write_b128 v20, a[72:75] offset:12288\n\t" "s_mov_b32 s41, 82688\n\t" \
   "buffer_load_dwordx4 a[180:183], v17, %[rb], s41 offen\n\t" "s_waitcnt vmcnt(59)\n\t" "ds_write_b128 v20, a[16:19] offset:16384\n\t" \
   "s_mov_b32 s41, 82944\n\t" "buffer_load_dwordx4 a[208:211], v17, %[rb], s41 offen\n\t" "s_waitcnt vmcnt(59)\n\t" \
   "ds_write_b128 v20, a[80:83] offset:20480\n\t" "s_mov_b32 s41, 102400\n\t" "buffer_load_dwordx4 a[212:215], v17, %[rb], s41 offen\n\t" \
   "s_waitcnt vmcnt(59)\n\t" "ds_write_b128 v20, a[24:27] offset:24576\n\t" "s_mov_b32 s41, 102656\n\t" \
   "buffer_load_dwordx4 a[240:243], v17, %[rb], s41 offen\n\t" "s_waitcnt vmcnt(59)\n\t" "ds_write_b128 v20, a[88:91] offset:28672\n\t" \
   "s_mov_b32 s41, 102912\n\t" "buffer_load_dwordx4 a[244:247], v17, %[rb], s41 offen\n\t" "s_waitcnt vmcnt(59)\n\t" \
   "ds_write_b128 v20, a[32:35] offset:32768\n\t" "s_mov_b32 s41, 103168\n\t" "buffer_load_dwordx4 v[240:243], v17, %[rb], s41 offen\n\t" \
   "s_waitcnt vmcnt(59)\n\t" "ds_write_b128 v20, a[96:99] offset:36864\n\t" "s_mov_b32 s41, 103424\n\t" \
   "buffer_load_dwordx4 v[244:247], v17, %[rb], s41 offen\n\t" "s_waitcnt vmcnt(59)\n\t" "ds_write_b128 v20, a[40:43] offset:40960\n\t" \
   "s_mov_b32 s41, 122880\n\t" "buffer_load_dwordx4 a[152:155], v17, %[rb], s41 offen\n\t" "s_waitcnt vmcnt(59)\n\t" \
   "ds_write_b128 v20, a[104:107] offset:45056\n\t" "s_mov_b32 s41, 123136\n\t" "buffer_load_dwordx4 a[156:159], v17, %[rb], s41 offen\n\t" \
   "s_waitcnt vmcnt(59)\n\t" "ds_write_b128 v20, a[48:51] offset:49152\n\t" "s_mov_b32 s41, 123392\n\t" \
   "buffer_load_dwordx4 a[184:187], v17, %[rb], s41 offen\n\t" "s_waitcnt vmcnt(59)\n\t" "ds_write_b128 v20, a[112:115] offset:53248\n\t" \
   "s_mov_b32 s41, 123648\n\t" "buffer_load_dwordx4 a[188:191], v17, %[rb], s41 offen\n\t" "s_waitcnt vmcnt(59)\n\t" \
   "ds_write_b128 v20, a[56:59] offset:57344\n\t" "s_mov_b32 s41, 123904\n\t" "buffer_load_dwordx4 a[216:219], v17, %[rb], s41 offen\n\t" \
   "s_waitcnt vmcnt(59)\n\t" "ds_write_b128 v20, a[120:123] offset:61440\n\t" "s_mov_b32 s41, 143360\n\t" \
   "buffer_load_dwordx4 a[220:223], v17, %[rb], s41 offen\n\t" "s_waitcnt vmcnt(59)\n\t" "ds_write_b128 v23, v[192:195] offset:0\n\t" \
   "s_mov_b32 s41, 143616\n\t" "buffer_load_dwordx4 a[248:251], v17, %[rb], s41 offen\n\t" "s_waitcnt vmcnt(59)\n\t" \
   "ds_write_b128 v23, v[208:211] offset:4096\n\t" "s_mov_b32 s41, 143872\n\t" "buffer_load_dwordx4 a[252:255], v17, %[rb], s41 offen\n\t" \
   "s_waitcnt vmcnt(59)\n\t" "ds_write_b128 v23, v[200:203] offset:8192\n\t" "s_mov_b32 s41, 144128\n\t" \
   "buffer_load_dwordx4 v[248:251], v17, %[rb], s41 offen\n\t" "s_waitcnt vmcnt(59)\n\t" "ds_write_b128 v23, v[216:219] offset:12288\n\t" \
   "s_mov_b32 s41, 144384\n\t" "buffer_load_dwordx4 v[252:255], v17, %[rb], s41 offen\n\t" "s_waitcnt lgkmcnt(0)\n\t" "s_barrier\n\t" \
   "s_lshl_b32 s40, %[p0], 13\n\t" "v_add_u32 v24, s40, v18\n\t" "v_add_u32 v25, s40, v19\n\t" "s_add_u32 s40, s40, 8192\n\t" \
   "s_cmp_eq_u32 s40, 81920\n\t" "s_cselect_b32 s40, 0, s40\n\t" "ds_read_b64_tr_b16 a[0:1], v24 offset:0\n\t" \
   "ds_read_b64_tr_b16 a[2:3], v24 offset:4096\n\t" "ds_read_b64_tr_b16 a[64:65], v25 offset:0\n\t" "ds_read_b64_tr_b16 a[66:67], v25 offset:4096\n\t" \
   "v_add_u32 v24, s40, v18\n\t" "v_add_u32 v25, s40, v19\n\t" "s_add_u32 s40, s40, 8192\n\t" "s_cmp_eq_u32 s40, 81920\n\t" \
   "s_cselect_b32 s40, 0, s40\n\t" "ds_read_b64_tr_b16 a[8:9], v24 offset:0\n\t" "ds_read_b64_tr_b16 a[10:11], v24 offset:4096\n\t" \
   "ds_read_b64_tr_b16 a[72:73], v25 offset:0\n\t" "ds_read_b64_tr_b16 a[74:75], v25 offset:4096\n\t" "v_add_u32 v24, s40, v18\n\t" \
   "v_add_u32 v25, s40, v19\n\t" "s_add_u32 s40, s40, 8192\n\t" "s_cmp_eq_u32 s40, 81920\n\t" "s_cselect_b32 s40, 0, s40\n\t" \
   "ds_read_b64_tr_b16 a[16:17], v24 offset:0\n\t" "ds_read_b64_tr_b16 a[18:19], v24 offset:4096\n\t" "ds_read_b64_tr_b16 a[80:81], v25 offset:0\n\t" \
   "ds_read_b64_tr_b16 a[82:83], v25 offset:4096\n\t" "v_add_u32 v24, s40, v18\n\t" "v_add_u32 v25, s40, v19\n\t" "s_add_u32 s40, s40, 8192\n\t" \
   "s_cmp_eq_u32 s40, 81920\n\t" "s_cselect_b32 s40, 0, s40\n\t" "ds_read_b64_tr_b16 a[24:25], v24 offset:0\n\t" \
   "ds_read_b64_tr_b16 a[26:27], v24 offset:4096\n\t" "ds_read_b64_tr_b16 a[88:89], v25 offset:0\n\t" \
   "ds_read_b64_tr_b16 a[90:91], v25 offset:4096\n\t" "v_add_u32 v24, s40, v18\n\t" "v_add_u32 v25, s40, v19\n\t" "s_add_u32 s40, s40, 8192\n\t" \
   "s_cmp_eq_u32 s40, 81920\n\t" "s_cselect_b32 s40, 0, s40\n\t" "ds_read_b64_tr_b16 a[32:33], v24 offset:0\n\t" \
   "ds_read_b64_tr_b16 a[34:35], v24 offset:4096\n\t" "ds_read_b64_tr_b16 a[96:97], v25 offset:0\n\t" \
   "ds_read_b64_tr_b16 a[98:99], v25 offset:4096\n\t" "v_add_u32 v24, s40, v18\n\t" "v_add_u32 v25, s40, v19\n\t" "s_add_u32 s40, s40, 8192\n\t" \
   "s_cmp_eq_u32 s40, 81920\n\t" "s_cselect_b32 s40, 0, s40\n\t" "ds_read_b64_tr_b16 a[40:41], v24 offset:0\n\t" \
   "ds_read_b64_tr_b16 a[42:43], v24 offset:4096\n\t" "ds_read_b64_tr_b16 a[104:105], v25 offset:0\n\t" \
   "ds_read_b64_tr_b16 a[106:107], v25 offset:4096\n\t" "v_add_u32 v24, s40, v18\n\t" "v_add_u32 v25, s40, v19\n\t" "s_add_u32 s40, s40, 8192\n\t" \
   "s_cmp_eq_u32 s40, 81920\n\t" "s_cselect_b32 s40, 0, s40\n\t" "ds_read_b64_tr_b16 a[48:49], v24 offset:0\n\t" \
   "ds_read_b64_tr_b16 a[50:51], v24 offset:4096\n\t" "ds_read_b64_tr_b16 a[112:113], v25 offset:0\n\t" \
   "ds_read_b64_tr_b16 a[114:115], v25 offset:4096\n\t" "v_add_u32 v24, s40, v18\n\t" "v_add_u32 v25, s40, v19\n\t" "s_add_u32 s40, s40, 8192\n\t" \
   "s_cmp_eq_u32 s40, 81920\n\t" "s_cselect_b32 s40, 0, s40\n\t" "ds_read_b64_tr_b16 a[56:57], v24 offset:0\n\t" \
   "ds_read_b64_tr_b16 a[58:59], v24 offset:4096\n\t" "ds_read_b64_tr_b16 a[120:121], v25 offset:0\n\t" \
   "ds_read_b64_tr_b16 a[122:123], v25 offset:4096\n\t" "v_add_u32 v24, s40, v18\n\t" "v_add_u32 v25, s40, v19\n\t" "s_add_u32 s40, s40, 8192\n\t" \
   "s_cmp_eq_u32 s40, 81920\n\t" "s_cselect_b32 s40, 0, s40\n\t" "ds_read_b64_tr_b16 v[192:193], v24 offset:0\n\t" \
   "ds_read_b64_tr_b16 v[194:195], v24 offset:4096\n\t" "ds_read_b64_tr_b16 v[208:209], v25 offset:0\n\t" \
   "ds_read_b64_tr_b16 v[210:211], v25 offset:4096\n\t" "v_add_u32 v24, s40, v18\n\t" "v_add_u32 v25, s40, v19\n\t" "s_add_u32 s40, s40, 8192\n\t" \
   "s_cmp_eq_u32 s40, 81920\n\t" "s_cselect_b32 s40, 0, s40\n\t" "ds_read_b64_tr_b16 v[200:201], v24 offset:0\n\t" \
   "ds_read_b64_tr_b16 v[202:203], v24 offset:4096\n\t" "ds_read_b64_tr_b16 v[216:217], v25 offset:0\n\t" \
   "ds_read_b64_tr_b16 v[218:219], v25 offset:4096\n\t" "s_waitcnt vmcnt(59)\n\t" "s_waitcnt lgkmcnt(0)\n\t" "s_barrier\n\t" \
   "ds_write_b128 v20, a[4:7] offset:0\n\t" "s_waitcnt vmcnt(58)\n\t" "ds_write_b128 v20, a[68:71] offset:4096\n\t" "s_waitcnt vmcnt(57)\n\t" \
   "ds_write_b128 v20, a[12:15] offset:8192\n\t" "s_waitcnt vmcnt(56)\n\t" "ds_write_b128 v20, a[76:79] offset:12288\n\t" "s_waitcnt vmcnt(55)\n\t" \
   "ds_write_b128 v20, a[20:23] offset:16384\n\t" "s_waitcnt vmcnt(54)\n\t" "ds_write_b128 v20, a[84:87] offset:20480\n\t" "s_waitcnt vmcnt(53)\n\t" \
   "ds_write_b128 v20, a[28:31] offset:24576\n\t" "s_waitcnt vmcnt(52)\n\t" "ds_write_b128 v20, a[92:95] offset:28672\n\t" "s_waitcnt vmcnt(51)\n\t" \
   "ds_write_b128 v20, a[36:39] offset:32768\n\t" "s_waitcnt vmcnt(50)\n\t" "ds_write_b128 v20, a[100:103] offset:36864\n\t" "s_waitcnt vmcnt(49)\n\t" \
   "ds_write_b128 v20, a[44:47] offset:40960\n\t" "s_waitcnt vmcnt(48)\n\t" "ds_write_b128 v20, a[108:111] offset:45056\n\t" "s_waitcnt vmcnt(47)\n\t" \
   "ds_write_b128 v20, a[52:55] offset:49152\n\t" "s_waitcnt vmcnt(46)\n\t" "ds_write_b128 v20, a[116:119] offset:53248\n\t" "s_waitcnt vmcnt(45)\n\t" \
   "ds_write_b128 v20, a[60:63] offset:57344\n\t" "s_waitcnt vmcnt(44)\n\t" "ds_write_b128 v20, a[124:127] offset:61440\n\t" "s_waitcnt vmcnt(43)\n\t" \
   "ds_write_b128 v23, v[196:199] offset:0\n\t" "s_waitcnt vmcnt(42)\n\t" "ds_write_b128 v23, v[212:215] offset:4096\n\t" "s_waitcnt vmcnt(41)\n\t" \
   "ds_write_b128 v23, v[204:207] offset:8192\n\t" "s_waitcnt vmcnt(40)\n\t" "ds_write_b128 v23, v[220:223] offset:12288\n\t" \
   "s_waitcnt lgkmcnt(0)\n\t" "s_barrier\n\t" "s_lshl_b32 s40, %[p0], 13\n\t" "v_add_u32 v24, s40, v18\n\t" "v_add_u32 v25, s40, v19\n\t" \
   "s_add_u32 s40, s40, 8192\n\t" "s_cmp_eq_u32 s40, 81920\n\t" "s_cselect_b32 s40, 0, s40\n\t" "ds_read_b64_tr_b16 a[4:5], v24 offset:0\n\t" \
   "ds_read_b64_tr_b16 a[6:7], v24 offset:4096\n\t" "ds_read_b64_tr_b16 a[68:69], v25 offset:0\n\t" "ds_read_b64_tr_b16 a[70:71], v25 offset:4096\n\t" \
   "v_add_u32 v24, s40, v18\n\t" "v_add_u32 v25, s40, v19\n\t" "s_add_u32 s40, s40, 8192\n\t" "s_cmp_eq_u32 s40, 81920\n\t" \
   "s_cselect_b32 s40, 0, s40\n\t" "ds_read_b64_tr_b16 a[12:13], v24 offset:0\n\t" "ds_read_b64_tr_b16 a[14:15], v24 offset:4096\n\t" \
   "ds_read_b64_tr_b16 a[76:77], v25 offset:0\n\t" "ds_read_b64_tr_b16 a[78:79], v25 offset:4096\n\t" "v_add_u32 v24, s40, v18\n\t" \
   "v_add_u32 v25, s40, v19\n\t" "s_add_u32 s40, s40, 8192\n\t" "s_cmp_eq_u32 s40, 81920\n\t" "s_cselect_b32 s40, 0, s40\n\t" \
   "ds_read_b64_tr_b16 a[20:21], v24 offset:0\n\t" "ds_read_b64_tr_b16 a[22:23], v24 offset:4096\n\t" "ds_read_b64_tr_b16 a[84:85], v25 offset:0\n\t" \
   "ds_read_b64_tr_b16 a[86:87], v25 offset:4096\n\t" "v_add_u32 v24, s40, v18\n\t" "v_add_u32 v25, s40, v19\n\t" "s_add_u32 s40, s40, 8192\n\t" \
   "s_cmp_eq_u32 s40, 81920\n\t" "s_cselect_b32 s40, 0, s40\n\t" "ds_read_b64_tr_b16 a[28:29], v24 offset:0\n\t" \
   "ds_read_b64_tr_b16 a[30:31], v24 offset:4096\n\t" "ds_read_b64_tr_b16 a[92:93], v25 offset:0\n\t" \
   "ds_read_b64_tr_b16 a[94:95], v25 offset:4096\n\t" "v_add_u32 v24, s40, v18\n\t" "v_add_u32 v25, s40, v19\n\t" "s_add_u32 s40, s40, 8192\n\t" \
   "s_cmp_eq_u32 s40, 81920\n\t" "s_cselect_b32 s40, 0, s40\n\t" "ds_read_b64_tr_b16 a[36:37], v24 offset:0\n\t" \
   "ds_read_b64_tr_b16 a[38:39], v24 offset:4096\n\t" "ds_read_b64_tr_b16 a[100:101], v25 offset:0\n\t" \
   "ds_read_b64_tr_b16 a[102:103], v25 offset:4096\n\t" "v_add_u32 v24, s40, v18\n\t" "v_add_u32 v25, s40, v19\n\t" "s_add_u32 s40, s40, 8192\n\t" \
   "s_cmp_eq_u32 s40, 81920\n\t" "s_cselect_b32 s40, 0, s40\n\t" "ds_read_b64_tr_b16 a[44:45], v24 offset:0\n\t" \
   "ds_read_b64_tr_b16 a[46:47], v24 offset:4096\n\t" "ds_read_b64_tr_b16 a[108:109], v25 offset:0\n\t" \
   "ds_read_b64_tr_b16 a[110:111], v25 offset:4096\n\t" "v_add_u32 v24, s40, v18\n\t" "v_add_u32 v25, s40, v19\n\t" "s_add_u32 s40, s40, 8192\n\t" \
   "s_cmp_eq_u32 s40, 81920\n\t" "s_cselect_b32 s40, 0, s40\n\t" "ds_read_b64_tr_b16 a[52:53], v24 offset:0\n\t" \
   "ds_read_b64_tr_b16 a[54:55], v24 offset:4096\n\t" "ds_read_b64_tr_b16 a[116:117], v25 offset:0\n\t" \
   "ds_read_b64_tr_b16 a[118:119], v25 offset:4096\n\t" "v_add_u32 v24, s40, v18\n\t" "v_add_u32 v25, s40, v19\n\t" "s_add_u32 s40, s40, 8192\n\t" \
   "s_cmp_eq_u32 s40, 81920\n\t" "s_cselect_b32 s40, 0, s40\n\t" "ds_read_b64_tr_b16 a[60:61], v24 offset:0\n\t" \
   "ds_read_b64_tr_b16 a[62:63], v24 offset:4096\n\t" "ds_read_b64_tr_b16 a[124:125], v25 offset:0\n\t" \
   "ds_read_b64_tr_b16 a[126:127], v25 offset:4096\n\t" "v_add_u32 v24, s40, v18\n\t" "v_add_u32 v25, s40, v19\n\t" "s_add_u32 s40, s40, 8192\n\t" \
   "s_cmp_eq_u32 s40, 81920\n\t" "s_cselect_b32 s40, 0, s40\n\t" "ds_read_b64_tr_b16 v[196:197], v24 offset:0\n\t" \
   "ds_read_b64_tr_b16 v[198:199], v24 offset:4096\n\t" "ds_read_b64_tr_b16 v[212:213], v25 offset:0\n\t" \
   "ds_read_b64_tr_b16 v[214:215], v25 offset:4096\n\t" "v_add_u32 v24, s40, v18\n\t" "v_add_u32 v25, s40, v19\n\t" "s_add_u32 s40, s40, 8192\n\t" \
   "s_cmp_eq_u32 s40, 81920\n\t" "s_cselect_b32 s40, 0, s40\n\t" "ds_read_b64_tr_b16 v[204:205], v24 offset:0\n\t" \
   "ds_read_b64_tr_b16 v[206:207], v24 offset:4096\n\t" "ds_read_b64_tr_b16 v[220:221], v25 offset:0\n\t" \
   "ds_read_b64_tr_b16 v[222:223], v25 offset:4096\n\t" "s_waitcnt vmcnt(39)\n\t" "s_waitcnt lgkmcnt(0)\n\t" "s_barrier\n\t" \
   "ds_write_b32 v21, a128 offset:0\n\t" "ds_write_b32 v21, a129 offset:21120\n\t" "ds_write_b32 v21, a130 offset:10560\n\t" \
   "ds_write_b32 v21, a131 offset:31680\n\t" "s_waitcnt vmcnt(38)\n\t" "ds_write_b32 v21, a132 offset:64\n\t" "ds_write_b32 v21, a133 offset:21184\n\t" \
   "ds_write_b32 v21, a134 offset:10624\n\t" "ds_write_b32 v21, a135 offset:31744\n\t" "s_waitcnt vmcnt(37)\n\t" \
   "ds_write_b32 v21, a160 offset:128\n\t" "ds_write_b32 v21, a161 offset:21248\n\t" "ds_write_b32 v21, a162 offset:10688\n\t" \
   "ds_write_b32 v21, a163 offset:31808\n\t" "s_waitcnt vmcnt(36)\n\t" "ds_write_b32 v21, a164 offset:192\n\t" \
   "ds_write_b32 v21, a165 offset:21312\n\t" "ds_write_b32 v21, a166 offset:10752\n\t" "ds_write_b32 v21, a167 offset:31872\n\t" \
   "s_waitcnt vmcnt(35)\n\t" "ds_write_b32 v21, a192 offset:256\n\t" "ds_write_b32 v21, a193 offset:21376\n\t" \
   "ds_write_b32 v21, a194 offset:10816\n\t" "ds_write_b32 v21, a195 offset:31936\n\t" "s_waitcnt vmcnt(34)\n\t" \
   "ds_write_b32 v21, a196 offset:5248\n\t" "ds_write_b32 v21, a197 offset:26368\n\t" "ds_write_b32 v21, a198 offset:15808\n\t" \
   "ds_write_b32 v21, a199 offset:36928\n\t" "s_waitcnt vmcnt(33)\n\t" "ds_write_b32 v21, a224 offset:5312\n\t" \
   "ds_write_b32 v21, a225 offset:26432\n\t" "ds_write_b32 v21, a226 offset:15872\n\t" "ds_write_b32 v21, a227 offset:36992\n\t" \
   "s_waitcnt vmcnt(32)\n\t" "ds_write_b32 v21, a228 offset:5376\n\t" "ds_write_b32 v21, a229 offset:26496\n\t" \
   "ds_write_b32 v21, a230 offset:15936\n\t" "ds_write_b32 v21, a231 offset:37056\n\t" "s_waitcnt vmcnt(31)\n\t" \
   "ds_write_b32 v21, v224 offset:5440\n\t" "ds_write_b32 v21, v225 offset:26560\n\t" "ds_write_b32 v21, v226 offset:16000\n\t" \
   "ds_write_b32 v21, v227 offset:37120\n\t" "s_waitcnt vmcnt(30)\n\t" "ds_write_b32 v21, v228 offset:5504\n\t" \
   "ds_write_b32 v21, v229 offset:26624\n\t" "ds_write_b32 v21, v230 offset:16064\n\t" "ds_write_b32 v21, v231 offset:37184\n\t" \
   "s_waitcnt lgkmcnt(0)\n\t" "s_barrier\n\t" "ds_read_b64_tr_b16 a[128:129], v22 offset:0\n\t" "ds_read_b64_tr_b16 a[130:131], v22 offset:656\n\t" \
   "ds_read_b64_tr_b16 a[132:133], v22 offset:2624\n\t" "ds_read_b64_tr_b16 a[134:135], v22 offset:3280\n\t" \
   "ds_read_b64_tr_b16 a[160:161], v22 offset:16\n\t" "ds_read_b64_tr_b16 a[162:163], v22 offset:672\n\t" \
   "ds_read_b64_tr_b16 a[164:165], v22 offset:2640\n\t" "ds_read_b64_tr_b16 a[166:167], v22 offset:3296\n\t" \
   "ds_read_b64_tr_b16 a[192:193], v22 offset:32\n\t" "ds_read_b64_tr_b16 a[194:195], v22 offset:688\n\t" \
   "ds_read_b64_tr_b16 a[196:197], v22 offset:2656\n\t" "ds_read_b64_tr_b16 a[198:199], v22 offset:3312\n\t" \
   "ds_read_b64_tr_b16 a[224:225], v22 offset:48\n\t" "ds_read_b64_tr_b16 a[226:227], v22 offset:704\n\t" \
   "ds_read_b64_tr_b16 a[228:229], v22 offset:2672\n\t" "ds_read_b64_tr_b16 a[230:231], v22 offset:3328\n\t" \
   "ds_read_b64_tr_b16 v[224:225], v22 offset:64\n\t" "ds_read_b64_tr_b16 v[226:227], v22 offset:720\n\t" \
   "ds_read_b64_tr_b16 v[228:229], v22 offset:2688\n\t" "ds_read_b64_tr_b16 v[230:231], v22 offset:3344\n\t" "s_waitcnt vmcnt(29)\n\t" \
   "s_waitcnt lgkmcnt(0)\n\t" "s_barrier\n\t" "ds_write_b32 v21, a136 offset:0\n\t" "ds_write_b32 v21, a137 offset:21120\n\t" \
   "ds_write_b32 v21, a138 offset:10560\n\t" "ds_write_b32 v21, a139 offset:31680\n\t" "s_waitcnt vmcnt(28)\n\t" "ds_write_b32 v21, a140 offset:64\n\t" \
   "ds_write_b32 v21, a141 offset:21184\n\t" "ds_write_b32 v21, a142 offset:10624\n\t" "ds_write_b32 v21, a143 offset:31744\n\t" \
   "s_waitcnt vmcnt(27)\n\t" "ds_write_b32 v21, a168 offset:128\n\t" "ds_write_b32 v21, a169 offset:21248\n\t" \
   "ds_write_b32 v21, a170 offset:10688\n\t" "ds_write_b32 v21, a171 offset:31808\n\t" "s_waitcnt vmcnt(26)\n\t" \
   "ds_write_b32 v21, a172 offset:192\n\t" "ds_write_b32 v21, a173 offset:21312\n\t" "ds_write_b32 v21, a174 offset:10752\n\t" \
   "ds_write_b32 v21, a175 offset:31872\n\t" "s_waitcnt vmcnt(25)\n\t" "ds_write_b32 v21, a200 offset:256\n\t" \
   "ds_write_b32 v21, a201 offset:21376\n\t" "ds_write_b32 v21, a202 offset:10816\n\t" "ds_write_b32 v21, a203 offset:31936\n\t" \
   "s_waitcnt vmcnt(24)\n\t" "ds_write_b32 v21, a204 offset:5248\n\t" "ds_write_b32 v21, a205 offset:26368\n\t" \
   "ds_write_b32 v21, a206 offset:15808\n\t" "ds_write_b32 v21, a207 offset:36928\n\t" "s_waitcnt vmcnt(23)\n\t" \
   "ds_write_b32 v21, a232 offset:5312\n\t" "ds_write_b32 v21, a233 offset:26432\n\t" "ds_write_b32 v21, a234 offset:15872\n\t" \
   "ds_write_b32 v21, a235 offset:36992\n\t" "s_waitcnt vmcnt(22)\n\t" "ds_write_b32 v21, a236 offset:5376\n\t" \
   "ds_write_b32 v21, a237 offset:26496\n\t" "ds_write_b32 v21, a238 offset:15936\n\t" "ds_write_b32 v21, a239 offset:37056\n\t" \
   "s_waitcnt vmcnt(21)\n\t" "ds_write_b32 v21, v232 offset:5440\n\t" "ds_write_b32 v21, v233 offset:26560\n\t" \
   "ds_write_b32 v21, v234 offset:16000\n\t" "ds_write_b32 v21, v235 offset:37120\n\t" "s_waitcnt vmcnt(20)\n\t" \
   "ds_write_b32 v21, v236 offset:5504\n\t" "ds_write_b32 v21, v237 offset:26624\n\t" "ds_write_b32 v21, v238 offset:16064\n\t" \
   "ds_write_b32 v21, v239 offset:37184\n\t" "s_waitcnt lgkmcnt(0)\n\t" "s_barrier\n\t" "ds_read_b64_tr_b16 a[136:137], v22 offset:0\n\t" \
   "ds_read_b64_tr_b16 a[138:139], v22 offset:656\n\t" "ds_read_b64_tr_b16 a[140:141], v22 offset:2624\n\t" \
   "ds_read_b64_tr_b16 a[142:143], v22 offset:3280\n\t" "ds_read_b64_tr_b16 a[168:169], v22 offset:16\n\t" \
   "ds_read_b64_tr_b16 a[170:171], v22 offset:672\n\t" "ds_read_b64_tr_b16 a[172:173], v22 offset:2640\n\t" \
   "ds_read_b64_tr_b16 a[174:175], v22 offset:3296\n\t" "ds_read_b64_tr_b16 a[200:201], v22 offset:32\n\t" \
   "ds_read_b64_tr_b16 a[202:203], v22 offset:688\n\t" "ds_read_b64_tr_b16 a[204:205], v22 offset:2656\n\t" \
   "ds_read_b64_tr_b16 a[206:207], v22 offset:3312\n\t" "ds_read_b64_tr_b16 a[232:233], v22 offset:48\n\t" \
   "ds_read_b64_tr_b16 a[234:235], v22 offset:704\n\t" "ds_read_b64_tr_b16 a[236:237], v22 offset:2672\n\t" \
   "ds_read_b64_tr_b16 a[238:239], v22 offset:3328\n\t" "ds_read_b64_tr_b16 v[232:233], v22 offset:64\n\t" \
   "ds_read_b64_tr_b16 v[234:235], v22 offset:720\n\t" "ds_read_b64_tr_b16 v[236:237], v22 offset:2688\n\t" \
   "ds_read_b64_tr_b16 v[238:239], v22 offset:3344\n\t" "s_waitcnt vmcnt(19)\n\t" "s_waitcnt lgkmcnt(0)\n\t" "s_barrier\n\t" \
   "ds_write_b32 v21, a144 offset:0\n\t" "ds_write_b32 v21, a145 offset:21120\n\t" "ds_write_b32 v21, a146 offset:10560\n\t" \
   "ds_write_b32 v21, a147 offset:31680\n\t" "s_waitcnt vmcnt(18)\n\t" "ds_write_b32 v21, a148 offset:64\n\t" "ds_write_b32 v21, a149 offset:21184\n\t" \
   "ds_write_b32 v21, a150 offset:10624\n\t" "ds_write_b32 v21, a151 offset:31744\n\t" "s_waitcnt vmcnt(17)\n\t" \
   "ds_write_b32 v21, a176 offset:128\n\t" "ds_write_b32 v21, a177 offset:21248\n\t" "ds_write_b32 v21, a178 offset:10688\n\t" \
   "ds_write_b32 v21, a179 offset:31808\n\t" "s_waitcnt vmcnt(16)\n\t" "ds_write_b32 v21, a180 offset:192\n\t" \
   "ds_write_b32 v21, a181 offset:21312\n\t" "ds_write_b32 v21, a182 offset:10752\n\t" "ds_write_b32 v21, a183 offset:31872\n\t" \
   "s_waitcnt vmcnt(15)\n\t" "ds_write_b32 v21, a208 offset:256\n\t" "ds_write_b32 v21, a209 offset:21376\n\t" \
   "ds_write_b32 v21, a210 offset:10816\n\t" "ds_write_b32 v21, a211 offset:31936\n\t" "s_waitcnt vmcnt(14)\n\t" \
   "ds_write_b32 v21, a212 offset:5248\n\t" "ds_write_b32 v21, a213 offset:26368\n\t" "ds_write_b32 v21, a214 offset:15808\n\t" \
   "ds_write_b32 v21, a215 offset:36928\n\t" "s_waitcnt vmcnt(13)\n\t" "ds_write_b32 v21, a240 offset:5312\n\t" \
   "ds_write_b32 v21, a241 offset:26432\n\t" "ds_write_b32 v21, a242 offset:15872\n\t" "ds_write_b32 v21, a243 offset:36992\n\t" \
   "s_waitcnt vmcnt(12)\n\t" "ds_write_b32 v21, a244 offset:5376\n\t" "ds_write_b32 v21, a245 offset:26496\n\t" \
   "ds_write_b32 v21, a246 offset:15936\n\t" "ds_write_b32 v21, a247 offset:37056\n\t" "s_waitcnt vmcnt(11)\n\t" \
   "ds_write_b32 v21, v240 offset:5440\n\t" "ds_write_b32 v21, v241 offset:26560\n\t" "ds_write_b32 v21, v242 offset:16000\n\t" \
   "ds_write_b32 v21, v243 offset:37120\n\t" "s_waitcnt vmcnt(10)\n\t" "ds_write_b32 v21, v244 offset:5504\n\t" \
   "ds_write_b32 v21, v245 offset:26624\n\t" "ds_write_b32 v21, v246 offset:16064\n\t" "ds_write_b32 v21, v247 offset:37184\n\t" \
   "s_waitcnt lgkmcnt(0)\n\t" "s_barrier\n\t" "ds_read_b64_tr_b16 a[144:145], v22 offset:0\n\t" "ds_read_b64_tr_b16 a[146:147], v22 offset:656\n\t" \
   "ds_read_b64_tr_b16 a[148:149], v22 offset:2624\n\t" "ds_read_b64_tr_b16 a[150:151], v22 offset:3280\n\t" \
   "ds_read_b64_tr_b16 a[176:177], v22 offset:16\n\t" "ds_read_b64_tr_b16 a[178:179], v22 offset:672\n\t" \
   "ds_read_b64_tr_b16 a[180:181], v22 offset:2640\n\t" "ds_read_b64_tr_b16 a[182:183], v22 offset:3296\n\t" \
   "ds_read_b64_tr_b16 a[208:209], v22 offset:32\n\t" "ds_read_b64_tr_b16 a[210:211], v22 offset:688\n\t" \
   "ds_read_b64_tr_b16 a[212:213], v22 offset:2656\n\t" "ds_read_b64_tr_b16 a[214:215], v22 offset:3312\n\t" \
   "ds_read_b64_tr_b16 a[240:241], v22 offset:48\n\t" "ds_read_b64_tr_b16 a[242:243], v22 offset:704\n\t" \
   "ds_read_b64_tr_b16 a[244:245], v22 offset:2672\n\t" "ds_read_b64_tr_b16 a[246:247], v22 offset:3328\n\t" \
   "ds_read_b64_tr_b16 v[240:241], v22 offset:64\n\t" "ds_read_b64_tr_b16 v[242:243], v22 offset:720\n\t" \
   "ds_read_b64_tr_b16 v[244:245], v22 offset:2688\n\t" "ds_read_b64_tr_b16 v[246:247], v22 offset:3344\n\t" "s_waitcnt vmcnt(9)\n\t" \
   "s_waitcnt lgkmcnt(0)\n\t" "s_barrier\n\t" "ds_write_b32 v21, a152 offset:0\n\t" "ds_write_b32 v21, a153 offset:21120\n\t" \
   "ds_write_b32 v21, a154 offset:10560\n\t" "ds_write_b32 v21, a155 offset:31680\n\t" "s_waitcnt vmcnt(8)\n\t" "ds_write_b32 v21, a156 offset:64\n\t" \
   "ds_write_b32 v21, a157 offset:21184\n\t" "ds_write_b32 v21, a158 offset:10624\n\t" "ds_write_b32 v21, a159 offset:31744\n\t" \
   "s_waitcnt vmcnt(7)\n\t" "ds_write_b32 v21, a184 offset:128\n\t" "ds_write_b32 v21, a185 offset:21248\n\t" "ds_write_b32 v21, a186 offset:10688\n\t" \
   "ds_write_b32 v21, a187 offset:31808\n\t" "s_waitcnt vmcnt(6)\n\t" "ds_write_b32 v21, a188 offset:192\n\t" "ds_write_b32 v21, a189 offset:21312\n\t" \
   "ds_write_b32 v21, a190 offset:10752\n\t" "ds_write_b32 v21, a191 offset:31872\n\t" "s_waitcnt vmcnt(5)\n\t" "ds_write_b32 v21, a216 offset:256\n\t" \
   "ds_write_b32 v21, a217 offset:21376\n\t" "ds_write_b32 v21, a218 offset:10816\n\t" "ds_write_b32 v21, a219 offset:31936\n\t" \
   "s_waitcnt vmcnt(4)\n\t" "ds_write_b32 v21, a220 offset:5248\n\t" "ds_write_b32 v21, a221 offset:26368\n\t" \
   "ds_write_b32 v21, a222 offset:15808\n\t" "ds_write_b32 v21, a223 offset:36928\n\t" "s_waitcnt vmcnt(3)\n\t" \
   "ds_write_b32 v21, a248 offset:5312\n\t" "ds_write_b32 v21, a249 offset:26432\n\t" "ds_write_b32 v21, a250 offset:15872\n\t" \
   "ds_write_b32 v21, a251 offset:36992\n\t" "s_waitcnt vmcnt(2)\n\t" "ds_write_b32 v21, a252 offset:5376\n\t" \
   "ds_write_b32 v21, a253 offset:26496\n\t" "ds_write_b32 v21, a254 offset:15936\n\t" "ds_write_b32 v21, a255 offset:37056\n\t" \
   "s_waitcnt vmcnt(1)\n\t" "ds_write_b32 v21, v248 offset:5440\n\t" "ds_write_b32 v21, v249 offset:26560\n\t" \
   "ds_write_b32 v21, v250 offset:16000\n\t" "ds_write_b32 v21, v251 offset:37120\n\t" "s_waitcnt vmcnt(0)\n\t" \
   "ds_write_b32 v21, v252 offset:5504\n\t" "ds_write_b32 v21, v253 offset:26624\n\t" "ds_write_b32 v21, v254 offset:16064\n\t" \
   "ds_write_b32 v21, v255 offset:37184\n\t" "s_waitcnt lgkmcnt(0)\n\t" "s_barrier\n\t" "ds_read_b64_tr_b16 a[152:153], v22 offset:0\n\t" \
   "ds_read_b64_tr_b16 a[154:155], v22 offset:656\n\t" "ds_read_b64_tr_b16 a[156:157], v22 offset:2624\n\t" \
   "ds_read_b64_tr_b16 a[158:159], v22 offset:3280\n\t" "ds_read_b64_tr_b16 a[184:185], v22 offset:16\n\t" \
   "ds_read_b64_tr_b16 a[186:187], v22 offset:672\n\t" "ds_read_b64_tr_b16 a[188:189], v22 offset:2640\n\t" \
   "ds_read_b64_tr_b16 a[190:191], v22 offset:3296\n\t" "ds_read_b64_tr_b16 a[216:217], v22 offset:32\n\t" \
   "ds_read_b64_tr_b16 a[218:219], v22 offset:688\n\t" "ds_read_b64_tr_b16 a[220:221], v22 offset:2656\n\t" \
   "ds_read_b64_tr_b16 a[222:223], v22 offset:3312\n\t" "ds_read_b64_tr_b16 a[248:249], v22 offset:48\n\t" \
   "ds_read_b64_tr_b16 a[250:251], v22 offset:704\n\t" "ds_read_b64_tr_b16 a[252:253], v22 offset:2672\n\t" \
   "ds_read_b64_tr_b16 a[254:255], v22 offset:3328\n\t" "ds_read_b64_tr_b16 v[248:249], v22 offset:64\n\t" \
   "ds_read_b64_tr_b16 v[250:251], v22 offset:720\n\t" "ds_read_b64_tr_b16 v[252:253], v22 offset:2688\n\t" \
   "ds_read_b64_tr_b16 v[254:255], v22 offset:3344\n\t" "s_waitcnt lgkmcnt(0)"
#define LDY_CLOB "v24", "v25", "s40", "s41"
__device__ __forceinline__ void load_rows(v32u (&w)[10], int kind, const u16* A, const u16* B, int h, int wv, unsigned slot) {
  const int l = lane_id(), i = l & 15, g = l >> 4;
  const v4u ra = make_rs(A, 640u * 8192u), rb = make_rs(B, 640u * 8192u);
  v8u ad = (unsigned)l;
  const int wr = 4 * wv + g, rr = 4 * g + (i >> 2);
  ad[0] = wr * 8192 + h * 256 + i * 16;
  ad[1] = (h * 128 + wr) * 1280 + i * 16;
  ad[2] = slot + rr * 256 + ((2 * wv) ^ (rr & 7)) * 32 + (i & 3) * 8;
  ad[3] = ad[2] ^ 32u;
  ad[4] = slot + wr * 256 + ((i >> 1) ^ (wr & 7)) * 32 + (i & 1) * 16;
  ad[5] = slot + wr * 328 + ((i >> 2) * 2 + (i & 1)) * 8 + ((i >> 1) & 1) * 4;
  ad[6] = slot + (kind == 3 ? (16 * (g & 1) + 4 * (g >> 1) + ((i >> 2) & 1)) * 328 + (i >> 3) * 8 : rr * 328) + (i & 3) * 10560 + wv * 80;
  ad[7] = ad[4] + 65536u;
#define LD_OPS                                                                                                                            \
  : "={a[0:31]}"(w[0]), "={a[32:63]}"(w[1]), "={a[64:95]}"(w[2]), "={a[96:127]}"(w[3]), "={a[128:159]}"(w[4]), "={a[160:191]}"(w[5]),     \
    "={a[192:223]}"(w[6]), "={a[224:255]}"(w[7]), "={v[192:223]}"(w[8]), "={v[224:255]}"(w[9])                                            \
  : "{v[16:23]}"(ad), [ra] "s"(ra), [rb] "s"(rb), [p0] "s"(wv < 3 ? 3 * wv : 8)                                                          \
  : LDP_CLOB, "scc", "memory"
  if (kind == 0) asm volatile(LDP_ASM LD_OPS);
  else if (kind == 1) asm volatile(LDK_ASM LD_OPS);
  else if (kind == 2) asm volatile(LDV_ASM LD_OPS);
  else asm volatile(LDY_ASM LD_OPS);
}
#define W_OPS "{a[0:31]}"(w[0]), "{a[32:63]}"(w[1]), "{a[64:95]}"(w[2]), "{a[96:127]}"(w[3]), "{a[128:159]}"(w[4]), "{a[160:191]}"(w[5]), \
              "{a[192:223]}"(w[6]), "{a[224:255]}"(w[7]), "{v[192:223]}"(w[8]), "{v[224:255]}"(w[9])

__device__ __forceinline__ int mem_lane(int lane) { return lane * 16; }
__device__ __forceinline__ int pair_of(int g) { return (g & 1) * 2 + (g >> 1); }

__device__ __forceinline__ void rec_p1(int wv_, const v32u (&w)[10], unsigned char* ws, const Pool& pool, int layer, int h, int xs, RecTune tn, unsigned long long* stamps) {
  const Clk0 c0 = clk_sample();
  const unsigned lds0 = (unsigned)(uintptr_t)(__attribute__((address_space(3))) unsigned char*)dyn_lds;
  const int lane = lane_id(), wv = wv_, g = lane >> 4, r = lane & 15;
  const v4u rs = make_rs(ws, (unsigned)OFF_END);
  const int p0 = wv < 3 ? 3 * wv : 8;
  v16u ad, la = (unsigned)lane;
  const int kqo = (int)(wv < 2 ? OFF_KF : OFF_QF) + ((h * RT + (wv & 1)) * 4 * 64 + lane) * 16;
  const int progo = (wv == 0 && lane == 0) ? prog_off(layer, 0, h) : OOB;
  {
    const int p2 = wv < 3 ? p0 + 2 : 0;
    ad[0] = W_MEM + (p0 >> 1) * WIN + (p0 & 1) * 1024 + mem_lane(lane);
    ad[1] = W_MEM + ((p0 + 1) >> 1) * WIN + ((p0 + 1) & 1) * 1024 + mem_lane(lane);
    ad[2] = W_MEM + (p2 >> 1) * WIN + (p2 & 1) * 1024 + mem_lane(lane);
    ad[3] = lane == 0 ? W_MISC + h * 64 + wv * 4 : OOB;
    ad[4] = copy_off(wv) + (h >> 2) * WIN + (h & 3) * 512 + (g >> 1) * 256 + (2 * (r & 7) + (r >> 3)) * 16 + (g & 1) * 8;
    ad[5] = g < 2 ? copy_off(wv + 4 * (r >> 3)) + (8 + (h >> 4)) * WIN + ((h >> 3) & 1) * 1024 + (((h >> 1) & 3) * 16 + (h & 1) * 8 + (r & 7)) * 16 + g * 8 : OOB;
    ad[6] = (r & 1) ? 0xEEEEu : 0x4444u;
    ad[7] = lds0 + (32 * wv + r) * 16 + g * 4;
    ad[8] = lds0 + (4 * g + (r >> 2)) * 16 + (r & 1) * 8;
    ad[9] = lds0 + L_M2 + wv * 3072 + lane * 16;
    ad[10] = kqo;
    ad[11] = M_TM + xs * 128 + (lane & 1) * 12;
    ad[12] = kqf_off(layer, 0, h) + (lane & 3) * 4;
    ad[13] = wv > 1 ? (int)OFF_PXF + ((h * 2 + (wv & 1)) * 64 + lane) * 16 : OOB;
    ad[14] = (g < 2 && wv > 1) ? (int)OFF_PXM + (((h >> 2) * 2 + (wv & 1)) * 64 + (h & 3) * 16 + r) * 16 + g * 8 : OOB;
    ad[15] = progo;
#pragma unroll
    for (int i = 0; i < 10; ++i) { int p = p0 + i; p = p >= 10 ? p - 10 : p; la[i] = lds0 + p * 1024 + lane * 16; }
    la[10] = lds0 + L_KQ + lane * 16;
    la[12] = lds0 + REC_ST + wv * 8;
    asm volatile("v_mov_b32 %0, 0xceabfb8f" : "=v"(la[13]));
    la[14] = 16 * (wv & 1) + r - 4 * g;
    la[15] = lds0 + L_KQ + (wv & 1) * 4096 + lane * 16;
  }
  __builtin_amdgcn_s_waitcnt(0x0F70);
  if (layer) __builtin_amdgcn_s_sleep(64);
  v4u clk = clk_init(c0);
  unsigned long long stat;
  asm volatile(P1_ASM
               : "=&{s[76:77]}"(stat), "+{s[56:59]}"(clk)
               : W_OPS, "{v[128:143]}"(ad), "{v[112:127]}"(la), [rs] "s"(rs), [sel] "s"(pool.sel), [pool] "s"(pool.base), [xs] "s"(xs),
                 [lad0] "s"(ladder(xs, xs & 1)), [s3] "s"(pool.s3), [m1] "s"(tn.m1), [l2s] "s"(lds0 + L_M2 + wv * 3072),
                 [hi8] "s"(0xFF00FF00FF00FF00ull), [wv] "s"(wv), [kf0] "s"((int)OFF_KF + h * RT * 4096),
                 [esc] "s"((int)OFF_FWD - W_MEM)
               : P1_CLOB RST_CLOB, "vcc", "scc", "m0", "memory");
  LDSBAR();
  {
    const unsigned one = 1u;
    const int so = (NSEG - 1) * 4096;
    VM_ST32(one, progo, so);
    VM_WAIT(0);
  }
}

__device__ __forceinline__ void rec_pv(int wv_, const v32u (&w)[10], unsigned char* ws, const Pool& pool, int layer, int h, int xs, RecTune tn, unsigned long long* stamps) {
  const Clk0 c0 = clk_sample();
  const unsigned lds0 = (unsigned)(uintptr_t)(__attribute__((address_space(3))) unsigned char*)dyn_lds;
  const int lane = lane_id(), wv = wv_, g = lane >> 4, r = lane & 15;
  const v4u rs = make_rs(ws, (unsigned)OFF_END);
  const int p0 = wv < 3 ? 3 * wv : 8, p2 = wv < 3 ? p0 + 2 : 0;
  v16u ad = (unsigned)lane, la = (unsigned)lane;
  ad[0] = W_MEM + (p0 >> 1) * WIN + (p0 & 1) * 1024 + mem_lane(lane);
  ad[1] = W_MEM + ((p0 + 1) >> 1) * WIN + ((p0 + 1) & 1) * 1024 + mem_lane(lane);
  ad[2] = W_MEM + (p2 >> 1) * WIN + (p2 & 1) * 1024 + mem_lane(lane);
  ad[3] = (r & 8) ? 0xEEEEu : 0x4444u;
  ad[4] = (wv * 10 + (g >> 1)) * 16384 + h * 256 + r * 16 + (g & 1) * 8;
  ad[5] = h * 16384 + wv * 2048 + lane * 16;
  ad[6] = (r & 1) ? 0xEEEEu : 0x4444u;
  ad[7] = lds0 + (32 * wv + r) * 16 + pair_of(g) * 4;
  ad[8] = lds0 + (4 * g + (r >> 2)) * 16 + (r & 1) * 8;
  ad[11] = M_TM + (4 + xs) * 128 + (lane & 1) * 12;
  ad[15] = (wv == 0 && lane == 0) ? prog_off(layer, 0, 32 + h) : OOB;
#pragma unroll
  for (int i = 0; i < 10; ++i) { int p = p0 + i; p = p >= 10 ? p - 10 : p; la[i] = lds0 + p * 1024 + lane * 16; }
  la[12] = lds0 + REC_ST + wv * 8;
  __builtin_amdgcn_s_waitcnt(0x0F70);
  if (layer) __builtin_amdgcn_s_sleep(64);
  v4u clk = clk_init(c0);
  unsigned long long stat;
  asm volatile(PV_ASM
               : "=&{s[76:77]}"(stat), "+{s[56:59]}"(clk)
               : W_OPS, "{v[128:143]}"(ad), "{v[112:127]}"(la), [rs] "s"(rs), [sel] "s"(pool.sel), [pool] "s"(pool.base), [xs] "s"(xs),
                 [lad0] "s"(ladder(xs, xs & 1)), [s3] "s"(pool.s3), [m1c] "s"(tn.m1 + tn.c2), [esc] "s"((int)OFF_FWD - W_MEM),
                 [ysel] "s"(pool.ysel), [yb] "s"(pool.base + P_Y)
               : PV_CLOB RST_CLOB, "vcc", "scc", "memory");
}

#define TSTART 3900
#define TD1 900u
#define TR2 400u
#define TEMA 1
#define TEMB 2
#define TBUP 48
#define TBDN 6
#define TLATE 520
#define TSEC 50
__device__ __forceinline__ unsigned track_dur(unsigned e, int m, int rob, int s) {
  const int lo = (int)e - (s < 8 ? 64 : (rob & 255)), hi = (int)e + (rob >> 8);
  if (s >= 8) m = (int)e + ((m - (int)e + (m > (int)e ? (1 << TEMA) - 1 : 0)) >> (m > (int)e ? TEMA : TEMB));
  return (unsigned)min(max(m, lo), hi);
}
#define TTS 200
#define TTU 240
__device__ __forceinline__ void rec_sched(unsigned char* ws, const Pool& pool, int layer, int c16, int xs, RecTune tn, unsigned long long* stamps) {
  const Clk0 c0 = clk_sample();
  const unsigned lds0 = (unsigned)(uintptr_t)(__attribute__((address_space(3))) unsigned char*)dyn_lds;
  const int lane = lane_id();
  const v4u rs = make_rs(ws, (unsigned)OFF_END);
  unsigned long long stat = 0;
  const int sm = M_STAMP + min(lane, 39) * 64, sp = W_MISC + (lane & 31) * 64;
  const int yo = M_TM + xs * 128, oc = (c16 == 0 && lane < 16) ? M_TM + lane * 128 : OOB;
  const unsigned cv = flag_finish<2>(rs, kqf_off(layer, 0, 32 + (lane & 31)), 0xFFFFFFFFu);
  v4u clk = clk_init(c0);
  PH(3 + layer * 6)
  unsigned r2a = TR2, d1a = TD1, dd = TD1 + tn.m2, tp = 0, tm = 0, anchor = clk.x - dd;
  unsigned rr = (unsigned)min(max(wave_imax((int)(cv * 8u - clk.x)) + TSTART, 300), 4000);
  int bonus = 0;
  bool clean = false;
#pragma unroll 1
  for (int s = 0; s < NSEG - 1; ++s) {
    const int rgm = pool.region((s + NSEG - 1) & (NSEG - 1)), rg = pool.region(s);
    const unsigned slot = lds0 + L_T + (s & 1) * 32, ao = anchor;
    unsigned b2 = 1u;
    v4u b1 = {1u, 1u, 1u, 1u};
    if (s > 0) {
      asm volatile(WAIT_ASM : "+{s[56:59]}"(clk) : [ta] "s"(tm + TTS) : WAIT_CLOB, "scc", "memory");
      VM_LD32(b2, sm, rgm);
      VM_LD128(b1, sp, rgm, 0);
      asm volatile("s_waitcnt vmcnt(0)" : "+v"(b2), "+v"(b1)::"memory");
      anchor = tm;
      if (__builtin_expect(__any(b2 == 0xFFFFFFFFu || (int)(b2 - (tm + tn.m1 - tn.vis)) > 0), 0)) {
        STAT(3);
        for (unsigned spins = 0; __any(b2 == 0xFFFFFFFFu) && spins < SPIN_CAP; ++spins) {
          __builtin_amdgcn_s_sleep(2);
          VM_LD32(b2, sm, rgm);
          VM_WAIT1(0, b2);
        }
        const int d = wave_imax((int)(b2 - tm)) + tn.vis - tn.m1;
        anchor = tm + (d <= 0 ? 0 : (d <= tn.dlt ? tn.dlt + TSEC : d + TLATE));
      }
    }
    const unsigned tpo = tp, tmo = tm;
    tp = anchor + dd;
    tm = nosent(tp + rr);
    LDS_V4(slot) = v4u{tp, tm, (unsigned)(s + 1), anchor};
    if (anchor != tmo && s > 2) bonus = TBUP; else bonus = max(bonus - TBDN, 0);
    dd = d1a + tn.m2 + bonus;
    rr = r2a + bonus;
    {
      const v4u wq = v4u{tm, dd, rr, dd + rr};
      VM_ST128(wq, oc, rg);
    }
    if (s > 1 && !__any((b2 & 1u) != 0u)) r2a = track_dur(r2a, wave_imax((int)(b2 - tpo)), tn.rob, s);
    if (s > 1 && clean && !__any(((b1.x | b1.y | b1.z | b1.w) & 1u) != 0u))
      d1a = track_dur(d1a, wave_imax(max(max((int)(b1.x - ao), (int)(b1.y - ao)), max((int)(b1.z - ao), (int)(b1.w - ao)))), tn.rob, s);
    clean = anchor == tmo;
    while ((int)(tp + 40u - rtc() * 8u) > 0 && (int)(tp - rtc() * 8u) < 32000) __builtin_amdgcn_s_sleep(1);
    v4u lw;
    VM_LD128(lw, yo, rg, 0);
    VM_WAIT1(0, lw);
    const unsigned l0 = (unsigned)__builtin_amdgcn_readfirstlane(lw.x), l1 = (unsigned)__builtin_amdgcn_readfirstlane(lw.y),
                   l2 = (unsigned)__builtin_amdgcn_readfirstlane(lw.z);
    if (max(max(l0, l1), l2) != 0xFFFFFFFFu) { tm = l0; dd = l1; rr = l2; }
  }
}

__device__ __forceinline__ void rec_fin(unsigned char* ws, const Pool& pool, int layer, int c16, int xs, RecTune tn, unsigned long long* stamps) {
  const Clk0 c0 = clk_sample();
  const unsigned lds0 = (unsigned)(uintptr_t)(__attribute__((address_space(3))) unsigned char*)dyn_lds;
  const int lane = lane_id(), g = lane >> 4, r = lane & 15;
  const v4u rs = make_rs(ws, (unsigned)OFF_END);
  v8u ad;
  ad[0] = lane * 16;
  ad[1] = g * 256 + r * 8;
  ad[2] = (r & 1) ? 0x03020706u : 0x05040100u;
  ad[3] = (lane & 31) * 64;
  ad[4] = W_MEM + copy_off(((r >> 1) & 1) * 5) + (c16 >> 3) * WIN + (c16 & 7) * 256 + (2 * (2 * g + (r & 1)) + (r >> 3)) * 16 + ((r >> 2) & 1) * 8;
  ad[5] = lane == 0 ? M_STAMP + c16 * 64 : OOB;
  ad[6] = (r & 1) ? 0xEEEEu : 0x4444u;
  ad[7] = lds0 + L_T;
  __builtin_amdgcn_s_sleep(64);
  v4u clk = clk_init(c0);
  unsigned long long stat;
  asm volatile(FIN_ASM
               : "=&{s[76:77]}"(stat), "+{s[56:59]}"(clk), "+{v[16:23]}"(ad)
               : [rs] "s"(rs), [sel] "s"(pool.sel), [pool] "s"(pool.base), [xs] "s"(xs), [lad0] "s"(ladder(xs, xs & 1)), [s3] "s"(pool.s3),
                 [su] "s"((int)OFF_UF + c16 * 32768), [yb] "s"(pool.base + P_Y + c16 * 16384), [ysel] "s"(pool.ysel), [tu] "s"(TTU),
                 [stl] "s"(lds0 + REC_ST - 32), [uf] "s"(kqf_off(layer, 0, 32))
               : FIN_CLOB RST_CLOB, "vcc", "scc", "memory");
}

constexpr int PJ_BUF = 43 * 1024, PJ_X = 16384, PJ_LDS = PJ_X + 3 * PJ_BUF;
#define KQ_ASM \
   "s_mov_b32 s44, %[rdy]\n\t" "s_mov_b32 s45, 0\n\t" "s_mov_b32 s46, 0\n\t" "s_cmp_eq_u32 s44, -1\n\t" "s_cbranch_scc1 68f\n\t" \
   "s_mul_i32 s52, s45, 1536\n\t" "s_mul_i32 s51, s45, 40960\n\t" "s_add_u32 m0, s46, %[ldx]\n\t" "s_mov_b32 s53, s46\n\t" \
   "buffer_load_dwordx4 v16, %[rs], s51 offen offset:0 sc1 lds\n\t" "buffer_load_dwordx4 v16, %[rs], s51 offen offset:1024 sc1 lds\n\t" \
   "buffer_load_dwordx4 v16, %[rs], s51 offen offset:2048 sc1 lds\n\t" "buffer_load_dwordx4 v16, %[rs], s51 offen offset:3072 sc1 lds\n\t" \
   "s_add_u32 m0, m0, 4096\n\t" "s_add_u32 s51, s51, 4096\n\t" "buffer_load_dwordx4 v16, %[rs], s51 offen offset:0 sc1 lds\n\t" \
   "buffer_load_dwordx4 v16, %[rs], s51 offen offset:1024 sc1 lds\n\t" "buffer_load_dwordx4 v16, %[rs], s51 offen offset:2048 sc1 lds\n\t" \
   "buffer_load_dwordx4 v16, %[rs], s51 offen offset:3072 sc1 lds\n\t" "s_add_u32 m0, m0, 4096\n\t" "s_add_u32 s51, s51, 4096\n\t" \
   "buffer_load_dwordx4 v16, %[rs], s51 offen offset:0 sc1 lds\n\t" "buffer_load_dwordx4 v16, %[rs], s51 offen offset:1024 sc1 lds\n\t" \
   "s_add_u32 m0, s53, %[ld1]\n\t" "s_nop 0\n\t" "buffer_load_dwordx4 v17, %[rs], s52 offen sc1 lds\n\t" "s_mov_b32 s45, 1\n\t" \
   "s_mov_b32 s46, 44032\n\t" "s_mul_i32 s52, s45, 1536\n\t" "s_mul_i32 s51, s45, 40960\n\t" "s_add_u32 m0, s46, %[ldx]\n\t" "s_mov_b32 s53, s46\n\t" \
   "buffer_load_dwordx4 v16, %[rs], s51 offen offset:0 sc1 lds\n\t" "buffer_load_dwordx4 v16, %[rs], s51 offen offset:1024 sc1 lds\n\t" \
   "buffer_load_dwordx4 v16, %[rs], s51 offen offset:2048 sc1 lds\n\t" "buffer_load_dwordx4 v16, %[rs], s51 offen offset:3072 sc1 lds\n\t" \
   "s_add_u32 m0, m0, 4096\n\t" "s_add_u32 s51, s51, 4096\n\t" "buffer_load_dwordx4 v16, %[rs], s51 offen offset:0 sc1 lds\n\t" \
   "buffer_load_dwordx4 v16, %[rs], s51 offen offset:1024 sc1 lds\n\t" "buffer_load_dwordx4 v16, %[rs], s51 offen offset:2048 sc1 lds\n\t" \
   "buffer_load_dwordx4 v16, %[rs], s51 offen offset:3072 sc1 lds\n\t" "s_add_u32 m0, m0, 4096\n\t" "s_add_u32 s51, s51, 4096\n\t" \
   "buffer_load_dwordx4 v16, %[rs], s51 offen offset:0 sc1 lds\n\t" "buffer_load_dwordx4 v16, %[rs], s51 offen offset:1024 sc1 lds\n\t" \
   "s_add_u32 m0, s53, %[ld1]\n\t" "s_nop 0\n\t" "buffer_load_dwordx4 v17, %[rs], s52 offen sc1 lds\n\t" "68:\n\t" "v_mov_b32 v80, 0x3f803f80\n\t" \
   "v_mov_b32 v81, 0x3f803f80\n\t" "v_mov_b32 v82, 0x3f803f80\n\t" "v_mov_b32 v83, 0x3f803f80\n\t" "v_mov_b32 v31, 1\n\t" "s_nop 1\n\t" \
   "v_mfma_f32_16x16x32_bf16 v[32:35], a[0:3], v[80:83], 0\n\t" "v_mfma_f32_16x16x32_bf16 v[36:39], a[4:7], v[80:83], 0\n\t" \
   "v_mfma_f32_16x16x32_bf16 v[40:43], a[8:11], v[80:83], 0\n\t" "v_mfma_f32_16x16x32_bf16 v[44:47], a[12:15], v[80:83], 0\n\t" \
   "v_mfma_f32_16x16x32_bf16 v[32:35], a[16:19], v[80:83], v[32:35]\n\t" "v_mfma_f32_16x16x32_bf16 v[36:39], a[20:23], v[80:83], v[36:39]\n\t" \
   "v_mfma_f32_16x16x32_bf16 v[40:43], a[24:27], v[80:83], v[40:43]\n\t" "v_mfma_f32_16x16x32_bf16 v[44:47], a[28:31], v[80:83], v[44:47]\n\t" \
   "v_mfma_f32_16x16x32_bf16 v[32:35], a[32:35], v[80:83], v[32:35]\n\t" "v_mfma_f32_16x16x32_bf16 v[36:39], a[36:39], v[80:83], v[36:39]\n\t" \
   "v_mfma_f32_16x16x32_bf16 v[40:43], a[40:43], v[80:83], v[40:43]\n\t" "v_mfma_f32_16x16x32_bf16 v[44:47], a[44:47], v[80:83], v[44:47]\n\t" \
   "v_mfma_f32_16x16x32_bf16 v[32:35], a[48:51], v[80:83], v[32:35]\n\t" "v_mfma_f32_16x16x32_bf16 v[36:39], a[52:55], v[80:83], v[36:39]\n\t" \
   "v_mfma_f32_16x16x32_bf16 v[40:43], a[56:59], v[80:83], v[40:43]\n\t" "v_mfma_f32_16x16x32_bf16 v[44:47], a[60:63], v[80:83], v[44:47]\n\t" \
   "v_mfma_f32_16x16x32_bf16 v[32:35], a[64:67], v[80:83], v[32:35]\n\t" "v_mfma_f32_16x16x32_bf16 v[36:39], a[68:71], v[80:83], v[36:39]\n\t" \
   "v_mfma_f32_16x16x32_bf16 v[40:43], a[72:75], v[80:83], v[40:43]\n\t" "v_mfma_f32_16x16x32_bf16 v[44:47], a[76:79], v[80:83], v[44:47]\n\t" \
   "v_mfma_f32_16x16x32_bf16 v[32:35], a[80:83], v[80:83], v[32:35]\n\t" "v_mfma_f32_16x16x32_bf16 v[36:39], a[84:87], v[80:83], v[36:39]\n\t" \
   "v_mfma_f32_16x16x32_bf16 v[40:43], a[88:91], v[80:83], v[40:43]\n\t" "v_mfma_f32_16x16x32_bf16 v[44:47], a[92:95], v[80:83], v[44:47]\n\t" \
   "v_mfma_f32_16x16x32_bf16 v[32:35], a[96:99], v[80:83], v[32:35]\n\t" "v_mfma_f32_16x16x32_bf16 v[36:39], a[100:103], v[80:83], v[36:39]\n\t" \
   "v_mfma_f32_16x16x32_bf16 v[40:43], a[104:107], v[80:83], v[40:43]\n\t" "v_mfma_f32_16x16x32_bf16 v[44:47], a[108:111], v[80:83], v[44:47]\n\t" \
   "v_mfma_f32_16x16x32_bf16 v[32:35], a[112:115], v[80:83], v[32:35]\n\t" "v_mfma_f32_16x16x32_bf16 v[36:39], a[116:119], v[80:83], v[36:39]\n\t" \
   "v_mfma_f32_16x16x32_bf16 v[40:43], a[120:123], v[80:83], v[40:43]\n\t" "v_mfma_f32_16x16x32_bf16 v[44:47], a[124:127], v[80:83], v[44:47]\n\t" \
   "v_mfma_f32_16x16x32_bf16 v[32:35], a[128:131], v[80:83], v[32:35]\n\t" "v_mfma_f32_16x16x32_bf16 v[36:39], a[132:135], v[80:83], v[36:39]\n\t" \
   "v_mfma_f32_16x16x32_bf16 v[40:43], a[136:139], v[80:83], v[40:43]\n\t" "v_mfma_f32_16x16x32_bf16 v[44:47], a[140:143], v[80:83], v[44:47]\n\t" \
   "v_mfma_f32_16x16x32_bf16 v[32:35], a[144:147], v[80:83], v[32:35]\n\t" "v_mfma_f32_16x16x32_bf16 v[36:39], a[148:151], v[80:83], v[36:39]\n\t" \
   "v_mfma_f32_16x16x32_bf16 v[40:43], a[152:155], v[80:83], v[40:43]\n\t" "v_mfma_f32_16x16x32_bf16 v[44:47], a[156:159], v[80:83], v[44:47]\n\t" \
   "v_mfma_f32_16x16x32_bf16 v[32:35], a[160:163], v[80:83], v[32:35]\n\t" "v_mfma_f32_16x16x32_bf16 v[36:39], a[164:167], v[80:83], v[36:39]\n\t" \
   "v_mfma_f32_16x16x32_bf16 v[40:43], a[168:171], v[80:83], v[40:43]\n\t" "v_mfma_f32_16x16x32_bf16 v[44:47], a[172:175], v[80:83], v[44:47]\n\t" \
   "v_mfma_f32_16x16x32_bf16 v[32:35], a[176:179], v[80:83], v[32:35]\n\t" "v_mfma_f32_16x16x32_bf16 v[36:39], a[180:183], v[80:83], v[36:39]\n\t" \
   "v_mfma_f32_16x16x32_bf16 v[40:43], a[184:187], v[80:83], v[40:43]\n\t" "v_mfma_f32_16x16x32_bf16 v[44:47], a[188:191], v[80:83], v[44:47]\n\t" \
   "v_mfma_f32_16x16x32_bf16 v[32:35], a[192:195], v[80:83], v[32:35]\n\t" "v_mfma_f32_16x16x32_bf16 v[36:39], a[196:199], v[80:83], v[36:39]\n\t" \
   "v_mfma_f32_16x16x32_bf16 v[40:43], a[200:203], v[80:83], v[40:43]\n\t" "v_mfma_f32_16x16x32_bf16 v[44:47], a[204:207], v[80:83], v[44:47]\n\t" \
   "v_mfma_f32_16x16x32_bf16 v[32:35], a[208:211], v[80:83], v[32:35]\n\t" "v_mfma_f32_16x16x32_bf16 v[36:39], a[212:215], v[80:83], v[36:39]\n\t" \
   "v_mfma_f32_16x16x32_bf16 v[40:43], a[216:219], v[80:83], v[40:43]\n\t" "v_mfma_f32_16x16x32_bf16 v[44:47], a[220:223], v[80:83], v[44:47]\n\t" \
   "v_mfma_f32_16x16x32_bf16 v[32:35], a[224:227], v[80:83], v[32:35]\n\t" "v_mfma_f32_16x16x32_bf16 v[36:39], a[228:231], v[80:83], v[36:39]\n\t" \
   "v_mfma_f32_16x16x32_bf16 v[40:43], a[232:235], v[80:83], v[40:43]\n\t" "v_mfma_f32_16x16x32_bf16 v[44:47], a[236:239], v[80:83], v[44:47]\n\t" \
   "v_mfma_f32_16x16x32_bf16 v[32:35], a[240:243], v[80:83], v[32:35]\n\t" "v_mfma_f32_16x16x32_bf16 v[36:39], a[244:247], v[80:83], v[36:39]\n\t" \
   "v_mfma_f32_16x16x32_bf16 v[40:43], a[248:251], v[80:83], v[40:43]\n\t" "v_mfma_f32_16x16x32_bf16 v[44:47], a[252:255], v[80:83], v[44:47]\n\t" \
   "v_mfma_f32_16x16x32_bf16 v[32:35], v[192:195], v[80:83], v[32:35]\n\t" "v_mfma_f32_16x16x32_bf16 v[36:39], v[196:199], v[80:83], v[36:39]\n\t" \
   "v_mfma_f32_16x16x32_bf16 v[40:43], v[200:203], v[80:83], v[40:43]\n\t" "v_mfma_f32_16x16x32_bf16 v[44:47], v[204:207], v[80:83], v[44:47]\n\t" \
   "v_mfma_f32_16x16x32_bf16 v[32:35], v[208:211], v[80:83], v[32:35]\n\t" "v_mfma_f32_16x16x32_bf16 v[36:39], v[212:215], v[80:83], v[36:39]\n\t" \
   "v_mfma_f32_16x16x32_bf16 v[40:43], v[216:219], v[80:83], v[40:43]\n\t" "v_mfma_f32_16x16x32_bf16 v[44:47], v[220:223], v[80:83], v[44:47]\n\t" \
   "v_mfma_f32_16x16x32_bf16 v[32:35], v[224:227], v[80:83], v[32:35]\n\t" "v_mfma_f32_16x16x32_bf16 v[36:39], v[228:231], v[80:83], v[36:39]\n\t" \
   "v_mfma_f32_16x16x32_bf16 v[40:43], v[232:235], v[80:83], v[40:43]\n\t" "v_mfma_f32_16x16x32_bf16 v[44:47], v[236:239], v[80:83], v[44:47]\n\t" \
   "v_mfma_f32_16x16x32_bf16 v[32:35], v[240:243], v[80:83], v[32:35]\n\t" "v_mfma_f32_16x16x32_bf16 v[36:39], v[244:247], v[80:83], v[36:39]\n\t" \
   "v_mfma_f32_16x16x32_bf16 v[40:43], v[248:251], v[80:83], v[40:43]\n\t" "v_mfma_f32_16x16x32_bf16 v[44:47], v[252:255], v[80:83], v[44:47]\n\t" \
   "s_mov_b32 s40, 0\n\t" "s_mov_b32 s41, 0\n\t" "s_mov_b32 s42, 0\n\t" "s_mov_b32 s43, 44032\n\t" "v_mov_b32 v28, v22\n\t" "v_mov_b32 v29, v23\n\t" \
   "s_mov_b32 s45, 2\n\t" "s_mov_b32 s46, 88064\n\t" "s_waitcnt vmcnt(11)\n\t" "s_barrier\n\t" "s_lshr_b32 s48, s45, 1\n\t" \
   "s_bitcmp1_b32 s44, s48\n\t" "s_cbranch_scc0 172f\n\t" "72:\n\t" "s_mul_i32 s52, s45, 1536\n\t" "s_mul_i32 s51, s45, 40960\n\t" \
   "s_add_u32 m0, s46, %[ldx]\n\t" "s_mov_b32 s53, s46\n\t" "buffer_load_dwordx4 v16, %[rs], s51 offen offset:0 sc1 lds\n\t" \
   "ds_read_b128 v[80:83], v28 offset:0\n\t" "ds_read_b128 v[84:87], v28 offset:1024\n\t" "ds_read_b128 v[88:91], v28 offset:2048\n\t" \
   "ds_read_b128 v[92:95], v28 offset:3072\n\t" "1:\n\t" "v_mov_b32 v26, v28\n\t" "v_mov_b32 v27, v29\n\t" "s_waitcnt lgkmcnt(2)\n\t" \
   "v_mfma_f32_16x16x32_bf16 v[48:51], a[0:3], v[80:83], 0\n\t" "ds_read_b128 v[112:115], v27 offset:0\n\t" \
   "v_mfma_f32_16x16x32_bf16 v[52:55], a[4:7], v[80:83], 0\n\t" "ds_read_b128 v[96:99], v26 offset:4096\n\t" \
   "v_mfma_f32_16x16x32_bf16 v[56:59], a[8:11], v[80:83], 0\n\t" "ds_read_b128 v[116:119], v27 offset:16\n\t" \
   "v_mfma_f32_16x16x32_bf16 v[60:63], a[12:15], v[80:83], 0\n\t" "v_mul_f32 v133, 0x3e0293ee, v132\n\t" "v_fmac_f32 v64, v32, v12\n\t" \
   "v_mfma_f32_16x16x32_bf16 v[48:51], a[16:19], v[84:87], v[48:51]\n\t" "v_fmac_f32 v65, v33, v12\n\t" "v_fmac_f32 v66, v34, v12\n\t" \
   "v_mfma_f32_16x16x32_bf16 v[52:55], a[20:23], v[84:87], v[52:55]\n\t" "ds_read_b128 v[100:103], v26 offset:5120\n\t" \
   "v_mfma_f32_16x16x32_bf16 v[56:59], a[24:27], v[84:87], v[56:59]\n\t" "v_fmac_f32 v67, v35, v12\n\t" "v_fmac_f32 v68, v36, v12\n\t" \
   "v_mfma_f32_16x16x32_bf16 v[60:63], a[28:31], v[84:87], v[60:63]\n\t" "ds_read_b64 v[120:121], v27 offset:32\n\t" "s_waitcnt lgkmcnt(5)\n\t" \
   "v_mfma_f32_16x16x32_bf16 v[48:51], a[32:35], v[88:91], v[48:51]\n\t" "ds_read_b128 v[122:125], v27 offset:768\n\t" \
   "v_mfma_f32_16x16x32_bf16 v[52:55], a[36:39], v[88:91], v[52:55]\n\t" "ds_read_b128 v[104:107], v26 offset:6144\n\t" \
   "ds_read_b128 v[126:129], v27 offset:784\n\t" "v_mfma_f32_16x16x32_bf16 v[56:59], a[40:43], v[88:91], v[56:59]\n\t" \
   "ds_read_b64 v[130:131], v27 offset:800\n\t" "v_mfma_f32_16x16x32_bf16 v[60:63], a[44:47], v[88:91], v[60:63]\n\t" "v_fmac_f32 v69, v37, v12\n\t" \
   "v_fmac_f32 v70, v38, v12\n\t" "v_mfma_f32_16x16x32_bf16 v[48:51], a[48:51], v[92:95], v[48:51]\n\t" "v_fmac_f32 v71, v39, v12\n\t" \
   "v_fmac_f32 v72, v40, v12\n\t" "v_mfma_f32_16x16x32_bf16 v[52:55], a[52:55], v[92:95], v[52:55]\n\t" "ds_read_b128 v[108:111], v26 offset:7168\n\t" \
   "v_mfma_f32_16x16x32_bf16 v[56:59], a[56:59], v[92:95], v[56:59]\n\t" "v_fmac_f32 v73, v41, v12\n\t" "v_fmac_f32 v74, v42, v12\n\t" \
   "v_mfma_f32_16x16x32_bf16 v[60:63], a[60:63], v[92:95], v[60:63]\n\t" "v_fmac_f32 v75, v43, v12\n\t" "s_waitcnt lgkmcnt(6)\n\t" \
   "v_mfma_f32_16x16x32_bf16 v[48:51], a[64:67], v[96:99], v[48:51]\n\t" "v_fmac_f32 v76, v44, v12\n\t" "v_fmac_f32 v77, v45, v12\n\t" \
   "v_mfma_f32_16x16x32_bf16 v[52:55], a[68:71], v[96:99], v[52:55]\n\t" "ds_read_b128 v[80:83], v26 offset:8192\n\t" \
   "v_mfma_f32_16x16x32_bf16 v[56:59], a[72:75], v[96:99], v[56:59]\n\t" "v_fmac_f32 v78, v46, v12\n\t" "v_fmac_f32 v79, v47, v12\n\t" \
   "v_mfma_f32_16x16x32_bf16 v[60:63], a[76:79], v[96:99], v[60:63]\n\t" "v_mul_f32 v64, v64, v133\n\t" "v_mul_f32 v65, v65, v133\n\t" \
   "v_mfma_f32_16x16x32_bf16 v[48:51], a[80:83], v[100:103], v[48:51]\n\t" "v_mul_f32 v66, v66, v133\n\t" "v_mul_f32 v67, v67, v133\n\t" \
   "v_mfma_f32_16x16x32_bf16 v[52:55], a[84:87], v[100:103], v[52:55]\n\t" "ds_read_b128 v[84:87], v26 offset:9216\n\t" \
   "v_mfma_f32_16x16x32_bf16 v[56:59], a[88:91], v[100:103], v[56:59]\n\t" "v_mul_f32 v68, v68, v133\n\t" "v_mul_f32 v69, v69, v133\n\t" \
   "v_mfma_f32_16x16x32_bf16 v[60:63], a[92:95], v[100:103], v[60:63]\n\t" "v_mul_f32 v70, v70, v133\n\t" "s_waitcnt lgkmcnt(2)\n\t" \
   "v_mfma_f32_16x16x32_bf16 v[48:51], a[96:99], v[104:107], v[48:51]\n\t" "v_mul_f32 v71, v71, v133\n\t" "v_mul_f32 v72, v72, v132\n\t" \
   "v_mfma_f32_16x16x32_bf16 v[52:55], a[100:103], v[104:107], v[52:55]\n\t" "ds_read_b128 v[88:91], v26 offset:10240\n\t" \
   "v_mfma_f32_16x16x32_bf16 v[56:59], a[104:107], v[104:107], v[56:59]\n\t" "v_mul_f32 v73, v73, v132\n\t" "v_mul_f32 v74, v74, v132\n\t" \
   "v_mfma_f32_16x16x32_bf16 v[60:63], a[108:111], v[104:107], v[60:63]\n\t" "v_mul_f32 v75, v75, v132\n\t" "v_mul_f32 v76, v76, v132\n\t" \
   "v_mfma_f32_16x16x32_bf16 v[48:51], a[112:115], v[108:111], v[48:51]\n\t" "v_mul_f32 v77, v77, v132\n\t" "v_mul_f32 v78, v78, v132\n\t" \
   "v_mfma_f32_16x16x32_bf16 v[52:55], a[116:119], v[108:111], v[52:55]\n\t" "ds_read_b128 v[92:95], v26 offset:11264\n\t" \
   "v_mfma_f32_16x16x32_bf16 v[56:59], a[120:123], v[108:111], v[56:59]\n\t" "v_mul_f32 v79, v79, v132\n\t" "v_cvt_pk_bf16_f32 v136, v64, v65\n\t" \
   "v_mfma_f32_16x16x32_bf16 v[60:63], a[124:127], v[108:111], v[60:63]\n\t" "v_cvt_pk_bf16_f32 v137, v66, v67\n\t" "s_waitcnt lgkmcnt(2)\n\t" \
   "v_mfma_f32_16x16x32_bf16 v[48:51], a[128:131], v[80:83], v[48:51]\n\t" "v_cvt_pk_bf16_f32 v138, v68, v69\n\t" \
   "v_cvt_pk_bf16_f32 v139, v70, v71\n\t" "v_mfma_f32_16x16x32_bf16 v[52:55], a[132:135], v[80:83], v[52:55]\n\t" \
   "ds_read_b128 v[96:99], v26 offset:12288\n\t" "v_mfma_f32_16x16x32_bf16 v[56:59], a[136:139], v[80:83], v[56:59]\n\t" \
   "v_cvt_pk_bf16_f32 v140, v72, v73\n\t" "v_cvt_pk_bf16_f32 v141, v74, v75\n\t" \
   "v_mfma_f32_16x16x32_bf16 v[60:63], a[140:143], v[80:83], v[60:63]\n\t" "v_cvt_pk_bf16_f32 v142, v76, v77\n\t" \
   "v_cvt_pk_bf16_f32 v143, v78, v79\n\t" "v_mfma_f32_16x16x32_bf16 v[48:51], a[144:147], v[84:87], v[48:51]\n\t" "s_lshl_b32 s47, s41, 13\n\t" \
   "s_add_i32 s47, s47, -4096\n\t" "v_mfma_f32_16x16x32_bf16 v[52:55], a[148:151], v[84:87], v[52:55]\n\t" \
   "ds_read_b128 v[100:103], v26 offset:13312\n\t" "v_mfma_f32_16x16x32_bf16 v[56:59], a[152:155], v[84:87], v[56:59]\n\t" "s_add_u32 s54, s41, 1\n\t" \
   "s_max_i32 s47, s47, 0\n\t" "v_mfma_f32_16x16x32_bf16 v[60:63], a[156:159], v[84:87], v[60:63]\n\t" "s_lshr_b64 s[60:61], %[sel], s54\n\t" \
   "s_cmp_eq_u32 s40, 0\n\t" "s_cselect_b32 s48, 0x7ffffff0, 0\n\t" "s_waitcnt lgkmcnt(2)\n\t" \
   "v_mfma_f32_16x16x32_bf16 v[48:51], a[160:163], v[88:91], v[48:51]\n\t" "s_lshl_b32 s55, s54, 19\n\t" "s_and_b32 s60, s60, 1\n\t" \
   "v_or_b32 v30, s48, v21\n\t" "s_add_u32 s55, s55, %[pool]\n\t" "v_mfma_f32_16x16x32_bf16 v[52:55], a[164:167], v[88:91], v[52:55]\n\t" \
   "ds_read_b128 v[104:107], v26 offset:14336\n\t" "s_add_u32 s48, s47, %[qf]\n\t" "s_lshl_b32 s60, s60, 13\n\t" \
   "buffer_store_dwordx4 v[136:139], v30, %[rs], s48 offen sc1\n\t" "v_mfma_f32_16x16x32_bf16 v[56:59], a[168:171], v[88:91], v[56:59]\n\t" \
   "s_add_u32 s55, s55, s60\n\t" "s_add_u32 s48, s47, %[kf]\n\t" "s_bitcmp1_b64 %[skip], s41\n\t" "s_cselect_b32 s58, 0, %[size]\n\t" \
   "v_mfma_f32_16x16x32_bf16 v[60:63], a[172:175], v[88:91], v[60:63]\n\t" "buffer_store_dwordx4 v[140:143], v30, %[rs], s48 offen sc1\n\t" \
   "v_mfma_f32_16x16x32_bf16 v[48:51], a[176:179], v[92:95], v[48:51]\n\t" "v_add_f32 v112, v112, v114\n\t" \
   "v_mfma_f32_16x16x32_bf16 v[52:55], a[180:183], v[92:95], v[52:55]\n\t" "ds_read_b128 v[108:111], v26 offset:15360\n\t" \
   "v_mfma_f32_16x16x32_bf16 v[56:59], a[184:187], v[92:95], v[56:59]\n\t" "v_add_f32 v113, v113, v115\n\t" "v_add_f32 v116, v116, v118\n\t" \
   "v_mfma_f32_16x16x32_bf16 v[60:63], a[188:191], v[92:95], v[60:63]\n\t" \
   "buffer_store_dwordx4 v[156:159], v152, s[56:59], s55 offen offset:0 sc1\n\t" "s_waitcnt lgkmcnt(2)\n\t" \
   "v_mfma_f32_16x16x32_bf16 v[48:51], a[192:195], v[96:99], v[48:51]\n\t" "v_add_f32 v117, v117, v119\n\t" "v_add_f32 v112, v112, v120\n\t" \
   "v_mfma_f32_16x16x32_bf16 v[52:55], a[196:199], v[96:99], v[52:55]\n\t" "ds_read_b128 v[80:83], v26 offset:16384\n\t" \
   "v_mfma_f32_16x16x32_bf16 v[56:59], a[200:203], v[96:99], v[56:59]\n\t" "v_add_f32 v113, v113, v121\n\t" "v_add_f32 v112, v112, v116\n\t" \
   "v_mfma_f32_16x16x32_bf16 v[60:63], a[204:207], v[96:99], v[60:63]\n\t" "v_add_f32 v113, v113, v117\n\t" "v_mul_f32 v112, 0xbacccccd, v112\n\t" \
   "v_mfma_f32_16x16x32_bf16 v[48:51], a[208:211], v[100:103], v[48:51]\n\t" \
   "buffer_store_dwordx4 v[156:159], v152, s[56:59], s55 offen offset:1024 sc1\n\t" \
   "v_mfma_f32_16x16x32_bf16 v[52:55], a[212:215], v[100:103], v[52:55]\n\t" "ds_read_b128 v[84:87], v26 offset:17408\n\t" \
   "v_mfma_f32_16x16x32_bf16 v[56:59], a[216:219], v[100:103], v[56:59]\n\t" "v_mul_f32 v113, 0x3acccccd, v113\n\t" \
   "v_fma_f32 v113, -v112, v112, v113\n\t" "v_mfma_f32_16x16x32_bf16 v[60:63], a[220:223], v[100:103], v[60:63]\n\t" "v_max_f32 v113, 0, v113\n\t" \
   "s_waitcnt lgkmcnt(2)\n\t" "v_mfma_f32_16x16x32_bf16 v[48:51], a[224:227], v[104:107], v[48:51]\n\t" "v_cvt_pk_bf16_f32 v113, v113, v112\n\t" \
   "v_and_b32 v8, 0xffff0000, v113\n\t" "v_mfma_f32_16x16x32_bf16 v[52:55], a[228:231], v[104:107], v[52:55]\n\t" \
   "ds_read_b128 v[88:91], v26 offset:18432\n\t" "v_mfma_f32_16x16x32_bf16 v[56:59], a[232:235], v[104:107], v[56:59]\n\t" \
   "v_lshlrev_b32 v113, 16, v113\n\t" "v_add_f32 v113, 0x3727c5ac, v113\n\t" "v_mfma_f32_16x16x32_bf16 v[60:63], a[236:239], v[104:107], v[60:63]\n\t" \
   "v_cvt_pk_bf16_f32 v113, v113, v113\n\t" "v_lshlrev_b32 v113, 16, v113\n\t" \
   "v_mfma_f32_16x16x32_bf16 v[48:51], a[240:243], v[108:111], v[48:51]\n\t" "v_sqrt_f32 v113, v113\n\t" \
   "v_mfma_f32_16x16x32_bf16 v[52:55], a[244:247], v[108:111], v[52:55]\n\t" "ds_read_b128 v[92:95], v26 offset:19456\n\t" \
   "v_mfma_f32_16x16x32_bf16 v[56:59], a[248:251], v[108:111], v[56:59]\n\t" "buffer_load_dwordx4 v16, %[rs], s51 offen offset:1024 sc1 lds\n\t" \
   "v_mfma_f32_16x16x32_bf16 v[60:63], a[252:255], v[108:111], v[60:63]\n\t" "s_lshr_b64 s[60:61], %[ysel], s54\n\t" "s_waitcnt lgkmcnt(2)\n\t" \
   "v_mfma_f32_16x16x32_bf16 v[48:51], v[192:195], v[80:83], v[48:51]\n\t" "s_nop 0\n\t" "s_mul_i32 s61, s54, 43\n\t" \
   "v_mfma_f32_16x16x32_bf16 v[52:55], v[196:199], v[80:83], v[52:55]\n\t" "ds_read_b128 v[96:99], v26 offset:20480\n\t" \
   "v_mfma_f32_16x16x32_bf16 v[56:59], v[200:203], v[80:83], v[56:59]\n\t" "v_add_f32 v122, v122, v124\n\t" \
   "v_mfma_f32_16x16x32_bf16 v[60:63], v[204:207], v[80:83], v[60:63]\n\t" "v_cvt_pk_bf16_f32 v113, v113, v113\n\t" "v_add_f32 v123, v123, v125\n\t" \
   "v_mfma_f32_16x16x32_bf16 v[48:51], v[208:211], v[84:87], v[48:51]\n\t" "s_mul_i32 s55, s54, 80\n\t" "v_add_f32 v126, v126, v128\n\t" \
   "v_mfma_f32_16x16x32_bf16 v[52:55], v[212:215], v[84:87], v[52:55]\n\t" "ds_read_b128 v[100:103], v26 offset:21504\n\t" \
   "v_mfma_f32_16x16x32_bf16 v[56:59], v[216:219], v[84:87], v[56:59]\n\t" "buffer_load_dwordx4 v16, %[rs], s51 offen offset:2048 sc1 lds\n\t" \
   "v_mfma_f32_16x16x32_bf16 v[60:63], v[220:223], v[84:87], v[60:63]\n\t" "v_lshlrev_b32 v113, 16, v113\n\t" "s_waitcnt lgkmcnt(2)\n\t" \
   "v_mfma_f32_16x16x32_bf16 v[48:51], v[224:227], v[88:91], v[48:51]\n\t" "s_and_b32 s60, s60, 1\n\t" "v_add_f32 v127, v127, v129\n\t" \
   "v_mfma_f32_16x16x32_bf16 v[52:55], v[228:231], v[88:91], v[52:55]\n\t" "ds_read_b128 v[104:107], v26 offset:22528\n\t" \
   "v_mfma_f32_16x16x32_bf16 v[56:59], v[232:235], v[88:91], v[56:59]\n\t" "v_rcp_f32 v10, v113\n\t" \
   "v_mfma_f32_16x16x32_bf16 v[60:63], v[236:239], v[88:91], v[60:63]\n\t" "s_lshr_b32 s61, s61, 7\n\t" "v_add_f32 v122, v122, v130\n\t" \
   "v_mfma_f32_16x16x32_bf16 v[48:51], v[240:243], v[92:95], v[48:51]\n\t" "buffer_load_dwordx4 v16, %[rs], s51 offen offset:3072 sc1 lds\n\t" \
   "s_add_u32 m0, m0, 4096\n\t" "s_add_u32 s51, s51, 4096\n\t" "v_mfma_f32_16x16x32_bf16 v[52:55], v[244:247], v[92:95], v[52:55]\n\t" \
   "ds_read_b128 v[108:111], v26 offset:23552\n\t" "v_mfma_f32_16x16x32_bf16 v[56:59], v[248:251], v[92:95], v[56:59]\n\t" \
   "s_add_u32 s55, s55, s60\n\t" "v_add_f32 v123, v123, v131\n\t" "v_mfma_f32_16x16x32_bf16 v[60:63], v[252:255], v[92:95], v[60:63]\n\t" \
   "s_lshl4_add_u32 s55, s61, s55\n\t" "s_waitcnt lgkmcnt(2)\n\t" "v_mfma_f32_16x16x32_bf16 v[64:67], a[0:3], v[96:99], 0\n\t" \
   "v_add_f32 v122, v122, v126\n\t" "v_add_f32 v123, v123, v127\n\t" "v_mfma_f32_16x16x32_bf16 v[68:71], a[4:7], v[96:99], 0\n\t" \
   "ds_read_b128 v[80:83], v26 offset:24576\n\t" "v_mfma_f32_16x16x32_bf16 v[72:75], a[8:11], v[96:99], 0\n\t" "s_lshl_b32 s55, s55, 13\n\t" \
   "v_mul_f32 v122, 0xbacccccd, v122\n\t" "v_mfma_f32_16x16x32_bf16 v[76:79], a[12:15], v[96:99], 0\n\t" "v_mul_f32 v11, 0x3e0293ee, v10\n\t" \
   "v_fmac_f32 v48, v32, v8\n\t" "v_mfma_f32_16x16x32_bf16 v[64:67], a[16:19], v[100:103], v[64:67]\n\t" "v_fmac_f32 v49, v33, v8\n\t" \
   "v_fmac_f32 v50, v34, v8\n\t" "v_mfma_f32_16x16x32_bf16 v[68:71], a[20:23], v[100:103], v[68:71]\n\t" "ds_read_b128 v[84:87], v26 offset:25600\n\t" \
   "v_mfma_f32_16x16x32_bf16 v[72:75], a[24:27], v[100:103], v[72:75]\n\t" "s_cmp_lt_i32 s40, 1\n\t" "s_cselect_b32 s49, 0x7ffffff0, 0\n\t" \
   "v_mfma_f32_16x16x32_bf16 v[76:79], a[28:31], v[100:103], v[76:79]\n\t" "v_or_b32 v9, s49, v20\n\t" "s_waitcnt lgkmcnt(2)\n\t" \
   "v_mfma_f32_16x16x32_bf16 v[64:67], a[32:35], v[104:107], v[64:67]\n\t" "s_sub_i32 s49, s41, 1\n\t" "s_max_i32 s49, s49, 0\n\t" \
   "buffer_load_dwordx4 v16, %[rs], s51 offen offset:0 sc1 lds\n\t" "v_mfma_f32_16x16x32_bf16 v[68:71], a[36:39], v[104:107], v[68:71]\n\t" \
   "ds_read_b128 v[88:91], v26 offset:26624\n\t" "v_mfma_f32_16x16x32_bf16 v[72:75], a[40:43], v[104:107], v[72:75]\n\t" "s_lshl_b32 s49, s49, 12\n\t" \
   "s_waitcnt vmcnt(6)\n\t" "v_mfma_f32_16x16x32_bf16 v[76:79], a[44:47], v[104:107], v[76:79]\n\t" \
   "buffer_store_dword v31, v9, %[rs], s49 offen sc1\n\t" "v_mfma_f32_16x16x32_bf16 v[64:67], a[48:51], v[108:111], v[64:67]\n\t" \
   "s_add_u32 s55, s55, %[yb]\n\t" "v_fmac_f32 v51, v35, v8\n\t" "v_mfma_f32_16x16x32_bf16 v[68:71], a[52:55], v[108:111], v[68:71]\n\t" \
   "ds_read_b128 v[92:95], v26 offset:27648\n\t" "s_cmp_eq_u32 s41, 63\n\t" "s_cselect_b32 s58, 0, %[size]\n\t" \
   "v_mfma_f32_16x16x32_bf16 v[72:75], a[56:59], v[108:111], v[72:75]\n\t" "v_fmac_f32 v52, v36, v8\n\t" "v_fmac_f32 v53, v37, v8\n\t" \
   "v_mfma_f32_16x16x32_bf16 v[76:79], a[60:63], v[108:111], v[76:79]\n\t" "v_fmac_f32 v54, v38, v8\n\t" "s_waitcnt lgkmcnt(2)\n\t" \
   "v_mfma_f32_16x16x32_bf16 v[64:67], a[64:67], v[80:83], v[64:67]\n\t" "buffer_load_dwordx4 v16, %[rs], s51 offen offset:1024 sc1 lds\n\t" \
   "v_mfma_f32_16x16x32_bf16 v[68:71], a[68:71], v[80:83], v[68:71]\n\t" "ds_read_b128 v[96:99], v26 offset:28672\n\t" \
   "v_mfma_f32_16x16x32_bf16 v[72:75], a[72:75], v[80:83], v[72:75]\n\t" "buffer_store_dwordx4 v[156:159], v153, s[56:59], s55 offen sc1\n\t" \
   "v_mfma_f32_16x16x32_bf16 v[76:79], a[76:79], v[80:83], v[76:79]\n\t" "v_fmac_f32 v55, v39, v8\n\t" "v_fmac_f32 v56, v40, v8\n\t" \
   "v_mfma_f32_16x16x32_bf16 v[64:67], a[80:83], v[84:87], v[64:67]\n\t" "v_fmac_f32 v57, v41, v8\n\t" "v_fmac_f32 v58, v42, v8\n\t" \
   "v_mfma_f32_16x16x32_bf16 v[68:71], a[84:87], v[84:87], v[68:71]\n\t" "ds_read_b128 v[100:103], v26 offset:29696\n\t" \
   "v_mfma_f32_16x16x32_bf16 v[72:75], a[88:91], v[84:87], v[72:75]\n\t" "v_fmac_f32 v59, v43, v8\n\t" "v_fmac_f32 v60, v44, v8\n\t" \
   "v_mfma_f32_16x16x32_bf16 v[76:79], a[92:95], v[84:87], v[76:79]\n\t" "v_fmac_f32 v61, v45, v8\n\t" "s_waitcnt lgkmcnt(2)\n\t" \
   "v_mfma_f32_16x16x32_bf16 v[64:67], a[96:99], v[88:91], v[64:67]\n\t" "buffer_load_dwordx4 v16, %[rs], s51 offen offset:2048 sc1 lds\n\t" \
   "v_mfma_f32_16x16x32_bf16 v[68:71], a[100:103], v[88:91], v[68:71]\n\t" "ds_read_b128 v[104:107], v26 offset:30720\n\t" \
   "v_mfma_f32_16x16x32_bf16 v[72:75], a[104:107], v[88:91], v[72:75]\n\t" "buffer_store_dwordx4 v[156:159], v154, s[56:59], s55 offen sc1\n\t" \
   "v_mfma_f32_16x16x32_bf16 v[76:79], a[108:111], v[88:91], v[76:79]\n\t" "v_fmac_f32 v62, v46, v8\n\t" "v_fmac_f32 v63, v47, v8\n\t" \
   "v_mfma_f32_16x16x32_bf16 v[64:67], a[112:115], v[92:95], v[64:67]\n\t" "v_mul_f32 v48, v48, v11\n\t" "v_mul_f32 v49, v49, v11\n\t" \
   "v_mfma_f32_16x16x32_bf16 v[68:71], a[116:119], v[92:95], v[68:71]\n\t" "ds_read_b128 v[108:111], v26 offset:31744\n\t" \
   "v_mfma_f32_16x16x32_bf16 v[72:75], a[120:123], v[92:95], v[72:75]\n\t" "v_mul_f32 v50, v50, v11\n\t" "v_mul_f32 v51, v51, v11\n\t" \
   "v_mfma_f32_16x16x32_bf16 v[76:79], a[124:127], v[92:95], v[76:79]\n\t" "v_mul_f32 v52, v52, v11\n\t" "s_waitcnt lgkmcnt(2)\n\t" \
   "v_mfma_f32_16x16x32_bf16 v[64:67], a[128:131], v[96:99], v[64:67]\n\t" "buffer_load_dwordx4 v16, %[rs], s51 offen offset:3072 sc1 lds\n\t" \
   "s_add_u32 m0, m0, 4096\n\t" "s_add_u32 s51, s51, 4096\n\t" "v_mfma_f32_16x16x32_bf16 v[68:71], a[132:135], v[96:99], v[68:71]\n\t" \
   "ds_read_b128 v[80:83], v26 offset:32768\n\t" "v_mfma_f32_16x16x32_bf16 v[72:75], a[136:139], v[96:99], v[72:75]\n\t" "v_mul_f32 v53, v53, v11\n\t" \
   "buffer_store_dwordx4 v[156:159], v155, s[56:59], s55 offen sc1\n\t" "v_mfma_f32_16x16x32_bf16 v[76:79], a[140:143], v[96:99], v[76:79]\n\t" \
   "v_mul_f32 v54, v54, v11\n\t" "v_mul_f32 v55, v55, v11\n\t" "v_mfma_f32_16x16x32_bf16 v[64:67], a[144:147], v[100:103], v[64:67]\n\t" \
   "v_mul_f32 v56, v56, v10\n\t" "v_mul_f32 v57, v57, v10\n\t" "v_mfma_f32_16x16x32_bf16 v[68:71], a[148:151], v[100:103], v[68:71]\n\t" \
   "ds_read_b128 v[84:87], v26 offset:33792\n\t" "v_mfma_f32_16x16x32_bf16 v[72:75], a[152:155], v[100:103], v[72:75]\n\t" \
   "v_mul_f32 v58, v58, v10\n\t" "v_mul_f32 v59, v59, v10\n\t" "v_mfma_f32_16x16x32_bf16 v[76:79], a[156:159], v[100:103], v[76:79]\n\t" \
   "v_mul_f32 v60, v60, v10\n\t" "s_waitcnt lgkmcnt(2)\n\t" "v_mfma_f32_16x16x32_bf16 v[64:67], a[160:163], v[104:107], v[64:67]\n\t" \
   "buffer_load_dwordx4 v16, %[rs], s51 offen offset:0 sc1 lds\n\t" "v_mfma_f32_16x16x32_bf16 v[68:71], a[164:167], v[104:107], v[68:71]\n\t" \
   "ds_read_b128 v[88:91], v26 offset:34816\n\t" "v_mfma_f32_16x16x32_bf16 v[72:75], a[168:171], v[104:107], v[72:75]\n\t" \
   "v_mul_f32 v61, v61, v10\n\t" "v_mul_f32 v62, v62, v10\n\t" "v_mfma_f32_16x16x32_bf16 v[76:79], a[172:175], v[104:107], v[76:79]\n\t" \
   "v_mul_f32 v63, v63, v10\n\t" "v_mul_f32 v123, 0x3acccccd, v123\n\t" "v_mfma_f32_16x16x32_bf16 v[64:67], a[176:179], v[108:111], v[64:67]\n\t" \
   "v_cvt_pk_bf16_f32 v136, v48, v49\n\t" "v_fma_f32 v123, -v122, v122, v123\n\t" \
   "v_mfma_f32_16x16x32_bf16 v[68:71], a[180:183], v[108:111], v[68:71]\n\t" "ds_read_b128 v[92:95], v26 offset:35840\n\t" \
   "v_mfma_f32_16x16x32_bf16 v[72:75], a[184:187], v[108:111], v[72:75]\n\t" "v_cvt_pk_bf16_f32 v137, v50, v51\n\t" "v_max_f32 v123, 0, v123\n\t" \
   "v_mfma_f32_16x16x32_bf16 v[76:79], a[188:191], v[108:111], v[76:79]\n\t" "v_cvt_pk_bf16_f32 v138, v52, v53\n\t" "s_waitcnt lgkmcnt(2)\n\t" \
   "v_mfma_f32_16x16x32_bf16 v[64:67], a[192:195], v[80:83], v[64:67]\n\t" "v_cvt_pk_bf16_f32 v123, v123, v122\n\t" \
   "v_cvt_pk_bf16_f32 v139, v54, v55\n\t" "buffer_load_dwordx4 v16, %[rs], s51 offen offset:1024 sc1 lds\n\t" "s_add_u32 m0, s53, %[ld1]\n\t" \
   "s_nop 0\n\t" "v_mfma_f32_16x16x32_bf16 v[68:71], a[196:199], v[80:83], v[68:71]\n\t" "ds_read_b128 v[96:99], v26 offset:36864\n\t" \
   "v_mfma_f32_16x16x32_bf16 v[72:75], a[200:203], v[80:83], v[72:75]\n\t" "v_and_b32 v12, 0xffff0000, v123\n\t" "v_cvt_pk_bf16_f32 v140, v56, v57\n\t" \
   "v_mfma_f32_16x16x32_bf16 v[76:79], a[204:207], v[80:83], v[76:79]\n\t" "v_lshlrev_b32 v123, 16, v123\n\t" "v_cvt_pk_bf16_f32 v141, v58, v59\n\t" \
   "v_mfma_f32_16x16x32_bf16 v[64:67], a[208:211], v[84:87], v[64:67]\n\t" "v_add_f32 v123, 0x3727c5ac, v123\n\t" \
   "v_cvt_pk_bf16_f32 v142, v60, v61\n\t" "v_mfma_f32_16x16x32_bf16 v[68:71], a[212:215], v[84:87], v[68:71]\n\t" \
   "ds_read_b128 v[100:103], v26 offset:37888\n\t" "v_mfma_f32_16x16x32_bf16 v[72:75], a[216:219], v[84:87], v[72:75]\n\t" \
   "v_cvt_pk_bf16_f32 v123, v123, v123\n\t" "v_cvt_pk_bf16_f32 v143, v62, v63\n\t" \
   "v_mfma_f32_16x16x32_bf16 v[76:79], a[220:223], v[84:87], v[76:79]\n\t" "v_lshlrev_b32 v123, 16, v123\n\t" "s_waitcnt lgkmcnt(2)\n\t" \
   "v_mfma_f32_16x16x32_bf16 v[64:67], a[224:227], v[88:91], v[64:67]\n\t" "s_lshl_b32 s47, s41, 13\n\t" "v_sqrt_f32 v123, v123\n\t" \
   "s_add_i32 s47, s47, 0\n\t" "v_mfma_f32_16x16x32_bf16 v[68:71], a[228:231], v[88:91], v[68:71]\n\t" "ds_read_b128 v[104:107], v26 offset:38912\n\t" \
   "buffer_load_dwordx4 v17, %[rs], s52 offen sc1 lds\n\t" "v_mfma_f32_16x16x32_bf16 v[72:75], a[232:235], v[88:91], v[72:75]\n\t" \
   "s_add_u32 s48, s47, %[qf]\n\t" "s_nop 0\n\t" "buffer_store_dwordx4 v[136:139], v21, %[rs], s48 offen sc1\n\t" \
   "v_mfma_f32_16x16x32_bf16 v[76:79], a[236:239], v[88:91], v[76:79]\n\t" "v_cvt_pk_bf16_f32 v123, v123, v123\n\t" "v_lshlrev_b32 v123, 16, v123\n\t" \
   "s_add_u32 s48, s47, %[kf]\n\t" "v_rcp_f32 v132, v123\n\t" "v_mfma_f32_16x16x32_bf16 v[64:67], a[240:243], v[92:95], v[64:67]\n\t" \
   "buffer_store_dwordx4 v[140:143], v21, %[rs], s48 offen sc1\n\t" "v_add_u32 v28, s43, v22\n\t" "v_add_u32 v29, s43, v23\n\t" \
   "v_mfma_f32_16x16x32_bf16 v[68:71], a[244:247], v[92:95], v[68:71]\n\t" "ds_read_b128 v[108:111], v26 offset:39936\n\t" \
   "v_mfma_f32_16x16x32_bf16 v[72:75], a[248:251], v[92:95], v[72:75]\n\t" "v_mfma_f32_16x16x32_bf16 v[76:79], a[252:255], v[92:95], v[76:79]\n\t" \
   "s_waitcnt vmcnt(18) lgkmcnt(0)\n\t" "s_barrier\n\t" "v_mfma_f32_16x16x32_bf16 v[64:67], v[192:195], v[96:99], v[64:67]\n\t" \
   "s_add_u32 s45, s40, 3\n\t" "s_min_u32 s45, s45, 63\n\t" "v_mfma_f32_16x16x32_bf16 v[68:71], v[196:199], v[96:99], v[68:71]\n\t" \
   "ds_read_b128 v[80:83], v28 offset:0\n\t" "v_mfma_f32_16x16x32_bf16 v[72:75], v[200:203], v[96:99], v[72:75]\n\t" "s_mov_b32 s46, s42\n\t" \
   "v_mfma_f32_16x16x32_bf16 v[76:79], v[204:207], v[96:99], v[76:79]\n\t" "s_lshr_b32 s48, s45, 1\n\t" "s_bitcmp1_b32 s44, s48\n\t" \
   "s_cbranch_scc0 174f\n\t" "74:\n\t" "v_mfma_f32_16x16x32_bf16 v[64:67], v[208:211], v[100:103], v[64:67]\n\t" "s_mul_i32 s52, s45, 1536\n\t" \
   "s_mul_i32 s51, s45, 40960\n\t" "s_add_u32 m0, s46, %[ldx]\n\t" "s_mov_b32 s53, s46\n\t" \
   "v_mfma_f32_16x16x32_bf16 v[68:71], v[212:215], v[100:103], v[68:71]\n\t" "ds_read_b128 v[84:87], v28 offset:1024\n\t" \
   "v_mfma_f32_16x16x32_bf16 v[72:75], v[216:219], v[100:103], v[72:75]\n\t" "buffer_load_dwordx4 v16, %[rs], s51 offen offset:0 sc1 lds\n\t" \
   "v_mfma_f32_16x16x32_bf16 v[76:79], v[220:223], v[100:103], v[76:79]\n\t" "s_mov_b32 s42, s43\n\t" \
   "v_mfma_f32_16x16x32_bf16 v[64:67], v[224:227], v[104:107], v[64:67]\n\t" "s_add_u32 s43, s43, 44032\n\t" \
   "v_mfma_f32_16x16x32_bf16 v[68:71], v[228:231], v[104:107], v[68:71]\n\t" "ds_read_b128 v[88:91], v28 offset:2048\n\t" \
   "v_mfma_f32_16x16x32_bf16 v[72:75], v[232:235], v[104:107], v[72:75]\n\t" "s_cmp_eq_u32 s43, 132096\n\t" "s_cselect_b32 s43, 0, s43\n\t" \
   "v_mfma_f32_16x16x32_bf16 v[76:79], v[236:239], v[104:107], v[76:79]\n\t" "s_add_u32 s41, s41, 1\n\t" "s_add_u32 s40, s40, 1\n\t" \
   "v_mfma_f32_16x16x32_bf16 v[64:67], v[240:243], v[108:111], v[64:67]\n\t" "v_mfma_f32_16x16x32_bf16 v[68:71], v[244:247], v[108:111], v[68:71]\n\t" \
   "ds_read_b128 v[92:95], v28 offset:3072\n\t" "v_mfma_f32_16x16x32_bf16 v[72:75], v[248:251], v[108:111], v[72:75]\n\t" \
   "v_mfma_f32_16x16x32_bf16 v[76:79], v[252:255], v[108:111], v[76:79]\n\t" "s_cmp_lt_u32 s40, 64\n\t" "s_cbranch_scc1 1b\n\t" "s_nop 7\n\t" \
   "v_mul_f32 v133, 0x3e0293ee, v132\n\t" "v_fmac_f32 v64, v32, v12\n\t" "v_fmac_f32 v65, v33, v12\n\t" "v_fmac_f32 v66, v34, v12\n\t" \
   "v_fmac_f32 v67, v35, v12\n\t" "v_fmac_f32 v68, v36, v12\n\t" "v_fmac_f32 v69, v37, v12\n\t" "v_fmac_f32 v70, v38, v12\n\t" \
   "v_fmac_f32 v71, v39, v12\n\t" "v_fmac_f32 v72, v40, v12\n\t" "v_fmac_f32 v73, v41, v12\n\t" "v_fmac_f32 v74, v42, v12\n\t" \
   "v_fmac_f32 v75, v43, v12\n\t" "v_fmac_f32 v76, v44, v12\n\t" "v_fmac_f32 v77, v45, v12\n\t" "v_fmac_f32 v78, v46, v12\n\t" \
   "v_fmac_f32 v79, v47, v12\n\t" "v_mul_f32 v64, v64, v133\n\t" "v_mul_f32 v65, v65, v133\n\t" "v_mul_f32 v66, v66, v133\n\t" \
   "v_mul_f32 v67, v67, v133\n\t" "v_mul_f32 v68, v68, v133\n\t" "v_mul_f32 v69, v69, v133\n\t" "v_mul_f32 v70, v70, v133\n\t" \
   "v_mul_f32 v71, v71, v133\n\t" "v_mul_f32 v72, v72, v132\n\t" "v_mul_f32 v73, v73, v132\n\t" "v_mul_f32 v74, v74, v132\n\t" \
   "v_mul_f32 v75, v75, v132\n\t" "v_mul_f32 v76, v76, v132\n\t" "v_mul_f32 v77, v77, v132\n\t" "v_mul_f32 v78, v78, v132\n\t" \
   "v_mul_f32 v79, v79, v132\n\t" "v_cvt_pk_bf16_f32 v136, v64, v65\n\t" "v_cvt_pk_bf16_f32 v137, v66, v67\n\t" "v_cvt_pk_bf16_f32 v138, v68, v69\n\t" \
   "v_cvt_pk_bf16_f32 v139, v70, v71\n\t" "v_cvt_pk_bf16_f32 v140, v72, v73\n\t" "v_cvt_pk_bf16_f32 v141, v74, v75\n\t" \
   "v_cvt_pk_bf16_f32 v142, v76, v77\n\t" "v_cvt_pk_bf16_f32 v143, v78, v79\n\t" "s_lshl_b32 s47, s41, 13\n\t" "s_add_i32 s47, s47, -4096\n\t" \
   "s_max_i32 s47, s47, 0\n\t" "s_cmp_eq_u32 s40, 0\n\t" "s_cselect_b32 s48, 0x7ffffff0, 0\n\t" "v_or_b32 v30, s48, v21\n\t" \
   "s_add_u32 s48, s47, %[qf]\n\t" "buffer_store_dwordx4 v[136:139], v30, %[rs], s48 offen sc1\n\t" "s_add_u32 s48, s47, %[kf]\n\t" \
   "buffer_store_dwordx4 v[140:143], v30, %[rs], s48 offen sc1\n\t" "s_waitcnt vmcnt(0) lgkmcnt(0)\n\t" "s_cmp_lt_i32 s40, 1\n\t" \
   "s_cselect_b32 s47, 0x7ffffff0, 0\n\t" "v_or_b32 v30, s47, v20\n\t" "s_sub_i32 s47, s41, 1\n\t" "s_max_i32 s47, s47, 0\n\t" \
   "s_lshl_b32 s47, s47, 12\n\t" "buffer_store_dword v31, v30, %[rs], s47 offen sc1\n\t" "s_branch 9f\n\t" "172:\n\t" "s_mov_b32 s47, 0\n\t" \
   "buffer_load_dword v30, v19, %[rs], 0 offen sc1\n\t" "s_waitcnt vmcnt(0)\n\t" "v_add_u32 v30, 1, v30\n\t" "v_cmp_lt_u32 vcc, 9, v30\n\t" \
   "s_mov_b32 s44, vcc_lo\n\t" "s_bitcmp1_b32 s44, s48\n\t" "s_cbranch_scc1 72b\n\t" "s_bitset1_b32 s44, s48\n\t" "s_lshl_b32 s48, s48, 7\n\t" \
   "s_add_u32 s48, s48, %[cnt]\n\t" "73:\n\t" "s_sleep 10\n\t" "buffer_load_dword v30, off, %[rs], s48 sc1\n\t" "s_add_u32 s47, s47, 1\n\t" \
   "s_waitcnt vmcnt(0)\n\t" "v_add_u32 v30, 1, v30\n\t" "v_cmp_lt_u32 vcc, 9, v30\n\t" "s_cmp_gt_u32 s47, 0x20000\n\t" "s_cbranch_scc1 72b\n\t" \
   "s_cbranch_vccz 73b\n\t" "s_branch 72b\n\t" "174:\n\t" "s_mov_b32 s47, 0\n\t" "buffer_load_dword v30, v19, %[rs], 0 offen sc1\n\t" \
   "s_waitcnt vmcnt(0)\n\t" "v_add_u32 v30, 1, v30\n\t" "v_cmp_lt_u32 vcc, 9, v30\n\t" "s_mov_b32 s44, vcc_lo\n\t" "s_bitcmp1_b32 s44, s48\n\t" \
   "s_cbranch_scc1 74b\n\t" "s_bitset1_b32 s44, s48\n\t" "s_lshl_b32 s48, s48, 7\n\t" "s_add_u32 s48, s48, %[cnt]\n\t" "75:\n\t" "s_sleep 10\n\t" \
   "buffer_load_dword v30, off, %[rs], s48 sc1\n\t" "s_add_u32 s47, s47, 1\n\t" "s_waitcnt vmcnt(0)\n\t" "v_add_u32 v30, 1, v30\n\t" \
   "v_cmp_lt_u32 vcc, 9, v30\n\t" "s_cmp_gt_u32 s47, 0x20000\n\t" "s_cbranch_scc1 74b\n\t" "s_cbranch_vccz 75b\n\t" "s_branch 74b\n\t" "9:\n\t" \
   "s_waitcnt vmcnt(0)"
#define KQ_CLOB "v8", "v9", "v10", "v11", "v12", "v13", "v14", "v15", "v32", "v33", "v34", "v35", "v36", "v37", "v38", "v39", "v40", "v41", "v42", "v43", "v44", "v45", "v46", "v47", "v48", "v49", "v50", "v51", "v52", "v53", "v54", "v55", "v56", "v57", "v58", "v59", "v60", "v61", "v62", "v63", "v64", "v65", "v66", "v67", "v68", "v69", "v70", "v71", "v72", "v73", "v74", "v75", "v76", "v77", "v78", "v79", "v80", "v81", "v82", "v83", "v84", "v85", "v86", "v87", "v88", "v89", "v90", "v91", "v92", "v93", "v94", "v95", "v96", "v97", "v98", "v99", "v100", "v101", "v102", "v103", "v104", "v105", "v106", "v107", "v108", "v109", "v110", "v111", "v112", "v113", "v114", "v115", "v116", "v117", "v118", "v119", "v120", "v121", "v122", "v123", "v124", "v125", "v126", "v127", "v128", "v129", "v130", "v131", "v132", "v133", "v134", "v135", "v136", "v137", "v138", "v139", "v140", "v141", "v142", "v143", "v144", "v145", "v146", "v147", "v148", "v149", "v150", "v151", "s40", "s41", "s42", "s43", "s44", "s45", "s46", "s47", "s48", "s49", "s50", "s51", "s52", "s53", "s54", "s55", "s60", "s61"
#define VU_ASM \
   "s_mov_b32 s44, %[rdy]\n\t" "s_mov_b32 s45, 0\n\t" "s_mov_b32 s46, 0\n\t" "s_cmp_eq_u32 s44, -1\n\t" "s_cbranch_scc1 68f\n\t" \
   "s_mul_i32 s52, s45, 1536\n\t" "s_mul_i32 s51, s45, 40960\n\t" "s_add_u32 m0, s46, %[ldx]\n\t" "s_mov_b32 s53, s46\n\t" \
   "buffer_load_dwordx4 v16, %[rs], s51 offen offset:0 sc1 lds\n\t" "buffer_load_dwordx4 v16, %[rs], s51 offen offset:1024 sc1 lds\n\t" \
   "buffer_load_dwordx4 v16, %[rs], s51 offen offset:2048 sc1 lds\n\t" "buffer_load_dwordx4 v16, %[rs], s51 offen offset:3072 sc1 lds\n\t" \
   "s_add_u32 m0, m0, 4096\n\t" "s_add_u32 s51, s51, 4096\n\t" "buffer_load_dwordx4 v16, %[rs], s51 offen offset:0 sc1 lds\n\t" \
   "buffer_load_dwordx4 v16, %[rs], s51 offen offset:1024 sc1 lds\n\t" "buffer_load_dwordx4 v16, %[rs], s51 offen offset:2048 sc1 lds\n\t" \
   "buffer_load_dwordx4 v16, %[rs], s51 offen offset:3072 sc1 lds\n\t" "s_add_u32 m0, m0, 4096\n\t" "s_add_u32 s51, s51, 4096\n\t" \
   "buffer_load_dwordx4 v16, %[rs], s51 offen offset:0 sc1 lds\n\t" "buffer_load_dwordx4 v16, %[rs], s51 offen offset:1024 sc1 lds\n\t" \
   "s_add_u32 m0, s53, %[ld1]\n\t" "s_nop 0\n\t" "buffer_load_dwordx4 v17, %[rs], s52 offen sc1 lds\n\t" "s_mov_b32 s45, 1\n\t" \
   "s_mov_b32 s46, 44032\n\t" "s_mul_i32 s52, s45, 1536\n\t" "s_mul_i32 s51, s45, 40960\n\t" "s_add_u32 m0, s46, %[ldx]\n\t" "s_mov_b32 s53, s46\n\t" \
   "buffer_load_dwordx4 v16, %[rs], s51 offen offset:0 sc1 lds\n\t" "buffer_load_dwordx4 v16, %[rs], s51 offen offset:1024 sc1 lds\n\t" \
   "buffer_load_dwordx4 v16, %[rs], s51 offen offset:2048 sc1 lds\n\t" "buffer_load_dwordx4 v16, %[rs], s51 offen offset:3072 sc1 lds\n\t" \
   "s_add_u32 m0, m0, 4096\n\t" "s_add_u32 s51, s51, 4096\n\t" "buffer_load_dwordx4 v16, %[rs], s51 offen offset:0 sc1 lds\n\t" \
   "buffer_load_dwordx4 v16, %[rs], s51 offen offset:1024 sc1 lds\n\t" "buffer_load_dwordx4 v16, %[rs], s51 offen offset:2048 sc1 lds\n\t" \
   "buffer_load_dwordx4 v16, %[rs], s51 offen offset:3072 sc1 lds\n\t" "s_add_u32 m0, m0, 4096\n\t" "s_add_u32 s51, s51, 4096\n\t" \
   "buffer_load_dwordx4 v16, %[rs], s51 offen offset:0 sc1 lds\n\t" "buffer_load_dwordx4 v16, %[rs], s51 offen offset:1024 sc1 lds\n\t" \
   "s_add_u32 m0, s53, %[ld1]\n\t" "s_nop 0\n\t" "buffer_load_dwordx4 v17, %[rs], s52 offen sc1 lds\n\t" "68:\n\t" "v_mov_b32 v80, 0x3f803f80\n\t" \
   "v_mov_b32 v81, 0x3f803f80\n\t" "v_mov_b32 v82, 0x3f803f80\n\t" "v_mov_b32 v83, 0x3f803f80\n\t" "v_mov_b32 v31, 1\n\t" "s_nop 1\n\t" \
   "v_mfma_f32_16x16x32_bf16 v[32:35], a[0:3], v[80:83], 0\n\t" "v_mfma_f32_16x16x32_bf16 v[36:39], a[4:7], v[80:83], 0\n\t" \
   "v_mfma_f32_16x16x32_bf16 v[32:35], a[8:11], v[80:83], v[32:35]\n\t" "v_mfma_f32_16x16x32_bf16 v[36:39], a[12:15], v[80:83], v[36:39]\n\t" \
   "v_mfma_f32_16x16x32_bf16 v[32:35], a[16:19], v[80:83], v[32:35]\n\t" "v_mfma_f32_16x16x32_bf16 v[36:39], a[20:23], v[80:83], v[36:39]\n\t" \
   "v_mfma_f32_16x16x32_bf16 v[32:35], a[24:27], v[80:83], v[32:35]\n\t" "v_mfma_f32_16x16x32_bf16 v[36:39], a[28:31], v[80:83], v[36:39]\n\t" \
   "v_mfma_f32_16x16x32_bf16 v[32:35], a[32:35], v[80:83], v[32:35]\n\t" "v_mfma_f32_16x16x32_bf16 v[36:39], a[36:39], v[80:83], v[36:39]\n\t" \
   "v_mfma_f32_16x16x32_bf16 v[32:35], a[40:43], v[80:83], v[32:35]\n\t" "v_mfma_f32_16x16x32_bf16 v[36:39], a[44:47], v[80:83], v[36:39]\n\t" \
   "v_mfma_f32_16x16x32_bf16 v[32:35], a[48:51], v[80:83], v[32:35]\n\t" "v_mfma_f32_16x16x32_bf16 v[36:39], a[52:55], v[80:83], v[36:39]\n\t" \
   "v_mfma_f32_16x16x32_bf16 v[32:35], a[56:59], v[80:83], v[32:35]\n\t" "v_mfma_f32_16x16x32_bf16 v[36:39], a[60:63], v[80:83], v[36:39]\n\t" \
   "v_mfma_f32_16x16x32_bf16 v[32:35], a[64:67], v[80:83], v[32:35]\n\t" "v_mfma_f32_16x16x32_bf16 v[36:39], a[68:71], v[80:83], v[36:39]\n\t" \
   "v_mfma_f32_16x16x32_bf16 v[32:35], a[72:75], v[80:83], v[32:35]\n\t" "v_mfma_f32_16x16x32_bf16 v[36:39], a[76:79], v[80:83], v[36:39]\n\t" \
   "v_mfma_f32_16x16x32_bf16 v[32:35], a[80:83], v[80:83], v[32:35]\n\t" "v_mfma_f32_16x16x32_bf16 v[36:39], a[84:87], v[80:83], v[36:39]\n\t" \
   "v_mfma_f32_16x16x32_bf16 v[32:35], a[88:91], v[80:83], v[32:35]\n\t" "v_mfma_f32_16x16x32_bf16 v[36:39], a[92:95], v[80:83], v[36:39]\n\t" \
   "v_mfma_f32_16x16x32_bf16 v[32:35], a[96:99], v[80:83], v[32:35]\n\t" "v_mfma_f32_16x16x32_bf16 v[36:39], a[100:103], v[80:83], v[36:39]\n\t" \
   "v_mfma_f32_16x16x32_bf16 v[32:35], a[104:107], v[80:83], v[32:35]\n\t" "v_mfma_f32_16x16x32_bf16 v[36:39], a[108:111], v[80:83], v[36:39]\n\t" \
   "v_mfma_f32_16x16x32_bf16 v[32:35], a[112:115], v[80:83], v[32:35]\n\t" "v_mfma_f32_16x16x32_bf16 v[36:39], a[116:119], v[80:83], v[36:39]\n\t" \
   "v_mfma_f32_16x16x32_bf16 v[32:35], a[120:123], v[80:83], v[32:35]\n\t" "v_mfma_f32_16x16x32_bf16 v[36:39], a[124:127], v[80:83], v[36:39]\n\t" \
   "v_mfma_f32_16x16x32_bf16 v[32:35], a[128:131], v[80:83], v[32:35]\n\t" "v_mfma_f32_16x16x32_bf16 v[36:39], a[132:135], v[80:83], v[36:39]\n\t" \
   "v_mfma_f32_16x16x32_bf16 v[32:35], a[136:139], v[80:83], v[32:35]\n\t" "v_mfma_f32_16x16x32_bf16 v[36:39], a[140:143], v[80:83], v[36:39]\n\t" \
   "v_mfma_f32_16x16x32_bf16 v[32:35], a[144:147], v[80:83], v[32:35]\n\t" "v_mfma_f32_16x16x32_bf16 v[36:39], a[148:151], v[80:83], v[36:39]\n\t" \
   "v_mfma_f32_16x16x32_bf16 v[32:35], a[152:155], v[80:83], v[32:35]\n\t" "v_mfma_f32_16x16x32_bf16 v[36:39], a[156:159], v[80:83], v[36:39]\n\t" \
   "s_mov_b32 s40, 0\n\t" "s_mov_b32 s41, 0\n\t" "s_mov_b32 s42, 0\n\t" "s_mov_b32 s43, 44032\n\t" "v_mov_b32 v28, v22\n\t" "v_mov_b32 v29, v23\n\t" \
   "s_mov_b32 s45, 2\n\t" "s_mov_b32 s46, 88064\n\t" "s_waitcnt vmcnt(11)\n\t" "s_barrier\n\t" "s_lshr_b32 s48, s45, 1\n\t" \
   "s_bitcmp1_b32 s44, s48\n\t" "s_cbranch_scc0 172f\n\t" "72:\n\t" "s_mul_i32 s52, s45, 1536\n\t" "s_mul_i32 s51, s45, 40960\n\t" \
   "s_add_u32 m0, s46, %[ldx]\n\t" "s_mov_b32 s53, s46\n\t" "buffer_load_dwordx4 v16, %[rs], s51 offen offset:0 sc1 lds\n\t" \
   "ds_read_b128 v[80:83], v28 offset:0\n\t" "ds_read_b128 v[84:87], v28 offset:1024\n\t" "ds_read_b128 v[88:91], v28 offset:2048\n\t" \
   "ds_read_b128 v[92:95], v28 offset:3072\n\t" "ds_read_b128 v[96:99], v28 offset:4096\n\t" "ds_read_b128 v[100:103], v28 offset:5120\n\t" \
   "ds_read_b128 v[104:107], v28 offset:6144\n\t" "v_mov_b32 v14, v24\n\t" "v_mov_b32 v15, v25\n\t" "v_mov_b32 v18, 0x7ffffff0\n\t" \
   "s_mov_b32 s49, 0\n\t" "v_mov_b32 v26, v28\n\t" "v_mov_b32 v27, v29\n\t" "s_waitcnt lgkmcnt(5)\n\t" \
   "v_mfma_f32_16x16x32_bf16 v[48:51], a[0:3], v[80:83], 0\n\t" "ds_read_b128 v[112:115], v27 offset:0\n\t" \
   "v_mfma_f32_16x16x32_bf16 v[52:55], a[4:7], v[80:83], 0\n\t" "ds_read_b128 v[108:111], v26 offset:7168\n\t" \
   "v_mfma_f32_16x16x32_bf16 v[48:51], a[8:11], v[84:87], v[48:51]\n\t" "ds_read_b128 v[116:119], v27 offset:16\n\t" \
   "v_mfma_f32_16x16x32_bf16 v[52:55], a[12:15], v[84:87], v[52:55]\n\t" "ds_read_b128 v[80:83], v26 offset:8192\n\t" "s_waitcnt lgkmcnt(7)\n\t" \
   "v_mfma_f32_16x16x32_bf16 v[48:51], a[16:19], v[88:91], v[48:51]\n\t" "ds_read_b64 v[120:121], v27 offset:32\n\t" \
   "v_mfma_f32_16x16x32_bf16 v[52:55], a[20:23], v[88:91], v[52:55]\n\t" "ds_read_b128 v[84:87], v26 offset:9216\n\t" \
   "v_mfma_f32_16x16x32_bf16 v[48:51], a[24:27], v[92:95], v[48:51]\n\t" "ds_read_b128 v[122:125], v27 offset:768\n\t" \
   "v_mfma_f32_16x16x32_bf16 v[52:55], a[28:31], v[92:95], v[52:55]\n\t" "ds_read_b128 v[88:91], v26 offset:10240\n\t" "s_waitcnt lgkmcnt(9)\n\t" \
   "v_mfma_f32_16x16x32_bf16 v[48:51], a[32:35], v[96:99], v[48:51]\n\t" "buffer_load_dwordx4 v16, %[rs], s51 offen offset:1024 sc1 lds\n\t" \
   "v_mfma_f32_16x16x32_bf16 v[52:55], a[36:39], v[96:99], v[52:55]\n\t" "ds_read_b128 v[92:95], v26 offset:11264\n\t" \
   "v_mfma_f32_16x16x32_bf16 v[48:51], a[40:43], v[100:103], v[48:51]\n\t" "ds_read_b128 v[126:129], v27 offset:784\n\t" \
   "v_mfma_f32_16x16x32_bf16 v[52:55], a[44:47], v[100:103], v[52:55]\n\t" "ds_read_b128 v[96:99], v26 offset:12288\n\t" "s_waitcnt lgkmcnt(9)\n\t" \
   "v_mfma_f32_16x16x32_bf16 v[48:51], a[48:51], v[104:107], v[48:51]\n\t" "ds_read_b64 v[130:131], v27 offset:800\n\t" \
   "v_mfma_f32_16x16x32_bf16 v[52:55], a[52:55], v[104:107], v[52:55]\n\t" "ds_read_b128 v[100:103], v26 offset:13312\n\t" \
   "v_mfma_f32_16x16x32_bf16 v[48:51], a[56:59], v[108:111], v[48:51]\n\t" "s_waitcnt lgkmcnt(8)\n\t" "v_add_f32 v112, v112, v114\n\t" \
   "v_mfma_f32_16x16x32_bf16 v[52:55], a[60:63], v[108:111], v[52:55]\n\t" "ds_read_b128 v[104:107], v26 offset:14336\n\t" "s_waitcnt lgkmcnt(8)\n\t" \
   "v_mfma_f32_16x16x32_bf16 v[48:51], a[64:67], v[80:83], v[48:51]\n\t" "buffer_load_dwordx4 v16, %[rs], s51 offen offset:2048 sc1 lds\n\t" \
   "v_mfma_f32_16x16x32_bf16 v[52:55], a[68:71], v[80:83], v[52:55]\n\t" "ds_read_b128 v[108:111], v26 offset:15360\n\t" \
   "v_mfma_f32_16x16x32_bf16 v[48:51], a[72:75], v[84:87], v[48:51]\n\t" "v_add_f32 v113, v113, v115\n\t" "v_add_f32 v116, v116, v118\n\t" \
   "v_mfma_f32_16x16x32_bf16 v[52:55], a[76:79], v[84:87], v[52:55]\n\t" "ds_read_b128 v[80:83], v26 offset:16384\n\t" "s_waitcnt lgkmcnt(7)\n\t" \
   "v_mfma_f32_16x16x32_bf16 v[48:51], a[80:83], v[88:91], v[48:51]\n\t" "v_add_f32 v117, v117, v119\n\t" "v_add_f32 v112, v112, v120\n\t" \
   "v_mfma_f32_16x16x32_bf16 v[52:55], a[84:87], v[88:91], v[52:55]\n\t" "ds_read_b128 v[84:87], v26 offset:17408\n\t" \
   "v_mfma_f32_16x16x32_bf16 v[48:51], a[88:91], v[92:95], v[48:51]\n\t" "v_add_f32 v113, v113, v121\n\t" "v_add_f32 v112, v112, v116\n\t" \
   "v_mfma_f32_16x16x32_bf16 v[52:55], a[92:95], v[92:95], v[52:55]\n\t" "ds_read_b128 v[88:91], v26 offset:18432\n\t" \
   "buffer_load_dwordx4 v16, %[rs], s51 offen offset:3072 sc1 lds\n\t" "s_add_u32 m0, m0, 4096\n\t" "s_add_u32 s51, s51, 4096\n\t" \
   "s_waitcnt lgkmcnt(5)\n\t" "v_mfma_f32_16x16x32_bf16 v[48:51], a[96:99], v[96:99], v[48:51]\n\t" "v_add_f32 v113, v113, v117\n\t" \
   "v_mul_f32 v112, 0xbacccccd, v112\n\t" "v_mfma_f32_16x16x32_bf16 v[52:55], a[100:103], v[96:99], v[52:55]\n\t" \
   "ds_read_b128 v[92:95], v26 offset:19456\n\t" "v_mfma_f32_16x16x32_bf16 v[48:51], a[104:107], v[100:103], v[48:51]\n\t" \
   "v_mul_f32 v113, 0x3acccccd, v113\n\t" "v_fma_f32 v113, -v112, v112, v113\n\t" \
   "v_mfma_f32_16x16x32_bf16 v[52:55], a[108:111], v[100:103], v[52:55]\n\t" "ds_read_b128 v[96:99], v26 offset:20480\n\t" "s_waitcnt lgkmcnt(5)\n\t" \
   "v_mfma_f32_16x16x32_bf16 v[48:51], a[112:115], v[104:107], v[48:51]\n\t" "v_max_f32 v113, 0, v113\n\t" "v_cvt_pk_bf16_f32 v113, v113, v112\n\t" \
   "v_mfma_f32_16x16x32_bf16 v[52:55], a[116:119], v[104:107], v[52:55]\n\t" "ds_read_b128 v[100:103], v26 offset:21504\n\t" \
   "v_mfma_f32_16x16x32_bf16 v[48:51], a[120:123], v[108:111], v[48:51]\n\t" "buffer_load_dwordx4 v16, %[rs], s51 offen offset:0 sc1 lds\n\t" \
   "v_mfma_f32_16x16x32_bf16 v[52:55], a[124:127], v[108:111], v[52:55]\n\t" "ds_read_b128 v[104:107], v26 offset:22528\n\t" "s_waitcnt lgkmcnt(5)\n\t" \
   "v_mfma_f32_16x16x32_bf16 v[48:51], a[128:131], v[80:83], v[48:51]\n\t" "v_and_b32 v8, 0xffff0000, v113\n\t" "v_lshlrev_b32 v113, 16, v113\n\t" \
   "v_mfma_f32_16x16x32_bf16 v[52:55], a[132:135], v[80:83], v[52:55]\n\t" "ds_read_b128 v[108:111], v26 offset:23552\n\t" \
   "v_mfma_f32_16x16x32_bf16 v[48:51], a[136:139], v[84:87], v[48:51]\n\t" "v_add_f32 v113, 0x3727c5ac, v113\n\t" \
   "v_cvt_pk_bf16_f32 v113, v113, v113\n\t" "v_mfma_f32_16x16x32_bf16 v[52:55], a[140:143], v[84:87], v[52:55]\n\t" \
   "ds_read_b128 v[80:83], v26 offset:24576\n\t" "s_waitcnt lgkmcnt(5)\n\t" "v_mfma_f32_16x16x32_bf16 v[48:51], a[144:147], v[88:91], v[48:51]\n\t" \
   "v_lshlrev_b32 v113, 16, v113\n\t" "v_add_u32 v28, s43, v22\n\t" "v_mfma_f32_16x16x32_bf16 v[52:55], a[148:151], v[88:91], v[52:55]\n\t" \
   "ds_read_b128 v[84:87], v26 offset:25600\n\t" "buffer_load_dwordx4 v16, %[rs], s51 offen offset:1024 sc1 lds\n\t" \
   "v_mfma_f32_16x16x32_bf16 v[48:51], a[152:155], v[92:95], v[48:51]\n\t" "v_sqrt_f32 v113, v113\n\t" \
   "v_mfma_f32_16x16x32_bf16 v[52:55], a[156:159], v[92:95], v[52:55]\n\t" "ds_read_b128 v[88:91], v26 offset:26624\n\t" "s_nop 0\n\t" \
   "s_waitcnt lgkmcnt(5)\n\t" "v_mfma_f32_16x16x32_bf16 v[56:59], a[0:3], v[96:99], 0\n\t" "v_cvt_pk_bf16_f32 v113, v113, v113\n\t" \
   "v_lshlrev_b32 v113, 16, v113\n\t" "v_mfma_f32_16x16x32_bf16 v[60:63], a[4:7], v[96:99], 0\n\t" "ds_read_b128 v[92:95], v26 offset:27648\n\t" \
   "v_rcp_f32 v10, v113\n\t" "v_mfma_f32_16x16x32_bf16 v[56:59], a[8:11], v[100:103], v[56:59]\n\t" "v_add_f32 v122, v122, v124\n\t" \
   "v_mfma_f32_16x16x32_bf16 v[60:63], a[12:15], v[100:103], v[60:63]\n\t" "ds_read_b128 v[96:99], v26 offset:28672\n\t" "s_waitcnt lgkmcnt(5)\n\t" \
   "v_mfma_f32_16x16x32_bf16 v[56:59], a[16:19], v[104:107], v[56:59]\n\t" "buffer_load_dwordx4 v16, %[rs], s51 offen offset:2048 sc1 lds\n\t" \
   "v_mfma_f32_16x16x32_bf16 v[60:63], a[20:23], v[104:107], v[60:63]\n\t" "ds_read_b128 v[100:103], v26 offset:29696\n\t" \
   "v_mfma_f32_16x16x32_bf16 v[56:59], a[24:27], v[108:111], v[56:59]\n\t" "v_add_f32 v123, v123, v125\n\t" "v_add_f32 v126, v126, v128\n\t" \
   "v_mfma_f32_16x16x32_bf16 v[60:63], a[28:31], v[108:111], v[60:63]\n\t" "ds_read_b128 v[104:107], v26 offset:30720\n\t" "s_waitcnt lgkmcnt(5)\n\t" \
   "v_mfma_f32_16x16x32_bf16 v[56:59], a[32:35], v[80:83], v[56:59]\n\t" "v_fmac_f32 v48, v32, v8\n\t" "v_add_f32 v127, v127, v129\n\t" \
   "v_mfma_f32_16x16x32_bf16 v[60:63], a[36:39], v[80:83], v[60:63]\n\t" "ds_read_b128 v[108:111], v26 offset:31744\n\t" \
   "v_mfma_f32_16x16x32_bf16 v[56:59], a[40:43], v[84:87], v[56:59]\n\t" "v_fmac_f32 v49, v33, v8\n\t" "v_add_f32 v122, v122, v130\n\t" \
   "v_mfma_f32_16x16x32_bf16 v[60:63], a[44:47], v[84:87], v[60:63]\n\t" "ds_read_b128 v[80:83], v26 offset:32768\n\t" \
   "buffer_load_dwordx4 v16, %[rs], s51 offen offset:3072 sc1 lds\n\t" "s_add_u32 m0, m0, 4096\n\t" "s_add_u32 s51, s51, 4096\n\t" \
   "s_waitcnt lgkmcnt(5)\n\t" "v_mfma_f32_16x16x32_bf16 v[56:59], a[48:51], v[88:91], v[56:59]\n\t" "v_fmac_f32 v50, v34, v8\n\t" \
   "v_add_f32 v123, v123, v131\n\t" "v_mfma_f32_16x16x32_bf16 v[60:63], a[52:55], v[88:91], v[60:63]\n\t" "ds_read_b128 v[84:87], v26 offset:33792\n\t" \
   "v_mfma_f32_16x16x32_bf16 v[56:59], a[56:59], v[92:95], v[56:59]\n\t" "v_fmac_f32 v51, v35, v8\n\t" "v_add_f32 v122, v122, v126\n\t" \
   "v_mfma_f32_16x16x32_bf16 v[60:63], a[60:63], v[92:95], v[60:63]\n\t" "ds_read_b128 v[88:91], v26 offset:34816\n\t" "s_waitcnt lgkmcnt(5)\n\t" \
   "v_mfma_f32_16x16x32_bf16 v[56:59], a[64:67], v[96:99], v[56:59]\n\t" "v_fmac_f32 v52, v36, v8\n\t" "v_add_f32 v123, v123, v127\n\t" \
   "v_mfma_f32_16x16x32_bf16 v[60:63], a[68:71], v[96:99], v[60:63]\n\t" "ds_read_b128 v[92:95], v26 offset:35840\n\t" \
   "v_mfma_f32_16x16x32_bf16 v[56:59], a[72:75], v[100:103], v[56:59]\n\t" "buffer_load_dwordx4 v16, %[rs], s51 offen offset:0 sc1 lds\n\t" \
   "v_mfma_f32_16x16x32_bf16 v[60:63], a[76:79], v[100:103], v[60:63]\n\t" "ds_read_b128 v[96:99], v26 offset:36864\n\t" "s_waitcnt lgkmcnt(5)\n\t" \
   "v_mfma_f32_16x16x32_bf16 v[56:59], a[80:83], v[104:107], v[56:59]\n\t" "v_fmac_f32 v53, v37, v8\n\t" "v_mul_f32 v122, 0xbacccccd, v122\n\t" \
   "v_mfma_f32_16x16x32_bf16 v[60:63], a[84:87], v[104:107], v[60:63]\n\t" "ds_read_b128 v[100:103], v26 offset:37888\n\t" \
   "v_mfma_f32_16x16x32_bf16 v[56:59], a[88:91], v[108:111], v[56:59]\n\t" "v_fmac_f32 v54, v38, v8\n\t" "v_mul_f32 v123, 0x3acccccd, v123\n\t" \
   "v_mfma_f32_16x16x32_bf16 v[60:63], a[92:95], v[108:111], v[60:63]\n\t" "ds_read_b128 v[104:107], v26 offset:38912\n\t" "s_waitcnt lgkmcnt(5)\n\t" \
   "v_mfma_f32_16x16x32_bf16 v[56:59], a[96:99], v[80:83], v[56:59]\n\t" "v_fmac_f32 v55, v39, v8\n\t" "v_fma_f32 v123, -v122, v122, v123\n\t" \
   "v_mfma_f32_16x16x32_bf16 v[60:63], a[100:103], v[80:83], v[60:63]\n\t" "ds_read_b128 v[108:111], v26 offset:39936\n\t" \
   "buffer_load_dwordx4 v16, %[rs], s51 offen offset:1024 sc1 lds\n\t" "s_add_u32 m0, s53, %[ld1]\n\t" "s_nop 0\n\t" \
   "v_mfma_f32_16x16x32_bf16 v[56:59], a[104:107], v[84:87], v[56:59]\n\t" "v_mul_f32 v48, v48, v10\n\t" "v_max_f32 v123, 0, v123\n\t" \
   "v_mfma_f32_16x16x32_bf16 v[60:63], a[108:111], v[84:87], v[60:63]\n\t" "v_mul_f32 v49, v49, v10\n\t" "s_waitcnt lgkmcnt(4)\n\t" \
   "v_mfma_f32_16x16x32_bf16 v[56:59], a[112:115], v[88:91], v[56:59]\n\t" "v_cvt_pk_bf16_f32 v123, v123, v122\n\t" "v_mul_f32 v50, v50, v10\n\t" \
   "v_mfma_f32_16x16x32_bf16 v[60:63], a[116:119], v[88:91], v[60:63]\n\t" "v_and_b32 v12, 0xffff0000, v123\n\t" "v_mul_f32 v51, v51, v10\n\t" \
   "v_mfma_f32_16x16x32_bf16 v[56:59], a[120:123], v[92:95], v[56:59]\n\t" "v_lshlrev_b32 v123, 16, v123\n\t" "v_mul_f32 v52, v52, v10\n\t" \
   "v_mfma_f32_16x16x32_bf16 v[60:63], a[124:127], v[92:95], v[60:63]\n\t" "v_add_f32 v123, 0x3727c5ac, v123\n\t" "s_waitcnt lgkmcnt(2)\n\t" \
   "v_mfma_f32_16x16x32_bf16 v[56:59], a[128:131], v[96:99], v[56:59]\n\t" "v_mul_f32 v53, v53, v10\n\t" "v_cvt_pk_bf16_f32 v123, v123, v123\n\t" \
   "buffer_load_dwordx4 v17, %[rs], s52 offen sc1 lds\n\t" "v_mfma_f32_16x16x32_bf16 v[60:63], a[132:135], v[96:99], v[60:63]\n\t" \
   "v_mul_f32 v54, v54, v10\n\t" "v_lshlrev_b32 v123, 16, v123\n\t" "v_mfma_f32_16x16x32_bf16 v[56:59], a[136:139], v[100:103], v[56:59]\n\t" \
   "v_mul_f32 v55, v55, v10\n\t" "v_cvt_pk_bf16_f32 v136, v48, v49\n\t" "v_mfma_f32_16x16x32_bf16 v[60:63], a[140:143], v[100:103], v[60:63]\n\t" \
   "v_sqrt_f32 v123, v123\n\t" "s_waitcnt lgkmcnt(0)\n\t" "v_mfma_f32_16x16x32_bf16 v[56:59], a[144:147], v[104:107], v[56:59]\n\t" \
   "v_cvt_pk_bf16_f32 v137, v50, v51\n\t" "s_nop 0\n\t" "v_mfma_f32_16x16x32_bf16 v[60:63], a[148:151], v[104:107], v[60:63]\n\t" \
   "v_cvt_pk_bf16_f32 v138, v52, v53\n\t" "v_cvt_pk_bf16_f32 v123, v123, v123\n\t" \
   "v_mfma_f32_16x16x32_bf16 v[56:59], a[152:155], v[108:111], v[56:59]\n\t" "v_cvt_pk_bf16_f32 v139, v54, v55\n\t" "v_lshlrev_b32 v123, 16, v123\n\t" \
   "ds_write_b128 v14, v[136:139] offset:0\n\t" "v_mfma_f32_16x16x32_bf16 v[60:63], a[156:159], v[108:111], v[60:63]\n\t" "v_rcp_f32 v132, v123\n\t" \
   "v_add_u32 v29, s43, v23\n\t" "s_memrealtime s[54:55]\n\t" "s_nop 6\n\t" "v_fmac_f32 v56, v32, v12\n\t" "v_fmac_f32 v57, v33, v12\n\t" \
   "v_fmac_f32 v58, v34, v12\n\t" "v_fmac_f32 v59, v35, v12\n\t" "v_fmac_f32 v60, v36, v12\n\t" "v_fmac_f32 v61, v37, v12\n\t" \
   "v_fmac_f32 v62, v38, v12\n\t" "v_fmac_f32 v63, v39, v12\n\t" "v_mul_f32 v56, v56, v132\n\t" "v_mul_f32 v57, v57, v132\n\t" \
   "v_mul_f32 v58, v58, v132\n\t" "v_mul_f32 v59, v59, v132\n\t" "v_mul_f32 v60, v60, v132\n\t" "v_mul_f32 v61, v61, v132\n\t" \
   "v_mul_f32 v62, v62, v132\n\t" "v_mul_f32 v63, v63, v132\n\t" "v_cvt_pk_bf16_f32 v140, v56, v57\n\t" "v_cvt_pk_bf16_f32 v141, v58, v59\n\t" \
   "v_cvt_pk_bf16_f32 v142, v60, v61\n\t" "v_cvt_pk_bf16_f32 v143, v62, v63\n\t" "ds_write_b128 v14, v[140:143] offset:4096\n\t" \
   "s_waitcnt vmcnt(11) lgkmcnt(0)\n\t" "s_barrier\n\t" "ds_read_b128 v[80:83], v28 offset:0\n\t" "ds_read_b128 v[84:87], v28 offset:1024\n\t" \
   "ds_read_b128 v[88:91], v28 offset:2048\n\t" "ds_read_b128 v[92:95], v28 offset:3072\n\t" "ds_read_b128 v[96:99], v28 offset:4096\n\t" \
   "ds_read_b128 v[100:103], v28 offset:5120\n\t" "ds_read_b128 v[104:107], v28 offset:6144\n\t" "s_add_u32 s45, s40, 3\n\t" \
   "s_min_u32 s45, s45, 63\n\t" "s_mov_b32 s46, s42\n\t" "s_lshr_b32 s48, s45, 1\n\t" "s_bitcmp1_b32 s44, s48\n\t" "s_cbranch_scc0 176f\n\t" "76:\n\t" \
   "s_mul_i32 s52, s45, 1536\n\t" "s_mul_i32 s51, s45, 40960\n\t" "s_add_u32 m0, s46, %[ldx]\n\t" "s_mov_b32 s53, s46\n\t" \
   "buffer_load_dwordx4 v16, %[rs], s51 offen offset:0 sc1 lds\n\t" "s_mov_b32 s42, s43\n\t" "s_add_u32 s43, s43, 44032\n\t" \
   "s_cmp_eq_u32 s43, 132096\n\t" "s_cselect_b32 s43, 0, s43\n\t" "s_add_u32 s41, s41, 1\n\t" "s_add_u32 s40, s40, 1\n\t" \
   "v_xor_b32 v14, 0x2000, v14\n\t" "v_xor_b32 v15, 0x2000, v15\n\t" "v_mov_b32 v31, s54\n\t" "v_min_u32 v31, -2, v31\n\t" "1:\n\t" \
   "v_mov_b32 v26, v28\n\t" "v_mov_b32 v27, v29\n\t" "s_waitcnt lgkmcnt(5)\n\t" "v_mfma_f32_16x16x32_bf16 v[48:51], a[0:3], v[80:83], 0\n\t" \
   "ds_read_b128 v[152:155], v15 offset:0\n\t" "ds_read_b128 v[168:171], v15 offset:4096\n\t" \
   "v_mfma_f32_16x16x32_bf16 v[52:55], a[4:7], v[80:83], 0\n\t" "ds_read_b128 v[108:111], v26 offset:7168\n\t" \
   "ds_read_b128 v[156:159], v15 offset:1024\n\t" "v_mfma_f32_16x16x32_bf16 v[48:51], a[8:11], v[84:87], v[48:51]\n\t" \
   "ds_read_b128 v[172:175], v15 offset:5120\n\t" "v_mfma_f32_16x16x32_bf16 v[52:55], a[12:15], v[84:87], v[52:55]\n\t" \
   "ds_read_b128 v[80:83], v26 offset:8192\n\t" "ds_read_b128 v[160:163], v15 offset:2048\n\t" "s_waitcnt lgkmcnt(10)\n\t" \
   "v_mfma_f32_16x16x32_bf16 v[48:51], a[16:19], v[88:91], v[48:51]\n\t" "ds_read_b128 v[176:179], v15 offset:6144\n\t" \
   "v_mfma_f32_16x16x32_bf16 v[52:55], a[20:23], v[88:91], v[52:55]\n\t" "ds_read_b128 v[84:87], v26 offset:9216\n\t" \
   "ds_read_b128 v[164:167], v15 offset:3072\n\t" "v_mfma_f32_16x16x32_bf16 v[48:51], a[24:27], v[92:95], v[48:51]\n\t" \
   "ds_read_b128 v[180:183], v15 offset:7168\n\t" "v_mfma_f32_16x16x32_bf16 v[52:55], a[28:31], v[92:95], v[52:55]\n\t" \
   "ds_read_b128 v[88:91], v26 offset:10240\n\t" "s_waitcnt lgkmcnt(13)\n\t" "v_mfma_f32_16x16x32_bf16 v[48:51], a[32:35], v[96:99], v[48:51]\n\t" \
   "v_cvt_pk_bf16_f32 v148, v72, v73\n\t" "v_cvt_pk_bf16_f32 v150, v74, v75\n\t" "v_mfma_f32_16x16x32_bf16 v[52:55], a[36:39], v[96:99], v[52:55]\n\t" \
   "ds_read_b128 v[92:95], v26 offset:11264\n\t" "s_waitcnt lgkmcnt(8)\n\t" "v_mfma_f32_16x16x32_bf16 v[64:67], v[152:155], a[160:163], 0\n\t" \
   "v_cvt_pk_bf16_f32 v149, v76, v77\n\t" "v_cvt_pk_bf16_f32 v151, v78, v79\n\t" \
   "v_mfma_f32_16x16x32_bf16 v[48:51], a[40:43], v[100:103], v[48:51]\n\t" "s_add_u32 s50, s49, 294912\n\t" \
   "v_mfma_f32_16x16x32_bf16 v[52:55], a[44:47], v[100:103], v[52:55]\n\t" "ds_read_b128 v[96:99], v26 offset:12288\n\t" \
   "v_mfma_f32_16x16x32_bf16 v[68:71], v[168:171], a[160:163], 0\n\t" "v_mfma_f32_16x16x32_bf16 v[48:51], a[48:51], v[104:107], v[48:51]\n\t" \
   "buffer_store_dwordx4 v[148:151], v18, %[rs], s50 offen sc1\n\t" "v_mfma_f32_16x16x32_bf16 v[52:55], a[52:55], v[104:107], v[52:55]\n\t" \
   "ds_read_b128 v[100:103], v26 offset:13312\n\t" "s_sub_i32 s49, s41, 1\n\t" \
   "v_mfma_f32_16x16x32_bf16 v[64:67], v[156:159], a[164:167], v[64:67]\n\t" "s_mul_i32 s49, s49, 1310720\n\t" \
   "v_mfma_f32_16x16x32_bf16 v[48:51], a[56:59], v[108:111], v[48:51]\n\t" "ds_read_b128 v[112:115], v27 offset:0\n\t" \
   "v_mfma_f32_16x16x32_bf16 v[52:55], a[60:63], v[108:111], v[52:55]\n\t" "ds_read_b128 v[104:107], v26 offset:14336\n\t" \
   "v_mfma_f32_16x16x32_bf16 v[68:71], v[172:175], a[164:167], v[68:71]\n\t" "s_waitcnt lgkmcnt(8)\n\t" \
   "v_mfma_f32_16x16x32_bf16 v[48:51], a[64:67], v[80:83], v[48:51]\n\t" "ds_read_b128 v[116:119], v27 offset:16\n\t" \
   "v_mfma_f32_16x16x32_bf16 v[52:55], a[68:71], v[80:83], v[52:55]\n\t" "ds_read_b128 v[108:111], v26 offset:15360\n\t" \
   "ds_read_b64 v[120:121], v27 offset:32\n\t" "s_waitcnt lgkmcnt(9)\n\t" "v_mfma_f32_16x16x32_bf16 v[64:67], v[160:163], a[168:171], v[64:67]\n\t" \
   "ds_read_b128 v[122:125], v27 offset:768\n\t" "v_mfma_f32_16x16x32_bf16 v[48:51], a[72:75], v[84:87], v[48:51]\n\t" \
   "ds_read_b128 v[126:129], v27 offset:784\n\t" "v_mfma_f32_16x16x32_bf16 v[52:55], a[76:79], v[84:87], v[52:55]\n\t" \
   "ds_read_b128 v[80:83], v26 offset:16384\n\t" "ds_read_b64 v[130:131], v27 offset:800\n\t" \
   "v_mfma_f32_16x16x32_bf16 v[68:71], v[176:179], a[168:171], v[68:71]\n\t" "v_add_u32 v28, s43, v22\n\t" "s_waitcnt lgkmcnt(11)\n\t" \
   "v_mfma_f32_16x16x32_bf16 v[48:51], a[80:83], v[88:91], v[48:51]\n\t" "buffer_load_dwordx4 v16, %[rs], s51 offen offset:1024 sc1 lds\n\t" \
   "v_mfma_f32_16x16x32_bf16 v[52:55], a[84:87], v[88:91], v[52:55]\n\t" "ds_read_b128 v[84:87], v26 offset:17408\n\t" \
   "v_mfma_f32_16x16x32_bf16 v[48:51], a[88:91], v[92:95], v[48:51]\n\t" "s_waitcnt lgkmcnt(5)\n\t" "v_add_f32 v112, v112, v114\n\t" \
   "v_mfma_f32_16x16x32_bf16 v[64:67], v[164:167], a[172:175], v[64:67]\n\t" "v_add_f32 v113, v113, v115\n\t" "v_add_f32 v116, v116, v118\n\t" \
   "v_mfma_f32_16x16x32_bf16 v[52:55], a[92:95], v[92:95], v[52:55]\n\t" "ds_read_b128 v[88:91], v26 offset:18432\n\t" \
   "v_mfma_f32_16x16x32_bf16 v[48:51], a[96:99], v[96:99], v[48:51]\n\t" "v_add_f32 v117, v117, v119\n\t" "v_add_f32 v112, v112, v120\n\t" \
   "v_mfma_f32_16x16x32_bf16 v[68:71], v[180:183], a[172:175], v[68:71]\n\t" "v_add_f32 v113, v113, v121\n\t" "v_add_f32 v112, v112, v116\n\t" \
   "v_mfma_f32_16x16x32_bf16 v[52:55], a[100:103], v[96:99], v[52:55]\n\t" "ds_read_b128 v[92:95], v26 offset:19456\n\t" \
   "v_mfma_f32_16x16x32_bf16 v[48:51], a[104:107], v[100:103], v[48:51]\n\t" "v_add_f32 v113, v113, v117\n\t" "v_mul_f32 v112, 0xbacccccd, v112\n\t" \
   "v_mfma_f32_16x16x32_bf16 v[72:75], v[152:155], a[176:179], 0\n\t" "v_cvt_pk_bf16_f32 v144, v64, v65\n\t" "v_mul_f32 v113, 0x3acccccd, v113\n\t" \
   "v_mfma_f32_16x16x32_bf16 v[52:55], a[108:111], v[100:103], v[52:55]\n\t" "ds_read_b128 v[96:99], v26 offset:20480\n\t" \
   "buffer_load_dwordx4 v16, %[rs], s51 offen offset:2048 sc1 lds\n\t" "v_mfma_f32_16x16x32_bf16 v[48:51], a[112:115], v[104:107], v[48:51]\n\t" \
   "v_cvt_pk_bf16_f32 v146, v66, v67\n\t" "v_cvt_pk_bf16_f32 v145, v68, v69\n\t" "v_mfma_f32_16x16x32_bf16 v[76:79], v[168:171], a[176:179], 0\n\t" \
   "v_fma_f32 v113, -v112, v112, v113\n\t" "v_cvt_pk_bf16_f32 v147, v70, v71\n\t" \
   "v_mfma_f32_16x16x32_bf16 v[52:55], a[116:119], v[104:107], v[52:55]\n\t" "ds_read_b128 v[100:103], v26 offset:21504\n\t" \
   "v_mfma_f32_16x16x32_bf16 v[48:51], a[120:123], v[108:111], v[48:51]\n\t" "s_add_u32 s50, s49, 0\n\t" "v_max_f32 v113, 0, v113\n\t" \
   "v_mfma_f32_16x16x32_bf16 v[72:75], v[156:159], a[180:183], v[72:75]\n\t" "buffer_store_dwordx4 v[144:147], v21, %[rs], s50 offen sc1\n\t" \
   "v_mfma_f32_16x16x32_bf16 v[52:55], a[124:127], v[108:111], v[52:55]\n\t" "ds_read_b128 v[104:107], v26 offset:22528\n\t" "s_waitcnt lgkmcnt(5)\n\t" \
   "v_mfma_f32_16x16x32_bf16 v[48:51], a[128:131], v[80:83], v[48:51]\n\t" "v_cvt_pk_bf16_f32 v113, v113, v112\n\t" \
   "v_and_b32 v8, 0xffff0000, v113\n\t" "v_mfma_f32_16x16x32_bf16 v[52:55], a[132:135], v[80:83], v[52:55]\n\t" \
   "ds_read_b128 v[108:111], v26 offset:23552\n\t" "v_mfma_f32_16x16x32_bf16 v[76:79], v[172:175], a[180:183], v[76:79]\n\t" \
   "v_lshlrev_b32 v113, 16, v113\n\t" "v_add_f32 v113, 0x3727c5ac, v113\n\t" "v_mfma_f32_16x16x32_bf16 v[48:51], a[136:139], v[84:87], v[48:51]\n\t" \
   "buffer_load_dwordx4 v16, %[rs], s51 offen offset:3072 sc1 lds\n\t" "s_add_u32 m0, m0, 4096\n\t" "s_add_u32 s51, s51, 4096\n\t" \
   "v_mfma_f32_16x16x32_bf16 v[52:55], a[140:143], v[84:87], v[52:55]\n\t" "ds_read_b128 v[80:83], v26 offset:24576\n\t" \
   "v_mfma_f32_16x16x32_bf16 v[72:75], v[160:163], a[184:187], v[72:75]\n\t" "v_cvt_pk_bf16_f32 v113, v113, v113\n\t" "s_waitcnt lgkmcnt(5)\n\t" \
   "v_mfma_f32_16x16x32_bf16 v[48:51], a[144:147], v[88:91], v[48:51]\n\t" "v_lshlrev_b32 v113, 16, v113\n\t" "v_add_u32 v29, s43, v23\n\t" \
   "v_mfma_f32_16x16x32_bf16 v[52:55], a[148:151], v[88:91], v[52:55]\n\t" "ds_read_b128 v[84:87], v26 offset:25600\n\t" \
   "v_mfma_f32_16x16x32_bf16 v[76:79], v[176:179], a[184:187], v[76:79]\n\t" "v_sqrt_f32 v113, v113\n\t" \
   "v_mfma_f32_16x16x32_bf16 v[48:51], a[152:155], v[92:95], v[48:51]\n\t" "s_nop 0\n\t" "v_cvt_pk_bf16_f32 v113, v113, v113\n\t" \
   "v_mfma_f32_16x16x32_bf16 v[52:55], a[156:159], v[92:95], v[52:55]\n\t" "ds_read_b128 v[88:91], v26 offset:26624\n\t" \
   "v_mfma_f32_16x16x32_bf16 v[72:75], v[164:167], a[188:191], v[72:75]\n\t" "v_lshlrev_b32 v113, 16, v113\n\t" "v_rcp_f32 v10, v113\n\t" \
   "s_waitcnt lgkmcnt(5)\n\t" "v_mfma_f32_16x16x32_bf16 v[56:59], a[0:3], v[96:99], 0\n\t" \
   "buffer_load_dwordx4 v16, %[rs], s51 offen offset:0 sc1 lds\n\t" "v_mfma_f32_16x16x32_bf16 v[60:63], a[4:7], v[96:99], 0\n\t" \
   "ds_read_b128 v[92:95], v26 offset:27648\n\t" "v_mfma_f32_16x16x32_bf16 v[76:79], v[180:183], a[188:191], v[76:79]\n\t" \
   "v_add_f32 v122, v122, v124\n\t" "v_mfma_f32_16x16x32_bf16 v[56:59], a[8:11], v[100:103], v[56:59]\n\t" "v_add_f32 v123, v123, v125\n\t" \
   "v_add_f32 v126, v126, v128\n\t" "v_mfma_f32_16x16x32_bf16 v[60:63], a[12:15], v[100:103], v[60:63]\n\t" \
   "ds_read_b128 v[96:99], v26 offset:28672\n\t" "s_waitcnt lgkmcnt(5)\n\t" "v_mfma_f32_16x16x32_bf16 v[56:59], a[16:19], v[104:107], v[56:59]\n\t" \
   "v_cvt_pk_bf16_f32 v148, v72, v73\n\t" "v_cvt_pk_bf16_f32 v150, v74, v75\n\t" "v_mfma_f32_16x16x32_bf16 v[64:67], v[152:155], a[192:195], 0\n\t" \
   "v_cvt_pk_bf16_f32 v149, v76, v77\n\t" "v_cvt_pk_bf16_f32 v151, v78, v79\n\t" \
   "v_mfma_f32_16x16x32_bf16 v[60:63], a[20:23], v[104:107], v[60:63]\n\t" "ds_read_b128 v[100:103], v26 offset:29696\n\t" \
   "v_mfma_f32_16x16x32_bf16 v[56:59], a[24:27], v[108:111], v[56:59]\n\t" "s_add_u32 s50, s49, 32768\n\t" "v_fmac_f32 v48, v32, v8\n\t" \
   "v_mfma_f32_16x16x32_bf16 v[68:71], v[168:171], a[192:195], 0\n\t" "buffer_store_dwordx4 v[148:151], v21, %[rs], s50 offen sc1\n\t" \
   "v_mfma_f32_16x16x32_bf16 v[60:63], a[28:31], v[108:111], v[60:63]\n\t" "ds_read_b128 v[104:107], v26 offset:30720\n\t" "s_waitcnt lgkmcnt(5)\n\t" \
   "v_mfma_f32_16x16x32_bf16 v[56:59], a[32:35], v[80:83], v[56:59]\n\t" "buffer_load_dwordx4 v16, %[rs], s51 offen offset:1024 sc1 lds\n\t" \
   "v_mfma_f32_16x16x32_bf16 v[64:67], v[156:159], a[196:199], v[64:67]\n\t" "v_add_f32 v127, v127, v129\n\t" "v_fmac_f32 v49, v33, v8\n\t" \
   "v_mfma_f32_16x16x32_bf16 v[60:63], a[36:39], v[80:83], v[60:63]\n\t" "ds_read_b128 v[108:111], v26 offset:31744\n\t" \
   "v_mfma_f32_16x16x32_bf16 v[56:59], a[40:43], v[84:87], v[56:59]\n\t" "v_add_f32 v122, v122, v130\n\t" "v_fmac_f32 v50, v34, v8\n\t" \
   "v_mfma_f32_16x16x32_bf16 v[68:71], v[172:175], a[196:199], v[68:71]\n\t" "v_add_f32 v123, v123, v131\n\t" "v_fmac_f32 v51, v35, v8\n\t" \
   "v_mfma_f32_16x16x32_bf16 v[60:63], a[44:47], v[84:87], v[60:63]\n\t" "ds_read_b128 v[80:83], v26 offset:32768\n\t" "s_waitcnt lgkmcnt(5)\n\t" \
   "v_mfma_f32_16x16x32_bf16 v[56:59], a[48:51], v[88:91], v[56:59]\n\t" "v_add_f32 v122, v122, v126\n\t" "v_fmac_f32 v52, v36, v8\n\t" \
   "v_mfma_f32_16x16x32_bf16 v[64:67], v[160:163], a[200:203], v[64:67]\n\t" "v_add_f32 v123, v123, v127\n\t" "v_fmac_f32 v53, v37, v8\n\t" \
   "v_mfma_f32_16x16x32_bf16 v[60:63], a[52:55], v[88:91], v[60:63]\n\t" "ds_read_b128 v[84:87], v26 offset:33792\n\t" \
   "v_mfma_f32_16x16x32_bf16 v[56:59], a[56:59], v[92:95], v[56:59]\n\t" "s_cmp_lt_i32 s40, 2\n\t" "s_cselect_b32 s47, 0x7ffffff0, 0\n\t" \
   "v_mfma_f32_16x16x32_bf16 v[68:71], v[176:179], a[200:203], v[68:71]\n\t" "v_or_b32 v30, s47, v20\n\t" "s_sub_i32 s47, s41, 2\n\t" \
   "buffer_load_dwordx4 v16, %[rs], s51 offen offset:2048 sc1 lds\n\t" "v_mfma_f32_16x16x32_bf16 v[60:63], a[60:63], v[92:95], v[60:63]\n\t" \
   "ds_read_b128 v[88:91], v26 offset:34816\n\t" "s_max_i32 s47, s47, 0\n\t" "s_waitcnt lgkmcnt(5)\n\t" \
   "v_mfma_f32_16x16x32_bf16 v[56:59], a[64:67], v[96:99], v[56:59]\n\t" "s_lshl_b32 s47, s47, 12\n\t" "s_waitcnt vmcnt(8)\n\t" \
   "v_mfma_f32_16x16x32_bf16 v[60:63], a[68:71], v[96:99], v[60:63]\n\t" "ds_read_b128 v[92:95], v26 offset:35840\n\t" \
   "buffer_store_dword v31, v30, %[rs], s47 offen sc1\n\t" "v_mfma_f32_16x16x32_bf16 v[64:67], v[164:167], a[204:207], v[64:67]\n\t" \
   "v_fmac_f32 v54, v38, v8\n\t" "v_mul_f32 v122, 0xbacccccd, v122\n\t" "v_mfma_f32_16x16x32_bf16 v[56:59], a[72:75], v[100:103], v[56:59]\n\t" \
   "v_mul_f32 v123, 0x3acccccd, v123\n\t" "v_fmac_f32 v55, v39, v8\n\t" "v_mfma_f32_16x16x32_bf16 v[60:63], a[76:79], v[100:103], v[60:63]\n\t" \
   "ds_read_b128 v[96:99], v26 offset:36864\n\t" "v_mfma_f32_16x16x32_bf16 v[68:71], v[180:183], a[204:207], v[68:71]\n\t" \
   "v_fma_f32 v123, -v122, v122, v123\n\t" "s_waitcnt lgkmcnt(5)\n\t" "v_mfma_f32_16x16x32_bf16 v[56:59], a[80:83], v[104:107], v[56:59]\n\t" \
   "v_mul_f32 v48, v48, v10\n\t" "v_max_f32 v123, 0, v123\n\t" "v_mfma_f32_16x16x32_bf16 v[60:63], a[84:87], v[104:107], v[60:63]\n\t" \
   "ds_read_b128 v[100:103], v26 offset:37888\n\t" "v_mfma_f32_16x16x32_bf16 v[72:75], v[152:155], a[208:211], 0\n\t" \
   "buffer_load_dwordx4 v16, %[rs], s51 offen offset:3072 sc1 lds\n\t" "s_add_u32 m0, m0, 4096\n\t" "s_add_u32 s51, s51, 4096\n\t" \
   "v_mfma_f32_16x16x32_bf16 v[56:59], a[88:91], v[108:111], v[56:59]\n\t" "v_cvt_pk_bf16_f32 v144, v64, v65\n\t" \
   "v_cvt_pk_bf16_f32 v146, v66, v67\n\t" "v_mfma_f32_16x16x32_bf16 v[60:63], a[92:95], v[108:111], v[60:63]\n\t" \
   "ds_read_b128 v[104:107], v26 offset:38912\n\t" "v_mfma_f32_16x16x32_bf16 v[76:79], v[168:171], a[208:211], 0\n\t" \
   "v_cvt_pk_bf16_f32 v145, v68, v69\n\t" "s_waitcnt lgkmcnt(5)\n\t" "v_mfma_f32_16x16x32_bf16 v[56:59], a[96:99], v[80:83], v[56:59]\n\t" \
   "v_cvt_pk_bf16_f32 v147, v70, v71\n\t" "s_add_u32 s50, s49, 65536\n\t" "v_mfma_f32_16x16x32_bf16 v[60:63], a[100:103], v[80:83], v[60:63]\n\t" \
   "ds_read_b128 v[108:111], v26 offset:39936\n\t" "v_mfma_f32_16x16x32_bf16 v[72:75], v[156:159], a[212:215], v[72:75]\n\t" \
   "buffer_store_dwordx4 v[144:147], v21, %[rs], s50 offen sc1\n\t" "v_mfma_f32_16x16x32_bf16 v[56:59], a[104:107], v[84:87], v[56:59]\n\t" \
   "v_mul_f32 v49, v49, v10\n\t" "v_mul_f32 v50, v50, v10\n\t" "v_mfma_f32_16x16x32_bf16 v[60:63], a[108:111], v[84:87], v[60:63]\n\t" \
   "v_cvt_pk_bf16_f32 v123, v123, v122\n\t" "s_waitcnt lgkmcnt(4)\n\t" "v_mfma_f32_16x16x32_bf16 v[56:59], a[112:115], v[88:91], v[56:59]\n\t" \
   "buffer_load_dwordx4 v16, %[rs], s51 offen offset:0 sc1 lds\n\t" "v_mfma_f32_16x16x32_bf16 v[76:79], v[172:175], a[212:215], v[76:79]\n\t" \
   "v_mul_f32 v51, v51, v10\n\t" "v_and_b32 v12, 0xffff0000, v123\n\t" "v_mfma_f32_16x16x32_bf16 v[60:63], a[116:119], v[88:91], v[60:63]\n\t" \
   "v_mul_f32 v52, v52, v10\n\t" "v_lshlrev_b32 v123, 16, v123\n\t" "v_mfma_f32_16x16x32_bf16 v[56:59], a[120:123], v[92:95], v[56:59]\n\t" \
   "v_mul_f32 v53, v53, v10\n\t" "v_add_f32 v123, 0x3727c5ac, v123\n\t" "v_mfma_f32_16x16x32_bf16 v[72:75], v[160:163], a[216:219], v[72:75]\n\t" \
   "v_mul_f32 v54, v54, v10\n\t" "v_cvt_pk_bf16_f32 v123, v123, v123\n\t" "v_mfma_f32_16x16x32_bf16 v[60:63], a[124:127], v[92:95], v[60:63]\n\t" \
   "v_mul_f32 v55, v55, v10\n\t" "s_waitcnt lgkmcnt(2)\n\t" "v_mfma_f32_16x16x32_bf16 v[56:59], a[128:131], v[96:99], v[56:59]\n\t" \
   "v_lshlrev_b32 v123, 16, v123\n\t" "v_cvt_pk_bf16_f32 v136, v48, v49\n\t" "v_mfma_f32_16x16x32_bf16 v[76:79], v[176:179], a[216:219], v[76:79]\n\t" \
   "v_sqrt_f32 v123, v123\n\t" "v_mfma_f32_16x16x32_bf16 v[60:63], a[132:135], v[96:99], v[60:63]\n\t" "v_cvt_pk_bf16_f32 v137, v50, v51\n\t" \
   "s_nop 0\n\t" "v_mfma_f32_16x16x32_bf16 v[56:59], a[136:139], v[100:103], v[56:59]\n\t" "v_cvt_pk_bf16_f32 v138, v52, v53\n\t" \
   "v_cvt_pk_bf16_f32 v123, v123, v123\n\t" "v_mfma_f32_16x16x32_bf16 v[72:75], v[164:167], a[220:223], v[72:75]\n\t" \
   "buffer_load_dwordx4 v16, %[rs], s51 offen offset:1024 sc1 lds\n\t" "s_add_u32 m0, s53, %[ld1]\n\t" "s_nop 0\n\t" \
   "v_mfma_f32_16x16x32_bf16 v[60:63], a[140:143], v[100:103], v[60:63]\n\t" "v_cvt_pk_bf16_f32 v139, v54, v55\n\t" "s_waitcnt lgkmcnt(0)\n\t" \
   "v_mfma_f32_16x16x32_bf16 v[56:59], a[144:147], v[104:107], v[56:59]\n\t" "v_lshlrev_b32 v123, 16, v123\n\t" \
   "v_mfma_f32_16x16x32_bf16 v[76:79], v[180:183], a[220:223], v[76:79]\n\t" "ds_write_b128 v14, v[136:139] offset:0\n\t" \
   "v_mfma_f32_16x16x32_bf16 v[60:63], a[148:151], v[104:107], v[60:63]\n\t" "v_rcp_f32 v132, v123\n\t" \
   "v_mfma_f32_16x16x32_bf16 v[56:59], a[152:155], v[108:111], v[56:59]\n\t" "buffer_load_dwordx4 v17, %[rs], s52 offen sc1 lds\n\t" \
   "v_mfma_f32_16x16x32_bf16 v[60:63], a[156:159], v[108:111], v[60:63]\n\t" "v_cvt_pk_bf16_f32 v148, v72, v73\n\t" \
   "v_cvt_pk_bf16_f32 v150, v74, v75\n\t" "v_mfma_f32_16x16x32_bf16 v[64:67], v[152:155], a[224:227], 0\n\t" "v_cvt_pk_bf16_f32 v149, v76, v77\n\t" \
   "v_cvt_pk_bf16_f32 v151, v78, v79\n\t" "v_mfma_f32_16x16x32_bf16 v[68:71], v[168:171], a[224:227], 0\n\t" "s_add_u32 s50, s49, 98304\n\t" \
   "v_mfma_f32_16x16x32_bf16 v[64:67], v[156:159], a[228:231], v[64:67]\n\t" "v_fmac_f32 v56, v32, v12\n\t" "v_fmac_f32 v57, v33, v12\n\t" \
   "v_mfma_f32_16x16x32_bf16 v[68:71], v[172:175], a[228:231], v[68:71]\n\t" "v_fmac_f32 v58, v34, v12\n\t" "v_fmac_f32 v59, v35, v12\n\t" \
   "v_mfma_f32_16x16x32_bf16 v[64:67], v[160:163], a[232:235], v[64:67]\n\t" "v_fmac_f32 v60, v36, v12\n\t" "v_fmac_f32 v61, v37, v12\n\t" \
   "v_mfma_f32_16x16x32_bf16 v[68:71], v[176:179], a[232:235], v[68:71]\n\t" "v_fmac_f32 v62, v38, v12\n\t" "v_fmac_f32 v63, v39, v12\n\t" \
   "buffer_store_dwordx4 v[148:151], v21, %[rs], s50 offen sc1\n\t" "v_mfma_f32_16x16x32_bf16 v[64:67], v[164:167], a[236:239], v[64:67]\n\t" \
   "v_mul_f32 v56, v56, v132\n\t" "v_mul_f32 v57, v57, v132\n\t" "v_mfma_f32_16x16x32_bf16 v[68:71], v[180:183], a[236:239], v[68:71]\n\t" \
   "v_mul_f32 v58, v58, v132\n\t" "v_mul_f32 v59, v59, v132\n\t" "v_mfma_f32_16x16x32_bf16 v[72:75], v[152:155], a[240:243], 0\n\t" \
   "v_mul_f32 v60, v60, v132\n\t" "v_mul_f32 v61, v61, v132\n\t" "v_mfma_f32_16x16x32_bf16 v[76:79], v[168:171], a[240:243], 0\n\t" \
   "v_mul_f32 v62, v62, v132\n\t" "v_mul_f32 v63, v63, v132\n\t" "v_mfma_f32_16x16x32_bf16 v[72:75], v[156:159], a[244:247], v[72:75]\n\t" \
   "v_cvt_pk_bf16_f32 v144, v64, v65\n\t" "v_cvt_pk_bf16_f32 v146, v66, v67\n\t" \
   "v_mfma_f32_16x16x32_bf16 v[76:79], v[172:175], a[244:247], v[76:79]\n\t" "v_cvt_pk_bf16_f32 v145, v68, v69\n\t" \
   "v_cvt_pk_bf16_f32 v140, v56, v57\n\t" "v_mfma_f32_16x16x32_bf16 v[72:75], v[160:163], a[248:251], v[72:75]\n\t" \
   "v_cvt_pk_bf16_f32 v147, v70, v71\n\t" "v_cvt_pk_bf16_f32 v141, v58, v59\n\t" \
   "v_mfma_f32_16x16x32_bf16 v[76:79], v[176:179], a[248:251], v[76:79]\n\t" "s_add_u32 s50, s49, 131072\n\t" "v_cvt_pk_bf16_f32 v142, v60, v61\n\t" \
   "v_mfma_f32_16x16x32_bf16 v[72:75], v[164:167], a[252:255], v[72:75]\n\t" "v_cvt_pk_bf16_f32 v143, v62, v63\n\t" \
   "v_mfma_f32_16x16x32_bf16 v[76:79], v[180:183], a[252:255], v[76:79]\n\t" "buffer_store_dwordx4 v[144:147], v21, %[rs], s50 offen sc1\n\t" \
   "v_mfma_f32_16x16x32_bf16 v[64:67], v[152:155], v[192:195], 0\n\t" "ds_write_b128 v14, v[140:143] offset:4096\n\t" \
   "v_mfma_f32_16x16x32_bf16 v[68:71], v[168:171], v[192:195], 0\n\t" "v_mfma_f32_16x16x32_bf16 v[64:67], v[156:159], v[196:199], v[64:67]\n\t" \
   "v_cvt_pk_bf16_f32 v148, v72, v73\n\t" "v_cvt_pk_bf16_f32 v150, v74, v75\n\t" \
   "v_mfma_f32_16x16x32_bf16 v[68:71], v[172:175], v[196:199], v[68:71]\n\t" "v_cvt_pk_bf16_f32 v149, v76, v77\n\t" \
   "v_cvt_pk_bf16_f32 v151, v78, v79\n\t" "s_waitcnt vmcnt(16) lgkmcnt(0)\n\t" "s_barrier\n\t" \
   "v_mfma_f32_16x16x32_bf16 v[64:67], v[160:163], v[200:203], v[64:67]\n\t" "s_add_u32 s45, s40, 3\n\t" "s_min_u32 s45, s45, 63\n\t" \
   "v_mfma_f32_16x16x32_bf16 v[68:71], v[176:179], v[200:203], v[68:71]\n\t" "s_add_u32 s50, s49, 163840\n\t" "s_mov_b32 s46, s42\n\t" \
   "v_mfma_f32_16x16x32_bf16 v[64:67], v[164:167], v[204:207], v[64:67]\n\t" "s_lshr_b32 s48, s45, 1\n\t" "s_bitcmp1_b32 s44, s48\n\t" \
   "s_cbranch_scc0 174f\n\t" "74:\n\t" "v_mfma_f32_16x16x32_bf16 v[68:71], v[180:183], v[204:207], v[68:71]\n\t" \
   "buffer_store_dwordx4 v[148:151], v21, %[rs], s50 offen sc1\n\t" "v_mfma_f32_16x16x32_bf16 v[72:75], v[152:155], v[208:211], 0\n\t" \
   "s_mul_i32 s52, s45, 1536\n\t" "s_mul_i32 s51, s45, 40960\n\t" "s_add_u32 m0, s46, %[ldx]\n\t" "s_mov_b32 s53, s46\n\t" \
   "v_mfma_f32_16x16x32_bf16 v[76:79], v[168:171], v[208:211], 0\n\t" "buffer_load_dwordx4 v16, %[rs], s51 offen offset:0 sc1 lds\n\t" \
   "v_mfma_f32_16x16x32_bf16 v[72:75], v[156:159], v[212:215], v[72:75]\n\t" "v_cvt_pk_bf16_f32 v144, v64, v65\n\t" \
   "v_cvt_pk_bf16_f32 v146, v66, v67\n\t" "v_mfma_f32_16x16x32_bf16 v[76:79], v[172:175], v[212:215], v[76:79]\n\t" \
   "v_cvt_pk_bf16_f32 v145, v68, v69\n\t" "v_cvt_pk_bf16_f32 v147, v70, v71\n\t" \
   "v_mfma_f32_16x16x32_bf16 v[72:75], v[160:163], v[216:219], v[72:75]\n\t" "ds_read_b128 v[80:83], v28 offset:0\n\t" \
   "v_mfma_f32_16x16x32_bf16 v[76:79], v[176:179], v[216:219], v[76:79]\n\t" "s_add_u32 s50, s49, 196608\n\t" "s_mov_b32 s42, s43\n\t" \
   "v_mfma_f32_16x16x32_bf16 v[72:75], v[164:167], v[220:223], v[72:75]\n\t" "ds_read_b128 v[84:87], v28 offset:1024\n\t" \
   "v_mfma_f32_16x16x32_bf16 v[76:79], v[180:183], v[220:223], v[76:79]\n\t" "buffer_store_dwordx4 v[144:147], v21, %[rs], s50 offen sc1\n\t" \
   "v_mfma_f32_16x16x32_bf16 v[64:67], v[152:155], v[224:227], 0\n\t" "ds_read_b128 v[88:91], v28 offset:2048\n\t" \
   "v_mfma_f32_16x16x32_bf16 v[68:71], v[168:171], v[224:227], 0\n\t" "s_add_u32 s43, s43, 44032\n\t" \
   "v_mfma_f32_16x16x32_bf16 v[64:67], v[156:159], v[228:231], v[64:67]\n\t" "v_cvt_pk_bf16_f32 v148, v72, v73\n\t" \
   "v_cvt_pk_bf16_f32 v150, v74, v75\n\t" "v_mfma_f32_16x16x32_bf16 v[68:71], v[172:175], v[228:231], v[68:71]\n\t" \
   "v_cvt_pk_bf16_f32 v149, v76, v77\n\t" "v_cvt_pk_bf16_f32 v151, v78, v79\n\t" \
   "v_mfma_f32_16x16x32_bf16 v[64:67], v[160:163], v[232:235], v[64:67]\n\t" "s_cmp_eq_u32 s43, 132096\n\t" "s_cselect_b32 s43, 0, s43\n\t" \
   "v_mfma_f32_16x16x32_bf16 v[68:71], v[176:179], v[232:235], v[68:71]\n\t" "ds_read_b128 v[92:95], v28 offset:3072\n\t" \
   "v_mfma_f32_16x16x32_bf16 v[64:67], v[164:167], v[236:239], v[64:67]\n\t" "s_add_u32 s50, s49, 229376\n\t" "s_add_u32 s41, s41, 1\n\t" \
   "v_mfma_f32_16x16x32_bf16 v[68:71], v[180:183], v[236:239], v[68:71]\n\t" "buffer_store_dwordx4 v[148:151], v21, %[rs], s50 offen sc1\n\t" \
   "v_mfma_f32_16x16x32_bf16 v[72:75], v[152:155], v[240:243], 0\n\t" "ds_read_b128 v[96:99], v28 offset:4096\n\t" \
   "v_mfma_f32_16x16x32_bf16 v[76:79], v[168:171], v[240:243], 0\n\t" "s_add_u32 s40, s40, 1\n\t" "v_xor_b32 v14, 0x2000, v14\n\t" \
   "v_mfma_f32_16x16x32_bf16 v[72:75], v[156:159], v[244:247], v[72:75]\n\t" "v_cvt_pk_bf16_f32 v144, v64, v65\n\t" \
   "v_cvt_pk_bf16_f32 v146, v66, v67\n\t" "v_mfma_f32_16x16x32_bf16 v[76:79], v[172:175], v[244:247], v[76:79]\n\t" \
   "v_cvt_pk_bf16_f32 v145, v68, v69\n\t" "v_cvt_pk_bf16_f32 v147, v70, v71\n\t" \
   "v_mfma_f32_16x16x32_bf16 v[72:75], v[160:163], v[248:251], v[72:75]\n\t" "ds_read_b128 v[100:103], v28 offset:5120\n\t" \
   "v_mfma_f32_16x16x32_bf16 v[76:79], v[176:179], v[248:251], v[76:79]\n\t" "s_add_u32 s50, s49, 262144\n\t" "v_xor_b32 v15, 0x2000, v15\n\t" \
   "v_mfma_f32_16x16x32_bf16 v[72:75], v[164:167], v[252:255], v[72:75]\n\t" "ds_read_b128 v[104:107], v28 offset:6144\n\t" \
   "buffer_store_dwordx4 v[144:147], v21, %[rs], s50 offen sc1\n\t" "v_mfma_f32_16x16x32_bf16 v[76:79], v[180:183], v[252:255], v[76:79]\n\t" \
   "v_mov_b32 v18, v21\n\t" "s_cmp_lt_u32 s40, 64\n\t" "s_cbranch_scc1 1b\n\t" "s_nop 7\n\t" "s_waitcnt lgkmcnt(0)\n\t" \
   "v_cvt_pk_bf16_f32 v148, v72, v73\n\t" "v_cvt_pk_bf16_f32 v150, v74, v75\n\t" "v_cvt_pk_bf16_f32 v149, v76, v77\n\t" \
   "v_cvt_pk_bf16_f32 v151, v78, v79\n\t" "s_add_u32 s50, s49, 294912\n\t" "buffer_store_dwordx4 v[148:151], v21, %[rs], s50 offen sc1\n\t" \
   "s_sub_i32 s49, s41, 1\n\t" "s_mul_i32 s49, s49, 1310720\n\t" "ds_read_b128 v[152:155], v15 offset:0\n\t" \
   "ds_read_b128 v[168:171], v15 offset:4096\n\t" "ds_read_b128 v[156:159], v15 offset:1024\n\t" "ds_read_b128 v[172:175], v15 offset:5120\n\t" \
   "ds_read_b128 v[160:163], v15 offset:2048\n\t" "ds_read_b128 v[176:179], v15 offset:6144\n\t" "ds_read_b128 v[164:167], v15 offset:3072\n\t" \
   "ds_read_b128 v[180:183], v15 offset:7168\n\t" "s_waitcnt lgkmcnt(4)\n\t" "v_mfma_f32_16x16x32_bf16 v[64:67], v[152:155], a[160:163], 0\n\t" \
   "v_mfma_f32_16x16x32_bf16 v[68:71], v[168:171], a[160:163], 0\n\t" "v_mfma_f32_16x16x32_bf16 v[64:67], v[156:159], a[164:167], v[64:67]\n\t" \
   "v_mfma_f32_16x16x32_bf16 v[68:71], v[172:175], a[164:167], v[68:71]\n\t" "s_waitcnt lgkmcnt(0)\n\t" \
   "v_mfma_f32_16x16x32_bf16 v[64:67], v[160:163], a[168:171], v[64:67]\n\t" "v_mfma_f32_16x16x32_bf16 v[68:71], v[176:179], a[168:171], v[68:71]\n\t" \
   "v_mfma_f32_16x16x32_bf16 v[64:67], v[164:167], a[172:175], v[64:67]\n\t" "v_mfma_f32_16x16x32_bf16 v[68:71], v[180:183], a[172:175], v[68:71]\n\t" \
   "v_mfma_f32_16x16x32_bf16 v[72:75], v[152:155], a[176:179], 0\n\t" "v_mfma_f32_16x16x32_bf16 v[76:79], v[168:171], a[176:179], 0\n\t" \
   "v_mfma_f32_16x16x32_bf16 v[72:75], v[156:159], a[180:183], v[72:75]\n\t" "v_cvt_pk_bf16_f32 v144, v64, v65\n\t" \
   "v_cvt_pk_bf16_f32 v146, v66, v67\n\t" "v_mfma_f32_16x16x32_bf16 v[76:79], v[172:175], a[180:183], v[76:79]\n\t" \
   "v_cvt_pk_bf16_f32 v145, v68, v69\n\t" "v_cvt_pk_bf16_f32 v147, v70, v71\n\t" \
   "v_mfma_f32_16x16x32_bf16 v[72:75], v[160:163], a[184:187], v[72:75]\n\t" "s_add_u32 s50, s49, 0\n\t" \
   "v_mfma_f32_16x16x32_bf16 v[76:79], v[176:179], a[184:187], v[76:79]\n\t" "buffer_store_dwordx4 v[144:147], v21, %[rs], s50 offen sc1\n\t" \
   "v_mfma_f32_16x16x32_bf16 v[72:75], v[164:167], a[188:191], v[72:75]\n\t" "v_mfma_f32_16x16x32_bf16 v[76:79], v[180:183], a[188:191], v[76:79]\n\t" \
   "v_mfma_f32_16x16x32_bf16 v[64:67], v[152:155], a[192:195], 0\n\t" "v_mfma_f32_16x16x32_bf16 v[68:71], v[168:171], a[192:195], 0\n\t" \
   "v_mfma_f32_16x16x32_bf16 v[64:67], v[156:159], a[196:199], v[64:67]\n\t" "v_cvt_pk_bf16_f32 v148, v72, v73\n\t" \
   "v_cvt_pk_bf16_f32 v150, v74, v75\n\t" "v_mfma_f32_16x16x32_bf16 v[68:71], v[172:175], a[196:199], v[68:71]\n\t" \
   "v_cvt_pk_bf16_f32 v149, v76, v77\n\t" "v_cvt_pk_bf16_f32 v151, v78, v79\n\t" \
   "v_mfma_f32_16x16x32_bf16 v[64:67], v[160:163], a[200:203], v[64:67]\n\t" "s_add_u32 s50, s49, 32768\n\t" \
   "v_mfma_f32_16x16x32_bf16 v[68:71], v[176:179], a[200:203], v[68:71]\n\t" "buffer_store_dwordx4 v[148:151], v21, %[rs], s50 offen sc1\n\t" \
   "v_mfma_f32_16x16x32_bf16 v[64:67], v[164:167], a[204:207], v[64:67]\n\t" "v_mfma_f32_16x16x32_bf16 v[68:71], v[180:183], a[204:207], v[68:71]\n\t" \
   "v_mfma_f32_16x16x32_bf16 v[72:75], v[152:155], a[208:211], 0\n\t" "v_mfma_f32_16x16x32_bf16 v[76:79], v[168:171], a[208:211], 0\n\t" \
   "v_mfma_f32_16x16x32_bf16 v[72:75], v[156:159], a[212:215], v[72:75]\n\t" "v_cvt_pk_bf16_f32 v144, v64, v65\n\t" \
   "v_cvt_pk_bf16_f32 v146, v66, v67\n\t" "v_mfma_f32_16x16x32_bf16 v[76:79], v[172:175], a[212:215], v[76:79]\n\t" \
   "v_cvt_pk_bf16_f32 v145, v68, v69\n\t" "v_cvt_pk_bf16_f32 v147, v70, v71\n\t" \
   "v_mfma_f32_16x16x32_bf16 v[72:75], v[160:163], a[216:219], v[72:75]\n\t" "s_add_u32 s50, s49, 65536\n\t" \
   "v_mfma_f32_16x16x32_bf16 v[76:79], v[176:179], a[216:219], v[76:79]\n\t" "buffer_store_dwordx4 v[144:147], v21, %[rs], s50 offen sc1\n\t" \
   "v_mfma_f32_16x16x32_bf16 v[72:75], v[164:167], a[220:223], v[72:75]\n\t" "v_mfma_f32_16x16x32_bf16 v[76:79], v[180:183], a[220:223], v[76:79]\n\t" \
   "v_mfma_f32_16x16x32_bf16 v[64:67], v[152:155], a[224:227], 0\n\t" "v_mfma_f32_16x16x32_bf16 v[68:71], v[168:171], a[224:227], 0\n\t" \
   "v_mfma_f32_16x16x32_bf16 v[64:67], v[156:159], a[228:231], v[64:67]\n\t" "v_cvt_pk_bf16_f32 v148, v72, v73\n\t" \
   "v_cvt_pk_bf16_f32 v150, v74, v75\n\t" "v_mfma_f32_16x16x32_bf16 v[68:71], v[172:175], a[228:231], v[68:71]\n\t" \
   "v_cvt_pk_bf16_f32 v149, v76, v77\n\t" "v_cvt_pk_bf16_f32 v151, v78, v79\n\t" \
   "v_mfma_f32_16x16x32_bf16 v[64:67], v[160:163], a[232:235], v[64:67]\n\t" "s_add_u32 s50, s49, 98304\n\t" \
   "v_mfma_f32_16x16x32_bf16 v[68:71], v[176:179], a[232:235], v[68:71]\n\t" "buffer_store_dwordx4 v[148:151], v21, %[rs], s50 offen sc1\n\t" \
   "v_mfma_f32_16x16x32_bf16 v[64:67], v[164:167], a[236:239], v[64:67]\n\t" "v_mfma_f32_16x16x32_bf16 v[68:71], v[180:183], a[236:239], v[68:71]\n\t" \
   "v_mfma_f32_16x16x32_bf16 v[72:75], v[152:155], a[240:243], 0\n\t" "v_mfma_f32_16x16x32_bf16 v[76:79], v[168:171], a[240:243], 0\n\t" \
   "v_mfma_f32_16x16x32_bf16 v[72:75], v[156:159], a[244:247], v[72:75]\n\t" "v_cvt_pk_bf16_f32 v144, v64, v65\n\t" \
   "v_cvt_pk_bf16_f32 v146, v66, v67\n\t" "v_mfma_f32_16x16x32_bf16 v[76:79], v[172:175], a[244:247], v[76:79]\n\t" \
   "v_cvt_pk_bf16_f32 v145, v68, v69\n\t" "v_cvt_pk_bf16_f32 v147, v70, v71\n\t" \
   "v_mfma_f32_16x16x32_bf16 v[72:75], v[160:163], a[248:251], v[72:75]\n\t" "s_add_u32 s50, s49, 131072\n\t" \
   "v_mfma_f32_16x16x32_bf16 v[76:79], v[176:179], a[248:251], v[76:79]\n\t" "buffer_store_dwordx4 v[144:147], v21, %[rs], s50 offen sc1\n\t" \
   "v_mfma_f32_16x16x32_bf16 v[72:75], v[164:167], a[252:255], v[72:75]\n\t" "v_mfma_f32_16x16x32_bf16 v[76:79], v[180:183], a[252:255], v[76:79]\n\t" \
   "v_mfma_f32_16x16x32_bf16 v[64:67], v[152:155], v[192:195], 0\n\t" "v_mfma_f32_16x16x32_bf16 v[68:71], v[168:171], v[192:195], 0\n\t" \
   "v_mfma_f32_16x16x32_bf16 v[64:67], v[156:159], v[196:199], v[64:67]\n\t" "v_cvt_pk_bf16_f32 v148, v72, v73\n\t" \
   "v_cvt_pk_bf16_f32 v150, v74, v75\n\t" "v_mfma_f32_16x16x32_bf16 v[68:71], v[172:175], v[196:199], v[68:71]\n\t" \
   "v_cvt_pk_bf16_f32 v149, v76, v77\n\t" "v_cvt_pk_bf16_f32 v151, v78, v79\n\t" \
   "v_mfma_f32_16x16x32_bf16 v[64:67], v[160:163], v[200:203], v[64:67]\n\t" "s_add_u32 s50, s49, 163840\n\t" \
   "v_mfma_f32_16x16x32_bf16 v[68:71], v[176:179], v[200:203], v[68:71]\n\t" "buffer_store_dwordx4 v[148:151], v21, %[rs], s50 offen sc1\n\t" \
   "v_mfma_f32_16x16x32_bf16 v[64:67], v[164:167], v[204:207], v[64:67]\n\t" "v_mfma_f32_16x16x32_bf16 v[68:71], v[180:183], v[204:207], v[68:71]\n\t" \
   "v_mfma_f32_16x16x32_bf16 v[72:75], v[152:155], v[208:211], 0\n\t" "v_mfma_f32_16x16x32_bf16 v[76:79], v[168:171], v[208:211], 0\n\t" \
   "v_mfma_f32_16x16x32_bf16 v[72:75], v[156:159], v[212:215], v[72:75]\n\t" "v_cvt_pk_bf16_f32 v144, v64, v65\n\t" \
   "v_cvt_pk_bf16_f32 v146, v66, v67\n\t" "v_mfma_f32_16x16x32_bf16 v[76:79], v[172:175], v[212:215], v[76:79]\n\t" \
   "v_cvt_pk_bf16_f32 v145, v68, v69\n\t" "v_cvt_pk_bf16_f32 v147, v70, v71\n\t" \
   "v_mfma_f32_16x16x32_bf16 v[72:75], v[160:163], v[216:219], v[72:75]\n\t" "s_add_u32 s50, s49, 196608\n\t" \
   "v_mfma_f32_16x16x32_bf16 v[76:79], v[176:179], v[216:219], v[76:79]\n\t" "buffer_store_dwordx4 v[144:147], v21, %[rs], s50 offen sc1\n\t" \
   "v_mfma_f32_16x16x32_bf16 v[72:75], v[164:167], v[220:223], v[72:75]\n\t" "v_mfma_f32_16x16x32_bf16 v[76:79], v[180:183], v[220:223], v[76:79]\n\t" \
   "v_mfma_f32_16x16x32_bf16 v[64:67], v[152:155], v[224:227], 0\n\t" "v_mfma_f32_16x16x32_bf16 v[68:71], v[168:171], v[224:227], 0\n\t" \
   "v_mfma_f32_16x16x32_bf16 v[64:67], v[156:159], v[228:231], v[64:67]\n\t" "v_cvt_pk_bf16_f32 v148, v72, v73\n\t" \
   "v_cvt_pk_bf16_f32 v150, v74, v75\n\t" "v_mfma_f32_16x16x32_bf16 v[68:71], v[172:175], v[228:231], v[68:71]\n\t" \
   "v_cvt_pk_bf16_f32 v149, v76, v77\n\t" "v_cvt_pk_bf16_f32 v151, v78, v79\n\t" \
   "v_mfma_f32_16x16x32_bf16 v[64:67], v[160:163], v[232:235], v[64:67]\n\t" "s_add_u32 s50, s49, 229376\n\t" \
   "v_mfma_f32_16x16x32_bf16 v[68:71], v[176:179], v[232:235], v[68:71]\n\t" "buffer_store_dwordx4 v[148:151], v21, %[rs], s50 offen sc1\n\t" \
   "v_mfma_f32_16x16x32_bf16 v[64:67], v[164:167], v[236:239], v[64:67]\n\t" "v_mfma_f32_16x16x32_bf16 v[68:71], v[180:183], v[236:239], v[68:71]\n\t" \
   "v_mfma_f32_16x16x32_bf16 v[72:75], v[152:155], v[240:243], 0\n\t" "v_mfma_f32_16x16x32_bf16 v[76:79], v[168:171], v[240:243], 0\n\t" \
   "v_mfma_f32_16x16x32_bf16 v[72:75], v[156:159], v[244:247], v[72:75]\n\t" "v_cvt_pk_bf16_f32 v144, v64, v65\n\t" \
   "v_cvt_pk_bf16_f32 v146, v66, v67\n\t" "v_mfma_f32_16x16x32_bf16 v[76:79], v[172:175], v[244:247], v[76:79]\n\t" \
   "v_cvt_pk_bf16_f32 v145, v68, v69\n\t" "v_cvt_pk_bf16_f32 v147, v70, v71\n\t" \
   "v_mfma_f32_16x16x32_bf16 v[72:75], v[160:163], v[248:251], v[72:75]\n\t" "s_add_u32 s50, s49, 262144\n\t" \
   "v_mfma_f32_16x16x32_bf16 v[76:79], v[176:179], v[248:251], v[76:79]\n\t" "buffer_store_dwordx4 v[144:147], v21, %[rs], s50 offen sc1\n\t" \
   "v_mfma_f32_16x16x32_bf16 v[72:75], v[164:167], v[252:255], v[72:75]\n\t" "v_mfma_f32_16x16x32_bf16 v[76:79], v[180:183], v[252:255], v[76:79]\n\t" \
   "s_nop 7\n\t" "v_cvt_pk_bf16_f32 v148, v72, v73\n\t" "v_cvt_pk_bf16_f32 v150, v74, v75\n\t" "v_cvt_pk_bf16_f32 v149, v76, v77\n\t" \
   "v_cvt_pk_bf16_f32 v151, v78, v79\n\t" "s_add_u32 s50, s49, 294912\n\t" "buffer_store_dwordx4 v[148:151], v21, %[rs], s50 offen sc1\n\t" \
   "s_waitcnt vmcnt(0)\n\t" "s_cmp_lt_i32 s40, 2\n\t" "s_cselect_b32 s47, 0x7ffffff0, 0\n\t" "v_or_b32 v30, s47, v20\n\t" "s_sub_i32 s47, s41, 2\n\t" \
   "s_max_i32 s47, s47, 0\n\t" "s_lshl_b32 s47, s47, 12\n\t" "buffer_store_dword v31, v30, %[rs], s47 offen sc1\n\t" "s_cmp_lt_i32 s40, 1\n\t" \
   "s_cselect_b32 s47, 0x7ffffff0, 0\n\t" "v_or_b32 v30, s47, v20\n\t" "s_sub_i32 s47, s41, 1\n\t" "s_max_i32 s47, s47, 0\n\t" \
   "s_lshl_b32 s47, s47, 12\n\t" "buffer_store_dword v31, v30, %[rs], s47 offen sc1\n\t" "s_branch 9f\n\t" "172:\n\t" "s_mov_b32 s47, 0\n\t" \
   "buffer_load_dword v30, v19, %[rs], 0 offen sc1\n\t" "s_waitcnt vmcnt(0)\n\t" "v_add_u32 v30, 1, v30\n\t" "v_cmp_lt_u32 vcc, 9, v30\n\t" \
   "s_mov_b32 s44, vcc_lo\n\t" "s_bitcmp1_b32 s44, s48\n\t" "s_cbranch_scc1 72b\n\t" "s_bitset1_b32 s44, s48\n\t" "s_lshl_b32 s48, s48, 7\n\t" \
   "s_add_u32 s48, s48, %[cnt]\n\t" "73:\n\t" "s_sleep 10\n\t" "buffer_load_dword v30, off, %[rs], s48 sc1\n\t" "s_add_u32 s47, s47, 1\n\t" \
   "s_waitcnt vmcnt(0)\n\t" "v_add_u32 v30, 1, v30\n\t" "v_cmp_lt_u32 vcc, 9, v30\n\t" "s_cmp_gt_u32 s47, 0x20000\n\t" "s_cbranch_scc1 72b\n\t" \
   "s_cbranch_vccz 73b\n\t" "s_branch 72b\n\t" "176:\n\t" "s_mov_b32 s47, 0\n\t" "buffer_load_dword v30, v19, %[rs], 0 offen sc1\n\t" \
   "s_waitcnt vmcnt(0)\n\t" "v_add_u32 v30, 1, v30\n\t" "v_cmp_lt_u32 vcc, 9, v30\n\t" "s_mov_b32 s44, vcc_lo\n\t" "s_bitcmp1_b32 s44, s48\n\t" \
   "s_cbranch_scc1 76b\n\t" "s_bitset1_b32 s44, s48\n\t" "s_lshl_b32 s48, s48, 7\n\t" "s_add_u32 s48, s48, %[cnt]\n\t" "77:\n\t" "s_sleep 10\n\t" \
   "buffer_load_dword v30, off, %[rs], s48 sc1\n\t" "s_add_u32 s47, s47, 1\n\t" "s_waitcnt vmcnt(0)\n\t" "v_add_u32 v30, 1, v30\n\t" \
   "v_cmp_lt_u32 vcc, 9, v30\n\t" "s_cmp_gt_u32 s47, 0x20000\n\t" "s_cbranch_scc1 76b\n\t" "s_cbranch_vccz 77b\n\t" "s_branch 76b\n\t" "174:\n\t" \
   "s_mov_b32 s47, 0\n\t" "buffer_load_dword v30, v19, %[rs], 0 offen sc1\n\t" "s_waitcnt vmcnt(0)\n\t" "v_add_u32 v30, 1, v30\n\t" \
   "v_cmp_lt_u32 vcc, 9, v30\n\t" "s_mov_b32 s44, vcc_lo\n\t" "s_bitcmp1_b32 s44, s48\n\t" "s_cbranch_scc1 74b\n\t" "s_bitset1_b32 s44, s48\n\t" \
   "s_lshl_b32 s48, s48, 7\n\t" "s_add_u32 s48, s48, %[cnt]\n\t" "75:\n\t" "s_sleep 10\n\t" "buffer_load_dword v30, off, %[rs], s48 sc1\n\t" \
   "s_add_u32 s47, s47, 1\n\t" "s_waitcnt vmcnt(0)\n\t" "v_add_u32 v30, 1, v30\n\t" "v_cmp_lt_u32 vcc, 9, v30\n\t" "s_cmp_gt_u32 s47, 0x20000\n\t" \
   "s_cbranch_scc1 74b\n\t" "s_cbranch_vccz 75b\n\t" "s_branch 74b\n\t" "9:\n\t" "s_waitcnt vmcnt(0)"
#define VU_CLOB "v8", "v9", "v10", "v11", "v12", "v13", "v14", "v15", "v32", "v33", "v34", "v35", "v36", "v37", "v38", "v39", "v40", "v41", "v42", "v43", "v44", "v45", "v46", "v47", "v48", "v49", "v50", "v51", "v52", "v53", "v54", "v55", "v56", "v57", "v58", "v59", "v60", "v61", "v62", "v63", "v64", "v65", "v66", "v67", "v68", "v69", "v70", "v71", "v72", "v73", "v74", "v75", "v76", "v77", "v78", "v79", "v80", "v81", "v82", "v83", "v84", "v85", "v86", "v87", "v88", "v89", "v90", "v91", "v92", "v93", "v94", "v95", "v96", "v97", "v98", "v99", "v100", "v101", "v102", "v103", "v104", "v105", "v106", "v107", "v108", "v109", "v110", "v111", "v112", "v113", "v114", "v115", "v116", "v117", "v118", "v119", "v120", "v121", "v122", "v123", "v124", "v125", "v126", "v127", "v128", "v129", "v130", "v131", "v132", "v133", "v134", "v135", "v136", "v137", "v138", "v139", "v140", "v141", "v142", "v143", "v144", "v145", "v146", "v147", "v148", "v149", "v150", "v151", "v152", "v153", "v154", "v155", "v156", "v157", "v158", "v159", "v160", "v161", "v162", "v163", "v164", "v165", "v166", "v167", "v168", "v169", "v170", "v171", "v172", "v173", "v174", "v175", "v176", "v177", "v178", "v179", "v180", "v181", "v182", "v183", "s40", "s41", "s42", "s43", "s44", "s45", "s46", "s47", "s48", "s49", "s50", "s51", "s52", "s53", "s54", "s55"
#define DF_ASM \
   "v_mov_b32 v80, 0x3f803f80\n\t" "v_mov_b32 v81, 0x3f803f80\n\t" "v_mov_b32 v82, 0x3f803f80\n\t" "v_mov_b32 v83, 0x3f803f80\n\t" \
   "s_mov_b32 s45, %[set]\n\t" "s_mov_b32 s46, 0\n\t" "v_mfma_f32_16x16x32_bf16 v[32:35], a[0:3], v[80:83], 0\n\t" \
   "v_mfma_f32_16x16x32_bf16 v[32:35], a[4:7], v[80:83], v[32:35]\n\t" "v_mfma_f32_16x16x32_bf16 v[32:35], a[8:11], v[80:83], v[32:35]\n\t" \
   "v_mfma_f32_16x16x32_bf16 v[32:35], a[12:15], v[80:83], v[32:35]\n\t" "v_mfma_f32_16x16x32_bf16 v[32:35], a[16:19], v[80:83], v[32:35]\n\t" \
   "v_mfma_f32_16x16x32_bf16 v[32:35], a[20:23], v[80:83], v[32:35]\n\t" "v_mfma_f32_16x16x32_bf16 v[32:35], a[24:27], v[80:83], v[32:35]\n\t" \
   "v_mfma_f32_16x16x32_bf16 v[32:35], a[28:31], v[80:83], v[32:35]\n\t" "v_mfma_f32_16x16x32_bf16 v[32:35], a[32:35], v[80:83], v[32:35]\n\t" \
   "v_mfma_f32_16x16x32_bf16 v[32:35], a[36:39], v[80:83], v[32:35]\n\t" "v_mfma_f32_16x16x32_bf16 v[32:35], a[40:43], v[80:83], v[32:35]\n\t" \
   "v_mfma_f32_16x16x32_bf16 v[32:35], a[44:47], v[80:83], v[32:35]\n\t" "v_mfma_f32_16x16x32_bf16 v[32:35], a[48:51], v[80:83], v[32:35]\n\t" \
   "v_mfma_f32_16x16x32_bf16 v[32:35], a[52:55], v[80:83], v[32:35]\n\t" "v_mfma_f32_16x16x32_bf16 v[32:35], a[56:59], v[80:83], v[32:35]\n\t" \
   "v_mfma_f32_16x16x32_bf16 v[32:35], a[60:63], v[80:83], v[32:35]\n\t" "v_mfma_f32_16x16x32_bf16 v[32:35], a[64:67], v[80:83], v[32:35]\n\t" \
   "v_mfma_f32_16x16x32_bf16 v[32:35], a[68:71], v[80:83], v[32:35]\n\t" "v_mfma_f32_16x16x32_bf16 v[32:35], a[72:75], v[80:83], v[32:35]\n\t" \
   "v_mfma_f32_16x16x32_bf16 v[32:35], a[76:79], v[80:83], v[32:35]\n\t" "v_mfma_f32_16x16x32_bf16 v[36:39], a[80:83], v[80:83], 0\n\t" \
   "v_mfma_f32_16x16x32_bf16 v[36:39], a[84:87], v[80:83], v[36:39]\n\t" "v_mfma_f32_16x16x32_bf16 v[36:39], a[88:91], v[80:83], v[36:39]\n\t" \
   "v_mfma_f32_16x16x32_bf16 v[36:39], a[92:95], v[80:83], v[36:39]\n\t" "v_mfma_f32_16x16x32_bf16 v[36:39], a[96:99], v[80:83], v[36:39]\n\t" \
   "v_mfma_f32_16x16x32_bf16 v[36:39], a[100:103], v[80:83], v[36:39]\n\t" "v_mfma_f32_16x16x32_bf16 v[36:39], a[104:107], v[80:83], v[36:39]\n\t" \
   "v_mfma_f32_16x16x32_bf16 v[36:39], a[108:111], v[80:83], v[36:39]\n\t" "v_mfma_f32_16x16x32_bf16 v[36:39], a[112:115], v[80:83], v[36:39]\n\t" \
   "v_mfma_f32_16x16x32_bf16 v[36:39], a[116:119], v[80:83], v[36:39]\n\t" "v_mfma_f32_16x16x32_bf16 v[36:39], a[120:123], v[80:83], v[36:39]\n\t" \
   "v_mfma_f32_16x16x32_bf16 v[36:39], a[124:127], v[80:83], v[36:39]\n\t" "v_mfma_f32_16x16x32_bf16 v[36:39], a[128:131], v[80:83], v[36:39]\n\t" \
   "v_mfma_f32_16x16x32_bf16 v[36:39], a[132:135], v[80:83], v[36:39]\n\t" "v_mfma_f32_16x16x32_bf16 v[36:39], a[136:139], v[80:83], v[36:39]\n\t" \
   "v_mfma_f32_16x16x32_bf16 v[36:39], a[140:143], v[80:83], v[36:39]\n\t" "v_mfma_f32_16x16x32_bf16 v[36:39], a[144:147], v[80:83], v[36:39]\n\t" \
   "v_mfma_f32_16x16x32_bf16 v[36:39], a[148:151], v[80:83], v[36:39]\n\t" "v_mfma_f32_16x16x32_bf16 v[36:39], a[152:155], v[80:83], v[36:39]\n\t" \
   "v_mfma_f32_16x16x32_bf16 v[36:39], a[156:159], v[80:83], v[36:39]\n\t" "1:\n\t" "s_lshl_b32 s48, s45, 7\n\t" "s_mov_b32 s47, 0\n\t" \
   "s_cmp_lg_u32 %[ldx], %[ld0]\n\t" "s_cbranch_scc1 3f\n\t" "2:\n\t" "buffer_load_dword v30, v19, %[rs], s48 offen sc1\n\t" \
   "s_add_u32 s47, s47, 1\n\t" "s_waitcnt vmcnt(0)\n\t" "v_readfirstlane_b32 s44, v30\n\t" "s_add_u32 s44, s44, 1\n\t" "s_cmp_ge_u32 s44, 5\n\t" \
   "s_cbranch_scc1 3f\n\t" "s_cmp_gt_u32 s47, 0x20000\n\t" "s_cbranch_scc1 3f\n\t" "s_sleep 3\n\t" "s_cmp_lg_u32 s44, 0\n\t" "s_cbranch_scc1 2b\n\t" \
   "s_sleep 8\n\t" "s_branch 2b\n\t" "3:\n\t" "s_barrier\n\t" "s_mul_i32 s52, s45, 1536\n\t" "s_mul_i32 s51, s45, 40960\n\t" \
   "s_add_u32 m0, s46, %[ldx]\n\t" "s_mov_b32 s53, s46\n\t" "buffer_load_dwordx4 v16, %[rs], s51 offen offset:0 sc1 lds\n\t" \
   "buffer_load_dwordx4 v16, %[rs], s51 offen offset:1024 sc1 lds\n\t" "buffer_load_dwordx4 v16, %[rs], s51 offen offset:2048 sc1 lds\n\t" \
   "buffer_load_dwordx4 v16, %[rs], s51 offen offset:3072 sc1 lds\n\t" "s_add_u32 m0, m0, 4096\n\t" "s_add_u32 s51, s51, 4096\n\t" \
   "buffer_load_dwordx4 v16, %[rs], s51 offen offset:0 sc1 lds\n\t" "buffer_load_dwordx4 v16, %[rs], s51 offen offset:1024 sc1 lds\n\t" \
   "buffer_load_dwordx4 v16, %[rs], s51 offen offset:2048 sc1 lds\n\t" "buffer_load_dwordx4 v16, %[rs], s51 offen offset:3072 sc1 lds\n\t" \
   "s_add_u32 m0, m0, 4096\n\t" "s_add_u32 s51, s51, 4096\n\t" "buffer_load_dwordx4 v16, %[rs], s51 offen offset:0 sc1 lds\n\t" \
   "buffer_load_dwordx4 v16, %[rs], s51 offen offset:1024 sc1 lds\n\t" "s_add_u32 m0, s53, %[ld1]\n\t" "s_nop 0\n\t" \
   "buffer_load_dwordx4 v17, %[rs], s52 offen sc1 lds\n\t" "v_add_u32 v26, s46, v22\n\t" "v_add_u32 v27, s46, v23\n\t" "s_waitcnt vmcnt(0)\n\t" \
   "s_barrier\n\t" "ds_read_b128 v[80:83], v26 offset:0\n\t" "ds_read_b128 v[84:87], v26 offset:1024\n\t" "ds_read_b128 v[88:91], v26 offset:2048\n\t" \
   "ds_read_b128 v[92:95], v26 offset:3072\n\t" "ds_read_b128 v[96:99], v26 offset:4096\n\t" "ds_read_b128 v[100:103], v26 offset:5120\n\t" \
   "ds_read_b128 v[104:107], v26 offset:6144\n\t" "s_waitcnt lgkmcnt(5)\n\t" "v_mfma_f32_16x16x32_bf16 v[52:55], a[80:83], v[80:83], 0\n\t" \
   "ds_read_b128 v[112:115], v27 offset:0\n\t" "v_mfma_f32_16x16x32_bf16 v[48:51], a[0:3], v[80:83], 0\n\t" \
   "ds_read_b128 v[108:111], v26 offset:7168\n\t" "ds_read_b128 v[116:119], v27 offset:16\n\t" \
   "v_mfma_f32_16x16x32_bf16 v[52:55], a[84:87], v[84:87], v[52:55]\n\t" "ds_read_b64 v[120:121], v27 offset:32\n\t" \
   "v_mfma_f32_16x16x32_bf16 v[48:51], a[4:7], v[84:87], v[48:51]\n\t" "ds_read_b128 v[80:83], v26 offset:8192\n\t" "s_waitcnt lgkmcnt(8)\n\t" \
   "v_mfma_f32_16x16x32_bf16 v[52:55], a[88:91], v[88:91], v[52:55]\n\t" "ds_read_b128 v[122:125], v27 offset:768\n\t" \
   "v_mfma_f32_16x16x32_bf16 v[48:51], a[8:11], v[88:91], v[48:51]\n\t" "ds_read_b128 v[84:87], v26 offset:9216\n\t" \
   "ds_read_b128 v[126:129], v27 offset:784\n\t" "v_mfma_f32_16x16x32_bf16 v[52:55], a[92:95], v[92:95], v[52:55]\n\t" \
   "v_mfma_f32_16x16x32_bf16 v[48:51], a[12:15], v[92:95], v[48:51]\n\t" "ds_read_b128 v[88:91], v26 offset:10240\n\t" \
   "ds_read_b64 v[130:131], v27 offset:800\n\t" "s_waitcnt lgkmcnt(11)\n\t" "v_mfma_f32_16x16x32_bf16 v[52:55], a[96:99], v[96:99], v[52:55]\n\t" \
   "s_lshl_b32 s48, s45, 9\n\t" "v_mfma_f32_16x16x32_bf16 v[48:51], a[16:19], v[96:99], v[48:51]\n\t" "ds_read_b128 v[92:95], v26 offset:11264\n\t" \
   "s_add_u32 s47, s48, 131072\n\t" "v_mfma_f32_16x16x32_bf16 v[52:55], a[100:103], v[100:103], v[52:55]\n\t" \
   "v_mfma_f32_16x16x32_bf16 v[48:51], a[20:23], v[100:103], v[48:51]\n\t" "ds_read_b128 v[96:99], v26 offset:12288\n\t" "s_waitcnt lgkmcnt(8)\n\t" \
   "v_add_f32 v112, v112, v114\n\t" "v_mfma_f32_16x16x32_bf16 v[52:55], a[104:107], v[104:107], v[52:55]\n\t" "v_add_f32 v113, v113, v115\n\t" \
   "v_mfma_f32_16x16x32_bf16 v[48:51], a[24:27], v[104:107], v[48:51]\n\t" "ds_read_b128 v[100:103], v26 offset:13312\n\t" \
   "v_mfma_f32_16x16x32_bf16 v[52:55], a[108:111], v[108:111], v[52:55]\n\t" "v_add_f32 v116, v116, v118\n\t" \
   "v_mfma_f32_16x16x32_bf16 v[48:51], a[28:31], v[108:111], v[48:51]\n\t" "ds_read_b128 v[104:107], v26 offset:14336\n\t" \
   "v_add_f32 v117, v117, v119\n\t" "s_waitcnt lgkmcnt(7)\n\t" "v_mfma_f32_16x16x32_bf16 v[52:55], a[112:115], v[80:83], v[52:55]\n\t" \
   "v_mfma_f32_16x16x32_bf16 v[48:51], a[32:35], v[80:83], v[48:51]\n\t" "ds_read_b128 v[108:111], v26 offset:15360\n\t" \
   "v_add_f32 v112, v112, v120\n\t" "v_mfma_f32_16x16x32_bf16 v[52:55], a[116:119], v[84:87], v[52:55]\n\t" "v_add_f32 v113, v113, v121\n\t" \
   "v_mfma_f32_16x16x32_bf16 v[48:51], a[36:39], v[84:87], v[48:51]\n\t" "ds_read_b128 v[80:83], v26 offset:16384\n\t" "v_add_f32 v112, v112, v116\n\t" \
   "s_waitcnt lgkmcnt(5)\n\t" "v_mfma_f32_16x16x32_bf16 v[52:55], a[120:123], v[88:91], v[52:55]\n\t" \
   "v_mfma_f32_16x16x32_bf16 v[48:51], a[40:43], v[88:91], v[48:51]\n\t" "ds_read_b128 v[84:87], v26 offset:17408\n\t" "v_add_f32 v113, v113, v117\n\t" \
   "v_mfma_f32_16x16x32_bf16 v[52:55], a[124:127], v[92:95], v[52:55]\n\t" "v_mul_f32 v112, 0xbacccccd, v112\n\t" \
   "v_mfma_f32_16x16x32_bf16 v[48:51], a[44:47], v[92:95], v[48:51]\n\t" "ds_read_b128 v[88:91], v26 offset:18432\n\t" "s_waitcnt lgkmcnt(5)\n\t" \
   "v_mfma_f32_16x16x32_bf16 v[52:55], a[128:131], v[96:99], v[52:55]\n\t" "v_mul_f32 v113, 0x3acccccd, v113\n\t" \
   "v_mfma_f32_16x16x32_bf16 v[48:51], a[48:51], v[96:99], v[48:51]\n\t" "ds_read_b128 v[92:95], v26 offset:19456\n\t" \
   "v_fma_f32 v113, -v112, v112, v113\n\t" "v_mfma_f32_16x16x32_bf16 v[52:55], a[132:135], v[100:103], v[52:55]\n\t" "v_max_f32 v113, 0, v113\n\t" \
   "v_mfma_f32_16x16x32_bf16 v[48:51], a[52:55], v[100:103], v[48:51]\n\t" "ds_read_b128 v[96:99], v26 offset:20480\n\t" "s_waitcnt lgkmcnt(5)\n\t" \
   "v_mfma_f32_16x16x32_bf16 v[52:55], a[136:139], v[104:107], v[52:55]\n\t" "v_cvt_pk_bf16_f32 v113, v113, v112\n\t" \
   "v_mfma_f32_16x16x32_bf16 v[48:51], a[56:59], v[104:107], v[48:51]\n\t" "ds_read_b128 v[100:103], v26 offset:21504\n\t" \
   "v_and_b32 v8, 0xffff0000, v113\n\t" "v_mfma_f32_16x16x32_bf16 v[52:55], a[140:143], v[108:111], v[52:55]\n\t" \
   "v_mfma_f32_16x16x32_bf16 v[48:51], a[60:63], v[108:111], v[48:51]\n\t" "ds_read_b128 v[104:107], v26 offset:22528\n\t" \
   "v_lshlrev_b32 v113, 16, v113\n\t" "s_waitcnt lgkmcnt(5)\n\t" "v_mfma_f32_16x16x32_bf16 v[52:55], a[144:147], v[80:83], v[52:55]\n\t" \
   "v_add_f32 v113, 0x3727c5ac, v113\n\t" "v_mfma_f32_16x16x32_bf16 v[48:51], a[64:67], v[80:83], v[48:51]\n\t" \
   "ds_read_b128 v[108:111], v26 offset:23552\n\t" "v_mfma_f32_16x16x32_bf16 v[52:55], a[148:151], v[84:87], v[52:55]\n\t" \
   "v_cvt_pk_bf16_f32 v113, v113, v113\n\t" "v_mfma_f32_16x16x32_bf16 v[48:51], a[68:71], v[84:87], v[48:51]\n\t" \
   "ds_read_b128 v[80:83], v26 offset:24576\n\t" "v_lshlrev_b32 v113, 16, v113\n\t" "s_waitcnt lgkmcnt(5)\n\t" \
   "v_mfma_f32_16x16x32_bf16 v[52:55], a[152:155], v[88:91], v[52:55]\n\t" "v_sqrt_f32 v113, v113\n\t" \
   "v_mfma_f32_16x16x32_bf16 v[48:51], a[72:75], v[88:91], v[48:51]\n\t" "ds_read_b128 v[84:87], v26 offset:25600\n\t" \
   "v_mfma_f32_16x16x32_bf16 v[52:55], a[156:159], v[92:95], v[52:55]\n\t" "s_nop 0\n\t" \
   "v_mfma_f32_16x16x32_bf16 v[48:51], a[76:79], v[92:95], v[48:51]\n\t" "ds_read_b128 v[88:91], v26 offset:26624\n\t" \
   "v_cvt_pk_bf16_f32 v113, v113, v113\n\t" "s_waitcnt lgkmcnt(5)\n\t" "v_mfma_f32_16x16x32_bf16 v[60:63], a[80:83], v[96:99], 0\n\t" \
   "v_mfma_f32_16x16x32_bf16 v[56:59], a[0:3], v[96:99], 0\n\t" "ds_read_b128 v[92:95], v26 offset:27648\n\t" "v_lshlrev_b32 v113, 16, v113\n\t" \
   "v_mfma_f32_16x16x32_bf16 v[60:63], a[84:87], v[100:103], v[60:63]\n\t" "v_rcp_f32 v10, v113\n\t" \
   "v_mfma_f32_16x16x32_bf16 v[56:59], a[4:7], v[100:103], v[56:59]\n\t" "ds_read_b128 v[96:99], v26 offset:28672\n\t" "s_waitcnt lgkmcnt(5)\n\t" \
   "v_mfma_f32_16x16x32_bf16 v[60:63], a[88:91], v[104:107], v[60:63]\n\t" "v_fmac_f32 v48, v32, v8\n\t" "v_fmac_f32 v49, v33, v8\n\t" \
   "v_mfma_f32_16x16x32_bf16 v[56:59], a[8:11], v[104:107], v[56:59]\n\t" "ds_read_b128 v[100:103], v26 offset:29696\n\t" "v_fmac_f32 v50, v34, v8\n\t" \
   "v_mfma_f32_16x16x32_bf16 v[60:63], a[92:95], v[108:111], v[60:63]\n\t" "v_fmac_f32 v51, v35, v8\n\t" \
   "v_mfma_f32_16x16x32_bf16 v[56:59], a[12:15], v[108:111], v[56:59]\n\t" "ds_read_b128 v[104:107], v26 offset:30720\n\t" \
   "v_fmac_f32 v52, v36, v8\n\t" "s_waitcnt lgkmcnt(5)\n\t" "v_mfma_f32_16x16x32_bf16 v[60:63], a[96:99], v[80:83], v[60:63]\n\t" \
   "v_fmac_f32 v53, v37, v8\n\t" "v_mfma_f32_16x16x32_bf16 v[56:59], a[16:19], v[80:83], v[56:59]\n\t" "ds_read_b128 v[108:111], v26 offset:31744\n\t" \
   "v_fmac_f32 v54, v38, v8\n\t" "v_mfma_f32_16x16x32_bf16 v[60:63], a[100:103], v[84:87], v[60:63]\n\t" "v_fmac_f32 v55, v39, v8\n\t" \
   "v_mfma_f32_16x16x32_bf16 v[56:59], a[20:23], v[84:87], v[56:59]\n\t" "ds_read_b128 v[80:83], v26 offset:32768\n\t" "v_mul_f32 v48, v48, v10\n\t" \
   "v_mul_f32 v49, v49, v10\n\t" "s_waitcnt lgkmcnt(5)\n\t" "v_mfma_f32_16x16x32_bf16 v[60:63], a[104:107], v[88:91], v[60:63]\n\t" \
   "v_mul_f32 v50, v50, v10\n\t" "v_mfma_f32_16x16x32_bf16 v[56:59], a[24:27], v[88:91], v[56:59]\n\t" "ds_read_b128 v[84:87], v26 offset:33792\n\t" \
   "v_mul_f32 v51, v51, v10\n\t" "v_mfma_f32_16x16x32_bf16 v[60:63], a[108:111], v[92:95], v[60:63]\n\t" "v_mul_f32 v52, v52, v10\n\t" \
   "v_mfma_f32_16x16x32_bf16 v[56:59], a[28:31], v[92:95], v[56:59]\n\t" "ds_read_b128 v[88:91], v26 offset:34816\n\t" "v_mul_f32 v53, v53, v10\n\t" \
   "s_waitcnt lgkmcnt(5)\n\t" "v_mfma_f32_16x16x32_bf16 v[60:63], a[112:115], v[96:99], v[60:63]\n\t" "v_mul_f32 v54, v54, v10\n\t" \
   "v_mfma_f32_16x16x32_bf16 v[56:59], a[32:35], v[96:99], v[56:59]\n\t" "ds_read_b128 v[92:95], v26 offset:35840\n\t" "v_mul_f32 v55, v55, v10\n\t" \
   "v_mfma_f32_16x16x32_bf16 v[60:63], a[116:119], v[100:103], v[60:63]\n\t" "buffer_store_dwordx4 v[48:51], v21, %[rs], s48 offen offset:0 sc1\n\t" \
   "buffer_store_dwordx4 v[52:55], v21, %[rs], s47 offen offset:0 sc1\n\t" "v_mfma_f32_16x16x32_bf16 v[56:59], a[36:39], v[100:103], v[56:59]\n\t" \
   "ds_read_b128 v[96:99], v26 offset:36864\n\t" "v_add_f32 v122, v122, v124\n\t" "s_waitcnt lgkmcnt(5)\n\t" \
   "v_mfma_f32_16x16x32_bf16 v[60:63], a[120:123], v[104:107], v[60:63]\n\t" "v_add_f32 v123, v123, v125\n\t" \
   "v_mfma_f32_16x16x32_bf16 v[56:59], a[40:43], v[104:107], v[56:59]\n\t" "ds_read_b128 v[100:103], v26 offset:37888\n\t" \
   "v_add_f32 v126, v126, v128\n\t" "v_mfma_f32_16x16x32_bf16 v[60:63], a[124:127], v[108:111], v[60:63]\n\t" "v_add_f32 v127, v127, v129\n\t" \
   "v_mfma_f32_16x16x32_bf16 v[56:59], a[44:47], v[108:111], v[56:59]\n\t" "ds_read_b128 v[104:107], v26 offset:38912\n\t" \
   "v_add_f32 v122, v122, v130\n\t" "s_waitcnt lgkmcnt(5)\n\t" "v_mfma_f32_16x16x32_bf16 v[60:63], a[128:131], v[80:83], v[60:63]\n\t" \
   "v_add_f32 v123, v123, v131\n\t" "v_mfma_f32_16x16x32_bf16 v[56:59], a[48:51], v[80:83], v[56:59]\n\t" \
   "ds_read_b128 v[108:111], v26 offset:39936\n\t" "v_add_f32 v122, v122, v126\n\t" "v_add_f32 v123, v123, v127\n\t" \
   "v_mfma_f32_16x16x32_bf16 v[60:63], a[132:135], v[84:87], v[60:63]\n\t" "v_mul_f32 v122, 0xbacccccd, v122\n\t" \
   "v_mfma_f32_16x16x32_bf16 v[56:59], a[52:55], v[84:87], v[56:59]\n\t" "v_mul_f32 v123, 0x3acccccd, v123\n\t" "s_waitcnt lgkmcnt(4)\n\t" \
   "v_mfma_f32_16x16x32_bf16 v[60:63], a[136:139], v[88:91], v[60:63]\n\t" "v_fma_f32 v123, -v122, v122, v123\n\t" \
   "v_mfma_f32_16x16x32_bf16 v[56:59], a[56:59], v[88:91], v[56:59]\n\t" "v_max_f32 v123, 0, v123\n\t" \
   "v_mfma_f32_16x16x32_bf16 v[60:63], a[140:143], v[92:95], v[60:63]\n\t" "v_cvt_pk_bf16_f32 v123, v123, v122\n\t" \
   "v_mfma_f32_16x16x32_bf16 v[56:59], a[60:63], v[92:95], v[56:59]\n\t" "v_and_b32 v12, 0xffff0000, v123\n\t" "s_waitcnt lgkmcnt(2)\n\t" \
   "v_mfma_f32_16x16x32_bf16 v[60:63], a[144:147], v[96:99], v[60:63]\n\t" "v_lshlrev_b32 v123, 16, v123\n\t" "v_add_f32 v123, 0x3727c5ac, v123\n\t" \
   "v_mfma_f32_16x16x32_bf16 v[56:59], a[64:67], v[96:99], v[56:59]\n\t" "v_cvt_pk_bf16_f32 v123, v123, v123\n\t" \
   "v_mfma_f32_16x16x32_bf16 v[60:63], a[148:151], v[100:103], v[60:63]\n\t" "v_lshlrev_b32 v123, 16, v123\n\t" \
   "v_mfma_f32_16x16x32_bf16 v[56:59], a[68:71], v[100:103], v[56:59]\n\t" "v_sqrt_f32 v123, v123\n\t" "s_waitcnt lgkmcnt(0)\n\t" \
   "v_mfma_f32_16x16x32_bf16 v[60:63], a[152:155], v[104:107], v[60:63]\n\t" "s_nop 0\n\t" \
   "v_mfma_f32_16x16x32_bf16 v[56:59], a[72:75], v[104:107], v[56:59]\n\t" "v_cvt_pk_bf16_f32 v123, v123, v123\n\t" \
   "v_mfma_f32_16x16x32_bf16 v[60:63], a[156:159], v[108:111], v[60:63]\n\t" "v_lshlrev_b32 v123, 16, v123\n\t" \
   "v_mfma_f32_16x16x32_bf16 v[56:59], a[76:79], v[108:111], v[56:59]\n\t" "v_rcp_f32 v132, v123\n\t" "s_xor_b32 s46, s46, 44032\n\t" \
   "s_add_u32 s45, s45, 4\n\t" "s_nop 5\n\t" "v_fmac_f32 v56, v32, v12\n\t" "v_fmac_f32 v57, v33, v12\n\t" "v_fmac_f32 v58, v34, v12\n\t" \
   "v_fmac_f32 v59, v35, v12\n\t" "v_fmac_f32 v60, v36, v12\n\t" "v_fmac_f32 v61, v37, v12\n\t" "v_fmac_f32 v62, v38, v12\n\t" \
   "v_fmac_f32 v63, v39, v12\n\t" "v_mul_f32 v56, v56, v132\n\t" "v_mul_f32 v57, v57, v132\n\t" "v_mul_f32 v58, v58, v132\n\t" \
   "v_mul_f32 v59, v59, v132\n\t" "v_mul_f32 v60, v60, v132\n\t" "v_mul_f32 v61, v61, v132\n\t" "v_mul_f32 v62, v62, v132\n\t" \
   "v_mul_f32 v63, v63, v132\n\t" "buffer_store_dwordx4 v[56:59], v21, %[rs], s48 offen offset:256 sc1\n\t" \
   "buffer_store_dwordx4 v[60:63], v21, %[rs], s47 offen offset:256 sc1\n\t" "s_cmp_lt_u32 s45, 64\n\t" "s_cbranch_scc1 1b\n\t" \
   "s_waitcnt vmcnt(0) lgkmcnt(0)"
#define DF_CLOB "v8", "v9", "v10", "v11", "v12", "v13", "v14", "v15", "v32", "v33", "v34", "v35", "v36", "v37", "v38", "v39", "v40", "v41", "v42", "v43", "v44", "v45", "v46", "v47", "v48", "v49", "v50", "v51", "v52", "v53", "v54", "v55", "v56", "v57", "v58", "v59", "v60", "v61", "v62", "v63", "v64", "v65", "v66", "v67", "v68", "v69", "v70", "v71", "v72", "v73", "v74", "v75", "v76", "v77", "v78", "v79", "v80", "v81", "v82", "v83", "v84", "v85", "v86", "v87", "v88", "v89", "v90", "v91", "v92", "v93", "v94", "v95", "v96", "v97", "v98", "v99", "v100", "v101", "v102", "v103", "v104", "v105", "v106", "v107", "v108", "v109", "v110", "v111", "v112", "v113", "v114", "v115", "v116", "v117", "v118", "v119", "v120", "v121", "v122", "v123", "v124", "v125", "v126", "v127", "v128", "v129", "v130", "v131", "v132", "v133", "v134", "v135", "s40", "s41", "s42", "s43", "s44", "s45", "s46", "s47", "s48", "s49", "s50", "s51", "s52", "s53"
#define PRE_ASM \
   "s_mov_b32 s45, 0\n\t" "s_mov_b32 s46, 0\n\t" "s_mul_i32 s52, s45, 1536\n\t" "s_mul_i32 s51, s45, 40960\n\t" "s_add_u32 m0, s46, %[ldx]\n\t" \
   "s_mov_b32 s53, s46\n\t" "buffer_load_dwordx4 v16, %[rs], s51 offen offset:0 sc1 lds\n\t" \
   "buffer_load_dwordx4 v16, %[rs], s51 offen offset:1024 sc1 lds\n\t" "buffer_load_dwordx4 v16, %[rs], s51 offen offset:2048 sc1 lds\n\t" \
   "buffer_load_dwordx4 v16, %[rs], s51 offen offset:3072 sc1 lds\n\t" "s_add_u32 m0, m0, 4096\n\t" "s_add_u32 s51, s51, 4096\n\t" \
   "buffer_load_dwordx4 v16, %[rs], s51 offen offset:0 sc1 lds\n\t" "buffer_load_dwordx4 v16, %[rs], s51 offen offset:1024 sc1 lds\n\t" \
   "buffer_load_dwordx4 v16, %[rs], s51 offen offset:2048 sc1 lds\n\t" "buffer_load_dwordx4 v16, %[rs], s51 offen offset:3072 sc1 lds\n\t" \
   "s_add_u32 m0, m0, 4096\n\t" "s_add_u32 s51, s51, 4096\n\t" "buffer_load_dwordx4 v16, %[rs], s51 offen offset:0 sc1 lds\n\t" \
   "buffer_load_dwordx4 v16, %[rs], s51 offen offset:1024 sc1 lds\n\t" "s_add_u32 m0, s53, %[ld1]\n\t" "s_nop 0\n\t" \
   "buffer_load_dwordx4 v17, %[rs], s52 offen sc1 lds\n\t" "s_mov_b32 s45, 1\n\t" "s_mov_b32 s46, 44032\n\t" "s_mul_i32 s52, s45, 1536\n\t" \
   "s_mul_i32 s51, s45, 40960\n\t" "s_add_u32 m0, s46, %[ldx]\n\t" "s_mov_b32 s53, s46\n\t" \
   "buffer_load_dwordx4 v16, %[rs], s51 offen offset:0 sc1 lds\n\t" "buffer_load_dwordx4 v16, %[rs], s51 offen offset:1024 sc1 lds\n\t" \
   "buffer_load_dwordx4 v16, %[rs], s51 offen offset:2048 sc1 lds\n\t" "buffer_load_dwordx4 v16, %[rs], s51 offen offset:3072 sc1 lds\n\t" \
   "s_add_u32 m0, m0, 4096\n\t" "s_add_u32 s51, s51, 4096\n\t" "buffer_load_dwordx4 v16, %[rs], s51 offen offset:0 sc1 lds\n\t" \
   "buffer_load_dwordx4 v16, %[rs], s51 offen offset:1024 sc1 lds\n\t" "buffer_load_dwordx4 v16, %[rs], s51 offen offset:2048 sc1 lds\n\t" \
   "buffer_load_dwordx4 v16, %[rs], s51 offen offset:3072 sc1 lds\n\t" "s_add_u32 m0, m0, 4096\n\t" "s_add_u32 s51, s51, 4096\n\t" \
   "buffer_load_dwordx4 v16, %[rs], s51 offen offset:0 sc1 lds\n\t" "buffer_load_dwordx4 v16, %[rs], s51 offen offset:1024 sc1 lds\n\t" \
   "s_add_u32 m0, s53, %[ld1]\n\t" "s_nop 0\n\t" "buffer_load_dwordx4 v17, %[rs], s52 offen sc1 lds\n\t" ""
#define PRE_CLOB "s45", "s46", "s47", "s48", "s49", "s50", "s51", "s52", "s53"
__device__ __forceinline__ void pre_proj(int wv, rsrc_t rb) {
  const int lane = lane_id();
  const unsigned lds0 = (unsigned)(uintptr_t)(__attribute__((address_space(3))) unsigned char*)dyn_lds;
  asm volatile(PRE_ASM ::"{v16}"((int)OFF_XR + wv * 10240 + lane * 16), "{v17}"(wv * 64 + lane < 96 ? (int)OFF_ST + wv * 1024 + lane * 16 : OOB), [rs] "s"(rb),
               [ldx] "s"(lds0 + PJ_X + wv * 10240), [ld1] "s"(lds0 + PJ_X + 40960 + (wv < 2 ? wv : 2) * 1024) : PRE_CLOB, "scc", "m0", "memory");
}
template <bool VU>
__device__ __forceinline__ void job_proj(int wv_, const v32u (&w)[10], unsigned char* ws, const Pool& pool, int layer, int h) {
  const unsigned lds0 = (unsigned)(uintptr_t)(__attribute__((address_space(3))) unsigned char*)dyn_lds;
  const int lane = lane_id(), wv = wv_, g = lane >> 4, r = lane & 15;
  const v4u rs = make_rs(ws, (unsigned)OFF_END);
  v16u ad = (unsigned)lane;
  ad[0] = (int)OFF_XR + wv * 10240 + lane * 16;
  ad[1] = wv * 64 + lane < 96 ? (int)OFF_ST + wv * 1024 + lane * 16 : OOB;
  ad[3] = cnt_off(layer, CNT_IDFT, lane & 31);
  ad[4] = lane == 0 ? kqf_off(layer, 0, (VU ? 32 : 0) + h) + wv * 4 : OOB;
  ad[5] = (VU ? (int)OFF_UF + (wv * 320 + h) * 1024 : (h * RT * 4 + wv) * 1024) + lane * 16;
  ad[6] = lds0 + PJ_X + lane * 16;
  ad[7] = lds0 + PJ_X + 40960 + r * 48;
  ad[8] = lds0 + wv * 1024 + lane * 16;
  ad[9] = lds0 + 8192 + lane * 16;
  v4u rf = rs;
  v8u fo;
  if constexpr (!VU) {
    fo[0] = h * 16384 + wv * 2048 + lane * 16;
#pragma unroll
    for (int k = 0; k < 3; ++k) { const int u = h * 10 + wv + 4 * k; fo[1 + k] = wv + 4 * k < 10 ? (u >> 3) * 16384 + (u & 7) * 1024 + lane * 16 : OOB; }
    asm volatile("v_mov_b32 %0, -1" : "=v"(fo[4]));
    fo[5] = fo[6] = fo[7] = fo[4];
  }
  if (layer > 0) dep_wait(wv_, ws, fan_off(1, 1, (VU ? 32 : 0) + h), 1u);
  PH(2 + layer * 6)
#define PJ_OPS                                                                                                                      \
  : W_OPS, [rs] "s"(rs), [rdy] "s"((layer - 1) | 63), [ldx] "s"(lds0 + PJ_X + wv * 10240),                        \
    [ld1] "s"(lds0 + PJ_X + 40960 + (wv < 2 ? wv : 2) * 1024), [qf] "s"((int)OFF_QF), [kf] "s"((int)OFF_KF), [cnt] "s"(cnt_off(layer, CNT_IDFT, 0))
  if constexpr (VU) asm volatile(VU_ASM : "+{v[16:31]}"(ad) PJ_OPS : VU_CLOB, "vcc", "scc", "m0", "memory");
  else asm volatile(KQ_ASM : "+{v[16:31]}"(ad), "+&{s[56:59]}"(rf) PJ_OPS, "{v[152:159]}"(fo), [sel] "s"(pool.sel), [ysel] "s"(pool.ysel), [pool] "s"(pool.base),
                    [yb] "s"(pool.base + P_Y), [size] "s"((unsigned)OFF_END), [skip] "s"(h >= 20 && h < 30 ? 3ull << 62 : 2ull << 62)
                    : KQ_CLOB, "vcc", "scc", "m0", "memory");
  LDSBAR();
}

__device__ __forceinline__ void job_dft(int wv_, unsigned char* ws, int layer, int fg, int set) {
  const unsigned lds0 = (unsigned)(uintptr_t)(__attribute__((address_space(3))) unsigned char*)dyn_lds;
  const int lane = lane_id(), wv = wv_, g = lane >> 4, r = lane & 15;
  const v4u rs = make_rs(ws, (unsigned)OFF_END);
  const rsrc_t rb = ws_rsrc(ws);
  v32u w[5];
#pragma unroll
  for (int i = 0; i < 40; ++i) put4(w[i >> 3], i & 7, ldp(rb, (int)OFF_FWD + ((fg * 8 + wv * 2) * KT + i) * 1024 + lane * 16));
  v16u ad = (unsigned)lane;
  ad[0] = (int)OFF_XR + wv * 10240 + lane * 16;
  ad[1] = wv * 64 + lane < 96 ? (int)OFF_ST + wv * 1024 + lane * 16 : OOB;
  ad[3] = cnt_off(layer, CNT_OUT, 0);
  ad[5] = (int)OFF_X1T + (((fg * 8 + wv * 2) * 4 + g) * S_ + r) * 16;
  ad[6] = lds0 + lane * 16;
  ad[7] = lds0 + 40960 + r * 48;
  asm volatile(DF_ASM
               : "+{v[16:31]}"(ad)
               : "{a[0:31]}"(w[0]), "{a[32:63]}"(w[1]), "{a[64:95]}"(w[2]), "{a[96:127]}"(w[3]), "{a[128:159]}"(w[4]), [rs] "s"(rs), [set] "s"(set),
                 [ldx] "s"(lds0 + wv * 10240), [ld0] "s"(lds0), [ld1] "s"(lds0 + 40960 + (wv < 2 ? wv : 2) * 1024)
               : DF_CLOB, "vcc", "scc", "m0", "memory");
}

constexpr int NOUT = 44;
constexpr int MAIN_LDS = PJ_LDS;
static_assert(MAIN_LDS >= JOB_LDS && MAIN_LDS >= REC_LDS && MAIN_LDS <= 160 * 1024, "LDS");
__device__ __forceinline__ int take_role(int wv_, unsigned char* ws) {
  int* slot = (int*)dyn_lds;
  if (TIDX == 0) {
    const int x = (int)xcc_id();
    const int first = x;
    int role = 0;
#pragma unroll 1
    for (int i = 0; i < 8; ++i) {
      const int pl = first ^ i;
      const unsigned t = __hip_atomic_fetch_add((gu32*)(ws + tick_off(0, pl)), 1u, __ATOMIC_RELAXED, __HIP_MEMORY_SCOPE_AGENT) + 1u;
      if (t < 32u) { role = pl * 32 + (int)t; break; }
    }
    *slot = role;
  }
  __syncthreads();
  const int role = __builtin_amdgcn_readfirstlane(*slot);
  __syncthreads();
  return role;
}
#define TROB (24 * 256 + 16)
#define TC2 30
#define TVIS 32
#define TDLT 170
#define TM1 88
#define TM2 60
__global__ void __launch_bounds__(256, 1) k_main(unsigned char* ws, const u16* Wq, const u16* Wk, const u16* Wv, const u16* Wo, long nlayer
                                                ) {
  v4u* smem = (v4u*)dyn_lds;
  const int wv_ = __builtin_amdgcn_readfirstlane(threadIdx.x >> 6);
  const RecTune tn = {TM1, TM2, 4, TROB, TC2, TVIS, TDLT, 1};
  unsigned long long* dbg = nullptr;
#define RSTAMP_(i)
  const rsrc_t rs = ws_rsrc(ws);
  PH(1)
  v4i cal[18];
  pool_load(ws, cal);
  const int tk = take_role(wv_, ws), pl = tk >> 5, tt = tk & 31;
  Pool pool = pool_init(ws, cal);
  const int role = pl < 4 ? (tt < 16 ? (tt & 8) * 4 + pl * 8 + (tt & 7) : (tt < 26 ? 64 + pl * 10 + tt - 16 : 104 + pl * 6 + tt - 26)) : tk;
  if (role >= 104 && role < 128) return;
  if (role >= 64 && role < 104) {
#pragma unroll 1
    for (int layer = 0; layer < nlayer; ++layer) {
      if (layer > 0) {
        job_col(wv_, ws, rs, (f32x2*)dyn_lds, role + 57, fan_off(0, 1, (role + 57) & 63));
        col_signal(wv_, ws, 1, role + 57);
        PH(7)
      }
      ((unsigned*)(dyn_lds + L_T))[lane_id() & 15] = 0u;
      __syncthreads();
      if (wv_ == 3) rec_fin(ws, pool, layer, role - 64, pl, tn, dbg);
      else if (wv_ == 0) rec_sched(ws, pool, layer, role - 64, pl, tn, dbg);
      __syncthreads();
    }
    return;
  }
  if (tt < 16) {
    v32u w[10];
    const int hd = (pl - 4) * 8 + (tt & 7);
    const unsigned lds0 = (unsigned)(uintptr_t)(__attribute__((address_space(3))) unsigned char*)dyn_lds;
    if (role < 64) {
      const bool pv = role >= 32;
      load_rows(w, pv ? 3 : 0, pv ? Wv : Wq, pv ? Wo : Wk, role & 31, wv_, lds0);
      LDSBAR();
    } else {
      pre_proj(wv_, rs);
      const bool vu = (tt & 8) != 0;
      load_rows(w, vu ? 2 : 1, vu ? Wv : Wq, vu ? Wo : Wk, hd, wv_, lds0 + PJ_X + 2 * PJ_BUF);
    }
#pragma unroll 1
    for (int layer = 0; layer < nlayer; ++layer) {
      int ro = role, xs = pl, wq = wv_, h2 = hd | (tt & 8) << 8;
      asm volatile("" : "+s"(ro), "+s"(xs), "+s"(wq), "+s"(h2), "+s"(ws), "+s"(pool.base));
      if (layer > 0) {
        const int jb = ro < 64 ? ro + 57 : (xs - 4) * 16 + (ro & 31);
        if (ro < 64 || jb < 57) {
          __syncthreads();
          job_col(wq, ws, ws_rsrc(ws), (f32x2*)dyn_lds, jb, fan_off(0, 1, jb & 63));
          col_signal(wq, ws, 1, jb);
          RSTAMP_(5)
          PH(7)
        }
      }
      if (ro < 32) { rec_p1(wq, w, ws, pool, layer, ro, xs, tn, dbg); PH(4 + layer * 6) }
      else if (ro < 64) rec_pv(wq, w, ws, pool, layer, ro - 32, xs, tn, dbg);
      else {
        if (h2 >> 8) job_proj<true>(wq, w, ws, pool, layer, h2 & 31);
        else job_proj<false>(wq, w, ws, pool, layer, h2 & 31);
        RSTAMP_(2)
      }
    }
    return;
  }
#pragma unroll 1
  for (int layer = 0; layer < nlayer; ++layer) {
    if (layer > 0 && tt < 31) {
      const int i = tt - 16;
      job_idft<false>(wv_, ws, fan_off(1, 0, (pl - 4) * 16 + i), rs, smem, (i / 5) * 4 + pl - 4, 12, i % 5, nullptr, dbg + 60000 + role * 8);
    }
    RSTAMP_(2)
    __syncthreads();
    if (tt >= 27) {
      job_dft(wv_, ws, layer, tt - 27, pl - 4);
      if (layer == 0) fan_signal(wv_, ws, cnt_off(0, CNT_IDFT, 0), 20u, -1, 0u, fan_off(0, 1, 0));
    } else {
#pragma unroll 1
      for (int j = (pl - 4) * 11 + tt - 16; j < 320; j += NOUT) {
        flags32_wait(wv_, ws, kqf_off(layer, j / 5, 32));
        job_out(wv_, ws, rs, smem, pool.ysel, layer, j / 5, j % 5);
        dep_signal(wv_, ws, cnt_off(layer, CNT_OUT, j / 5));
      }
    }
    RSTAMP_(3)
    PH((tt >= 27 ? 6 : 5) + layer * 6)
  }
}

extern "C" void kernel_launch(void* const* d_in, const int* in_sizes, int n_in, void* d_out, int out_size, void* d_ws, size_t ws_size,
                              hipStream_t stream) {
  const u16* x = (const u16*)d_in[0];
  const u16* mem0 = (const u16*)d_in[1];
  const u16* Wq = (const u16*)d_in[2];
  const u16* Wk = (const u16*)d_in[3];
  const u16* Wv = (const u16*)d_in[4];
  const u16* Wo = (const u16*)d_in[5];
  const u16* wr = (const u16*)d_in[8];
  const u16* wi = (const u16*)d_in[9];
  unsigned char* ws = (unsigned char*)d_ws;
  u16* out = (u16*)d_out;
  (void)hipFuncSetAttribute((const void*)k_main, hipFuncAttributeMaxDynamicSharedMemorySize, MAIN_LDS);
  (void)hipFuncSetAttribute((const void*)k_fin, hipFuncAttributeMaxDynamicSharedMemorySize, FIN_LDS);
  k_prep<<<PB_TOTAL, 256, 0, stream>>>(x, mem0, Wq, Wk, Wv, Wo, wr, wi, ws);
  k_main<<<256, 256, MAIN_LDS, stream>>>(ws, Wq, Wk, Wv, Wo, 2);
  k_fin<<<NCOL + 320, 256, FIN_LDS, stream>>>(ws, out);
}
```

```cpp
#include <hip/hip_runtime.h>
#include <hip/hip_bf16.h>
#include <stdint.h>

typedef unsigned v4u __attribute__((ext_vector_type(4)));
typedef unsigned v2u __attribute__((ext_vector_type(2)));
typedef float f32x4 __attribute__((ext_vector_type(4)));
typedef float f32x2 __attribute__((ext_vector_type(2)));
typedef __bf16 bf16x8 __attribute__((ext_vector_type(8)));
typedef __bf16 bf16x2 __attribute__((ext_vector_type(2)));
typedef unsigned short u16;

constexpr int S_ = 2048, D_ = 640, NSEG = 64, KT = 20, RT = 128;
constexpr int NFT = 40;
constexpr int NFK = 20;
constexpr int WIN = 32768, REG = 16 * WIN;
constexpr int W_MEM = 10 * WIN, W_MISC = 15 * WIN;
__host__ __device__ constexpr int copy_off(int c) { return (c & 3) * 2048 + (c >> 2) * 16384; }
constexpr int M_STAMP = W_MISC + copy_off(4), M_TM = M_STAMP + 40 * 64, M_CAL = W_MISC + copy_off(3);
constexpr int PAGE = 4 * REG, NYP = 22, P_Y = NSEG * REG;
constexpr size_t SZ_POOL = (size_t)P_Y + (size_t)(NYP + 1) * PAGE;

constexpr size_t OFF_CNT = 0;
constexpr int CNT_IDFT = 0, CNT_OUT = 1;
constexpr size_t OFF_PROG = OFF_CNT + (size_t)3 * 32 * 128;
constexpr size_t OFF_KQF = OFF_PROG + (size_t)NSEG * 4096;
constexpr size_t OFF_TICK = OFF_KQF + (size_t)NSEG * 4096;
#define FNAP 6
#define FSP 16
#define FNG 32
constexpr int FAN_G = (161 * FSP + 127) / 128 * 128 > 3840 ? (161 * FSP + 127) / 128 * 128 : 3840;
constexpr size_t OFF_FAN = OFF_TICK + (size_t)16 * 128;
constexpr size_t HO_LAYER = OFF_FAN + (size_t)2 * FNG * FAN_G;
constexpr size_t OFF_HO = SZ_POOL;
constexpr size_t OFF_SEL = OFF_HO + 2 * HO_LAYER;
__host__ __device__ constexpr int prog_off(int layer, int rb, int slot) { return (int)(OFF_HO + layer * HO_LAYER + OFF_PROG) + (rb * 64 + slot) * 64; }
__host__ __device__ constexpr int kqf_off(int layer, int sg, int h) { return (int)(OFF_HO + layer * HO_LAYER + OFF_KQF) + (sg * 64 + h) * 64; }
__host__ __device__ constexpr int cnt_off(int layer, int kind, int rb) { return (int)(OFF_HO + layer * HO_LAYER + OFF_CNT) + (kind * 32 + rb) * 128; }
__host__ __device__ constexpr int tick_off(int layer, int i) { return (int)(OFF_HO + layer * HO_LAYER + OFF_TICK) + i * 128; }
__host__ __device__ constexpr int fan_off(int layer, int set, int i) { return (int)(OFF_HO + layer * HO_LAYER + OFF_FAN) + (set * FNG + i) * FAN_G; }
constexpr size_t OFF_XR = (OFF_SEL + 8192 + 4095) / 4096 * 4096;
constexpr size_t OFF_ST = OFF_XR + (size_t)RT * KT * 1024;
constexpr size_t OFF_QF = OFF_ST + (size_t)S_ * 48;
constexpr size_t OFF_KF = OFF_QF + (size_t)32 * RT * 4 * 1024;
constexpr size_t OFF_UF = OFF_KF + (size_t)32 * RT * 4 * 1024;
constexpr size_t OFF_PXF = OFF_UF + (size_t)NSEG * 80 * 16 * 1024;
constexpr size_t OFF_PXM = OFF_PXF + (size_t)NSEG * 32 * 2 * 1024;
constexpr size_t OFF_FWD = OFF_PXM + (size_t)NSEG * 8 * 2 * 1024;
constexpr size_t OFF_INV = OFF_FWD + (size_t)NFT * KT * 1024;
constexpr size_t OFF_X1T = OFF_INV + (size_t)40 * NFK * 1024;
constexpr size_t OFF_ZT = OFF_X1T + (size_t)160 * S_ * 8;
constexpr size_t OFF_WT = OFF_ZT + (size_t)161 * S_ * 8;
constexpr size_t OFF_DBG = OFF_WT + (size_t)321 * S_ * 4;
constexpr size_t OFF_END = OFF_DBG + (size_t)1048576;
static_assert(OFF_END <= ((size_t)256 << 20), "workspace too large");

constexpr int AUX_SC1 = 16;
#define PH(k)

__device__ __forceinline__ f32x4 mfma(v4u a, v4u b, f32x4 c) {
  return __builtin_amdgcn_mfma_f32_16x16x32_bf16(__builtin_bit_cast(bf16x8, a), __builtin_bit_cast(bf16x8, b), c, 0, 0, 0);
}
__device__ __forceinline__ unsigned pk(float a, float b) {
  f32x2 v = {a, b};
  return __builtin_bit_cast(unsigned, __builtin_convertvector(v, bf16x2));
}
__device__ __forceinline__ float lo_f(unsigned u) { return __builtin_bit_cast(float, u << 16); }
__device__ __forceinline__ float hi_f(unsigned u) { return __builtin_bit_cast(float, u & 0xFFFF0000u); }
__device__ __forceinline__ float bfr(float x) { return lo_f(pk(x, x)); }
__device__ __forceinline__ v4u pk8(f32x4 a, f32x4 b) { return v4u{pk(a.x, a.y), pk(a.z, a.w), pk(b.x, b.y), pk(b.z, b.w)}; }
__device__ __forceinline__ int sigma(int g, int jj) { return 16 * (jj >> 2) + 4 * g + (jj & 3); }
__device__ __forceinline__ int colmap(int c8, int jj) {
  const int p = c8 >> 3, ga = (c8 >> 1) & 3, half = c8 & 1;
  return 32 * (2 * p + (ga >> 1)) + 16 * ((jj >> 1) & 1) + 4 * (2 * (ga & 1) + (jj >> 2)) + 2 * half + (jj & 1);
}
__device__ __forceinline__ int pool_base(const void* ws) { return (int)((0u - (unsigned)(uintptr_t)ws) & (unsigned)(PAGE - 1)); }
__device__ __forceinline__ unsigned xcc_id() { return (unsigned)__builtin_amdgcn_s_getreg(20 | (3 << 11)) & 7u; }
__device__ __forceinline__ float shx(float v, int m) { return __shfl_xor(v, m); }
__device__ __forceinline__ unsigned nosent(unsigned x) { return x < 0xFFFFFFFEu ? x : 0xFFFFFFFEu; }

__device__ __forceinline__ f32x2 frag_stat(v4u c) {
  const float v[8] = {lo_f(c.x), hi_f(c.x), lo_f(c.y), hi_f(c.y), lo_f(c.z), hi_f(c.z), lo_f(c.w), hi_f(c.w)};
  float q = 0.f;
#pragma unroll
  for (int j = 0; j < 8; ++j) q = __builtin_fmaf(v[j], v[j], q);
  return f32x2{((v[0] + v[1]) + (v[2] + v[3])) + ((v[4] + v[5]) + (v[6] + v[7])), q};
}
__device__ __forceinline__ f32x2 rows_sum(f32x2 a) {
  a.x += shx(a.x, 16); a.y += shx(a.y, 16); a.x += shx(a.x, 32); a.y += shx(a.y, 32);
  return a;
}

__device__ __forceinline__ void dft_frag(unsigned char* ws, bool fwd, int fi, int lane) {
  const int g = lane >> 4, r = lane & 15;
  float hi[8];
#pragma unroll
  for (int jj = 0; jj < 8; ++jj) {
    int f, d;
    if (fwd) { const int kt = fi % KT, ft = fi / KT; f = 16 * ft + r; d = 32 * kt + sigma(g, jj); }
    else { const int fk = fi % NFK, dt = fi / NFK; d = 16 * dt + r; f = 32 * fk + 8 * g + jj; }
    const int k = f == 1 ? 320 : f >> 1;
    const int idx = (k * d) % D_;
    const float a = (float)idx * (2.0f / D_);
    float v = ((f & 1) && f != 1) ? -sinpif(a) : cospif(a);
    if (!fwd && f > 1) v *= 2.f;
    hi[jj] = v;
  }
  v4u* dst = (v4u*)(ws + (fwd ? OFF_FWD : OFF_INV));
  dst[(size_t)fi * 64 + lane] = v4u{pk(hi[0], hi[1]), pk(hi[2], hi[3]), pk(hi[4], hi[5]), pk(hi[6], hi[7])};
}

constexpr int PB_XR = RT * KT / 16;
constexpr int PB_FWD = 2 * NFT * KT / 4;
constexpr int PB_WT = 32 * 6;
constexpr int PB_HO = (int)((2 * HO_LAYER + 32767) / 32768);
constexpr int PB_CAL = 4 * NSEG + 8 * NYP;
constexpr int PB_FILL = 2 * 16, PB_YF = 20;
constexpr int PB_TOTAL = PB_CAL + PB_XR + PB_FWD + PB_WT + PB_FILL + PB_YF + PB_HO;
#define PB_LIST {PB_CAL, 0, PB_XR, PB_FWD, PB_WT, PB_FILL, PB_YF, PB_HO}
static_assert(HO_LAYER % 16 == 0, "fill granularity");
template <int AUX>
__device__ __forceinline__ void fill_pool(unsigned char* ws, int rg, int half, int tid, int q0 = 0, int q1 = 64) {
  const __amdgpu_buffer_rsrc_t rs = __builtin_amdgcn_make_buffer_rsrc(ws, 0, (int)SZ_POOL, 0x00020000);
  const int base = pool_base(ws) + rg * REG + half * 8192 + tid * 16;
#pragma unroll 8
  for (int q = q0; q < q1; ++q) {
    const bool skip = (rg == NSEG - 1 && q >= 40 && q < 60) || ((q | 2) == 63 && tid >= 128);
    __builtin_amdgcn_raw_buffer_store_b128(v4u{0xFFFFFFFFu, 0xFFFFFFFFu, 0xFFFFFFFFu, 0xFFFFFFFFu}, rs,
                                           skip ? 0x7FFFFFF0 : base + (q >> 1) * 16384 + (q & 1) * 4096, 0, AUX);
  }
}
__device__ __forceinline__ void fill_mem0(unsigned char* ws, const u16* __restrict__ mem0, int w, int tid) {
  const int c = tid & 127, c8 = (w * 8 + (c >> 4)) * 2 + (c & 1), row = (c >> 1) & 7;
  unsigned t[4];
#pragma unroll
  for (int k = 0; k < 4; ++k) t[k] = nosent(*(const unsigned*)(mem0 + (size_t)row * D_ + colmap(c8, 2 * k)));
  v4u* dst = (v4u*)(ws + pool_base(ws) + (size_t)(NSEG - 1) * REG + W_MEM + w * WIN);
#pragma unroll
  for (int q = 0; q < 8; ++q) dst[q * 256 + tid] = v4u{t[0], t[1], t[2], t[3]};
}
typedef int v4i __attribute__((ext_vector_type(4)));
__device__ __forceinline__ v4i calibrate(unsigned char* ws, unsigned* lds, int off) {
  const __amdgpu_buffer_rsrc_t rs = __builtin_amdgcn_make_buffer_rsrc(ws, 0, (int)SZ_POOL, 0x00020000);
  const int base = pool_base(ws) + off, tid = threadIdx.x, wv = __builtin_amdgcn_readfirstlane(tid >> 6);
  __builtin_amdgcn_raw_buffer_store_b128(v4u{1u, 2u, 3u, 4u}, rs, base + wv * 128 + ((tid >> 3) & 3) * 8192 + (tid & 7) * 16, 0, AUX_SC1);
  const unsigned char* p = ws + base;
  unsigned t0, t1, t2, t3, t4;
  asm volatile("s_load_dword %0, %5, 0x180\n\ts_load_dword %1, %5, 0x2180\n\ts_load_dword %2, %5, 0x4180\n\ts_load_dword %3, %5, 0x6180\n\t"
               "s_waitcnt vmcnt(0) lgkmcnt(0)\n\ts_memtime s[40:41]\n\ts_waitcnt lgkmcnt(0)\n\ts_mov_b32 %0, s40\n\t"
               "s_load_dword %1, %6, 0\n\ts_waitcnt lgkmcnt(0)\n\ts_memtime s[40:41]\n\ts_waitcnt lgkmcnt(0)\n\ts_mov_b32 %1, s40\n\t"
               "s_load_dword %2, %6, 0x2000\n\ts_waitcnt lgkmcnt(0)\n\ts_memtime s[40:41]\n\ts_waitcnt lgkmcnt(0)\n\ts_mov_b32 %2, s40\n\t"
               "s_load_dword %3, %6, 0x4000\n\ts_waitcnt lgkmcnt(0)\n\ts_memtime s[40:41]\n\ts_waitcnt lgkmcnt(0)\n\ts_mov_b32 %3, s40\n\t"
               "s_load_dword %4, %6, 0x6000\n\ts_waitcnt lgkmcnt(0)\n\ts_memtime s[40:41]\n\ts_waitcnt lgkmcnt(0)\n\ts_mov_b32 %4, s40"
               : "=&s"(t0), "=&s"(t1), "=&s"(t2), "=&s"(t3), "=&s"(t4) : "s"(p), "s"(p + wv * 128) : "s40", "s41", "memory");
  *(v4u*)(lds + wv * 4) = v4u{t1 - t0, t2 - t1, t3 - t2, t4 - t3};
  __syncthreads();
  unsigned best[4];
#pragma unroll
  for (int c = 0; c < 4; ++c) best[c] = min(min(lds[c], lds[4 + c]), lds[8 + c]);
  const int x = (int)xcc_id(), sg = x < 4 ? 1 - 2 * (x & 1) : 0;
  const int d = min(max((int)(best[0] + best[2]) - (int)(best[1] + best[3]), -400), 400);
  return v4i{x < 4 ? d : -d, sg * min(max((int)best[0] - (int)best[2], -80), 80), sg * min(max((int)best[1] - (int)best[3], -80), 80), 0};
}
__device__ __forceinline__ int y_region(unsigned long long sel, int s) { return (s * 80 + (s * 43 >> 7) * 16 + (int)((sel >> s) & 1ull)) * 8192; }

__global__ void __launch_bounds__(256) k_prep(const u16* __restrict__ x,
                                              const u16* __restrict__ mem0, const u16* __restrict__ Wq,
                                              const u16* __restrict__ Wk, const u16* __restrict__ Wv, const u16* __restrict__ Wo,
                                              const u16* __restrict__ wr, const u16* __restrict__ wi, unsigned char* __restrict__ ws) {
  __shared__ __attribute__((aligned(16))) unsigned t32[64 * 65];
  int b = blockIdx.x;
  const int tid = threadIdx.x, lane = tid & 63, wv = tid >> 6, g = lane >> 4, r = lane & 15;
  if (b < PB_CAL) {
    PH(0)
    const int c = b - 4 * NSEG;
    const v4i sel = calibrate(ws, t32, (c < 0 ? (b >> 2) * REG : P_Y + (c >> 3) * PAGE + 3 * REG - (c & 4) * (WIN / 4)) + M_CAL + (b & 3) * 512);
    if (tid == 0) { if (c < 0) ((v4i*)(ws + OFF_SEL))[b] = sel; else ((int*)(ws + OFF_SEL + 4096))[c] = sel.x; }
    if (b < 5) fill_mem0(ws, mem0, b, tid);
    return;
  }
  b -= PB_CAL;
  if (b < PB_XR) {
    f32x2 st = {0.f, 0.f};
#pragma unroll
    for (int q = 0; q < 4; ++q) {
      const int f = b * 16 + wv * 4 + q, kt = f % KT, rt = f / KT;
      const u16* xr = x + (size_t)(rt * 16 + r) * D_ + 32 * kt + 4 * g;
      const v2u c0 = *(const v2u*)xr, c1 = *(const v2u*)(xr + 16);
      const v4u c = v4u{c0.x, c0.y, c1.x, c1.y};
      ((v4u*)(ws + OFF_XR))[(size_t)f * 64 + lane] = c;
      st += frag_stat(c);
    }
    st = rows_sum(st);
    const int f4 = b * 4 + wv;
    if (g == 0) ((f32x2*)(ws + OFF_ST))[((f4 / 5) * 16 + r) * 6 + f4 % 5] = st;
    return;
  }
  b -= PB_XR;
  if (b < PB_FWD) { dft_frag(ws, b < PB_FWD / 2, (b * 4 + wv) % (NFT * KT), lane); return; }
  b -= PB_FWD;
  if (b < PB_WT) {
    const int m0 = (b & 31) * 64, k0 = (b >> 5) * 64, n0 = (b & 24) * 2 + (b & 7) * 256;
#pragma unroll
    for (int q = 0; q < 16; ++q) {
      const int m = q * 4 + wv, k = k0 + lane;
      if (k < 321) t32[m * 65 + lane] = (unsigned)wr[(size_t)(n0 + q + 64 * wv) * 321 + k] | ((unsigned)wi[(size_t)(n0 + q + 64 * wv) * 321 + k] << 16);
    }
    __syncthreads();
#pragma unroll
    for (int q = 0; q < 16; ++q) {
      const int k = q * 4 + wv;
      if (k0 + k < 321) ((unsigned*)(ws + OFF_WT))[(size_t)(k0 + k) * S_ + m0 + lane] = t32[lane * 65 + k];
    }
    return;
  }
  b -= PB_WT;
  if (b < PB_FILL) { fill_pool<0>(ws, ((b >> 4) + 63) & 63, (b >> 3) & 1, tid, (b & 7) * 8, (b & 7) * 8 + 8); return; }
  b -= PB_FILL;
  if (b < PB_YF) {
#pragma unroll
    for (int q = 0; q < 8; ++q) ((v4u*)(ws + pool_base(ws) + P_Y))[b * 2048 + q * 256 + tid] = v4u{0xFFFFFFFFu, 0xFFFFFFFFu, 0xFFFFFFFFu, 0xFFFFFFFFu};
    return;
  }
  b -= PB_YF;
#pragma unroll
  for (int q = 0; q < 8; ++q) {
    const int i = b * 2048 + q * 256 + tid;
    if ((size_t)i * 16 < 2 * HO_LAYER) ((v4u*)(ws + OFF_HO))[i] = v4u{0xFFFFFFFFu, 0xFFFFFFFFu, 0xFFFFFFFFu, 0xFFFFFFFFu};
  }
}

__device__ __forceinline__ int lane_id() {
  int l;
  asm volatile("v_mbcnt_lo_u32_b32 %0, -1, 0\n\tv_mbcnt_hi_u32_b32 %0, -1, %0" : "=v"(l));
  return l;
}
#define TIDX (wv_ * 64 + lane_id())
typedef __amdgpu_buffer_rsrc_t rsrc_t;
__device__ __forceinline__ rsrc_t ws_rsrc(unsigned char* ws) { return __builtin_amdgcn_make_buffer_rsrc(ws, 0, (int)OFF_END, 0x00020000); }
__device__ __forceinline__ v4u ldc(rsrc_t rs, int off) { return __builtin_amdgcn_raw_buffer_load_b128(rs, off, 0, AUX_SC1); }
__device__ __forceinline__ v4u ldp(rsrc_t rs, int off) { return __builtin_amdgcn_raw_buffer_load_b128(rs, off, 0, 0); }
__device__ __forceinline__ void stc(rsrc_t rs, int off, v4u v) { __builtin_amdgcn_raw_buffer_store_b128(v, rs, off, 0, AUX_SC1); }

typedef __attribute__((address_space(1))) unsigned gu32;
__device__ __forceinline__ void prog_prewait(int wv_, unsigned char* ws, int off) {
  if (TIDX == 0) {
    gu32* p = (gu32*)(ws + off);
    for (unsigned spins = 0; spins < (1u << 16); ++spins) {
      if (__hip_atomic_load(p, __ATOMIC_RELAXED, __HIP_MEMORY_SCOPE_AGENT) != 0xFFFFFFFFu) break;
      __builtin_amdgcn_s_sleep(48);
    }
  }
}
__device__ __forceinline__ void prog_wait(int wv_, unsigned char* ws, int off) {
  if (wv_ == 0) {
    const int l = lane_id();
    gu32* p0 = (gu32*)(ws + off + l * 64);
    for (unsigned spins = 0; spins < (1u << 17); ++spins) {
      const unsigned a = __hip_atomic_load(p0, __ATOMIC_RELAXED, __HIP_MEMORY_SCOPE_AGENT);
      if (!__any(a == 0xFFFFFFFFu)) break;
      __builtin_amdgcn_s_sleep(8);
    }
  }
  __syncthreads();
}
__device__ __forceinline__ void flags32_wait(int wv_, unsigned char* ws, int off) {
  if (wv_ == 0) {
    const int l = lane_id();
    __attribute__((address_space(1))) unsigned long long* p0 = (__attribute__((address_space(1))) unsigned long long*)(ws + off + (l & 31) * 64 + (l >> 5) * 8);
    for (unsigned spins = 0; spins < (1u << 17); ++spins) {
      const unsigned long long v = __hip_atomic_load(p0, __ATOMIC_RELAXED, __HIP_MEMORY_SCOPE_AGENT);
      if (!__any(max((unsigned)v, (unsigned)(v >> 32)) == 0xFFFFFFFFu)) break;
      __builtin_amdgcn_s_sleep(16);
    }
  }
  __syncthreads();
}
__device__ __forceinline__ void dep_signal(int wv_, unsigned char* ws, int off, unsigned inc = 1u) {
  asm volatile("s_waitcnt vmcnt(0)" ::: "memory");
  __syncthreads();
  if (TIDX == 0) __hip_atomic_fetch_add((gu32*)(ws + off), inc, __ATOMIC_RELAXED, __HIP_MEMORY_SCOPE_AGENT);
}

template <int SP> __device__ __forceinline__ void fan_signal(int wv_, unsigned char* ws, int fan, int i) {
  asm volatile("s_waitcnt vmcnt(0)" ::: "memory");
  __syncthreads();
  const int l = lane_id();
  if (wv_ == 0 && l < FNG) __hip_atomic_store((gu32*)(ws + fan + l * FAN_G + i * SP), 1u, __ATOMIC_RELAXED, __HIP_MEMORY_SCOPE_AGENT);
}
template <int SP, int N> __device__ __forceinline__ void fan_wait(int wv_, unsigned char* ws, int fan, int salt) {
  if (wv_ * 64 < N) {
    gu32* p = (gu32*)(ws + fan + (((int)xcc_id() * (FNG / 8) + (salt & (FNG / 8 - 1))) * FAN_G + min(TIDX, N - 1) * SP));
    for (unsigned spins = 0; spins < (1u << 17); ++spins) {
      const unsigned long long m = __ballot(__hip_atomic_load(p, __ATOMIC_RELAXED, __HIP_MEMORY_SCOPE_AGENT) == 0xFFFFFFFFu);
      if (m == 0) break;
      __builtin_amdgcn_s_sleep(FNAP);
      if (N == 20 && (m & 0x3FF)) __builtin_amdgcn_s_sleep(40);
    }
  }
  __syncthreads();
}

__device__ __forceinline__ void stat_put(v4u* lds, int wv, int rt, int lane, v4u y) {
  const f32x2 st = rows_sum(frag_stat(y));
  if (lane < 16) ((f32x2*)(lds + 5120))[(wv * 2 + rt) * 16 + lane] = st;
}
__device__ __forceinline__ void stat_store(rsrc_t rs, v4u* lds, int wv, int lane, int sg, int cg) {
  __syncthreads();
  if (wv == 0) {
    const f32x2* sp = (const f32x2*)(lds + 5120) + (lane & 31);
    const f32x2 st = (sp[0] + sp[32]) + (sp[64] + sp[96]);
    __builtin_amdgcn_raw_buffer_store_b64(__builtin_bit_cast(v2u, st), rs, lane < 32 ? (int)OFF_ST + ((sg * 32 + lane) * 6 + cg) * 8 : 0x7FFFFFF0, 0, AUX_SC1);
  }
}

__device__ __forceinline__ void job_out(int wv_, unsigned char* ws, rsrc_t rs, v4u* lds, unsigned long long ysel, int layer, int s, int cg) {
  const int lane = lane_id(), wv = wv_, g = lane >> 4, r = lane & 15;
  const int kt = cg * 4 + wv;
  const int c16 = 2 * kt + (r >> 3), ci = (g * 16 + (r & 4) * 2 + (r & 3)) * 16;
  const int ubase = (int)OFF_UF + (s * 40 + c16) * 32768 + ci;
  const int ybase = pool_base(ws) + P_Y + y_region(ysel, s) + c16 * 16384 + ci;
  v4u z[40][2];
#pragma unroll
  for (int ks = 0; ks < 32; ++ks) { z[ks][0] = ldc(rs, ubase + ks * 1024); z[ks][1] = ldc(rs, ubase + ks * 1024 + 64); }
  const int xo = (int)OFF_XR + ((2 * s * KT + kt) * 64 + lane) * 16;
  const v4u x0 = ldc(rs, xo), x1 = ldc(rs, xo + KT * 1024);
  if (s > 1) prog_prewait(wv_, ws, prog_off(layer, s - 2, (s + cg * 7) & 31));
#define OSTAMP(i)
  OSTAMP(4)
  prog_wait(wv_, ws, prog_off(layer, s, 0));
  OSTAMP(0)
  {
    const unsigned l0 = (unsigned)(uintptr_t)(__attribute__((address_space(3))) v4u*)lds;
    const int vo = lane * 16;
#pragma unroll
    for (int q = 0; q < 5; ++q) {
      const unsigned d = l0 + (q < 4 ? wv * 16384 + q * 4096 : 65536 + wv * 4096);
      const int so = q < 4 ? (int)OFF_PXF + s * 65536 + wv * 16384 + q * 4096 : (int)OFF_PXM + s * 16384 + wv * 4096;
      asm volatile("s_mov_b32 m0, %0\n\ts_nop 0\n\tbuffer_load_dwordx4 %1, %2, %3 offen sc1 lds\n\t"
                   "buffer_load_dwordx4 %1, %2, %3 offen offset:1024 sc1 lds\n\tbuffer_load_dwordx4 %1, %2, %3 offen offset:2048 sc1 lds\n\t"
                   "buffer_load_dwordx4 %1, %2, %3 offen offset:3072 sc1 lds" ::"s"(d), "v"(vo), "s"(rs), "s"(so) : "m0", "memory");
    }
#pragma unroll
    for (int hq = 0; hq < 8; ++hq) { z[32 + hq][0] = ldc(rs, ybase + hq * 1024); z[32 + hq][1] = ldc(rs, ybase + hq * 1024 + 64); }
  }
  f32x4 acc[2][2] = {{{0, 0, 0, 0}, {0, 0, 0, 0}}, {{0, 0, 0, 0}, {0, 0, 0, 0}}};
  asm volatile("s_waitcnt vmcnt(16)\n\ts_barrier" ::: "memory");
  OSTAMP(1)
  v4u pf[2][8];
#pragma unroll
  for (int i = 0; i < 8; ++i) pf[0][i] = lds[i * 64 + lane];
#pragma unroll
  for (int b = 0; b < 10; ++b) {
    if (b < 9) {
#pragma unroll
      for (int i = 0; i < 8; ++i) pf[(b + 1) & 1][i] = lds[((b + 1) * 8 + i) * 64 + lane];
    }
    __builtin_amdgcn_sched_barrier(0);
#pragma unroll
    for (int i = 0; i < 8; ++i) {
      const v4u z0 = z[b * 4 + (i >> 1)][0], z1 = z[b * 4 + (i >> 1)][1];
      acc[0][i & 1] = mfma(b < 8 ? z0 : z0.xzyw, pf[b & 1][i], acc[0][i & 1]);
      acc[1][i & 1] = mfma(b < 8 ? z1 : z1.xzyw, pf[b & 1][i], acc[1][i & 1]);
    }
    __builtin_amdgcn_sched_barrier(0);
  }
#pragma unroll
  for (int qt = 0; qt < 2; ++qt) {
    const v4u x = qt ? x1 : x0;
    const auto s0 = __builtin_amdgcn_permlane16_swap(pk(acc[0][qt].x, acc[0][qt].y), pk(acc[1][qt].x, acc[1][qt].y), false, false);
    const auto s1 = __builtin_amdgcn_permlane16_swap(pk(acc[0][qt].z, acc[0][qt].w), pk(acc[1][qt].z, acc[1][qt].w), false, false);
    v4u y;
    y.x = pk(lo_f(x.x) + lo_f(s0[0]), hi_f(x.x) + hi_f(s0[0]));
    y.y = pk(lo_f(x.y) + lo_f(s0[1]), hi_f(x.y) + hi_f(s0[1]));
    y.z = pk(lo_f(x.z) + lo_f(s1[0]), hi_f(x.z) + hi_f(s1[0]));
    y.w = pk(lo_f(x.w) + lo_f(s1[1]), hi_f(x.w) + hi_f(s1[1]));
    stc(rs, xo + qt * KT * 1024, y);
    stat_put(lds, wv, qt, lane, y);
  }
  OSTAMP(2)
  stat_store(rs, lds, wv, lane, s, cg);
}

typedef unsigned v8u __attribute__((ext_vector_type(8)));
typedef unsigned v16u __attribute__((ext_vector_type(16)));
typedef unsigned v32u __attribute__((ext_vector_type(32)));
__device__ __forceinline__ float cosi(int i) { return __builtin_amdgcn_cosf((float)i * (1.f / 2048)); }
__device__ __forceinline__ float sini(int i) { return __builtin_amdgcn_sinf((float)i * (1.f / 2048)); }
__device__ __forceinline__ void cis8(v32u& d, int x0, int dx, int d4, unsigned m) {
#pragma unroll
  for (int q = 0; q < 4; ++q) {
    const int x = x0 + dx * 2 * (q & 1) + d4 * (q >> 1);
    d[q] = pk(cosi(x), cosi(x + dx)); d[4 + q] = d[q] ^ m;
    d[8 + q] = pk(sini(x), sini(x + dx)); d[12 + q] = d[8 + q] ^ m;
  }
}
#define LDS_V4(a) (*(__attribute__((address_space(3))) v4u*)(uintptr_t)(a))
#define LDS_U32(a) (*(__attribute__((address_space(3))) unsigned*)(uintptr_t)(a))
#define LDS_V2(a) (*(__attribute__((address_space(3))) v2u*)(uintptr_t)(a))
#define COL_ASM \
   "s_waitcnt lgkmcnt(0)\n\t" "s_barrier\n\t" "ds_read_b64_tr_b16 v[136:137], v16 offset:0\n\t" "ds_read_b64_tr_b16 v[138:139], v16 offset:1056\n\t" \
   "ds_read_b64_tr_b16 v[140:141], v16 offset:8448\n\t" "ds_read_b64_tr_b16 v[142:143], v16 offset:9504\n\t" \
   "ds_read_b64_tr_b16 v[144:145], v16 offset:8\n\t" "ds_read_b64_tr_b16 v[146:147], v16 offset:1064\n\t" \
   "ds_read_b64_tr_b16 v[148:149], v16 offset:8456\n\t" "ds_read_b64_tr_b16 v[150:151], v16 offset:9512\n\t" "s_waitcnt lgkmcnt(4)\n\t" \
   "v_mfma_f32_16x16x32_bf16 v[160:163], v[136:139], v[32:35], 0\n\t" "v_mfma_f32_16x16x32_bf16 v[164:167], v[136:139], v[40:43], 0\n\t" \
   "v_mfma_f32_16x16x32_bf16 v[160:163], v[140:143], v[36:39], v[160:163]\n\t" \
   "v_mfma_f32_16x16x32_bf16 v[164:167], v[140:143], v[44:47], v[164:167]\n\t" "ds_read_b64_tr_b16 v[152:153], v16 offset:16\n\t" \
   "ds_read_b64_tr_b16 v[154:155], v16 offset:1072\n\t" "ds_read_b64_tr_b16 v[156:157], v16 offset:8464\n\t" \
   "ds_read_b64_tr_b16 v[158:159], v16 offset:9520\n\t" "s_nop 3\n\t" "s_waitcnt lgkmcnt(4)\n\t" \
   "v_mfma_f32_16x16x32_bf16 v[168:171], v[144:147], v[32:35], 0\n\t" "v_add_f32 v160, v160, v165\n\t" "v_sub_f32 v161, v161, v164\n\t" \
   "v_mfma_f32_16x16x32_bf16 v[172:175], v[144:147], v[40:43], 0\n\t" "v_add_f32 v162, v162, v167\n\t" "v_sub_f32 v163, v163, v166\n\t" \
   "v_mfma_f32_16x16x32_bf16 v[168:171], v[148:151], v[36:39], v[168:171]\n\t" "v_mul_f32 v24, v161, v49\n\t" "v_mul_f32 v25, v161, v48\n\t" \
   "v_mfma_f32_16x16x32_bf16 v[172:175], v[148:151], v[44:47], v[172:175]\n\t" "v_mul_f32 v26, v163, v49\n\t" "v_mul_f32 v27, v163, v48\n\t" \
   "v_fma_f32 v177, v160, v49, v25\n\t" "v_fma_f32 v176, v160, v48, -v24\n\t" "v_fma_f32 v179, v162, v49, v27\n\t" \
   "v_fma_f32 v178, v162, v48, -v26\n\t" "ds_read_b64_tr_b16 v[136:137], v16 offset:24\n\t" "ds_read_b64_tr_b16 v[138:139], v16 offset:1080\n\t" \
   "ds_read_b64_tr_b16 v[140:141], v16 offset:8472\n\t" "ds_read_b64_tr_b16 v[142:143], v16 offset:9528\n\t" "s_waitcnt lgkmcnt(4)\n\t" \
   "v_mfma_f32_16x16x32_bf16 v[160:163], v[152:155], v[32:35], 0\n\t" "v_add_f32 v168, v168, v173\n\t" "v_sub_f32 v169, v169, v172\n\t" \
   "v_mfma_f32_16x16x32_bf16 v[164:167], v[152:155], v[40:43], 0\n\t" "v_add_f32 v170, v170, v175\n\t" "v_sub_f32 v171, v171, v174\n\t" \
   "v_mfma_f32_16x16x32_bf16 v[160:163], v[156:159], v[36:39], v[160:163]\n\t" "v_mul_f32 v24, v169, v51\n\t" "v_mul_f32 v25, v169, v50\n\t" \
   "v_mfma_f32_16x16x32_bf16 v[164:167], v[156:159], v[44:47], v[164:167]\n\t" "v_mul_f32 v26, v171, v51\n\t" "v_mul_f32 v27, v171, v50\n\t" \
   "v_fma_f32 v181, v168, v51, v25\n\t" "v_fma_f32 v180, v168, v50, -v24\n\t" "v_fma_f32 v183, v170, v51, v27\n\t" \
   "v_fma_f32 v182, v170, v50, -v26\n\t" "v_cvt_pk_bf16_f32 v12, v177, v181\n\t" "v_cvt_pk_bf16_f32 v8, v176, v180\n\t" \
   "v_cvt_pk_bf16_f32 v188, v179, v183\n\t" "v_cvt_pk_bf16_f32 v184, v178, v182\n\t" "ds_read_b64_tr_b16 v[144:145], v16 offset:128\n\t" \
   "ds_read_b64_tr_b16 v[146:147], v16 offset:1184\n\t" "ds_read_b64_tr_b16 v[148:149], v16 offset:8576\n\t" \
   "ds_read_b64_tr_b16 v[150:151], v16 offset:9632\n\t" "s_waitcnt lgkmcnt(4)\n\t" "v_mfma_f32_16x16x32_bf16 v[168:171], v[136:139], v[32:35], 0\n\t" \
   "v_add_f32 v160, v160, v165\n\t" "v_sub_f32 v161, v161, v164\n\t" "v_mfma_f32_16x16x32_bf16 v[172:175], v[136:139], v[40:43], 0\n\t" \
   "v_add_f32 v162, v162, v167\n\t" "v_sub_f32 v163, v163, v166\n\t" "v_mfma_f32_16x16x32_bf16 v[168:171], v[140:143], v[36:39], v[168:171]\n\t" \
   "v_mul_f32 v24, v161, v53\n\t" "v_mul_f32 v25, v161, v52\n\t" "v_mfma_f32_16x16x32_bf16 v[172:175], v[140:143], v[44:47], v[172:175]\n\t" \
   "v_mul_f32 v26, v163, v53\n\t" "v_mul_f32 v27, v163, v52\n\t" "v_fma_f32 v177, v160, v53, v25\n\t" "v_fma_f32 v176, v160, v52, -v24\n\t" \
   "v_fma_f32 v179, v162, v53, v27\n\t" "v_fma_f32 v178, v162, v52, -v26\n\t" "ds_read_b64_tr_b16 v[152:153], v16 offset:136\n\t" \
   "ds_read_b64_tr_b16 v[154:155], v16 offset:1192\n\t" "ds_read_b64_tr_b16 v[156:157], v16 offset:8584\n\t" \
   "ds_read_b64_tr_b16 v[158:159], v16 offset:9640\n\t" "s_waitcnt lgkmcnt(4)\n\t" "v_mfma_f32_16x16x32_bf16 v[160:163], v[144:147], v[32:35], 0\n\t" \
   "v_add_f32 v168, v168, v173\n\t" "v_sub_f32 v169, v169, v172\n\t" "v_mfma_f32_16x16x32_bf16 v[164:167], v[144:147], v[40:43], 0\n\t" \
   "v_add_f32 v170, v170, v175\n\t" "v_sub_f32 v171, v171, v174\n\t" "v_mfma_f32_16x16x32_bf16 v[160:163], v[148:151], v[36:39], v[160:163]\n\t" \
   "v_mul_f32 v24, v169, v55\n\t" "v_mul_f32 v25, v169, v54\n\t" "v_mfma_f32_16x16x32_bf16 v[164:167], v[148:151], v[44:47], v[164:167]\n\t" \
   "v_mul_f32 v26, v171, v55\n\t" "v_mul_f32 v27, v171, v54\n\t" "v_fma_f32 v181, v168, v55, v25\n\t" "v_fma_f32 v180, v168, v54, -v24\n\t" \
   "v_fma_f32 v183, v170, v55, v27\n\t" "v_fma_f32 v182, v170, v54, -v26\n\t" "v_cvt_pk_bf16_f32 v13, v177, v181\n\t" \
   "v_cvt_pk_bf16_f32 v9, v176, v180\n\t" "v_cvt_pk_bf16_f32 v189, v179, v183\n\t" "v_cvt_pk_bf16_f32 v185, v178, v182\n\t" \
   "ds_read_b64_tr_b16 v[136:137], v16 offset:144\n\t" "ds_read_b64_tr_b16 v[138:139], v16 offset:1200\n\t" \
   "ds_read_b64_tr_b16 v[140:141], v16 offset:8592\n\t" "ds_read_b64_tr_b16 v[142:143], v16 offset:9648\n\t" "s_waitcnt lgkmcnt(4)\n\t" \
   "v_mfma_f32_16x16x32_bf16 v[168:171], v[152:155], v[32:35], 0\n\t" "v_add_f32 v160, v160, v165\n\t" "v_sub_f32 v161, v161, v164\n\t" \
   "v_mfma_f32_16x16x32_bf16 v[172:175], v[152:155], v[40:43], 0\n\t" "v_add_f32 v162, v162, v167\n\t" "v_sub_f32 v163, v163, v166\n\t" \
   "v_mfma_f32_16x16x32_bf16 v[168:171], v[156:159], v[36:39], v[168:171]\n\t" "v_mul_f32 v24, v161, v57\n\t" "v_mul_f32 v25, v161, v56\n\t" \
   "v_mfma_f32_16x16x32_bf16 v[172:175], v[156:159], v[44:47], v[172:175]\n\t" "v_mul_f32 v26, v163, v57\n\t" "v_mul_f32 v27, v163, v56\n\t" \
   "v_fma_f32 v177, v160, v57, v25\n\t" "v_fma_f32 v176, v160, v56, -v24\n\t" "v_fma_f32 v179, v162, v57, v27\n\t" \
   "v_fma_f32 v178, v162, v56, -v26\n\t" "ds_read_b64_tr_b16 v[144:145], v16 offset:152\n\t" "ds_read_b64_tr_b16 v[146:147], v16 offset:1208\n\t" \
   "ds_read_b64_tr_b16 v[148:149], v16 offset:8600\n\t" "ds_read_b64_tr_b16 v[150:151], v16 offset:9656\n\t" "s_waitcnt lgkmcnt(4)\n\t" \
   "v_mfma_f32_16x16x32_bf16 v[160:163], v[136:139], v[32:35], 0\n\t" "v_add_f32 v168, v168, v173\n\t" "v_sub_f32 v169, v169, v172\n\t" \
   "v_mfma_f32_16x16x32_bf16 v[164:167], v[136:139], v[40:43], 0\n\t" "v_add_f32 v170, v170, v175\n\t" "v_sub_f32 v171, v171, v174\n\t" \
   "v_mfma_f32_16x16x32_bf16 v[160:163], v[140:143], v[36:39], v[160:163]\n\t" "v_mul_f32 v24, v169, v59\n\t" "v_mul_f32 v25, v169, v58\n\t" \
   "v_mfma_f32_16x16x32_bf16 v[164:167], v[140:143], v[44:47], v[164:167]\n\t" "v_mul_f32 v26, v171, v59\n\t" "v_mul_f32 v27, v171, v58\n\t" \
   "v_fma_f32 v181, v168, v59, v25\n\t" "v_fma_f32 v180, v168, v58, -v24\n\t" "v_fma_f32 v183, v170, v59, v27\n\t" \
   "v_fma_f32 v182, v170, v58, -v26\n\t" "v_cvt_pk_bf16_f32 v14, v177, v181\n\t" "v_cvt_pk_bf16_f32 v10, v176, v180\n\t" \
   "v_cvt_pk_bf16_f32 v190, v179, v183\n\t" "v_cvt_pk_bf16_f32 v186, v178, v182\n\t" "s_nop 1\n\t" "s_waitcnt lgkmcnt(0)\n\t" \
   "v_mfma_f32_16x16x32_bf16 v[168:171], v[144:147], v[32:35], 0\n\t" "v_add_f32 v160, v160, v165\n\t" "v_sub_f32 v161, v161, v164\n\t" \
   "v_mfma_f32_16x16x32_bf16 v[172:175], v[144:147], v[40:43], 0\n\t" "v_add_f32 v162, v162, v167\n\t" "v_sub_f32 v163, v163, v166\n\t" \
   "v_mfma_f32_16x16x32_bf16 v[168:171], v[148:151], v[36:39], v[168:171]\n\t" "v_mul_f32 v24, v161, v61\n\t" "v_mul_f32 v25, v161, v60\n\t" \
   "v_mfma_f32_16x16x32_bf16 v[172:175], v[148:151], v[44:47], v[172:175]\n\t" "v_mul_f32 v26, v163, v61\n\t" "v_mul_f32 v27, v163, v60\n\t" \
   "v_fma_f32 v177, v160, v61, v25\n\t" "v_fma_f32 v176, v160, v60, -v24\n\t" "v_fma_f32 v179, v162, v61, v27\n\t" \
   "v_fma_f32 v178, v162, v60, -v26\n\t" "s_nop 1\n\t" "v_add_f32 v168, v168, v173\n\t" "v_sub_f32 v169, v169, v172\n\t" "v_mul_f32 v24, v169, v63\n\t" \
   "v_mul_f32 v25, v169, v62\n\t" "v_fma_f32 v181, v168, v63, v25\n\t" "v_fma_f32 v180, v168, v62, -v24\n\t" "v_cvt_pk_bf16_f32 v15, v177, v181\n\t" \
   "v_cvt_pk_bf16_f32 v11, v176, v180\n\t" "s_nop 1\n\t" "v_mfma_f32_16x16x32_bf16 v[160:163], v[64:67], v[8:11], 0\n\t" \
   "v_add_f32 v170, v170, v175\n\t" "v_sub_f32 v171, v171, v174\n\t" "v_mfma_f32_16x16x32_bf16 v[164:167], v[64:67], v[12:15], 0\n\t" \
   "v_mul_f32 v24, v171, v63\n\t" "v_mfma_f32_16x16x32_bf16 v[160:163], v[72:75], v[12:15], v[160:163]\n\t" "v_mul_f32 v25, v171, v62\n\t" \
   "v_mfma_f32_16x16x32_bf16 v[164:167], v[128:131], v[8:11], v[164:167]\n\t" "v_fma_f32 v183, v170, v63, v25\n\t" \
   "v_fma_f32 v182, v170, v62, -v24\n\t" "v_cvt_pk_bf16_f32 v191, v179, v183\n\t" "v_cvt_pk_bf16_f32 v187, v178, v182\n\t" "s_nop 4\n\t" \
   "v_mfma_f32_16x16x32_bf16 v[168:171], v[68:71], v[8:11], 0\n\t" "v_mul_f32 v24, v164, v97\n\t" "v_mul_f32 v25, v164, v96\n\t" \
   "v_mul_f32 v26, v165, v99\n\t" "v_mfma_f32_16x16x32_bf16 v[172:175], v[68:71], v[12:15], 0\n\t" "v_mul_f32 v27, v165, v98\n\t" \
   "v_mul_f32 v28, v166, v101\n\t" "v_mul_f32 v29, v166, v100\n\t" "v_mfma_f32_16x16x32_bf16 v[168:171], v[76:79], v[12:15], v[168:171]\n\t" \
   "v_mul_f32 v30, v167, v103\n\t" "v_mul_f32 v31, v167, v102\n\t" "v_fma_f32 v164, v160, v97, v25\n\t" \
   "v_mfma_f32_16x16x32_bf16 v[172:175], v[132:135], v[8:11], v[172:175]\n\t" "v_fma_f32 v160, v160, v96, -v24\n\t" \
   "v_fma_f32 v165, v161, v99, v27\n\t" "v_fma_f32 v161, v161, v98, -v26\n\t" "v_fma_f32 v166, v162, v101, v29\n\t" \
   "v_fma_f32 v162, v162, v100, -v28\n\t" "v_fma_f32 v167, v163, v103, v31\n\t" "v_fma_f32 v163, v163, v102, -v30\n\t" \
   "v_cvt_pk_bf16_f32 v140, v164, v165\n\t" "v_cvt_pk_bf16_f32 v136, v160, v161\n\t" "v_cvt_pk_bf16_f32 v141, v166, v167\n\t" \
   "v_cvt_pk_bf16_f32 v137, v162, v163\n\t" "v_mfma_f32_16x16x32_bf16 v[160:163], v[64:67], v[184:187], 0\n\t" "v_mul_f32 v24, v172, v105\n\t" \
   "v_mul_f32 v25, v172, v104\n\t" "v_mul_f32 v26, v173, v107\n\t" "v_mfma_f32_16x16x32_bf16 v[164:167], v[64:67], v[188:191], 0\n\t" \
   "v_mul_f32 v27, v173, v106\n\t" "v_mul_f32 v28, v174, v109\n\t" "v_mul_f32 v29, v174, v108\n\t" \
   "v_mfma_f32_16x16x32_bf16 v[160:163], v[72:75], v[188:191], v[160:163]\n\t" "v_mul_f32 v30, v175, v111\n\t" "v_mul_f32 v31, v175, v110\n\t" \
   "v_fma_f32 v172, v168, v105, v25\n\t" "v_mfma_f32_16x16x32_bf16 v[164:167], v[128:131], v[184:187], v[164:167]\n\t" \
   "v_fma_f32 v168, v168, v104, -v24\n\t" "v_fma_f32 v173, v169, v107, v27\n\t" "v_fma_f32 v169, v169, v106, -v26\n\t" \
   "v_fma_f32 v174, v170, v109, v29\n\t" "v_fma_f32 v170, v170, v108, -v28\n\t" "v_fma_f32 v175, v171, v111, v31\n\t" \
   "v_fma_f32 v171, v171, v110, -v30\n\t" "v_cvt_pk_bf16_f32 v142, v172, v173\n\t" "v_cvt_pk_bf16_f32 v138, v168, v169\n\t" \
   "v_cvt_pk_bf16_f32 v143, v174, v175\n\t" "v_cvt_pk_bf16_f32 v139, v170, v171\n\t" "v_mfma_f32_16x16x32_bf16 v[168:171], v[68:71], v[184:187], 0\n\t" \
   "v_mul_f32 v24, v164, v113\n\t" "v_mul_f32 v25, v164, v112\n\t" "v_mul_f32 v26, v165, v115\n\t" \
   "v_mfma_f32_16x16x32_bf16 v[172:175], v[68:71], v[188:191], 0\n\t" "v_mul_f32 v27, v165, v114\n\t" "v_mul_f32 v28, v166, v117\n\t" \
   "v_mul_f32 v29, v166, v116\n\t" "v_mfma_f32_16x16x32_bf16 v[168:171], v[76:79], v[188:191], v[168:171]\n\t" "v_mul_f32 v30, v167, v119\n\t" \
   "v_mul_f32 v31, v167, v118\n\t" "v_fma_f32 v164, v160, v113, v25\n\t" "v_mfma_f32_16x16x32_bf16 v[172:175], v[132:135], v[184:187], v[172:175]\n\t" \
   "v_fma_f32 v160, v160, v112, -v24\n\t" "v_fma_f32 v165, v161, v115, v27\n\t" "v_fma_f32 v161, v161, v114, -v26\n\t" \
   "v_fma_f32 v166, v162, v117, v29\n\t" "v_fma_f32 v162, v162, v116, -v28\n\t" "v_fma_f32 v167, v163, v119, v31\n\t" \
   "v_fma_f32 v163, v163, v118, -v30\n\t" "v_cvt_pk_bf16_f32 v148, v164, v165\n\t" "v_cvt_pk_bf16_f32 v144, v160, v161\n\t" \
   "v_cvt_pk_bf16_f32 v149, v166, v167\n\t" "v_cvt_pk_bf16_f32 v145, v162, v163\n\t" "v_mfma_f32_16x16x32_bf16 v[160:163], v[136:139], v[64:67], 0\n\t" \
   "v_mul_f32 v24, v172, v121\n\t" "v_mul_f32 v25, v172, v120\n\t" "v_mul_f32 v26, v173, v123\n\t" \
   "v_mfma_f32_16x16x32_bf16 v[164:167], v[140:143], v[64:67], 0\n\t" "v_mul_f32 v27, v173, v122\n\t" "v_mul_f32 v28, v174, v125\n\t" \
   "v_mul_f32 v29, v174, v124\n\t" "v_mfma_f32_16x16x32_bf16 v[160:163], v[140:143], v[128:131], v[160:163]\n\t" "v_mul_f32 v30, v175, v127\n\t" \
   "v_mul_f32 v31, v175, v126\n\t" "v_fma_f32 v172, v168, v121, v25\n\t" "v_mfma_f32_16x16x32_bf16 v[164:167], v[136:139], v[72:75], v[164:167]\n\t" \
   "v_fma_f32 v168, v168, v120, -v24\n\t" "v_fma_f32 v173, v169, v123, v27\n\t" "v_fma_f32 v169, v169, v122, -v26\n\t" \
   "v_fma_f32 v174, v170, v125, v29\n\t" "v_fma_f32 v170, v170, v124, -v28\n\t" "v_fma_f32 v175, v171, v127, v31\n\t" \
   "v_fma_f32 v171, v171, v126, -v30\n\t" "v_cvt_pk_bf16_f32 v150, v172, v173\n\t" "v_cvt_pk_bf16_f32 v146, v168, v169\n\t" \
   "v_cvt_pk_bf16_f32 v151, v174, v175\n\t" "v_cvt_pk_bf16_f32 v147, v170, v171\n\t" "v_mfma_f32_16x16x32_bf16 v[168:171], v[136:139], v[68:71], 0\n\t" \
   "v_mul_f32 v24, v164, v81\n\t" "v_mul_f32 v25, v164, v80\n\t" "v_mul_f32 v26, v165, v83\n\t" \
   "v_mfma_f32_16x16x32_bf16 v[172:175], v[140:143], v[68:71], 0\n\t" "v_mul_f32 v27, v165, v82\n\t" "v_mul_f32 v28, v166, v85\n\t" \
   "v_mul_f32 v29, v166, v84\n\t" "v_mfma_f32_16x16x32_bf16 v[168:171], v[140:143], v[132:135], v[168:171]\n\t" "v_mul_f32 v30, v167, v87\n\t" \
   "v_mul_f32 v31, v167, v86\n\t" "v_fma_f32 v164, v160, v81, v25\n\t" "v_mfma_f32_16x16x32_bf16 v[172:175], v[136:139], v[76:79], v[172:175]\n\t" \
   "v_fma_f32 v160, v160, v80, -v24\n\t" "v_fma_f32 v165, v161, v83, v27\n\t" "v_fma_f32 v161, v161, v82, -v26\n\t" \
   "v_fma_f32 v166, v162, v85, v29\n\t" "v_fma_f32 v162, v162, v84, -v28\n\t" "v_fma_f32 v167, v163, v87, v31\n\t" \
   "v_fma_f32 v163, v163, v86, -v30\n\t" "v_cvt_pk_bf16_f32 v178, v164, v165\n\t" "v_cvt_pk_bf16_f32 v176, v160, v161\n\t" \
   "v_cvt_pk_bf16_f32 v179, v166, v167\n\t" "v_cvt_pk_bf16_f32 v177, v162, v163\n\t" "ds_write_b64 v17, v[178:179] offset:128\n\t" \
   "ds_write_b64 v17, v[176:177] offset:0\n\t" "v_mfma_f32_16x16x32_bf16 v[160:163], v[144:147], v[64:67], 0\n\t" "v_mul_f32 v24, v172, v89\n\t" \
   "v_mul_f32 v25, v172, v88\n\t" "v_mul_f32 v26, v173, v91\n\t" "v_mfma_f32_16x16x32_bf16 v[164:167], v[148:151], v[64:67], 0\n\t" \
   "v_mul_f32 v27, v173, v90\n\t" "v_mul_f32 v28, v174, v93\n\t" "v_mul_f32 v29, v174, v92\n\t" \
   "v_mfma_f32_16x16x32_bf16 v[160:163], v[148:151], v[128:131], v[160:163]\n\t" "v_mul_f32 v30, v175, v95\n\t" "v_mul_f32 v31, v175, v94\n\t" \
   "v_fma_f32 v172, v168, v89, v25\n\t" "v_mfma_f32_16x16x32_bf16 v[164:167], v[144:147], v[72:75], v[164:167]\n\t" \
   "v_fma_f32 v168, v168, v88, -v24\n\t" "v_fma_f32 v173, v169, v91, v27\n\t" "v_fma_f32 v169, v169, v90, -v26\n\t" \
   "v_fma_f32 v174, v170, v93, v29\n\t" "v_fma_f32 v170, v170, v92, -v28\n\t" "v_fma_f32 v175, v171, v95, v31\n\t" \
   "v_fma_f32 v171, v171, v94, -v30\n\t" "v_cvt_pk_bf16_f32 v182, v172, v173\n\t" "v_cvt_pk_bf16_f32 v180, v168, v169\n\t" \
   "v_cvt_pk_bf16_f32 v183, v174, v175\n\t" "v_cvt_pk_bf16_f32 v181, v170, v171\n\t" "ds_write_b64 v17, v[182:183] offset:8384\n\t" \
   "ds_write_b64 v17, v[180:181] offset:8256\n\t" "v_mfma_f32_16x16x32_bf16 v[168:171], v[144:147], v[68:71], 0\n\t" "v_mul_f32 v24, v164, v81\n\t" \
   "v_mul_f32 v25, v164, v80\n\t" "v_mul_f32 v26, v165, v83\n\t" "v_mfma_f32_16x16x32_bf16 v[172:175], v[148:151], v[68:71], 0\n\t" \
   "v_mul_f32 v27, v165, v82\n\t" "v_mul_f32 v28, v166, v85\n\t" "v_mul_f32 v29, v166, v84\n\t" \
   "v_mfma_f32_16x16x32_bf16 v[168:171], v[148:151], v[132:135], v[168:171]\n\t" "v_mul_f32 v30, v167, v87\n\t" "v_mul_f32 v31, v167, v86\n\t" \
   "v_fma_f32 v164, v160, v81, v25\n\t" "v_mfma_f32_16x16x32_bf16 v[172:175], v[144:147], v[76:79], v[172:175]\n\t" \
   "v_fma_f32 v160, v160, v80, -v24\n\t" "v_fma_f32 v165, v161, v83, v27\n\t" "v_fma_f32 v161, v161, v82, -v26\n\t" \
   "v_fma_f32 v166, v162, v85, v29\n\t" "v_fma_f32 v162, v162, v84, -v28\n\t" "v_fma_f32 v167, v163, v87, v31\n\t" \
   "v_fma_f32 v163, v163, v86, -v30\n\t" "v_cvt_pk_bf16_f32 v178, v164, v165\n\t" "v_cvt_pk_bf16_f32 v176, v160, v161\n\t" \
   "v_cvt_pk_bf16_f32 v179, v166, v167\n\t" "v_cvt_pk_bf16_f32 v177, v162, v163\n\t" "ds_write_b64 v18, v[178:179] offset:128\n\t" \
   "ds_write_b64 v18, v[176:177] offset:0\n\t" "v_mul_f32 v24, v172, v89\n\t" "v_mul_f32 v25, v172, v88\n\t" "v_mul_f32 v26, v173, v91\n\t" \
   "v_mul_f32 v27, v173, v90\n\t" "v_mul_f32 v28, v174, v93\n\t" "v_mul_f32 v29, v174, v92\n\t" "v_mul_f32 v30, v175, v95\n\t" \
   "v_mul_f32 v31, v175, v94\n\t" "v_fma_f32 v172, v168, v89, v25\n\t" "v_fma_f32 v168, v168, v88, -v24\n\t" "v_fma_f32 v173, v169, v91, v27\n\t" \
   "v_fma_f32 v169, v169, v90, -v26\n\t" "v_fma_f32 v174, v170, v93, v29\n\t" "v_fma_f32 v170, v170, v92, -v28\n\t" \
   "v_fma_f32 v175, v171, v95, v31\n\t" "v_fma_f32 v171, v171, v94, -v30\n\t" "v_cvt_pk_bf16_f32 v182, v172, v173\n\t" \
   "v_cvt_pk_bf16_f32 v180, v168, v169\n\t" "v_cvt_pk_bf16_f32 v183, v174, v175\n\t" "v_cvt_pk_bf16_f32 v181, v170, v171\n\t" \
   "ds_write_b64 v18, v[182:183] offset:8384\n\t" "ds_write_b64 v18, v[180:181] offset:8256\n\t" "s_waitcnt lgkmcnt(0)\n\t" "s_barrier\n\t" \
   "ds_read_b128 v[136:139], v19 offset:0\n\t" "ds_read_b128 v[140:143], v20 offset:0\n\t" "ds_read_b128 v[144:147], v19 offset:2064\n\t" \
   "ds_read_b128 v[148:151], v20 offset:2064\n\t" "s_waitcnt lgkmcnt(2)\n\t" "v_mfma_f32_16x16x32_bf16 v[160:163], v[136:139], v[32:35], 0\n\t" \
   "ds_read_b128 v[152:155], v19 offset:4128\n\t" "ds_read_b128 v[156:159], v20 offset:4128\n\t" \
   "v_mfma_f32_16x16x32_bf16 v[164:167], v[136:139], v[40:43], 0\n\t" "v_mfma_f32_16x16x32_bf16 v[160:163], v[140:143], v[36:39], v[160:163]\n\t" \
   "v_mfma_f32_16x16x32_bf16 v[164:167], v[140:143], v[44:47], v[164:167]\n\t" "s_waitcnt lgkmcnt(2)\n\t" \
   "v_mfma_f32_16x16x32_bf16 v[168:171], v[144:147], v[32:35], 0\n\t" "ds_read_b128 v[136:139], v19 offset:6192\n\t" \
   "ds_read_b128 v[140:143], v20 offset:6192\n\t" "v_mfma_f32_16x16x32_bf16 v[172:175], v[144:147], v[40:43], 0\n\t" "v_sub_f32 v160, v160, v165\n\t" \
   "v_add_f32 v161, v161, v164\n\t" "v_sub_f32 v162, v162, v167\n\t" "v_mfma_f32_16x16x32_bf16 v[168:171], v[148:151], v[36:39], v[168:171]\n\t" \
   "v_add_f32 v163, v163, v166\n\t" "v_cvt_pk_bf16_f32 v8, v160, v161\n\t" "v_cvt_pk_bf16_f32 v9, v162, v163\n\t" \
   "v_mfma_f32_16x16x32_bf16 v[172:175], v[148:151], v[44:47], v[172:175]\n\t" "s_waitcnt lgkmcnt(2)\n\t" \
   "v_mfma_f32_16x16x32_bf16 v[160:163], v[152:155], v[32:35], 0\n\t" "ds_read_b128 v[144:147], v19 offset:8256\n\t" \
   "ds_read_b128 v[148:151], v20 offset:8256\n\t" "v_mfma_f32_16x16x32_bf16 v[164:167], v[152:155], v[40:43], 0\n\t" "v_sub_f32 v168, v168, v173\n\t" \
   "v_add_f32 v169, v169, v172\n\t" "v_sub_f32 v170, v170, v175\n\t" "v_mfma_f32_16x16x32_bf16 v[160:163], v[156:159], v[36:39], v[160:163]\n\t" \
   "v_add_f32 v171, v171, v174\n\t" "v_cvt_pk_bf16_f32 v10, v168, v169\n\t" "v_cvt_pk_bf16_f32 v11, v170, v171\n\t" \
   "v_mfma_f32_16x16x32_bf16 v[164:167], v[156:159], v[44:47], v[164:167]\n\t" "buffer_store_dwordx4 v[8:11], v21, %[rs], 0 offen offset:0 sc1\n\t" \
   "s_waitcnt lgkmcnt(2)\n\t" "v_mfma_f32_16x16x32_bf16 v[168:171], v[136:139], v[32:35], 0\n\t" "ds_read_b128 v[152:155], v19 offset:10320\n\t" \
   "ds_read_b128 v[156:159], v20 offset:10320\n\t" "v_mfma_f32_16x16x32_bf16 v[172:175], v[136:139], v[40:43], 0\n\t" "v_sub_f32 v160, v160, v165\n\t" \
   "v_add_f32 v161, v161, v164\n\t" "v_sub_f32 v162, v162, v167\n\t" "v_mfma_f32_16x16x32_bf16 v[168:171], v[140:143], v[36:39], v[168:171]\n\t" \
   "v_add_f32 v163, v163, v166\n\t" "v_cvt_pk_bf16_f32 v12, v160, v161\n\t" "v_cvt_pk_bf16_f32 v13, v162, v163\n\t" \
   "v_mfma_f32_16x16x32_bf16 v[172:175], v[140:143], v[44:47], v[172:175]\n\t" "s_waitcnt lgkmcnt(2)\n\t" \
   "v_mfma_f32_16x16x32_bf16 v[160:163], v[144:147], v[32:35], 0\n\t" "ds_read_b128 v[136:139], v19 offset:12384\n\t" \
   "ds_read_b128 v[140:143], v20 offset:12384\n\t" "v_mfma_f32_16x16x32_bf16 v[164:167], v[144:147], v[40:43], 0\n\t" "v_sub_f32 v168, v168, v173\n\t" \
   "v_add_f32 v169, v169, v172\n\t" "v_sub_f32 v170, v170, v175\n\t" "v_mfma_f32_16x16x32_bf16 v[160:163], v[148:151], v[36:39], v[160:163]\n\t" \
   "v_add_f32 v171, v171, v174\n\t" "v_cvt_pk_bf16_f32 v14, v168, v169\n\t" "v_cvt_pk_bf16_f32 v15, v170, v171\n\t" \
   "v_mfma_f32_16x16x32_bf16 v[164:167], v[148:151], v[44:47], v[164:167]\n\t" "buffer_store_dwordx4 v[12:15], v21, %[rs], 0 offen offset:64 sc1\n\t" \
   "s_waitcnt lgkmcnt(2)\n\t" "v_mfma_f32_16x16x32_bf16 v[168:171], v[152:155], v[32:35], 0\n\t" "ds_read_b128 v[144:147], v19 offset:14448\n\t" \
   "ds_read_b128 v[148:151], v20 offset:14448\n\t" "v_mfma_f32_16x16x32_bf16 v[172:175], v[152:155], v[40:43], 0\n\t" "v_sub_f32 v160, v160, v165\n\t" \
   "v_add_f32 v161, v161, v164\n\t" "v_sub_f32 v162, v162, v167\n\t" "v_mfma_f32_16x16x32_bf16 v[168:171], v[156:159], v[36:39], v[168:171]\n\t" \
   "v_add_f32 v163, v163, v166\n\t" "v_cvt_pk_bf16_f32 v8, v160, v161\n\t" "v_cvt_pk_bf16_f32 v9, v162, v163\n\t" \
   "v_mfma_f32_16x16x32_bf16 v[172:175], v[156:159], v[44:47], v[172:175]\n\t" "s_waitcnt lgkmcnt(2)\n\t" \
   "v_mfma_f32_16x16x32_bf16 v[160:163], v[136:139], v[32:35], 0\n\t" "s_nop 0\n\t" "s_nop 0\n\t" \
   "v_mfma_f32_16x16x32_bf16 v[164:167], v[136:139], v[40:43], 0\n\t" "v_sub_f32 v168, v168, v173\n\t" "v_add_f32 v169, v169, v172\n\t" \
   "v_sub_f32 v170, v170, v175\n\t" "v_mfma_f32_16x16x32_bf16 v[160:163], v[140:143], v[36:39], v[160:163]\n\t" "v_add_f32 v171, v171, v174\n\t" \
   "v_cvt_pk_bf16_f32 v10, v168, v169\n\t" "v_cvt_pk_bf16_f32 v11, v170, v171\n\t" \
   "v_mfma_f32_16x16x32_bf16 v[164:167], v[140:143], v[44:47], v[164:167]\n\t" "buffer_store_dwordx4 v[8:11], v21, %[rs], 0 offen offset:128 sc1\n\t" \
   "s_waitcnt lgkmcnt(0)\n\t" "v_mfma_f32_16x16x32_bf16 v[168:171], v[144:147], v[32:35], 0\n\t" "s_nop 0\n\t" "s_nop 0\n\t" \
   "v_mfma_f32_16x16x32_bf16 v[172:175], v[144:147], v[40:43], 0\n\t" "v_sub_f32 v160, v160, v165\n\t" "v_add_f32 v161, v161, v164\n\t" \
   "v_sub_f32 v162, v162, v167\n\t" "v_mfma_f32_16x16x32_bf16 v[168:171], v[148:151], v[36:39], v[168:171]\n\t" "v_add_f32 v163, v163, v166\n\t" \
   "v_cvt_pk_bf16_f32 v12, v160, v161\n\t" "v_cvt_pk_bf16_f32 v13, v162, v163\n\t" \
   "v_mfma_f32_16x16x32_bf16 v[172:175], v[148:151], v[44:47], v[172:175]\n\t" "s_nop 7\n\t" "v_sub_f32 v168, v168, v173\n\t" \
   "v_add_f32 v169, v169, v172\n\t" "v_sub_f32 v170, v170, v175\n\t" "v_add_f32 v171, v171, v174\n\t" "v_cvt_pk_bf16_f32 v14, v168, v169\n\t" \
   "v_cvt_pk_bf16_f32 v15, v170, v171\n\t" "buffer_store_dwordx4 v[12:15], v21, %[rs], 0 offen offset:192 sc1\n\t" "s_nop 1"
#define COL_CLOB "v8", "v9", "v10", "v11", "v12", "v13", "v14", "v15", "v24", "v25", "v26", "v27", "v28", "v29", "v30", "v31", "v136", "v137", "v138", "v139", "v140", "v141", "v142", "v143", "v144", "v145", "v146", "v147", "v148", "v149", "v150", "v151", "v152", "v153", "v154", "v155", "v156", "v157", "v158", "v159", "v160", "v161", "v162", "v163", "v164", "v165", "v166", "v167", "v168", "v169", "v170", "v171", "v172", "v173", "v174", "v175", "v176", "v177", "v178", "v179", "v180", "v181", "v182", "v183", "v184", "v185", "v186", "v187", "v188", "v189", "v190", "v191"
constexpr int NCOL = 161, COL_I2 = 64 * 264, COL_LDS = COL_I2 + 8 * 2064;
__device__ __forceinline__ void job_col(int wv, unsigned char* ws, rsrc_t rs, unsigned lds, int jb, int go) {
  const int lane = lane_id(), g = lane >> 4, r = lane & 15, j = wv * 64 + lane;
  const int xin = (int)OFF_X1T + (jb == 160 ? 0 : jb) * S_ * 8 + j * 16;
  v4u x[4], wf[4];
  if (go < 0) {
#pragma unroll
    for (int q = 0; q < 4; ++q) x[q] = ldc(rs, xin + q * 4096);
  }
#pragma unroll
  for (int q = 0; q < 4; ++q) wf[q] = ldp(rs, (int)OFF_WT + (jb == 160 ? 320 : 2 * jb + (q >> 1)) * S_ * 4 + ((wv * 2 + (q & 1)) * 64 + lane) * 16);
  v32u k0, k1, k2;
  v8u k3, ad = (unsigned)lane;
  {
    const int n = 16 * wv + r;
    cis8(k0, n * (512 * (g & 1) + 256 * (g >> 1)), n * 32, n * 128, (r & 1) ? 0x80008000u : 0u);
    cis8(k1, r * g * 256, r * 64, r * 1024, 0x80000000u);
#pragma unroll
    for (int q = 0; q < 8; ++q) {
      const int i = -n * sigma(g, q), k = (r + 16 * (q >> 2)) * (16 * wv + 4 * g + (q & 3));
      const float sc = 1.0f / ((float)S_ * (float)D_);
      k0[16 + 2 * q] = __builtin_bit_cast(unsigned, cosi(i)); k0[17 + 2 * q] = __builtin_bit_cast(unsigned, sini(i));
      k1[16 + 2 * q] = __builtin_bit_cast(unsigned, cosi(k) * sc); k1[17 + 2 * q] = __builtin_bit_cast(unsigned, sini(k) * sc);
      k3[q] = k1[8 + q] ^ 0x80008000u;
    }
    ad[0] = lds + 264 * (16 * (g & 1) + 8 * (g >> 1) + (r >> 2)) + 32 * (r & 3);
    ad[1] = lds + COL_I2 + (2 * (r >> 3) + (r & 1)) * 2064 + ((r >> 1) & 3) * 512 + (g & 1) * 8;
    ad[2] = ad[1] + 256 + (((4 * (wv >> 1) + 2 * (g >> 1) + (wv & 1)) ^ ((r & 6) + 1)) << 4);
    ad[1] += ((4 * (wv >> 1) + 2 * (g >> 1) + (wv & 1)) ^ (r & 6)) << 4;
    ad[3] = lds + COL_I2 + r * 128 + ((g ^ (r >> 1)) << 4);
    ad[4] = lds + COL_I2 + r * 128 + (((4 + g) ^ (r >> 1)) << 4);
    ad[5] = (int)OFF_ZT + (jb * S_ + 32 * (16 * wv + r) + 2 * g) * 8;
    asm volatile("" : "+{v[32:63]}"(k0), "+{v[64:95]}"(k1), "+{v[128:135]}"(k3), "+{v[16:23]}"(ad));
  }
  if (go < 0) __builtin_amdgcn_sched_barrier(0);
  const unsigned sh = jb == 160 ? 16u : 0u, mk = jb == 0 ? 0xFFFFu : ~0u, d = lds + (j >> 4) * 264 + r * 16;
#pragma unroll
  for (int p = 0; p < 2; ++p) {
    if (p == (go < 0)) {
#pragma unroll
      for (int q = 0; q < 16; ++q) { k2[2 * q] = wf[q >> 2][q & 3] << 16; k2[2 * q + 1] = wf[q >> 2][q & 3] & 0xFFFF0000u; }
      asm volatile("" : "+{v[96:127]}"(k2));
    } else {
      if (go >= 0) {
        fan_wait<64, 20>(wv, ws, go, jb);
#pragma unroll
        for (int q = 0; q < 4; ++q) x[q] = ldc(rs, xin + q * 4096);
      }
#define CSTAMP(i)
      CSTAMP(0)
#pragma unroll
      for (int q = 0; q < 4; ++q) {
        LDS_V2(d + q * 16 * 264) = v2u{(x[q].x >> sh) & mk, x[q].y};
        LDS_V2(d + q * 16 * 264 + 8) = v2u{(x[q].z >> sh) & mk, x[q].w};
      }
    }
  }
  CSTAMP(1)
  asm volatile(COL_ASM ::"{v[32:63]}"(k0), "{v[64:95]}"(k1), "{v[96:127]}"(k2), "{v[128:135]}"(k3), "{v[16:23]}"(ad), [rs] "s"(rs) : COL_CLOB, "memory");
  CSTAMP(2)
  CSTAMP(3)
}
template <bool FINAL>
__device__ __forceinline__ void job_idft(int wv_, unsigned char* ws, int colcnt, rsrc_t rs, v4u* lds, int sg0, int sgstep, int dq, u16* out, unsigned long long* dbg) {
  const int lane = lane_id(), wv = wv_, g = lane >> 4, r = lane & 15;
  const int kt = dq * 4 + wv;
  v4u a[2][NFK];
#pragma unroll
  for (int fk = 0; fk < NFK; ++fk) {
    a[0][fk] = ldp(rs, (int)OFF_INV + ((2 * kt) * NFK + fk) * 1024 + lane * 16);
    a[1][fk] = ldp(rs, (int)OFF_INV + ((2 * kt + 1) * NFK + fk) * 1024 + lane * 16);
  }
#define ISTAMP(i)
  ISTAMP(0)
  if (!FINAL) fan_wait<64, 20>(wv_, ws, fan_off(0, 1, 0), dq);
  fan_wait<FSP, NCOL>(wv_, ws, colcnt, sg0 + dq);
  ISTAMP(1)
#pragma unroll 1
  for (int sg = sg0; sg < NSEG; sg += sgstep) {
    const int xo = (int)OFF_XR + ((sg * 2 * KT + kt) * 64 + lane) * 16;
    {
      const int jj = lane >> 3, rp = lane & 7, row = sg * 32 + (wv & 1) * 16 + 2 * rp;
      const int src = (int)OFF_ZT + (((wv >> 1) * 80 + jj) * S_ + row) * 8;
      v4u t[10];
#pragma unroll
      for (int i = 0; i < 10; ++i) t[i] = ldc(rs, src + i * 8 * S_ * 8);
      const v4u ny = ldc(rs, (int)OFF_ZT + (160 * S_ + row) * 8);
      if (wv < 2 && jj == 0) {
        t[0].x = (t[0].x & 0xFFFFu) | (ny.x << 16);
        t[0].z = (t[0].z & 0xFFFFu) | (ny.z << 16);
      }
      v2u* dst = (v2u*)(lds + ((wv & 1) * NFK + (wv >> 1) * 10) * 64 + (jj >> 1) * 16 + 2 * rp) + (jj & 1);
#pragma unroll
      for (int i = 0; i < 10; ++i) { dst[i * 128] = v2u{t[i].x, t[i].y}; dst[i * 128 + 2] = v2u{t[i].z, t[i].w}; }
    }
    const v4u x0 = ldc(rs, xo), x1 = ldc(rs, xo + KT * 1024);
    f32x4 acc[2][2] = {{{0, 0, 0, 0}, {0, 0, 0, 0}}, {{0, 0, 0, 0}, {0, 0, 0, 0}}};
    __syncthreads();
    ISTAMP(2)
#pragma unroll
    for (int k0 = 0; k0 < NFK; k0 += 5) {
      v4u b0[5], b1[5];
#pragma unroll
      for (int d = 0; d < 5; ++d) { b0[d] = lds[(k0 + d) * 64 + lane]; b1[d] = lds[(NFK + k0 + d) * 64 + lane]; }
#pragma unroll
      for (int d = 0; d < 5; ++d) {
        acc[0][0] = mfma(a[0][k0 + d], b0[d], acc[0][0]); acc[0][1] = mfma(a[0][k0 + d], b1[d], acc[0][1]);
        acc[1][0] = mfma(a[1][k0 + d], b0[d], acc[1][0]); acc[1][1] = mfma(a[1][k0 + d], b1[d], acc[1][1]);
      }
    }
#pragma unroll
    for (int rt = 0; rt < 2; ++rt) {
      const v4u x = rt ? x1 : x0;
      v4u y;
      y.x = pk(lo_f(x.x) + bfr(acc[0][rt].x), hi_f(x.x) + bfr(acc[0][rt].y));
      y.y = pk(lo_f(x.y) + bfr(acc[0][rt].z), hi_f(x.y) + bfr(acc[0][rt].w));
      y.z = pk(lo_f(x.z) + bfr(acc[1][rt].x), hi_f(x.z) + bfr(acc[1][rt].y));
      y.w = pk(lo_f(x.w) + bfr(acc[1][rt].z), hi_f(x.w) + bfr(acc[1][rt].w));
      if (FINAL) {
        u16* op = out + (size_t)(sg * 32 + rt * 16 + r) * D_ + 32 * kt + 4 * g;
        *(v2u*)op = v2u{y.x, y.y};
        *(v2u*)(op + 16) = v2u{y.z, y.w};
      } else {
        stc(rs, xo + rt * KT * 1024, y);
        stat_put(lds, wv, rt, lane, y);
      }
    }
    ISTAMP(3)
    if (!FINAL) {
      stat_store(rs, lds, wv, lane, sg, dq);
      if (sg < 12) fan_signal<64>(wv_, ws, fan_off(1, 1, 0), sg * 5 + dq);
      else dep_signal(wv_, ws, cnt_off(1, CNT_IDFT, sg >> 1));
    }
  }
}

constexpr int JOB_LDS = 4 * NFK * 1024;
extern __shared__ __attribute__((aligned(16))) unsigned char dyn_lds[];
constexpr int FIN_LDS = 2 * NFK * 1024;
static_assert(FIN_LDS >= COL_LDS, "LDS");
__device__ __forceinline__ unsigned lds_base() { return (unsigned)(uintptr_t)(__attribute__((address_space(3))) unsigned char*)dyn_lds; }
#define FIN_DELAY 1
__global__ void __launch_bounds__(256, 2) k_fin(unsigned char* ws, u16* out) {
  const int wv_ = __builtin_amdgcn_readfirstlane(threadIdx.x >> 6), b = blockIdx.x;
  const rsrc_t rs = ws_rsrc(ws);
  PH(14)
  if (b < NCOL) {
    job_col(wv_, ws, rs, lds_base(), b, -1);
    fan_signal<FSP>(wv_, ws, fan_off(0, 0, 0), b);
    PH(13)
  } else {
    for (int i = 0; i < FIN_DELAY; ++i) __builtin_amdgcn_s_sleep(32);
    job_idft<true>(wv_, ws, fan_off(0, 0, 0), rs, (v4u*)dyn_lds, (b - NCOL) / 5, NSEG, (b - NCOL) % 5, out, nullptr);
    PH(15)
  }
}

constexpr unsigned SPIN_CAP = 1u << 16;

#define VM_LD128(dst, off, so, IMM) asm volatile("buffer_load_dwordx4 %0, %1, %2, %3 offen offset:" #IMM " sc1" : "=v"(dst) : "v"(off), "s"(rs), "s"(so) : "memory")
#define VM_LD32(dst, off, so) asm volatile("buffer_load_dword %0, %1, %2, %3 offen sc1" : "=v"(dst) : "v"(off), "s"(rs), "s"(so) : "memory")
#define VM_ST32(val, off, so) asm volatile("buffer_store_dword %0, %1, %2, %3 offen sc1" ::"v"(val), "v"(off), "s"(rs), "s"(so) : "memory")
#define VM_ST128(val, off, so) asm volatile("buffer_store_dwordx4 %0, %1, %2, %3 offen sc1\n\ts_nop 1" ::"v"(val), "v"(off), "s"(rs), "s"(so) : "memory")
#define VM_WAIT(N) asm volatile("s_waitcnt vmcnt(" #N ")" ::: "memory")
#define VM_WAIT1(N, a) asm volatile("s_waitcnt vmcnt(" #N ")" : "+v"(a)::"memory")
__device__ __forceinline__ v4u make_rs(const void* p, unsigned bytes) {
  const unsigned long long a = (unsigned long long)p;
  v4u r;
  r.x = (unsigned)__builtin_amdgcn_readfirstlane((unsigned)a);
  r.y = (unsigned)__builtin_amdgcn_readfirstlane((unsigned)(a >> 32) & 0xFFFFu);
  r.z = bytes;
  r.w = 0x00020000u;
  return r;
}
__device__ __forceinline__ unsigned rtc() { return (unsigned)__builtin_amdgcn_s_memrealtime(); }
struct Clk0 { unsigned r, c; };
__device__ __forceinline__ Clk0 clk_sample() {
  unsigned long long r, c;
  asm volatile("s_memrealtime %0\n\ts_memtime %1\n\ts_waitcnt lgkmcnt(0)" : "=s"(r), "=s"(c)::"memory");
  return Clk0{(unsigned)r, (unsigned)c};
}
__device__ __forceinline__ v4u clk_init(Clk0 a) {
  const Clk0 b = clk_sample();
  unsigned k = 1400u;
  if (b.r - a.r >= 100u) k = (unsigned)((float)(b.r - a.r) * 32768.f / (float)(b.c - a.c));
  k = (unsigned)__builtin_amdgcn_readfirstlane((int)min(max(k, 1200u), 3000u));
  asm volatile("" : "+s"(k));
  return v4u{b.r * 8u + 16u, b.c, k, 0u};
}
#define LDSBAR() asm volatile("s_waitcnt lgkmcnt(0)\n\ts_barrier" ::: "memory")
#define MEMPIN() asm volatile("" ::: "memory")

__device__ __forceinline__ int wave_imax(int v) {
  asm volatile("s_nop 1\n\tv_max_i32_dpp %0, %0, %0 quad_perm:[1,0,3,2] row_mask:0xf bank_mask:0xf\n\t"
               "s_nop 1\n\tv_max_i32_dpp %0, %0, %0 quad_perm:[2,3,0,1] row_mask:0xf bank_mask:0xf\n\t"
               "s_nop 1\n\tv_max_i32_dpp %0, %0, %0 row_half_mirror row_mask:0xf bank_mask:0xf\n\t"
               "s_nop 1\n\tv_max_i32_dpp %0, %0, %0 row_mirror row_mask:0xf bank_mask:0xf\n\ts_nop 0"
               : "+v"(v));
  const int a = __builtin_amdgcn_readlane(v, 0), b = __builtin_amdgcn_readlane(v, 16), c = __builtin_amdgcn_readlane(v, 32),
            d = __builtin_amdgcn_readlane(v, 48);
  return max(max(a, b), max(c, d));
}

constexpr int OOB = 0x7FFFFFF0;
__device__ __forceinline__ int ladder(int x, int i) { return copy_off(((x + (i >> 1)) & 3) | ((i & 1) << 2)); }

struct RecTune { int m1, m2, dl1, rob, c2, vis, dlt, du; };

#define STAT(r)
__device__ __forceinline__ unsigned max4(v4u v) { return max(max(v.x, v.y), max(v.z, v.w)); }
template <int NAP = 6>
__device__ __forceinline__ unsigned flag_finish(const v4u rs, int off, unsigned cv) {
  for (unsigned spins = 0; __any(cv == 0xFFFFFFFFu) && spins < SPIN_CAP; ++spins) {
    __builtin_amdgcn_s_sleep(NAP);
    v4u t;
    VM_LD128(t, off, 0, 0);
    VM_WAIT1(0, t);
    cv = max4(t);
  }
  return cv;
}
struct Pool {
  unsigned long long sel, s3, ysel; int base;
  __device__ __forceinline__ int near(int s) const { return (int)((s3 >> s) & 1ull) * 16384; }
  __device__ __forceinline__ int region(int s) const { return base + s * REG + (int)((sel >> s) & 1ull) * 8192; }
};
__device__ __forceinline__ void pool_load(const unsigned char* ws, v4i (&e)[18]) {
  const int s = lane_id();
#pragma unroll
  for (int i = 0; i < 18; ++i) e[i] = ((const v4i*)(ws + OFF_SEL))[i < 16 ? (s & 60) * 4 + i : 240 + (s * 43 >> 7) * 2 + i];
}
__device__ __forceinline__ Pool pool_init(unsigned char* ws, v4i (&e)[18]) {
  v4i a = {0, 0, 0, 0};
#pragma unroll
  for (int i = 0; i < 18; ++i) asm volatile("" : "+v"(e[i]));
#pragma unroll
  for (int i = 0; i < 16; ++i) { a.x += e[i].x; a.y += (i & 4) ? -e[i].y : e[i].y; a.z += (i & 4) ? -e[i].z : e[i].z; }
  const v4i y = e[16] + e[17];
  int yx = 2 * (y.x + y.y + y.z + y.w), b = a.x > 0 ? a.z : a.y;
  const int s = lane_id(), fx = (s & 4) ? -1 : 1, fy = ((s * 43 >> 7) & 1) ? -1 : 1;
  int g = a.x * fx + yx * fy, m = abs(a.x) + abs(yx), bs = (a.x * fx > 0 ? b : 0), bt = b - bs;
#pragma unroll
  for (int k = 1; k < 64; k <<= 1) { g += __shfl_xor(g, k); m += __shfl_xor(m, k); bs += __shfl_xor(bs, k); bt += __shfl_xor(bt, k); }
  if (abs(g) * 8 > m * 7) {
    const bool odd = (a.x * fx > 0) != (g > 0);
    if (!odd || abs(a.x) < 2000) { a.x = g * fx; b = g > 0 ? bs : bt; }
    if (abs(yx) < 2000) yx = g * fy;
  }
  Pool p;
  p.sel = __ballot(a.x > 0);
  p.s3 = __ballot(b > 0) ^ 0xAAAAAAAAAAAAAAAAull;
  p.ysel = __ballot(yx > 0);
  p.base = __builtin_amdgcn_readfirstlane(pool_base(ws));
  return p;
}
constexpr int L_QT = 10240, L_KT = 12288, L_CL = 14336, L_KQ = 16384;
constexpr int L_T = 14336;
constexpr int L_M2 = L_KQ + 65536;
constexpr int REC_ST = L_M2 + 12288;
constexpr int REC_LDS = REC_ST + 4096 + 64;

#define RSTAMP(i)
#define YSTAMP(n)
#define RST_CLOB
#define P1TRACE
#define PVTRACE
#define FINTRACE
#define P1_ASM \
   "s_mov_b32 s64, -1\n\t" "s_mov_b32 s76, 0\n\t" "s_mov_b32 s77, 0\n\t" "s_mov_b32 s71, 63\n\t" "s_mov_b32 s47, 1300\n\t" "s_mov_b32 s49, 0\n\t" \
   "s_mov_b32 s51, 0\n\t" "s_mov_b32 s52, 0\n\t" "s_mov_b32 s70, 0\n\t" "s_mov_b32 s69, 0\n\t" "v_cmp_gt_i32 s[20:21], 0, v126\n\t" \
   "v_cmp_gt_i32 s[22:23], 1, v126\n\t" "v_cmp_gt_i32 s[24:25], 2, v126\n\t" "v_cmp_gt_i32 s[26:27], 3, v126\n\t" "v_cmp_gt_i32 s[28:29], 16, v126\n\t" \
   "v_cmp_gt_i32 s[30:31], 17, v126\n\t" "v_cmp_gt_i32 s[32:33], 18, v126\n\t" "v_cmp_gt_i32 s[34:35], 19, v126\n\t" "s_waitcnt vmcnt(0)\n\t" \
   "s_lshr_b64 s[72:73], %[sel], s71\n\t" "s_lshl_b32 s65, s71, 19\n\t" "s_and_b32 s72, s72, 1\n\t" "s_add_u32 s65, s65, %[pool]\n\t" \
   "s_lshl_b32 s72, s72, 13\n\t" "s_add_u32 s65, s65, s72\n\t" "s_lshr_b64 s[72:73], %[s3], s71\n\t" "s_and_b32 s54, s72, 1\n\t" \
   "s_lshl_b32 s54, s54, 14\n\t" "s_memtime s[62:63]\n\t" "s_memrealtime s[60:61]\n\t" "40:\n\t" "s_cmp_eq_u32 s64, 0\n\t" "s_cbranch_scc1 95f\n\t" \
   "96:\n\t" "s_add_i32 s72, s64, 2\n\t" "s_min_i32 s72, s72, 63\n\t" "s_lshl_b32 s59, s72, 12\n\t" "s_lshl_b32 s79, s72, 13\n\t" \
   "buffer_load_dword v108, v139, %[rs], s65 offen sc1\n\t" "s_cmp_gt_u32 %[wv], 1\n\t" "s_waitcnt vmcnt(4)\n\t" "s_cbranch_scc1 44f\n\t" \
   "buffer_load_dword v15, v140, %[rs], s59 offen sc1\n\t" "ds_write_b128 v127, v[16:19] offset:0\n\t" "ds_write_b128 v127, v[20:23] offset:1024\n\t" \
   "ds_write_b128 v127, v[24:27] offset:2048\n\t" "ds_write_b128 v127, v[28:31] offset:3072\n\t" "s_waitcnt lgkmcnt(0)\n\t" \
   "s_sub_u32 s74, s62, s57\n\t" "s_mul_i32 s74, s74, s58\n\t" "s_lshr_b32 s74, s74, 12\n\t" "s_add_u32 s74, s74, s56\n\t" "s_lshl_b32 s60, s60, 3\n\t" \
   "s_sub_u32 s75, s74, s60\n\t" "s_cmp_lt_u32 s75, 32\n\t" "s_cbranch_scc1 38f\n\t" "s_cmp_lt_i32 s75, 0\n\t" "s_cselect_b32 s61, 0, 31\n\t" \
   "s_add_u32 s74, s60, s61\n\t" "s_sub_i32 s75, s61, s75\n\t" "s_add_i32 s75, s75, 1\n\t" "s_ashr_i32 s75, s75, 1\n\t" "s_min_i32 s75, s75, 8\n\t" \
   "s_max_i32 s75, s75, -8\n\t" "s_mov_b32 s56, s74\n\t" "s_mov_b32 s57, s62\n\t" "s_add_u32 s58, s58, s75\n\t" "38:\n\t" "s_min_u32 s72, s70, 1\n\t" \
   "s_and_b32 s71, s74, -4\n\t" "s_or_b32 s71, s71, s72\n\t" "v_mov_b32 v111, s71\n\t" "buffer_store_dword v111, v131, %[rs], s65 offen sc1\n\t" \
   P1TRACE "s_barrier\n\t" "ds_read_b128 v[160:163], v122 offset:0\n\t" "ds_read_b128 v[164:167], v122 offset:1024\n\t" \
   "ds_read_b128 v[168:171], v122 offset:2048\n\t" "ds_read_b128 v[172:175], v122 offset:3072\n\t" "ds_read_b128 v[176:179], v122 offset:4096\n\t" \
   "ds_read_b128 v[180:183], v122 offset:5120\n\t" "ds_read_b128 v[184:187], v122 offset:6144\n\t" "ds_read_b128 v[188:191], v122 offset:7168\n\t" \
   "s_cmp_lt_i32 s64, 1\n\t" "s_cselect_b32 s72, 0x7ffffff0, 0\n\t" "s_max_i32 s71, s64, 1\n\t" "v_or_b32 v109, s72, v143\n\t" \
   "s_lshl_b32 s71, s71, 12\n\t" "v_mov_b32 v110, 1\n\t" "s_sub_u32 s71, s71, 4096\n\t" "buffer_store_dword v110, v109, %[rs], s71 offen sc1\n\t" \
   "s_add_u32 s48, s69, s47\n\t" "s_cmp_eq_u32 s70, 0\n\t" "s_cselect_b32 s48, s48, -1\n\t" "s_mov_b32 s66, s65\n\t" "s_add_i32 s64, s64, 1\n\t" \
   "s_and_b32 s71, s64, 63\n\t" "s_lshr_b64 s[72:73], %[sel], s71\n\t" "s_lshl_b32 s65, s71, 19\n\t" "s_and_b32 s72, s72, 1\n\t" \
   "s_add_u32 s65, s65, %[pool]\n\t" "s_lshl_b32 s72, s72, 13\n\t" "s_add_u32 s65, s65, s72\n\t" "s_xor_b32 s67, s54, %[lad0]\n\t" \
   "s_xor_b32 s68, s67, 0x4000\n\t" "s_add_u32 s67, s67, s66\n\t" "s_add_u32 s68, s68, s66\n\t" "s_lshr_b64 s[72:73], %[s3], s71\n\t" \
   "s_and_b32 s54, s72, 1\n\t" "s_lshl_b32 s54, s54, 14\n\t" "s_cmp_eq_u32 s64, 0\n\t" "s_cselect_b32 s70, 1, 0\n\t" "s_and_b32 s55, %[wv], 1\n\t" \
   "s_lshl_b32 s55, s55, 14\n\t" "s_xor_b32 s55, s55, s54\n\t" "s_xor_b32 s50, s55, 0x4000\n\t" "s_add_u32 s55, s55, s65\n\t" \
   "s_add_u32 s50, s50, s65\n\t" "v_mov_b32 v48, 0\n\t" "v_mov_b32 v49, 0\n\t" "v_mov_b32 v50, 0\n\t" "v_mov_b32 v51, 0\n\t" "v_mov_b32 v52, 0\n\t" \
   "v_mov_b32 v53, 0\n\t" "v_mov_b32 v54, 0\n\t" "v_mov_b32 v55, 0\n\t" "v_mov_b32 v56, 0\n\t" "v_mov_b32 v57, 0\n\t" "v_mov_b32 v58, 0\n\t" \
   "v_mov_b32 v59, 0\n\t" "v_mov_b32 v60, 0\n\t" "v_mov_b32 v61, 0\n\t" "v_mov_b32 v62, 0\n\t" "v_mov_b32 v63, 0\n\t" "s_waitcnt vmcnt(2)\n\t" \
   "v_cmp_eq_u32 vcc, -1, v15\n\t" "s_nop 0\n\t" "s_cbranch_vccnz 141f\n\t" "41:\n\t" "s_branch 48f\n\t" "44:\n\t" \
   "v_mfma_f32_16x16x32_bf16 v[32:35], v[160:163], v[16:19], 0\n\t" "s_max_i32 s51, s64, 0\n\t" "s_lshl_b32 s52, s51, 16\n\t" \
   "s_lshl_b32 s51, s51, 14\n\t" "v_mfma_f32_16x16x32_bf16 v[36:39], v[176:179], v[16:19], 0\n\t" "s_cmp_lt_i32 s64, 0\n\t" \
   "s_cselect_b32 s49, 0x7ffffff0, 0\n\t" "s_add_i32 s72, s64, 1\n\t" "v_mfma_f32_16x16x32_bf16 v[32:35], v[164:167], v[20:23], v[32:35]\n\t" \
   "s_min_i32 s72, s72, 63\n\t" "s_lshl_b32 s79, s72, 13\n\t" "v_mfma_f32_16x16x32_bf16 v[36:39], v[180:183], v[20:23], v[36:39]\n\t" \
   "v_mfma_f32_16x16x32_bf16 v[32:35], v[168:171], v[24:27], v[32:35]\n\t" "s_waitcnt lgkmcnt(0)\n\t" \
   "v_mfma_f32_16x16x32_bf16 v[36:39], v[184:187], v[24:27], v[36:39]\n\t" "s_sub_u32 s74, s62, s57\n\t" "s_mul_i32 s74, s74, s58\n\t" \
   "s_lshr_b32 s74, s74, 12\n\t" "v_mfma_f32_16x16x32_bf16 v[32:35], v[172:175], v[28:31], v[32:35]\n\t" "s_add_u32 s74, s74, s56\n\t" \
   "s_lshl_b32 s60, s60, 3\n\t" "s_sub_u32 s75, s74, s60\n\t" "v_mfma_f32_16x16x32_bf16 v[36:39], v[188:191], v[28:31], v[36:39]\n\t" \
   "s_cmp_lt_u32 s75, 32\n\t" "s_cbranch_scc1 39f\n\t" "s_cmp_lt_i32 s75, 0\n\t" "s_cselect_b32 s61, 0, 31\n\t" "s_add_u32 s74, s60, s61\n\t" \
   "s_sub_i32 s75, s61, s75\n\t" "s_add_i32 s75, s75, 1\n\t" "s_ashr_i32 s75, s75, 1\n\t" "s_min_i32 s75, s75, 8\n\t" "s_max_i32 s75, s75, -8\n\t" \
   "s_mov_b32 s56, s74\n\t" "s_mov_b32 s57, s62\n\t" "s_add_u32 s58, s58, s75\n\t" "39:\n\t" "s_barrier\n\t" \
   "v_mfma_f32_16x16x32_bf16 v[40:43], v[144:147], v[16:19], 0\n\t" "s_min_u32 s72, s70, 1\n\t" "s_and_b32 s71, s74, -4\n\t" \
   "s_or_b32 s71, s71, s72\n\t" "v_mov_b32 v111, s71\n\t" "v_mfma_f32_16x16x32_bf16 v[40:43], v[148:151], v[20:23], v[40:43]\n\t" \
   "buffer_store_dword v111, v131, %[rs], s65 offen sc1\n\t" P1TRACE "v_mov_b32 v48, 0\n\t" "v_mov_b32 v49, 0\n\t" \
   "v_mfma_f32_16x16x32_bf16 v[40:43], v[152:155], v[24:27], v[40:43]\n\t" "v_mov_b32 v50, 0\n\t" "v_mov_b32 v51, 0\n\t" "v_mov_b32 v52, 0\n\t" \
   "v_mov_b32 v53, 0\n\t" "v_mfma_f32_16x16x32_bf16 v[40:43], v[156:159], v[28:31], v[40:43]\n\t" "v_mov_b32 v54, 0\n\t" "v_mov_b32 v55, 0\n\t" \
   "v_cndmask_b32 v32, v32, v125, s[20:21]\n\t" "v_cndmask_b32 v33, v33, v125, s[22:23]\n\t" "v_cndmask_b32 v34, v34, v125, s[24:25]\n\t" \
   "v_cndmask_b32 v35, v35, v125, s[26:27]\n\t" "v_cndmask_b32 v36, v36, v125, s[28:29]\n\t" "v_cndmask_b32 v37, v37, v125, s[30:31]\n\t" \
   "v_cndmask_b32 v38, v38, v125, s[32:33]\n\t" "v_cndmask_b32 v39, v39, v125, s[34:35]\n\t" "v_exp_f32 v64, v32\n\t" "s_add_u32 s48, s69, s47\n\t" \
   "s_cmp_eq_u32 s70, 0\n\t" "s_cselect_b32 s48, s48, -1\n\t" "v_exp_f32 v65, v33\n\t" "s_mov_b32 s66, s65\n\t" "s_add_i32 s64, s64, 1\n\t" \
   "s_and_b32 s71, s64, 63\n\t" "v_exp_f32 v66, v34\n\t" "s_lshr_b64 s[72:73], %[sel], s71\n\t" "s_lshl_b32 s65, s71, 19\n\t" \
   "s_and_b32 s72, s72, 1\n\t" "v_exp_f32 v67, v35\n\t" "s_add_u32 s65, s65, %[pool]\n\t" "s_lshl_b32 s72, s72, 13\n\t" "s_add_u32 s65, s65, s72\n\t" \
   "v_exp_f32 v68, v36\n\t" "s_xor_b32 s67, s54, %[lad0]\n\t" "s_xor_b32 s68, s67, 0x4000\n\t" "s_add_u32 s67, s67, s66\n\t" "v_exp_f32 v69, v37\n\t" \
   "s_add_u32 s68, s68, s66\n\t" "s_lshr_b64 s[72:73], %[s3], s71\n\t" "s_and_b32 s54, s72, 1\n\t" "v_exp_f32 v70, v38\n\t" \
   "s_lshl_b32 s54, s54, 14\n\t" "s_cmp_eq_u32 s64, 0\n\t" "s_cselect_b32 s70, 1, 0\n\t" "v_exp_f32 v71, v39\n\t" "s_and_b32 s55, %[wv], 1\n\t" \
   "s_lshl_b32 s55, s55, 14\n\t" "s_xor_b32 s55, s55, s54\n\t" "v_exp_f32 v72, v40\n\t" "s_xor_b32 s50, s55, 0x4000\n\t" "s_add_u32 s55, s55, s65\n\t" \
   "s_add_u32 s50, s50, s65\n\t" "v_exp_f32 v73, v41\n\t" "v_mov_b32 v56, 0\n\t" "v_mov_b32 v57, 0\n\t" "v_mov_b32 v58, 0\n\t" "v_exp_f32 v74, v42\n\t" \
   "v_mov_b32 v59, 0\n\t" "v_mov_b32 v60, 0\n\t" "v_mov_b32 v61, 0\n\t" "v_exp_f32 v75, v43\n\t" "v_mov_b32 v62, 0\n\t" "v_mov_b32 v63, 0\n\t" \
   "v_add_f32 v80, v64, v65\n\t" "v_add_f32 v81, v66, v67\n\t" "v_add_f32 v82, v68, v69\n\t" "v_add_f32 v83, v70, v71\n\t" \
   "v_add_f32 v84, v72, v73\n\t" "v_add_f32 v85, v74, v75\n\t" "v_add_f32 v80, v80, v81\n\t" "v_add_f32 v82, v82, v83\n\t" \
   "v_add_f32 v84, v84, v85\n\t" "v_add_f32 v80, v80, v82\n\t" "v_fma_f32 v82, v80, 2.0, v84\n\t" "v_fma_f32 v80, v80, 2.0, v84\n\t" \
   "ds_read_b128 v[160:163], v122 offset:0\n\t" "ds_read_b128 v[164:167], v122 offset:1024\n\t" "v_permlane16_swap_b32 v80, v82\n\t" \
   "v_add_f32 v81, v80, v82\n\t" "v_add_f32 v80, v80, v82\n\t" "ds_read_b128 v[168:171], v122 offset:2048\n\t" \
   "ds_read_b128 v[172:175], v122 offset:3072\n\t" "v_permlane32_swap_b32 v80, v81\n\t" "v_add_f32 v80, v80, v81\n\t" "v_rcp_f32 v90, v80\n\t" \
   "v_add_u32 v81, 0xe0800000, v80\n\t" "v_add_f32 v90, v90, v90\n\t" "v_cmp_lt_u32 vcc, 0x40000000, v81\n\t" \
   "ds_read_b128 v[176:179], v122 offset:4096\n\t" "ds_read_b128 v[180:183], v122 offset:5120\n\t" "ds_read_b128 v[184:187], v122 offset:6144\n\t" \
   "ds_read_b128 v[188:191], v122 offset:7168\n\t" "s_cbranch_vccnz 74f\n\t" "75:\n\t" "v_pk_mul_f32 v[64:65], v[64:65], v[90:91] op_sel_hi:[1,0]\n\t" \
   "v_pk_mul_f32 v[66:67], v[66:67], v[90:91] op_sel_hi:[1,0]\n\t" "v_pk_mul_f32 v[68:69], v[68:69], v[90:91] op_sel_hi:[1,0]\n\t" \
   "v_pk_mul_f32 v[70:71], v[70:71], v[90:91] op_sel_hi:[1,0]\n\t" "v_pk_mul_f32 v[72:73], v[72:73], v[90:91] op_sel_hi:[1,0]\n\t" \
   "v_pk_mul_f32 v[74:75], v[74:75], v[90:91] op_sel_hi:[1,0]\n\t" "v_cvt_pk_bf16_f32 v44, v64, v65\n\t" "v_cvt_pk_bf16_f32 v46, v66, v67\n\t" \
   "v_cvt_pk_bf16_f32 v45, v68, v69\n\t" "v_cvt_pk_bf16_f32 v47, v70, v71\n\t" "v_cvt_pk_bf16_f32 v32, v72, v73\n\t" \
   "v_cvt_pk_bf16_f32 v33, v74, v75\n\t" "s_waitcnt vmcnt(1)\n\t" "48:\n\t" "v_readlane_b32 s69, v108, 0\n\t" "v_readlane_b32 s71, v108, 1\n\t" \
   "s_cmp_eq_u32 s64, 64\n\t" "s_cbranch_scc1 90f\n\t" "s_cmp_eq_u32 s69, -1\n\t" "s_cbranch_scc1 63f\n\t" "s_cmp_eq_u32 s71, -1\n\t" \
   "s_cselect_b32 s47, s47, s71\n\t" "50:\n\t" "s_add_u32 s73, s69, %[m1]\n\t" RSTAMP(5) "51:\n\t" "s_memrealtime s[60:61]\n\t" \
   "s_memtime s[62:63]\n\t" "s_waitcnt lgkmcnt(0)\n\t" "s_sub_u32 s74, s62, s57\n\t" "s_mul_i32 s74, s74, s58\n\t" "s_lshr_b32 s74, s74, 12\n\t" \
   "s_add_u32 s74, s74, s56\n\t" "s_lshl_b32 s60, s60, 3\n\t" "s_sub_u32 s75, s74, s60\n\t" "s_cmp_lt_u32 s75, 32\n\t" "s_cbranch_scc1 52f\n\t" \
   "s_cmp_lt_i32 s75, 0\n\t" "s_cselect_b32 s61, 0, 31\n\t" "s_add_u32 s74, s60, s61\n\t" "s_sub_i32 s75, s61, s75\n\t" "s_add_i32 s75, s75, 1\n\t" \
   "s_ashr_i32 s75, s75, 1\n\t" "s_min_i32 s75, s75, 8\n\t" "s_max_i32 s75, s75, -8\n\t" "s_mov_b32 s56, s74\n\t" "s_mov_b32 s57, s62\n\t" \
   "s_add_u32 s58, s58, s75\n\t" "52:\n\t" "s_sub_i32 s74, s73, s74\n\t" "s_cmp_le_i32 s74, 150\n\t" "s_cbranch_scc1 53f\n\t" \
   "s_cmp_lt_i32 s74, 260\n\t" "s_cbranch_scc1 51b\n\t" "s_sleep 2\n\t" "s_cmp_lt_i32 s74, 32000\n\t" "s_cbranch_scc1 51b\n\t" "s_mov_b32 s74, 0\n\t" \
   "53:\n\t" "s_sub_i32 s74, s74, 56\n\t" "s_max_i32 s74, s74, 0\n\t" "s_mul_i32 s74, s74, 23\n\t" "s_lshr_b32 s74, s74, 5\n\t" \
   "s_and_b32 s74, s74, -4\n\t" "s_getpc_b64 s[60:61]\n\t" "54:\n\t" "s_sub_u32 s74, 55f-54b, s74\n\t" "s_add_u32 s60, s60, s74\n\t" \
   "s_addc_u32 s61, s61, 0\n\t" "s_setpc_b64 s[60:61]\n\t" "s_nop 3\n\t" "s_nop 3\n\t" "s_nop 3\n\t" "s_nop 3\n\t" "s_nop 3\n\t" "s_nop 3\n\t" \
   "s_nop 3\n\t" "s_nop 3\n\t" "s_nop 3\n\t" "s_nop 3\n\t" "s_nop 3\n\t" "s_nop 3\n\t" "s_nop 3\n\t" "s_nop 3\n\t" "s_nop 3\n\t" "s_nop 3\n\t" \
   "s_nop 3\n\t" "s_nop 3\n\t" "s_nop 3\n\t" "s_nop 3\n\t" "s_nop 3\n\t" "s_nop 3\n\t" "s_nop 3\n\t" "s_nop 3\n\t" "55:\n\t" "20:\n\t" RSTAMP(4) \
   "buffer_load_dwordx4 v[64:67], v128, %[rs], s67 offen sc1\n\t" "buffer_load_dwordx4 v[68:71], v129, %[rs], s67 offen sc1\n\t" \
   "buffer_load_dwordx4 v[72:75], v130, %[rs], s67 offen sc1\n\t" "s_nop 7\n\t" "s_nop 7\n\t" "s_nop 7\n\t" "s_cmp_lg_u32 s70, 0\n\t" \
   "s_cbranch_scc1 27f\n\t" "buffer_load_dwordx4 v[16:19], v138, %[rs], s79 offen offset:0 sc1\n\t" \
   "buffer_load_dwordx4 v[20:23], v138, %[rs], s79 offen offset:1024 sc1\n\t" \
   "buffer_load_dwordx4 v[24:27], v138, %[rs], s79 offen offset:2048 sc1\n\t" \
   "buffer_load_dwordx4 v[28:31], v138, %[rs], s79 offen offset:3072 sc1\n\t" "v_or_b32 v108, s49, v141\n\t" "v_or_b32 v109, s49, v142\n\t" \
   "buffer_store_dwordx4 v[44:47], v108, %[rs], s52 offen sc1\n\t" "buffer_store_dwordx2 v[32:33], v109, %[rs], s51 offen sc1\n\t" "28:\n\t" RSTAMP(3) \
   "s_waitcnt vmcnt(8)\n\t" "ds_write_b128 v112, v[64:67]\n\t" "v_smfmac_f32_16x16x64_bf16 v[48:51], v[64:67], a[0:7], v134\n\t" \
   "s_waitcnt vmcnt(7)\n\t" "ds_write_b128 v113, v[68:71]\n\t" "s_waitcnt vmcnt(6)\n\t" "ds_write_b128 v114, v[72:75]\n\t" RSTAMP(7) "2:\n\t" \
   "v_smfmac_f32_16x16x64_bf16 v[52:55], v[64:67], a[64:71], v134\n\t" "v_max3_u32 v110, v64, v66, v68\n\t" "v_max3_u32 v111, v70, v72, v74\n\t" \
   "v_smfmac_f32_16x16x64_bf16 v[48:51], v[68:71], a[8:15], v134\n\t" "v_max_u32 v110, v110, v111\n\t" "v_cmp_eq_u32 vcc, -1, v110\n\t" \
   "v_smfmac_f32_16x16x64_bf16 v[52:55], v[68:71], a[72:79], v134\n\t" "s_cbranch_vccnz 8f\n\t" \
   "v_smfmac_f32_16x16x64_bf16 v[48:51], v[72:75], a[16:23], v134\n\t" "s_waitcnt lgkmcnt(0)\n\t" "s_barrier\n\t" RSTAMP(0) \
   "ds_read_b128 v[76:79], v115\n\t" "ds_read_b128 v[80:83], v116\n\t" "v_smfmac_f32_16x16x64_bf16 v[52:55], v[72:75], a[80:87], v134\n\t" \
   "ds_read_b128 v[84:87], v117\n\t" "ds_read_b128 v[88:91], v118\n\t" "v_smfmac_f32_16x16x64_bf16 v[56:59], v[64:67], a[128:135], v134\n\t" \
   "ds_read_b128 v[92:95], v119\n\t" "ds_read_b128 v[96:99], v120\n\t" "v_smfmac_f32_16x16x64_bf16 v[60:63], v[64:67], a[192:199], v134\n\t" \
   "ds_read_b128 v[100:103], v121\n\t" "v_smfmac_f32_16x16x64_bf16 v[56:59], v[68:71], a[136:143], v134\n\t" \
   "v_smfmac_f32_16x16x64_bf16 v[60:63], v[68:71], a[200:207], v134\n\t" "s_waitcnt lgkmcnt(6)\n\t" \
   "v_smfmac_f32_16x16x64_bf16 v[48:51], v[76:79], a[24:31], v134\n\t" "v_smfmac_f32_16x16x64_bf16 v[52:55], v[76:79], a[88:95], v134\n\t" \
   "s_waitcnt lgkmcnt(5)\n\t" "v_smfmac_f32_16x16x64_bf16 v[48:51], v[80:83], a[32:39], v134\n\t" \
   "v_smfmac_f32_16x16x64_bf16 v[52:55], v[80:83], a[96:103], v134\n\t" "s_waitcnt lgkmcnt(4)\n\t" \
   "v_smfmac_f32_16x16x64_bf16 v[48:51], v[84:87], a[40:47], v134\n\t" "v_smfmac_f32_16x16x64_bf16 v[52:55], v[84:87], a[104:111], v134\n\t" \
   "s_waitcnt lgkmcnt(3)\n\t" "v_smfmac_f32_16x16x64_bf16 v[48:51], v[88:91], a[48:55], v134\n\t" \
   "v_smfmac_f32_16x16x64_bf16 v[52:55], v[88:91], a[112:119], v134\n\t" "s_waitcnt lgkmcnt(2)\n\t" \
   "v_smfmac_f32_16x16x64_bf16 v[48:51], v[92:95], a[56:63], v134\n\t" "v_smfmac_f32_16x16x64_bf16 v[52:55], v[92:95], a[120:127], v134\n\t" \
   "s_waitcnt lgkmcnt(0)\n\t" "v_smfmac_f32_16x16x64_bf16 v[48:51], v[96:99], v[192:199], v134\n\t" \
   "v_smfmac_f32_16x16x64_bf16 v[48:51], v[100:103], v[200:207], v134\n\t" "v_smfmac_f32_16x16x64_bf16 v[52:55], v[96:99], v[208:215], v134\n\t" \
   "v_smfmac_f32_16x16x64_bf16 v[52:55], v[100:103], v[216:223], v134\n\t" "v_smfmac_f32_16x16x64_bf16 v[56:59], v[72:75], a[144:151], v134\n\t" \
   "v_add_f32 v48, v48, v49\n\t" "v_add_f32 v49, v50, v51\n\t" "v_smfmac_f32_16x16x64_bf16 v[60:63], v[72:75], a[208:215], v134\n\t" \
   "v_mul_f32 v48, 0x3e0293ee, v48\n\t" "v_mul_f32 v49, 0x3e0293ee, v49\n\t" "v_smfmac_f32_16x16x64_bf16 v[56:59], v[76:79], a[152:159], v134\n\t" \
   "v_add_f32 v52, v52, v53\n\t" "v_add_f32 v53, v54, v55\n\t" "v_smfmac_f32_16x16x64_bf16 v[56:59], v[80:83], a[160:167], v134\n\t" \
   "v_mul_f32 v52, 0x3e0293ee, v52\n\t" "v_mul_f32 v53, 0x3e0293ee, v53\n\t" "v_smfmac_f32_16x16x64_bf16 v[56:59], v[84:87], a[168:175], v134\n\t" \
   "v_cvt_pk_bf16_f32 v48, v48, v49\n\t" "ds_write_b32 v135, v48 offset:10240\n\t" \
   "v_smfmac_f32_16x16x64_bf16 v[56:59], v[88:91], a[176:183], v134\n\t" "v_cvt_pk_bf16_f32 v52, v52, v53\n\t" \
   "ds_write_b32 v135, v52 offset:10496\n\t" "v_smfmac_f32_16x16x64_bf16 v[56:59], v[92:95], a[184:191], v134\n\t" \
   "v_smfmac_f32_16x16x64_bf16 v[56:59], v[96:99], v[224:231], v134\n\t" "v_smfmac_f32_16x16x64_bf16 v[56:59], v[100:103], v[232:239], v134\n\t" \
   "s_waitcnt lgkmcnt(0)\n\t" "s_barrier\n\t" "ds_read_b64_tr_b16 v[64:65], v136 offset:10240\n\t" "ds_read_b64_tr_b16 v[66:67], v136 offset:10496\n\t" \
   "v_smfmac_f32_16x16x64_bf16 v[60:63], v[76:79], a[216:223], v134\n\t" "ds_read_b64_tr_b16 v[68:69], v136 offset:10752\n\t" \
   "v_smfmac_f32_16x16x64_bf16 v[60:63], v[80:83], a[224:231], v134\n\t" "ds_read_b64_tr_b16 v[70:71], v136 offset:11008\n\t" \
   "v_smfmac_f32_16x16x64_bf16 v[60:63], v[84:87], a[232:239], v134\n\t" "ds_read_b64_tr_b16 v[72:73], v136 offset:11264\n\t" \
   "v_smfmac_f32_16x16x64_bf16 v[60:63], v[88:91], a[240:247], v134\n\t" "ds_read_b64_tr_b16 v[74:75], v136 offset:11520\n\t" \
   "v_add_f32 v56, v56, v57\n\t" "v_add_f32 v57, v58, v59\n\t" "v_smfmac_f32_16x16x64_bf16 v[60:63], v[92:95], a[248:255], v134\n\t" \
   "ds_read_b64_tr_b16 v[104:105], v136 offset:11776\n\t" "ds_read_b64_tr_b16 v[106:107], v136 offset:12032\n\t" \
   "v_smfmac_f32_16x16x64_bf16 v[60:63], v[96:99], v[240:247], v134\n\t" "v_cvt_pk_bf16_f32 v56, v56, v57\n\t" \
   "ds_write_b32 v135, v56 offset:12288\n\t" "v_smfmac_f32_16x16x64_bf16 v[60:63], v[100:103], v[248:255], v134\n\t" RSTAMP(6) \
   "s_waitcnt lgkmcnt(7)\n\t" "v_mfma_f32_16x16x32_bf16 v[48:51], v[160:163], v[64:67], 0\n\t" \
   "v_mfma_f32_16x16x32_bf16 v[52:55], v[176:179], v[64:67], 0\n\t" "s_waitcnt lgkmcnt(3)\n\t" "s_nop 0\n\t" "v_add_f32 v60, v60, v61\n\t" \
   "v_add_f32 v61, v62, v63\n\t" "v_cvt_pk_bf16_f32 v60, v60, v61\n\t" "ds_write_b32 v135, v60 offset:12544\n\t" \
   "v_mfma_f32_16x16x32_bf16 v[48:51], v[164:167], v[68:71], v[48:51]\n\t" "v_mfma_f32_16x16x32_bf16 v[52:55], v[180:183], v[68:71], v[52:55]\n\t" \
   "v_mfma_f32_16x16x32_bf16 v[48:51], v[168:171], v[72:75], v[48:51]\n\t" "v_mfma_f32_16x16x32_bf16 v[52:55], v[184:187], v[72:75], v[52:55]\n\t" \
   "s_waitcnt lgkmcnt(2)\n\t" "v_mfma_f32_16x16x32_bf16 v[48:51], v[172:175], v[104:107], v[48:51]\n\t" \
   "v_mfma_f32_16x16x32_bf16 v[52:55], v[188:191], v[104:107], v[52:55]\n\t" "s_waitcnt lgkmcnt(0)\n\t" "s_barrier\n\t" \
   "ds_read_b64_tr_b16 v[144:145], v136 offset:12288\n\t" "ds_read_b64_tr_b16 v[146:147], v136 offset:12544\n\t" \
   "ds_read_b64_tr_b16 v[148:149], v136 offset:12800\n\t" "ds_read_b64_tr_b16 v[150:151], v136 offset:13056\n\t" \
   "ds_read_b64_tr_b16 v[152:153], v136 offset:13312\n\t" "ds_read_b64_tr_b16 v[154:155], v136 offset:13568\n\t" \
   "ds_read_b64_tr_b16 v[156:157], v136 offset:13824\n\t" "ds_read_b64_tr_b16 v[158:159], v136 offset:14080\n\t" "s_cmp_eq_u32 s64, 0\n\t" \
   "s_cbranch_scc1 92f\n\t" "93:\n\t" RSTAMP(1) "v_cndmask_b32 v48, v48, v52, %[hi8]\n\t" "v_cndmask_b32 v49, v49, v53, %[hi8]\n\t" \
   "v_cndmask_b32 v50, v50, v54, %[hi8]\n\t" "v_cndmask_b32 v51, v51, v55, %[hi8]\n\t" "v_exp_f32 v80, v48\n\t" "v_exp_f32 v81, v49\n\t" \
   "s_waitcnt lgkmcnt(6)\n\t" "v_mfma_f32_16x16x32_bf16 v[76:79], v[144:147], v[64:67], 0\n\t" "v_exp_f32 v82, v50\n\t" "v_exp_f32 v83, v51\n\t" \
   "s_waitcnt lgkmcnt(4)\n\t" "v_mfma_f32_16x16x32_bf16 v[76:79], v[148:151], v[68:71], v[76:79]\n\t" "v_add_f32 v96, v80, v81\n\t" \
   "v_add_f32 v103, v82, v83\n\t" "s_waitcnt lgkmcnt(2)\n\t" "v_mfma_f32_16x16x32_bf16 v[76:79], v[152:155], v[72:75], v[76:79]\n\t" \
   "v_add_f32 v94, v96, v103\n\t" "v_add_f32 v96, v96, v103\n\t" "s_waitcnt lgkmcnt(0)\n\t" \
   "v_mfma_f32_16x16x32_bf16 v[76:79], v[156:159], v[104:107], v[76:79]\n\t" "v_permlane32_swap_b32 v96, v94\n\t" "v_add_f32 v96, v96, v94\n\t" \
   "s_nop 1\n\t" "v_add_f32_dpp v96, v96, v96 row_ror:8 row_mask:0xf bank_mask:0xf\n\t" "s_nop 2\n\t" "v_exp_f32 v88, v76\n\t" "v_exp_f32 v89, v77\n\t" \
   "v_add_f32 v103, v96, v88\n\t" "v_exp_f32 v90, v78\n\t" "v_add_f32 v103, v103, v89\n\t" "v_exp_f32 v91, v79\n\t" "v_add_f32 v103, v103, v90\n\t" \
   "v_add_f32 v97, v103, v91\n\t" "v_add_f32 v103, v103, v91\n\t" "s_nop 1\n\t" "v_permlane16_swap_b32 v103, v97\n\t" "v_add_f32 v103, v103, v97\n\t" \
   "v_rcp_f32 v100, v103\n\t" "v_add_u32 v94, 0xe0800000, v103\n\t" "v_pk_mul_f32 v[80:81], v[80:81], v[100:101] op_sel_hi:[1,0]\n\t" \
   "v_cmp_lt_u32 vcc, 0x40000000, v94\n\t" "v_pk_mul_f32 v[82:83], v[82:83], v[100:101] op_sel_hi:[1,0]\n\t" "s_cbranch_vccnz 70f\n\t" \
   "v_cvt_pk_bf16_f32 v48, v80, v81\n\t" "v_cvt_pk_bf16_f32 v49, v82, v83\n\t" "buffer_store_dwordx2 v[48:49], v132, %[rs], s55 offen sc1\n\t" \
   "v_pk_mul_f32 v[88:89], v[88:89], v[100:101] op_sel_hi:[1,0]\n\t" "v_pk_mul_f32 v[90:91], v[90:91], v[100:101] op_sel_hi:[1,0]\n\t" \
   "v_cvt_pk_bf16_f32 v52, v88, v89\n\t" "v_cvt_pk_bf16_f32 v53, v90, v91\n\t" "71:\n\t" \
   "buffer_store_dwordx2 v[52:53], v133, %[rs], s65 offen sc1\n\t" "s_memtime s[62:63]\n\t" "s_memrealtime s[60:61]\n\t" \
   "buffer_store_dwordx2 v[48:49], v132, %[rs], s50 offen sc1\n\t" RSTAMP(2) "s_branch 40b\n\t" "70:\n\t" "v_max3_f32 v94, v48, v49, v50\n\t" \
   "v_max3_f32 v102, v76, v77, v78\n\t" "v_max3_f32 v94, v94, v51, v79\n\t" "v_max_f32 v92, v94, v102\n\t" "s_nop 1\n\t" \
   "v_max_f32_dpp v102, v92, v92 row_ror:8 row_mask:0xf bank_mask:0xf\n\t" "v_max_f32_dpp v94, v92, v92 row_ror:8 row_mask:0xf bank_mask:0xf\n\t" \
   "s_nop 1\n\t" "v_permlane32_swap_b32 v102, v94\n\t" "v_max_f32 v95, v102, v94\n\t" "v_max_f32 v102, v102, v94\n\t" "s_nop 1\n\t" \
   "v_permlane16_swap_b32 v102, v95\n\t" "v_max_f32 v102, v102, v95\n\t" "v_sub_f32 v80, v48, v102\n\t" "v_sub_f32 v88, v76, v102\n\t" \
   "v_sub_f32 v81, v49, v102\n\t" "v_sub_f32 v89, v77, v102\n\t" "v_sub_f32 v82, v50, v102\n\t" "v_sub_f32 v90, v78, v102\n\t" \
   "v_sub_f32 v83, v51, v102\n\t" "v_sub_f32 v91, v79, v102\n\t" "v_exp_f32 v80, v80\n\t" "v_exp_f32 v88, v88\n\t" "v_exp_f32 v81, v81\n\t" \
   "v_exp_f32 v89, v89\n\t" "v_exp_f32 v82, v82\n\t" "v_exp_f32 v90, v90\n\t" "v_exp_f32 v83, v83\n\t" "v_exp_f32 v91, v91\n\t" "s_nop 0\n\t" \
   "v_add_f32 v96, v80, v81\n\t" "v_add_f32 v103, v82, v83\n\t" "v_add_f32 v94, v96, v103\n\t" "v_add_f32 v96, v96, v103\n\t" "s_nop 1\n\t" \
   "v_permlane32_swap_b32 v96, v94\n\t" "v_add_f32 v96, v96, v94\n\t" "s_nop 1\n\t" \
   "v_add_f32_dpp v96, v96, v96 row_ror:8 row_mask:0xf bank_mask:0xf\n\t" "v_add_f32 v103, v96, v88\n\t" "v_add_f32 v103, v103, v89\n\t" \
   "v_add_f32 v103, v103, v90\n\t" "v_add_f32 v97, v103, v91\n\t" "v_add_f32 v103, v103, v91\n\t" "s_nop 1\n\t" "v_permlane16_swap_b32 v103, v97\n\t" \
   "v_add_f32 v103, v103, v97\n\t" "v_rcp_f32 v100, v103\n\t" "s_nop 0\n\t" "v_pk_mul_f32 v[80:81], v[80:81], v[100:101] op_sel_hi:[1,0]\n\t" \
   "v_pk_mul_f32 v[82:83], v[82:83], v[100:101] op_sel_hi:[1,0]\n\t" "v_pk_mul_f32 v[88:89], v[88:89], v[100:101] op_sel_hi:[1,0]\n\t" \
   "v_pk_mul_f32 v[90:91], v[90:91], v[100:101] op_sel_hi:[1,0]\n\t" "v_cvt_pk_bf16_f32 v48, v80, v81\n\t" "v_cvt_pk_bf16_f32 v49, v82, v83\n\t" \
   "v_cvt_pk_bf16_f32 v52, v88, v89\n\t" "v_cvt_pk_bf16_f32 v53, v90, v91\n\t" "v_min_u32 v48, -2, v48\n\t" "v_min_u32 v52, -2, v52\n\t" \
   "buffer_store_dwordx2 v[48:49], v132, %[rs], s55 offen sc1\n\t" "s_branch 71b\n\t" "74:\n\t" "v_max3_f32 v80, v32, v33, v34\n\t" \
   "v_max3_f32 v81, v35, v36, v37\n\t" "v_max3_f32 v82, v38, v39, v40\n\t" "v_max3_f32 v83, v41, v42, v43\n\t" "v_max3_f32 v80, v80, v81, v82\n\t" \
   "v_max_f32 v81, v80, v83\n\t" "v_max_f32 v80, v80, v83\n\t" "s_nop 1\n\t" "v_permlane16_swap_b32 v80, v81\n\t" "v_max_f32 v82, v80, v81\n\t" \
   "v_max_f32 v80, v80, v81\n\t" "s_nop 1\n\t" "v_permlane32_swap_b32 v80, v82\n\t" "v_max_f32 v80, v80, v82\n\t" "v_sub_f32 v64, v32, v80\n\t" \
   "v_sub_f32 v65, v33, v80\n\t" "v_sub_f32 v66, v34, v80\n\t" "v_sub_f32 v67, v35, v80\n\t" "v_sub_f32 v68, v36, v80\n\t" \
   "v_sub_f32 v69, v37, v80\n\t" "v_sub_f32 v70, v38, v80\n\t" "v_sub_f32 v71, v39, v80\n\t" "v_sub_f32 v72, v40, v80\n\t" \
   "v_sub_f32 v73, v41, v80\n\t" "v_sub_f32 v74, v42, v80\n\t" "v_sub_f32 v75, v43, v80\n\t" "v_exp_f32 v64, v64\n\t" "v_exp_f32 v65, v65\n\t" \
   "v_exp_f32 v66, v66\n\t" "v_exp_f32 v67, v67\n\t" "v_exp_f32 v68, v68\n\t" "v_exp_f32 v69, v69\n\t" "v_exp_f32 v70, v70\n\t" \
   "v_exp_f32 v71, v71\n\t" "v_exp_f32 v72, v72\n\t" "v_exp_f32 v73, v73\n\t" "v_exp_f32 v74, v74\n\t" "v_exp_f32 v75, v75\n\t" "s_nop 0\n\t" \
   "v_add_f32 v80, v64, v65\n\t" "v_add_f32 v81, v66, v67\n\t" "v_add_f32 v82, v68, v69\n\t" "v_add_f32 v83, v70, v71\n\t" \
   "v_add_f32 v84, v72, v73\n\t" "v_add_f32 v85, v74, v75\n\t" "v_add_f32 v80, v80, v81\n\t" "v_add_f32 v82, v82, v83\n\t" \
   "v_add_f32 v84, v84, v85\n\t" "v_add_f32 v80, v80, v82\n\t" "v_fma_f32 v82, v80, 2.0, v84\n\t" "v_fma_f32 v80, v80, 2.0, v84\n\t" "s_nop 0\n\t" \
   "s_nop 0\n\t" "v_permlane16_swap_b32 v80, v82\n\t" "v_add_f32 v81, v80, v82\n\t" "v_add_f32 v80, v80, v82\n\t" "s_nop 0\n\t" "s_nop 0\n\t" \
   "v_permlane32_swap_b32 v80, v81\n\t" "v_add_f32 v80, v80, v81\n\t" "v_rcp_f32 v90, v80\n\t" "s_nop 0\n\t" "v_add_f32 v90, v90, v90\n\t" \
   "s_branch 75b\n\t" "63:\n\t" "s_cmp_lg_u32 s64, 0\n\t" "s_cbranch_scc1 64f\n\t" "s_sub_u32 s69, s56, 0x10000\n\t" "s_and_b32 s69, s69, -2\n\t" \
   "s_branch 50b\n\t" "64:\n\t" "s_mov_b32 s69, s48\n\t" "s_cmp_lt_u32 s64, 3\n\t" "s_cselect_b32 s69, -1, s69\n\t" "s_cmp_lg_u32 s69, -1\n\t" \
   "s_cbranch_scc1 50b\n\t" "s_add_u32 s76, s76, 0x1000000\n\t" "s_mov_b32 s73, 0\n\t" "65:\n\t" "s_add_u32 s73, s73, 1\n\t" \
   "s_min_u32 s72, s73, 7\n\t" "s_mul_i32 s72, s72, 5\n\t" "s_add_u32 s72, s72, %[xs]\n\t" "s_and_b32 s72, s72, 7\n\t" "s_lshl_b32 s71, %[xs], 7\n\t" \
   "v_subrev_u32 v109, s71, v139\n\t" "s_lshl_b32 s72, s72, 7\n\t" "s_add_u32 s72, s72, s66\n\t" "s_cmp_lt_u32 s73, 7\n\t" "s_cbranch_scc1 66f\n\t" \
   "s_sleep 4\n\t" "66:\n\t" "buffer_load_dword v108, v109, %[rs], s72 offen sc1\n\t" "s_waitcnt vmcnt(0)\n\t" "v_readfirstlane_b32 s69, v108\n\t" \
   "s_cmp_gt_u32 s73, 0x10000\n\t" "s_cbranch_scc1 50b\n\t" "s_cmp_eq_u32 s69, -1\n\t" "s_cbranch_scc1 65b\n\t" "s_branch 50b\n\t" "8:\n\t" \
   "s_nop 7\n\t" "s_waitcnt vmcnt(6) lgkmcnt(0)\n\t" "s_add_u32 s70, s70, 1\n\t" "s_cmp_gt_u32 s70, 2\n\t" "s_cbranch_scc1 81f\n\t" \
   "s_lshl_b32 s71, s70, 3\n\t" "s_lshl_b32 s71, 1, s71\n\t" "s_add_u32 s76, s76, s71\n\t" "81:\n\t" "v_mov_b32 v48, 0\n\t" "v_mov_b32 v49, 0\n\t" \
   "v_mov_b32 v50, 0\n\t" "v_mov_b32 v51, 0\n\t" "v_mov_b32 v52, 0\n\t" "v_mov_b32 v53, 0\n\t" "v_mov_b32 v54, 0\n\t" "v_mov_b32 v55, 0\n\t" \
   "v_mov_b32 v56, 0\n\t" "v_mov_b32 v57, 0\n\t" "v_mov_b32 v58, 0\n\t" "v_mov_b32 v59, 0\n\t" "v_mov_b32 v60, 0\n\t" "v_mov_b32 v61, 0\n\t" \
   "v_mov_b32 v62, 0\n\t" "v_mov_b32 v63, 0\n\t" "s_min_u32 s71, s70, 7\n\t" "s_lshr_b32 s67, s71, 1\n\t" "s_add_u32 s67, s67, %[xs]\n\t" \
   "s_and_b32 s67, s67, 3\n\t" "s_lshl_b32 s67, s67, 11\n\t" "s_and_b32 s72, s71, 1\n\t" "s_lshl_b32 s72, s72, 14\n\t" "s_add_u32 s67, s67, s72\n\t" \
   "s_add_u32 s67, s67, s66\n\t" "s_cmp_eq_u32 s70, 1\n\t" "s_cselect_b32 s67, s68, s67\n\t" "s_cmp_lt_u32 s70, 7\n\t" "s_cbranch_scc1 20b\n\t" \
   "s_sleep 3\n\t" "s_cmp_lt_u32 s70, 0x20000\n\t" "s_cbranch_scc1 20b\n\t" "s_mov_b32 s67, %[esc]\n\t" "s_mov_b32 s68, %[esc]\n\t" "s_branch 20b\n\t" \
   "27:\n\t" "v_mov_b32 v98, 0x7ffffff0\n\t" "buffer_store_dword v98, v98, %[rs], 0 offen\n\t" "buffer_store_dword v98, v98, %[rs], 0 offen\n\t" \
   "buffer_store_dword v98, v98, %[rs], 0 offen\n\t" "buffer_store_dword v98, v98, %[rs], 0 offen\n\t" \
   "buffer_store_dword v98, v98, %[rs], 0 offen\n\t" "buffer_store_dword v98, v98, %[rs], 0 offen\n\t" "s_branch 28b\n\t" "141:\n\t" \
   "s_mov_b32 s74, 0\n\t" "s_cmp_eq_u32 s64, 0\n\t" "s_cbranch_scc1 41b\n\t" "42:\n\t" "buffer_load_dword v15, v140, %[rs], s59 offen sc1\n\t" \
   "s_add_u32 s74, s74, 1\n\t" "s_waitcnt vmcnt(0)\n\t" "v_cmp_eq_u32 vcc, -1, v15\n\t" "s_cmp_gt_u32 s74, 0x10000\n\t" "s_cbranch_scc1 41b\n\t" \
   "s_cbranch_vccz 41b\n\t" "s_sleep 2\n\t" "s_branch 42b\n\t" "95:\n\t" "s_movk_i32 s59, 0x1000\n\t" "s_branch 197f\n\t" "97:\n\t" \
   "buffer_load_dwordx4 v[16:19], v138, %[rs], s79 offen offset:0 sc1\n\t" "buffer_load_dwordx4 v[20:23], v138, %[rs], s79 offen offset:1024 sc1\n\t" \
   "buffer_load_dwordx4 v[24:27], v138, %[rs], s79 offen offset:2048 sc1\n\t" \
   "buffer_load_dwordx4 v[28:31], v138, %[rs], s79 offen offset:3072 sc1\n\t" "v_or_b32 v108, s49, v141\n\t" "v_or_b32 v109, s49, v142\n\t" \
   "buffer_store_dwordx4 v[44:47], v108, %[rs], s52 offen sc1\n\t" "buffer_store_dwordx2 v[32:33], v109, %[rs], s51 offen sc1\n\t" \
   "s_waitcnt vmcnt(0)\n\t" "s_branch 96b\n\t" "197:\n\t" "s_mov_b32 s74, 0\n\t" "98:\n\t" "buffer_load_dword v15, v140, %[rs], s59 offen sc1\n\t" \
   "s_add_u32 s74, s74, 1\n\t" "s_waitcnt vmcnt(0)\n\t" "v_cmp_eq_u32 vcc, -1, v15\n\t" "s_cmp_gt_u32 s74, 0x10000\n\t" "s_cbranch_scc1 97b\n\t" \
   "s_cbranch_vccz 97b\n\t" "s_sleep 2\n\t" "s_branch 98b\n\t" "92:\n\t" "s_mov_b32 s59, 0\n\t" "s_branch 145f\n\t" "45:\n\t" \
   "v_mbcnt_lo_u32_b32 v98, -1, 0\n\t" "v_mbcnt_hi_u32_b32 v98, -1, v98\n\t" "v_lshlrev_b32 v98, 4, v98\n\t" "s_add_u32 s72, %[kf0], 4096\n\t" \
   "buffer_load_dwordx4 v[160:163], v98, %[rs], %[kf0] offen offset:0 sc1\n\t" \
   "buffer_load_dwordx4 v[164:167], v98, %[rs], %[kf0] offen offset:1024 sc1\n\t" \
   "buffer_load_dwordx4 v[168:171], v98, %[rs], %[kf0] offen offset:2048 sc1\n\t" \
   "buffer_load_dwordx4 v[172:175], v98, %[rs], %[kf0] offen offset:3072 sc1\n\t" \
   "buffer_load_dwordx4 v[176:179], v98, %[rs], s72 offen offset:0 sc1\n\t" "buffer_load_dwordx4 v[180:183], v98, %[rs], s72 offen offset:1024 sc1\n\t" \
   "buffer_load_dwordx4 v[184:187], v98, %[rs], s72 offen offset:2048 sc1\n\t" \
   "buffer_load_dwordx4 v[188:191], v98, %[rs], s72 offen offset:3072 sc1\n\t" "s_waitcnt vmcnt(0)\n\t" \
   "v_mfma_f32_16x16x32_bf16 v[48:51], v[160:163], v[64:67], 0\n\t" "v_mfma_f32_16x16x32_bf16 v[52:55], v[176:179], v[64:67], 0\n\t" \
   "v_mfma_f32_16x16x32_bf16 v[48:51], v[164:167], v[68:71], v[48:51]\n\t" "v_mfma_f32_16x16x32_bf16 v[52:55], v[180:183], v[68:71], v[52:55]\n\t" \
   "v_mfma_f32_16x16x32_bf16 v[48:51], v[168:171], v[72:75], v[48:51]\n\t" "v_mfma_f32_16x16x32_bf16 v[52:55], v[184:187], v[72:75], v[52:55]\n\t" \
   "v_mfma_f32_16x16x32_bf16 v[48:51], v[172:175], v[104:107], v[48:51]\n\t" "v_mfma_f32_16x16x32_bf16 v[52:55], v[188:191], v[104:107], v[52:55]\n\t" \
   "s_nop 7\n\t" "s_branch 93b\n\t" "145:\n\t" "s_mov_b32 s74, 0\n\t" "46:\n\t" "buffer_load_dword v15, v140, %[rs], s59 offen sc1\n\t" \
   "s_add_u32 s74, s74, 1\n\t" "s_waitcnt vmcnt(0)\n\t" "v_cmp_eq_u32 vcc, -1, v15\n\t" "s_cmp_gt_u32 s74, 0x10000\n\t" "s_cbranch_scc1 45b\n\t" \
   "s_cbranch_vccz 45b\n\t" "s_sleep 2\n\t" "s_branch 46b\n\t" "90:\n\t" "v_or_b32 v108, s49, v141\n\t" "v_or_b32 v109, s49, v142\n\t" \
   "buffer_store_dwordx4 v[44:47], v108, %[rs], s52 offen sc1\n\t" "buffer_store_dwordx2 v[32:33], v109, %[rs], s51 offen sc1\n\t" \
   "s_waitcnt vmcnt(0) lgkmcnt(0)"
#define P1_CLOB "v15", "v16", "v17", "v18", "v19", "v20", "v21", "v22", "v23", "v24", "v25", "v26", "v27", "v28", "v29", "v30", "v31", "v32", "v33", "v34", "v35", "v36", "v37", "v38", "v39", "v40", "v41", "v42", "v43", "v44", "v45", "v46", "v47", "v48", "v49", "v50", "v51", "v52", "v53", "v54", "v55", "v56", "v57", "v58", "v59", "v60", "v61", "v62", "v63", "v64", "v65", "v66", "v67", "v68", "v69", "v70", "v71", "v72", "v73", "v74", "v75", "v76", "v77", "v78", "v79", "v80", "v81", "v82", "v83", "v84", "v85", "v86", "v87", "v88", "v89", "v90", "v91", "v92", "v93", "v94", "v95", "v96", "v97", "v98", "v99", "v100", "v101", "v102", "v103", "v104", "v105", "v106", "v107", "v108", "v109", "v110", "v111", "v144", "v145", "v146", "v147", "v148", "v149", "v150", "v151", "v152", "v153", "v154", "v155", "v156", "v157", "v158", "v159", "v160", "v161", "v162", "v163", "v164", "v165", "v166", "v167", "v168", "v169", "v170", "v171", "v172", "v173", "v174", "v175", "v176", "v177", "v178", "v179", "v180", "v181", "v182", "v183", "v184", "v185", "v186", "v187", "v188", "v189", "v190", "v191", "s60", "s61", "s62", "s63", "s64", "s65", "s66", "s67", "s68", "s69", "s70", "s71", "s72", "s73", "s74", "s75", "s47", "s48", "s49", "s50", "s51", "s52", "s54", "s55", "s79", "s20", "s21", "s22", "s23", "s24", "s25", "s26", "s27", "s28", "s29", "s30", "s31", "s32", "s33", "s34", "s35"
#define PV_ASM \
   "s_mov_b32 s64, -1\n\t" "s_mov_b32 s76, 0\n\t" "s_mov_b32 s77, 0\n\t" "s_mov_b32 s71, 63\n\t" "s_mov_b32 s47, 1300\n\t" "s_mov_b32 s70, 0\n\t" \
   "s_mov_b32 s69, 0\n\t" "s_lshr_b64 s[72:73], %[sel], s71\n\t" "s_lshl_b32 s65, s71, 19\n\t" "s_and_b32 s72, s72, 1\n\t" \
   "s_add_u32 s65, s65, %[pool]\n\t" "s_lshl_b32 s72, s72, 13\n\t" "s_add_u32 s65, s65, s72\n\t" "s_lshr_b64 s[72:73], %[s3], s71\n\t" \
   "s_and_b32 s54, s72, 1\n\t" "s_lshl_b32 s54, s54, 14\n\t" "40:\n\t" "s_cmp_lt_i32 s64, 0\n\t" "s_cselect_b32 s72, 0x7ffffff0, 0\n\t" \
   "s_max_i32 s71, s64, 0\n\t" "v_or_b32 v109, s72, v143\n\t" "s_lshl_b32 s71, s71, 12\n\t" "v_mov_b32 v110, 1\n\t" "s_cmp_eq_u32 s64, 63\n\t" \
   "s_cbranch_scc1 70f\n\t" "71:\n\t" "s_waitcnt vmcnt(0)\n\t" RSTAMP(3) "s_barrier\n\t" "buffer_load_dword v108, v139, %[rs], s65 offen sc1\n\t" \
   "buffer_store_dword v110, v109, %[rs], s71 offen sc1\n\t" PVTRACE "s_add_u32 s48, s69, s47\n\t" "s_cmp_eq_u32 s70, 0\n\t" \
   "s_cselect_b32 s48, s48, -1\n\t" "s_mov_b32 s66, s65\n\t" "s_add_i32 s64, s64, 1\n\t" "s_and_b32 s71, s64, 63\n\t" \
   "s_lshr_b64 s[72:73], %[sel], s71\n\t" "s_lshl_b32 s65, s71, 19\n\t" "s_and_b32 s72, s72, 1\n\t" "s_add_u32 s65, s65, %[pool]\n\t" \
   "s_lshl_b32 s72, s72, 13\n\t" "s_add_u32 s65, s65, s72\n\t" "s_xor_b32 s67, s54, %[lad0]\n\t" "s_xor_b32 s68, s67, 0x4000\n\t" \
   "s_add_u32 s67, s67, s66\n\t" "s_add_u32 s68, s68, s66\n\t" "s_lshr_b64 s[72:73], %[s3], s71\n\t" "s_and_b32 s54, s72, 1\n\t" \
   "s_lshl_b32 s54, s54, 14\n\t" "s_mov_b32 s70, 0\n\t" "s_lshr_b64 s[72:73], %[ysel], s71\n\t" "s_mul_i32 s73, s71, 43\n\t" \
   "s_mul_i32 s49, s71, 80\n\t" "s_and_b32 s72, s72, 1\n\t" "s_lshr_b32 s73, s73, 7\n\t" "s_add_u32 s49, s49, s72\n\t" \
   "s_lshl4_add_u32 s49, s73, s49\n\t" "s_lshl_b32 s49, s49, 13\n\t" "s_add_u32 s49, s49, %[yb]\n\t" "s_add_u32 s50, s49, 32768\n\t" \
   "s_add_u32 s51, s49, 65536\n\t" "s_add_u32 s52, s49, 98304\n\t" "s_add_u32 s53, s49, 131072\n\t" "v_mov_b32 v48, 0\n\t" "v_mov_b32 v49, 0\n\t" \
   "v_mov_b32 v50, 0\n\t" "v_mov_b32 v51, 0\n\t" "v_mov_b32 v52, 0\n\t" "v_mov_b32 v53, 0\n\t" "v_mov_b32 v54, 0\n\t" "v_mov_b32 v55, 0\n\t" \
   "v_mov_b32 v144, 0\n\t" "v_mov_b32 v145, 0\n\t" "v_mov_b32 v146, 0\n\t" "v_mov_b32 v147, 0\n\t" "v_mov_b32 v148, 0\n\t" "v_mov_b32 v149, 0\n\t" \
   "v_mov_b32 v150, 0\n\t" "v_mov_b32 v151, 0\n\t" "v_mov_b32 v152, 0\n\t" "v_mov_b32 v153, 0\n\t" "v_mov_b32 v154, 0\n\t" "v_mov_b32 v155, 0\n\t" \
   "v_mov_b32 v156, 0\n\t" "v_mov_b32 v157, 0\n\t" "v_mov_b32 v158, 0\n\t" "v_mov_b32 v159, 0\n\t" "v_mov_b32 v160, 0\n\t" "v_mov_b32 v161, 0\n\t" \
   "v_mov_b32 v162, 0\n\t" "v_mov_b32 v163, 0\n\t" "s_cmp_eq_u32 s64, 64\n\t" "s_cbranch_scc1 90f\n\t" "s_waitcnt vmcnt(1)\n\t" \
   "v_readlane_b32 s69, v108, 0\n\t" "v_readlane_b32 s71, v108, 1\n\t" "s_cmp_eq_u32 s69, -1\n\t" "s_cbranch_scc1 63f\n\t" "s_cmp_eq_u32 s71, -1\n\t" \
   "s_cselect_b32 s47, s47, s71\n\t" "50:\n\t" "s_add_u32 s73, s69, %[m1c]\n\t" RSTAMP(5) "51:\n\t" "s_memrealtime s[60:61]\n\t" \
   "s_memtime s[62:63]\n\t" "s_waitcnt lgkmcnt(0)\n\t" "s_sub_u32 s74, s62, s57\n\t" "s_mul_i32 s74, s74, s58\n\t" "s_lshr_b32 s74, s74, 12\n\t" \
   "s_add_u32 s74, s74, s56\n\t" "s_lshl_b32 s60, s60, 3\n\t" "s_sub_u32 s75, s74, s60\n\t" "s_cmp_lt_u32 s75, 32\n\t" "s_cbranch_scc1 52f\n\t" \
   "s_cmp_lt_i32 s75, 0\n\t" "s_cselect_b32 s61, 0, 31\n\t" "s_add_u32 s74, s60, s61\n\t" "s_sub_i32 s75, s61, s75\n\t" "s_add_i32 s75, s75, 1\n\t" \
   "s_ashr_i32 s75, s75, 1\n\t" "s_min_i32 s75, s75, 8\n\t" "s_max_i32 s75, s75, -8\n\t" "s_mov_b32 s56, s74\n\t" "s_mov_b32 s57, s62\n\t" \
   "s_add_u32 s58, s58, s75\n\t" "52:\n\t" "s_sub_i32 s74, s73, s74\n\t" "s_cmp_le_i32 s74, 150\n\t" "s_cbranch_scc1 53f\n\t" \
   "s_cmp_lt_i32 s74, 260\n\t" "s_cbranch_scc1 51b\n\t" "s_sleep 2\n\t" "s_cmp_lt_i32 s74, 32000\n\t" "s_cbranch_scc1 51b\n\t" "s_mov_b32 s74, 0\n\t" \
   "53:\n\t" "s_sub_i32 s74, s74, 56\n\t" "s_max_i32 s74, s74, 0\n\t" "s_mul_i32 s74, s74, 23\n\t" "s_lshr_b32 s74, s74, 5\n\t" \
   "s_and_b32 s74, s74, -4\n\t" "s_getpc_b64 s[60:61]\n\t" "54:\n\t" "s_sub_u32 s74, 55f-54b, s74\n\t" "s_add_u32 s60, s60, s74\n\t" \
   "s_addc_u32 s61, s61, 0\n\t" "s_setpc_b64 s[60:61]\n\t" "s_nop 3\n\t" "s_nop 3\n\t" "s_nop 3\n\t" "s_nop 3\n\t" "s_nop 3\n\t" "s_nop 3\n\t" \
   "s_nop 3\n\t" "s_nop 3\n\t" "s_nop 3\n\t" "s_nop 3\n\t" "s_nop 3\n\t" "s_nop 3\n\t" "s_nop 3\n\t" "s_nop 3\n\t" "s_nop 3\n\t" "s_nop 3\n\t" \
   "s_nop 3\n\t" "s_nop 3\n\t" "s_nop 3\n\t" "s_nop 3\n\t" "s_nop 3\n\t" "s_nop 3\n\t" "s_nop 3\n\t" "s_nop 3\n\t" "55:\n\t" "20:\n\t" RSTAMP(4) \
   "buffer_load_dwordx4 v[64:67], v128, %[rs], s67 offen sc1\n\t" "buffer_load_dwordx4 v[68:71], v129, %[rs], s67 offen sc1\n\t" \
   "buffer_load_dwordx4 v[72:75], v130, %[rs], s67 offen sc1\n\t" "s_waitcnt vmcnt(2)\n\t" "ds_write_b128 v112, v[64:67]\n\t" \
   "v_smfmac_f32_16x16x64_bf16 v[48:51], v[64:67], a[0:7], v134\n\t" "s_waitcnt vmcnt(1)\n\t" "ds_write_b128 v113, v[68:71]\n\t" \
   "s_waitcnt vmcnt(0)\n\t" "ds_write_b128 v114, v[72:75]\n\t" RSTAMP(7) "v_smfmac_f32_16x16x64_bf16 v[52:55], v[64:67], a[64:71], v134\n\t" \
   "v_max3_u32 v110, v64, v66, v68\n\t" "v_max3_u32 v111, v70, v72, v74\n\t" "v_smfmac_f32_16x16x64_bf16 v[48:51], v[68:71], a[8:15], v134\n\t" \
   "v_max_u32 v110, v110, v111\n\t" "v_cmp_eq_u32 vcc, -1, v110\n\t" "v_smfmac_f32_16x16x64_bf16 v[52:55], v[68:71], a[72:79], v134\n\t" \
   "s_cbranch_vccnz 8f\n\t" "s_waitcnt lgkmcnt(0)\n\t" "s_barrier\n\t" RSTAMP(0) "ds_read_b128 v[76:79], v115\n\t" "ds_read_b128 v[80:83], v116\n\t" \
   "ds_read_b128 v[84:87], v117\n\t" "v_smfmac_f32_16x16x64_bf16 v[48:51], v[72:75], a[16:23], v134\n\t" "ds_read_b128 v[88:91], v118\n\t" \
   "ds_read_b128 v[92:95], v119\n\t" "v_smfmac_f32_16x16x64_bf16 v[52:55], v[72:75], a[80:87], v134\n\t" "ds_read_b128 v[96:99], v120\n\t" \
   "ds_read_b128 v[100:103], v121\n\t" "s_waitcnt lgkmcnt(6)\n\t" "v_smfmac_f32_16x16x64_bf16 v[48:51], v[76:79], a[24:31], v134\n\t" \
   "v_smfmac_f32_16x16x64_bf16 v[52:55], v[76:79], a[88:95], v134\n\t" "s_waitcnt lgkmcnt(5)\n\t" \
   "v_smfmac_f32_16x16x64_bf16 v[48:51], v[80:83], a[32:39], v134\n\t" "v_smfmac_f32_16x16x64_bf16 v[52:55], v[80:83], a[96:103], v134\n\t" \
   "s_waitcnt lgkmcnt(4)\n\t" "v_smfmac_f32_16x16x64_bf16 v[48:51], v[84:87], a[40:47], v134\n\t" \
   "v_smfmac_f32_16x16x64_bf16 v[52:55], v[84:87], a[104:111], v134\n\t" "s_waitcnt lgkmcnt(3)\n\t" \
   "v_smfmac_f32_16x16x64_bf16 v[48:51], v[88:91], a[48:55], v134\n\t" "v_smfmac_f32_16x16x64_bf16 v[52:55], v[88:91], a[112:119], v134\n\t" \
   "s_waitcnt lgkmcnt(0)\n\t" "v_smfmac_f32_16x16x64_bf16 v[48:51], v[92:95], a[56:63], v134\n\t" \
   "v_smfmac_f32_16x16x64_bf16 v[48:51], v[96:99], v[192:199], v134\n\t" "v_smfmac_f32_16x16x64_bf16 v[48:51], v[100:103], v[200:207], v134\n\t" \
   "v_smfmac_f32_16x16x64_bf16 v[52:55], v[92:95], a[120:127], v134\n\t" "v_smfmac_f32_16x16x64_bf16 v[52:55], v[96:99], v[208:215], v134\n\t" \
   "v_smfmac_f32_16x16x64_bf16 v[52:55], v[100:103], v[216:223], v134\n\t" "v_add_f32 v48, v48, v49\n\t" "v_add_f32 v49, v50, v51\n\t" \
   "v_cvt_pk_bf16_f32 v48, v48, v49\n\t" "ds_write_b32 v135, v48 offset:10240\n\t" "s_nop 3\n\t" "v_add_f32 v52, v52, v53\n\t" \
   "v_add_f32 v53, v54, v55\n\t" "v_cvt_pk_bf16_f32 v52, v52, v53\n\t" "ds_write_b32 v135, v52 offset:10496\n\t" "s_waitcnt lgkmcnt(0)\n\t" \
   "s_barrier\n\t" RSTAMP(6) "ds_read_b64_tr_b16 v[16:17], v136 offset:10240\n\t" "ds_read_b64_tr_b16 v[18:19], v136 offset:10496\n\t" \
   "ds_read_b64_tr_b16 v[20:21], v136 offset:10752\n\t" "ds_read_b64_tr_b16 v[22:23], v136 offset:11008\n\t" \
   "ds_read_b64_tr_b16 v[24:25], v136 offset:11264\n\t" "ds_read_b64_tr_b16 v[26:27], v136 offset:11520\n\t" \
   "ds_read_b64_tr_b16 v[28:29], v136 offset:11776\n\t" "ds_read_b64_tr_b16 v[30:31], v136 offset:12032\n\t" "s_waitcnt lgkmcnt(6)\n\t" \
   "v_smfmac_f32_16x16x64_bf16 v[144:147], v[16:19], a[128:135], v131\n\t" "v_smfmac_f32_16x16x64_bf16 v[148:151], v[16:19], a[160:167], v131\n\t" \
   "v_smfmac_f32_16x16x64_bf16 v[152:155], v[16:19], a[192:199], v131\n\t" "v_smfmac_f32_16x16x64_bf16 v[156:159], v[16:19], a[224:231], v131\n\t" \
   "v_smfmac_f32_16x16x64_bf16 v[160:163], v[16:19], v[224:231], v131\n\t" "s_waitcnt lgkmcnt(0)\n\t" \
   "v_smfmac_f32_16x16x64_bf16 v[144:147], v[20:23], a[136:143], v131\n\t" "v_smfmac_f32_16x16x64_bf16 v[144:147], v[24:27], a[144:151], v131\n\t" \
   "v_smfmac_f32_16x16x64_bf16 v[144:147], v[28:31], a[152:159], v131\n\t" "v_smfmac_f32_16x16x64_bf16 v[148:151], v[20:23], a[168:175], v131\n\t" \
   "v_smfmac_f32_16x16x64_bf16 v[148:151], v[24:27], a[176:183], v131\n\t" "v_smfmac_f32_16x16x64_bf16 v[148:151], v[28:31], a[184:191], v131\n\t" \
   "v_smfmac_f32_16x16x64_bf16 v[152:155], v[20:23], a[200:207], v131\n\t" "v_cvt_pk_bf16_f32 v164, v144, v145\n\t" \
   "v_cvt_pk_bf16_f32 v165, v146, v147\n\t" "v_smfmac_f32_16x16x64_bf16 v[152:155], v[24:27], a[208:215], v131\n\t" "v_min_u32 v164, -2, v164\n\t" \
   "buffer_store_dwordx2 v[164:165], v132, %[rs], s49 offen sc1\n\t" "v_smfmac_f32_16x16x64_bf16 v[152:155], v[28:31], a[216:223], v131\n\t" \
   "v_smfmac_f32_16x16x64_bf16 v[156:159], v[20:23], a[232:239], v131\n\t" "v_cvt_pk_bf16_f32 v166, v148, v149\n\t" \
   "v_cvt_pk_bf16_f32 v167, v150, v151\n\t" "v_smfmac_f32_16x16x64_bf16 v[156:159], v[24:27], a[240:247], v131\n\t" "v_min_u32 v166, -2, v166\n\t" \
   "buffer_store_dwordx2 v[166:167], v132, %[rs], s50 offen sc1\n\t" "v_smfmac_f32_16x16x64_bf16 v[156:159], v[28:31], a[248:255], v131\n\t" \
   "v_smfmac_f32_16x16x64_bf16 v[160:163], v[20:23], v[232:239], v131\n\t" "v_cvt_pk_bf16_f32 v168, v152, v153\n\t" \
   "v_cvt_pk_bf16_f32 v169, v154, v155\n\t" "v_smfmac_f32_16x16x64_bf16 v[160:163], v[24:27], v[240:247], v131\n\t" "v_min_u32 v168, -2, v168\n\t" \
   "buffer_store_dwordx2 v[168:169], v132, %[rs], s51 offen sc1\n\t" "v_smfmac_f32_16x16x64_bf16 v[160:163], v[28:31], v[248:255], v131\n\t" \
   "v_cvt_pk_bf16_f32 v170, v156, v157\n\t" "v_cvt_pk_bf16_f32 v171, v158, v159\n\t" "v_min_u32 v170, -2, v170\n\t" \
   "buffer_store_dwordx2 v[170:171], v132, %[rs], s52 offen sc1\n\t" "s_nop 3\n\t" "v_cvt_pk_bf16_f32 v172, v160, v161\n\t" \
   "v_cvt_pk_bf16_f32 v173, v162, v163\n\t" "v_min_u32 v172, -2, v172\n\t" "buffer_store_dwordx2 v[172:173], v132, %[rs], s53 offen sc1\n\t" RSTAMP(2) \
   "s_branch 40b\n\t" "70:\n\t" "s_mov_b32 s46, 0\n\t" "s_lshr_b64 s[72:73], %[sel], s46\n\t" "s_lshl_b32 s45, s46, 19\n\t" "s_and_b32 s72, s72, 1\n\t" \
   "s_add_u32 s45, s45, %[pool]\n\t" "s_lshl_b32 s72, s72, 13\n\t" "s_add_u32 s45, s45, s72\n\t" "v_mov_b32 v76, -1\n\t" "v_mov_b32 v77, -1\n\t" \
   "v_mov_b32 v78, -1\n\t" "v_mov_b32 v79, -1\n\t" "buffer_store_dwordx4 v[76:79], v133, %[rs], s45 offen sc1\n\t" \
   "buffer_store_dwordx4 v[76:79], v133, %[rs], s45 offen offset:1024 sc1\n\t" "s_branch 71b\n\t" "63:\n\t" "s_cmp_lg_u32 s64, 0\n\t" \
   "s_cbranch_scc1 64f\n\t" "s_sub_u32 s69, s56, 0x10000\n\t" "s_and_b32 s69, s69, -2\n\t" "s_branch 50b\n\t" "64:\n\t" "s_mov_b32 s69, s48\n\t" \
   "s_cmp_lt_u32 s64, 3\n\t" "s_cselect_b32 s69, -1, s69\n\t" "s_cmp_lg_u32 s69, -1\n\t" "s_cbranch_scc1 50b\n\t" "s_add_u32 s76, s76, 0x1000000\n\t" \
   "s_mov_b32 s73, 0\n\t" "65:\n\t" "s_add_u32 s73, s73, 1\n\t" "s_sleep 6\n\t" "s_and_b32 s72, s73, 3\n\t" "s_add_u32 s72, s72, 8\n\t" \
   "s_sub_u32 s72, s72, %[xs]\n\t" "s_lshl_b32 s72, s72, 7\n\t" "s_add_u32 s72, s72, s66\n\t" "buffer_load_dword v108, v139, %[rs], s72 offen sc1\n\t" \
   "s_waitcnt vmcnt(0)\n\t" "v_readfirstlane_b32 s69, v108\n\t" "s_cmp_gt_u32 s73, 0x10000\n\t" "s_cbranch_scc1 50b\n\t" "s_cmp_eq_u32 s69, -1\n\t" \
   "s_cbranch_scc1 65b\n\t" "s_branch 50b\n\t" "8:\n\t" "s_nop 7\n\t" "s_waitcnt lgkmcnt(0)\n\t" "s_add_u32 s70, s70, 1\n\t" "s_cmp_gt_u32 s70, 2\n\t" \
   "s_cbranch_scc1 81f\n\t" "s_lshl_b32 s71, s70, 3\n\t" "s_lshl_b32 s71, 1, s71\n\t" "s_add_u32 s76, s76, s71\n\t" "81:\n\t" "v_mov_b32 v48, 0\n\t" \
   "v_mov_b32 v49, 0\n\t" "v_mov_b32 v50, 0\n\t" "v_mov_b32 v51, 0\n\t" "v_mov_b32 v52, 0\n\t" "v_mov_b32 v53, 0\n\t" "v_mov_b32 v54, 0\n\t" \
   "v_mov_b32 v55, 0\n\t" "s_add_u32 s71, s70, 1\n\t" "s_min_u32 s71, s71, 7\n\t" "s_lshr_b32 s67, s71, 1\n\t" "s_add_u32 s67, s67, %[xs]\n\t" \
   "s_and_b32 s67, s67, 3\n\t" "s_lshl_b32 s67, s67, 11\n\t" "s_and_b32 s72, s71, 1\n\t" "s_lshl_b32 s72, s72, 14\n\t" "s_add_u32 s67, s67, s72\n\t" \
   "s_add_u32 s67, s67, s66\n\t" "s_cmp_lt_u32 s70, 6\n\t" "s_cbranch_scc1 20b\n\t" "s_sleep 3\n\t" "s_cmp_lt_u32 s70, 0x20000\n\t" \
   "s_cbranch_scc1 20b\n\t" "s_mov_b32 s67, %[esc]\n\t" "s_branch 20b\n\t" "90:\n\t" "s_waitcnt vmcnt(0) lgkmcnt(0)"
#define PV_CLOB "v16", "v17", "v18", "v19", "v20", "v21", "v22", "v23", "v24", "v25", "v26", "v27", "v28", "v29", "v30", "v31", "v48", "v49", "v50", "v51", "v52", "v53", "v54", "v55", "v56", "v57", "v58", "v59", "v60", "v61", "v62", "v63", "v64", "v65", "v66", "v67", "v68", "v69", "v70", "v71", "v72", "v73", "v74", "v75", "v76", "v77", "v78", "v79", "v80", "v81", "v82", "v83", "v84", "v85", "v86", "v87", "v88", "v89", "v90", "v91", "v92", "v93", "v94", "v95", "v96", "v97", "v98", "v99", "v100", "v101", "v102", "v103", "v104", "v105", "v106", "v107", "v108", "v109", "v110", "v111", "v144", "v145", "v146", "v147", "v148", "v149", "v150", "v151", "v152", "v153", "v154", "v155", "v156", "v157", "v158", "v159", "v160", "v161", "v162", "v163", "v164", "v165", "v166", "v167", "v168", "v169", "v170", "v171", "v172", "v173", "v174", "v175", "s60", "s61", "s62", "s63", "s64", "s65", "s66", "s67", "s68", "s69", "s70", "s71", "s72", "s73", "s74", "s75", "s45", "s46", "s47", "s48", "s49", "s50", "s51", "s52", "s53", "s54"
#define WAIT_ASM \
   "11:\n\t" "s_memrealtime s[60:61]\n\t" "s_memtime s[62:63]\n\t" "s_waitcnt lgkmcnt(0)\n\t" "s_sub_u32 s74, s62, s57\n\t" \
   "s_mul_i32 s74, s74, s58\n\t" "s_lshr_b32 s74, s74, 12\n\t" "s_add_u32 s74, s74, s56\n\t" "s_lshl_b32 s60, s60, 3\n\t" "s_sub_u32 s75, s74, s60\n\t" \
   "s_cmp_lt_u32 s75, 32\n\t" "s_cbranch_scc1 12f\n\t" "s_cmp_lt_i32 s75, 0\n\t" "s_cselect_b32 s61, 0, 31\n\t" "s_add_u32 s74, s60, s61\n\t" \
   "s_sub_i32 s75, s61, s75\n\t" "s_add_i32 s75, s75, 1\n\t" "s_ashr_i32 s75, s75, 1\n\t" "s_min_i32 s75, s75, 8\n\t" "s_max_i32 s75, s75, -8\n\t" \
   "s_mov_b32 s56, s74\n\t" "s_mov_b32 s57, s62\n\t" "s_add_u32 s58, s58, s75\n\t" "12:\n\t" "s_sub_i32 s74, %[ta], s74\n\t" \
   "s_cmp_le_i32 s74, 150\n\t" "s_cbranch_scc1 13f\n\t" "s_cmp_lt_i32 s74, 260\n\t" "s_cbranch_scc1 11b\n\t" "s_sleep 2\n\t" \
   "s_cmp_lt_i32 s74, 32000\n\t" "s_cbranch_scc1 11b\n\t" "s_mov_b32 s74, 0\n\t" "13:\n\t" "s_sub_i32 s74, s74, 56\n\t" "s_max_i32 s74, s74, 0\n\t" \
   "s_mul_i32 s74, s74, 23\n\t" "s_lshr_b32 s74, s74, 5\n\t" "s_and_b32 s74, s74, -4\n\t" "s_getpc_b64 s[60:61]\n\t" "14:\n\t" \
   "s_sub_u32 s74, 15f-14b, s74\n\t" "s_add_u32 s60, s60, s74\n\t" "s_addc_u32 s61, s61, 0\n\t" "s_setpc_b64 s[60:61]\n\t" "s_nop 3\n\t" "s_nop 3\n\t" \
   "s_nop 3\n\t" "s_nop 3\n\t" "s_nop 3\n\t" "s_nop 3\n\t" "s_nop 3\n\t" "s_nop 3\n\t" "s_nop 3\n\t" "s_nop 3\n\t" "s_nop 3\n\t" "s_nop 3\n\t" \
   "s_nop 3\n\t" "s_nop 3\n\t" "s_nop 3\n\t" "s_nop 3\n\t" "s_nop 3\n\t" "s_nop 3\n\t" "s_nop 3\n\t" "s_nop 3\n\t" "s_nop 3\n\t" "s_nop 3\n\t" \
   "s_nop 3\n\t" "s_nop 3\n\t" "15:\n\t" ""
#define WAIT_CLOB "s60", "s61", "s62", "s63", "s74", "s75"
#define FIN_ASM \
   "s_mov_b32 s40, 0\n\t" "s_mov_b32 s76, 0\n\t" "s_mov_b32 s77, 0\n\t" "s_sub_u32 s45, s56, 0x10000\n\t" "v_mov_b32 v13, -2\n\t" \
   "v_mov_b32 v11, -1\n\t" "30:\n\t" "s_lshr_b64 s[72:73], %[sel], s40\n\t" "s_lshl_b32 s41, s40, 19\n\t" "s_and_b32 s72, s72, 1\n\t" \
   "s_add_u32 s41, s41, %[pool]\n\t" "s_lshl_b32 s72, s72, 13\n\t" "s_add_u32 s41, s41, s72\n\t" "s_lshr_b64 s[72:73], %[s3], s40\n\t" \
   "s_and_b32 s47, s72, 1\n\t" "s_lshl_b32 s47, s47, 14\n\t" "s_mul_i32 s49, s40, 1310720\n\t" "s_add_u32 s49, s49, %[su]\n\t" \
   "s_lshr_b64 s[72:73], %[ysel], s40\n\t" "s_mul_i32 s73, s40, 43\n\t" "s_mul_i32 s50, s40, 80\n\t" "s_and_b32 s72, s72, 1\n\t" \
   "s_lshr_b32 s73, s73, 7\n\t" "s_add_u32 s50, s50, s72\n\t" "s_lshl4_add_u32 s50, s73, s50\n\t" "s_lshl_b32 s50, s50, 13\n\t" \
   "s_add_u32 s50, s50, %[yb]\n\t" "s_add_u32 s51, s50, 4096\n\t" "s_lshl_b32 s52, s40, 12\n\t" "s_add_u32 s52, s52, %[uf]\n\t" \
   "s_add_u32 s69, s45, %[tu]\n\t" "v_max3_u32 v15, v8, v9, v10\n\t" "v_max_u32 v15, v15, v11\n\t" "v_cmp_eq_u32 vcc, -1, v15\n\t" \
   "s_add_u32 s40, s40, 1\n\t" "s_cbranch_vccnz 95f\n\t" "96:\n\t" "11:\n\t" "s_memrealtime s[60:61]\n\t" "s_memtime s[62:63]\n\t" \
   "s_waitcnt lgkmcnt(0)\n\t" "s_sub_u32 s74, s62, s57\n\t" "s_mul_i32 s74, s74, s58\n\t" "s_lshr_b32 s74, s74, 12\n\t" "s_add_u32 s74, s74, s56\n\t" \
   "s_lshl_b32 s60, s60, 3\n\t" "s_sub_u32 s75, s74, s60\n\t" "s_cmp_lt_u32 s75, 32\n\t" "s_cbranch_scc1 12f\n\t" "s_cmp_lt_i32 s75, 0\n\t" \
   "s_cselect_b32 s61, 0, 31\n\t" "s_add_u32 s74, s60, s61\n\t" "s_sub_i32 s75, s61, s75\n\t" "s_add_i32 s75, s75, 1\n\t" "s_ashr_i32 s75, s75, 1\n\t" \
   "s_min_i32 s75, s75, 8\n\t" "s_max_i32 s75, s75, -8\n\t" "s_mov_b32 s56, s74\n\t" "s_mov_b32 s57, s62\n\t" "s_add_u32 s58, s58, s75\n\t" "12:\n\t" \
   "s_sub_i32 s74, s69, s74\n\t" "s_cmp_le_i32 s74, 150\n\t" "s_cbranch_scc1 13f\n\t" "s_cmp_lt_i32 s74, 260\n\t" "s_cbranch_scc1 11b\n\t" \
   "s_sleep 2\n\t" "s_cmp_lt_i32 s74, 32000\n\t" "s_cbranch_scc1 11b\n\t" "s_mov_b32 s74, 0\n\t" "13:\n\t" "s_sub_i32 s74, s74, 56\n\t" \
   "s_max_i32 s74, s74, 0\n\t" "s_mul_i32 s74, s74, 23\n\t" "s_lshr_b32 s74, s74, 5\n\t" "s_and_b32 s74, s74, -4\n\t" "s_getpc_b64 s[60:61]\n\t" \
   "14:\n\t" "s_sub_u32 s74, 15f-14b, s74\n\t" "s_add_u32 s60, s60, s74\n\t" "s_addc_u32 s61, s61, 0\n\t" "s_setpc_b64 s[60:61]\n\t" "s_nop 3\n\t" \
   "s_nop 3\n\t" "s_nop 3\n\t" "s_nop 3\n\t" "s_nop 3\n\t" "s_nop 3\n\t" "s_nop 3\n\t" "s_nop 3\n\t" "s_nop 3\n\t" "s_nop 3\n\t" "s_nop 3\n\t" \
   "s_nop 3\n\t" "s_nop 3\n\t" "s_nop 3\n\t" "s_nop 3\n\t" "s_nop 3\n\t" "s_nop 3\n\t" "s_nop 3\n\t" "s_nop 3\n\t" "s_nop 3\n\t" "s_nop 3\n\t" \
   "s_nop 3\n\t" "s_nop 3\n\t" "s_nop 3\n\t" "15:\n\t" RSTAMP(4) "buffer_load_dwordx4 a[0:3], v16, %[rs], s49 offen offset:0 sc1\n\t" "s_nop 0\n\t" \
   "buffer_load_dwordx4 a[4:7], v16, %[rs], s49 offen offset:1024 sc1\n\t" "s_nop 0\n\t" \
   "buffer_load_dwordx4 a[8:11], v16, %[rs], s49 offen offset:2048 sc1\n\t" "s_nop 0\n\t" \
   "buffer_load_dwordx4 a[12:15], v16, %[rs], s49 offen offset:3072 sc1\n\t" "s_nop 0\n\t" "s_add_u32 s49, s49, 4096\n\t" \
   "buffer_load_dwordx4 a[16:19], v16, %[rs], s49 offen offset:0 sc1\n\t" "s_nop 0\n\t" \
   "buffer_load_dwordx4 a[20:23], v16, %[rs], s49 offen offset:1024 sc1\n\t" "s_nop 0\n\t" \
   "buffer_load_dwordx4 a[24:27], v16, %[rs], s49 offen offset:2048 sc1\n\t" "s_nop 0\n\t" \
   "buffer_load_dwordx4 a[28:31], v16, %[rs], s49 offen offset:3072 sc1\n\t" "s_nop 0\n\t" "s_add_u32 s49, s49, 4096\n\t" \
   "buffer_load_dwordx4 a[32:35], v16, %[rs], s49 offen offset:0 sc1\n\t" "s_nop 0\n\t" \
   "buffer_load_dwordx4 a[36:39], v16, %[rs], s49 offen offset:1024 sc1\n\t" "s_nop 0\n\t" \
   "buffer_load_dwordx4 a[40:43], v16, %[rs], s49 offen offset:2048 sc1\n\t" "s_nop 0\n\t" \
   "buffer_load_dwordx4 a[44:47], v16, %[rs], s49 offen offset:3072 sc1\n\t" "s_nop 0\n\t" "s_add_u32 s49, s49, 4096\n\t" \
   "buffer_load_dwordx4 a[48:51], v16, %[rs], s49 offen offset:0 sc1\n\t" "s_nop 0\n\t" \
   "buffer_load_dwordx4 a[52:55], v16, %[rs], s49 offen offset:1024 sc1\n\t" "s_nop 0\n\t" \
   "buffer_load_dwordx4 a[56:59], v16, %[rs], s49 offen offset:2048 sc1\n\t" "s_nop 0\n\t" \
   "buffer_load_dwordx4 a[60:63], v16, %[rs], s49 offen offset:3072 sc1\n\t" "s_nop 0\n\t" "s_add_u32 s49, s49, 4096\n\t" \
   "buffer_load_dwordx4 a[64:67], v16, %[rs], s49 offen offset:0 sc1\n\t" "s_nop 0\n\t" \
   "buffer_load_dwordx4 a[68:71], v16, %[rs], s49 offen offset:1024 sc1\n\t" "s_nop 0\n\t" \
   "buffer_load_dwordx4 a[72:75], v16, %[rs], s49 offen offset:2048 sc1\n\t" "s_nop 0\n\t" \
   "buffer_load_dwordx4 a[76:79], v16, %[rs], s49 offen offset:3072 sc1\n\t" "s_nop 0\n\t" "s_add_u32 s49, s49, 4096\n\t" \
   "buffer_load_dwordx4 a[80:83], v16, %[rs], s49 offen offset:0 sc1\n\t" "s_nop 0\n\t" \
   "buffer_load_dwordx4 a[84:87], v16, %[rs], s49 offen offset:1024 sc1\n\t" "s_nop 0\n\t" \
   "buffer_load_dwordx4 a[88:91], v16, %[rs], s49 offen offset:2048 sc1\n\t" "s_nop 0\n\t" \
   "buffer_load_dwordx4 a[92:95], v16, %[rs], s49 offen offset:3072 sc1\n\t" "s_nop 0\n\t" "s_add_u32 s49, s49, 4096\n\t" \
   "buffer_load_dwordx4 a[96:99], v16, %[rs], s49 offen offset:0 sc1\n\t" "s_nop 0\n\t" \
   "buffer_load_dwordx4 a[100:103], v16, %[rs], s49 offen offset:1024 sc1\n\t" "s_nop 0\n\t" \
   "buffer_load_dwordx4 a[104:107], v16, %[rs], s49 offen offset:2048 sc1\n\t" "s_nop 0\n\t" \
   "buffer_load_dwordx4 a[108:111], v16, %[rs], s49 offen offset:3072 sc1\n\t" "s_nop 0\n\t" "s_add_u32 s49, s49, 4096\n\t" \
   "buffer_load_dwordx4 a[112:115], v16, %[rs], s49 offen offset:0 sc1\n\t" "s_nop 0\n\t" \
   "buffer_load_dwordx4 a[116:119], v16, %[rs], s49 offen offset:1024 sc1\n\t" "s_nop 0\n\t" \
   "buffer_load_dwordx4 a[120:123], v16, %[rs], s49 offen offset:2048 sc1\n\t" "s_nop 0\n\t" \
   "buffer_load_dwordx4 a[124:127], v16, %[rs], s49 offen offset:3072 sc1\n\t" "s_nop 0\n\t" "s_add_u32 s52, s52, 4096\n\t" \
   "buffer_load_dwordx4 v[8:11], v19, %[rs], s52 offen sc1\n\t" RSTAMP(0) "s_mov_b32 s46, 0\n\t" "s_xor_b32 s64, s47, %[lad0]\n\t" \
   "s_and_b32 s65, s64, 0x4000\n\t" "s_xor_b32 s64, s64, s65\n\t" "v_or_b32 v224, s65, v16\n\t" "v_or_b32 v225, s65, v17\n\t" \
   "v_xor_b32 v226, 0x4000, v224\n\t" "v_xor_b32 v227, 0x4000, v225\n\t" "s_add_u32 s64, s64, s41\n\t" "v_xor_b32 v14, s47, v20\n\t" \
   "v_xor_b32 v15, 0x4000, v14\n\t" "s_add_u32 s65, s64, 32768\n\t" "s_add_u32 s66, s64, 65536\n\t" "s_add_u32 s67, s64, 98304\n\t" \
   "s_add_u32 s68, s64, 131072\n\t" "s_add_u32 s69, s64, 163840\n\t" "s_add_u32 s70, s64, 196608\n\t" "s_add_u32 s71, s64, 229376\n\t" \
   "s_add_u32 s72, s64, 262144\n\t" "s_add_u32 s73, s64, 294912\n\t" "s_add_u32 s53, s41, 4096\n\t" "v_mov_b32 v28, 0\n\t" "v_mov_b32 v29, 0\n\t" \
   "v_mov_b32 v30, 0\n\t" "v_mov_b32 v31, 0\n\t" "19:\n\t" "ds_read_b128 v[24:27], v23\n\t" "s_waitcnt lgkmcnt(0)\n\t" \
   "v_readfirstlane_b32 s74, v26\n\t" "v_readfirstlane_b32 s44, v24\n\t" "v_readfirstlane_b32 s45, v25\n\t" "s_sub_u32 s48, s44, 110\n\t" \
   "s_cmp_lt_u32 s74, s40\n\t" "s_cbranch_scc1 18f\n\t" RSTAMP(7) "21:\n\t" "s_memrealtime s[60:61]\n\t" "s_memtime s[62:63]\n\t" \
   "s_waitcnt lgkmcnt(0)\n\t" "s_sub_u32 s74, s62, s57\n\t" "s_mul_i32 s74, s74, s58\n\t" "s_lshr_b32 s74, s74, 12\n\t" "s_add_u32 s74, s74, s56\n\t" \
   "s_lshl_b32 s60, s60, 3\n\t" "s_sub_u32 s75, s74, s60\n\t" "s_cmp_lt_u32 s75, 32\n\t" "s_cbranch_scc1 22f\n\t" "s_cmp_lt_i32 s75, 0\n\t" \
   "s_cselect_b32 s61, 0, 31\n\t" "s_add_u32 s74, s60, s61\n\t" "s_sub_i32 s75, s61, s75\n\t" "s_add_i32 s75, s75, 1\n\t" "s_ashr_i32 s75, s75, 1\n\t" \
   "s_min_i32 s75, s75, 8\n\t" "s_max_i32 s75, s75, -8\n\t" "s_mov_b32 s56, s74\n\t" "s_mov_b32 s57, s62\n\t" "s_add_u32 s58, s58, s75\n\t" "22:\n\t" \
   "s_sub_i32 s74, s48, s74\n\t" "s_cmp_le_i32 s74, 150\n\t" "s_cbranch_scc1 23f\n\t" "s_cmp_lt_i32 s74, 260\n\t" "s_cbranch_scc1 21b\n\t" \
   "s_sleep 2\n\t" "s_cmp_lt_i32 s74, 32000\n\t" "s_cbranch_scc1 21b\n\t" "s_mov_b32 s74, 0\n\t" "23:\n\t" "s_sub_i32 s74, s74, 56\n\t" \
   "s_max_i32 s74, s74, 0\n\t" "s_mul_i32 s74, s74, 23\n\t" "s_lshr_b32 s74, s74, 5\n\t" "s_and_b32 s74, s74, -4\n\t" "s_getpc_b64 s[60:61]\n\t" \
   "24:\n\t" "s_sub_u32 s74, 25f-24b, s74\n\t" "s_add_u32 s60, s60, s74\n\t" "s_addc_u32 s61, s61, 0\n\t" "s_setpc_b64 s[60:61]\n\t" "s_nop 3\n\t" \
   "s_nop 3\n\t" "s_nop 3\n\t" "s_nop 3\n\t" "s_nop 3\n\t" "s_nop 3\n\t" "s_nop 3\n\t" "s_nop 3\n\t" "s_nop 3\n\t" "s_nop 3\n\t" "s_nop 3\n\t" \
   "s_nop 3\n\t" "s_nop 3\n\t" "s_nop 3\n\t" "s_nop 3\n\t" "s_nop 3\n\t" "s_nop 3\n\t" "s_nop 3\n\t" "s_nop 3\n\t" "s_nop 3\n\t" "s_nop 3\n\t" \
   "s_nop 3\n\t" "s_nop 3\n\t" "s_nop 3\n\t" "25:\n\t" "buffer_load_dwordx4 v[32:35], v16, %[rs], s50 offen offset:0 sc1\n\t" \
   "buffer_load_dwordx4 v[36:39], v16, %[rs], s50 offen offset:1024 sc1\n\t" "buffer_load_dwordx4 v[40:43], v16, %[rs], s50 offen offset:2048 sc1\n\t" \
   "buffer_load_dwordx4 v[44:47], v16, %[rs], s50 offen offset:3072 sc1\n\t" "buffer_load_dwordx4 v[48:51], v16, %[rs], s51 offen offset:0 sc1\n\t" \
   "buffer_load_dwordx4 v[52:55], v16, %[rs], s51 offen offset:1024 sc1\n\t" "buffer_load_dwordx4 v[56:59], v16, %[rs], s51 offen offset:2048 sc1\n\t" \
   "buffer_load_dwordx4 v[60:63], v16, %[rs], s51 offen offset:3072 sc1\n\t" "s_nop 3\n\t" "s_nop 3\n\t" "s_nop 3\n\t" "s_nop 3\n\t" "s_nop 3\n\t" \
   "s_nop 3\n\t" "s_nop 3\n\t" "s_nop 3\n\t" "s_nop 3\n\t" "s_nop 3\n\t" "s_nop 3\n\t" "s_nop 3\n\t" "s_nop 3\n\t" "s_nop 3\n\t" "s_nop 3\n\t" \
   "s_nop 3\n\t" RSTAMP(6) "buffer_load_dwordx4 v[64:67], v224, %[rs], s64 offen offset:0 sc1\n\t" \
   "buffer_load_dwordx4 v[68:71], v224, %[rs], s64 offen offset:1024 sc1\n\t" "buffer_load_dwordx4 v[72:75], v224, %[rs], s65 offen offset:0 sc1\n\t" \
   "buffer_load_dwordx4 v[76:79], v224, %[rs], s65 offen offset:1024 sc1\n\t" "buffer_load_dwordx4 v[80:83], v224, %[rs], s66 offen offset:0 sc1\n\t" \
   "buffer_load_dwordx4 v[84:87], v224, %[rs], s66 offen offset:1024 sc1\n\t" "buffer_load_dwordx4 v[88:91], v224, %[rs], s67 offen offset:0 sc1\n\t" \
   "buffer_load_dwordx4 v[92:95], v224, %[rs], s67 offen offset:1024 sc1\n\t" "buffer_load_dwordx4 v[96:99], v224, %[rs], s68 offen offset:0 sc1\n\t" \
   "buffer_load_dwordx4 v[100:103], v224, %[rs], s68 offen offset:1024 sc1\n\t" \
   "buffer_load_dwordx4 v[104:107], v224, %[rs], s69 offen offset:0 sc1\n\t" \
   "buffer_load_dwordx4 v[108:111], v224, %[rs], s69 offen offset:1024 sc1\n\t" \
   "buffer_load_dwordx4 v[112:115], v224, %[rs], s70 offen offset:0 sc1\n\t" \
   "buffer_load_dwordx4 v[116:119], v224, %[rs], s70 offen offset:1024 sc1\n\t" \
   "buffer_load_dwordx4 v[120:123], v224, %[rs], s71 offen offset:0 sc1\n\t" \
   "buffer_load_dwordx4 v[124:127], v224, %[rs], s71 offen offset:1024 sc1\n\t" \
   "buffer_load_dwordx2 v[128:129], v225, %[rs], s72 offen offset:0 sc1\n\t" \
   "buffer_load_dwordx2 v[130:131], v225, %[rs], s72 offen offset:128 sc1\n\t" \
   "buffer_load_dwordx2 v[132:133], v225, %[rs], s72 offen offset:1024 sc1\n\t" \
   "buffer_load_dwordx2 v[134:135], v225, %[rs], s72 offen offset:1152 sc1\n\t" \
   "buffer_load_dwordx2 v[136:137], v225, %[rs], s73 offen offset:0 sc1\n\t" \
   "buffer_load_dwordx2 v[138:139], v225, %[rs], s73 offen offset:128 sc1\n\t" \
   "buffer_load_dwordx2 v[140:141], v225, %[rs], s73 offen offset:1024 sc1\n\t" \
   "buffer_load_dwordx2 v[142:143], v225, %[rs], s73 offen offset:1152 sc1\n\t" YSTAMP(24) "s_waitcnt vmcnt(23)\n\t" RSTAMP(1) \
   "v_smfmac_f32_16x16x64_bf16 v[28:31], v[64:67], a[0:7], v22\n\t" "s_waitcnt vmcnt(22)\n\t" \
   "v_smfmac_f32_16x16x64_bf16 v[28:31], v[68:71], a[8:15], v22\n\t" "s_waitcnt vmcnt(21)\n\t" \
   "v_smfmac_f32_16x16x64_bf16 v[28:31], v[72:75], a[16:23], v22\n\t" "s_waitcnt vmcnt(20)\n\t" \
   "v_smfmac_f32_16x16x64_bf16 v[28:31], v[76:79], a[24:31], v22\n\t" "s_waitcnt vmcnt(19)\n\t" \
   "v_smfmac_f32_16x16x64_bf16 v[28:31], v[80:83], a[32:39], v22\n\t" "s_waitcnt vmcnt(18)\n\t" \
   "v_smfmac_f32_16x16x64_bf16 v[28:31], v[84:87], a[40:47], v22\n\t" "s_waitcnt vmcnt(17)\n\t" \
   "v_smfmac_f32_16x16x64_bf16 v[28:31], v[88:91], a[48:55], v22\n\t" "s_waitcnt vmcnt(16)\n\t" \
   "v_smfmac_f32_16x16x64_bf16 v[28:31], v[92:95], a[56:63], v22\n\t" "s_waitcnt vmcnt(15)\n\t" \
   "v_smfmac_f32_16x16x64_bf16 v[28:31], v[96:99], a[64:71], v22\n\t" "s_waitcnt vmcnt(14)\n\t" \
   "v_smfmac_f32_16x16x64_bf16 v[28:31], v[100:103], a[72:79], v22\n\t" "s_waitcnt vmcnt(13)\n\t" \
   "v_smfmac_f32_16x16x64_bf16 v[28:31], v[104:107], a[80:87], v22\n\t" "s_waitcnt vmcnt(12)\n\t" \
   "v_smfmac_f32_16x16x64_bf16 v[28:31], v[108:111], a[88:95], v22\n\t" "s_waitcnt vmcnt(11)\n\t" \
   "v_smfmac_f32_16x16x64_bf16 v[28:31], v[112:115], a[96:103], v22\n\t" "s_waitcnt vmcnt(10)\n\t" \
   "v_smfmac_f32_16x16x64_bf16 v[28:31], v[116:119], a[104:111], v22\n\t" "s_waitcnt vmcnt(9)\n\t" \
   "v_smfmac_f32_16x16x64_bf16 v[28:31], v[120:123], a[112:119], v22\n\t" "s_waitcnt vmcnt(8)\n\t" \
   "v_smfmac_f32_16x16x64_bf16 v[28:31], v[124:127], a[120:127], v22\n\t" "s_waitcnt vmcnt(6)\n\t" \
   "v_smfmac_f32_16x16x64_bf16 v[28:31], v[128:131], v[32:39], v22\n\t" "s_waitcnt vmcnt(4)\n\t" \
   "v_smfmac_f32_16x16x64_bf16 v[28:31], v[132:135], v[40:47], v22\n\t" "s_waitcnt vmcnt(2)\n\t" \
   "v_smfmac_f32_16x16x64_bf16 v[28:31], v[136:139], v[48:55], v22\n\t" "s_waitcnt vmcnt(0)\n\t" \
   "v_smfmac_f32_16x16x64_bf16 v[28:31], v[140:143], v[56:63], v22\n\t" "s_nop 7\n\t" "v_add_f32 v28, v28, v29\n\t" "v_add_f32 v30, v30, v31\n\t" \
   "v_cvt_pk_bf16_f32 v24, v28, v30\n\t" "v_cmp_u_f32 vcc, v28, v30\n\t" "s_nop 0\n\t" \
   "v_mov_b32_dpp v25, v24 quad_perm:[1,0,3,2] row_mask:0xf bank_mask:0xf\n\t" "s_cbranch_vccnz 8f\n\t" "2:\n\t" RSTAMP(2) \
   "v_perm_b32 v26, v25, v24, v18\n\t" "s_nop 1\n\t" "v_min_u32_dpp v24, v26, v13 quad_perm:[0,1,0,1] row_mask:0xf bank_mask:0xf\n\t" \
   "v_mov_b32_dpp v25, v26 quad_perm:[2,3,2,3] row_mask:0xf bank_mask:0xf\n\t" "buffer_store_dwordx2 v[24:25], v14, %[rs], s41 offen sc1\n\t" \
   "buffer_store_dwordx2 v[24:25], v14, %[rs], s53 offen sc1\n\t" "s_memtime s[62:63]\n\t" "s_memrealtime s[60:61]\n\t" \
   "buffer_store_dwordx2 v[24:25], v15, %[rs], s41 offen sc1\n\t" "buffer_store_dwordx2 v[24:25], v15, %[rs], s53 offen sc1\n\t" RSTAMP(3) \
   "s_waitcnt lgkmcnt(0)\n\t" "s_sub_u32 s74, s62, s57\n\t" "s_mul_i32 s74, s74, s58\n\t" "s_lshr_b32 s74, s74, 12\n\t" "s_add_u32 s74, s74, s56\n\t" \
   "s_lshl_b32 s60, s60, 3\n\t" "s_sub_u32 s75, s74, s60\n\t" "s_cmp_lt_u32 s75, 32\n\t" "s_cbranch_scc1 38f\n\t" "s_cmp_lt_i32 s75, 0\n\t" \
   "s_cselect_b32 s61, 0, 31\n\t" "s_add_u32 s74, s60, s61\n\t" "s_sub_i32 s75, s61, s75\n\t" "s_add_i32 s75, s75, 1\n\t" "s_ashr_i32 s75, s75, 1\n\t" \
   "s_min_i32 s75, s75, 8\n\t" "s_max_i32 s75, s75, -8\n\t" "s_mov_b32 s56, s74\n\t" "s_mov_b32 s57, s62\n\t" "s_add_u32 s58, s58, s75\n\t" "38:\n\t" \
   "s_min_u32 s75, s46, 1\n\t" "s_and_b32 s74, s74, -4\n\t" "s_or_b32 s74, s74, s75\n\t" "v_mov_b32 v26, s74\n\t" \
   "buffer_store_dword v26, v21, %[rs], s41 offen sc1\n\t" FINTRACE "v_xor_b32 v23, 32, v23\n\t" "s_cmp_lt_u32 s40, 63\n\t" "s_cbranch_scc1 30b\n\t" \
   "s_branch 90f\n\t" "18:\n\t" "s_sleep 2\n\t" "s_branch 19b\n\t" "8:\n\t" "s_mov_b32 s74, 0\n\t" "87:\n\t" "v_max3_u32 v12, v32, v34, v36\n\t" \
   "v_max3_u32 v12, v12, v38, v40\n\t" "v_max3_u32 v12, v12, v42, v44\n\t" "v_max3_u32 v12, v12, v46, v48\n\t" "v_max3_u32 v12, v12, v50, v52\n\t" \
   "v_max3_u32 v12, v12, v54, v56\n\t" "v_max3_u32 v12, v12, v58, v60\n\t" "v_max_u32 v12, v12, v62\n\t" "v_cmp_eq_u32 vcc, -1, v12\n\t" \
   "s_cmp_gt_u32 s74, 0x10000\n\t" "s_cbranch_scc1 86f\n\t" "s_cbranch_vccz 86f\n\t" "s_cmp_eq_u32 s74, 0\n\t" "s_cselect_b32 s75, 0x100, 0\n\t" \
   "s_cbranch_scc1 88f\n\t" "s_sleep 3\n\t" "88:\n\t" "buffer_load_dwordx4 v[32:35], v16, %[rs], s50 offen offset:0 sc1\n\t" \
   "buffer_load_dwordx4 v[36:39], v16, %[rs], s50 offen offset:1024 sc1\n\t" "buffer_load_dwordx4 v[40:43], v16, %[rs], s50 offen offset:2048 sc1\n\t" \
   "buffer_load_dwordx4 v[44:47], v16, %[rs], s50 offen offset:3072 sc1\n\t" "buffer_load_dwordx4 v[48:51], v16, %[rs], s51 offen offset:0 sc1\n\t" \
   "buffer_load_dwordx4 v[52:55], v16, %[rs], s51 offen offset:1024 sc1\n\t" "buffer_load_dwordx4 v[56:59], v16, %[rs], s51 offen offset:2048 sc1\n\t" \
   "buffer_load_dwordx4 v[60:63], v16, %[rs], s51 offen offset:3072 sc1\n\t" "s_add_u32 s76, s76, s75\n\t" "s_add_u32 s74, s74, 1\n\t" \
   "s_waitcnt vmcnt(0)\n\t" "s_branch 87b\n\t" "86:\n\t" "v_mov_b32 v27, 0\n\t" "v_mov_b32 v28, 0\n\t" "v_mov_b32 v29, 0\n\t" "v_mov_b32 v30, 0\n\t" \
   "v_mov_b32 v31, 0\n\t" "v_max3_u32 v27, v27, v64, v66\n\t" "v_max3_u32 v27, v27, v68, v70\n\t" "v_max3_u32 v27, v27, v72, v74\n\t" \
   "v_max3_u32 v27, v27, v76, v78\n\t" "v_max3_u32 v27, v27, v80, v82\n\t" "v_max3_u32 v27, v27, v84, v86\n\t" "v_max3_u32 v27, v27, v88, v90\n\t" \
   "v_max3_u32 v27, v27, v92, v94\n\t" "v_max3_u32 v27, v27, v96, v98\n\t" "v_max3_u32 v27, v27, v100, v102\n\t" "v_max3_u32 v27, v27, v104, v106\n\t" \
   "v_max3_u32 v27, v27, v108, v110\n\t" "v_max3_u32 v27, v27, v112, v114\n\t" "v_max3_u32 v27, v27, v116, v118\n\t" \
   "v_max3_u32 v27, v27, v120, v122\n\t" "v_max3_u32 v27, v27, v124, v126\n\t" "v_max3_u32 v27, v27, v128, v130\n\t" \
   "v_max3_u32 v27, v27, v132, v134\n\t" "v_max3_u32 v27, v27, v136, v138\n\t" "v_max3_u32 v27, v27, v140, v142\n\t" "v_cmp_eq_u32 vcc, -1, v27\n\t" \
   "s_nop 0\n\t" "s_cbranch_vccnz 85f\n\t" "v_smfmac_f32_16x16x64_bf16 v[28:31], v[64:67], a[0:7], v22\n\t" \
   "v_smfmac_f32_16x16x64_bf16 v[28:31], v[68:71], a[8:15], v22\n\t" "v_smfmac_f32_16x16x64_bf16 v[28:31], v[72:75], a[16:23], v22\n\t" \
   "v_smfmac_f32_16x16x64_bf16 v[28:31], v[76:79], a[24:31], v22\n\t" "v_smfmac_f32_16x16x64_bf16 v[28:31], v[80:83], a[32:39], v22\n\t" \
   "v_smfmac_f32_16x16x64_bf16 v[28:31], v[84:87], a[40:47], v22\n\t" "v_smfmac_f32_16x16x64_bf16 v[28:31], v[88:91], a[48:55], v22\n\t" \
   "v_smfmac_f32_16x16x64_bf16 v[28:31], v[92:95], a[56:63], v22\n\t" "v_smfmac_f32_16x16x64_bf16 v[28:31], v[96:99], a[64:71], v22\n\t" \
   "v_smfmac_f32_16x16x64_bf16 v[28:31], v[100:103], a[72:79], v22\n\t" "v_smfmac_f32_16x16x64_bf16 v[28:31], v[104:107], a[80:87], v22\n\t" \
   "v_smfmac_f32_16x16x64_bf16 v[28:31], v[108:111], a[88:95], v22\n\t" "v_smfmac_f32_16x16x64_bf16 v[28:31], v[112:115], a[96:103], v22\n\t" \
   "v_smfmac_f32_16x16x64_bf16 v[28:31], v[116:119], a[104:111], v22\n\t" "v_smfmac_f32_16x16x64_bf16 v[28:31], v[120:123], a[112:119], v22\n\t" \
   "v_smfmac_f32_16x16x64_bf16 v[28:31], v[124:127], a[120:127], v22\n\t" "v_smfmac_f32_16x16x64_bf16 v[28:31], v[128:131], v[32:39], v22\n\t" \
   "v_smfmac_f32_16x16x64_bf16 v[28:31], v[132:135], v[40:47], v22\n\t" "v_smfmac_f32_16x16x64_bf16 v[28:31], v[136:139], v[48:55], v22\n\t" \
   "v_smfmac_f32_16x16x64_bf16 v[28:31], v[140:143], v[56:63], v22\n\t" "84:\n\t" "v_cmp_eq_u32 vcc, -1, v27\n\t" "s_cmp_gt_u32 s46, 0x10000\n\t" \
   "s_cbranch_scc1 83f\n\t" "s_cbranch_vccz 83f\n\t" "85:\n\t" "s_add_u32 s46, s46, 1\n\t" "s_cmp_gt_u32 s46, 2\n\t" "s_cbranch_scc1 81f\n\t" \
   "s_lshl_b32 s74, s46, 3\n\t" "s_lshl_b32 s74, 1, s74\n\t" "s_add_u32 s77, s77, s74\n\t" "81:\n\t" "s_min_u32 s74, s46, 7\n\t" \
   "s_lshr_b32 s64, s74, 1\n\t" "s_add_u32 s64, s64, %[xs]\n\t" "s_and_b32 s64, s64, 3\n\t" "s_lshl_b32 s64, s64, 11\n\t" "s_and_b32 s75, s74, 1\n\t" \
   "s_lshl_b32 s75, s75, 14\n\t" "s_xor_b32 s75, s75, s47\n\t" "s_xor_b32 s75, s75, %[lad0]\n\t" "s_and_b32 s75, s75, 0x4000\n\t" \
   "s_add_u32 s64, s64, s75\n\t" "s_add_u32 s64, s64, s41\n\t" "s_add_u32 s65, s64, 32768\n\t" "s_add_u32 s66, s64, 65536\n\t" \
   "s_add_u32 s67, s64, 98304\n\t" "s_add_u32 s68, s64, 131072\n\t" "s_add_u32 s69, s64, 163840\n\t" "s_add_u32 s70, s64, 196608\n\t" \
   "s_add_u32 s71, s64, 229376\n\t" "s_add_u32 s72, s64, 262144\n\t" "s_add_u32 s73, s64, 294912\n\t" "v_mov_b32 v28, 0\n\t" "v_mov_b32 v29, 0\n\t" \
   "v_mov_b32 v30, 0\n\t" "v_mov_b32 v31, 0\n\t" "v_mov_b32 v27, 0\n\t" "s_cmp_lt_u32 s46, 7\n\t" "s_cbranch_scc1 82f\n\t" "s_sleep 3\n\t" "82:\n\t" \
   RSTAMP(6) "buffer_load_dwordx4 v[64:67], v16, %[rs], s64 offen offset:0 sc1\n\t" \
   "buffer_load_dwordx4 v[68:71], v16, %[rs], s64 offen offset:1024 sc1\n\t" "buffer_load_dwordx4 v[72:75], v16, %[rs], s65 offen offset:0 sc1\n\t" \
   "buffer_load_dwordx4 v[76:79], v16, %[rs], s65 offen offset:1024 sc1\n\t" "buffer_load_dwordx4 v[80:83], v16, %[rs], s66 offen offset:0 sc1\n\t" \
   "buffer_load_dwordx4 v[84:87], v16, %[rs], s66 offen offset:1024 sc1\n\t" "buffer_load_dwordx4 v[88:91], v16, %[rs], s67 offen offset:0 sc1\n\t" \
   "buffer_load_dwordx4 v[92:95], v16, %[rs], s67 offen offset:1024 sc1\n\t" "buffer_load_dwordx4 v[96:99], v16, %[rs], s68 offen offset:0 sc1\n\t" \
   "buffer_load_dwordx4 v[100:103], v16, %[rs], s68 offen offset:1024 sc1\n\t" "buffer_load_dwordx4 v[104:107], v16, %[rs], s69 offen offset:0 sc1\n\t" \
   "buffer_load_dwordx4 v[108:111], v16, %[rs], s69 offen offset:1024 sc1\n\t" "buffer_load_dwordx4 v[112:115], v16, %[rs], s70 offen offset:0 sc1\n\t" \
   "buffer_load_dwordx4 v[116:119], v16, %[rs], s70 offen offset:1024 sc1\n\t" "buffer_load_dwordx4 v[120:123], v16, %[rs], s71 offen offset:0 sc1\n\t" \
   "buffer_load_dwordx4 v[124:127], v16, %[rs], s71 offen offset:1024 sc1\n\t" "buffer_load_dwordx2 v[128:129], v17, %[rs], s72 offen offset:0 sc1\n\t" \
   "buffer_load_dwordx2 v[130:131], v17, %[rs], s72 offen offset:128 sc1\n\t" \
   "buffer_load_dwordx2 v[132:133], v17, %[rs], s72 offen offset:1024 sc1\n\t" \
   "buffer_load_dwordx2 v[134:135], v17, %[rs], s72 offen offset:1152 sc1\n\t" "buffer_load_dwordx2 v[136:137], v17, %[rs], s73 offen offset:0 sc1\n\t" \
   "buffer_load_dwordx2 v[138:139], v17, %[rs], s73 offen offset:128 sc1\n\t" \
   "buffer_load_dwordx2 v[140:141], v17, %[rs], s73 offen offset:1024 sc1\n\t" \
   "buffer_load_dwordx2 v[142:143], v17, %[rs], s73 offen offset:1152 sc1\n\t" "s_waitcnt vmcnt(23)\n\t" \
   "v_smfmac_f32_16x16x64_bf16 v[28:31], v[64:67], a[0:7], v22\n\t" "v_max3_u32 v27, v27, v64, v66\n\t" "s_waitcnt vmcnt(22)\n\t" \
   "v_smfmac_f32_16x16x64_bf16 v[28:31], v[68:71], a[8:15], v22\n\t" "v_max3_u32 v27, v27, v68, v70\n\t" "s_waitcnt vmcnt(21)\n\t" \
   "v_smfmac_f32_16x16x64_bf16 v[28:31], v[72:75], a[16:23], v22\n\t" "v_max3_u32 v27, v27, v72, v74\n\t" "s_waitcnt vmcnt(20)\n\t" \
   "v_smfmac_f32_16x16x64_bf16 v[28:31], v[76:79], a[24:31], v22\n\t" "v_max3_u32 v27, v27, v76, v78\n\t" "s_waitcnt vmcnt(19)\n\t" \
   "v_smfmac_f32_16x16x64_bf16 v[28:31], v[80:83], a[32:39], v22\n\t" "v_max3_u32 v27, v27, v80, v82\n\t" "s_waitcnt vmcnt(18)\n\t" \
   "v_smfmac_f32_16x16x64_bf16 v[28:31], v[84:87], a[40:47], v22\n\t" "v_max3_u32 v27, v27, v84, v86\n\t" "s_waitcnt vmcnt(17)\n\t" \
   "v_smfmac_f32_16x16x64_bf16 v[28:31], v[88:91], a[48:55], v22\n\t" "v_max3_u32 v27, v27, v88, v90\n\t" "s_waitcnt vmcnt(16)\n\t" \
   "v_smfmac_f32_16x16x64_bf16 v[28:31], v[92:95], a[56:63], v22\n\t" "v_max3_u32 v27, v27, v92, v94\n\t" "s_waitcnt vmcnt(15)\n\t" \
   "v_smfmac_f32_16x16x64_bf16 v[28:31], v[96:99], a[64:71], v22\n\t" "v_max3_u32 v27, v27, v96, v98\n\t" "s_waitcnt vmcnt(14)\n\t" \
   "v_smfmac_f32_16x16x64_bf16 v[28:31], v[100:103], a[72:79], v22\n\t" "v_max3_u32 v27, v27, v100, v102\n\t" "s_waitcnt vmcnt(13)\n\t" \
   "v_smfmac_f32_16x16x64_bf16 v[28:31], v[104:107], a[80:87], v22\n\t" "v_max3_u32 v27, v27, v104, v106\n\t" "s_waitcnt vmcnt(12)\n\t" \
   "v_smfmac_f32_16x16x64_bf16 v[28:31], v[108:111], a[88:95], v22\n\t" "v_max3_u32 v27, v27, v108, v110\n\t" "s_waitcnt vmcnt(11)\n\t" \
   "v_smfmac_f32_16x16x64_bf16 v[28:31], v[112:115], a[96:103], v22\n\t" "v_max3_u32 v27, v27, v112, v114\n\t" "s_waitcnt vmcnt(10)\n\t" \
   "v_smfmac_f32_16x16x64_bf16 v[28:31], v[116:119], a[104:111], v22\n\t" "v_max3_u32 v27, v27, v116, v118\n\t" "s_waitcnt vmcnt(9)\n\t" \
   "v_smfmac_f32_16x16x64_bf16 v[28:31], v[120:123], a[112:119], v22\n\t" "v_max3_u32 v27, v27, v120, v122\n\t" "s_waitcnt vmcnt(8)\n\t" \
   "v_smfmac_f32_16x16x64_bf16 v[28:31], v[124:127], a[120:127], v22\n\t" "v_max3_u32 v27, v27, v124, v126\n\t" "s_waitcnt vmcnt(6)\n\t" \
   "v_smfmac_f32_16x16x64_bf16 v[28:31], v[128:131], v[32:39], v22\n\t" "v_max3_u32 v27, v27, v128, v130\n\t" "s_waitcnt vmcnt(4)\n\t" \
   "v_smfmac_f32_16x16x64_bf16 v[28:31], v[132:135], v[40:47], v22\n\t" "v_max3_u32 v27, v27, v132, v134\n\t" "s_waitcnt vmcnt(2)\n\t" \
   "v_smfmac_f32_16x16x64_bf16 v[28:31], v[136:139], v[48:55], v22\n\t" "v_max3_u32 v27, v27, v136, v138\n\t" "s_waitcnt vmcnt(0)\n\t" \
   "v_smfmac_f32_16x16x64_bf16 v[28:31], v[140:143], v[56:63], v22\n\t" "v_max3_u32 v27, v27, v140, v142\n\t" "s_branch 84b\n\t" "83:\n\t" \
   "s_or_b32 s46, s46, 0x10000000\n\t" "s_nop 4\n\t" "v_add_f32 v28, v28, v29\n\t" "v_add_f32 v30, v30, v31\n\t" "v_cvt_pk_bf16_f32 v24, v28, v30\n\t" \
   "s_nop 1\n\t" "v_mov_b32_dpp v25, v24 quad_perm:[1,0,3,2] row_mask:0xf bank_mask:0xf\n\t" "s_branch 2b\n\t" "95:\n\t" "s_mov_b32 s74, 0\n\t" \
   "97:\n\t" "buffer_load_dwordx4 v[8:11], v19, %[rs], s52 offen sc1\n\t" "s_add_u32 s74, s74, 1\n\t" "s_waitcnt vmcnt(0)\n\t" \
   "v_max3_u32 v15, v8, v9, v10\n\t" "v_max_u32 v15, v15, v11\n\t" "v_cmp_eq_u32 vcc, -1, v15\n\t" "s_cmp_gt_u32 s74, 0x10000\n\t" \
   "s_cbranch_scc1 96b\n\t" "s_cbranch_vccz 96b\n\t" "s_sleep 2\n\t" "s_branch 97b\n\t" "90:\n\t" "s_waitcnt vmcnt(0) lgkmcnt(0)"
#define FIN_CLOB "v8", "v9", "v10", "v11", "v12", "v13", "v14", "v15", "v24", "v25", "v26", "v27", "v28", "v29", "v30", "v31", "v32", "v33", "v34", "v35", "v36", "v37", "v38", "v39", "v40", "v41", "v42", "v43", "v44", "v45", "v46", "v47", "v48", "v49", "v50", "v51", "v52", "v53", "v54", "v55", "v56", "v57", "v58", "v59", "v60", "v61", "v62", "v63", "v64", "v65", "v66", "v67", "v68", "v69", "v70", "v71", "v72", "v73", "v74", "v75", "v76", "v77", "v78", "v79", "v80", "v81", "v82", "v83", "v84", "v85", "v86", "v87", "v88", "v89", "v90", "v91", "v92", "v93", "v94", "v95", "v96", "v97", "v98", "v99", "v100", "v101", "v102", "v103", "v104", "v105", "v106", "v107", "v108", "v109", "v110", "v111", "v112", "v113", "v114", "v115", "v116", "v117", "v118", "v119", "v120", "v121", "v122", "v123", "v124", "v125", "v126", "v127", "v128", "v129", "v130", "v131", "v132", "v133", "v134", "v135", "v136", "v137", "v138", "v139", "v140", "v141", "v142", "v143", "v144", "v145", "v146", "v147", "v148", "v149", "v150", "v151", "v152", "v153", "v154", "v155", "v156", "v157", "v158", "v159", "v160", "v161", "v162", "v163", "v164", "v165", "v166", "v167", "v168", "v169", "v170", "v171", "v172", "v173", "v174", "v175", "v176", "v177", "v178", "v179", "v180", "v181", "v182", "v183", "v184", "v185", "v186", "v187", "v188", "v189", "v190", "v191", "v192", "v193", "v194", "v195", "v196", "v197", "v198", "v199", "v200", "v201", "v202", "v203", "v204", "v205", "v206", "v207", "v208", "v209", "v210", "v211", "v212", "v213", "v214", "v215", "v216", "v217", "v218", "v219", "v220", "v221", "v222", "v223", "v224", "v225", "v226", "v227", "s40", "s41", "s42", "s43", "s44", "s45", "s46", "s47", "s48", "s49", "s50", "s51", "s52", "s53", "s60", "s61", "s62", "s63", "s64", "s65", "s66", "s67", "s68", "s69", "s70", "s71", "s72", "s73", "s74", "s75", "a0", "a1", "a2", "a3", "a4", "a5", "a6", "a7", "a8", "a9", "a10", "a11", "a12", "a13", "a14", "a15", "a16", "a17", "a18", "a19", "a20", "a21", "a22", "a23", "a24", "a25", "a26", "a27", "a28", "a29", "a30", "a31", "a32", "a33", "a34", "a35", "a36", "a37", "a38", "a39", "a40", "a41", "a42", "a43", "a44", "a45", "a46", "a47", "a48", "a49", "a50", "a51", "a52", "a53", "a54", "a55", "a56", "a57", "a58", "a59", "a60", "a61", "a62", "a63", "a64", "a65", "a66", "a67", "a68", "a69", "a70", "a71", "a72", "a73", "a74", "a75", "a76", "a77", "a78", "a79", "a80", "a81", "a82", "a83", "a84", "a85", "a86", "a87", "a88", "a89", "a90", "a91", "a92", "a93", "a94", "a95", "a96", "a97", "a98", "a99", "a100", "a101", "a102", "a103", "a104", "a105", "a106", "a107", "a108", "a109", "a110", "a111", "a112", "a113", "a114", "a115", "a116", "a117", "a118", "a119", "a120", "a121", "a122", "a123", "a124", "a125", "a126", "a127"
__device__ __forceinline__ void put4(v32u& d, int i, v4u v) { d[4 * i] = v.x; d[4 * i + 1] = v.y; d[4 * i + 2] = v.z; d[4 * i + 3] = v.w; }
#define LDP_ASM \
   "s_mov_b32 s41, 0\n\t" "buffer_load_dwordx4 a[0:3], v16, %[ra], s41 offen\n\t" "s_mov_b32 s41, 131072\n\t" \
   "buffer_load_dwordx4 a[64:67], v16, %[ra], s41 offen\n\t" "s_mov_b32 s41, 524288\n\t" "buffer_load_dwordx4 a[8:11], v16, %[ra], s41 offen\n\t" \
   "s_mov_b32 s41, 655360\n\t" "buffer_load_dwordx4 a[72:75], v16, %[ra], s41 offen\n\t" "s_mov_b32 s41, 1048576\n\t" \
   "buffer_load_dwordx4 a[16:19], v16, %[ra], s41 offen\n\t" "s_mov_b32 s41, 1179648\n\t" "buffer_load_dwordx4 a[80:83], v16, %[ra], s41 offen\n\t" \
   "s_mov_b32 s41, 1572864\n\t" "buffer_load_dwordx4 a[24:27], v16, %[ra], s41 offen\n\t" "s_mov_b32 s41, 1703936\n\t" \
   "buffer_load_dwordx4 a[88:91], v16, %[ra], s41 offen\n\t" "s_mov_b32 s41, 2097152\n\t" "buffer_load_dwordx4 a[32:35], v16, %[ra], s41 offen\n\t" \
   "s_mov_b32 s41, 2228224\n\t" "buffer_load_dwordx4 a[96:99], v16, %[ra], s41 offen\n\t" "s_mov_b32 s41, 2621440\n\t" \
   "buffer_load_dwordx4 a[40:43], v16, %[ra], s41 offen\n\t" "s_mov_b32 s41, 2752512\n\t" "buffer_load_dwordx4 a[104:107], v16, %[ra], s41 offen\n\t" \
   "s_mov_b32 s41, 3145728\n\t" "buffer_load_dwordx4 a[48:51], v16, %[ra], s41 offen\n\t" "s_mov_b32 s41, 3276800\n\t" \
   "buffer_load_dwordx4 a[112:115], v16, %[ra], s41 offen\n\t" "s_mov_b32 s41, 3670016\n\t" "buffer_load_dwordx4 a[56:59], v16, %[ra], s41 offen\n\t" \
   "s_mov_b32 s41, 3801088\n\t" "buffer_load_dwordx4 a[120:123], v16, %[ra], s41 offen\n\t" "s_mov_b32 s41, 4194304\n\t" \
   "buffer_load_dwordx4 v[192:195], v16, %[ra], s41 offen\n\t" "s_mov_b32 s41, 4325376\n\t" "buffer_load_dwordx4 v[208:211], v16, %[ra], s41 offen\n\t" \
   "s_mov_b32 s41, 4718592\n\t" "buffer_load_dwordx4 v[200:203], v16, %[ra], s41 offen\n\t" "s_mov_b32 s41, 4849664\n\t" \
   "buffer_load_dwordx4 v[216:219], v16, %[ra], s41 offen\n\t" "s_mov_b32 s41, 0\n\t" "buffer_load_dwordx4 a[128:131], v16, %[rb], s41 offen\n\t" \
   "s_mov_b32 s41, 131072\n\t" "buffer_load_dwordx4 a[192:195], v16, %[rb], s41 offen\n\t" "s_mov_b32 s41, 524288\n\t" \
   "buffer_load_dwordx4 a[136:139], v16, %[rb], s41 offen\n\t" "s_mov_b32 s41, 655360\n\t" "buffer_load_dwordx4 a[200:203], v16, %[rb], s41 offen\n\t" \
   "s_mov_b32 s41, 1048576\n\t" "buffer_load_dwordx4 a[144:147], v16, %[rb], s41 offen\n\t" "s_mov_b32 s41, 1179648\n\t" \
   "buffer_load_dwordx4 a[208:211], v16, %[rb], s41 offen\n\t" "s_mov_b32 s41, 1572864\n\t" "buffer_load_dwordx4 a[152:155], v16, %[rb], s41 offen\n\t" \
   "s_mov_b32 s41, 1703936\n\t" "buffer_load_dwordx4 a[216:219], v16, %[rb], s41 offen\n\t" "s_mov_b32 s41, 2097152\n\t" \
   "buffer_load_dwordx4 a[160:163], v16, %[rb], s41 offen\n\t" "s_mov_b32 s41, 2228224\n\t" "buffer_load_dwordx4 a[224:227], v16, %[rb], s41 offen\n\t" \
   "s_mov_b32 s41, 2621440\n\t" "buffer_load_dwordx4 a[168:171], v16, %[rb], s41 offen\n\t" "s_mov_b32 s41, 2752512\n\t" \
   "buffer_load_dwordx4 a[232:235], v16, %[rb], s41 offen\n\t" "s_mov_b32 s41, 3145728\n\t" "buffer_load_dwordx4 a[176:179], v16, %[rb], s41 offen\n\t" \
   "s_mov_b32 s41, 3276800\n\t" "buffer_load_dwordx4 a[240:243], v16, %[rb], s41 offen\n\t" "s_mov_b32 s41, 3670016\n\t" \
   "buffer_load_dwordx4 a[184:187], v16, %[rb], s41 offen\n\t" "s_mov_b32 s41, 3801088\n\t" "buffer_load_dwordx4 a[248:251], v16, %[rb], s41 offen\n\t" \
   "s_mov_b32 s41, 4194304\n\t" "buffer_load_dwordx4 v[224:227], v16, %[rb], s41 offen\n\t" "s_mov_b32 s41, 4325376\n\t" \
   "buffer_load_dwordx4 v[240:243], v16, %[rb], s41 offen\n\t" "s_mov_b32 s41, 4718592\n\t" "buffer_load_dwordx4 v[232:235], v16, %[rb], s41 offen\n\t" \
   "s_mov_b32 s41, 4849664\n\t" "buffer_load_dwordx4 v[248:251], v16, %[rb], s41 offen\n\t" "s_mov_b32 s41, 262144\n\t" \
   "buffer_load_dwordx4 a[4:7], v16, %[ra], s41 offen\n\t" "s_mov_b32 s41, 393216\n\t" "buffer_load_dwordx4 a[68:71], v16, %[ra], s41 offen\n\t" \
   "s_mov_b32 s41, 786432\n\t" "buffer_load_dwordx4 a[12:15], v16, %[ra], s41 offen\n\t" "s_mov_b32 s41, 917504\n\t" \
   "buffer_load_dwordx4 a[76:79], v16, %[ra], s41 offen\n\t" "s_mov_b32 s41, 1310720\n\t" "buffer_load_dwordx4 a[20:23], v16, %[ra], s41 offen\n\t" \
   "s_mov_b32 s41, 1441792\n\t" "buffer_load_dwordx4 a[84:87], v16, %[ra], s41 offen\n\t" "s_mov_b32 s41, 1835008\n\t" \
   "buffer_load_dwordx4 a[28:31], v16, %[ra], s41 offen\n\t" "s_mov_b32 s41, 1966080\n\t" "buffer_load_dwordx4 a[92:95], v16, %[ra], s41 offen\n\t" \
   "s_mov_b32 s41, 2359296\n\t" "buffer_load_dwordx4 a[36:39], v16, %[ra], s41 offen\n\t" "s_mov_b32 s41, 2490368\n\t" \
   "buffer_load_dwordx4 a[100:103], v16, %[ra], s41 offen\n\t" "s_mov_b32 s41, 2883584\n\t" "buffer_load_dwordx4 a[44:47], v16, %[ra], s41 offen\n\t" \
   "s_mov_b32 s41, 3014656\n\t" "buffer_load_dwordx4 a[108:111], v16, %[ra], s41 offen\n\t" "s_mov_b32 s41, 3407872\n\t" \
   "buffer_load_dwordx4 a[52:55], v16, %[ra], s41 offen\n\t" "s_mov_b32 s41, 3538944\n\t" "buffer_load_dwordx4 a[116:119], v16, %[ra], s41 offen\n\t" \
   "s_mov_b32 s41, 3932160\n\t" "buffer_load_dwordx4 a[60:63], v16, %[ra], s41 offen\n\t" "s_mov_b32 s41, 4063232\n\t" \
   "buffer_load_dwordx4 a[124:127], v16, %[ra], s41 offen\n\t" "s_mov_b32 s41, 4456448\n\t" "buffer_load_dwordx4 v[196:199], v16, %[ra], s41 offen\n\t" \
   "s_mov_b32 s41, 4587520\n\t" "buffer_load_dwordx4 v[212:215], v16, %[ra], s41 offen\n\t" "s_mov_b32 s41, 4980736\n\t" \
   "buffer_load_dwordx4 v[204:207], v16, %[ra], s41 offen\n\t" "s_mov_b32 s41, 5111808\n\t" "buffer_load_dwordx4 v[220:223], v16, %[ra], s41 offen\n\t" \
   "s_waitcnt vmcnt(59)\n\t" "ds_write_b128 v20, a[0:3] offset:0\n\t" "s_mov_b32 s41, 262144\n\t" \
   "buffer_load_dwordx4 a[132:135], v16, %[rb], s41 offen\n\t" "s_waitcnt vmcnt(59)\n\t" "ds_write_b128 v20, a[64:67] offset:4096\n\t" \
   "s_mov_b32 s41, 393216\n\t" "buffer_load_dwordx4 a[196:199], v16, %[rb], s41 offen\n\t" "s_waitcnt vmcnt(59)\n\t" \
   "ds_write_b128 v20, a[8:11] offset:8192\n\t" "s_mov_b32 s41, 786432\n\t" "buffer_load_dwordx4 a[140:143], v16, %[rb], s41 offen\n\t" \
   "s_waitcnt vmcnt(59)\n\t" "ds_write_b128 v20, a[72:75] offset:12288\n\t" "s_mov_b32 s41, 917504\n\t" \
   "buffer_load_dwordx4 a[204:207], v16, %[rb], s41 offen\n\t" "s_waitcnt vmcnt(59)\n\t" "ds_write_b128 v20, a[16:19] offset:16384\n\t" \
   "s_mov_b32 s41, 1310720\n\t" "buffer_load_dwordx4 a[148:151], v16, %[rb], s41 offen\n\t" "s_waitcnt vmcnt(59)\n\t" \
   "ds_write_b128 v20, a[80:83] offset:20480\n\t" "s_mov_b32 s41, 1441792\n\t" "buffer_load_dwordx4 a[212:215], v16, %[rb], s41 offen\n\t" \
   "s_waitcnt vmcnt(59)\n\t" "ds_write_b128 v20, a[24:27] offset:24576\n\t" "s_mov_b32 s41, 1835008\n\t" \
   "buffer_load_dwordx4 a[156:159], v16, %[rb], s41 offen\n\t" "s_waitcnt vmcnt(59)\n\t" "ds_write_b128 v20, a[88:91] offset:28672\n\t" \
   "s_mov_b32 s41, 1966080\n\t" "buffer_load_dwordx4 a[220:223], v16, %[rb], s41 offen\n\t" "s_waitcnt vmcnt(59)\n\t" \
   "ds_write_b128 v20, a[32:35] offset:32768\n\t" "s_mov_b32 s41, 2359296\n\t" "buffer_load_dwordx4 a[164:167], v16, %[rb], s41 offen\n\t" \
   "s_waitcnt vmcnt(59)\n\t" "ds_write_b128 v20, a[96:99] offset:36864\n\t" "s_mov_b32 s41, 2490368\n\t" \
   "buffer_load_dwordx4 a[228:231], v16, %[rb], s41 offen\n\t" "s_waitcnt vmcnt(59)\n\t" "ds_write_b128 v20, a[40:43] offset:40960\n\t" \
   "s_mov_b32 s41, 2883584\n\t" "buffer_load_dwordx4 a[172:175], v16, %[rb], s41 offen\n\t" "s_waitcnt vmcnt(59)\n\t" \
   "ds_write_b128 v20, a[104:107] offset:45056\n\t" "s_mov_b32 s41, 3014656\n\t" "buffer_load_dwordx4 a[236:239], v16, %[rb], s41 offen\n\t" \
   "s_waitcnt vmcnt(59)\n\t" "ds_write_b128 v20, a[48:51] offset:49152\n\t" "s_mov_b32 s41, 3407872\n\t" \
   "buffer_load_dwordx4 a[180:183], v16, %[rb], s41 offen\n\t" "s_waitcnt vmcnt(59)\n\t" "ds_write_b128 v20, a[112:115] offset:53248\n\t" \
   "s_mov_b32 s41, 3538944\n\t" "buffer_load_dwordx4 a[244:247], v16, %[rb], s41 offen\n\t" "s_waitcnt vmcnt(59)\n\t" \
   "ds_write_b128 v20, a[56:59] offset:57344\n\t" "s_mov_b32 s41, 3932160\n\t" "buffer_load_dwordx4 a[188:191], v16, %[rb], s41 offen\n\t" \
   "s_waitcnt vmcnt(59)\n\t" "ds_write_b128 v20, a[120:123] offset:61440\n\t" "s_mov_b32 s41, 4063232\n\t" \
   "buffer_load_dwordx4 a[252:255], v16, %[rb], s41 offen\n\t" "s_waitcnt vmcnt(59)\n\t" "ds_write_b128 v23, v[192:195] offset:0\n\t" \
   "s_mov_b32 s41, 4456448\n\t" "buffer_load_dwordx4 v[228:231], v16, %[rb], s41 offen\n\t" "s_waitcnt vmcnt(59)\n\t" \
   "ds_write_b128 v23, v[208:211] offset:4096\n\t" "s_mov_b32 s41, 4587520\n\t" "buffer_load_dwordx4 v[244:247], v16, %[rb], s41 offen\n\t" \
   "s_waitcnt vmcnt(59)\n\t" "ds_write_b128 v23, v[200:203] offset:8192\n\t" "s_mov_b32 s41, 4980736\n\t" \
   "buffer_load_dwordx4 v[236:239], v16, %[rb], s41 offen\n\t" "s_waitcnt vmcnt(59)\n\t" "ds_write_b128 v23, v[216:219] offset:12288\n\t" \
   "s_mov_b32 s41, 5111808\n\t" "buffer_load_dwordx4 v[252:255], v16, %[rb], s41 offen\n\t" "s_waitcnt lgkmcnt(0)\n\t" "s_barrier\n\t" \
   "s_lshl_b32 s40, %[p0], 13\n\t" "v_add_u32 v24, s40, v18\n\t" "v_add_u32 v25, s40, v19\n\t" "s_add_u32 s40, s40, 8192\n\t" \
   "s_cmp_eq_u32 s40, 81920\n\t" "s_cselect_b32 s40, 0, s40\n\t" "ds_read_b64_tr_b16 a[0:1], v24 offset:0\n\t" \
   "ds_read_b64_tr_b16 a[2:3], v24 offset:4096\n\t" "ds_read_b64_tr_b16 a[64:65], v25 offset:0\n\t" "ds_read_b64_tr_b16 a[66:67], v25 offset:4096\n\t" \
   "v_add_u32 v24, s40, v18\n\t" "v_add_u32 v25, s40, v19\n\t" "s_add_u32 s40, s40, 8192\n\t" "s_cmp_eq_u32 s40, 81920\n\t" \
   "s_cselect_b32 s40, 0, s40\n\t" "ds_read_b64_tr_b16 a[8:9], v24 offset:0\n\t" "ds_read_b64_tr_b16 a[10:11], v24 offset:4096\n\t" \
   "ds_read_b64_tr_b16 a[72:73], v25 offset:0\n\t" "ds_read_b64_tr_b16 a[74:75], v25 offset:4096\n\t" "v_add_u32 v24, s40, v18\n\t" \
   "v_add_u32 v25, s40, v19\n\t" "s_add_u32 s40, s40, 8192\n\t" "s_cmp_eq_u32 s40, 81920\n\t" "s_cselect_b32 s40, 0, s40\n\t" \
   "ds_read_b64_tr_b16 a[16:17], v24 offset:0\n\t" "ds_read_b64_tr_b16 a[18:19], v24 offset:4096\n\t" "ds_read_b64_tr_b16 a[80:81], v25 offset:0\n\t" \
   "ds_read_b64_tr_b16 a[82:83], v25 offset:4096\n\t" "v_add_u32 v24, s40, v18\n\t" "v_add_u32 v25, s40, v19\n\t" "s_add_u32 s40, s40, 8192\n\t" \
   "s_cmp_eq_u32 s40, 81920\n\t" "s_cselect_b32 s40, 0, s40\n\t" "ds_read_b64_tr_b16 a[24:25], v24 offset:0\n\t" \
   "ds_read_b64_tr_b16 a[26:27], v24 offset:4096\n\t" "ds_read_b64_tr_b16 a[88:89], v25 offset:0\n\t" \
   "ds_read_b64_tr_b16 a[90:91], v25 offset:4096\n\t" "v_add_u32 v24, s40, v18\n\t" "v_add_u32 v25, s40, v19\n\t" "s_add_u32 s40, s40, 8192\n\t" \
   "s_cmp_eq_u32 s40, 81920\n\t" "s_cselect_b32 s40, 0, s40\n\t" "ds_read_b64_tr_b16 a[32:33], v24 offset:0\n\t" \
   "ds_read_b64_tr_b16 a[34:35], v24 offset:4096\n\t" "ds_read_b64_tr_b16 a[96:97], v25 offset:0\n\t" \
   "ds_read_b64_tr_b16 a[98:99], v25 offset:4096\n\t" "v_add_u32 v24, s40, v18\n\t" "v_add_u32 v25, s40, v19\n\t" "s_add_u32 s40, s40, 8192\n\t" \
   "s_cmp_eq_u32 s40, 81920\n\t" "s_cselect_b32 s40, 0, s40\n\t" "ds_read_b64_tr_b16 a[40:41], v24 offset:0\n\t" \
   "ds_read_b64_tr_b16 a[42:43], v24 offset:4096\n\t" "ds_read_b64_tr_b16 a[104:105], v25 offset:0\n\t" \
   "ds_read_b64_tr_b16 a[106:107], v25 offset:4096\n\t" "v_add_u32 v24, s40, v18\n\t" "v_add_u32 v25, s40, v19\n\t" "s_add_u32 s40, s40, 8192\n\t" \
   "s_cmp_eq_u32 s40, 81920\n\t" "s_cselect_b32 s40, 0, s40\n\t" "ds_read_b64_tr_b16 a[48:49], v24 offset:0\n\t" \
   "ds_read_b64_tr_b16 a[50:51], v24 offset:4096\n\t" "ds_read_b64_tr_b16 a[112:113], v25 offset:0\n\t" \
   "ds_read_b64_tr_b16 a[114:115], v25 offset:4096\n\t" "v_add_u32 v24, s40, v18\n\t" "v_add_u32 v25, s40, v19\n\t" "s_add_u32 s40, s40, 8192\n\t" \
   "s_cmp_eq_u32 s40, 81920\n\t" "s_cselect_b32 s40, 0, s40\n\t" "ds_read_b64_tr_b16 a[56:57], v24 offset:0\n\t" \
   "ds_read_b64_tr_b16 a[58:59], v24 offset:4096\n\t" "ds_read_b64_tr_b16 a[120:121], v25 offset:0\n\t" \
   "ds_read_b64_tr_b16 a[122:123], v25 offset:4096\n\t" "v_add_u32 v24, s40, v18\n\t" "v_add_u32 v25, s40, v19\n\t" "s_add_u32 s40, s40, 8192\n\t" \
   "s_cmp_eq_u32 s40, 81920\n\t" "s_cselect_b32 s40, 0, s40\n\t" "ds_read_b64_tr_b16 v[192:193], v24 offset:0\n\t" \
   "ds_read_b64_tr_b16 v[194:195], v24 offset:4096\n\t" "ds_read_b64_tr_b16 v[208:209], v25 offset:0\n\t" \
   "ds_read_b64_tr_b16 v[210:211], v25 offset:4096\n\t" "v_add_u32 v24, s40, v18\n\t" "v_add_u32 v25, s40, v19\n\t" "s_add_u32 s40, s40, 8192\n\t" \
   "s_cmp_eq_u32 s40, 81920\n\t" "s_cselect_b32 s40, 0, s40\n\t" "ds_read_b64_tr_b16 v[200:201], v24 offset:0\n\t" \
   "ds_read_b64_tr_b16 v[202:203], v24 offset:4096\n\t" "ds_read_b64_tr_b16 v[216:217], v25 offset:0\n\t" \
   "ds_read_b64_tr_b16 v[218:219], v25 offset:4096\n\t" "s_waitcnt vmcnt(59)\n\t" "s_waitcnt lgkmcnt(0)\n\t" "s_barrier\n\t" \
   "ds_write_b128 v20, a[128:131] offset:0\n\t" "s_waitcnt vmcnt(58)\n\t" "ds_write_b128 v20, a[192:195] offset:4096\n\t" "s_waitcnt vmcnt(57)\n\t" \
   "ds_write_b128 v20, a[136:139] offset:8192\n\t" "s_waitcnt vmcnt(56)\n\t" "ds_write_b128 v20, a[200:203] offset:12288\n\t" "s_waitcnt vmcnt(55)\n\t" \
   "ds_write_b128 v20, a[144:147] offset:16384\n\t" "s_waitcnt vmcnt(54)\n\t" "ds_write_b128 v20, a[208:211] offset:20480\n\t" \
   "s_waitcnt vmcnt(53)\n\t" "ds_write_b128 v20, a[152:155] offset:24576\n\t" "s_waitcnt vmcnt(52)\n\t" \
   "ds_write_b128 v20, a[216:219] offset:28672\n\t" "s_waitcnt vmcnt(51)\n\t" "ds_write_b128 v20, a[160:163] offset:32768\n\t" \
   "s_waitcnt vmcnt(50)\n\t" "ds_write_b128 v20, a[224:227] offset:36864\n\t" "s_waitcnt vmcnt(49)\n\t" \
   "ds_write_b128 v20, a[168:171] offset:40960\n\t" "s_waitcnt vmcnt(48)\n\t" "ds_write_b128 v20, a[232:235] offset:45056\n\t" \
   "s_waitcnt vmcnt(47)\n\t" "ds_write_b128 v20, a[176:179] offset:49152\n\t" "s_waitcnt vmcnt(46)\n\t" \
   "ds_write_b128 v20, a[240:243] offset:53248\n\t" "s_waitcnt vmcnt(45)\n\t" "ds_write_b128 v20, a[184:187] offset:57344\n\t" \
   "s_waitcnt vmcnt(44)\n\t" "ds_write_b128 v20, a[248:251] offset:61440\n\t" "s_waitcnt vmcnt(43)\n\t" "ds_write_b128 v23, v[224:227] offset:0\n\t" \
   "s_waitcnt vmcnt(42)\n\t" "ds_write_b128 v23, v[240:243] offset:4096\n\t" "s_waitcnt vmcnt(41)\n\t" "ds_write_b128 v23, v[232:235] offset:8192\n\t" \
   "s_waitcnt vmcnt(40)\n\t" "ds_write_b128 v23, v[248:251] offset:12288\n\t" "s_waitcnt lgkmcnt(0)\n\t" "s_barrier\n\t" \
   "s_lshl_b32 s40, %[p0], 13\n\t" "v_add_u32 v24, s40, v18\n\t" "v_add_u32 v25, s40, v19\n\t" "s_add_u32 s40, s40, 8192\n\t" \
   "s_cmp_eq_u32 s40, 81920\n\t" "s_cselect_b32 s40, 0, s40\n\t" "ds_read_b64_tr_b16 a[128:129], v24 offset:0\n\t" \
   "ds_read_b64_tr_b16 a[130:131], v24 offset:4096\n\t" "ds_read_b64_tr_b16 a[192:193], v25 offset:0\n\t" \
   "ds_read_b64_tr_b16 a[194:195], v25 offset:4096\n\t" "v_add_u32 v24, s40, v18\n\t" "v_add_u32 v25, s40, v19\n\t" "s_add_u32 s40, s40, 8192\n\t" \
   "s_cmp_eq_u32 s40, 81920\n\t" "s_cselect_b32 s40, 0, s40\n\t" "ds_read_b64_tr_b16 a[136:137], v24 offset:0\n\t" \
   "ds_read_b64_tr_b16 a[138:139], v24 offset:4096\n\t" "ds_read_b64_tr_b16 a[200:201], v25 offset:0\n\t" \
   "ds_read_b64_tr_b16 a[202:203], v25 offset:4096\n\t" "v_add_u32 v24, s40, v18\n\t" "v_add_u32 v25, s40, v19\n\t" "s_add_u32 s40, s40, 8192\n\t" \
   "s_cmp_eq_u32 s40, 81920\n\t" "s_cselect_b32 s40, 0, s40\n\t" "ds_read_b64_tr_b16 a[144:145], v24 offset:0\n\t" \
   "ds_read_b64_tr_b16 a[146:147], v24 offset:4096\n\t" "ds_read_b64_tr_b16 a[208:209], v25 offset:0\n\t" \
   "ds_read_b64_tr_b16 a[210:211], v25 offset:4096\n\t" "v_add_u32 v24, s40, v18\n\t" "v_add_u32 v25, s40, v19\n\t" "s_add_u32 s40, s40, 8192\n\t" \
   "s_cmp_eq_u32 s40, 81920\n\t" "s_cselect_b32 s40, 0, s40\n\t" "ds_read_b64_tr_b16 a[152:153], v24 offset:0\n\t" \
   "ds_read_b64_tr_b16 a[154:155], v24 offset:4096\n\t" "ds_read_b64_tr_b16 a[216:217], v25 offset:0\n\t" \
   "ds_read_b64_tr_b16 a[218:219], v25 offset:4096\n\t" "v_add_u32 v24, s40, v18\n\t" "v_add_u32 v25, s40, v19\n\t" "s_add_u32 s40, s40, 8192\n\t" \
   "s_cmp_eq_u32 s40, 81920\n\t" "s_cselect_b32 s40, 0, s40\n\t" "ds_read_b64_tr_b16 a[160:161], v24 offset:0\n\t" \
   "ds_read_b64_tr_b16 a[162:163], v24 offset:4096\n\t" "ds_read_b64_tr_b16 a[224:225], v25 offset:0\n\t" \
   "ds_read_b64_tr_b16 a[226:227], v25 offset:4096\n\t" "v_add_u32 v24, s40, v18\n\t" "v_add_u32 v25, s40, v19\n\t" "s_add_u32 s40, s40, 8192\n\t" \
   "s_cmp_eq_u32 s40, 81920\n\t" "s_cselect_b32 s40, 0, s40\n\t" "ds_read_b64_tr_b16 a[168:169], v24 offset:0\n\t" \
   "ds_read_b64_tr_b16 a[170:171], v24 offset:4096\n\t" "ds_read_b64_tr_b16 a[232:233], v25 offset:0\n\t" \
   "ds_read_b64_tr_b16 a[234:235], v25 offset:4096\n\t" "v_add_u32 v24, s40, v18\n\t" "v_add_u32 v25, s40, v19\n\t" "s_add_u32 s40, s40, 8192\n\t" \
   "s_cmp_eq_u32 s40, 81920\n\t" "s_cselect_b32 s40, 0, s40\n\t" "ds_read_b64_tr_b16 a[176:177], v24 offset:0\n\t" \
   "ds_read_b64_tr_b16 a[178:179], v24 offset:4096\n\t" "ds_read_b64_tr_b16 a[240:241], v25 offset:0\n\t" \
   "ds_read_b64_tr_b16 a[242:243], v25 offset:4096\n\t" "v_add_u32 v24, s40, v18\n\t" "v_add_u32 v25, s40, v19\n\t" "s_add_u32 s40, s40, 8192\n\t" \
   "s_cmp_eq_u32 s40, 81920\n\t" "s_cselect_b32 s40, 0, s40\n\t" "ds_read_b64_tr_b16 a[184:185], v24 offset:0\n\t" \
   "ds_read_b64_tr_b16 a[186:187], v24 offset:4096\n\t" "ds_read_b64_tr_b16 a[248:249], v25 offset:0\n\t" \
   "ds_read_b64_tr_b16 a[250:251], v25 offset:4096\n\t" "v_add_u32 v24, s40, v18\n\t" "v_add_u32 v25, s40, v19\n\t" "s_add_u32 s40, s40, 8192\n\t" \
   "s_cmp_eq_u32 s40, 81920\n\t" "s_cselect_b32 s40, 0, s40\n\t" "ds_read_b64_tr_b16 v[224:225], v24 offset:0\n\t" \
   "ds_read_b64_tr_b16 v[226:227], v24 offset:4096\n\t" "ds_read_b64_tr_b16 v[240:241], v25 offset:0\n\t" \
   "ds_read_b64_tr_b16 v[242:243], v25 offset:4096\n\t" "v_add_u32 v24, s40, v18\n\t" "v_add_u32 v25, s40, v19\n\t" "s_add_u32 s40, s40, 8192\n\t" \
   "s_cmp_eq_u32 s40, 81920\n\t" "s_cselect_b32 s40, 0, s40\n\t" "ds_read_b64_tr_b16 v[232:233], v24 offset:0\n\t" \
   "ds_read_b64_tr_b16 v[234:235], v24 offset:4096\n\t" "ds_read_b64_tr_b16 v[248:249], v25 offset:0\n\t" \
   "ds_read_b64_tr_b16 v[250:251], v25 offset:4096\n\t" "s_waitcnt vmcnt(39)\n\t" "s_waitcnt lgkmcnt(0)\n\t" "s_barrier\n\t" \
   "ds_write_b128 v20, a[4:7] offset:0\n\t" "s_waitcnt vmcnt(38)\n\t" "ds_write_b128 v20, a[68:71] offset:4096\n\t" "s_waitcnt vmcnt(37)\n\t" \
   "ds_write_b128 v20, a[12:15] offset:8192\n\t" "s_waitcnt vmcnt(36)\n\t" "ds_write_b128 v20, a[76:79] offset:12288\n\t" "s_waitcnt vmcnt(35)\n\t" \
   "ds_write_b128 v20, a[20:23] offset:16384\n\t" "s_waitcnt vmcnt(34)\n\t" "ds_write_b128 v20, a[84:87] offset:20480\n\t" "s_waitcnt vmcnt(33)\n\t" \
   "ds_write_b128 v20, a[28:31] offset:24576\n\t" "s_waitcnt vmcnt(32)\n\t" "ds_write_b128 v20, a[92:95] offset:28672\n\t" "s_waitcnt vmcnt(31)\n\t" \
   "ds_write_b128 v20, a[36:39] offset:32768\n\t" "s_waitcnt vmcnt(30)\n\t" "ds_write_b128 v20, a[100:103] offset:36864\n\t" "s_waitcnt vmcnt(29)\n\t" \
   "ds_write_b128 v20, a[44:47] offset:40960\n\t" "s_waitcnt vmcnt(28)\n\t" "ds_write_b128 v20, a[108:111] offset:45056\n\t" "s_waitcnt vmcnt(27)\n\t" \
   "ds_write_b128 v20, a[52:55] offset:49152\n\t" "s_waitcnt vmcnt(26)\n\t" "ds_write_b128 v20, a[116:119] offset:53248\n\t" "s_waitcnt vmcnt(25)\n\t" \
   "ds_write_b128 v20, a[60:63] offset:57344\n\t" "s_waitcnt vmcnt(24)\n\t" "ds_write_b128 v20, a[124:127] offset:61440\n\t" "s_waitcnt vmcnt(23)\n\t" \
   "ds_write_b128 v23, v[196:199] offset:0\n\t" "s_waitcnt vmcnt(22)\n\t" "ds_write_b128 v23, v[212:215] offset:4096\n\t" "s_waitcnt vmcnt(21)\n\t" \
   "ds_write_b128 v23, v[204:207] offset:8192\n\t" "s_waitcnt vmcnt(20)\n\t" "ds_write_b128 v23, v[220:223] offset:12288\n\t" \
   "s_waitcnt lgkmcnt(0)\n\t" "s_barrier\n\t" "s_lshl_b32 s40, %[p0], 13\n\t" "v_add_u32 v24, s40, v18\n\t" "v_add_u32 v25, s40, v19\n\t" \
   "s_add_u32 s40, s40, 8192\n\t" "s_cmp_eq_u32 s40, 81920\n\t" "s_cselect_b32 s40, 0, s40\n\t" "ds_read_b64_tr_b16 a[4:5], v24 offset:0\n\t" \
   "ds_read_b64_tr_b16 a[6:7], v24 offset:4096\n\t" "ds_read_b64_tr_b16 a[68:69], v25 offset:0\n\t" "ds_read_b64_tr_b16 a[70:71], v25 offset:4096\n\t" \
   "v_add_u32 v24, s40, v18\n\t" "v_add_u32 v25, s40, v19\n\t" "s_add_u32 s40, s40, 8192\n\t" "s_cmp_eq_u32 s40, 81920\n\t" \
   "s_cselect_b32 s40, 0, s40\n\t" "ds_read_b64_tr_b16 a[12:13], v24 offset:0\n\t" "ds_read_b64_tr_b16 a[14:15], v24 offset:4096\n\t" \
   "ds_read_b64_tr_b16 a[76:77], v25 offset:0\n\t" "ds_read_b64_tr_b16 a[78:79], v25 offset:4096\n\t" "v_add_u32 v24, s40, v18\n\t" \
   "v_add_u32 v25, s40, v19\n\t" "s_add_u32 s40, s40, 8192\n\t" "s_cmp_eq_u32 s40, 81920\n\t" "s_cselect_b32 s40, 0, s40\n\t" \
   "ds_read_b64_tr_b16 a[20:21], v24 offset:0\n\t" "ds_read_b64_tr_b16 a[22:23], v24 offset:4096\n\t" "ds_read_b64_tr_b16 a[84:85], v25 offset:0\n\t" \
   "ds_read_b64_tr_b16 a[86:87], v25 offset:4096\n\t" "v_add_u32 v24, s40, v18\n\t" "v_add_u32 v25, s40, v19\n\t" "s_add_u32 s40, s40, 8192\n\t" \
   "s_cmp_eq_u32 s40, 81920\n\t" "s_cselect_b32 s40, 0, s40\n\t" "ds_read_b64_tr_b16 a[28:29], v24 offset:0\n\t" \
   "ds_read_b64_tr_b16 a[30:31], v24 offset:4096\n\t" "ds_read_b64_tr_b16 a[92:93], v25 offset:0\n\t" \
   "ds_read_b64_tr_b16 a[94:95], v25 offset:4096\n\t" "v_add_u32 v24, s40, v18\n\t" "v_add_u32 v25, s40, v19\n\t" "s_add_u32 s40, s40, 8192\n\t" \
   "s_cmp_eq_u32 s40, 81920\n\t" "s_cselect_b32 s40, 0, s40\n\t" "ds_read_b64_tr_b16 a[36:37], v24 offset:0\n\t" \
   "ds_read_b64_tr_b16 a[38:39], v24 offset:4096\n\t" "ds_read_b64_tr_b16 a[100:101], v25 offset:0\n\t" \
   "ds_read_b64_tr_b16 a[102:103], v25 offset:4096\n\t" "v_add_u32 v24, s40, v18\n\t" "v_add_u32 v25, s40, v19\n\t" "s_add_u32 s40, s40, 8192\n\t" \
   "s_cmp_eq_u32 s40, 81920\n\t" "s_cselect_b32 s40, 0, s40\n\t" "ds_read_b64_tr_b16 a[44:45], v24 offset:0\n\t" \
   "ds_read_b64_tr_b16 a[46:47], v24 offset:4096\n\t" "ds_read_b64_tr_b16 a[108:109], v25 offset:0\n\t" \
   "ds_read_b64_tr_b16 a[110:111], v25 offset:4096\n\t" "v_add_u32 v24, s40, v18\n\t" "v_add_u32 v25, s40, v19\n\t" "s_add_u32 s40, s40, 8192\n\t" \
   "s_cmp_eq_u32 s40, 81920\n\t" "s_cselect_b32 s40, 0, s40\n\t" "ds_read_b64_tr_b16 a[52:53], v24 offset:0\n\t" \
   "ds_read_b64_tr_b16 a[54:55], v24 offset:4096\n\t" "ds_read_b64_tr_b16 a[116:117], v25 offset:0\n\t" \
   "ds_read_b64_tr_b16 a[118:119], v25 offset:4096\n\t" "v_add_u32 v24, s40, v18\n\t" "v_add_u32 v25, s40, v19\n\t" "s_add_u32 s40, s40, 8192\n\t" \
   "s_cmp_eq_u32 s40, 81920\n\t" "s_cselect_b32 s40, 0, s40\n\t" "ds_read_b64_tr_b16 a[60:61], v24 offset:0\n\t" \
   "ds_read_b64_tr_b16 a[62:63], v24 offset:4096\n\t" "ds_read_b64_tr_b16 a[124:125], v25 offset:0\n\t" \
   "ds_read_b64_tr_b16 a[126:127], v25 offset:4096\n\t" "v_add_u32 v24, s40, v18\n\t" "v_add_u32 v25, s40, v19\n\t" "s_add_u32 s40, s40, 8192\n\t" \
   "s_cmp_eq_u32 s40, 81920\n\t" "s_cselect_b32 s40, 0, s40\n\t" "ds_read_b64_tr_b16 v[196:197], v24 offset:0\n\t" \
   "ds_read_b64_tr_b16 v[198:199], v24 offset:4096\n\t" "ds_read_b64_tr_b16 v[212:213], v25 offset:0\n\t" \
   "ds_read_b64_tr_b16 v[214:215], v25 offset:4096\n\t" "v_add_u32 v24, s40, v18\n\t" "v_add_u32 v25, s40, v19\n\t" "s_add_u32 s40, s40, 8192\n\t" \
   "s_cmp_eq_u32 s40, 81920\n\t" "s_cselect_b32 s40, 0, s40\n\t" "ds_read_b64_tr_b16 v[204:205], v24 offset:0\n\t" \
   "ds_read_b64_tr_b16 v[206:207], v24 offset:4096\n\t" "ds_read_b64_tr_b16 v[220:221], v25 offset:0\n\t" \
   "ds_read_b64_tr_b16 v[222:223], v25 offset:4096\n\t" "s_waitcnt vmcnt(19)\n\t" "s_waitcnt lgkmcnt(0)\n\t" "s_barrier\n\t" \
   "ds_write_b128 v20, a[132:135] offset:0\n\t" "s_waitcnt vmcnt(18)\n\t" "ds_write_b128 v20, a[196:199] offset:4096\n\t" "s_waitcnt vmcnt(17)\n\t" \
   "ds_write_b128 v20, a[140:143] offset:8192\n\t" "s_waitcnt vmcnt(16)\n\t" "ds_write_b128 v20, a[204:207] offset:12288\n\t" "s_waitcnt vmcnt(15)\n\t" \
   "ds_write_b128 v20, a[148:151] offset:16384\n\t" "s_waitcnt vmcnt(14)\n\t" "ds_write_b128 v20, a[212:215] offset:20480\n\t" \
   "s_waitcnt vmcnt(13)\n\t" "ds_write_b128 v20, a[156:159] offset:24576\n\t" "s_waitcnt vmcnt(12)\n\t" \
   "ds_write_b128 v20, a[220:223] offset:28672\n\t" "s_waitcnt vmcnt(11)\n\t" "ds_write_b128 v20, a[164:167] offset:32768\n\t" \
   "s_waitcnt vmcnt(10)\n\t" "ds_write_b128 v20, a[228:231] offset:36864\n\t" "s_waitcnt vmcnt(9)\n\t" "ds_write_b128 v20, a[172:175] offset:40960\n\t" \
   "s_waitcnt vmcnt(8)\n\t" "ds_write_b128 v20, a[236:239] offset:45056\n\t" "s_waitcnt vmcnt(7)\n\t" "ds_write_b128 v20, a[180:183] offset:49152\n\t" \
   "s_waitcnt vmcnt(6)\n\t" "ds_write_b128 v20, a[244:247] offset:53248\n\t" "s_waitcnt vmcnt(5)\n\t" "ds_write_b128 v20, a[188:191] offset:57344\n\t" \
   "s_waitcnt vmcnt(4)\n\t" "ds_write_b128 v20, a[252:255] offset:61440\n\t" "s_waitcnt vmcnt(3)\n\t" "ds_write_b128 v23, v[228:231] offset:0\n\t" \
   "s_waitcnt vmcnt(2)\n\t" "ds_write_b128 v23, v[244:247] offset:4096\n\t" "s_waitcnt vmcnt(1)\n\t" "ds_write_b128 v23, v[236:239] offset:8192\n\t" \
   "s_waitcnt vmcnt(0)\n\t" "ds_write_b128 v23, v[252:255] offset:12288\n\t" "s_waitcnt lgkmcnt(0)\n\t" "s_barrier\n\t" "s_lshl_b32 s40, %[p0], 13\n\t" \
   "v_add_u32 v24, s40, v18\n\t" "v_add_u32 v25, s40, v19\n\t" "s_add_u32 s40, s40, 8192\n\t" "s_cmp_eq_u32 s40, 81920\n\t" \
   "s_cselect_b32 s40, 0, s40\n\t" "ds_read_b64_tr_b16 a[132:133], v24 offset:0\n\t" "ds_read_b64_tr_b16 a[134:135], v24 offset:4096\n\t" \
   "ds_read_b64_tr_b16 a[196:197], v25 offset:0\n\t" "ds_read_b64_tr_b16 a[198:199], v25 offset:4096\n\t" "v_add_u32 v24, s40, v18\n\t" \
   "v_add_u32 v25, s40, v19\n\t" "s_add_u32 s40, s40, 8192\n\t" "s_cmp_eq_u32 s40, 81920\n\t" "s_cselect_b32 s40, 0, s40\n\t" \
   "ds_read_b64_tr_b16 a[140:141], v24 offset:0\n\t" "ds_read_b64_tr_b16 a[142:143], v24 offset:4096\n\t" \
   "ds_read_b64_tr_b16 a[204:205], v25 offset:0\n\t" "ds_read_b64_tr_b16 a[206:207], v25 offset:4096\n\t" "v_add_u32 v24, s40, v18\n\t" \
   "v_add_u32 v25, s40, v19\n\t" "s_add_u32 s40, s40, 8192\n\t" "s_cmp_eq_u32 s40, 81920\n\t" "s_cselect_b32 s40, 0, s40\n\t" \
   "ds_read_b64_tr_b16 a[148:149], v24 offset:0\n\t" "ds_read_b64_tr_b16 a[150:151], v24 offset:4096\n\t" \
   "ds_read_b64_tr_b16 a[212:213], v25 offset:0\n\t" "ds_read_b64_tr_b16 a[214:215], v25 offset:4096\n\t" "v_add_u32 v24, s40, v18\n\t" \
   "v_add_u32 v25, s40, v19\n\t" "s_add_u32 s40, s40, 8192\n\t" "s_cmp_eq_u32 s40, 81920\n\t" "s_cselect_b32 s40, 0, s40\n\t" \
   "ds_read_b64_tr_b16 a[156:157], v24 offset:0\n\t" "ds_read_b64_tr_b16 a[158:159], v24 offset:4096\n\t" \
   "ds_read_b64_tr_b16 a[220:221], v25 offset:0\n\t" "ds_read_b64_tr_b16 a[222:223], v25 offset:4096\n\t" "v_add_u32 v24, s40, v18\n\t" \
   "v_add_u32 v25, s40, v19\n\t" "s_add_u32 s40, s40, 8192\n\t" "s_cmp_eq_u32 s40, 81920\n\t" "s_cselect_b32 s40, 0, s40\n\t" \
   "ds_read_b64_tr_b16 a[164:165], v24 offset:0\n\t" "ds_read_b64_tr_b16 a[166:167], v24 offset:4096\n\t" \
   "ds_read_b64_tr_b16 a[228:229], v25 offset:0\n\t" "ds_read_b64_tr_b16 a[230:231], v25 offset:4096\n\t" "v_add_u32 v24, s40, v18\n\t" \
   "v_add_u32 v25, s40, v19\n\t" "s_add_u32 s40, s40, 8192\n\t" "s_cmp_eq_u32 s40, 81920\n\t" "s_cselect_b32 s40, 0, s40\n\t" \
   "ds_read_b64_tr_b16 a[172:173], v24 offset:0\n\t" "ds_read_b64_tr_b16 a[174:175], v24 offset:4096\n\t" \
   "ds_read_b64_tr_b16 a[236:237], v25 offset:0\n\t" "ds_read_b64_tr_b16 a[238:239], v25 offset:4096\n\t" "v_add_u32 v24, s40, v18\n\t" \
   "v_add_u32 v25, s40, v19\n\t" "s_add_u32 s40, s40, 8192\n\t" "s_cmp_eq_u32 s40, 81920\n\t" "s_cselect_b32 s40, 0, s40\n\t" \
   "ds_read_b64_tr_b16 a[180:181], v24 offset:0\n\t" "ds_read_b64_tr_b16 a[182:183], v24 offset:4096\n\t" \
   "ds_read_b64_tr_b16 a[244:245], v25 offset:0\n\t" "ds_read_b64_tr_b16 a[246:247], v25 offset:4096\n\t" "v_add_u32 v24, s40, v18\n\t" \
   "v_add_u32 v25, s40, v19\n\t" "s_add_u32 s40, s40, 8192\n\t" "s_cmp_eq_u32 s40, 81920\n\t" "s_cselect_b32 s40, 0, s40\n\t" \
   "ds_read_b64_tr_b16 a[188:189], v24 offset:0\n\t" "ds_read_b64_tr_b16 a[190:191], v24 offset:4096\n\t" \
   "ds_read_b64_tr_b16 a[252:253], v25 offset:0\n\t" "ds_read_b64_tr_b16 a[254:255], v25 offset:4096\n\t" "v_add_u32 v24, s40, v18\n\t" \
   "v_add_u32 v25, s40, v19\n\t" "s_add_u32 s40, s40, 8192\n\t" "s_cmp_eq_u32 s40, 81920\n\t" "s_cselect_b32 s40, 0, s40\n\t" \
   "ds_read_b64_tr_b16 v[228:229], v24 offset:0\n\t" "ds_read_b64_tr_b16 v[230:231], v24 offset:4096\n\t" \
   "ds_read_b64_tr_b16 v[244:245], v25 offset:0\n\t" "ds_read_b64_tr_b16 v[246:247], v25 offset:4096\n\t" "v_add_u32 v24, s40, v18\n\t" \
   "v_add_u32 v25, s40, v19\n\t" "s_add_u32 s40, s40, 8192\n\t" "s_cmp_eq_u32 s40, 81920\n\t" "s_cselect_b32 s40, 0, s40\n\t" \
   "ds_read_b64_tr_b16 v[236:237], v24 offset:0\n\t" "ds_read_b64_tr_b16 v[238:239], v24 offset:4096\n\t" \
   "ds_read_b64_tr_b16 v[252:253], v25 offset:0\n\t" "ds_read_b64_tr_b16 v[254:255], v25 offset:4096\n\t" "s_waitcnt lgkmcnt(0)"
#define LDP_CLOB "v24", "v25", "s40", "s41"
#define LDK_ASM \
   "s_mov_b32 s41, 0\n\t" "buffer_load_dwordx4 a[0:3], v16, %[ra], s41 offen\n\t" "s_mov_b32 s41, 131072\n\t" \
   "buffer_load_dwordx4 a[4:7], v16, %[ra], s41 offen\n\t" "s_mov_b32 s41, 262144\n\t" "buffer_load_dwordx4 a[16:19], v16, %[ra], s41 offen\n\t" \
   "s_mov_b32 s41, 393216\n\t" "buffer_load_dwordx4 a[20:23], v16, %[ra], s41 offen\n\t" "s_mov_b32 s41, 524288\n\t" \
   "buffer_load_dwordx4 a[32:35], v16, %[ra], s41 offen\n\t" "s_mov_b32 s41, 655360\n\t" "buffer_load_dwordx4 a[36:39], v16, %[ra], s41 offen\n\t" \
   "s_mov_b32 s41, 786432\n\t" "buffer_load_dwordx4 a[48:51], v16, %[ra], s41 offen\n\t" "s_mov_b32 s41, 917504\n\t" \
   "buffer_load_dwordx4 a[52:55], v16, %[ra], s41 offen\n\t" "s_mov_b32 s41, 1048576\n\t" "buffer_load_dwordx4 a[64:67], v16, %[ra], s41 offen\n\t" \
   "s_mov_b32 s41, 1179648\n\t" "buffer_load_dwordx4 a[68:71], v16, %[ra], s41 offen\n\t" "s_mov_b32 s41, 0\n\t" \
   "buffer_load_dwordx4 a[8:11], v16, %[rb], s41 offen\n\t" "s_mov_b32 s41, 131072\n\t" "buffer_load_dwordx4 a[12:15], v16, %[rb], s41 offen\n\t" \
   "s_mov_b32 s41, 262144\n\t" "buffer_load_dwordx4 a[24:27], v16, %[rb], s41 offen\n\t" "s_mov_b32 s41, 393216\n\t" \
   "buffer_load_dwordx4 a[28:31], v16, %[rb], s41 offen\n\t" "s_mov_b32 s41, 524288\n\t" "buffer_load_dwordx4 a[40:43], v16, %[rb], s41 offen\n\t" \
   "s_mov_b32 s41, 655360\n\t" "buffer_load_dwordx4 a[44:47], v16, %[rb], s41 offen\n\t" "s_mov_b32 s41, 786432\n\t" \
   "buffer_load_dwordx4 a[56:59], v16, %[rb], s41 offen\n\t" "s_mov_b32 s41, 917504\n\t" "buffer_load_dwordx4 a[60:63], v16, %[rb], s41 offen\n\t" \
   "s_mov_b32 s41, 1048576\n\t" "buffer_load_dwordx4 a[72:75], v16, %[rb], s41 offen\n\t" "s_mov_b32 s41, 1179648\n\t" \
   "buffer_load_dwordx4 a[76:79], v16, %[rb], s41 offen\n\t" "s_mov_b32 s41, 1310720\n\t" "buffer_load_dwordx4 a[80:83], v16, %[ra], s41 offen\n\t" \
   "s_mov_b32 s41, 1441792\n\t" "buffer_load_dwordx4 a[84:87], v16, %[ra], s41 offen\n\t" "s_mov_b32 s41, 1572864\n\t" \
   "buffer_load_dwordx4 a[96:99], v16, %[ra], s41 offen\n\t" "s_mov_b32 s41, 1703936\n\t" "buffer_load_dwordx4 a[100:103], v16, %[ra], s41 offen\n\t" \
   "s_mov_b32 s41, 1835008\n\t" "buffer_load_dwordx4 a[112:115], v16, %[ra], s41 offen\n\t" "s_mov_b32 s41, 1966080\n\t" \
   "buffer_load_dwordx4 a[116:119], v16, %[ra], s41 offen\n\t" "s_mov_b32 s41, 2097152\n\t" "buffer_load_dwordx4 a[128:131], v16, %[ra], s41 offen\n\t" \
   "s_mov_b32 s41, 2228224\n\t" "buffer_load_dwordx4 a[132:135], v16, %[ra], s41 offen\n\t" "s_mov_b32 s41, 2359296\n\t" \
   "buffer_load_dwordx4 a[144:147], v16, %[ra], s41 offen\n\t" "s_mov_b32 s41, 2490368\n\t" "buffer_load_dwordx4 a[148:151], v16, %[ra], s41 offen\n\t" \
   "s_mov_b32 s41, 1310720\n\t" "buffer_load_dwordx4 a[88:91], v16, %[rb], s41 offen\n\t" "s_mov_b32 s41, 1441792\n\t" \
   "buffer_load_dwordx4 a[92:95], v16, %[rb], s41 offen\n\t" "s_mov_b32 s41, 1572864\n\t" "buffer_load_dwordx4 a[104:107], v16, %[rb], s41 offen\n\t" \
   "s_mov_b32 s41, 1703936\n\t" "buffer_load_dwordx4 a[108:111], v16, %[rb], s41 offen\n\t" "s_mov_b32 s41, 1835008\n\t" \
   "buffer_load_dwordx4 a[120:123], v16, %[rb], s41 offen\n\t" "s_mov_b32 s41, 1966080\n\t" "buffer_load_dwordx4 a[124:127], v16, %[rb], s41 offen\n\t" \
   "s_mov_b32 s41, 2097152\n\t" "buffer_load_dwordx4 a[136:139], v16, %[rb], s41 offen\n\t" "s_mov_b32 s41, 2228224\n\t" \
   "buffer_load_dwordx4 a[140:143], v16, %[rb], s41 offen\n\t" "s_mov_b32 s41, 2359296\n\t" "buffer_load_dwordx4 a[152:155], v16, %[rb], s41 offen\n\t" \
   "s_mov_b32 s41, 2490368\n\t" "buffer_load_dwordx4 a[156:159], v16, %[rb], s41 offen\n\t" "s_mov_b32 s41, 2621440\n\t" \
   "buffer_load_dwordx4 a[160:163], v16, %[ra], s41 offen\n\t" "s_mov_b32 s41, 2752512\n\t" "buffer_load_dwordx4 a[164:167], v16, %[ra], s41 offen\n\t" \
   "s_mov_b32 s41, 2883584\n\t" "buffer_load_dwordx4 a[176:179], v16, %[ra], s41 offen\n\t" "s_mov_b32 s41, 3014656\n\t" \
   "buffer_load_dwordx4 a[180:183], v16, %[ra], s41 offen\n\t" "s_mov_b32 s41, 3145728\n\t" "buffer_load_dwordx4 a[192:195], v16, %[ra], s41 offen\n\t" \
   "s_mov_b32 s41, 3276800\n\t" "buffer_load_dwordx4 a[196:199], v16, %[ra], s41 offen\n\t" "s_mov_b32 s41, 3407872\n\t" \
   "buffer_load_dwordx4 a[208:211], v16, %[ra], s41 offen\n\t" "s_mov_b32 s41, 3538944\n\t" "buffer_load_dwordx4 a[212:215], v16, %[ra], s41 offen\n\t" \
   "s_mov_b32 s41, 3670016\n\t" "buffer_load_dwordx4 a[224:227], v16, %[ra], s41 offen\n\t" "s_mov_b32 s41, 3801088\n\t" \
   "buffer_load_dwordx4 a[228:231], v16, %[ra], s41 offen\n\t" "s_mov_b32 s41, 2621440\n\t" "buffer_load_dwordx4 a[168:171], v16, %[rb], s41 offen\n\t" \
   "s_mov_b32 s41, 2752512\n\t" "buffer_load_dwordx4 a[172:175], v16, %[rb], s41 offen\n\t" "s_mov_b32 s41, 2883584\n\t" \
   "buffer_load_dwordx4 a[184:187], v16, %[rb], s41 offen\n\t" "s_mov_b32 s41, 3014656\n\t" "buffer_load_dwordx4 a[188:191], v16, %[rb], s41 offen\n\t" \
   "s_mov_b32 s41, 3145728\n\t" "buffer_load_dwordx4 a[200:203], v16, %[rb], s41 offen\n\t" "s_mov_b32 s41, 3276800\n\t" \
   "buffer_load_dwordx4 a[204:207], v16, %[rb], s41 offen\n\t" "s_mov_b32 s41, 3407872\n\t" "buffer_load_dwordx4 a[216:219], v16, %[rb], s41 offen\n\t" \
   "s_mov_b32 s41, 3538944\n\t" "buffer_load_dwordx4 a[220:223], v16, %[rb], s41 offen\n\t" "s_mov_b32 s41, 3670016\n\t" \
   "buffer_load_dwordx4 a[232:235], v16, %[rb], s41 offen\n\t" "s_mov_b32 s41, 3801088\n\t" "buffer_load_dwordx4 a[236:239], v16, %[rb], s41 offen\n\t" \
   "s_waitcnt vmcnt(59)\n\t" "ds_write_b128 v20, a[0:3] offset:0\n\t" "s_mov_b32 s41, 3932160\n\t" \
   "buffer_load_dwordx4 a[240:243], v16, %[ra], s41 offen\n\t" "s_waitcnt vmcnt(59)\n\t" "ds_write_b128 v20, a[4:7] offset:4096\n\t" \
   "s_mov_b32 s41, 4063232\n\t" "buffer_load_dwordx4 a[244:247], v16, %[ra], s41 offen\n\t" "s_waitcnt vmcnt(59)\n\t" \
   "ds_write_b128 v20, a[16:19] offset:8192\n\t" "s_mov_b32 s41, 4194304\n\t" "buffer_load_dwordx4 v[192:195], v16, %[ra], s41 offen\n\t" \
   "s_waitcnt vmcnt(59)\n\t" "ds_write_b128 v20, a[20:23] offset:12288\n\t" "s_mov_b32 s41, 4325376\n\t" \
   "buffer_load_dwordx4 v[196:199], v16, %[ra], s41 offen\n\t" "s_waitcnt vmcnt(59)\n\t" "ds_write_b128 v20, a[32:35] offset:16384\n\t" \
   "s_mov_b32 s41, 4456448\n\t" "buffer_load_dwordx4 v[208:211], v16, %[ra], s41 offen\n\t" "s_waitcnt vmcnt(59)\n\t" \
   "ds_write_b128 v20, a[36:39] offset:20480\n\t" "s_mov_b32 s41, 4587520\n\t" "buffer_load_dwordx4 v[212:215], v16, %[ra], s41 offen\n\t" \
   "s_waitcnt vmcnt(59)\n\t" "ds_write_b128 v20, a[48:51] offset:24576\n\t" "s_mov_b32 s41, 4718592\n\t" \
   "buffer_load_dwordx4 v[224:227], v16, %[ra], s41 offen\n\t" "s_waitcnt vmcnt(59)\n\t" "ds_write_b128 v20, a[52:55] offset:28672\n\t" \
   "s_mov_b32 s41, 4849664\n\t" "buffer_load_dwordx4 v[228:231], v16, %[ra], s41 offen\n\t" "s_waitcnt vmcnt(59)\n\t" \
   "ds_write_b128 v20, a[64:67] offset:32768\n\t" "s_mov_b32 s41, 4980736\n\t" "buffer_load_dwordx4 v[240:243], v16, %[ra], s41 offen\n\t" \
   "s_waitcnt vmcnt(59)\n\t" "ds_write_b128 v20, a[68:71] offset:36864\n\t" "s_mov_b32 s41, 5111808\n\t" \
   "buffer_load_dwordx4 v[244:247], v16, %[ra], s41 offen\n\t" "s_waitcnt lgkmcnt(0)\n\t" "s_barrier\n\t" "ds_read_b64_tr_b16 a[0:1], v18 offset:0\n\t" \
   "ds_read_b64_tr_b16 a[2:3], v18 offset:4096\n\t" "ds_read_b64_tr_b16 a[4:5], v19 offset:0\n\t" "ds_read_b64_tr_b16 a[6:7], v19 offset:4096\n\t" \
   "ds_read_b64_tr_b16 a[16:17], v18 offset:8192\n\t" "ds_read_b64_tr_b16 a[18:19], v18 offset:12288\n\t" \
   "ds_read_b64_tr_b16 a[20:21], v19 offset:8192\n\t" "ds_read_b64_tr_b16 a[22:23], v19 offset:12288\n\t" \
   "ds_read_b64_tr_b16 a[32:33], v18 offset:16384\n\t" "ds_read_b64_tr_b16 a[34:35], v18 offset:20480\n\t" \
   "ds_read_b64_tr_b16 a[36:37], v19 offset:16384\n\t" "ds_read_b64_tr_b16 a[38:39], v19 offset:20480\n\t" \
   "ds_read_b64_tr_b16 a[48:49], v18 offset:24576\n\t" "ds_read_b64_tr_b16 a[50:51], v18 offset:28672\n\t" \
   "ds_read_b64_tr_b16 a[52:53], v19 offset:24576\n\t" "ds_read_b64_tr_b16 a[54:55], v19 offset:28672\n\t" \
   "ds_read_b64_tr_b16 a[64:65], v18 offset:32768\n\t" "ds_read_b64_tr_b16 a[66:67], v18 offset:36864\n\t" \
   "ds_read_b64_tr_b16 a[68:69], v19 offset:32768\n\t" "ds_read_b64_tr_b16 a[70:71], v19 offset:36864\n\t" "s_waitcnt vmcnt(59)\n\t" \
   "s_waitcnt lgkmcnt(0)\n\t" "s_barrier\n\t" "ds_write_b128 v20, a[8:11] offset:0\n\t" "s_mov_b32 s41, 3932160\n\t" \
   "buffer_load_dwordx4 a[248:251], v16, %[rb], s41 offen\n\t" "s_waitcnt vmcnt(59)\n\t" "ds_write_b128 v20, a[12:15] offset:4096\n\t" \
   "s_mov_b32 s41, 4063232\n\t" "buffer_load_dwordx4 a[252:255], v16, %[rb], s41 offen\n\t" "s_waitcnt vmcnt(59)\n\t" \
   "ds_write_b128 v20, a[24:27] offset:8192\n\t" "s_mov_b32 s41, 4194304\n\t" "buffer_load_dwordx4 v[200:203], v16, %[rb], s41 offen\n\t" \
   "s_waitcnt vmcnt(59)\n\t" "ds_write_b128 v20, a[28:31] offset:12288\n\t" "s_mov_b32 s41, 4325376\n\t" \
   "buffer_load_dwordx4 v[204:207], v16, %[rb], s41 offen\n\t" "s_waitcnt vmcnt(59)\n\t" "ds_write_b128 v20, a[40:43] offset:16384\n\t" \
   "s_mov_b32 s41, 4456448\n\t" "buffer_load_dwordx4 v[216:219], v16, %[rb], s41 offen\n\t" "s_waitcnt vmcnt(59)\n\t" \
   "ds_write_b128 v20, a[44:47] offset:20480\n\t" "s_mov_b32 s41, 4587520\n\t" "buffer_load_dwordx4 v[220:223], v16, %[rb], s41 offen\n\t" \
   "s_waitcnt vmcnt(59)\n\t" "ds_write_b128 v20, a[56:59] offset:24576\n\t" "s_mov_b32 s41, 4718592\n\t" \
   "buffer_load_dwordx4 v[232:235], v16, %[rb], s41 offen\n\t" "s_waitcnt vmcnt(59)\n\t" "ds_write_b128 v20, a[60:63] offset:28672\n\t" \
   "s_mov_b32 s41, 4849664\n\t" "buffer_load_dwordx4 v[236:239], v16, %[rb], s41 offen\n\t" "s_waitcnt vmcnt(59)\n\t" \
   "ds_write_b128 v20, a[72:75] offset:32768\n\t" "s_mov_b32 s41, 4980736\n\t" "buffer_load_dwordx4 v[248:251], v16, %[rb], s41 offen\n\t" \
   "s_waitcnt vmcnt(59)\n\t" "ds_write_b128 v20, a[76:79] offset:36864\n\t" "s_mov_b32 s41, 5111808\n\t" \
   "buffer_load_dwordx4 v[252:255], v16, %[rb], s41 offen\n\t" "s_waitcnt lgkmcnt(0)\n\t" "s_barrier\n\t" "ds_read_b64_tr_b16 a[8:9], v18 offset:0\n\t" \
   "ds_read_b64_tr_b16 a[10:11], v18 offset:4096\n\t" "ds_read_b64_tr_b16 a[12:13], v19 offset:0\n\t" \
   "ds_read_b64_tr_b16 a[14:15], v19 offset:4096\n\t" "ds_read_b64_tr_b16 a[24:25], v18 offset:8192\n\t" \
   "ds_read_b64_tr_b16 a[26:27], v18 offset:12288\n\t" "ds_read_b64_tr_b16 a[28:29], v19 offset:8192\n\t" \
   "ds_read_b64_tr_b16 a[30:31], v19 offset:12288\n\t" "ds_read_b64_tr_b16 a[40:41], v18 offset:16384\n\t" \
   "ds_read_b64_tr_b16 a[42:43], v18 offset:20480\n\t" "ds_read_b64_tr_b16 a[44:45], v19 offset:16384\n\t" \
   "ds_read_b64_tr_b16 a[46:47], v19 offset:20480\n\t" "ds_read_b64_tr_b16 a[56:57], v18 offset:24576\n\t" \
   "ds_read_b64_tr_b16 a[58:59], v18 offset:28672\n\t" "ds_read_b64_tr_b16 a[60:61], v19 offset:24576\n\t" \
   "ds_read_b64_tr_b16 a[62:63], v19 offset:28672\n\t" "ds_read_b64_tr_b16 a[72:73], v18 offset:32768\n\t" \
   "ds_read_b64_tr_b16 a[74:75], v18 offset:36864\n\t" "ds_read_b64_tr_b16 a[76:77], v19 offset:32768\n\t" \
   "ds_read_b64_tr_b16 a[78:79], v19 offset:36864\n\t" "s_waitcnt vmcnt(59)\n\t" "s_waitcnt lgkmcnt(0)\n\t" "s_barrier\n\t" \
   "ds_write_b128 v20, a[80:83] offset:0\n\t" "s_waitcnt vmcnt(58)\n\t" "ds_write_b128 v20, a[84:87] offset:4096\n\t" "s_waitcnt vmcnt(57)\n\t" \
   "ds_write_b128 v20, a[96:99] offset:8192\n\t" "s_waitcnt vmcnt(56)\n\t" "ds_write_b128 v20, a[100:103] offset:12288\n\t" "s_waitcnt vmcnt(55)\n\t" \
   "ds_write_b128 v20, a[112:115] offset:16384\n\t" "s_waitcnt vmcnt(54)\n\t" "ds_write_b128 v20, a[116:119] offset:20480\n\t" \
   "s_waitcnt vmcnt(53)\n\t" "ds_write_b128 v20, a[128:131] offset:24576\n\t" "s_waitcnt vmcnt(52)\n\t" \
   "ds_write_b128 v20, a[132:135] offset:28672\n\t" "s_waitcnt vmcnt(51)\n\t" "ds_write_b128 v20, a[144:147] offset:32768\n\t" \
   "s_waitcnt vmcnt(50)\n\t" "ds_write_b128 v20, a[148:151] offset:36864\n\t" "s_waitcnt lgkmcnt(0)\n\t" "s_barrier\n\t" \
   "ds_read_b64_tr_b16 a[80:81], v18 offset:0\n\t" "ds_read_b64_tr_b16 a[82:83], v18 offset:4096\n\t" "ds_read_b64_tr_b16 a[84:85], v19 offset:0\n\t" \
   "ds_read_b64_tr_b16 a[86:87], v19 offset:4096\n\t" "ds_read_b64_tr_b16 a[96:97], v18 offset:8192\n\t" \
   "ds_read_b64_tr_b16 a[98:99], v18 offset:12288\n\t" "ds_read_b64_tr_b16 a[100:101], v19 offset:8192\n\t" \
   "ds_read_b64_tr_b16 a[102:103], v19 offset:12288\n\t" "ds_read_b64_tr_b16 a[112:113], v18 offset:16384\n\t" \
   "ds_read_b64_tr_b16 a[114:115], v18 offset:20480\n\t" "ds_read_b64_tr_b16 a[116:117], v19 offset:16384\n\t" \
   "ds_read_b64_tr_b16 a[118:119], v19 offset:20480\n\t" "ds_read_b64_tr_b16 a[128:129], v18 offset:24576\n\t" \
   "ds_read_b64_tr_b16 a[130:131], v18 offset:28672\n\t" "ds_read_b64_tr_b16 a[132:133], v19 offset:24576\n\t" \
   "ds_read_b64_tr_b16 a[134:135], v19 offset:28672\n\t" "ds_read_b64_tr_b16 a[144:145], v18 offset:32768\n\t" \
   "ds_read_b64_tr_b16 a[146:147], v18 offset:36864\n\t" "ds_read_b64_tr_b16 a[148:149], v19 offset:32768\n\t" \
   "ds_read_b64_tr_b16 a[150:151], v19 offset:36864\n\t" "s_waitcnt vmcnt(49)\n\t" "s_waitcnt lgkmcnt(0)\n\t" "s_barrier\n\t" \
   "ds_write_b128 v20, a[88:91] offset:0\n\t" "s_waitcnt vmcnt(48)\n\t" "ds_write_b128 v20, a[92:95] offset:4096\n\t" "s_waitcnt vmcnt(47)\n\t" \
   "ds_write_b128 v20, a[104:107] offset:8192\n\t" "s_waitcnt vmcnt(46)\n\t" "ds_write_b128 v20, a[108:111] offset:12288\n\t" "s_waitcnt vmcnt(45)\n\t" \
   "ds_write_b128 v20, a[120:123] offset:16384\n\t" "s_waitcnt vmcnt(44)\n\t" "ds_write_b128 v20, a[124:127] offset:20480\n\t" \
   "s_waitcnt vmcnt(43)\n\t" "ds_write_b128 v20, a[136:139] offset:24576\n\t" "s_waitcnt vmcnt(42)\n\t" \
   "ds_write_b128 v20, a[140:143] offset:28672\n\t" "s_waitcnt vmcnt(41)\n\t" "ds_write_b128 v20, a[152:155] offset:32768\n\t" \
   "s_waitcnt vmcnt(40)\n\t" "ds_write_b128 v20, a[156:159] offset:36864\n\t" "s_waitcnt lgkmcnt(0)\n\t" "s_barrier\n\t" \
   "ds_read_b64_tr_b16 a[88:89], v18 offset:0\n\t" "ds_read_b64_tr_b16 a[90:91], v18 offset:4096\n\t" "ds_read_b64_tr_b16 a[92:93], v19 offset:0\n\t" \
   "ds_read_b64_tr_b16 a[94:95], v19 offset:4096\n\t" "ds_read_b64_tr_b16 a[104:105], v18 offset:8192\n\t" \
   "ds_read_b64_tr_b16 a[106:107], v18 offset:12288\n\t" "ds_read_b64_tr_b16 a[108:109], v19 offset:8192\n\t" \
   "ds_read_b64_tr_b16 a[110:111], v19 offset:12288\n\t" "ds_read_b64_tr_b16 a[120:121], v18 offset:16384\n\t" \
   "ds_read_b64_tr_b16 a[122:123], v18 offset:20480\n\t" "ds_read_b64_tr_b16 a[124:125], v19 offset:16384\n\t" \
   "ds_read_b64_tr_b16 a[126:127], v19 offset:20480\n\t" "ds_read_b64_tr_b16 a[136:137], v18 offset:24576\n\t" \
   "ds_read_b64_tr_b16 a[138:139], v18 offset:28672\n\t" "ds_read_b64_tr_b16 a[140:141], v19 offset:24576\n\t" \
   "ds_read_b64_tr_b16 a[142:143], v19 offset:28672\n\t" "ds_read_b64_tr_b16 a[152:153], v18 offset:32768\n\t" \
   "ds_read_b64_tr_b16 a[154:155], v18 offset:36864\n\t" "ds_read_b64_tr_b16 a[156:157], v19 offset:32768\n\t" \
   "ds_read_b64_tr_b16 a[158:159], v19 offset:36864\n\t" "s_waitcnt vmcnt(39)\n\t" "s_waitcnt lgkmcnt(0)\n\t" "s_barrier\n\t" \
   "ds_write_b128 v20, a[160:163] offset:0\n\t" "s_waitcnt vmcnt(38)\n\t" "ds_write_b128 v20, a[164:167] offset:4096\n\t" "s_waitcnt vmcnt(37)\n\t" \
   "ds_write_b128 v20, a[176:179] offset:8192\n\t" "s_waitcnt vmcnt(36)\n\t" "ds_write_b128 v20, a[180:183] offset:12288\n\t" "s_waitcnt vmcnt(35)\n\t" \
   "ds_write_b128 v20, a[192:195] offset:16384\n\t" "s_waitcnt vmcnt(34)\n\t" "ds_write_b128 v20, a[196:199] offset:20480\n\t" \
   "s_waitcnt vmcnt(33)\n\t" "ds_write_b128 v20, a[208:211] offset:24576\n\t" "s_waitcnt vmcnt(32)\n\t" \
   "ds_write_b128 v20, a[212:215] offset:28672\n\t" "s_waitcnt vmcnt(31)\n\t" "ds_write_b128 v20, a[224:227] offset:32768\n\t" \
   "s_waitcnt vmcnt(30)\n\t" "ds_write_b128 v20, a[228:231] offset:36864\n\t" "s_waitcnt lgkmcnt(0)\n\t" "s_barrier\n\t" \
   "ds_read_b64_tr_b16 a[160:161], v18 offset:0\n\t" "ds_read_b64_tr_b16 a[162:163], v18 offset:4096\n\t" \
   "ds_read_b64_tr_b16 a[164:165], v19 offset:0\n\t" "ds_read_b64_tr_b16 a[166:167], v19 offset:4096\n\t" \
   "ds_read_b64_tr_b16 a[176:177], v18 offset:8192\n\t" "ds_read_b64_tr_b16 a[178:179], v18 offset:12288\n\t" \
   "ds_read_b64_tr_b16 a[180:181], v19 offset:8192\n\t" "ds_read_b64_tr_b16 a[182:183], v19 offset:12288\n\t" \
   "ds_read_b64_tr_b16 a[192:193], v18 offset:16384\n\t" "ds_read_b64_tr_b16 a[194:195], v18 offset:20480\n\t" \
   "ds_read_b64_tr_b16 a[196:197], v19 offset:16384\n\t" "ds_read_b64_tr_b16 a[198:199], v19 offset:20480\n\t" \
   "ds_read_b64_tr_b16 a[208:209], v18 offset:24576\n\t" "ds_read_b64_tr_b16 a[210:211], v18 offset:28672\n\t" \
   "ds_read_b64_tr_b16 a[212:213], v19 offset:24576\n\t" "ds_read_b64_tr_b16 a[214:215], v19 offset:28672\n\t" \
   "ds_read_b64_tr_b16 a[224:225], v18 offset:32768\n\t" "ds_read_b64_tr_b16 a[226:227], v18 offset:36864\n\t" \
   "ds_read_b64_tr_b16 a[228:229], v19 offset:32768\n\t" "ds_read_b64_tr_b16 a[230:231], v19 offset:36864\n\t" "s_waitcnt vmcnt(29)\n\t" \
   "s_waitcnt lgkmcnt(0)\n\t" "s_barrier\n\t" "ds_write_b128 v20, a[168:171] offset:0\n\t" "s_waitcnt vmcnt(28)\n\t" \
   "ds_write_b128 v20, a[172:175] offset:4096\n\t" "s_waitcnt vmcnt(27)\n\t" "ds_write_b128 v20, a[184:187] offset:8192\n\t" "s_waitcnt vmcnt(26)\n\t" \
   "ds_write_b128 v20, a[188:191] offset:12288\n\t" "s_waitcnt vmcnt(25)\n\t" "ds_write_b128 v20, a[200:203] offset:16384\n\t" \
   "s_waitcnt vmcnt(24)\n\t" "ds_write_b128 v20, a[204:207] offset:20480\n\t" "s_waitcnt vmcnt(23)\n\t" \
   "ds_write_b128 v20, a[216:219] offset:24576\n\t" "s_waitcnt vmcnt(22)\n\t" "ds_write_b128 v20, a[220:223] offset:28672\n\t" \
   "s_waitcnt vmcnt(21)\n\t" "ds_write_b128 v20, a[232:235] offset:32768\n\t" "s_waitcnt vmcnt(20)\n\t" \
   "ds_write_b128 v20, a[236:239] offset:36864\n\t" "s_waitcnt lgkmcnt(0)\n\t" "s_barrier\n\t" "ds_read_b64_tr_b16 a[168:169], v18 offset:0\n\t" \
   "ds_read_b64_tr_b16 a[170:171], v18 offset:4096\n\t" "ds_read_b64_tr_b16 a[172:173], v19 offset:0\n\t" \
   "ds_read_b64_tr_b16 a[174:175], v19 offset:4096\n\t" "ds_read_b64_tr_b16 a[184:185], v18 offset:8192\n\t" \
   "ds_read_b64_tr_b16 a[186:187], v18 offset:12288\n\t" "ds_read_b64_tr_b16 a[188:189], v19 offset:8192\n\t" \
   "ds_read_b64_tr_b16 a[190:191], v19 offset:12288\n\t" "ds_read_b64_tr_b16 a[200:201], v18 offset:16384\n\t" \
   "ds_read_b64_tr_b16 a[202:203], v18 offset:20480\n\t" "ds_read_b64_tr_b16 a[204:205], v19 offset:16384\n\t" \
   "ds_read_b64_tr_b16 a[206:207], v19 offset:20480\n\t" "ds_read_b64_tr_b16 a[216:217], v18 offset:24576\n\t" \
   "ds_read_b64_tr_b16 a[218:219], v18 offset:28672\n\t" "ds_read_b64_tr_b16 a[220:221], v19 offset:24576\n\t" \
   "ds_read_b64_tr_b16 a[222:223], v19 offset:28672\n\t" "ds_read_b64_tr_b16 a[232:233], v18 offset:32768\n\t" \
   "ds_read_b64_tr_b16 a[234:235], v18 offset:36864\n\t" "ds_read_b64_tr_b16 a[236:237], v19 offset:32768\n\t" \
   "ds_read_b64_tr_b16 a[238:239], v19 offset:36864\n\t" "s_waitcnt vmcnt(19)\n\t" "s_waitcnt lgkmcnt(0)\n\t" "s_barrier\n\t" \
   "ds_write_b128 v20, a[240:243] offset:0\n\t" "s_waitcnt vmcnt(18)\n\t" "ds_write_b128 v20, a[244:247] offset:4096\n\t" "s_waitcnt vmcnt(17)\n\t" \
   "ds_write_b128 v20, v[192:195] offset:8192\n\t" "s_waitcnt vmcnt(16)\n\t" "ds_write_b128 v20, v[196:199] offset:12288\n\t" "s_waitcnt vmcnt(15)\n\t" \
   "ds_write_b128 v20, v[208:211] offset:16384\n\t" "s_waitcnt vmcnt(14)\n\t" "ds_write_b128 v20, v[212:215] offset:20480\n\t" \
   "s_waitcnt vmcnt(13)\n\t" "ds_write_b128 v20, v[224:227] offset:24576\n\t" "s_waitcnt vmcnt(12)\n\t" \
   "ds_write_b128 v20, v[228:231] offset:28672\n\t" "s_waitcnt vmcnt(11)\n\t" "ds_write_b128 v20, v[240:243] offset:32768\n\t" \
   "s_waitcnt vmcnt(10)\n\t" "ds_write_b128 v20, v[244:247] offset:36864\n\t" "s_waitcnt lgkmcnt(0)\n\t" "s_barrier\n\t" \
   "ds_read_b64_tr_b16 a[240:241], v18 offset:0\n\t" "ds_read_b64_tr_b16 a[242:243], v18 offset:4096\n\t" \
   "ds_read_b64_tr_b16 a[244:245], v19 offset:0\n\t" "ds_read_b64_tr_b16 a[246:247], v19 offset:4096\n\t" \
   "ds_read_b64_tr_b16 v[192:193], v18 offset:8192\n\t" "ds_read_b64_tr_b16 v[194:195], v18 offset:12288\n\t" \
   "ds_read_b64_tr_b16 v[196:197], v19 offset:8192\n\t" "ds_read_b64_tr_b16 v[198:199], v19 offset:12288\n\t" \
   "ds_read_b64_tr_b16 v[208:209], v18 offset:16384\n\t" "ds_read_b64_tr_b16 v[210:211], v18 offset:20480\n\t" \
   "ds_read_b64_tr_b16 v[212:213], v19 offset:16384\n\t" "ds_read_b64_tr_b16 v[214:215], v19 offset:20480\n\t" \
   "ds_read_b64_tr_b16 v[224:225], v18 offset:24576\n\t" "ds_read_b64_tr_b16 v[226:227], v18 offset:28672\n\t" \
   "ds_read_b64_tr_b16 v[228:229], v19 offset:24576\n\t" "ds_read_b64_tr_b16 v[230:231], v19 offset:28672\n\t" \
   "ds_read_b64_tr_b16 v[240:241], v18 offset:32768\n\t" "ds_read_b64_tr_b16 v[242:243], v18 offset:36864\n\t" \
   "ds_read_b64_tr_b16 v[244:245], v19 offset:32768\n\t" "ds_read_b64_tr_b16 v[246:247], v19 offset:36864\n\t" "s_waitcnt vmcnt(9)\n\t" \
   "s_waitcnt lgkmcnt(0)\n\t" "s_barrier\n\t" "ds_write_b128 v20, a[248:251] offset:0\n\t" "s_waitcnt vmcnt(8)\n\t" \
   "ds_write_b128 v20, a[252:255] offset:4096\n\t" "s_waitcnt vmcnt(7)\n\t" "ds_write_b128 v20, v[200:203] offset:8192\n\t" "s_waitcnt vmcnt(6)\n\t" \
   "ds_write_b128 v20, v[204:207] offset:12288\n\t" "s_waitcnt vmcnt(5)\n\t" "ds_write_b128 v20, v[216:219] offset:16384\n\t" "s_waitcnt vmcnt(4)\n\t" \
   "ds_write_b128 v20, v[220:223] offset:20480\n\t" "s_waitcnt vmcnt(3)\n\t" "ds_write_b128 v20, v[232:235] offset:24576\n\t" "s_waitcnt vmcnt(2)\n\t" \
   "ds_write_b128 v20, v[236:239] offset:28672\n\t" "s_waitcnt vmcnt(1)\n\t" "ds_write_b128 v20, v[248:251] offset:32768\n\t" "s_waitcnt vmcnt(0)\n\t" \
   "ds_write_b128 v20, v[252:255] offset:36864\n\t" "s_waitcnt lgkmcnt(0)\n\t" "s_barrier\n\t" "ds_read_b64_tr_b16 a[248:249], v18 offset:0\n\t" \
   "ds_read_b64_tr_b16 a[250:251], v18 offset:4096\n\t" "ds_read_b64_tr_b16 a[252:253], v19 offset:0\n\t" \
   "ds_read_b64_tr_b16 a[254:255], v19 offset:4096\n\t" "ds_read_b64_tr_b16 v[200:201], v18 offset:8192\n\t" \
   "ds_read_b64_tr_b16 v[202:203], v18 offset:12288\n\t" "ds_read_b64_tr_b16 v[204:205], v19 offset:8192\n\t" \
   "ds_read_b64_tr_b16 v[206:207], v19 offset:12288\n\t" "ds_read_b64_tr_b16 v[216:217], v18 offset:16384\n\t" \
   "ds_read_b64_tr_b16 v[218:219], v18 offset:20480\n\t" "ds_read_b64_tr_b16 v[220:221], v19 offset:16384\n\t" \
   "ds_read_b64_tr_b16 v[222:223], v19 offset:20480\n\t" "ds_read_b64_tr_b16 v[232:233], v18 offset:24576\n\t" \
   "ds_read_b64_tr_b16 v[234:235], v18 offset:28672\n\t" "ds_read_b64_tr_b16 v[236:237], v19 offset:24576\n\t" \
   "ds_read_b64_tr_b16 v[238:239], v19 offset:28672\n\t" "ds_read_b64_tr_b16 v[248:249], v18 offset:32768\n\t" \
   "ds_read_b64_tr_b16 v[250:251], v18 offset:36864\n\t" "ds_read_b64_tr_b16 v[252:253], v19 offset:32768\n\t" \
   "ds_read_b64_tr_b16 v[254:255], v19 offset:36864\n\t" "s_waitcnt lgkmcnt(0)"
#define LDK_CLOB "v24", "v25", "s40", "s41"
#define LDV_ASM \
   "s_mov_b32 s41, 0\n\t" "buffer_load_dwordx4 a[0:3], v16, %[ra], s41 offen\n\t" "s_mov_b32 s41, 131072\n\t" \
   "buffer_load_dwordx4 a[4:7], v16, %[ra], s41 offen\n\t" "s_mov_b32 s41, 262144\n\t" "buffer_load_dwordx4 a[8:11], v16, %[ra], s41 offen\n\t" \
   "s_mov_b32 s41, 393216\n\t" "buffer_load_dwordx4 a[12:15], v16, %[ra], s41 offen\n\t" "s_mov_b32 s41, 524288\n\t" \
   "buffer_load_dwordx4 a[16:19], v16, %[ra], s41 offen\n\t" "s_mov_b32 s41, 655360\n\t" "buffer_load_dwordx4 a[20:23], v16, %[ra], s41 offen\n\t" \
   "s_mov_b32 s41, 786432\n\t" "buffer_load_dwordx4 a[24:27], v16, %[ra], s41 offen\n\t" "s_mov_b32 s41, 917504\n\t" \
   "buffer_load_dwordx4 a[28:31], v16, %[ra], s41 offen\n\t" "s_mov_b32 s41, 1048576\n\t" "buffer_load_dwordx4 a[32:35], v16, %[ra], s41 offen\n\t" \
   "s_mov_b32 s41, 1179648\n\t" "buffer_load_dwordx4 a[36:39], v16, %[ra], s41 offen\n\t" "s_mov_b32 s41, 1310720\n\t" \
   "buffer_load_dwordx4 a[40:43], v16, %[ra], s41 offen\n\t" "s_mov_b32 s41, 1441792\n\t" "buffer_load_dwordx4 a[44:47], v16, %[ra], s41 offen\n\t" \
   "s_mov_b32 s41, 1572864\n\t" "buffer_load_dwordx4 a[48:51], v16, %[ra], s41 offen\n\t" "s_mov_b32 s41, 1703936\n\t" \
   "buffer_load_dwordx4 a[52:55], v16, %[ra], s41 offen\n\t" "s_mov_b32 s41, 1835008\n\t" "buffer_load_dwordx4 a[56:59], v16, %[ra], s41 offen\n\t" \
   "s_mov_b32 s41, 1966080\n\t" "buffer_load_dwordx4 a[60:63], v16, %[ra], s41 offen\n\t" "s_mov_b32 s41, 2097152\n\t" \
   "buffer_load_dwordx4 a[64:67], v16, %[ra], s41 offen\n\t" "s_mov_b32 s41, 2228224\n\t" "buffer_load_dwordx4 a[68:71], v16, %[ra], s41 offen\n\t" \
   "s_mov_b32 s41, 2359296\n\t" "buffer_load_dwordx4 a[72:75], v16, %[ra], s41 offen\n\t" "s_mov_b32 s41, 2490368\n\t" \
   "buffer_load_dwordx4 a[76:79], v16, %[ra], s41 offen\n\t" "s_mov_b32 s41, 2621440\n\t" "buffer_load_dwordx4 a[80:83], v16, %[ra], s41 offen\n\t" \
   "s_mov_b32 s41, 2752512\n\t" "buffer_load_dwordx4 a[84:87], v16, %[ra], s41 offen\n\t" "s_mov_b32 s41, 2883584\n\t" \
   "buffer_load_dwordx4 a[88:91], v16, %[ra], s41 offen\n\t" "s_mov_b32 s41, 3014656\n\t" "buffer_load_dwordx4 a[92:95], v16, %[ra], s41 offen\n\t" \
   "s_mov_b32 s41, 3145728\n\t" "buffer_load_dwordx4 a[96:99], v16, %[ra], s41 offen\n\t" "s_mov_b32 s41, 3276800\n\t" \
   "buffer_load_dwordx4 a[100:103], v16, %[ra], s41 offen\n\t" "s_mov_b32 s41, 3407872\n\t" "buffer_load_dwordx4 a[104:107], v16, %[ra], s41 offen\n\t" \
   "s_mov_b32 s41, 3538944\n\t" "buffer_load_dwordx4 a[108:111], v16, %[ra], s41 offen\n\t" "s_mov_b32 s41, 3670016\n\t" \
   "buffer_load_dwordx4 a[112:115], v16, %[ra], s41 offen\n\t" "s_mov_b32 s41, 3801088\n\t" "buffer_load_dwordx4 a[116:119], v16, %[ra], s41 offen\n\t" \
   "s_mov_b32 s41, 3932160\n\t" "buffer_load_dwordx4 a[120:123], v16, %[ra], s41 offen\n\t" "s_mov_b32 s41, 4063232\n\t" \
   "buffer_load_dwordx4 a[124:127], v16, %[ra], s41 offen\n\t" "s_mov_b32 s41, 4194304\n\t" "buffer_load_dwordx4 a[128:131], v16, %[ra], s41 offen\n\t" \
   "s_mov_b32 s41, 4325376\n\t" "buffer_load_dwordx4 a[132:135], v16, %[ra], s41 offen\n\t" "s_mov_b32 s41, 4456448\n\t" \
   "buffer_load_dwordx4 a[136:139], v16, %[ra], s41 offen\n\t" "s_mov_b32 s41, 4587520\n\t" "buffer_load_dwordx4 a[140:143], v16, %[ra], s41 offen\n\t" \
   "s_mov_b32 s41, 4718592\n\t" "buffer_load_dwordx4 a[144:147], v16, %[ra], s41 offen\n\t" "s_mov_b32 s41, 4849664\n\t" \
   "buffer_load_dwordx4 a[148:151], v16, %[ra], s41 offen\n\t" "s_mov_b32 s41, 4980736\n\t" "buffer_load_dwordx4 a[152:155], v16, %[ra], s41 offen\n\t" \
   "s_mov_b32 s41, 5111808\n\t" "buffer_load_dwordx4 a[156:159], v16, %[ra], s41 offen\n\t" "s_mov_b32 s41, 0\n\t" \
   "buffer_load_dwordx4 a[160:163], v17, %[rb], s41 offen\n\t" "s_mov_b32 s41, 256\n\t" "buffer_load_dwordx4 a[176:179], v17, %[rb], s41 offen\n\t" \
   "s_mov_b32 s41, 512\n\t" "buffer_load_dwordx4 a[192:195], v17, %[rb], s41 offen\n\t" "s_mov_b32 s41, 768\n\t" \
   "buffer_load_dwordx4 a[208:211], v17, %[rb], s41 offen\n\t" "s_mov_b32 s41, 1024\n\t" "buffer_load_dwordx4 a[224:227], v17, %[rb], s41 offen\n\t" \
   "s_mov_b32 s41, 20480\n\t" "buffer_load_dwordx4 a[240:243], v17, %[rb], s41 offen\n\t" "s_mov_b32 s41, 20736\n\t" \
   "buffer_load_dwordx4 v[192:195], v17, %[rb], s41 offen\n\t" "s_mov_b32 s41, 20992\n\t" "buffer_load_dwordx4 v[208:211], v17, %[rb], s41 offen\n\t" \
   "s_mov_b32 s41, 21248\n\t" "buffer_load_dwordx4 v[224:227], v17, %[rb], s41 offen\n\t" "s_mov_b32 s41, 21504\n\t" \
   "buffer_load_dwordx4 v[240:243], v17, %[rb], s41 offen\n\t" "s_mov_b32 s41, 40960\n\t" "buffer_load_dwordx4 a[164:167], v17, %[rb], s41 offen\n\t" \
   "s_mov_b32 s41, 41216\n\t" "buffer_load_dwordx4 a[180:183], v17, %[rb], s41 offen\n\t" "s_mov_b32 s41, 41472\n\t" \
   "buffer_load_dwordx4 a[196:199], v17, %[rb], s41 offen\n\t" "s_mov_b32 s41, 41728\n\t" "buffer_load_dwordx4 a[212:215], v17, %[rb], s41 offen\n\t" \
   "s_mov_b32 s41, 41984\n\t" "buffer_load_dwordx4 a[228:231], v17, %[rb], s41 offen\n\t" "s_mov_b32 s41, 61440\n\t" \
   "buffer_load_dwordx4 a[244:247], v17, %[rb], s41 offen\n\t" "s_mov_b32 s41, 61696\n\t" "buffer_load_dwordx4 v[196:199], v17, %[rb], s41 offen\n\t" \
   "s_mov_b32 s41, 61952\n\t" "buffer_load_dwordx4 v[212:215], v17, %[rb], s41 offen\n\t" "s_mov_b32 s41, 62208\n\t" \
   "buffer_load_dwordx4 v[228:231], v17, %[rb], s41 offen\n\t" "s_mov_b32 s41, 62464\n\t" "buffer_load_dwordx4 v[244:247], v17, %[rb], s41 offen\n\t" \
   "s_waitcnt vmcnt(59)\n\t" "ds_write_b128 v20, a[0:3] offset:0\n\t" "s_mov_b32 s41, 81920\n\t" \
   "buffer_load_dwordx4 a[168:171], v17, %[rb], s41 offen\n\t" "s_waitcnt vmcnt(59)\n\t" "ds_write_b128 v20, a[4:7] offset:4096\n\t" \
   "s_mov_b32 s41, 82176\n\t" "buffer_load_dwordx4 a[184:187], v17, %[rb], s41 offen\n\t" "s_waitcnt vmcnt(59)\n\t" \
   "ds_write_b128 v20, a[8:11] offset:8192\n\t" "s_mov_b32 s41, 82432\n\t" "buffer_load_dwordx4 a[200:203], v17, %[rb], s41 offen\n\t" \
   "s_waitcnt vmcnt(59)\n\t" "ds_write_b128 v20, a[12:15] offset:12288\n\t" "s_mov_b32 s41, 82688\n\t" \
   "buffer_load_dwordx4 a[216:219], v17, %[rb], s41 offen\n\t" "s_waitcnt vmcnt(59)\n\t" "ds_write_b128 v20, a[16:19] offset:16384\n\t" \
   "s_mov_b32 s41, 82944\n\t" "buffer_load_dwordx4 a[232:235], v17, %[rb], s41 offen\n\t" "s_waitcnt vmcnt(59)\n\t" \
   "ds_write_b128 v20, a[20:23] offset:20480\n\t" "s_mov_b32 s41, 102400\n\t" "buffer_load_dwordx4 a[248:251], v17, %[rb], s41 offen\n\t" \
   "s_waitcnt vmcnt(59)\n\t" "ds_write_b128 v20, a[24:27] offset:24576\n\t" "s_mov_b32 s41, 102656\n\t" \
   "buffer_load_dwordx4 v[200:203], v17, %[rb], s41 offen\n\t" "s_waitcnt vmcnt(59)\n\t" "ds_write_b128 v20, a[28:31] offset:28672\n\t" \
   "s_mov_b32 s41, 102912\n\t" "buffer_load_dwordx4 v[216:219], v17, %[rb], s41 offen\n\t" "s_waitcnt vmcnt(59)\n\t" \
   "ds_write_b128 v20, a[32:35] offset:32768\n\t" "s_mov_b32 s41, 103168\n\t" "buffer_load_dwordx4 v[232:235], v17, %[rb], s41 offen\n\t" \
   "s_waitcnt vmcnt(59)\n\t" "ds_write_b128 v20, a[36:39] offset:36864\n\t" "s_mov_b32 s41, 103424\n\t" \
   "buffer_load_dwordx4 v[248:251], v17, %[rb], s41 offen\n\t" "s_waitcnt lgkmcnt(0)\n\t" "s_barrier\n\t" "ds_read_b64_tr_b16 a[0:1], v18 offset:0\n\t" \
   "ds_read_b64_tr_b16 a[2:3], v18 offset:4096\n\t" "ds_read_b64_tr_b16 a[4:5], v19 offset:0\n\t" "ds_read_b64_tr_b16 a[6:7], v19 offset:4096\n\t" \
   "ds_read_b64_tr_b16 a[8:9], v18 offset:8192\n\t" "ds_read_b64_tr_b16 a[10:11], v18 offset:12288\n\t" \
   "ds_read_b64_tr_b16 a[12:13], v19 offset:8192\n\t" "ds_read_b64_tr_b16 a[14:15], v19 offset:12288\n\t" \
   "ds_read_b64_tr_b16 a[16:17], v18 offset:16384\n\t" "ds_read_b64_tr_b16 a[18:19], v18 offset:20480\n\t" \
   "ds_read_b64_tr_b16 a[20:21], v19 offset:16384\n\t" "ds_read_b64_tr_b16 a[22:23], v19 offset:20480\n\t" \
   "ds_read_b64_tr_b16 a[24:25], v18 offset:24576\n\t" "ds_read_b64_tr_b16 a[26:27], v18 offset:28672\n\t" \
   "ds_read_b64_tr_b16 a[28:29], v19 offset:24576\n\t" "ds_read_b64_tr_b16 a[30:31], v19 offset:28672\n\t" \
   "ds_read_b64_tr_b16 a[32:33], v18 offset:32768\n\t" "ds_read_b64_tr_b16 a[34:35], v18 offset:36864\n\t" \
   "ds_read_b64_tr_b16 a[36:37], v19 offset:32768\n\t" "ds_read_b64_tr_b16 a[38:39], v19 offset:36864\n\t" "s_waitcnt vmcnt(59)\n\t" \
   "s_waitcnt lgkmcnt(0)\n\t" "s_barrier\n\t" "ds_write_b128 v20, a[40:43] offset:0\n\t" "s_mov_b32 s41, 122880\n\t" \
   "buffer_load_dwordx4 a[172:175], v17, %[rb], s41 offen\n\t" "s_waitcnt vmcnt(59)\n\t" "ds_write_b128 v20, a[44:47] offset:4096\n\t" \
   "s_mov_b32 s41, 123136\n\t" "buffer_load_dwordx4 a[188:191], v17, %[rb], s41 offen\n\t" "s_waitcnt vmcnt(59)\n\t" \
   "ds_write_b128 v20, a[48:51] offset:8192\n\t" "s_mov_b32 s41, 123392\n\t" "buffer_load_dwordx4 a[204:207], v17, %[rb], s41 offen\n\t" \
   "s_waitcnt vmcnt(59)\n\t" "ds_write_b128 v20, a[52:55] offset:12288\n\t" "s_mov_b32 s41, 123648\n\t" \
   "buffer_load_dwordx4 a[220:223], v17, %[rb], s41 offen\n\t" "s_waitcnt vmcnt(59)\n\t" "ds_write_b128 v20, a[56:59] offset:16384\n\t" \
   "s_mov_b32 s41, 123904\n\t" "buffer_load_dwordx4 a[236:239], v17, %[rb], s41 offen\n\t" "s_waitcnt vmcnt(59)\n\t" \
   "ds_write_b128 v20, a[60:63] offset:20480\n\t" "s_mov_b32 s41, 143360\n\t" "buffer_load_dwordx4 a[252:255], v17, %[rb], s41 offen\n\t" \
   "s_waitcnt vmcnt(59)\n\t" "ds_write_b128 v20, a[64:67] offset:24576\n\t" "s_mov_b32 s41, 143616\n\t" \
   "buffer_load_dwordx4 v[204:207], v17, %[rb], s41 offen\n\t" "s_waitcnt vmcnt(59)\n\t" "ds_write_b128 v20, a[68:71] offset:28672\n\t" \
   "s_mov_b32 s41, 143872\n\t" "buffer_load_dwordx4 v[220:223], v17, %[rb], s41 offen\n\t" "s_waitcnt vmcnt(59)\n\t" \
   "ds_write_b128 v20, a[72:75] offset:32768\n\t" "s_mov_b32 s41, 144128\n\t" "buffer_load_dwordx4 v[236:239], v17, %[rb], s41 offen\n\t" \
   "s_waitcnt vmcnt(59)\n\t" "ds_write_b128 v20, a[76:79] offset:36864\n\t" "s_mov_b32 s41, 144384\n\t" \
   "buffer_load_dwordx4 v[252:255], v17, %[rb], s41 offen\n\t" "s_waitcnt lgkmcnt(0)\n\t" "s_barrier\n\t" \
   "ds_read_b64_tr_b16 a[40:41], v18 offset:0\n\t" "ds_read_b64_tr_b16 a[42:43], v18 offset:4096\n\t" "ds_read_b64_tr_b16 a[44:45], v19 offset:0\n\t" \
   "ds_read_b64_tr_b16 a[46:47], v19 offset:4096\n\t" "ds_read_b64_tr_b16 a[48:49], v18 offset:8192\n\t" \
   "ds_read_b64_tr_b16 a[50:51], v18 offset:12288\n\t" "ds_read_b64_tr_b16 a[52:53], v19 offset:8192\n\t" \
   "ds_read_b64_tr_b16 a[54:55], v19 offset:12288\n\t" "ds_read_b64_tr_b16 a[56:57], v18 offset:16384\n\t" \
   "ds_read_b64_tr_b16 a[58:59], v18 offset:20480\n\t" "ds_read_b64_tr_b16 a[60:61], v19 offset:16384\n\t" \
   "ds_read_b64_tr_b16 a[62:63], v19 offset:20480\n\t" "ds_read_b64_tr_b16 a[64:65], v18 offset:24576\n\t" \
   "ds_read_b64_tr_b16 a[66:67], v18 offset:28672\n\t" "ds_read_b64_tr_b16 a[68:69], v19 offset:24576\n\t" \
   "ds_read_b64_tr_b16 a[70:71], v19 offset:28672\n\t" "ds_read_b64_tr_b16 a[72:73], v18 offset:32768\n\t" \
   "ds_read_b64_tr_b16 a[74:75], v18 offset:36864\n\t" "ds_read_b64_tr_b16 a[76:77], v19 offset:32768\n\t" \
   "ds_read_b64_tr_b16 a[78:79], v19 offset:36864\n\t" "s_waitcnt vmcnt(59)\n\t" "s_waitcnt lgkmcnt(0)\n\t" "s_barrier\n\t" \
   "ds_write_b128 v20, a[80:83] offset:0\n\t" "s_waitcnt vmcnt(58)\n\t" "ds_write_b128 v20, a[84:87] offset:4096\n\t" "s_waitcnt vmcnt(57)\n\t" \
   "ds_write_b128 v20, a[88:91] offset:8192\n\t" "s_waitcnt vmcnt(56)\n\t" "ds_write_b128 v20, a[92:95] offset:12288\n\t" "s_waitcnt vmcnt(55)\n\t" \
   "ds_write_b128 v20, a[96:99] offset:16384\n\t" "s_waitcnt vmcnt(54)\n\t" "ds_write_b128 v20, a[100:103] offset:20480\n\t" "s_waitcnt vmcnt(53)\n\t" \
   "ds_write_b128 v20, a[104:107] offset:24576\n\t" "s_waitcnt vmcnt(52)\n\t" "ds_write_b128 v20, a[108:111] offset:28672\n\t" \
   "s_waitcnt vmcnt(51)\n\t" "ds_write_b128 v20, a[112:115] offset:32768\n\t" "s_waitcnt vmcnt(50)\n\t" \
   "ds_write_b128 v20, a[116:119] offset:36864\n\t" "s_waitcnt lgkmcnt(0)\n\t" "s_barrier\n\t" "ds_read_b64_tr_b16 a[80:81], v18 offset:0\n\t" \
   "ds_read_b64_tr_b16 a[82:83], v18 offset:4096\n\t" "ds_read_b64_tr_b16 a[84:85], v19 offset:0\n\t" \
   "ds_read_b64_tr_b16 a[86:87], v19 offset:4096\n\t" "ds_read_b64_tr_b16 a[88:89], v18 offset:8192\n\t" \
   "ds_read_b64_tr_b16 a[90:91], v18 offset:12288\n\t" "ds_read_b64_tr_b16 a[92:93], v19 offset:8192\n\t" \
   "ds_read_b64_tr_b16 a[94:95], v19 offset:12288\n\t" "ds_read_b64_tr_b16 a[96:97], v18 offset:16384\n\t" \
   "ds_read_b64_tr_b16 a[98:99], v18 offset:20480\n\t" "ds_read_b64_tr_b16 a[100:101], v19 offset:16384\n\t" \
   "ds_read_b64_tr_b16 a[102:103], v19 offset:20480\n\t" "ds_read_b64_tr_b16 a[104:105], v18 offset:24576\n\t" \
   "ds_read_b64_tr_b16 a[106:107], v18 offset:28672\n\t" "ds_read_b64_tr_b16 a[108:109], v19 offset:24576\n\t" \
   "ds_read_b64_tr_b16 a[110:111], v19 offset:28672\n\t" "ds_read_b64_tr_b16 a[112:113], v18 offset:32768\n\t" \
   "ds_read_b64_tr_b16 a[114:115], v18 offset:36864\n\t" "ds_read_b64_tr_b16 a[116:117], v19 offset:32768\n\t" \
   "ds_read_b64_tr_b16 a[118:119], v19 offset:36864\n\t" "s_waitcnt vmcnt(49)\n\t" "s_waitcnt lgkmcnt(0)\n\t" "s_barrier\n\t" \
   "ds_write_b128 v20, a[120:123] offset:0\n\t" "s_waitcnt vmcnt(48)\n\t" "ds_write_b128 v20, a[124:127] offset:4096\n\t" "s_waitcnt vmcnt(47)\n\t" \
   "ds_write_b128 v20, a[128:131] offset:8192\n\t" "s_waitcnt vmcnt(46)\n\t" "ds_write_b128 v20, a[132:135] offset:12288\n\t" "s_waitcnt vmcnt(45)\n\t" \
   "ds_write_b128 v20, a[136:139] offset:16384\n\t" "s_waitcnt vmcnt(44)\n\t" "ds_write_b128 v20, a[140:143] offset:20480\n\t" \
   "s_waitcnt vmcnt(43)\n\t" "ds_write_b128 v20, a[144:147] offset:24576\n\t" "s_waitcnt vmcnt(42)\n\t" \
   "ds_write_b128 v20, a[148:151] offset:28672\n\t" "s_waitcnt vmcnt(41)\n\t" "ds_write_b128 v20, a[152:155] offset:32768\n\t" \
   "s_waitcnt vmcnt(40)\n\t" "ds_write_b128 v20, a[156:159] offset:36864\n\t" "s_waitcnt lgkmcnt(0)\n\t" "s_barrier\n\t" \
   "ds_read_b64_tr_b16 a[120:121], v18 offset:0\n\t" "ds_read_b64_tr_b16 a[122:123], v18 offset:4096\n\t" \
   "ds_read_b64_tr_b16 a[124:125], v19 offset:0\n\t" "ds_read_b64_tr_b16 a[126:127], v19 offset:4096\n\t" \
   "ds_read_b64_tr_b16 a[128:129], v18 offset:8192\n\t" "ds_read_b64_tr_b16 a[130:131], v18 offset:12288\n\t" \
   "ds_read_b64_tr_b16 a[132:133], v19 offset:8192\n\t" "ds_read_b64_tr_b16 a[134:135], v19 offset:12288\n\t" \
   "ds_read_b64_tr_b16 a[136:137], v18 offset:16384\n\t" "ds_read_b64_tr_b16 a[138:139], v18 offset:20480\n\t" \
   "ds_read_b64_tr_b16 a[140:141], v19 offset:16384\n\t" "ds_read_b64_tr_b16 a[142:143], v19 offset:20480\n\t" \
   "ds_read_b64_tr_b16 a[144:145], v18 offset:24576\n\t" "ds_read_b64_tr_b16 a[146:147], v18 offset:28672\n\t" \
   "ds_read_b64_tr_b16 a[148:149], v19 offset:24576\n\t" "ds_read_b64_tr_b16 a[150:151], v19 offset:28672\n\t" \
   "ds_read_b64_tr_b16 a[152:153], v18 offset:32768\n\t" "ds_read_b64_tr_b16 a[154:155], v18 offset:36864\n\t" \
   "ds_read_b64_tr_b16 a[156:157], v19 offset:32768\n\t" "ds_read_b64_tr_b16 a[158:159], v19 offset:36864\n\t" "s_waitcnt vmcnt(39)\n\t" \
   "s_waitcnt lgkmcnt(0)\n\t" "s_barrier\n\t" "ds_write_b32 v21, a160 offset:0\n\t" "ds_write_b32 v21, a161 offset:21120\n\t" \
   "ds_write_b32 v21, a162 offset:10560\n\t" "ds_write_b32 v21, a163 offset:31680\n\t" "s_waitcnt vmcnt(38)\n\t" "ds_write_b32 v21, a176 offset:64\n\t" \
   "ds_write_b32 v21, a177 offset:21184\n\t" "ds_write_b32 v21, a178 offset:10624\n\t" "ds_write_b32 v21, a179 offset:31744\n\t" \
   "s_waitcnt vmcnt(37)\n\t" "ds_write_b32 v21, a192 offset:128\n\t" "ds_write_b32 v21, a193 offset:21248\n\t" \
   "ds_write_b32 v21, a194 offset:10688\n\t" "ds_write_b32 v21, a195 offset:31808\n\t" "s_waitcnt vmcnt(36)\n\t" \
   "ds_write_b32 v21, a208 offset:192\n\t" "ds_write_b32 v21, a209 offset:21312\n\t" "ds_write_b32 v21, a210 offset:10752\n\t" \
   "ds_write_b32 v21, a211 offset:31872\n\t" "s_waitcnt vmcnt(35)\n\t" "ds_write_b32 v21, a224 offset:256\n\t" \
   "ds_write_b32 v21, a225 offset:21376\n\t" "ds_write_b32 v21, a226 offset:10816\n\t" "ds_write_b32 v21, a227 offset:31936\n\t" \
   "s_waitcnt vmcnt(34)\n\t" "ds_write_b32 v21, a240 offset:5248\n\t" "ds_write_b32 v21, a241 offset:26368\n\t" \
   "ds_write_b32 v21, a242 offset:15808\n\t" "ds_write_b32 v21, a243 offset:36928\n\t" "s_waitcnt vmcnt(33)\n\t" \
   "ds_write_b32 v21, v192 offset:5312\n\t" "ds_write_b32 v21, v193 offset:26432\n\t" "ds_write_b32 v21, v194 offset:15872\n\t" \
   "ds_write_b32 v21, v195 offset:36992\n\t" "s_waitcnt vmcnt(32)\n\t" "ds_write_b32 v21, v208 offset:5376\n\t" \
   "ds_write_b32 v21, v209 offset:26496\n\t" "ds_write_b32 v21, v210 offset:15936\n\t" "ds_write_b32 v21, v211 offset:37056\n\t" \
   "s_waitcnt vmcnt(31)\n\t" "ds_write_b32 v21, v224 offset:5440\n\t" "ds_write_b32 v21, v225 offset:26560\n\t" \
   "ds_write_b32 v21, v226 offset:16000\n\t" "ds_write_b32 v21, v227 offset:37120\n\t" "s_waitcnt vmcnt(30)\n\t" \
   "ds_write_b32 v21, v240 offset:5504\n\t" "ds_write_b32 v21, v241 offset:26624\n\t" "ds_write_b32 v21, v242 offset:16064\n\t" \
   "ds_write_b32 v21, v243 offset:37184\n\t" "s_waitcnt lgkmcnt(0)\n\t" "s_barrier\n\t" "ds_read_b64_tr_b16 a[160:161], v22 offset:0\n\t" \
   "ds_read_b64_tr_b16 a[162:163], v22 offset:5248\n\t" "ds_read_b64_tr_b16 a[176:177], v22 offset:8\n\t" \
   "ds_read_b64_tr_b16 a[178:179], v22 offset:5256\n\t" "ds_read_b64_tr_b16 a[192:193], v22 offset:16\n\t" \
   "ds_read_b64_tr_b16 a[194:195], v22 offset:5264\n\t" "ds_read_b64_tr_b16 a[208:209], v22 offset:24\n\t" \
   "ds_read_b64_tr_b16 a[210:211], v22 offset:5272\n\t" "ds_read_b64_tr_b16 a[224:225], v22 offset:32\n\t" \
   "ds_read_b64_tr_b16 a[226:227], v22 offset:5280\n\t" "ds_read_b64_tr_b16 a[240:241], v22 offset:40\n\t" \
   "ds_read_b64_tr_b16 a[242:243], v22 offset:5288\n\t" "ds_read_b64_tr_b16 v[192:193], v22 offset:48\n\t" \
   "ds_read_b64_tr_b16 v[194:195], v22 offset:5296\n\t" "ds_read_b64_tr_b16 v[208:209], v22 offset:56\n\t" \
   "ds_read_b64_tr_b16 v[210:211], v22 offset:5304\n\t" "ds_read_b64_tr_b16 v[224:225], v22 offset:64\n\t" \
   "ds_read_b64_tr_b16 v[226:227], v22 offset:5312\n\t" "ds_read_b64_tr_b16 v[240:241], v22 offset:72\n\t" \
   "ds_read_b64_tr_b16 v[242:243], v22 offset:5320\n\t" "s_waitcnt vmcnt(29)\n\t" "s_waitcnt lgkmcnt(0)\n\t" "s_barrier\n\t" \
   "ds_write_b32 v21, a164 offset:0\n\t" "ds_write_b32 v21, a165 offset:21120\n\t" "ds_write_b32 v21, a166 offset:10560\n\t" \
   "ds_write_b32 v21, a167 offset:31680\n\t" "s_waitcnt vmcnt(28)\n\t" "ds_write_b32 v21, a180 offset:64\n\t" "ds_write_b32 v21, a181 offset:21184\n\t" \
   "ds_write_b32 v21, a182 offset:10624\n\t" "ds_write_b32 v21, a183 offset:31744\n\t" "s_waitcnt vmcnt(27)\n\t" \
   "ds_write_b32 v21, a196 offset:128\n\t" "ds_write_b32 v21, a197 offset:21248\n\t" "ds_write_b32 v21, a198 offset:10688\n\t" \
   "ds_write_b32 v21, a199 offset:31808\n\t" "s_waitcnt vmcnt(26)\n\t" "ds_write_b32 v21, a212 offset:192\n\t" \
   "ds_write_b32 v21, a213 offset:21312\n\t" "ds_write_b32 v21, a214 offset:10752\n\t" "ds_write_b32 v21, a215 offset:31872\n\t" \
   "s_waitcnt vmcnt(25)\n\t" "ds_write_b32 v21, a228 offset:256\n\t" "ds_write_b32 v21, a229 offset:21376\n\t" \
   "ds_write_b32 v21, a230 offset:10816\n\t" "ds_write_b32 v21, a231 offset:31936\n\t" "s_waitcnt vmcnt(24)\n\t" \
   "ds_write_b32 v21, a244 offset:5248\n\t" "ds_write_b32 v21, a245 offset:26368\n\t" "ds_write_b32 v21, a246 offset:15808\n\t" \
   "ds_write_b32 v21, a247 offset:36928\n\t" "s_waitcnt vmcnt(23)\n\t" "ds_write_b32 v21, v196 offset:5312\n\t" \
   "ds_write_b32 v21, v197 offset:26432\n\t" "ds_write_b32 v21, v198 offset:15872\n\t" "ds_write_b32 v21, v199 offset:36992\n\t" \
   "s_waitcnt vmcnt(22)\n\t" "ds_write_b32 v21, v212 offset:5376\n\t" "ds_write_b32 v21, v213 offset:26496\n\t" \
   "ds_write_b32 v21, v214 offset:15936\n\t" "ds_write_b32 v21, v215 offset:37056\n\t" "s_waitcnt vmcnt(21)\n\t" \
   "ds_write_b32 v21, v228 offset:5440\n\t" "ds_write_b32 v21, v229 offset:26560\n\t" "ds_write_b32 v21, v230 offset:16000\n\t" \
   "ds_write_b32 v21, v231 offset:37120\n\t" "s_waitcnt vmcnt(20)\n\t" "ds_write_b32 v21, v244 offset:5504\n\t" \
   "ds_write_b32 v21, v245 offset:26624\n\t" "ds_write_b32 v21, v246 offset:16064\n\t" "ds_write_b32 v21, v247 offset:37184\n\t" \
   "s_waitcnt lgkmcnt(0)\n\t" "s_barrier\n\t" "ds_read_b64_tr_b16 a[164:165], v22 offset:0\n\t" "ds_read_b64_tr_b16 a[166:167], v22 offset:5248\n\t" \
   "ds_read_b64_tr_b16 a[180:181], v22 offset:8\n\t" "ds_read_b64_tr_b16 a[182:183], v22 offset:5256\n\t" \
   "ds_read_b64_tr_b16 a[196:197], v22 offset:16\n\t" "ds_read_b64_tr_b16 a[198:199], v22 offset:5264\n\t" \
   "ds_read_b64_tr_b16 a[212:213], v22 offset:24\n\t" "ds_read_b64_tr_b16 a[214:215], v22 offset:5272\n\t" \
   "ds_read_b64_tr_b16 a[228:229], v22 offset:32\n\t" "ds_read_b64_tr_b16 a[230:231], v22 offset:5280\n\t" \
   "ds_read_b64_tr_b16 a[244:245], v22 offset:40\n\t" "ds_read_b64_tr_b16 a[246:247], v22 offset:5288\n\t" \
   "ds_read_b64_tr_b16 v[196:197], v22 offset:48\n\t" "ds_read_b64_tr_b16 v[198:199], v22 offset:5296\n\t" \
   "ds_read_b64_tr_b16 v[212:213], v22 offset:56\n\t" "ds_read_b64_tr_b16 v[214:215], v22 offset:5304\n\t" \
   "ds_read_b64_tr_b16 v[228:229], v22 offset:64\n\t" "ds_read_b64_tr_b16 v[230:231], v22 offset:5312\n\t" \
   "ds_read_b64_tr_b16 v[244:245], v22 offset:72\n\t" "ds_read_b64_tr_b16 v[246:247], v22 offset:5320\n\t" "s_waitcnt vmcnt(19)\n\t" \
   "s_waitcnt lgkmcnt(0)\n\t" "s_barrier\n\t" "ds_write_b32 v21, a168 offset:0\n\t" "ds_write_b32 v21, a169 offset:21120\n\t" \
   "ds_write_b32 v21, a170 offset:10560\n\t" "ds_write_b32 v21, a171 offset:31680\n\t" "s_waitcnt vmcnt(18)\n\t" "ds_write_b32 v21, a184 offset:64\n\t" \
   "ds_write_b32 v21, a185 offset:21184\n\t" "ds_write_b32 v21, a186 offset:10624\n\t" "ds_write_b32 v21, a187 offset:31744\n\t" \
   "s_waitcnt vmcnt(17)\n\t" "ds_write_b32 v21, a200 offset:128\n\t" "ds_write_b32 v21, a201 offset:21248\n\t" \
   "ds_write_b32 v21, a202 offset:10688\n\t" "ds_write_b32 v21, a203 offset:31808\n\t" "s_waitcnt vmcnt(16)\n\t" \
   "ds_write_b32 v21, a216 offset:192\n\t" "ds_write_b32 v21, a217 offset:21312\n\t" "ds_write_b32 v21, a218 offset:10752\n\t" \
   "ds_write_b32 v21, a219 offset:31872\n\t" "s_waitcnt vmcnt(15)\n\t" "ds_write_b32 v21, a232 offset:256\n\t" \
   "ds_write_b32 v21, a233 offset:21376\n\t" "ds_write_b32 v21, a234 offset:10816\n\t" "ds_write_b32 v21, a235 offset:31936\n\t" \
   "s_waitcnt vmcnt(14)\n\t" "ds_write_b32 v21, a248 offset:5248\n\t" "ds_write_b32 v21, a249 offset:26368\n\t" \
   "ds_write_b32 v21, a250 offset:15808\n\t" "ds_write_b32 v21, a251 offset:36928\n\t" "s_waitcnt vmcnt(13)\n\t" \
   "ds_write_b32 v21, v200 offset:5312\n\t" "ds_write_b32 v21, v201 offset:26432\n\t" "ds_write_b32 v21, v202 offset:15872\n\t" \
   "ds_write_b32 v21, v203 offset:36992\n\t" "s_waitcnt vmcnt(12)\n\t" "ds_write_b32 v21, v216 offset:5376\n\t" \
   "ds_write_b32 v21, v217 offset:26496\n\t" "ds_write_b32 v21, v218 offset:15936\n\t" "ds_write_b32 v21, v219 offset:37056\n\t" \
   "s_waitcnt vmcnt(11)\n\t" "ds_write_b32 v21, v232 offset:5440\n\t" "ds_write_b32 v21, v233 offset:26560\n\t" \
   "ds_write_b32 v21, v234 offset:16000\n\t" "ds_write_b32 v21, v235 offset:37120\n\t" "s_waitcnt vmcnt(10)\n\t" \
   "ds_write_b32 v21, v248 offset:5504\n\t" "ds_write_b32 v21, v249 offset:26624\n\t" "ds_write_b32 v21, v250 offset:16064\n\t" \
   "ds_write_b32 v21, v251 offset:37184\n\t" "s_waitcnt lgkmcnt(0)\n\t" "s_barrier\n\t" "ds_read_b64_tr_b16 a[168:169], v22 offset:0\n\t" \
   "ds_read_b64_tr_b16 a[170:171], v22 offset:5248\n\t" "ds_read_b64_tr_b16 a[184:185], v22 offset:8\n\t" \
   "ds_read_b64_tr_b16 a[186:187], v22 offset:5256\n\t" "ds_read_b64_tr_b16 a[200:201], v22 offset:16\n\t" \
   "ds_read_b64_tr_b16 a[202:203], v22 offset:5264\n\t" "ds_read_b64_tr_b16 a[216:217], v22 offset:24\n\t" \
   "ds_read_b64_tr_b16 a[218:219], v22 offset:5272\n\t" "ds_read_b64_tr_b16 a[232:233], v22 offset:32\n\t" \
   "ds_read_b64_tr_b16 a[234:235], v22 offset:5280\n\t" "ds_read_b64_tr_b16 a[248:249], v22 offset:40\n\t" \
   "ds_read_b64_tr_b16 a[250:251], v22 offset:5288\n\t" "ds_read_b64_tr_b16 v[200:201], v22 offset:48\n\t" \
   "ds_read_b64_tr_b16 v[202:203], v22 offset:5296\n\t" "ds_read_b64_tr_b16 v[216:217], v22 offset:56\n\t" \
   "ds_read_b64_tr_b16 v[218:219], v22 offset:5304\n\t" "ds_read_b64_tr_b16 v[232:233], v22 offset:64\n\t" \
   "ds_read_b64_tr_b16 v[234:235], v22 offset:5312\n\t" "ds_read_b64_tr_b16 v[248:249], v22 offset:72\n\t" \
   "ds_read_b64_tr_b16 v[250:251], v22 offset:5320\n\t" "s_waitcnt vmcnt(9)\n\t" "s_waitcnt lgkmcnt(0)\n\t" "s_barrier\n\t" \
   "ds_write_b32 v21, a172 offset:0\n\t" "ds_write_b32 v21, a173 offset:21120\n\t" "ds_write_b32 v21, a174 offset:10560\n\t" \
   "ds_write_b32 v21, a175 offset:31680\n\t" "s_waitcnt vmcnt(8)\n\t" "ds_write_b32 v21, a188 offset:64\n\t" "ds_write_b32 v21, a189 offset:21184\n\t" \
   "ds_write_b32 v21, a190 offset:10624\n\t" "ds_write_b32 v21, a191 offset:31744\n\t" "s_waitcnt vmcnt(7)\n\t" "ds_write_b32 v21, a204 offset:128\n\t" \
   "ds_write_b32 v21, a205 offset:21248\n\t" "ds_write_b32 v21, a206 offset:10688\n\t" "ds_write_b32 v21, a207 offset:31808\n\t" \
   "s_waitcnt vmcnt(6)\n\t" "ds_write_b32 v21, a220 offset:192\n\t" "ds_write_b32 v21, a221 offset:21312\n\t" "ds_write_b32 v21, a222 offset:10752\n\t" \
   "ds_write_b32 v21, a223 offset:31872\n\t" "s_waitcnt vmcnt(5)\n\t" "ds_write_b32 v21, a236 offset:256\n\t" "ds_write_b32 v21, a237 offset:21376\n\t" \
   "ds_write_b32 v21, a238 offset:10816\n\t" "ds_write_b32 v21, a239 offset:31936\n\t" "s_waitcnt vmcnt(4)\n\t" \
   "ds_write_b32 v21, a252 offset:5248\n\t" "ds_write_b32 v21, a253 offset:26368\n\t" "ds_write_b32 v21, a254 offset:15808\n\t" \
   "ds_write_b32 v21, a255 offset:36928\n\t" "s_waitcnt vmcnt(3)\n\t" "ds_write_b32 v21, v204 offset:5312\n\t" \
   "ds_write_b32 v21, v205 offset:26432\n\t" "ds_write_b32 v21, v206 offset:15872\n\t" "ds_write_b32 v21, v207 offset:36992\n\t" \
   "s_waitcnt vmcnt(2)\n\t" "ds_write_b32 v21, v220 offset:5376\n\t" "ds_write_b32 v21, v221 offset:26496\n\t" \
   "ds_write_b32 v21, v222 offset:15936\n\t" "ds_write_b32 v21, v223 offset:37056\n\t" "s_waitcnt vmcnt(1)\n\t" \
   "ds_write_b32 v21, v236 offset:5440\n\t" "ds_write_b32 v21, v237 offset:26560\n\t" "ds_write_b32 v21, v238 offset:16000\n\t" \
   "ds_write_b32 v21, v239 offset:37120\n\t" "s_waitcnt vmcnt(0)\n\t" "ds_write_b32 v21, v252 offset:5504\n\t" \
   "ds_write_b32 v21, v253 offset:26624\n\t" "ds_write_b32 v21, v254 offset:16064\n\t" "ds_write_b32 v21, v255 offset:37184\n\t" \
   "s_waitcnt lgkmcnt(0)\n\t" "s_barrier\n\t" "ds_read_b64_tr_b16 a[172:173], v22 offset:0\n\t" "ds_read_b64_tr_b16 a[174:175], v22 offset:5248\n\t" \
   "ds_read_b64_tr_b16 a[188:189], v22 offset:8\n\t" "ds_read_b64_tr_b16 a[190:191], v22 offset:5256\n\t" \
   "ds_read_b64_tr_b16 a[204:205], v22 offset:16\n\t" "ds_read_b64_tr_b16 a[206:207], v22 offset:5264\n\t" \
   "ds_read_b64_tr_b16 a[220:221], v22 offset:24\n\t" "ds_read_b64_tr_b16 a[222:223], v22 offset:5272\n\t" \
   "ds_read_b64_tr_b16 a[236:237], v22 offset:32\n\t" "ds_read_b64_tr_b16 a[238:239], v22 offset:5280\n\t" \
   "ds_read_b64_tr_b16 a[252:253], v22 offset:40\n\t" "ds_read_b64_tr_b16 a[254:255], v22 offset:5288\n\t" \
   "ds_read_b64_tr_b16 v[204:205], v22 offset:48\n\t" "ds_read_b64_tr_b16 v[206:207], v22 offset:5296\n\t" \
   "ds_read_b64_tr_b16 v[220:221], v22 offset:56\n\t" "ds_read_b64_tr_b16 v[222:223], v22 offset:5304\n\t" \
   "ds_read_b64_tr_b16 v[236:237], v22 offset:64\n\t" "ds_read_b64_tr_b16 v[238:239], v22 offset:5312\n\t" \
   "ds_read_b64_tr_b16 v[252:253], v22 offset:72\n\t" "ds_read_b64_tr_b16 v[254:255], v22 offset:5320\n\t" "s_waitcnt lgkmcnt(0)"
#define LDV_CLOB "v24", "v25", "s40", "s41"
#define LDY_ASM \
   "s_mov_b32 s41, 0\n\t" "buffer_load_dwordx4 a[0:3], v16, %[ra], s41 offen\n\t" "s_mov_b32 s41, 131072\n\t" \
   "buffer_load_dwordx4 a[64:67], v16, %[ra], s41 offen\n\t" "s_mov_b32 s41, 524288\n\t" "buffer_load_dwordx4 a[8:11], v16, %[ra], s41 offen\n\t" \
   "s_mov_b32 s41, 655360\n\t" "buffer_load_dwordx4 a[72:75], v16, %[ra], s41 offen\n\t" "s_mov_b32 s41, 1048576\n\t" \
   "buffer_load_dwordx4 a[16:19], v16, %[ra], s41 offen\n\t" "s_mov_b32 s41, 1179648\n\t" "buffer_load_dwordx4 a[80:83], v16, %[ra], s41 offen\n\t" \
   "s_mov_b32 s41, 1572864\n\t" "buffer_load_dwordx4 a[24:27], v16, %[ra], s41 offen\n\t" "s_mov_b32 s41, 1703936\n\t" \
   "buffer_load_dwordx4 a[88:91], v16, %[ra], s41 offen\n\t" "s_mov_b32 s41, 2097152\n\t" "buffer_load_dwordx4 a[32:35], v16, %[ra], s41 offen\n\t" \
   "s_mov_b32 s41, 2228224\n\t" "buffer_load_dwordx4 a[96:99], v16, %[ra], s41 offen\n\t" "s_mov_b32 s41, 2621440\n\t" \
   "buffer_load_dwordx4 a[40:43], v16, %[ra], s41 offen\n\t" "s_mov_b32 s41, 2752512\n\t" "buffer_load_dwordx4 a[104:107], v16, %[ra], s41 offen\n\t" \
   "s_mov_b32 s41, 3145728\n\t" "buffer_load_dwordx4 a[48:51], v16, %[ra], s41 offen\n\t" "s_mov_b32 s41, 3276800\n\t" \
   "buffer_load_dwordx4 a[112:115], v16, %[ra], s41 offen\n\t" "s_mov_b32 s41, 3670016\n\t" "buffer_load_dwordx4 a[56:59], v16, %[ra], s41 offen\n\t" \
   "s_mov_b32 s41, 3801088\n\t" "buffer_load_dwordx4 a[120:123], v16, %[ra], s41 offen\n\t" "s_mov_b32 s41, 4194304\n\t" \
   "buffer_load_dwordx4 v[192:195], v16, %[ra], s41 offen\n\t" "s_mov_b32 s41, 4325376\n\t" "buffer_load_dwordx4 v[208:211], v16, %[ra], s41 offen\n\t" \
   "s_mov_b32 s41, 4718592\n\t" "buffer_load_dwordx4 v[200:203], v16, %[ra], s41 offen\n\t" "s_mov_b32 s41, 4849664\n\t" \
   "buffer_load_dwordx4 v[216:219], v16, %[ra], s41 offen\n\t" "s_mov_b32 s41, 262144\n\t" "buffer_load_dwordx4 a[4:7], v16, %[ra], s41 offen\n\t" \
   "s_mov_b32 s41, 393216\n\t" "buffer_load_dwordx4 a[68:71], v16, %[ra], s41 offen\n\t" "s_mov_b32 s41, 786432\n\t" \
   "buffer_load_dwordx4 a[12:15], v16, %[ra], s41 offen\n\t" "s_mov_b32 s41, 917504\n\t" "buffer_load_dwordx4 a[76:79], v16, %[ra], s41 offen\n\t" \
   "s_mov_b32 s41, 1310720\n\t" "buffer_load_dwordx4 a[20:23], v16, %[ra], s41 offen\n\t" "s_mov_b32 s41, 1441792\n\t" \
   "buffer_load_dwordx4 a[84:87], v16, %[ra], s41 offen\n\t" "s_mov_b32 s41, 1835008\n\t" "buffer_load_dwordx4 a[28:31], v16, %[ra], s41 offen\n\t" \
   "s_mov_b32 s41, 1966080\n\t" "buffer_load_dwordx4 a[92:95], v16, %[ra], s41 offen\n\t" "s_mov_b32 s41, 2359296\n\t" \
   "buffer_load_dwordx4 a[36:39], v16, %[ra], s41 offen\n\t" "s_mov_b32 s41, 2490368\n\t" "buffer_load_dwordx4 a[100:103], v16, %[ra], s41 offen\n\t" \
   "s_mov_b32 s41, 2883584\n\t" "buffer_load_dwordx4 a[44:47], v16, %[ra], s41 offen\n\t" "s_mov_b32 s41, 3014656\n\t" \
   "buffer_load_dwordx4 a[108:111], v16, %[ra], s41 offen\n\t" "s_mov_b32 s41, 3407872\n\t" "buffer_load_dwordx4 a[52:55], v16, %[ra], s41 offen\n\t" \
   "s_mov_b32 s41, 3538944\n\t" "buffer_load_dwordx4 a[116:119], v16, %[ra], s41 offen\n\t" "s_mov_b32 s41, 3932160\n\t" \
   "buffer_load_dwordx4 a[60:63], v16, %[ra], s41 offen\n\t" "s_mov_b32 s41, 4063232\n\t" "buffer_load_dwordx4 a[124:127], v16, %[ra], s41 offen\n\t" \
   "s_mov_b32 s41, 4456448\n\t" "buffer_load_dwordx4 v[196:199], v16, %[ra], s41 offen\n\t" "s_mov_b32 s41, 4587520\n\t" \
   "buffer_load_dwordx4 v[212:215], v16, %[ra], s41 offen\n\t" "s_mov_b32 s41, 4980736\n\t" "buffer_load_dwordx4 v[204:207], v16, %[ra], s41 offen\n\t" \
   "s_mov_b32 s41, 5111808\n\t" "buffer_load_dwordx4 v[220:223], v16, %[ra], s41 offen\n\t" "s_mov_b32 s41, 0\n\t" \
   "buffer_load_dwordx4 a[128:131], v17, %[rb], s41 offen\n\t" "s_mov_b32 s41, 256\n\t" "buffer_load_dwordx4 a[132:135], v17, %[rb], s41 offen\n\t" \
   "s_mov_b32 s41, 512\n\t" "buffer_load_dwordx4 a[160:163], v17, %[rb], s41 offen\n\t" "s_mov_b32 s41, 768\n\t" \
   "buffer_load_dwordx4 a[164:167], v17, %[rb], s41 offen\n\t" "s_mov_b32 s41, 1024\n\t" "buffer_load_dwordx4 a[192:195], v17, %[rb], s41 offen\n\t" \
   "s_mov_b32 s41, 20480\n\t" "buffer_load_dwordx4 a[196:199], v17, %[rb], s41 offen\n\t" "s_mov_b32 s41, 20736\n\t" \
   "buffer_load_dwordx4 a[224:227], v17, %[rb], s41 offen\n\t" "s_mov_b32 s41, 20992\n\t" "buffer_load_dwordx4 a[228:231], v17, %[rb], s41 offen\n\t" \
   "s_mov_b32 s41, 21248\n\t" "buffer_load_dwordx4 v[224:227], v17, %[rb], s41 offen\n\t" "s_mov_b32 s41, 21504\n\t" \
   "buffer_load_dwordx4 v[228:231], v17, %[rb], s41 offen\n\t" "s_mov_b32 s41, 40960\n\t" "buffer_load_dwordx4 a[136:139], v17, %[rb], s41 offen\n\t" \
   "s_mov_b32 s41, 41216\n\t" "buffer_load_dwordx4 a[140:143], v17, %[rb], s41 offen\n\t" "s_mov_b32 s41, 41472\n\t" \
   "buffer_load_dwordx4 a[168:171], v17, %[rb], s41 offen\n\t" "s_mov_b32 s41, 41728\n\t" "buffer_load_dwordx4 a[172:175], v17, %[rb], s41 offen\n\t" \
   "s_mov_b32 s41, 41984\n\t" "buffer_load_dwordx4 a[200:203], v17, %[rb], s41 offen\n\t" "s_mov_b32 s41, 61440\n\t" \
   "buffer_load_dwordx4 a[204:207], v17, %[rb], s41 offen\n\t" "s_mov_b32 s41, 61696\n\t" "buffer_load_dwordx4 a[232:235], v17, %[rb], s41 offen\n\t" \
   "s_mov_b32 s41, 61952\n\t" "buffer_load_dwordx4 a[236:239], v17, %[rb], s41 offen\n\t" "s_mov_b32 s41, 62208\n\t" \
   "buffer_load_dwordx4 v[232:235], v17, %[rb], s41 offen\n\t" "s_mov_b32 s41, 62464\n\t" "buffer_load_dwordx4 v[236:239], v17, %[rb], s41 offen\n\t" \
   "s_waitcnt vmcnt(59)\n\t" "ds_write_b128 v20, a[0:3] offset:0\n\t" "s_mov_b32 s41, 81920\n\t" \
   "buffer_load_dwordx4 a[144:147], v17, %[rb], s41 offen\n\t" "s_waitcnt vmcnt(59)\n\t" "ds_write_b128 v20, a[64:67] offset:4096\n\t" \
   "s_mov_b32 s41, 82176\n\t" "buffer_load_dwordx4 a[148:151], v17, %[rb], s41 offen\n\t" "s_waitcnt vmcnt(59)\n\t" \
   "ds_write_b128 v20, a[8:11] offset:8192\n\t" "s_mov_b32 s41, 82432\n\t" "buffer_load_dwordx4 a[176:179], v17, %[rb], s41 offen\n\t" \
   "s_waitcnt vmcnt(59)\n\t" "ds_write_b128 v20, a[72:75] offset:12288\n\t" "s_mov_b32 s41, 82688\n\t" \
   "buffer_load_dwordx4 a[180:183], v17, %[rb], s41 offen\n\t" "s_waitcnt vmcnt(59)\n\t" "ds_write_b128 v20, a[16:19] offset:16384\n\t" \
   "s_mov_b32 s41, 82944\n\t" "buffer_load_dwordx4 a[208:211], v17, %[rb], s41 offen\n\t" "s_waitcnt vmcnt(59)\n\t" \
   "ds_write_b128 v20, a[80:83] offset:20480\n\t" "s_mov_b32 s41, 102400\n\t" "buffer_load_dwordx4 a[212:215], v17, %[rb], s41 offen\n\t" \
   "s_waitcnt vmcnt(59)\n\t" "ds_write_b128 v20, a[24:27] offset:24576\n\t" "s_mov_b32 s41, 102656\n\t" \
   "buffer_load_dwordx4 a[240:243], v17, %[rb], s41 offen\n\t" "s_waitcnt vmcnt(59)\n\t" "ds_write_b128 v20, a[88:91] offset:28672\n\t" \
   "s_mov_b32 s41, 102912\n\t" "buffer_load_dwordx4 a[244:247], v17, %[rb], s41 offen\n\t" "s_waitcnt vmcnt(59)\n\t" \
   "ds_write_b128 v20, a[32:35] offset:32768\n\t" "s_mov_b32 s41, 103168\n\t" "buffer_load_dwordx4 v[240:243], v17, %[rb], s41 offen\n\t" \
   "s_waitcnt vmcnt(59)\n\t" "ds_write_b128 v20, a[96:99] offset:36864\n\t" "s_mov_b32 s41, 103424\n\t" \
   "buffer_load_dwordx4 v[244:247], v17, %[rb], s41 offen\n\t" "s_waitcnt vmcnt(59)\n\t" "ds_write_b128 v20, a[40:43] offset:40960\n\t" \
   "s_mov_b32 s41, 122880\n\t" "buffer_load_dwordx4 a[152:155], v17, %[rb], s41 offen\n\t" "s_waitcnt vmcnt(59)\n\t" \
   "ds_write_b128 v20, a[104:107] offset:45056\n\t" "s_mov_b32 s41, 123136\n\t" "buffer_load_dwordx4 a[156:159], v17, %[rb], s41 offen\n\t" \
   "s_waitcnt vmcnt(59)\n\t" "ds_write_b128 v20, a[48:51] offset:49152\n\t" "s_mov_b32 s41, 123392\n\t" \
   "buffer_load_dwordx4 a[184:187], v17, %[rb], s41 offen\n\t" "s_waitcnt vmcnt(59)\n\t" "ds_write_b128 v20, a[112:115] offset:53248\n\t" \
   "s_mov_b32 s41, 123648\n\t" "buffer_load_dwordx4 a[188:191], v17, %[rb], s41 offen\n\t" "s_waitcnt vmcnt(59)\n\t" \
   "ds_write_b128 v20, a[56:59] offset:57344\n\t" "s_mov_b32 s41, 123904\n\t" "buffer_load_dwordx4 a[216:219], v17, %[rb], s41 offen\n\t" \
   "s_waitcnt vmcnt(59)\n\t" "ds_write_b128 v20, a[120:123] offset:61440\n\t" "s_mov_b32 s41, 143360\n\t" \
   "buffer_load_dwordx4 a[220:223], v17, %[rb], s41 offen\n\t" "s_waitcnt vmcnt(59)\n\t" "ds_write_b128 v23, v[192:195] offset:0\n\t" \
   "s_mov_b32 s41, 143616\n\t" "buffer_load_dwordx4 a[248:251], v17, %[rb], s41 offen\n\t" "s_waitcnt vmcnt(59)\n\t" \
   "ds_write_b128 v23, v[208:211] offset:4096\n\t" "s_mov_b32 s41, 143872\n\t" "buffer_load_dwordx4 a[252:255], v17, %[rb], s41 offen\n\t" \
   "s_waitcnt vmcnt(59)\n\t" "ds_write_b128 v23, v[200:203] offset:8192\n\t" "s_mov_b32 s41, 144128\n\t" \
   "buffer_load_dwordx4 v[248:251], v17, %[rb], s41 offen\n\t" "s_waitcnt vmcnt(59)\n\t" "ds_write_b128 v23, v[216:219] offset:12288\n\t" \
   "s_mov_b32 s41, 144384\n\t" "buffer_load_dwordx4 v[252:255], v17, %[rb], s41 offen\n\t" "s_waitcnt lgkmcnt(0)\n\t" "s_barrier\n\t" \
   "s_lshl_b32 s40, %[p0], 13\n\t" "v_add_u32 v24, s40, v18\n\t" "v_add_u32 v25, s40, v19\n\t" "s_add_u32 s40, s40, 8192\n\t" \
   "s_cmp_eq_u32 s40, 81920\n\t" "s_cselect_b32 s40, 0, s40\n\t" "ds_read_b64_tr_b16 a[0:1], v24 offset:0\n\t" \
   "ds_read_b64_tr_b16 a[2:3], v24 offset:4096\n\t" "ds_read_b64_tr_b16 a[64:65], v25 offset:0\n\t" "ds_read_b64_tr_b16 a[66:67], v25 offset:4096\n\t" \
   "v_add_u32 v24, s40, v18\n\t" "v_add_u32 v25, s40, v19\n\t" "s_add_u32 s40, s40, 8192\n\t" "s_cmp_eq_u32 s40, 81920\n\t" \
   "s_cselect_b32 s40, 0, s40\n\t" "ds_read_b64_tr_b16 a[8:9], v24 offset:0\n\t" "ds_read_b64_tr_b16 a[10:11], v24 offset:4096\n\t" \
   "ds_read_b64_tr_b16 a[72:73], v25 offset:0\n\t" "ds_read_b64_tr_b16 a[74:75], v25 offset:4096\n\t" "v_add_u32 v24, s40, v18\n\t" \
   "v_add_u32 v25, s40, v19\n\t" "s_add_u32 s40, s40, 8192\n\t" "s_cmp_eq_u32 s40, 81920\n\t" "s_cselect_b32 s40, 0, s40\n\t" \
   "ds_read_b64_tr_b16 a[16:17], v24 offset:0\n\t" "ds_read_b64_tr_b16 a[18:19], v24 offset:4096\n\t" "ds_read_b64_tr_b16 a[80:81], v25 offset:0\n\t" \
   "ds_read_b64_tr_b16 a[82:83], v25 offset:4096\n\t" "v_add_u32 v24, s40, v18\n\t" "v_add_u32 v25, s40, v19\n\t" "s_add_u32 s40, s40, 8192\n\t" \
   "s_cmp_eq_u32 s40, 81920\n\t" "s_cselect_b32 s40, 0, s40\n\t" "ds_read_b64_tr_b16 a[24:25], v24 offset:0\n\t" \
   "ds_read_b64_tr_b16 a[26:27], v24 offset:4096\n\t" "ds_read_b64_tr_b16 a[88:89], v25 offset:0\n\t" \
   "ds_read_b64_tr_b16 a[90:91], v25 offset:4096\n\t" "v_add_u32 v24, s40, v18\n\t" "v_add_u32 v25, s40, v19\n\t" "s_add_u32 s40, s40, 8192\n\t" \
   "s_cmp_eq_u32 s40, 81920\n\t" "s_cselect_b32 s40, 0, s40\n\t" "ds_read_b64_tr_b16 a[32:33], v24 offset:0\n\t" \
   "ds_read_b64_tr_b16 a[34:35], v24 offset:4096\n\t" "ds_read_b64_tr_b16 a[96:97], v25 offset:0\n\t" \
   "ds_read_b64_tr_b16 a[98:99], v25 offset:4096\n\t" "v_add_u32 v24, s40, v18\n\t" "v_add_u32 v25, s40, v19\n\t" "s_add_u32 s40, s40, 8192\n\t" \
   "s_cmp_eq_u32 s40, 81920\n\t" "s_cselect_b32 s40, 0, s40\n\t" "ds_read_b64_tr_b16 a[40:41], v24 offset:0\n\t" \
   "ds_read_b64_tr_b16 a[42:43], v24 offset:4096\n\t" "ds_read_b64_tr_b16 a[104:105], v25 offset:0\n\t" \
   "ds_read_b64_tr_b16 a[106:107], v25 offset:4096\n\t" "v_add_u32 v24, s40, v18\n\t" "v_add_u32 v25, s40, v19\n\t" "s_add_u32 s40, s40, 8192\n\t" \
   "s_cmp_eq_u32 s40, 81920\n\t" "s_cselect_b32 s40, 0, s40\n\t" "ds_read_b64_tr_b16 a[48:49], v24 offset:0\n\t" \
   "ds_read_b64_tr_b16 a[50:51], v24 offset:4096\n\t" "ds_read_b64_tr_b16 a[112:113], v25 offset:0\n\t" \
   "ds_read_b64_tr_b16 a[114:115], v25 offset:4096\n\t" "v_add_u32 v24, s40, v18\n\t" "v_add_u32 v25, s40, v19\n\t" "s_add_u32 s40, s40, 8192\n\t" \
   "s_cmp_eq_u32 s40, 81920\n\t" "s_cselect_b32 s40, 0, s40\n\t" "ds_read_b64_tr_b16 a[56:57], v24 offset:0\n\t" \
   "ds_read_b64_tr_b16 a[58:59], v24 offset:4096\n\t" "ds_read_b64_tr_b16 a[120:121], v25 offset:0\n\t" \
   "ds_read_b64_tr_b16 a[122:123], v25 offset:4096\n\t" "v_add_u32 v24, s40, v18\n\t" "v_add_u32 v25, s40, v19\n\t" "s_add_u32 s40, s40, 8192\n\t" \
   "s_cmp_eq_u32 s40, 81920\n\t" "s_cselect_b32 s40, 0, s40\n\t" "ds_read_b64_tr_b16 v[192:193], v24 offset:0\n\t" \
   "ds_read_b64_tr_b16 v[194:195], v24 offset:4096\n\t" "ds_read_b64_tr_b16 v[208:209], v25 offset:0\n\t" \
   "ds_read_b64_tr_b16 v[210:211], v25 offset:4096\n\t" "v_add_u32 v24, s40, v18\n\t" "v_add_u32 v25, s40, v19\n\t" "s_add_u32 s40, s40, 8192\n\t" \
   "s_cmp_eq_u32 s40, 81920\n\t" "s_cselect_b32 s40, 0, s40\n\t" "ds_read_b64_tr_b16 v[200:201], v24 offset:0\n\t" \
   "ds_read_b64_tr_b16 v[202:203], v24 offset:4096\n\t" "ds_read_b64_tr_b16 v[216:217], v25 offset:0\n\t" \
   "ds_read_b64_tr_b16 v[218:219], v25 offset:4096\n\t" "s_waitcnt vmcnt(59)\n\t" "s_waitcnt lgkmcnt(0)\n\t" "s_barrier\n\t" \
   "ds_write_b128 v20, a[4:7] offset:0\n\t" "s_waitcnt vmcnt(58)\n\t" "ds_write_b128 v20, a[68:71] offset:4096\n\t" "s_waitcnt vmcnt(57)\n\t" \
   "ds_write_b128 v20, a[12:15] offset:8192\n\t" "s_waitcnt vmcnt(56)\n\t" "ds_write_b128 v20, a[76:79] offset:12288\n\t" "s_waitcnt vmcnt(55)\n\t" \
   "ds_write_b128 v20, a[20:23] offset:16384\n\t" "s_waitcnt vmcnt(54)\n\t" "ds_write_b128 v20, a[84:87] offset:20480\n\t" "s_waitcnt vmcnt(53)\n\t" \
   "ds_write_b128 v20, a[28:31] offset:24576\n\t" "s_waitcnt vmcnt(52)\n\t" "ds_write_b128 v20, a[92:95] offset:28672\n\t" "s_waitcnt vmcnt(51)\n\t" \
   "ds_write_b128 v20, a[36:39] offset:32768\n\t" "s_waitcnt vmcnt(50)\n\t" "ds_write_b128 v20, a[100:103] offset:36864\n\t" "s_waitcnt vmcnt(49)\n\t" \
   "ds_write_b128 v20, a[44:47] offset:40960\n\t" "s_waitcnt vmcnt(48)\n\t" "ds_write_b128 v20, a[108:111] offset:45056\n\t" "s_waitcnt vmcnt(47)\n\t" \
   "ds_write_b128 v20, a[52:55] offset:49152\n\t" "s_waitcnt vmcnt(46)\n\t" "ds_write_b128 v20, a[116:119] offset:53248\n\t" "s_waitcnt vmcnt(45)\n\t" \
   "ds_write_b128 v20, a[60:63] offset:57344\n\t" "s_waitcnt vmcnt(44)\n\t" "ds_write_b128 v20, a[124:127] offset:61440\n\t" "s_waitcnt vmcnt(43)\n\t" \
   "ds_write_b128 v23, v[196:199] offset:0\n\t" "s_waitcnt vmcnt(42)\n\t" "ds_write_b128 v23, v[212:215] offset:4096\n\t" "s_waitcnt vmcnt(41)\n\t" \
   "ds_write_b128 v23, v[204:207] offset:8192\n\t" "s_waitcnt vmcnt(40)\n\t" "ds_write_b128 v23, v[220:223] offset:12288\n\t" \
   "s_waitcnt lgkmcnt(0)\n\t" "s_barrier\n\t" "s_lshl_b32 s40, %[p0], 13\n\t" "v_add_u32 v24, s40, v18\n\t" "v_add_u32 v25, s40, v19\n\t" \
   "s_add_u32 s40, s40, 8192\n\t" "s_cmp_eq_u32 s40, 81920\n\t" "s_cselect_b32 s40, 0, s40\n\t" "ds_read_b64_tr_b16 a[4:5], v24 offset:0\n\t" \
   "ds_read_b64_tr_b16 a[6:7], v24 offset:4096\n\t" "ds_read_b64_tr_b16 a[68:69], v25 offset:0\n\t" "ds_read_b64_tr_b16 a[70:71], v25 offset:4096\n\t" \
   "v_add_u32 v24, s40, v18\n\t" "v_add_u32 v25, s40, v19\n\t" "s_add_u32 s40, s40, 8192\n\t" "s_cmp_eq_u32 s40, 81920\n\t" \
   "s_cselect_b32 s40, 0, s40\n\t" "ds_read_b64_tr_b16 a[12:13], v24 offset:0\n\t" "ds_read_b64_tr_b16 a[14:15], v24 offset:4096\n\t" \
   "ds_read_b64_tr_b16 a[76:77], v25 offset:0\n\t" "ds_read_b64_tr_b16 a[78:79], v25 offset:4096\n\t" "v_add_u32 v24, s40, v18\n\t" \
   "v_add_u32 v25, s40, v19\n\t" "s_add_u32 s40, s40, 8192\n\t" "s_cmp_eq_u32 s40, 81920\n\t" "s_cselect_b32 s40, 0, s40\n\t" \
   "ds_read_b64_tr_b16 a[20:21], v24 offset:0\n\t" "ds_read_b64_tr_b16 a[22:23], v24 offset:4096\n\t" "ds_read_b64_tr_b16 a[84:85], v25 offset:0\n\t" \
   "ds_read_b64_tr_b16 a[86:87], v25 offset:4096\n\t" "v_add_u32 v24, s40, v18\n\t" "v_add_u32 v25, s40, v19\n\t" "s_add_u32 s40, s40, 8192\n\t" \
   "s_cmp_eq_u32 s40, 81920\n\t" "s_cselect_b32 s40, 0, s40\n\t" "ds_read_b64_tr_b16 a[28:29], v24 offset:0\n\t" \
   "ds_read_b64_tr_b16 a[30:31], v24 offset:4096\n\t" "ds_read_b64_tr_b16 a[92:93], v25 offset:0\n\t" \
   "ds_read_b64_tr_b16 a[94:95], v25 offset:4096\n\t" "v_add_u32 v24, s40, v18\n\t" "v_add_u32 v25, s40, v19\n\t" "s_add_u32 s40, s40, 8192\n\t" \
   "s_cmp_eq_u32 s40, 81920\n\t" "s_cselect_b32 s40, 0, s40\n\t" "ds_read_b64_tr_b16 a[36:37], v24 offset:0\n\t" \
   "ds_read_b64_tr_b16 a[38:39], v24 offset:4096\n\t" "ds_read_b64_tr_b16 a[100:101], v25 offset:0\n\t" \
   "ds_read_b64_tr_b16 a[102:103], v25 offset:4096\n\t" "v_add_u32 v24, s40, v18\n\t" "v_add_u32 v25, s40, v19\n\t" "s_add_u32 s40, s40, 8192\n\t" \
   "s_cmp_eq_u32 s40, 81920\n\t" "s_cselect_b32 s40, 0, s40\n\t" "ds_read_b64_tr_b16 a[44:45], v24 offset:0\n\t" \
   "ds_read_b64_tr_b16 a[46:47], v24 offset:4096\n\t" "ds_read_b64_tr_b16 a[108:109], v25 offset:0\n\t" \
   "ds_read_b64_tr_b16 a[110:111], v25 offset:4096\n\t" "v_add_u32 v24, s40, v18\n\t" "v_add_u32 v25, s40, v19\n\t" "s_add_u32 s40, s40, 8192\n\t" \
   "s_cmp_eq_u32 s40, 81920\n\t" "s_cselect_b32 s40, 0, s40\n\t" "ds_read_b64_tr_b16 a[52:53], v24 offset:0\n\t" \
   "ds_read_b64_tr_b16 a[54:55], v24 offset:4096\n\t" "ds_read_b64_tr_b16 a[116:117], v25 offset:0\n\t" \
   "ds_read_b64_tr_b16 a[118:119], v25 offset:4096\n\t" "v_add_u32 v24, s40, v18\n\t" "v_add_u32 v25, s40, v19\n\t" "s_add_u32 s40, s40, 8192\n\t" \
   "s_cmp_eq_u32 s40, 81920\n\t" "s_cselect_b32 s40, 0, s40\n\t" "ds_read_b64_tr_b16 a[60:61], v24 offset:0\n\t" \
   "ds_read_b64_tr_b16 a[62:63], v24 offset:4096\n\t" "ds_read_b64_tr_b16 a[124:125], v25 offset:0\n\t" \
   "ds_read_b64_tr_b16 a[126:127], v25 offset:4096\n\t" "v_add_u32 v24, s40, v18\n\t" "v_add_u32 v25, s40, v19\n\t" "s_add_u32 s40, s40, 8192\n\t" \
   "s_cmp_eq_u32 s40, 81920\n\t" "s_cselect_b32 s40, 0, s40\n\t" "ds_read_b64_tr_b16 v[196:197], v24 offset:0\n\t" \
   "ds_read_b64_tr_b16 v[198:199], v24 offset:4096\n\t" "ds_read_b64_tr_b16 v[212:213], v25 offset:0\n\t" \
   "ds_read_b64_tr_b16 v[214:215], v25 offset:4096\n\t" "v_add_u32 v24, s40, v18\n\t" "v_add_u32 v25, s40, v19\n\t" "s_add_u32 s40, s40, 8192\n\t" \
   "s_cmp_eq_u32 s40, 81920\n\t" "s_cselect_b32 s40, 0, s40\n\t" "ds_read_b64_tr_b16 v[204:205], v24 offset:0\n\t" \
   "ds_read_b64_tr_b16 v[206:207], v24 offset:4096\n\t" "ds_read_b64_tr_b16 v[220:221], v25 offset:0\n\t" \
   "ds_read_b64_tr_b16 v[222:223], v25 offset:4096\n\t" "s_waitcnt vmcnt(39)\n\t" "s_waitcnt lgkmcnt(0)\n\t" "s_barrier\n\t" \
   "ds_write_b32 v21, a128 offset:0\n\t" "ds_write_b32 v21, a129 offset:21120\n\t" "ds_write_b32 v21, a130 offset:10560\n\t" \
   "ds_write_b32 v21, a131 offset:31680\n\t" "s_waitcnt vmcnt(38)\n\t" "ds_write_b32 v21, a132 offset:64\n\t" "ds_write_b32 v21, a133 offset:21184\n\t" \
   "ds_write_b32 v21, a134 offset:10624\n\t" "ds_write_b32 v21, a135 offset:31744\n\t" "s_waitcnt vmcnt(37)\n\t" \
   "ds_write_b32 v21, a160 offset:128\n\t" "ds_write_b32 v21, a161 offset:21248\n\t" "ds_write_b32 v21, a162 offset:10688\n\t" \
   "ds_write_b32 v21, a163 offset:31808\n\t" "s_waitcnt vmcnt(36)\n\t" "ds_write_b32 v21, a164 offset:192\n\t" \
   "ds_write_b32 v21, a165 offset:21312\n\t" "ds_write_b32 v21, a166 offset:10752\n\t" "ds_write_b32 v21, a167 offset:31872\n\t" \
   "s_waitcnt vmcnt(35)\n\t" "ds_write_b32 v21, a192 offset:256\n\t" "ds_write_b32 v21, a193 offset:21376\n\t" \
   "ds_write_b32 v21, a194 offset:10816\n\t" "ds_write_b32 v21, a195 offset:31936\n\t" "s_waitcnt vmcnt(34)\n\t" \
   "ds_write_b32 v21, a196 offset:5248\n\t" "ds_write_b32 v21, a197 offset:26368\n\t" "ds_write_b32 v21, a198 offset:15808\n\t" \
   "ds_write_b32 v21, a199 offset:36928\n\t" "s_waitcnt vmcnt(33)\n\t" "ds_write_b32 v21, a224 offset:5312\n\t" \
   "ds_write_b32 v21, a225 offset:26432\n\t" "ds_write_b32 v21, a226 offset:15872\n\t" "ds_write_b32 v21, a227 offset:36992\n\t" \
   "s_waitcnt vmcnt(32)\n\t" "ds_write_b32 v21, a228 offset:5376\n\t" "ds_write_b32 v21, a229 offset:26496\n\t" \
   "ds_write_b32 v21, a230 offset:15936\n\t" "ds_write_b32 v21, a231 offset:37056\n\t" "s_waitcnt vmcnt(31)\n\t" \
   "ds_write_b32 v21, v224 offset:5440\n\t" "ds_write_b32 v21, v225 offset:26560\n\t" "ds_write_b32 v21, v226 offset:16000\n\t" \
   "ds_write_b32 v21, v227 offset:37120\n\t" "s_waitcnt vmcnt(30)\n\t" "ds_write_b32 v21, v228 offset:5504\n\t" \
   "ds_write_b32 v21, v229 offset:26624\n\t" "ds_write_b32 v21, v230 offset:16064\n\t" "ds_write_b32 v21, v231 offset:37184\n\t" \
   "s_waitcnt lgkmcnt(0)\n\t" "s_barrier\n\t" "ds_read_b64_tr_b16 a[128:129], v22 offset:0\n\t" "ds_read_b64_tr_b16 a[130:131], v22 offset:656\n\t" \
   "ds_read_b64_tr_b16 a[132:133], v22 offset:2624\n\t" "ds_read_b64_tr_b16 a[134:135], v22 offset:3280\n\t" \
   "ds_read_b64_tr_b16 a[160:161], v22 offset:16\n\t" "ds_read_b64_tr_b16 a[162:163], v22 offset:672\n\t" \
   "ds_read_b64_tr_b16 a[164:165], v22 offset:2640\n\t" "ds_read_b64_tr_b16 a[166:167], v22 offset:3296\n\t" \
   "ds_read_b64_tr_b16 a[192:193], v22 offset:32\n\t" "ds_read_b64_tr_b16 a[194:195], v22 offset:688\n\t" \
   "ds_read_b64_tr_b16 a[196:197], v22 offset:2656\n\t" "ds_read_b64_tr_b16 a[198:199], v22 offset:3312\n\t" \
   "ds_read_b64_tr_b16 a[224:225], v22 offset:48\n\t" "ds_read_b64_tr_b16 a[226:227], v22 offset:704\n\t" \
   "ds_read_b64_tr_b16 a[228:229], v22 offset:2672\n\t" "ds_read_b64_tr_b16 a[230:231], v22 offset:3328\n\t" \
   "ds_read_b64_tr_b16 v[224:225], v22 offset:64\n\t" "ds_read_b64_tr_b16 v[226:227], v22 offset:720\n\t" \
   "ds_read_b64_tr_b16 v[228:229], v22 offset:2688\n\t" "ds_read_b64_tr_b16 v[230:231], v22 offset:3344\n\t" "s_waitcnt vmcnt(29)\n\t" \
   "s_waitcnt lgkmcnt(0)\n\t" "s_barrier\n\t" "ds_write_b32 v21, a136 offset:0\n\t" "ds_write_b32 v21, a137 offset:21120\n\t" \
   "ds_write_b32 v21, a138 offset:10560\n\t" "ds_write_b32 v21, a139 offset:31680\n\t" "s_waitcnt vmcnt(28)\n\t" "ds_write_b32 v21, a140 offset:64\n\t" \
   "ds_write_b32 v21, a141 offset:21184\n\t" "ds_write_b32 v21, a142 offset:10624\n\t" "ds_write_b32 v21, a143 offset:31744\n\t" \
   "s_waitcnt vmcnt(27)\n\t" "ds_write_b32 v21, a168 offset:128\n\t" "ds_write_b32 v21, a169 offset:21248\n\t" \
   "ds_write_b32 v21, a170 offset:10688\n\t" "ds_write_b32 v21, a171 offset:31808\n\t" "s_waitcnt vmcnt(26)\n\t" \
   "ds_write_b32 v21, a172 offset:192\n\t" "ds_write_b32 v21, a173 offset:21312\n\t" "ds_write_b32 v21, a174 offset:10752\n\t" \
   "ds_write_b32 v21, a175 offset:31872\n\t" "s_waitcnt vmcnt(25)\n\t" "ds_write_b32 v21, a200 offset:256\n\t" \
   "ds_write_b32 v21, a201 offset:21376\n\t" "ds_write_b32 v21, a202 offset:10816\n\t" "ds_write_b32 v21, a203 offset:31936\n\t" \
   "s_waitcnt vmcnt(24)\n\t" "ds_write_b32 v21, a204 offset:5248\n\t" "ds_write_b32 v21, a205 offset:26368\n\t" \
   "ds_write_b32 v21, a206 offset:15808\n\t" "ds_write_b32 v21, a207 offset:36928\n\t" "s_waitcnt vmcnt(23)\n\t" \
   "ds_write_b32 v21, a232 offset:5312\n\t" "ds_write_b32 v21, a233 offset:26432\n\t" "ds_write_b32 v21, a234 offset:15872\n\t" \
   "ds_write_b32 v21, a235 offset:36992\n\t" "s_waitcnt vmcnt(22)\n\t" "ds_write_b32 v21, a236 offset:5376\n\t" \
   "ds_write_b32 v21, a237 offset:26496\n\t" "ds_write_b32 v21, a238 offset:15936\n\t" "ds_write_b32 v21, a239 offset:37056\n\t" \
   "s_waitcnt vmcnt(21)\n\t" "ds_write_b32 v21, v232 offset:5440\n\t" "ds_write_b32 v21, v233 offset:26560\n\t" \
   "ds_write_b32 v21, v234 offset:16000\n\t" "ds_write_b32 v21, v235 offset:37120\n\t" "s_waitcnt vmcnt(20)\n\t" \
   "ds_write_b32 v21, v236 offset:5504\n\t" "ds_write_b32 v21, v237 offset:26624\n\t" "ds_write_b32 v21, v238 offset:16064\n\t" \
   "ds_write_b32 v21, v239 offset:37184\n\t" "s_waitcnt lgkmcnt(0)\n\t" "s_barrier\n\t" "ds_read_b64_tr_b16 a[136:137], v22 offset:0\n\t" \
   "ds_read_b64_tr_b16 a[138:139], v22 offset:656\n\t" "ds_read_b64_tr_b16 a[140:141], v22 offset:2624\n\t" \
   "ds_read_b64_tr_b16 a[142:143], v22 offset:3280\n\t" "ds_read_b64_tr_b16 a[168:169], v22 offset:16\n\t" \
   "ds_read_b64_tr_b16 a[170:171], v22 offset:672\n\t" "ds_read_b64_tr_b16 a[172:173], v22 offset:2640\n\t" \
   "ds_read_b64_tr_b16 a[174:175], v22 offset:3296\n\t" "ds_read_b64_tr_b16 a[200:201], v22 offset:32\n\t" \
   "ds_read_b64_tr_b16 a[202:203], v22 offset:688\n\t" "ds_read_b64_tr_b16 a[204:205], v22 offset:2656\n\t" \
   "ds_read_b64_tr_b16 a[206:207], v22 offset:3312\n\t" "ds_read_b64_tr_b16 a[232:233], v22 offset:48\n\t" \
   "ds_read_b64_tr_b16 a[234:235], v22 offset:704\n\t" "ds_read_b64_tr_b16 a[236:237], v22 offset:2672\n\t" \
   "ds_read_b64_tr_b16 a[238:239], v22 offset:3328\n\t" "ds_read_b64_tr_b16 v[232:233], v22 offset:64\n\t" \
   "ds_read_b64_tr_b16 v[234:235], v22 offset:720\n\t" "ds_read_b64_tr_b16 v[236:237], v22 offset:2688\n\t" \
   "ds_read_b64_tr_b16 v[238:239], v22 offset:3344\n\t" "s_waitcnt vmcnt(19)\n\t" "s_waitcnt lgkmcnt(0)\n\t" "s_barrier\n\t" \
   "ds_write_b32 v21, a144 offset:0\n\t" "ds_write_b32 v21, a145 offset:21120\n\t" "ds_write_b32 v21, a146 offset:10560\n\t" \
   "ds_write_b32 v21, a147 offset:31680\n\t" "s_waitcnt vmcnt(18)\n\t" "ds_write_b32 v21, a148 offset:64\n\t" "ds_write_b32 v21, a149 offset:21184\n\t" \
   "ds_write_b32 v21, a150 offset:10624\n\t" "ds_write_b32 v21, a151 offset:31744\n\t" "s_waitcnt vmcnt(17)\n\t" \
   "ds_write_b32 v21, a176 offset:128\n\t" "ds_write_b32 v21, a177 offset:21248\n\t" "ds_write_b32 v21, a178 offset:10688\n\t" \
   "ds_write_b32 v21, a179 offset:31808\n\t" "s_waitcnt vmcnt(16)\n\t" "ds_write_b32 v21, a180 offset:192\n\t" \
   "ds_write_b32 v21, a181 offset:21312\n\t" "ds_write_b32 v21, a182 offset:10752\n\t" "ds_write_b32 v21, a183 offset:31872\n\t" \
   "s_waitcnt vmcnt(15)\n\t" "ds_write_b32 v21, a208 offset:256\n\t" "ds_write_b32 v21, a209 offset:21376\n\t" \
   "ds_write_b32 v21, a210 offset:10816\n\t" "ds_write_b32 v21, a211 offset:31936\n\t" "s_waitcnt vmcnt(14)\n\t" \
   "ds_write_b32 v21, a212 offset:5248\n\t" "ds_write_b32 v21, a213 offset:26368\n\t" "ds_write_b32 v21, a214 offset:15808\n\t" \
   "ds_write_b32 v21, a215 offset:36928\n\t" "s_waitcnt vmcnt(13)\n\t" "ds_write_b32 v21, a240 offset:5312\n\t" \
   "ds_write_b32 v21, a241 offset:26432\n\t" "ds_write_b32 v21, a242 offset:15872\n\t" "ds_write_b32 v21, a243 offset:36992\n\t" \
   "s_waitcnt vmcnt(12)\n\t" "ds_write_b32 v21, a244 offset:5376\n\t" "ds_write_b32 v21, a245 offset:26496\n\t" \
   "ds_write_b32 v21, a246 offset:15936\n\t" "ds_write_b32 v21, a247 offset:37056\n\t" "s_waitcnt vmcnt(11)\n\t" \
   "ds_write_b32 v21, v240 offset:5440\n\t" "ds_write_b32 v21, v241 offset:26560\n\t" "ds_write_b32 v21, v242 offset:16000\n\t" \
   "ds_write_b32 v21, v243 offset:37120\n\t" "s_waitcnt vmcnt(10)\n\t" "ds_write_b32 v21, v244 offset:5504\n\t" \
   "ds_write_b32 v21, v245 offset:26624\n\t" "ds_write_b32 v21, v246 offset:16064\n\t" "ds_write_b32 v21, v247 offset:37184\n\t" \
   "s_waitcnt lgkmcnt(0)\n\t" "s_barrier\n\t" "ds_read_b64_tr_b16 a[144:145], v22 offset:0\n\t" "ds_read_b64_tr_b16 a[146:147], v22 offset:656\n\t" \
   "ds_read_b64_tr_b16 a[148:149], v22 offset:2624\n\t" "ds_read_b64_tr_b16 a[150:151], v22 offset:3280\n\t" \
   "ds_read_b64_tr_b16 a[176:177], v22 offset:16\n\t" "ds_read_b64_tr_b16 a[178:179], v22 offset:672\n\t" \
   "ds_read_b64_tr_b16 a[180:181], v22 offset:2640\n\t" "ds_read_b64_tr_b16 a[182:183], v22 offset:3296\n\t" \
   "ds_read_b64_tr_b16 a[208:209], v22 offset:32\n\t" "ds_read_b64_tr_b16 a[210:211], v22 offset:688\n\t" \
   "ds_read_b64_tr_b16 a[212:213], v22 offset:2656\n\t" "ds_read_b64_tr_b16 a[214:215], v22 offset:3312\n\t" \
   "ds_read_b64_tr_b16 a[240:241], v22 offset:48\n\t" "ds_read_b64_tr_b16 a[242:243], v22 offset:704\n\t" \
   "ds_read_b64_tr_b16 a[244:245], v22 offset:2672\n\t" "ds_read_b64_tr_b16 a[246:247], v22 offset:3328\n\t" \
   "ds_read_b64_tr_b16 v[240:241], v22 offset:64\n\t" "ds_read_b64_tr_b16 v[242:243], v22 offset:720\n\t" \
   "ds_read_b64_tr_b16 v[244:245], v22 offset:2688\n\t" "ds_read_b64_tr_b16 v[246:247], v22 offset:3344\n\t" "s_waitcnt vmcnt(9)\n\t" \
   "s_waitcnt lgkmcnt(0)\n\t" "s_barrier\n\t" "ds_write_b32 v21, a152 offset:0\n\t" "ds_write_b32 v21, a153 offset:21120\n\t" \
   "ds_write_b32 v21, a154 offset:10560\n\t" "ds_write_b32 v21, a155 offset:31680\n\t" "s_waitcnt vmcnt(8)\n\t" "ds_write_b32 v21, a156 offset:64\n\t" \
   "ds_write_b32 v21, a157 offset:21184\n\t" "ds_write_b32 v21, a158 offset:10624\n\t" "ds_write_b32 v21, a159 offset:31744\n\t" \
   "s_waitcnt vmcnt(7)\n\t" "ds_write_b32 v21, a184 offset:128\n\t" "ds_write_b32 v21, a185 offset:21248\n\t" "ds_write_b32 v21, a186 offset:10688\n\t" \
   "ds_write_b32 v21, a187 offset:31808\n\t" "s_waitcnt vmcnt(6)\n\t" "ds_write_b32 v21, a188 offset:192\n\t" "ds_write_b32 v21, a189 offset:21312\n\t" \
   "ds_write_b32 v21, a190 offset:10752\n\t" "ds_write_b32 v21, a191 offset:31872\n\t" "s_waitcnt vmcnt(5)\n\t" "ds_write_b32 v21, a216 offset:256\n\t" \
   "ds_write_b32 v21, a217 offset:21376\n\t" "ds_write_b32 v21, a218 offset:10816\n\t" "ds_write_b32 v21, a219 offset:31936\n\t" \
   "s_waitcnt vmcnt(4)\n\t" "ds_write_b32 v21, a220 offset:5248\n\t" "ds_write_b32 v21, a221 offset:26368\n\t" \
   "ds_write_b32 v21, a222 offset:15808\n\t" "ds_write_b32 v21, a223 offset:36928\n\t" "s_waitcnt vmcnt(3)\n\t" \
   "ds_write_b32 v21, a248 offset:5312\n\t" "ds_write_b32 v21, a249 offset:26432\n\t" "ds_write_b32 v21, a250 offset:15872\n\t" \
   "ds_write_b32 v21, a251 offset:36992\n\t" "s_waitcnt vmcnt(2)\n\t" "ds_write_b32 v21, a252 offset:5376\n\t" \
   "ds_write_b32 v21, a253 offset:26496\n\t" "ds_write_b32 v21, a254 offset:15936\n\t" "ds_write_b32 v21, a255 offset:37056\n\t" \
   "s_waitcnt vmcnt(1)\n\t" "ds_write_b32 v21, v248 offset:5440\n\t" "ds_write_b32 v21, v249 offset:26560\n\t" \
   "ds_write_b32 v21, v250 offset:16000\n\t" "ds_write_b32 v21, v251 offset:37120\n\t" "s_waitcnt vmcnt(0)\n\t" \
   "ds_write_b32 v21, v252 offset:5504\n\t" "ds_write_b32 v21, v253 offset:26624\n\t" "ds_write_b32 v21, v254 offset:16064\n\t" \
   "ds_write_b32 v21, v255 offset:37184\n\t" "s_waitcnt lgkmcnt(0)\n\t" "s_barrier\n\t" "ds_read_b64_tr_b16 a[152:153], v22 offset:0\n\t" \
   "ds_read_b64_tr_b16 a[154:155], v22 offset:656\n\t" "ds_read_b64_tr_b16 a[156:157], v22 offset:2624\n\t" \
   "ds_read_b64_tr_b16 a[158:159], v22 offset:3280\n\t" "ds_read_b64_tr_b16 a[184:185], v22 offset:16\n\t" \
   "ds_read_b64_tr_b16 a[186:187], v22 offset:672\n\t" "ds_read_b64_tr_b16 a[188:189], v22 offset:2640\n\t" \
   "ds_read_b64_tr_b16 a[190:191], v22 offset:3296\n\t" "ds_read_b64_tr_b16 a[216:217], v22 offset:32\n\t" \
   "ds_read_b64_tr_b16 a[218:219], v22 offset:688\n\t" "ds_read_b64_tr_b16 a[220:221], v22 offset:2656\n\t" \
   "ds_read_b64_tr_b16 a[222:223], v22 offset:3312\n\t" "ds_read_b64_tr_b16 a[248:249], v22 offset:48\n\t" \
   "ds_read_b64_tr_b16 a[250:251], v22 offset:704\n\t" "ds_read_b64_tr_b16 a[252:253], v22 offset:2672\n\t" \
   "ds_read_b64_tr_b16 a[254:255], v22 offset:3328\n\t" "ds_read_b64_tr_b16 v[248:249], v22 offset:64\n\t" \
   "ds_read_b64_tr_b16 v[250:251], v22 offset:720\n\t" "ds_read_b64_tr_b16 v[252:253], v22 offset:2688\n\t" \
   "ds_read_b64_tr_b16 v[254:255], v22 offset:3344\n\t" "s_waitcnt lgkmcnt(0)"
#define LDY_CLOB "v24", "v25", "s40", "s41"
__device__ __forceinline__ void load_rows(v32u (&w)[10], int kind, const u16* A, const u16* B, int h, int wv, unsigned slot) {
  const int l = lane_id(), i = l & 15, g = l >> 4;
  const v4u ra = make_rs(A, 640u * 8192u), rb = make_rs(B, 640u * 8192u);
  v8u ad = (unsigned)l;
  const int wr = 4 * wv + g, rr = 4 * g + (i >> 2);
  ad[0] = wr * 8192 + h * 256 + i * 16;
  ad[1] = (h * 128 + wr) * 1280 + i * 16;
  ad[2] = slot + rr * 256 + ((2 * wv) ^ (rr & 7)) * 32 + (i & 3) * 8;
  ad[3] = ad[2] ^ 32u;
  ad[4] = slot + wr * 256 + ((i >> 1) ^ (wr & 7)) * 32 + (i & 1) * 16;
  ad[5] = slot + wr * 328 + ((i >> 2) * 2 + (i & 1)) * 8 + ((i >> 1) & 1) * 4;
  ad[6] = slot + (kind == 3 ? (16 * (g & 1) + 4 * (g >> 1) + ((i >> 2) & 1)) * 328 + (i >> 3) * 8 : rr * 328) + (i & 3) * 10560 + wv * 80;
  ad[7] = ad[4] + 65536u;
#define LD_OPS                                                                                                                            \
  : "={a[0:31]}"(w[0]), "={a[32:63]}"(w[1]), "={a[64:95]}"(w[2]), "={a[96:127]}"(w[3]), "={a[128:159]}"(w[4]), "={a[160:191]}"(w[5]),     \
    "={a[192:223]}"(w[6]), "={a[224:255]}"(w[7]), "={v[192:223]}"(w[8]), "={v[224:255]}"(w[9])                                            \
  : "{v[16:23]}"(ad), [ra] "s"(ra), [rb] "s"(rb), [p0] "s"(wv < 3 ? 3 * wv : 8)                                                          \
  : LDP_CLOB, "scc", "memory"
  if (kind == 0) asm volatile(LDP_ASM LD_OPS);
  else if (kind == 1) asm volatile(LDK_ASM LD_OPS);
  else if (kind == 2) asm volatile(LDV_ASM LD_OPS);
  else asm volatile(LDY_ASM LD_OPS);
}
#define W_OPS "{a[0:31]}"(w[0]), "{a[32:63]}"(w[1]), "{a[64:95]}"(w[2]), "{a[96:127]}"(w[3]), "{a[128:159]}"(w[4]), "{a[160:191]}"(w[5]), \
              "{a[192:223]}"(w[6]), "{a[224:255]}"(w[7]), "{v[192:223]}"(w[8]), "{v[224:255]}"(w[9])

__device__ __forceinline__ int mem_lane(int lane) { return lane * 16; }
__device__ __forceinline__ int pair_of(int g) { return (g & 1) * 2 + (g >> 1); }

__device__ __forceinline__ void rec_p1(int wv_, const v32u (&w)[10], unsigned char* ws, const Pool& pool, int layer, int h, int xs, RecTune tn, unsigned long long* stamps) {
  const Clk0 c0 = clk_sample();
  const unsigned lds0 = (unsigned)(uintptr_t)(__attribute__((address_space(3))) unsigned char*)dyn_lds;
  const int lane = lane_id(), wv = wv_, g = lane >> 4, r = lane & 15;
  const v4u rs = make_rs(ws, (unsigned)OFF_END);
  const int p0 = wv < 3 ? 3 * wv : 8;
  v16u ad, la = (unsigned)lane;
  const int kqo = (int)(wv < 2 ? OFF_KF : OFF_QF) + ((h * RT + (wv & 1)) * 4 * 64 + lane) * 16;
  const int progo = (wv == 0 && lane == 0) ? prog_off(layer, 0, h) : OOB;
  {
    const int p2 = wv < 3 ? p0 + 2 : 0;
    ad[0] = W_MEM + (p0 >> 1) * WIN + (p0 & 1) * 1024 + mem_lane(lane);
    ad[1] = W_MEM + ((p0 + 1) >> 1) * WIN + ((p0 + 1) & 1) * 1024 + mem_lane(lane);
    ad[2] = W_MEM + (p2 >> 1) * WIN + (p2 & 1) * 1024 + mem_lane(lane);
    ad[3] = lane == 0 ? W_MISC + h * 64 + wv * 4 : OOB;
    ad[4] = copy_off(wv) + (h >> 2) * WIN + (h & 3) * 512 + (g >> 1) * 256 + (2 * (r & 7) + (r >> 3)) * 16 + (g & 1) * 8;
    ad[5] = g < 2 ? copy_off(wv + 4 * (r >> 3)) + (8 + (h >> 4)) * WIN + ((h >> 3) & 1) * 1024 + (((h >> 1) & 3) * 16 + (h & 1) * 8 + (r & 7)) * 16 + g * 8 : OOB;
    ad[6] = (r & 1) ? 0xEEEEu : 0x4444u;
    ad[7] = lds0 + (32 * wv + r) * 16 + g * 4;
    ad[8] = lds0 + (4 * g + (r >> 2)) * 16 + (r & 1) * 8;
    ad[9] = lds0 + L_M2 + wv * 3072 + lane * 16;
    ad[10] = kqo;
    ad[11] = M_TM + xs * 128 + (lane & 1) * 12;
    ad[12] = kqf_off(layer, 0, h) + (lane & 3) * 4;
    ad[13] = wv > 1 ? (int)OFF_PXF + ((h * 2 + (wv & 1)) * 64 + lane) * 16 : OOB;
    ad[14] = (g < 2 && wv > 1) ? (int)OFF_PXM + (((h >> 2) * 2 + (wv & 1)) * 64 + (h & 3) * 16 + r) * 16 + g * 8 : OOB;
    ad[15] = progo;
#pragma unroll
    for (int i = 0; i < 10; ++i) { int p = p0 + i; p = p >= 10 ? p - 10 : p; la[i] = lds0 + p * 1024 + lane * 16; }
    la[10] = lds0 + L_KQ + lane * 16;
    la[12] = lds0 + REC_ST + wv * 8;
    asm volatile("v_mov_b32 %0, 0xceabfb8f" : "=v"(la[13]));
    la[14] = 16 * (wv & 1) + r - 4 * g;
    la[15] = lds0 + L_KQ + (wv & 1) * 4096 + lane * 16;
  }
  __builtin_amdgcn_s_waitcnt(0x0F70);
  if (layer) __builtin_amdgcn_s_sleep(64);
  v4u clk = clk_init(c0);
  unsigned long long stat;
  asm volatile(P1_ASM
               : "=&{s[76:77]}"(stat), "+{s[56:59]}"(clk)
               : W_OPS, "{v[128:143]}"(ad), "{v[112:127]}"(la), [rs] "s"(rs), [sel] "s"(pool.sel), [pool] "s"(pool.base), [xs] "s"(xs),
                 [lad0] "s"(ladder(xs, xs & 1)), [s3] "s"(pool.s3), [m1] "s"(tn.m1), [l2s] "s"(lds0 + L_M2 + wv * 3072),
                 [hi8] "s"(0xFF00FF00FF00FF00ull), [wv] "s"(wv), [kf0] "s"((int)OFF_KF + h * RT * 4096),
                 [esc] "s"((int)OFF_FWD - W_MEM)
               : P1_CLOB RST_CLOB, "vcc", "scc", "m0", "memory");
  LDSBAR();
  {
    const unsigned one = 1u;
    const int so = (NSEG - 1) * 4096;
    VM_ST32(one, progo, so);
    VM_WAIT(0);
  }
}

__device__ __forceinline__ void rec_pv(int wv_, const v32u (&w)[10], unsigned char* ws, const Pool& pool, int layer, int h, int xs, RecTune tn, unsigned long long* stamps) {
  const Clk0 c0 = clk_sample();
  const unsigned lds0 = (unsigned)(uintptr_t)(__attribute__((address_space(3))) unsigned char*)dyn_lds;
  const int lane = lane_id(), wv = wv_, g = lane >> 4, r = lane & 15;
  const v4u rs = make_rs(ws, (unsigned)OFF_END);
  const int p0 = wv < 3 ? 3 * wv : 8, p2 = wv < 3 ? p0 + 2 : 0;
  v16u ad = (unsigned)lane, la = (unsigned)lane;
  ad[0] = W_MEM + (p0 >> 1) * WIN + (p0 & 1) * 1024 + mem_lane(lane);
  ad[1] = W_MEM + ((p0 + 1) >> 1) * WIN + ((p0 + 1) & 1) * 1024 + mem_lane(lane);
  ad[2] = W_MEM + (p2 >> 1) * WIN + (p2 & 1) * 1024 + mem_lane(lane);
  ad[3] = (r & 8) ? 0xEEEEu : 0x4444u;
  ad[4] = (wv * 10 + (g >> 1)) * 16384 + h * 256 + r * 16 + (g & 1) * 8;
  ad[5] = h * 16384 + wv * 2048 + lane * 16;
  ad[6] = (r & 1) ? 0xEEEEu : 0x4444u;
  ad[7] = lds0 + (32 * wv + r) * 16 + pair_of(g) * 4;
  ad[8] = lds0 + (4 * g + (r >> 2)) * 16 + (r & 1) * 8;
  ad[11] = M_TM + (4 + xs) * 128 + (lane & 1) * 12;
  ad[15] = (wv == 0 && lane == 0) ? prog_off(layer, 0, 32 + h) : OOB;
#pragma unroll
  for (int i = 0; i < 10; ++i) { int p = p0 + i; p = p >= 10 ? p - 10 : p; la[i] = lds0 + p * 1024 + lane * 16; }
  la[12] = lds0 + REC_ST + wv * 8;
  __builtin_amdgcn_s_waitcnt(0x0F70);
  if (layer) __builtin_amdgcn_s_sleep(64);
  v4u clk = clk_init(c0);
  unsigned long long stat;
  asm volatile(PV_ASM
               : "=&{s[76:77]}"(stat), "+{s[56:59]}"(clk)
               : W_OPS, "{v[128:143]}"(ad), "{v[112:127]}"(la), [rs] "s"(rs), [sel] "s"(pool.sel), [pool] "s"(pool.base), [xs] "s"(xs),
                 [lad0] "s"(ladder(xs, xs & 1)), [s3] "s"(pool.s3), [m1c] "s"(tn.m1 + tn.c2), [esc] "s"((int)OFF_FWD - W_MEM),
                 [ysel] "s"(pool.ysel), [yb] "s"(pool.base + P_Y)
               : PV_CLOB RST_CLOB, "vcc", "scc", "memory");
}

#define TSTART 5000
#define TD1 900u
#define TR2 400u
#define TEMA 1
#define TEMB 2
#define TBUP 48
#define TBDN 6
#define TLATE 520
#define TSEC 50
__device__ __forceinline__ unsigned track_dur(unsigned e, int m, int rob, int s) {
  const int lo = (int)e - (s < 8 ? 64 : (rob & 255)), hi = (int)e + (rob >> 8);
  if (s >= 8) m = (int)e + ((m - (int)e + (m > (int)e ? (1 << TEMA) - 1 : 0)) >> (m > (int)e ? TEMA : TEMB));
  return (unsigned)min(max(m, lo), hi);
}
#define TTS 200
#define TTU 240
__device__ __forceinline__ void rec_sched(unsigned char* ws, const Pool& pool, int layer, int c16, int xs, RecTune tn, unsigned long long* stamps) {
  const Clk0 c0 = clk_sample();
  const unsigned lds0 = (unsigned)(uintptr_t)(__attribute__((address_space(3))) unsigned char*)dyn_lds;
  const int lane = lane_id();
  const v4u rs = make_rs(ws, (unsigned)OFF_END);
  unsigned long long stat = 0;
  const int sm = M_STAMP + min(lane, 39) * 64, sp = W_MISC + (lane & 31) * 64;
  const int yo = M_TM + xs * 128, oc = (c16 == 0 && lane < 16) ? M_TM + lane * 128 : OOB;
  const unsigned cv = flag_finish<2>(rs, kqf_off(layer, 0, 32 + (lane & 31)), 0xFFFFFFFFu);
  v4u clk = clk_init(c0);
  PH(3 + layer * 6)
  unsigned r2a = TR2, d1a = TD1, dd = TD1 + tn.m2, tp = 0, tm = 0, anchor = clk.x - dd;
  unsigned rr = (unsigned)min(max(wave_imax((int)(cv * 8u - clk.x)) + TSTART, 300), 4000);
  int bonus = 0;
  bool clean = false;
#pragma unroll 1
  for (int s = 0; s < NSEG - 1; ++s) {
    const int rgm = pool.region((s + NSEG - 1) & (NSEG - 1)), rg = pool.region(s);
    const unsigned slot = lds0 + L_T + (s & 1) * 32, ao = anchor;
    unsigned b2 = 1u;
    v4u b1 = {1u, 1u, 1u, 1u};
    if (s > 0) {
      asm volatile(WAIT_ASM : "+{s[56:59]}"(clk) : [ta] "s"(tm + TTS) : WAIT_CLOB, "scc", "memory");
      VM_LD32(b2, sm, rgm);
      VM_LD128(b1, sp, rgm, 0);
      asm volatile("s_waitcnt vmcnt(0)" : "+v"(b2), "+v"(b1)::"memory");
      anchor = tm;
      if (__builtin_expect(__any(b2 == 0xFFFFFFFFu || (int)(b2 - (tm + tn.m1 - tn.vis)) > 0), 0)) {
        STAT(3);
        for (unsigned spins = 0; __any(b2 == 0xFFFFFFFFu) && spins < SPIN_CAP; ++spins) {
          __builtin_amdgcn_s_sleep(2);
          VM_LD32(b2, sm, rgm);
          VM_WAIT1(0, b2);
        }
        const int d = wave_imax((int)(b2 - tm)) + tn.vis - tn.m1;
        anchor = tm + (d <= 0 ? 0 : (d <= tn.dlt ? tn.dlt + TSEC : d + TLATE));
      }
    }
    const unsigned tpo = tp, tmo = tm;
    tp = anchor + dd;
    tm = nosent(tp + rr);
    LDS_V4(slot) = v4u{tp, tm, (unsigned)(s + 1), anchor};
    if (anchor != tmo && s > 2) bonus = TBUP; else bonus = max(bonus - TBDN, 0);
    dd = d1a + tn.m2 + bonus;
    rr = r2a + bonus;
    {
      const v4u wq = v4u{tm, dd, rr, dd + rr};
      VM_ST128(wq, oc, rg);
    }
    if (s > 1 && !__any((b2 & 1u) != 0u)) r2a = track_dur(r2a, wave_imax((int)(b2 - tpo)), tn.rob, s);
    if (s > 1 && clean && !__any(((b1.x | b1.y | b1.z | b1.w) & 1u) != 0u))
      d1a = track_dur(d1a, wave_imax(max(max((int)(b1.x - ao), (int)(b1.y - ao)), max((int)(b1.z - ao), (int)(b1.w - ao)))), tn.rob, s);
    clean = anchor == tmo;
    while ((int)(tp + 40u - rtc() * 8u) > 0 && (int)(tp - rtc() * 8u) < 32000) __builtin_amdgcn_s_sleep(1);
    v4u lw;
    VM_LD128(lw, yo, rg, 0);
    VM_WAIT1(0, lw);
    const unsigned l0 = (unsigned)__builtin_amdgcn_readfirstlane(lw.x), l1 = (unsigned)__builtin_amdgcn_readfirstlane(lw.y),
                   l2 = (unsigned)__builtin_amdgcn_readfirstlane(lw.z);
    if (max(max(l0, l1), l2) != 0xFFFFFFFFu) { tm = l0; dd = l1; rr = l2; }
  }
}

__device__ __forceinline__ void rec_fin(unsigned char* ws, const Pool& pool, int layer, int c16, int xs, RecTune tn, unsigned long long* stamps) {
  const Clk0 c0 = clk_sample();
  const unsigned lds0 = (unsigned)(uintptr_t)(__attribute__((address_space(3))) unsigned char*)dyn_lds;
  const int lane = lane_id(), g = lane >> 4, r = lane & 15;
  const v4u rs = make_rs(ws, (unsigned)OFF_END);
  v8u ad;
  ad[0] = lane * 16;
  ad[1] = g * 256 + r * 8;
  ad[2] = (r & 1) ? 0x03020706u : 0x05040100u;
  ad[3] = (lane & 31) * 64;
  ad[4] = W_MEM + copy_off(((r >> 1) & 1) * 5) + (c16 >> 3) * WIN + (c16 & 7) * 256 + (2 * (2 * g + (r & 1)) + (r >> 3)) * 16 + ((r >> 2) & 1) * 8;
  ad[5] = lane == 0 ? M_STAMP + c16 * 64 : OOB;
  ad[6] = (r & 1) ? 0xEEEEu : 0x4444u;
  ad[7] = lds0 + L_T;
  __builtin_amdgcn_s_sleep(64);
  v4u clk = clk_init(c0);
  unsigned long long stat;
  asm volatile(FIN_ASM
               : "=&{s[76:77]}"(stat), "+{s[56:59]}"(clk), "+{v[16:23]}"(ad)
               : [rs] "s"(rs), [sel] "s"(pool.sel), [pool] "s"(pool.base), [xs] "s"(xs), [lad0] "s"(ladder(xs, xs & 1)), [s3] "s"(pool.s3),
                 [su] "s"((int)OFF_UF + c16 * 32768), [yb] "s"(pool.base + P_Y + c16 * 16384), [ysel] "s"(pool.ysel), [tu] "s"(TTU),
                 [stl] "s"(lds0 + REC_ST - 32), [uf] "s"(kqf_off(layer, 0, 32))
               : FIN_CLOB RST_CLOB, "vcc", "scc", "memory");
}

constexpr int PJ_BUF = 43 * 1024, PJ_X = 16384, PJ_LDS = PJ_X + 3 * PJ_BUF;
#define KQ_ASM \
   "s_mov_b32 s44, %[rdy]\n\t" "s_mov_b32 s45, 0\n\t" "s_mov_b32 s46, 0\n\t" "s_cmp_eq_u32 s44, -1\n\t" "s_cbranch_scc1 68f\n\t" \
   "s_mul_i32 s52, s45, 1536\n\t" "s_mul_i32 s51, s45, 40960\n\t" "s_add_u32 m0, s46, %[ldx]\n\t" "s_mov_b32 s53, s46\n\t" \
   "buffer_load_dwordx4 v16, %[rs], s51 offen offset:0 sc1 lds\n\t" "buffer_load_dwordx4 v16, %[rs], s51 offen offset:1024 sc1 lds\n\t" \
   "buffer_load_dwordx4 v16, %[rs], s51 offen offset:2048 sc1 lds\n\t" "buffer_load_dwordx4 v16, %[rs], s51 offen offset:3072 sc1 lds\n\t" \
   "s_add_u32 m0, m0, 4096\n\t" "s_add_u32 s51, s51, 4096\n\t" "buffer_load_dwordx4 v16, %[rs], s51 offen offset:0 sc1 lds\n\t" \
   "buffer_load_dwordx4 v16, %[rs], s51 offen offset:1024 sc1 lds\n\t" "buffer_load_dwordx4 v16, %[rs], s51 offen offset:2048 sc1 lds\n\t" \
   "buffer_load_dwordx4 v16, %[rs], s51 offen offset:3072 sc1 lds\n\t" "s_add_u32 m0, m0, 4096\n\t" "s_add_u32 s51, s51, 4096\n\t" \
   "buffer_load_dwordx4 v16, %[rs], s51 offen offset:0 sc1 lds\n\t" "buffer_load_dwordx4 v16, %[rs], s51 offen offset:1024 sc1 lds\n\t" \
   "s_add_u32 m0, s53, %[ld1]\n\t" "s_nop 0\n\t" "buffer_load_dwordx4 v17, %[rs], s52 offen sc1 lds\n\t" "s_mov_b32 s45, 1\n\t" \
   "s_mov_b32 s46, 44032\n\t" "s_mul_i32 s52, s45, 1536\n\t" "s_mul_i32 s51, s45, 40960\n\t" "s_add_u32 m0, s46, %[ldx]\n\t" "s_mov_b32 s53, s46\n\t" \
   "buffer_load_dwordx4 v16, %[rs], s51 offen offset:0 sc1 lds\n\t" "buffer_load_dwordx4 v16, %[rs], s51 offen offset:1024 sc1 lds\n\t" \
   "buffer_load_dwordx4 v16, %[rs], s51 offen offset:2048 sc1 lds\n\t" "buffer_load_dwordx4 v16, %[rs], s51 offen offset:3072 sc1 lds\n\t" \
   "s_add_u32 m0, m0, 4096\n\t" "s_add_u32 s51, s51, 4096\n\t" "buffer_load_dwordx4 v16, %[rs], s51 offen offset:0 sc1 lds\n\t" \
   "buffer_load_dwordx4 v16, %[rs], s51 offen offset:1024 sc1 lds\n\t" "buffer_load_dwordx4 v16, %[rs], s51 offen offset:2048 sc1 lds\n\t" \
   "buffer_load_dwordx4 v16, %[rs], s51 offen offset:3072 sc1 lds\n\t" "s_add_u32 m0, m0, 4096\n\t" "s_add_u32 s51, s51, 4096\n\t" \
   "buffer_load_dwordx4 v16, %[rs], s51 offen offset:0 sc1 lds\n\t" "buffer_load_dwordx4 v16, %[rs], s51 offen offset:1024 sc1 lds\n\t" \
   "s_add_u32 m0, s53, %[ld1]\n\t" "s_nop 0\n\t" "buffer_load_dwordx4 v17, %[rs], s52 offen sc1 lds\n\t" "68:\n\t" "v_mov_b32 v80, 0x3f803f80\n\t" \
   "v_mov_b32 v81, 0x3f803f80\n\t" "v_mov_b32 v82, 0x3f803f80\n\t" "v_mov_b32 v83, 0x3f803f80\n\t" "v_mov_b32 v31, 1\n\t" "s_nop 1\n\t" \
   "v_mfma_f32_16x16x32_bf16 v[32:35], a[0:3], v[80:83], 0\n\t" "v_mfma_f32_16x16x32_bf16 v[36:39], a[4:7], v[80:83], 0\n\t" \
   "v_mfma_f32_16x16x32_bf16 v[40:43], a[8:11], v[80:83], 0\n\t" "v_mfma_f32_16x16x32_bf16 v[44:47], a[12:15], v[80:83], 0\n\t" \
   "v_mfma_f32_16x16x32_bf16 v[32:35], a[16:19], v[80:83], v[32:35]\n\t" "v_mfma_f32_16x16x32_bf16 v[36:39], a[20:23], v[80:83], v[36:39]\n\t" \
   "v_mfma_f32_16x16x32_bf16 v[40:43], a[24:27], v[80:83], v[40:43]\n\t" "v_mfma_f32_16x16x32_bf16 v[44:47], a[28:31], v[80:83], v[44:47]\n\t" \
   "v_mfma_f32_16x16x32_bf16 v[32:35], a[32:35], v[80:83], v[32:35]\n\t" "v_mfma_f32_16x16x32_bf16 v[36:39], a[36:39], v[80:83], v[36:39]\n\t" \
   "v_mfma_f32_16x16x32_bf16 v[40:43], a[40:43], v[80:83], v[40:43]\n\t" "v_mfma_f32_16x16x32_bf16 v[44:47], a[44:47], v[80:83], v[44:47]\n\t" \
   "v_mfma_f32_16x16x32_bf16 v[32:35], a[48:51], v[80:83], v[32:35]\n\t" "v_mfma_f32_16x16x32_bf16 v[36:39], a[52:55], v[80:83], v[36:39]\n\t" \
   "v_mfma_f32_16x16x32_bf16 v[40:43], a[56:59], v[80:83], v[40:43]\n\t" "v_mfma_f32_16x16x32_bf16 v[44:47], a[60:63], v[80:83], v[44:47]\n\t" \
   "v_mfma_f32_16x16x32_bf16 v[32:35], a[64:67], v[80:83], v[32:35]\n\t" "v_mfma_f32_16x16x32_bf16 v[36:39], a[68:71], v[80:83], v[36:39]\n\t" \
   "v_mfma_f32_16x16x32_bf16 v[40:43], a[72:75], v[80:83], v[40:43]\n\t" "v_mfma_f32_16x16x32_bf16 v[44:47], a[76:79], v[80:83], v[44:47]\n\t" \
   "v_mfma_f32_16x16x32_bf16 v[32:35], a[80:83], v[80:83], v[32:35]\n\t" "v_mfma_f32_16x16x32_bf16 v[36:39], a[84:87], v[80:83], v[36:39]\n\t" \
   "v_mfma_f32_16x16x32_bf16 v[40:43], a[88:91], v[80:83], v[40:43]\n\t" "v_mfma_f32_16x16x32_bf16 v[44:47], a[92:95], v[80:83], v[44:47]\n\t" \
   "v_mfma_f32_16x16x32_bf16 v[32:35], a[96:99], v[80:83], v[32:35]\n\t" "v_mfma_f32_16x16x32_bf16 v[36:39], a[100:103], v[80:83], v[36:39]\n\t" \
   "v_mfma_f32_16x16x32_bf16 v[40:43], a[104:107], v[80:83], v[40:43]\n\t" "v_mfma_f32_16x16x32_bf16 v[44:47], a[108:111], v[80:83], v[44:47]\n\t" \
   "v_mfma_f32_16x16x32_bf16 v[32:35], a[112:115], v[80:83], v[32:35]\n\t" "v_mfma_f32_16x16x32_bf16 v[36:39], a[116:119], v[80:83], v[36:39]\n\t" \
   "v_mfma_f32_16x16x32_bf16 v[40:43], a[120:123], v[80:83], v[40:43]\n\t" "v_mfma_f32_16x16x32_bf16 v[44:47], a[124:127], v[80:83], v[44:47]\n\t" \
   "v_mfma_f32_16x16x32_bf16 v[32:35], a[128:131], v[80:83], v[32:35]\n\t" "v_mfma_f32_16x16x32_bf16 v[36:39], a[132:135], v[80:83], v[36:39]\n\t" \
   "v_mfma_f32_16x16x32_bf16 v[40:43], a[136:139], v[80:83], v[40:43]\n\t" "v_mfma_f32_16x16x32_bf16 v[44:47], a[140:143], v[80:83], v[44:47]\n\t" \
   "v_mfma_f32_16x16x32_bf16 v[32:35], a[144:147], v[80:83], v[32:35]\n\t" "v_mfma_f32_16x16x32_bf16 v[36:39], a[148:151], v[80:83], v[36:39]\n\t" \
   "v_mfma_f32_16x16x32_bf16 v[40:43], a[152:155], v[80:83], v[40:43]\n\t" "v_mfma_f32_16x16x32_bf16 v[44:47], a[156:159], v[80:83], v[44:47]\n\t" \
   "v_mfma_f32_16x16x32_bf16 v[32:35], a[160:163], v[80:83], v[32:35]\n\t" "v_mfma_f32_16x16x32_bf16 v[36:39], a[164:167], v[80:83], v[36:39]\n\t" \
   "v_mfma_f32_16x16x32_bf16 v[40:43], a[168:171], v[80:83], v[40:43]\n\t" "v_mfma_f32_16x16x32_bf16 v[44:47], a[172:175], v[80:83], v[44:47]\n\t" \
   "v_mfma_f32_16x16x32_bf16 v[32:35], a[176:179], v[80:83], v[32:35]\n\t" "v_mfma_f32_16x16x32_bf16 v[36:39], a[180:183], v[80:83], v[36:39]\n\t" \
   "v_mfma_f32_16x16x32_bf16 v[40:43], a[184:187], v[80:83], v[40:43]\n\t" "v_mfma_f32_16x16x32_bf16 v[44:47], a[188:191], v[80:83], v[44:47]\n\t" \
   "v_mfma_f32_16x16x32_bf16 v[32:35], a[192:195], v[80:83], v[32:35]\n\t" "v_mfma_f32_16x16x32_bf16 v[36:39], a[196:199], v[80:83], v[36:39]\n\t" \
   "v_mfma_f32_16x16x32_bf16 v[40:43], a[200:203], v[80:83], v[40:43]\n\t" "v_mfma_f32_16x16x32_bf16 v[44:47], a[204:207], v[80:83], v[44:47]\n\t" \
   "v_mfma_f32_16x16x32_bf16 v[32:35], a[208:211], v[80:83], v[32:35]\n\t" "v_mfma_f32_16x16x32_bf16 v[36:39], a[212:215], v[80:83], v[36:39]\n\t" \
   "v_mfma_f32_16x16x32_bf16 v[40:43], a[216:219], v[80:83], v[40:43]\n\t" "v_mfma_f32_16x16x32_bf16 v[44:47], a[220:223], v[80:83], v[44:47]\n\t" \
   "v_mfma_f32_16x16x32_bf16 v[32:35], a[224:227], v[80:83], v[32:35]\n\t" "v_mfma_f32_16x16x32_bf16 v[36:39], a[228:231], v[80:83], v[36:39]\n\t" \
   "v_mfma_f32_16x16x32_bf16 v[40:43], a[232:235], v[80:83], v[40:43]\n\t" "v_mfma_f32_16x16x32_bf16 v[44:47], a[236:239], v[80:83], v[44:47]\n\t" \
   "v_mfma_f32_16x16x32_bf16 v[32:35], a[240:243], v[80:83], v[32:35]\n\t" "v_mfma_f32_16x16x32_bf16 v[36:39], a[244:247], v[80:83], v[36:39]\n\t" \
   "v_mfma_f32_16x16x32_bf16 v[40:43], a[248:251], v[80:83], v[40:43]\n\t" "v_mfma_f32_16x16x32_bf16 v[44:47], a[252:255], v[80:83], v[44:47]\n\t" \
   "v_mfma_f32_16x16x32_bf16 v[32:35], v[192:195], v[80:83], v[32:35]\n\t" "v_mfma_f32_16x16x32_bf16 v[36:39], v[196:199], v[80:83], v[36:39]\n\t" \
   "v_mfma_f32_16x16x32_bf16 v[40:43], v[200:203], v[80:83], v[40:43]\n\t" "v_mfma_f32_16x16x32_bf16 v[44:47], v[204:207], v[80:83], v[44:47]\n\t" \
   "v_mfma_f32_16x16x32_bf16 v[32:35], v[208:211], v[80:83], v[32:35]\n\t" "v_mfma_f32_16x16x32_bf16 v[36:39], v[212:215], v[80:83], v[36:39]\n\t" \
   "v_mfma_f32_16x16x32_bf16 v[40:43], v[216:219], v[80:83], v[40:43]\n\t" "v_mfma_f32_16x16x32_bf16 v[44:47], v[220:223], v[80:83], v[44:47]\n\t" \
   "v_mfma_f32_16x16x32_bf16 v[32:35], v[224:227], v[80:83], v[32:35]\n\t" "v_mfma_f32_16x16x32_bf16 v[36:39], v[228:231], v[80:83], v[36:39]\n\t" \
   "v_mfma_f32_16x16x32_bf16 v[40:43], v[232:235], v[80:83], v[40:43]\n\t" "v_mfma_f32_16x16x32_bf16 v[44:47], v[236:239], v[80:83], v[44:47]\n\t" \
   "v_mfma_f32_16x16x32_bf16 v[32:35], v[240:243], v[80:83], v[32:35]\n\t" "v_mfma_f32_16x16x32_bf16 v[36:39], v[244:247], v[80:83], v[36:39]\n\t" \
   "v_mfma_f32_16x16x32_bf16 v[40:43], v[248:251], v[80:83], v[40:43]\n\t" "v_mfma_f32_16x16x32_bf16 v[44:47], v[252:255], v[80:83], v[44:47]\n\t" \
   "s_mov_b32 s40, 0\n\t" "s_mov_b32 s41, 0\n\t" "s_mov_b32 s42, 0\n\t" "s_mov_b32 s43, 44032\n\t" "v_mov_b32 v28, v22\n\t" "v_mov_b32 v29, v23\n\t" \
   "s_mov_b32 s45, 2\n\t" "s_mov_b32 s46, 88064\n\t" "s_waitcnt vmcnt(11)\n\t" "s_barrier\n\t" "s_lshr_b32 s48, s45, 1\n\t" \
   "s_bitcmp1_b32 s44, s48\n\t" "s_cbranch_scc0 172f\n\t" "72:\n\t" "s_mul_i32 s52, s45, 1536\n\t" "s_mul_i32 s51, s45, 40960\n\t" \
   "s_add_u32 m0, s46, %[ldx]\n\t" "s_mov_b32 s53, s46\n\t" "buffer_load_dwordx4 v16, %[rs], s51 offen offset:0 sc1 lds\n\t" \
   "ds_read_b128 v[80:83], v28 offset:0\n\t" "ds_read_b128 v[84:87], v28 offset:1024\n\t" "ds_read_b128 v[88:91], v28 offset:2048\n\t" \
   "ds_read_b128 v[92:95], v28 offset:3072\n\t" "1:\n\t" "v_mov_b32 v26, v28\n\t" "v_mov_b32 v27, v29\n\t" "s_waitcnt lgkmcnt(2)\n\t" \
   "v_mfma_f32_16x16x32_bf16 v[48:51], a[0:3], v[80:83], 0\n\t" "ds_read_b128 v[112:115], v27 offset:0\n\t" \
   "v_mfma_f32_16x16x32_bf16 v[52:55], a[4:7], v[80:83], 0\n\t" "ds_read_b128 v[96:99], v26 offset:4096\n\t" \
   "v_mfma_f32_16x16x32_bf16 v[56:59], a[8:11], v[80:83], 0\n\t" "ds_read_b128 v[116:119], v27 offset:16\n\t" \
   "v_mfma_f32_16x16x32_bf16 v[60:63], a[12:15], v[80:83], 0\n\t" "v_mul_f32 v133, 0x3e0293ee, v132\n\t" "v_fmac_f32 v64, v32, v12\n\t" \
   "v_mfma_f32_16x16x32_bf16 v[48:51], a[16:19], v[84:87], v[48:51]\n\t" "v_fmac_f32 v65, v33, v12\n\t" "v_fmac_f32 v66, v34, v12\n\t" \
   "v_mfma_f32_16x16x32_bf16 v[52:55], a[20:23], v[84:87], v[52:55]\n\t" "ds_read_b128 v[100:103], v26 offset:5120\n\t" \
   "v_mfma_f32_16x16x32_bf16 v[56:59], a[24:27], v[84:87], v[56:59]\n\t" "v_fmac_f32 v67, v35, v12\n\t" "v_fmac_f32 v68, v36, v12\n\t" \
   "v_mfma_f32_16x16x32_bf16 v[60:63], a[28:31], v[84:87], v[60:63]\n\t" "ds_read_b64 v[120:121], v27 offset:32\n\t" "s_waitcnt lgkmcnt(5)\n\t" \
   "v_mfma_f32_16x16x32_bf16 v[48:51], a[32:35], v[88:91], v[48:51]\n\t" "ds_read_b128 v[122:125], v27 offset:768\n\t" \
   "v_mfma_f32_16x16x32_bf16 v[52:55], a[36:39], v[88:91], v[52:55]\n\t" "ds_read_b128 v[104:107], v26 offset:6144\n\t" \
   "ds_read_b128 v[126:129], v27 offset:784\n\t" "v_mfma_f32_16x16x32_bf16 v[56:59], a[40:43], v[88:91], v[56:59]\n\t" \
   "ds_read_b64 v[130:131], v27 offset:800\n\t" "v_mfma_f32_16x16x32_bf16 v[60:63], a[44:47], v[88:91], v[60:63]\n\t" "v_fmac_f32 v69, v37, v12\n\t" \
   "v_fmac_f32 v70, v38, v12\n\t" "v_mfma_f32_16x16x32_bf16 v[48:51], a[48:51], v[92:95], v[48:51]\n\t" "v_fmac_f32 v71, v39, v12\n\t" \
   "v_fmac_f32 v72, v40, v12\n\t" "v_mfma_f32_16x16x32_bf16 v[52:55], a[52:55], v[92:95], v[52:55]\n\t" "ds_read_b128 v[108:111], v26 offset:7168\n\t" \
   "v_mfma_f32_16x16x32_bf16 v[56:59], a[56:59], v[92:95], v[56:59]\n\t" "v_fmac_f32 v73, v41, v12\n\t" "v_fmac_f32 v74, v42, v12\n\t" \
   "v_mfma_f32_16x16x32_bf16 v[60:63], a[60:63], v[92:95], v[60:63]\n\t" "v_fmac_f32 v75, v43, v12\n\t" "s_waitcnt lgkmcnt(6)\n\t" \
   "v_mfma_f32_16x16x32_bf16 v[48:51], a[64:67], v[96:99], v[48:51]\n\t" "v_fmac_f32 v76, v44, v12\n\t" "v_fmac_f32 v77, v45, v12\n\t" \
   "v_mfma_f32_16x16x32_bf16 v[52:55], a[68:71], v[96:99], v[52:55]\n\t" "ds_read_b128 v[80:83], v26 offset:8192\n\t" \
   "v_mfma_f32_16x16x32_bf16 v[56:59], a[72:75], v[96:99], v[56:59]\n\t" "v_fmac_f32 v78, v46, v12\n\t" "v_fmac_f32 v79, v47, v12\n\t" \
   "v_mfma_f32_16x16x32_bf16 v[60:63], a[76:79], v[96:99], v[60:63]\n\t" "v_mul_f32 v64, v64, v133\n\t" "v_mul_f32 v65, v65, v133\n\t" \
   "v_mfma_f32_16x16x32_bf16 v[48:51], a[80:83], v[100:103], v[48:51]\n\t" "v_mul_f32 v66, v66, v133\n\t" "v_mul_f32 v67, v67, v133\n\t" \
   "v_mfma_f32_16x16x32_bf16 v[52:55], a[84:87], v[100:103], v[52:55]\n\t" "ds_read_b128 v[84:87], v26 offset:9216\n\t" \
   "v_mfma_f32_16x16x32_bf16 v[56:59], a[88:91], v[100:103], v[56:59]\n\t" "v_mul_f32 v68, v68, v133\n\t" "v_mul_f32 v69, v69, v133\n\t" \
   "v_mfma_f32_16x16x32_bf16 v[60:63], a[92:95], v[100:103], v[60:63]\n\t" "v_mul_f32 v70, v70, v133\n\t" "s_waitcnt lgkmcnt(2)\n\t" \
   "v_mfma_f32_16x16x32_bf16 v[48:51], a[96:99], v[104:107], v[48:51]\n\t" "v_mul_f32 v71, v71, v133\n\t" "v_mul_f32 v72, v72, v132\n\t" \
   "v_mfma_f32_16x16x32_bf16 v[52:55], a[100:103], v[104:107], v[52:55]\n\t" "ds_read_b128 v[88:91], v26 offset:10240\n\t" \
   "v_mfma_f32_16x16x32_bf16 v[56:59], a[104:107], v[104:107], v[56:59]\n\t" "v_mul_f32 v73, v73, v132\n\t" "v_mul_f32 v74, v74, v132\n\t" \
   "v_mfma_f32_16x16x32_bf16 v[60:63], a[108:111], v[104:107], v[60:63]\n\t" "v_mul_f32 v75, v75, v132\n\t" "v_mul_f32 v76, v76, v132\n\t" \
   "v_mfma_f32_16x16x32_bf16 v[48:51], a[112:115], v[108:111], v[48:51]\n\t" "v_mul_f32 v77, v77, v132\n\t" "v_mul_f32 v78, v78, v132\n\t" \
   "v_mfma_f32_16x16x32_bf16 v[52:55], a[116:119], v[108:111], v[52:55]\n\t" "ds_read_b128 v[92:95], v26 offset:11264\n\t" \
   "v_mfma_f32_16x16x32_bf16 v[56:59], a[120:123], v[108:111], v[56:59]\n\t" "v_mul_f32 v79, v79, v132\n\t" "v_cvt_pk_bf16_f32 v136, v64, v65\n\t" \
   "v_mfma_f32_16x16x32_bf16 v[60:63], a[124:127], v[108:111], v[60:63]\n\t" "v_cvt_pk_bf16_f32 v137, v66, v67\n\t" "s_waitcnt lgkmcnt(2)\n\t" \
   "v_mfma_f32_16x16x32_bf16 v[48:51], a[128:131], v[80:83], v[48:51]\n\t" "v_cvt_pk_bf16_f32 v138, v68, v69\n\t" \
   "v_cvt_pk_bf16_f32 v139, v70, v71\n\t" "v_mfma_f32_16x16x32_bf16 v[52:55], a[132:135], v[80:83], v[52:55]\n\t" \
   "ds_read_b128 v[96:99], v26 offset:12288\n\t" "v_mfma_f32_16x16x32_bf16 v[56:59], a[136:139], v[80:83], v[56:59]\n\t" \
   "v_cvt_pk_bf16_f32 v140, v72, v73\n\t" "v_cvt_pk_bf16_f32 v141, v74, v75\n\t" \
   "v_mfma_f32_16x16x32_bf16 v[60:63], a[140:143], v[80:83], v[60:63]\n\t" "v_cvt_pk_bf16_f32 v142, v76, v77\n\t" \
   "v_cvt_pk_bf16_f32 v143, v78, v79\n\t" "v_mfma_f32_16x16x32_bf16 v[48:51], a[144:147], v[84:87], v[48:51]\n\t" "s_lshl_b32 s47, s41, 13\n\t" \
   "s_add_i32 s47, s47, -4096\n\t" "v_mfma_f32_16x16x32_bf16 v[52:55], a[148:151], v[84:87], v[52:55]\n\t" \
   "ds_read_b128 v[100:103], v26 offset:13312\n\t" "v_mfma_f32_16x16x32_bf16 v[56:59], a[152:155], v[84:87], v[56:59]\n\t" "s_add_u32 s54, s41, 1\n\t" \
   "s_max_i32 s47, s47, 0\n\t" "v_mfma_f32_16x16x32_bf16 v[60:63], a[156:159], v[84:87], v[60:63]\n\t" "s_lshr_b64 s[60:61], %[sel], s54\n\t" \
   "s_cmp_eq_u32 s40, 0\n\t" "s_cselect_b32 s48, 0x7ffffff0, 0\n\t" "s_waitcnt lgkmcnt(2)\n\t" \
   "v_mfma_f32_16x16x32_bf16 v[48:51], a[160:163], v[88:91], v[48:51]\n\t" "s_lshl_b32 s55, s54, 19\n\t" "s_and_b32 s60, s60, 1\n\t" \
   "v_or_b32 v30, s48, v21\n\t" "s_add_u32 s55, s55, %[pool]\n\t" "v_mfma_f32_16x16x32_bf16 v[52:55], a[164:167], v[88:91], v[52:55]\n\t" \
   "ds_read_b128 v[104:107], v26 offset:14336\n\t" "s_add_u32 s48, s47, %[qf]\n\t" "s_lshl_b32 s60, s60, 13\n\t" \
   "buffer_store_dwordx4 v[136:139], v30, %[rs], s48 offen sc1\n\t" "v_mfma_f32_16x16x32_bf16 v[56:59], a[168:171], v[88:91], v[56:59]\n\t" \
   "s_add_u32 s55, s55, s60\n\t" "s_add_u32 s48, s47, %[kf]\n\t" "s_bitcmp1_b64 %[skip], s41\n\t" "s_cselect_b32 s58, 0, %[size]\n\t" \
   "v_mfma_f32_16x16x32_bf16 v[60:63], a[172:175], v[88:91], v[60:63]\n\t" "buffer_store_dwordx4 v[140:143], v30, %[rs], s48 offen sc1\n\t" \
   "v_mfma_f32_16x16x32_bf16 v[48:51], a[176:179], v[92:95], v[48:51]\n\t" "v_add_f32 v112, v112, v114\n\t" \
   "v_mfma_f32_16x16x32_bf16 v[52:55], a[180:183], v[92:95], v[52:55]\n\t" "ds_read_b128 v[108:111], v26 offset:15360\n\t" \
   "v_mfma_f32_16x16x32_bf16 v[56:59], a[184:187], v[92:95], v[56:59]\n\t" "v_add_f32 v113, v113, v115\n\t" "v_add_f32 v116, v116, v118\n\t" \
   "v_mfma_f32_16x16x32_bf16 v[60:63], a[188:191], v[92:95], v[60:63]\n\t" \
   "buffer_store_dwordx4 v[156:159], v152, s[56:59], s55 offen offset:0 sc1\n\t" "s_waitcnt lgkmcnt(2)\n\t" \
   "v_mfma_f32_16x16x32_bf16 v[48:51], a[192:195], v[96:99], v[48:51]\n\t" "v_add_f32 v117, v117, v119\n\t" "v_add_f32 v112, v112, v120\n\t" \
   "v_mfma_f32_16x16x32_bf16 v[52:55], a[196:199], v[96:99], v[52:55]\n\t" "ds_read_b128 v[80:83], v26 offset:16384\n\t" \
   "v_mfma_f32_16x16x32_bf16 v[56:59], a[200:203], v[96:99], v[56:59]\n\t" "v_add_f32 v113, v113, v121\n\t" "v_add_f32 v112, v112, v116\n\t" \
   "v_mfma_f32_16x16x32_bf16 v[60:63], a[204:207], v[96:99], v[60:63]\n\t" "v_add_f32 v113, v113, v117\n\t" "v_mul_f32 v112, 0xbacccccd, v112\n\t" \
   "v_mfma_f32_16x16x32_bf16 v[48:51], a[208:211], v[100:103], v[48:51]\n\t" \
   "buffer_store_dwordx4 v[156:159], v152, s[56:59], s55 offen offset:1024 sc1\n\t" \
   "v_mfma_f32_16x16x32_bf16 v[52:55], a[212:215], v[100:103], v[52:55]\n\t" "ds_read_b128 v[84:87], v26 offset:17408\n\t" \
   "v_mfma_f32_16x16x32_bf16 v[56:59], a[216:219], v[100:103], v[56:59]\n\t" "v_mul_f32 v113, 0x3acccccd, v113\n\t" \
   "v_fma_f32 v113, -v112, v112, v113\n\t" "v_mfma_f32_16x16x32_bf16 v[60:63], a[220:223], v[100:103], v[60:63]\n\t" "v_max_f32 v113, 0, v113\n\t" \
   "s_waitcnt lgkmcnt(2)\n\t" "v_mfma_f32_16x16x32_bf16 v[48:51], a[224:227], v[104:107], v[48:51]\n\t" "v_cvt_pk_bf16_f32 v113, v113, v112\n\t" \
   "v_and_b32 v8, 0xffff0000, v113\n\t" "v_mfma_f32_16x16x32_bf16 v[52:55], a[228:231], v[104:107], v[52:55]\n\t" \
   "ds_read_b128 v[88:91], v26 offset:18432\n\t" "v_mfma_f32_16x16x32_bf16 v[56:59], a[232:235], v[104:107], v[56:59]\n\t" \
   "v_lshlrev_b32 v113, 16, v113\n\t" "v_add_f32 v113, 0x3727c5ac, v113\n\t" "v_mfma_f32_16x16x32_bf16 v[60:63], a[236:239], v[104:107], v[60:63]\n\t" \
   "v_cvt_pk_bf16_f32 v113, v113, v113\n\t" "v_lshlrev_b32 v113, 16, v113\n\t" \
   "v_mfma_f32_16x16x32_bf16 v[48:51], a[240:243], v[108:111], v[48:51]\n\t" "v_sqrt_f32 v113, v113\n\t" \
   "v_mfma_f32_16x16x32_bf16 v[52:55], a[244:247], v[108:111], v[52:55]\n\t" "ds_read_b128 v[92:95], v26 offset:19456\n\t" \
   "v_mfma_f32_16x16x32_bf16 v[56:59], a[248:251], v[108:111], v[56:59]\n\t" "buffer_load_dwordx4 v16, %[rs], s51 offen offset:1024 sc1 lds\n\t" \
   "v_mfma_f32_16x16x32_bf16 v[60:63], a[252:255], v[108:111], v[60:63]\n\t" "s_lshr_b64 s[60:61], %[ysel], s54\n\t" "s_waitcnt lgkmcnt(2)\n\t" \
   "v_mfma_f32_16x16x32_bf16 v[48:51], v[192:195], v[80:83], v[48:51]\n\t" "s_nop 0\n\t" "s_mul_i32 s61, s54, 43\n\t" \
   "v_mfma_f32_16x16x32_bf16 v[52:55], v[196:199], v[80:83], v[52:55]\n\t" "ds_read_b128 v[96:99], v26 offset:20480\n\t" \
   "v_mfma_f32_16x16x32_bf16 v[56:59], v[200:203], v[80:83], v[56:59]\n\t" "v_add_f32 v122, v122, v124\n\t" \
   "v_mfma_f32_16x16x32_bf16 v[60:63], v[204:207], v[80:83], v[60:63]\n\t" "v_cvt_pk_bf16_f32 v113, v113, v113\n\t" "v_add_f32 v123, v123, v125\n\t" \
   "v_mfma_f32_16x16x32_bf16 v[48:51], v[208:211], v[84:87], v[48:51]\n\t" "s_mul_i32 s55, s54, 80\n\t" "v_add_f32 v126, v126, v128\n\t" \
   "v_mfma_f32_16x16x32_bf16 v[52:55], v[212:215], v[84:87], v[52:55]\n\t" "ds_read_b128 v[100:103], v26 offset:21504\n\t" \
   "v_mfma_f32_16x16x32_bf16 v[56:59], v[216:219], v[84:87], v[56:59]\n\t" "buffer_load_dwordx4 v16, %[rs], s51 offen offset:2048 sc1 lds\n\t" \
   "v_mfma_f32_16x16x32_bf16 v[60:63], v[220:223], v[84:87], v[60:63]\n\t" "v_lshlrev_b32 v113, 16, v113\n\t" "s_waitcnt lgkmcnt(2)\n\t" \
   "v_mfma_f32_16x16x32_bf16 v[48:51], v[224:227], v[88:91], v[48:51]\n\t" "s_and_b32 s60, s60, 1\n\t" "v_add_f32 v127, v127, v129\n\t" \
   "v_mfma_f32_16x16x32_bf16 v[52:55], v[228:231], v[88:91], v[52:55]\n\t" "ds_read_b128 v[104:107], v26 offset:22528\n\t" \
   "v_mfma_f32_16x16x32_bf16 v[56:59], v[232:235], v[88:91], v[56:59]\n\t" "v_rcp_f32 v10, v113\n\t" \
   "v_mfma_f32_16x16x32_bf16 v[60:63], v[236:239], v[88:91], v[60:63]\n\t" "s_lshr_b32 s61, s61, 7\n\t" "v_add_f32 v122, v122, v130\n\t" \
   "v_mfma_f32_16x16x32_bf16 v[48:51], v[240:243], v[92:95], v[48:51]\n\t" "buffer_load_dwordx4 v16, %[rs], s51 offen offset:3072 sc1 lds\n\t" \
   "s_add_u32 m0, m0, 4096\n\t" "s_add_u32 s51, s51, 4096\n\t" "v_mfma_f32_16x16x32_bf16 v[52:55], v[244:247], v[92:95], v[52:55]\n\t" \
   "ds_read_b128 v[108:111], v26 offset:23552\n\t" "v_mfma_f32_16x16x32_bf16 v[56:59], v[248:251], v[92:95], v[56:59]\n\t" \
   "s_add_u32 s55, s55, s60\n\t" "v_add_f32 v123, v123, v131\n\t" "v_mfma_f32_16x16x32_bf16 v[60:63], v[252:255], v[92:95], v[60:63]\n\t" \
   "s_lshl4_add_u32 s55, s61, s55\n\t" "s_waitcnt lgkmcnt(2)\n\t" "v_mfma_f32_16x16x32_bf16 v[64:67], a[0:3], v[96:99], 0\n\t" \
   "v_add_f32 v122, v122, v126\n\t" "v_add_f32 v123, v123, v127\n\t" "v_mfma_f32_16x16x32_bf16 v[68:71], a[4:7], v[96:99], 0\n\t" \
   "ds_read_b128 v[80:83], v26 offset:24576\n\t" "v_mfma_f32_16x16x32_bf16 v[72:75], a[8:11], v[96:99], 0\n\t" "s_lshl_b32 s55, s55, 13\n\t" \
   "v_mul_f32 v122, 0xbacccccd, v122\n\t" "v_mfma_f32_16x16x32_bf16 v[76:79], a[12:15], v[96:99], 0\n\t" "v_mul_f32 v11, 0x3e0293ee, v10\n\t" \
   "v_fmac_f32 v48, v32, v8\n\t" "v_mfma_f32_16x16x32_bf16 v[64:67], a[16:19], v[100:103], v[64:67]\n\t" "v_fmac_f32 v49, v33, v8\n\t" \
   "v_fmac_f32 v50, v34, v8\n\t" "v_mfma_f32_16x16x32_bf16 v[68:71], a[20:23], v[100:103], v[68:71]\n\t" "ds_read_b128 v[84:87], v26 offset:25600\n\t" \
   "v_mfma_f32_16x16x32_bf16 v[72:75], a[24:27], v[100:103], v[72:75]\n\t" "s_cmp_lt_i32 s40, 1\n\t" "s_cselect_b32 s49, 0x7ffffff0, 0\n\t" \
   "v_mfma_f32_16x16x32_bf16 v[76:79], a[28:31], v[100:103], v[76:79]\n\t" "v_or_b32 v9, s49, v20\n\t" "s_waitcnt lgkmcnt(2)\n\t" \
   "v_mfma_f32_16x16x32_bf16 v[64:67], a[32:35], v[104:107], v[64:67]\n\t" "s_sub_i32 s49, s41, 1\n\t" "s_max_i32 s49, s49, 0\n\t" \
   "buffer_load_dwordx4 v16, %[rs], s51 offen offset:0 sc1 lds\n\t" "v_mfma_f32_16x16x32_bf16 v[68:71], a[36:39], v[104:107], v[68:71]\n\t" \
   "ds_read_b128 v[88:91], v26 offset:26624\n\t" "v_mfma_f32_16x16x32_bf16 v[72:75], a[40:43], v[104:107], v[72:75]\n\t" "s_lshl_b32 s49, s49, 12\n\t" \
   "s_waitcnt vmcnt(6)\n\t" "v_mfma_f32_16x16x32_bf16 v[76:79], a[44:47], v[104:107], v[76:79]\n\t" \
   "buffer_store_dword v31, v9, %[rs], s49 offen sc1\n\t" "v_mfma_f32_16x16x32_bf16 v[64:67], a[48:51], v[108:111], v[64:67]\n\t" \
   "s_add_u32 s55, s55, %[yb]\n\t" "v_fmac_f32 v51, v35, v8\n\t" "v_mfma_f32_16x16x32_bf16 v[68:71], a[52:55], v[108:111], v[68:71]\n\t" \
   "ds_read_b128 v[92:95], v26 offset:27648\n\t" "s_cmp_eq_u32 s41, 63\n\t" "s_cselect_b32 s58, 0, %[size]\n\t" \
   "v_mfma_f32_16x16x32_bf16 v[72:75], a[56:59], v[108:111], v[72:75]\n\t" "v_fmac_f32 v52, v36, v8\n\t" "v_fmac_f32 v53, v37, v8\n\t" \
   "v_mfma_f32_16x16x32_bf16 v[76:79], a[60:63], v[108:111], v[76:79]\n\t" "v_fmac_f32 v54, v38, v8\n\t" "s_waitcnt lgkmcnt(2)\n\t" \
   "v_mfma_f32_16x16x32_bf16 v[64:67], a[64:67], v[80:83], v[64:67]\n\t" "buffer_load_dwordx4 v16, %[rs], s51 offen offset:1024 sc1 lds\n\t" \
   "v_mfma_f32_16x16x32_bf16 v[68:71], a[68:71], v[80:83], v[68:71]\n\t" "ds_read_b128 v[96:99], v26 offset:28672\n\t" \
   "v_mfma_f32_16x16x32_bf16 v[72:75], a[72:75], v[80:83], v[72:75]\n\t" "buffer_store_dwordx4 v[156:159], v153, s[56:59], s55 offen sc1\n\t" \
   "v_mfma_f32_16x16x32_bf16 v[76:79], a[76:79], v[80:83], v[76:79]\n\t" "v_fmac_f32 v55, v39, v8\n\t" "v_fmac_f32 v56, v40, v8\n\t" \
   "v_mfma_f32_16x16x32_bf16 v[64:67], a[80:83], v[84:87], v[64:67]\n\t" "v_fmac_f32 v57, v41, v8\n\t" "v_fmac_f32 v58, v42, v8\n\t" \
   "v_mfma_f32_16x16x32_bf16 v[68:71], a[84:87], v[84:87], v[68:71]\n\t" "ds_read_b128 v[100:103], v26 offset:29696\n\t" \
   "v_mfma_f32_16x16x32_bf16 v[72:75], a[88:91], v[84:87], v[72:75]\n\t" "v_fmac_f32 v59, v43, v8\n\t" "v_fmac_f32 v60, v44, v8\n\t" \
   "v_mfma_f32_16x16x32_bf16 v[76:79], a[92:95], v[84:87], v[76:79]\n\t" "v_fmac_f32 v61, v45, v8\n\t" "s_waitcnt lgkmcnt(2)\n\t" \
   "v_mfma_f32_16x16x32_bf16 v[64:67], a[96:99], v[88:91], v[64:67]\n\t" "buffer_load_dwordx4 v16, %[rs], s51 offen offset:2048 sc1 lds\n\t" \
   "v_mfma_f32_16x16x32_bf16 v[68:71], a[100:103], v[88:91], v[68:71]\n\t" "ds_read_b128 v[104:107], v26 offset:30720\n\t" \
   "v_mfma_f32_16x16x32_bf16 v[72:75], a[104:107], v[88:91], v[72:75]\n\t" "buffer_store_dwordx4 v[156:159], v154, s[56:59], s55 offen sc1\n\t" \
   "v_mfma_f32_16x16x32_bf16 v[76:79], a[108:111], v[88:91], v[76:79]\n\t" "v_fmac_f32 v62, v46, v8\n\t" "v_fmac_f32 v63, v47, v8\n\t" \
   "v_mfma_f32_16x16x32_bf16 v[64:67], a[112:115], v[92:95], v[64:67]\n\t" "v_mul_f32 v48, v48, v11\n\t" "v_mul_f32 v49, v49, v11\n\t" \
   "v_mfma_f32_16x16x32_bf16 v[68:71], a[116:119], v[92:95], v[68:71]\n\t" "ds_read_b128 v[108:111], v26 offset:31744\n\t" \
   "v_mfma_f32_16x16x32_bf16 v[72:75], a[120:123], v[92:95], v[72:75]\n\t" "v_mul_f32 v50, v50, v11\n\t" "v_mul_f32 v51, v51, v11\n\t" \
   "v_mfma_f32_16x16x32_bf16 v[76:79], a[124:127], v[92:95], v[76:79]\n\t" "v_mul_f32 v52, v52, v11\n\t" "s_waitcnt lgkmcnt(2)\n\t" \
   "v_mfma_f32_16x16x32_bf16 v[64:67], a[128:131], v[96:99], v[64:67]\n\t" "buffer_load_dwordx4 v16, %[rs], s51 offen offset:3072 sc1 lds\n\t" \
   "s_add_u32 m0, m0, 4096\n\t" "s_add_u32 s51, s51, 4096\n\t" "v_mfma_f32_16x16x32_bf16 v[68:71], a[132:135], v[96:99], v[68:71]\n\t" \
   "ds_read_b128 v[80:83], v26 offset:32768\n\t" "v_mfma_f32_16x16x32_bf16 v[72:75], a[136:139], v[96:99], v[72:75]\n\t" "v_mul_f32 v53, v53, v11\n\t" \
   "buffer_store_dwordx4 v[156:159], v155, s[56:59], s55 offen sc1\n\t" "v_mfma_f32_16x16x32_bf16 v[76:79], a[140:143], v[96:99], v[76:79]\n\t" \
   "v_mul_f32 v54, v54, v11\n\t" "v_mul_f32 v55, v55, v11\n\t" "v_mfma_f32_16x16x32_bf16 v[64:67], a[144:147], v[100:103], v[64:67]\n\t" \
   "v_mul_f32 v56, v56, v10\n\t" "v_mul_f32 v57, v57, v10\n\t" "v_mfma_f32_16x16x32_bf16 v[68:71], a[148:151], v[100:103], v[68:71]\n\t" \
   "ds_read_b128 v[84:87], v26 offset:33792\n\t" "v_mfma_f32_16x16x32_bf16 v[72:75], a[152:155], v[100:103], v[72:75]\n\t" \
   "v_mul_f32 v58, v58, v10\n\t" "v_mul_f32 v59, v59, v10\n\t" "v_mfma_f32_16x16x32_bf16 v[76:79], a[156:159], v[100:103], v[76:79]\n\t" \
   "v_mul_f32 v60, v60, v10\n\t" "s_waitcnt lgkmcnt(2)\n\t" "v_mfma_f32_16x16x32_bf16 v[64:67], a[160:163], v[104:107], v[64:67]\n\t" \
   "buffer_load_dwordx4 v16, %[rs], s51 offen offset:0 sc1 lds\n\t" "v_mfma_f32_16x16x32_bf16 v[68:71], a[164:167], v[104:107], v[68:71]\n\t" \
   "ds_read_b128 v[88:91], v26 offset:34816\n\t" "v_mfma_f32_16x16x32_bf16 v[72:75], a[168:171], v[104:107], v[72:75]\n\t" \
   "v_mul_f32 v61, v61, v10\n\t" "v_mul_f32 v62, v62, v10\n\t" "v_mfma_f32_16x16x32_bf16 v[76:79], a[172:175], v[104:107], v[76:79]\n\t" \
   "v_mul_f32 v63, v63, v10\n\t" "v_mul_f32 v123, 0x3acccccd, v123\n\t" "v_mfma_f32_16x16x32_bf16 v[64:67], a[176:179], v[108:111], v[64:67]\n\t" \
   "v_cvt_pk_bf16_f32 v136, v48, v49\n\t" "v_fma_f32 v123, -v122, v122, v123\n\t" \
   "v_mfma_f32_16x16x32_bf16 v[68:71], a[180:183], v[108:111], v[68:71]\n\t" "ds_read_b128 v[92:95], v26 offset:35840\n\t" \
   "v_mfma_f32_16x16x32_bf16 v[72:75], a[184:187], v[108:111], v[72:75]\n\t" "v_cvt_pk_bf16_f32 v137, v50, v51\n\t" "v_max_f32 v123, 0, v123\n\t" \
   "v_mfma_f32_16x16x32_bf16 v[76:79], a[188:191], v[108:111], v[76:79]\n\t" "v_cvt_pk_bf16_f32 v138, v52, v53\n\t" "s_waitcnt lgkmcnt(2)\n\t" \
   "v_mfma_f32_16x16x32_bf16 v[64:67], a[192:195], v[80:83], v[64:67]\n\t" "v_cvt_pk_bf16_f32 v123, v123, v122\n\t" \
   "v_cvt_pk_bf16_f32 v139, v54, v55\n\t" "buffer_load_dwordx4 v16, %[rs], s51 offen offset:1024 sc1 lds\n\t" "s_add_u32 m0, s53, %[ld1]\n\t" \
   "s_nop 0\n\t" "v_mfma_f32_16x16x32_bf16 v[68:71], a[196:199], v[80:83], v[68:71]\n\t" "ds_read_b128 v[96:99], v26 offset:36864\n\t" \
   "v_mfma_f32_16x16x32_bf16 v[72:75], a[200:203], v[80:83], v[72:75]\n\t" "v_and_b32 v12, 0xffff0000, v123\n\t" "v_cvt_pk_bf16_f32 v140, v56, v57\n\t" \
   "v_mfma_f32_16x16x32_bf16 v[76:79], a[204:207], v[80:83], v[76:79]\n\t" "v_lshlrev_b32 v123, 16, v123\n\t" "v_cvt_pk_bf16_f32 v141, v58, v59\n\t" \
   "v_mfma_f32_16x16x32_bf16 v[64:67], a[208:211], v[84:87], v[64:67]\n\t" "v_add_f32 v123, 0x3727c5ac, v123\n\t" \
   "v_cvt_pk_bf16_f32 v142, v60, v61\n\t" "v_mfma_f32_16x16x32_bf16 v[68:71], a[212:215], v[84:87], v[68:71]\n\t" \
   "ds_read_b128 v[100:103], v26 offset:37888\n\t" "v_mfma_f32_16x16x32_bf16 v[72:75], a[216:219], v[84:87], v[72:75]\n\t" \
   "v_cvt_pk_bf16_f32 v123, v123, v123\n\t" "v_cvt_pk_bf16_f32 v143, v62, v63\n\t" \
   "v_mfma_f32_16x16x32_bf16 v[76:79], a[220:223], v[84:87], v[76:79]\n\t" "v_lshlrev_b32 v123, 16, v123\n\t" "s_waitcnt lgkmcnt(2)\n\t" \
   "v_mfma_f32_16x16x32_bf16 v[64:67], a[224:227], v[88:91], v[64:67]\n\t" "s_lshl_b32 s47, s41, 13\n\t" "v_sqrt_f32 v123, v123\n\t" \
   "s_add_i32 s47, s47, 0\n\t" "v_mfma_f32_16x16x32_bf16 v[68:71], a[228:231], v[88:91], v[68:71]\n\t" "ds_read_b128 v[104:107], v26 offset:38912\n\t" \
   "buffer_load_dwordx4 v17, %[rs], s52 offen sc1 lds\n\t" "v_mfma_f32_16x16x32_bf16 v[72:75], a[232:235], v[88:91], v[72:75]\n\t" \
   "s_add_u32 s48, s47, %[qf]\n\t" "s_nop 0\n\t" "buffer_store_dwordx4 v[136:139], v21, %[rs], s48 offen sc1\n\t" \
   "v_mfma_f32_16x16x32_bf16 v[76:79], a[236:239], v[88:91], v[76:79]\n\t" "v_cvt_pk_bf16_f32 v123, v123, v123\n\t" "v_lshlrev_b32 v123, 16, v123\n\t" \
   "s_add_u32 s48, s47, %[kf]\n\t" "v_rcp_f32 v132, v123\n\t" "v_mfma_f32_16x16x32_bf16 v[64:67], a[240:243], v[92:95], v[64:67]\n\t" \
   "buffer_store_dwordx4 v[140:143], v21, %[rs], s48 offen sc1\n\t" "v_add_u32 v28, s43, v22\n\t" "v_add_u32 v29, s43, v23\n\t" \
   "v_mfma_f32_16x16x32_bf16 v[68:71], a[244:247], v[92:95], v[68:71]\n\t" "ds_read_b128 v[108:111], v26 offset:39936\n\t" \
   "v_mfma_f32_16x16x32_bf16 v[72:75], a[248:251], v[92:95], v[72:75]\n\t" "v_mfma_f32_16x16x32_bf16 v[76:79], a[252:255], v[92:95], v[76:79]\n\t" \
   "s_waitcnt vmcnt(18) lgkmcnt(0)\n\t" "s_barrier\n\t" "v_mfma_f32_16x16x32_bf16 v[64:67], v[192:195], v[96:99], v[64:67]\n\t" \
   "s_add_u32 s45, s40, 3\n\t" "s_min_u32 s45, s45, 63\n\t" "v_mfma_f32_16x16x32_bf16 v[68:71], v[196:199], v[96:99], v[68:71]\n\t" \
   "ds_read_b128 v[80:83], v28 offset:0\n\t" "v_mfma_f32_16x16x32_bf16 v[72:75], v[200:203], v[96:99], v[72:75]\n\t" "s_mov_b32 s46, s42\n\t" \
   "v_mfma_f32_16x16x32_bf16 v[76:79], v[204:207], v[96:99], v[76:79]\n\t" "s_lshr_b32 s48, s45, 1\n\t" "s_bitcmp1_b32 s44, s48\n\t" \
   "s_cbranch_scc0 174f\n\t" "74:\n\t" "v_mfma_f32_16x16x32_bf16 v[64:67], v[208:211], v[100:103], v[64:67]\n\t" "s_mul_i32 s52, s45, 1536\n\t" \
   "s_mul_i32 s51, s45, 40960\n\t" "s_add_u32 m0, s46, %[ldx]\n\t" "s_mov_b32 s53, s46\n\t" \
   "v_mfma_f32_16x16x32_bf16 v[68:71], v[212:215], v[100:103], v[68:71]\n\t" "ds_read_b128 v[84:87], v28 offset:1024\n\t" \
   "v_mfma_f32_16x16x32_bf16 v[72:75], v[216:219], v[100:103], v[72:75]\n\t" "buffer_load_dwordx4 v16, %[rs], s51 offen offset:0 sc1 lds\n\t" \
   "v_mfma_f32_16x16x32_bf16 v[76:79], v[220:223], v[100:103], v[76:79]\n\t" "s_mov_b32 s42, s43\n\t" \
   "v_mfma_f32_16x16x32_bf16 v[64:67], v[224:227], v[104:107], v[64:67]\n\t" "s_add_u32 s43, s43, 44032\n\t" \
   "v_mfma_f32_16x16x32_bf16 v[68:71], v[228:231], v[104:107], v[68:71]\n\t" "ds_read_b128 v[88:91], v28 offset:2048\n\t" \
   "v_mfma_f32_16x16x32_bf16 v[72:75], v[232:235], v[104:107], v[72:75]\n\t" "s_cmp_eq_u32 s43, 132096\n\t" "s_cselect_b32 s43, 0, s43\n\t" \
   "v_mfma_f32_16x16x32_bf16 v[76:79], v[236:239], v[104:107], v[76:79]\n\t" "s_add_u32 s41, s41, 1\n\t" "s_add_u32 s40, s40, 1\n\t" \
   "v_mfma_f32_16x16x32_bf16 v[64:67], v[240:243], v[108:111], v[64:67]\n\t" "v_mfma_f32_16x16x32_bf16 v[68:71], v[244:247], v[108:111], v[68:71]\n\t" \
   "ds_read_b128 v[92:95], v28 offset:3072\n\t" "v_mfma_f32_16x16x32_bf16 v[72:75], v[248:251], v[108:111], v[72:75]\n\t" \
   "v_mfma_f32_16x16x32_bf16 v[76:79], v[252:255], v[108:111], v[76:79]\n\t" "s_cmp_lt_u32 s40, 64\n\t" "s_cbranch_scc1 1b\n\t" "s_nop 7\n\t" \
   "v_mul_f32 v133, 0x3e0293ee, v132\n\t" "v_fmac_f32 v64, v32, v12\n\t" "v_fmac_f32 v65, v33, v12\n\t" "v_fmac_f32 v66, v34, v12\n\t" \
   "v_fmac_f32 v67, v35, v12\n\t" "v_fmac_f32 v68, v36, v12\n\t" "v_fmac_f32 v69, v37, v12\n\t" "v_fmac_f32 v70, v38, v12\n\t" \
   "v_fmac_f32 v71, v39, v12\n\t" "v_fmac_f32 v72, v40, v12\n\t" "v_fmac_f32 v73, v41, v12\n\t" "v_fmac_f32 v74, v42, v12\n\t" \
   "v_fmac_f32 v75, v43, v12\n\t" "v_fmac_f32 v76, v44, v12\n\t" "v_fmac_f32 v77, v45, v12\n\t" "v_fmac_f32 v78, v46, v12\n\t" \
   "v_fmac_f32 v79, v47, v12\n\t" "v_mul_f32 v64, v64, v133\n\t" "v_mul_f32 v65, v65, v133\n\t" "v_mul_f32 v66, v66, v133\n\t" \
   "v_mul_f32 v67, v67, v133\n\t" "v_mul_f32 v68, v68, v133\n\t" "v_mul_f32 v69, v69, v133\n\t" "v_mul_f32 v70, v70, v133\n\t" \
   "v_mul_f32 v71, v71, v133\n\t" "v_mul_f32 v72, v72, v132\n\t" "v_mul_f32 v73, v73, v132\n\t" "v_mul_f32 v74, v74, v132\n\t" \
   "v_mul_f32 v75, v75, v132\n\t" "v_mul_f32 v76, v76, v132\n\t" "v_mul_f32 v77, v77, v132\n\t" "v_mul_f32 v78, v78, v132\n\t" \
   "v_mul_f32 v79, v79, v132\n\t" "v_cvt_pk_bf16_f32 v136, v64, v65\n\t" "v_cvt_pk_bf16_f32 v137, v66, v67\n\t" "v_cvt_pk_bf16_f32 v138, v68, v69\n\t" \
   "v_cvt_pk_bf16_f32 v139, v70, v71\n\t" "v_cvt_pk_bf16_f32 v140, v72, v73\n\t" "v_cvt_pk_bf16_f32 v141, v74, v75\n\t" \
   "v_cvt_pk_bf16_f32 v142, v76, v77\n\t" "v_cvt_pk_bf16_f32 v143, v78, v79\n\t" "s_lshl_b32 s47, s41, 13\n\t" "s_add_i32 s47, s47, -4096\n\t" \
   "s_max_i32 s47, s47, 0\n\t" "s_cmp_eq_u32 s40, 0\n\t" "s_cselect_b32 s48, 0x7ffffff0, 0\n\t" "v_or_b32 v30, s48, v21\n\t" \
   "s_add_u32 s48, s47, %[qf]\n\t" "buffer_store_dwordx4 v[136:139], v30, %[rs], s48 offen sc1\n\t" "s_add_u32 s48, s47, %[kf]\n\t" \
   "buffer_store_dwordx4 v[140:143], v30, %[rs], s48 offen sc1\n\t" "s_waitcnt vmcnt(0) lgkmcnt(0)\n\t" "s_cmp_lt_i32 s40, 1\n\t" \
   "s_cselect_b32 s47, 0x7ffffff0, 0\n\t" "v_or_b32 v30, s47, v20\n\t" "s_sub_i32 s47, s41, 1\n\t" "s_max_i32 s47, s47, 0\n\t" \
   "s_lshl_b32 s47, s47, 12\n\t" "buffer_store_dword v31, v30, %[rs], s47 offen sc1\n\t" "s_branch 9f\n\t" "172:\n\t" "s_mov_b32 s47, 0\n\t" \
   "buffer_load_dword v30, v19, %[rs], 0 offen sc1\n\t" "s_waitcnt vmcnt(0)\n\t" "v_add_u32 v30, 1, v30\n\t" "v_cmp_lt_u32 vcc, 9, v30\n\t" \
   "s_mov_b32 s44, vcc_lo\n\t" "s_bitcmp1_b32 s44, s48\n\t" "s_cbranch_scc1 72b\n\t" "s_bitset1_b32 s44, s48\n\t" "s_lshl_b32 s48, s48, 7\n\t" \
   "s_add_u32 s48, s48, %[cnt]\n\t" "73:\n\t" "s_sleep 10\n\t" "buffer_load_dword v30, off, %[rs], s48 sc1\n\t" "s_add_u32 s47, s47, 1\n\t" \
   "s_waitcnt vmcnt(0)\n\t" "v_add_u32 v30, 1, v30\n\t" "v_cmp_lt_u32 vcc, 9, v30\n\t" "s_cmp_gt_u32 s47, 0x20000\n\t" "s_cbranch_scc1 72b\n\t" \
   "s_cbranch_vccz 73b\n\t" "s_branch 72b\n\t" "174:\n\t" "s_mov_b32 s47, 0\n\t" "buffer_load_dword v30, v19, %[rs], 0 offen sc1\n\t" \
   "s_waitcnt vmcnt(0)\n\t" "v_add_u32 v30, 1, v30\n\t" "v_cmp_lt_u32 vcc, 9, v30\n\t" "s_mov_b32 s44, vcc_lo\n\t" "s_bitcmp1_b32 s44, s48\n\t" \
   "s_cbranch_scc1 74b\n\t" "s_bitset1_b32 s44, s48\n\t" "s_lshl_b32 s48, s48, 7\n\t" "s_add_u32 s48, s48, %[cnt]\n\t" "75:\n\t" "s_sleep 10\n\t" \
   "buffer_load_dword v30, off, %[rs], s48 sc1\n\t" "s_add_u32 s47, s47, 1\n\t" "s_waitcnt vmcnt(0)\n\t" "v_add_u32 v30, 1, v30\n\t" \
   "v_cmp_lt_u32 vcc, 9, v30\n\t" "s_cmp_gt_u32 s47, 0x20000\n\t" "s_cbranch_scc1 74b\n\t" "s_cbranch_vccz 75b\n\t" "s_branch 74b\n\t" "9:\n\t" \
   "s_waitcnt vmcnt(0)"
#define KQ_CLOB "v8", "v9", "v10", "v11", "v12", "v13", "v14", "v15", "v32", "v33", "v34", "v35", "v36", "v37", "v38", "v39", "v40", "v41", "v42", "v43", "v44", "v45", "v46", "v47", "v48", "v49", "v50", "v51", "v52", "v53", "v54", "v55", "v56", "v57", "v58", "v59", "v60", "v61", "v62", "v63", "v64", "v65", "v66", "v67", "v68", "v69", "v70", "v71", "v72", "v73", "v74", "v75", "v76", "v77", "v78", "v79", "v80", "v81", "v82", "v83", "v84", "v85", "v86", "v87", "v88", "v89", "v90", "v91", "v92", "v93", "v94", "v95", "v96", "v97", "v98", "v99", "v100", "v101", "v102", "v103", "v104", "v105", "v106", "v107", "v108", "v109", "v110", "v111", "v112", "v113", "v114", "v115", "v116", "v117", "v118", "v119", "v120", "v121", "v122", "v123", "v124", "v125", "v126", "v127", "v128", "v129", "v130", "v131", "v132", "v133", "v134", "v135", "v136", "v137", "v138", "v139", "v140", "v141", "v142", "v143", "v144", "v145", "v146", "v147", "v148", "v149", "v150", "v151", "s40", "s41", "s42", "s43", "s44", "s45", "s46", "s47", "s48", "s49", "s50", "s51", "s52", "s53", "s54", "s55", "s60", "s61"
#define VU_ASM \
   "s_mov_b32 s44, %[rdy]\n\t" "s_mov_b32 s45, 0\n\t" "s_mov_b32 s46, 0\n\t" "s_cmp_eq_u32 s44, -1\n\t" "s_cbranch_scc1 68f\n\t" \
   "s_mul_i32 s52, s45, 1536\n\t" "s_mul_i32 s51, s45, 40960\n\t" "s_add_u32 m0, s46, %[ldx]\n\t" "s_mov_b32 s53, s46\n\t" \
   "buffer_load_dwordx4 v16, %[rs], s51 offen offset:0 sc1 lds\n\t" "buffer_load_dwordx4 v16, %[rs], s51 offen offset:1024 sc1 lds\n\t" \
   "buffer_load_dwordx4 v16, %[rs], s51 offen offset:2048 sc1 lds\n\t" "buffer_load_dwordx4 v16, %[rs], s51 offen offset:3072 sc1 lds\n\t" \
   "s_add_u32 m0, m0, 4096\n\t" "s_add_u32 s51, s51, 4096\n\t" "buffer_load_dwordx4 v16, %[rs], s51 offen offset:0 sc1 lds\n\t" \
   "buffer_load_dwordx4 v16, %[rs], s51 offen offset:1024 sc1 lds\n\t" "buffer_load_dwordx4 v16, %[rs], s51 offen offset:2048 sc1 lds\n\t" \
   "buffer_load_dwordx4 v16, %[rs], s51 offen offset:3072 sc1 lds\n\t" "s_add_u32 m0, m0, 4096\n\t" "s_add_u32 s51, s51, 4096\n\t" \
   "buffer_load_dwordx4 v16, %[rs], s51 offen offset:0 sc1 lds\n\t" "buffer_load_dwordx4 v16, %[rs], s51 offen offset:1024 sc1 lds\n\t" \
   "s_add_u32 m0, s53, %[ld1]\n\t" "s_nop 0\n\t" "buffer_load_dwordx4 v17, %[rs], s52 offen sc1 lds\n\t" "s_mov_b32 s45, 1\n\t" \
   "s_mov_b32 s46, 44032\n\t" "s_mul_i32 s52, s45, 1536\n\t" "s_mul_i32 s51, s45, 40960\n\t" "s_add_u32 m0, s46, %[ldx]\n\t" "s_mov_b32 s53, s46\n\t" \
   "buffer_load_dwordx4 v16, %[rs], s51 offen offset:0 sc1 lds\n\t" "buffer_load_dwordx4 v16, %[rs], s51 offen offset:1024 sc1 lds\n\t" \
   "buffer_load_dwordx4 v16, %[rs], s51 offen offset:2048 sc1 lds\n\t" "buffer_load_dwordx4 v16, %[rs], s51 offen offset:3072 sc1 lds\n\t" \
   "s_add_u32 m0, m0, 4096\n\t" "s_add_u32 s51, s51, 4096\n\t" "buffer_load_dwordx4 v16, %[rs], s51 offen offset:0 sc1 lds\n\t" \
   "buffer_load_dwordx4 v16, %[rs], s51 offen offset:1024 sc1 lds\n\t" "buffer_load_dwordx4 v16, %[rs], s51 offen offset:2048 sc1 lds\n\t" \
   "buffer_load_dwordx4 v16, %[rs], s51 offen offset:3072 sc1 lds\n\t" "s_add_u32 m0, m0, 4096\n\t" "s_add_u32 s51, s51, 4096\n\t" \
   "buffer_load_dwordx4 v16, %[rs], s51 offen offset:0 sc1 lds\n\t" "buffer_load_dwordx4 v16, %[rs], s51 offen offset:1024 sc1 lds\n\t" \
   "s_add_u32 m0, s53, %[ld1]\n\t" "s_nop 0\n\t" "buffer_load_dwordx4 v17, %[rs], s52 offen sc1 lds\n\t" "68:\n\t" "v_mov_b32 v80, 0x3f803f80\n\t" \
   "v_mov_b32 v81, 0x3f803f80\n\t" "v_mov_b32 v82, 0x3f803f80\n\t" "v_mov_b32 v83, 0x3f803f80\n\t" "v_mov_b32 v31, 1\n\t" "s_nop 1\n\t" \
   "v_mfma_f32_16x16x32_bf16 v[32:35], a[0:3], v[80:83], 0\n\t" "v_mfma_f32_16x16x32_bf16 v[36:39], a[4:7], v[80:83], 0\n\t" \
   "v_mfma_f32_16x16x32_bf16 v[32:35], a[8:11], v[80:83], v[32:35]\n\t" "v_mfma_f32_16x16x32_bf16 v[36:39], a[12:15], v[80:83], v[36:39]\n\t" \
   "v_mfma_f32_16x16x32_bf16 v[32:35], a[16:19], v[80:83], v[32:35]\n\t" "v_mfma_f32_16x16x32_bf16 v[36:39], a[20:23], v[80:83], v[36:39]\n\t" \
   "v_mfma_f32_16x16x32_bf16 v[32:35], a[24:27], v[80:83], v[32:35]\n\t" "v_mfma_f32_16x16x32_bf16 v[36:39], a[28:31], v[80:83], v[36:39]\n\t" \
   "v_mfma_f32_16x16x32_bf16 v[32:35], a[32:35], v[80:83], v[32:35]\n\t" "v_mfma_f32_16x16x32_bf16 v[36:39], a[36:39], v[80:83], v[36:39]\n\t" \
   "v_mfma_f32_16x16x32_bf16 v[32:35], a[40:43], v[80:83], v[32:35]\n\t" "v_mfma_f32_16x16x32_bf16 v[36:39], a[44:47], v[80:83], v[36:39]\n\t" \
   "v_mfma_f32_16x16x32_bf16 v[32:35], a[48:51], v[80:83], v[32:35]\n\t" "v_mfma_f32_16x16x32_bf16 v[36:39], a[52:55], v[80:83], v[36:39]\n\t" \
   "v_mfma_f32_16x16x32_bf16 v[32:35], a[56:59], v[80:83], v[32:35]\n\t" "v_mfma_f32_16x16x32_bf16 v[36:39], a[60:63], v[80:83], v[36:39]\n\t" \
   "v_mfma_f32_16x16x32_bf16 v[32:35], a[64:67], v[80:83], v[32:35]\n\t" "v_mfma_f32_16x16x32_bf16 v[36:39], a[68:71], v[80:83], v[36:39]\n\t" \
   "v_mfma_f32_16x16x32_bf16 v[32:35], a[72:75], v[80:83], v[32:35]\n\t" "v_mfma_f32_16x16x32_bf16 v[36:39], a[76:79], v[80:83], v[36:39]\n\t" \
   "v_mfma_f32_16x16x32_bf16 v[32:35], a[80:83], v[80:83], v[32:35]\n\t" "v_mfma_f32_16x16x32_bf16 v[36:39], a[84:87], v[80:83], v[36:39]\n\t" \
   "v_mfma_f32_16x16x32_bf16 v[32:35], a[88:91], v[80:83], v[32:35]\n\t" "v_mfma_f32_16x16x32_bf16 v[36:39], a[92:95], v[80:83], v[36:39]\n\t" \
   "v_mfma_f32_16x16x32_bf16 v[32:35], a[96:99], v[80:83], v[32:35]\n\t" "v_mfma_f32_16x16x32_bf16 v[36:39], a[100:103], v[80:83], v[36:39]\n\t" \
   "v_mfma_f32_16x16x32_bf16 v[32:35], a[104:107], v[80:83], v[32:35]\n\t" "v_mfma_f32_16x16x32_bf16 v[36:39], a[108:111], v[80:83], v[36:39]\n\t" \
   "v_mfma_f32_16x16x32_bf16 v[32:35], a[112:115], v[80:83], v[32:35]\n\t" "v_mfma_f32_16x16x32_bf16 v[36:39], a[116:119], v[80:83], v[36:39]\n\t" \
   "v_mfma_f32_16x16x32_bf16 v[32:35], a[120:123], v[80:83], v[32:35]\n\t" "v_mfma_f32_16x16x32_bf16 v[36:39], a[124:127], v[80:83], v[36:39]\n\t" \
   "v_mfma_f32_16x16x32_bf16 v[32:35], a[128:131], v[80:83], v[32:35]\n\t" "v_mfma_f32_16x16x32_bf16 v[36:39], a[132:135], v[80:83], v[36:39]\n\t" \
   "v_mfma_f32_16x16x32_bf16 v[32:35], a[136:139], v[80:83], v[32:35]\n\t" "v_mfma_f32_16x16x32_bf16 v[36:39], a[140:143], v[80:83], v[36:39]\n\t" \
   "v_mfma_f32_16x16x32_bf16 v[32:35], a[144:147], v[80:83], v[32:35]\n\t" "v_mfma_f32_16x16x32_bf16 v[36:39], a[148:151], v[80:83], v[36:39]\n\t" \
   "v_mfma_f32_16x16x32_bf16 v[32:35], a[152:155], v[80:83], v[32:35]\n\t" "v_mfma_f32_16x16x32_bf16 v[36:39], a[156:159], v[80:83], v[36:39]\n\t" \
   "s_mov_b32 s40, 0\n\t" "s_mov_b32 s41, 0\n\t" "s_mov_b32 s42, 0\n\t" "s_mov_b32 s43, 44032\n\t" "v_mov_b32 v28, v22\n\t" "v_mov_b32 v29, v23\n\t" \
   "s_mov_b32 s45, 2\n\t" "s_mov_b32 s46, 88064\n\t" "s_waitcnt vmcnt(11)\n\t" "s_barrier\n\t" "s_lshr_b32 s48, s45, 1\n\t" \
   "s_bitcmp1_b32 s44, s48\n\t" "s_cbranch_scc0 172f\n\t" "72:\n\t" "s_mul_i32 s52, s45, 1536\n\t" "s_mul_i32 s51, s45, 40960\n\t" \
   "s_add_u32 m0, s46, %[ldx]\n\t" "s_mov_b32 s53, s46\n\t" "buffer_load_dwordx4 v16, %[rs], s51 offen offset:0 sc1 lds\n\t" \
   "ds_read_b128 v[80:83], v28 offset:0\n\t" "ds_read_b128 v[84:87], v28 offset:1024\n\t" "ds_read_b128 v[88:91], v28 offset:2048\n\t" \
   "ds_read_b128 v[92:95], v28 offset:3072\n\t" "ds_read_b128 v[96:99], v28 offset:4096\n\t" "ds_read_b128 v[100:103], v28 offset:5120\n\t" \
   "ds_read_b128 v[104:107], v28 offset:6144\n\t" "v_mov_b32 v14, v24\n\t" "v_mov_b32 v15, v25\n\t" "v_mov_b32 v18, 0x7ffffff0\n\t" \
   "s_mov_b32 s49, 0\n\t" "v_mov_b32 v26, v28\n\t" "v_mov_b32 v27, v29\n\t" "s_waitcnt lgkmcnt(5)\n\t" \
   "v_mfma_f32_16x16x32_bf16 v[48:51], a[0:3], v[80:83], 0\n\t" "ds_read_b128 v[112:115], v27 offset:0\n\t" \
   "v_mfma_f32_16x16x32_bf16 v[52:55], a[4:7], v[80:83], 0\n\t" "ds_read_b128 v[108:111], v26 offset:7168\n\t" \
   "v_mfma_f32_16x16x32_bf16 v[48:51], a[8:11], v[84:87], v[48:51]\n\t" "ds_read_b128 v[116:119], v27 offset:16\n\t" \
   "v_mfma_f32_16x16x32_bf16 v[52:55], a[12:15], v[84:87], v[52:55]\n\t" "ds_read_b128 v[80:83], v26 offset:8192\n\t" "s_waitcnt lgkmcnt(7)\n\t" \
   "v_mfma_f32_16x16x32_bf16 v[48:51], a[16:19], v[88:91], v[48:51]\n\t" "ds_read_b64 v[120:121], v27 offset:32\n\t" \
   "v_mfma_f32_16x16x32_bf16 v[52:55], a[20:23], v[88:91], v[52:55]\n\t" "ds_read_b128 v[84:87], v26 offset:9216\n\t" \
   "v_mfma_f32_16x16x32_bf16 v[48:51], a[24:27], v[92:95], v[48:51]\n\t" "ds_read_b128 v[122:125], v27 offset:768\n\t" \
   "v_mfma_f32_16x16x32_bf16 v[52:55], a[28:31], v[92:95], v[52:55]\n\t" "ds_read_b128 v[88:91], v26 offset:10240\n\t" "s_waitcnt lgkmcnt(9)\n\t" \
   "v_mfma_f32_16x16x32_bf16 v[48:51], a[32:35], v[96:99], v[48:51]\n\t" "buffer_load_dwordx4 v16, %[rs], s51 offen offset:1024 sc1 lds\n\t" \
   "v_mfma_f32_16x16x32_bf16 v[52:55], a[36:39], v[96:99], v[52:55]\n\t" "ds_read_b128 v[92:95], v26 offset:11264\n\t" \
   "v_mfma_f32_16x16x32_bf16 v[48:51], a[40:43], v[100:103], v[48:51]\n\t" "ds_read_b128 v[126:129], v27 offset:784\n\t" \
   "v_mfma_f32_16x16x32_bf16 v[52:55], a[44:47], v[100:103], v[52:55]\n\t" "ds_read_b128 v[96:99], v26 offset:12288\n\t" "s_waitcnt lgkmcnt(9)\n\t" \
   "v_mfma_f32_16x16x32_bf16 v[48:51], a[48:51], v[104:107], v[48:51]\n\t" "ds_read_b64 v[130:131], v27 offset:800\n\t" \
   "v_mfma_f32_16x16x32_bf16 v[52:55], a[52:55], v[104:107], v[52:55]\n\t" "ds_read_b128 v[100:103], v26 offset:13312\n\t" \
   "v_mfma_f32_16x16x32_bf16 v[48:51], a[56:59], v[108:111], v[48:51]\n\t" "s_waitcnt lgkmcnt(8)\n\t" "v_add_f32 v112, v112, v114\n\t" \
   "v_mfma_f32_16x16x32_bf16 v[52:55], a[60:63], v[108:111], v[52:55]\n\t" "ds_read_b128 v[104:107], v26 offset:14336\n\t" "s_waitcnt lgkmcnt(8)\n\t" \
   "v_mfma_f32_16x16x32_bf16 v[48:51], a[64:67], v[80:83], v[48:51]\n\t" "buffer_load_dwordx4 v16, %[rs], s51 offen offset:2048 sc1 lds\n\t" \
   "v_mfma_f32_16x16x32_bf16 v[52:55], a[68:71], v[80:83], v[52:55]\n\t" "ds_read_b128 v[108:111], v26 offset:15360\n\t" \
   "v_mfma_f32_16x16x32_bf16 v[48:51], a[72:75], v[84:87], v[48:51]\n\t" "v_add_f32 v113, v113, v115\n\t" "v_add_f32 v116, v116, v118\n\t" \
   "v_mfma_f32_16x16x32_bf16 v[52:55], a[76:79], v[84:87], v[52:55]\n\t" "ds_read_b128 v[80:83], v26 offset:16384\n\t" "s_waitcnt lgkmcnt(7)\n\t" \
   "v_mfma_f32_16x16x32_bf16 v[48:51], a[80:83], v[88:91], v[48:51]\n\t" "v_add_f32 v117, v117, v119\n\t" "v_add_f32 v112, v112, v120\n\t" \
   "v_mfma_f32_16x16x32_bf16 v[52:55], a[84:87], v[88:91], v[52:55]\n\t" "ds_read_b128 v[84:87], v26 offset:17408\n\t" \
   "v_mfma_f32_16x16x32_bf16 v[48:51], a[88:91], v[92:95], v[48:51]\n\t" "v_add_f32 v113, v113, v121\n\t" "v_add_f32 v112, v112, v116\n\t" \
   "v_mfma_f32_16x16x32_bf16 v[52:55], a[92:95], v[92:95], v[52:55]\n\t" "ds_read_b128 v[88:91], v26 offset:18432\n\t" \
   "buffer_load_dwordx4 v16, %[rs], s51 offen offset:3072 sc1 lds\n\t" "s_add_u32 m0, m0, 4096\n\t" "s_add_u32 s51, s51, 4096\n\t" \
   "s_waitcnt lgkmcnt(5)\n\t" "v_mfma_f32_16x16x32_bf16 v[48:51], a[96:99], v[96:99], v[48:51]\n\t" "v_add_f32 v113, v113, v117\n\t" \
   "v_mul_f32 v112, 0xbacccccd, v112\n\t" "v_mfma_f32_16x16x32_bf16 v[52:55], a[100:103], v[96:99], v[52:55]\n\t" \
   "ds_read_b128 v[92:95], v26 offset:19456\n\t" "v_mfma_f32_16x16x32_bf16 v[48:51], a[104:107], v[100:103], v[48:51]\n\t" \
   "v_mul_f32 v113, 0x3acccccd, v113\n\t" "v_fma_f32 v113, -v112, v112, v113\n\t" \
   "v_mfma_f32_16x16x32_bf16 v[52:55], a[108:111], v[100:103], v[52:55]\n\t" "ds_read_b128 v[96:99], v26 offset:20480\n\t" "s_waitcnt lgkmcnt(5)\n\t" \
   "v_mfma_f32_16x16x32_bf16 v[48:51], a[112:115], v[104:107], v[48:51]\n\t" "v_max_f32 v113, 0, v113\n\t" "v_cvt_pk_bf16_f32 v113, v113, v112\n\t" \
   "v_mfma_f32_16x16x32_bf16 v[52:55], a[116:119], v[104:107], v[52:55]\n\t" "ds_read_b128 v[100:103], v26 offset:21504\n\t" \
   "v_mfma_f32_16x16x32_bf16 v[48:51], a[120:123], v[108:111], v[48:51]\n\t" "buffer_load_dwordx4 v16, %[rs], s51 offen offset:0 sc1 lds\n\t" \
   "v_mfma_f32_16x16x32_bf16 v[52:55], a[124:127], v[108:111], v[52:55]\n\t" "ds_read_b128 v[104:107], v26 offset:22528\n\t" "s_waitcnt lgkmcnt(5)\n\t" \
   "v_mfma_f32_16x16x32_bf16 v[48:51], a[128:131], v[80:83], v[48:51]\n\t" "v_and_b32 v8, 0xffff0000, v113\n\t" "v_lshlrev_b32 v113, 16, v113\n\t" \
   "v_mfma_f32_16x16x32_bf16 v[52:55], a[132:135], v[80:83], v[52:55]\n\t" "ds_read_b128 v[108:111], v26 offset:23552\n\t" \
   "v_mfma_f32_16x16x32_bf16 v[48:51], a[136:139], v[84:87], v[48:51]\n\t" "v_add_f32 v113, 0x3727c5ac, v113\n\t" \
   "v_cvt_pk_bf16_f32 v113, v113, v113\n\t" "v_mfma_f32_16x16x32_bf16 v[52:55], a[140:143], v[84:87], v[52:55]\n\t" \
   "ds_read_b128 v[80:83], v26 offset:24576\n\t" "s_waitcnt lgkmcnt(5)\n\t" "v_mfma_f32_16x16x32_bf16 v[48:51], a[144:147], v[88:91], v[48:51]\n\t" \
   "v_lshlrev_b32 v113, 16, v113\n\t" "v_add_u32 v28, s43, v22\n\t" "v_mfma_f32_16x16x32_bf16 v[52:55], a[148:151], v[88:91], v[52:55]\n\t" \
   "ds_read_b128 v[84:87], v26 offset:25600\n\t" "buffer_load_dwordx4 v16, %[rs], s51 offen offset:1024 sc1 lds\n\t" \
   "v_mfma_f32_16x16x32_bf16 v[48:51], a[152:155], v[92:95], v[48:51]\n\t" "v_sqrt_f32 v113, v113\n\t" \
   "v_mfma_f32_16x16x32_bf16 v[52:55], a[156:159], v[92:95], v[52:55]\n\t" "ds_read_b128 v[88:91], v26 offset:26624\n\t" "s_nop 0\n\t" \
   "s_waitcnt lgkmcnt(5)\n\t" "v_mfma_f32_16x16x32_bf16 v[56:59], a[0:3], v[96:99], 0\n\t" "v_cvt_pk_bf16_f32 v113, v113, v113\n\t" \
   "v_lshlrev_b32 v113, 16, v113\n\t" "v_mfma_f32_16x16x32_bf16 v[60:63], a[4:7], v[96:99], 0\n\t" "ds_read_b128 v[92:95], v26 offset:27648\n\t" \
   "v_rcp_f32 v10, v113\n\t" "v_mfma_f32_16x16x32_bf16 v[56:59], a[8:11], v[100:103], v[56:59]\n\t" "v_add_f32 v122, v122, v124\n\t" \
   "v_mfma_f32_16x16x32_bf16 v[60:63], a[12:15], v[100:103], v[60:63]\n\t" "ds_read_b128 v[96:99], v26 offset:28672\n\t" "s_waitcnt lgkmcnt(5)\n\t" \
   "v_mfma_f32_16x16x32_bf16 v[56:59], a[16:19], v[104:107], v[56:59]\n\t" "buffer_load_dwordx4 v16, %[rs], s51 offen offset:2048 sc1 lds\n\t" \
   "v_mfma_f32_16x16x32_bf16 v[60:63], a[20:23], v[104:107], v[60:63]\n\t" "ds_read_b128 v[100:103], v26 offset:29696\n\t" \
   "v_mfma_f32_16x16x32_bf16 v[56:59], a[24:27], v[108:111], v[56:59]\n\t" "v_add_f32 v123, v123, v125\n\t" "v_add_f32 v126, v126, v128\n\t" \
   "v_mfma_f32_16x16x32_bf16 v[60:63], a[28:31], v[108:111], v[60:63]\n\t" "ds_read_b128 v[104:107], v26 offset:30720\n\t" "s_waitcnt lgkmcnt(5)\n\t" \
   "v_mfma_f32_16x16x32_bf16 v[56:59], a[32:35], v[80:83], v[56:59]\n\t" "v_fmac_f32 v48, v32, v8\n\t" "v_add_f32 v127, v127, v129\n\t" \
   "v_mfma_f32_16x16x32_bf16 v[60:63], a[36:39], v[80:83], v[60:63]\n\t" "ds_read_b128 v[108:111], v26 offset:31744\n\t" \
   "v_mfma_f32_16x16x32_bf16 v[56:59], a[40:43], v[84:87], v[56:59]\n\t" "v_fmac_f32 v49, v33, v8\n\t" "v_add_f32 v122, v122, v130\n\t" \
   "v_mfma_f32_16x16x32_bf16 v[60:63], a[44:47], v[84:87], v[60:63]\n\t" "ds_read_b128 v[80:83], v26 offset:32768\n\t" \
   "buffer_load_dwordx4 v16, %[rs], s51 offen offset:3072 sc1 lds\n\t" "s_add_u32 m0, m0, 4096\n\t" "s_add_u32 s51, s51, 4096\n\t" \
   "s_waitcnt lgkmcnt(5)\n\t" "v_mfma_f32_16x16x32_bf16 v[56:59], a[48:51], v[88:91], v[56:59]\n\t" "v_fmac_f32 v50, v34, v8\n\t" \
   "v_add_f32 v123, v123, v131\n\t" "v_mfma_f32_16x16x32_bf16 v[60:63], a[52:55], v[88:91], v[60:63]\n\t" "ds_read_b128 v[84:87], v26 offset:33792\n\t" \
   "v_mfma_f32_16x16x32_bf16 v[56:59], a[56:59], v[92:95], v[56:59]\n\t" "v_fmac_f32 v51, v35, v8\n\t" "v_add_f32 v122, v122, v126\n\t" \
   "v_mfma_f32_16x16x32_bf16 v[60:63], a[60:63], v[92:95], v[60:63]\n\t" "ds_read_b128 v[88:91], v26 offset:34816\n\t" "s_waitcnt lgkmcnt(5)\n\t" \
   "v_mfma_f32_16x16x32_bf16 v[56:59], a[64:67], v[96:99], v[56:59]\n\t" "v_fmac_f32 v52, v36, v8\n\t" "v_add_f32 v123, v123, v127\n\t" \
   "v_mfma_f32_16x16x32_bf16 v[60:63], a[68:71], v[96:99], v[60:63]\n\t" "ds_read_b128 v[92:95], v26 offset:35840\n\t" \
   "v_mfma_f32_16x16x32_bf16 v[56:59], a[72:75], v[100:103], v[56:59]\n\t" "buffer_load_dwordx4 v16, %[rs], s51 offen offset:0 sc1 lds\n\t" \
   "v_mfma_f32_16x16x32_bf16 v[60:63], a[76:79], v[100:103], v[60:63]\n\t" "ds_read_b128 v[96:99], v26 offset:36864\n\t" "s_waitcnt lgkmcnt(5)\n\t" \
   "v_mfma_f32_16x16x32_bf16 v[56:59], a[80:83], v[104:107], v[56:59]\n\t" "v_fmac_f32 v53, v37, v8\n\t" "v_mul_f32 v122, 0xbacccccd, v122\n\t" \
   "v_mfma_f32_16x16x32_bf16 v[60:63], a[84:87], v[104:107], v[60:63]\n\t" "ds_read_b128 v[100:103], v26 offset:37888\n\t" \
   "v_mfma_f32_16x16x32_bf16 v[56:59], a[88:91], v[108:111], v[56:59]\n\t" "v_fmac_f32 v54, v38, v8\n\t" "v_mul_f32 v123, 0x3acccccd, v123\n\t" \
   "v_mfma_f32_16x16x32_bf16 v[60:63], a[92:95], v[108:111], v[60:63]\n\t" "ds_read_b128 v[104:107], v26 offset:38912\n\t" "s_waitcnt lgkmcnt(5)\n\t" \
   "v_mfma_f32_16x16x32_bf16 v[56:59], a[96:99], v[80:83], v[56:59]\n\t" "v_fmac_f32 v55, v39, v8\n\t" "v_fma_f32 v123, -v122, v122, v123\n\t" \
   "v_mfma_f32_16x16x32_bf16 v[60:63], a[100:103], v[80:83], v[60:63]\n\t" "ds_read_b128 v[108:111], v26 offset:39936\n\t" \
   "buffer_load_dwordx4 v16, %[rs], s51 offen offset:1024 sc1 lds\n\t" "s_add_u32 m0, s53, %[ld1]\n\t" "s_nop 0\n\t" \
   "v_mfma_f32_16x16x32_bf16 v[56:59], a[104:107], v[84:87], v[56:59]\n\t" "v_mul_f32 v48, v48, v10\n\t" "v_max_f32 v123, 0, v123\n\t" \
   "v_mfma_f32_16x16x32_bf16 v[60:63], a[108:111], v[84:87], v[60:63]\n\t" "v_mul_f32 v49, v49, v10\n\t" "s_waitcnt lgkmcnt(4)\n\t" \
   "v_mfma_f32_16x16x32_bf16 v[56:59], a[112:115], v[88:91], v[56:59]\n\t" "v_cvt_pk_bf16_f32 v123, v123, v122\n\t" "v_mul_f32 v50, v50, v10\n\t" \
   "v_mfma_f32_16x16x32_bf16 v[60:63], a[116:119], v[88:91], v[60:63]\n\t" "v_and_b32 v12, 0xffff0000, v123\n\t" "v_mul_f32 v51, v51, v10\n\t" \
   "v_mfma_f32_16x16x32_bf16 v[56:59], a[120:123], v[92:95], v[56:59]\n\t" "v_lshlrev_b32 v123, 16, v123\n\t" "v_mul_f32 v52, v52, v10\n\t" \
   "v_mfma_f32_16x16x32_bf16 v[60:63], a[124:127], v[92:95], v[60:63]\n\t" "v_add_f32 v123, 0x3727c5ac, v123\n\t" "s_waitcnt lgkmcnt(2)\n\t" \
   "v_mfma_f32_16x16x32_bf16 v[56:59], a[128:131], v[96:99], v[56:59]\n\t" "v_mul_f32 v53, v53, v10\n\t" "v_cvt_pk_bf16_f32 v123, v123, v123\n\t" \
   "buffer_load_dwordx4 v17, %[rs], s52 offen sc1 lds\n\t" "v_mfma_f32_16x16x32_bf16 v[60:63], a[132:135], v[96:99], v[60:63]\n\t" \
   "v_mul_f32 v54, v54, v10\n\t" "v_lshlrev_b32 v123, 16, v123\n\t" "v_mfma_f32_16x16x32_bf16 v[56:59], a[136:139], v[100:103], v[56:59]\n\t" \
   "v_mul_f32 v55, v55, v10\n\t" "v_cvt_pk_bf16_f32 v136, v48, v49\n\t" "v_mfma_f32_16x16x32_bf16 v[60:63], a[140:143], v[100:103], v[60:63]\n\t" \
   "v_sqrt_f32 v123, v123\n\t" "s_waitcnt lgkmcnt(0)\n\t" "v_mfma_f32_16x16x32_bf16 v[56:59], a[144:147], v[104:107], v[56:59]\n\t" \
   "v_cvt_pk_bf16_f32 v137, v50, v51\n\t" "s_nop 0\n\t" "v_mfma_f32_16x16x32_bf16 v[60:63], a[148:151], v[104:107], v[60:63]\n\t" \
   "v_cvt_pk_bf16_f32 v138, v52, v53\n\t" "v_cvt_pk_bf16_f32 v123, v123, v123\n\t" \
   "v_mfma_f32_16x16x32_bf16 v[56:59], a[152:155], v[108:111], v[56:59]\n\t" "v_cvt_pk_bf16_f32 v139, v54, v55\n\t" "v_lshlrev_b32 v123, 16, v123\n\t" \
   "ds_write_b128 v14, v[136:139] offset:0\n\t" "v_mfma_f32_16x16x32_bf16 v[60:63], a[156:159], v[108:111], v[60:63]\n\t" "v_rcp_f32 v132, v123\n\t" \
   "v_add_u32 v29, s43, v23\n\t" "s_memrealtime s[54:55]\n\t" "s_nop 6\n\t" "v_fmac_f32 v56, v32, v12\n\t" "v_fmac_f32 v57, v33, v12\n\t" \
   "v_fmac_f32 v58, v34, v12\n\t" "v_fmac_f32 v59, v35, v12\n\t" "v_fmac_f32 v60, v36, v12\n\t" "v_fmac_f32 v61, v37, v12\n\t" \
   "v_fmac_f32 v62, v38, v12\n\t" "v_fmac_f32 v63, v39, v12\n\t" "v_mul_f32 v56, v56, v132\n\t" "v_mul_f32 v57, v57, v132\n\t" \
   "v_mul_f32 v58, v58, v132\n\t" "v_mul_f32 v59, v59, v132\n\t" "v_mul_f32 v60, v60, v132\n\t" "v_mul_f32 v61, v61, v132\n\t" \
   "v_mul_f32 v62, v62, v132\n\t" "v_mul_f32 v63, v63, v132\n\t" "v_cvt_pk_bf16_f32 v140, v56, v57\n\t" "v_cvt_pk_bf16_f32 v141, v58, v59\n\t" \
   "v_cvt_pk_bf16_f32 v142, v60, v61\n\t" "v_cvt_pk_bf16_f32 v143, v62, v63\n\t" "ds_write_b128 v14, v[140:143] offset:4096\n\t" \
   "s_waitcnt vmcnt(11) lgkmcnt(0)\n\t" "s_barrier\n\t" "ds_read_b128 v[80:83], v28 offset:0\n\t" "ds_read_b128 v[84:87], v28 offset:1024\n\t" \
   "ds_read_b128 v[88:91], v28 offset:2048\n\t" "ds_read_b128 v[92:95], v28 offset:3072\n\t" "ds_read_b128 v[96:99], v28 offset:4096\n\t" \
   "ds_read_b128 v[100:103], v28 offset:5120\n\t" "ds_read_b128 v[104:107], v28 offset:6144\n\t" "s_add_u32 s45, s40, 3\n\t" \
   "s_min_u32 s45, s45, 63\n\t" "s_mov_b32 s46, s42\n\t" "s_lshr_b32 s48, s45, 1\n\t" "s_bitcmp1_b32 s44, s48\n\t" "s_cbranch_scc0 176f\n\t" "76:\n\t" \
   "s_mul_i32 s52, s45, 1536\n\t" "s_mul_i32 s51, s45, 40960\n\t" "s_add_u32 m0, s46, %[ldx]\n\t" "s_mov_b32 s53, s46\n\t" \
   "buffer_load_dwordx4 v16, %[rs], s51 offen offset:0 sc1 lds\n\t" "s_mov_b32 s42, s43\n\t" "s_add_u32 s43, s43, 44032\n\t" \
   "s_cmp_eq_u32 s43, 132096\n\t" "s_cselect_b32 s43, 0, s43\n\t" "s_add_u32 s41, s41, 1\n\t" "s_add_u32 s40, s40, 1\n\t" \
   "v_xor_b32 v14, 0x2000, v14\n\t" "v_xor_b32 v15, 0x2000, v15\n\t" "v_mov_b32 v31, s54\n\t" "v_min_u32 v31, -2, v31\n\t" "1:\n\t" \
   "v_mov_b32 v26, v28\n\t" "v_mov_b32 v27, v29\n\t" "s_waitcnt lgkmcnt(5)\n\t" "v_mfma_f32_16x16x32_bf16 v[48:51], a[0:3], v[80:83], 0\n\t" \
   "ds_read_b128 v[152:155], v15 offset:0\n\t" "ds_read_b128 v[168:171], v15 offset:4096\n\t" \
   "v_mfma_f32_16x16x32_bf16 v[52:55], a[4:7], v[80:83], 0\n\t" "ds_read_b128 v[108:111], v26 offset:7168\n\t" \
   "ds_read_b128 v[156:159], v15 offset:1024\n\t" "v_mfma_f32_16x16x32_bf16 v[48:51], a[8:11], v[84:87], v[48:51]\n\t" \
   "ds_read_b128 v[172:175], v15 offset:5120\n\t" "v_mfma_f32_16x16x32_bf16 v[52:55], a[12:15], v[84:87], v[52:55]\n\t" \
   "ds_read_b128 v[80:83], v26 offset:8192\n\t" "ds_read_b128 v[160:163], v15 offset:2048\n\t" "s_waitcnt lgkmcnt(10)\n\t" \
   "v_mfma_f32_16x16x32_bf16 v[48:51], a[16:19], v[88:91], v[48:51]\n\t" "ds_read_b128 v[176:179], v15 offset:6144\n\t" \
   "v_mfma_f32_16x16x32_bf16 v[52:55], a[20:23], v[88:91], v[52:55]\n\t" "ds_read_b128 v[84:87], v26 offset:9216\n\t" \
   "ds_read_b128 v[164:167], v15 offset:3072\n\t" "v_mfma_f32_16x16x32_bf16 v[48:51], a[24:27], v[92:95], v[48:51]\n\t" \
   "ds_read_b128 v[180:183], v15 offset:7168\n\t" "v_mfma_f32_16x16x32_bf16 v[52:55], a[28:31], v[92:95], v[52:55]\n\t" \
   "ds_read_b128 v[88:91], v26 offset:10240\n\t" "s_waitcnt lgkmcnt(13)\n\t" "v_mfma_f32_16x16x32_bf16 v[48:51], a[32:35], v[96:99], v[48:51]\n\t" \
   "v_cvt_pk_bf16_f32 v148, v72, v73\n\t" "v_cvt_pk_bf16_f32 v150, v74, v75\n\t" "v_mfma_f32_16x16x32_bf16 v[52:55], a[36:39], v[96:99], v[52:55]\n\t" \
   "ds_read_b128 v[92:95], v26 offset:11264\n\t" "s_waitcnt lgkmcnt(8)\n\t" "v_mfma_f32_16x16x32_bf16 v[64:67], v[152:155], a[160:163], 0\n\t" \
   "v_cvt_pk_bf16_f32 v149, v76, v77\n\t" "v_cvt_pk_bf16_f32 v151, v78, v79\n\t" \
   "v_mfma_f32_16x16x32_bf16 v[48:51], a[40:43], v[100:103], v[48:51]\n\t" "s_add_u32 s50, s49, 294912\n\t" \
   "v_mfma_f32_16x16x32_bf16 v[52:55], a[44:47], v[100:103], v[52:55]\n\t" "ds_read_b128 v[96:99], v26 offset:12288\n\t" \
   "v_mfma_f32_16x16x32_bf16 v[68:71], v[168:171], a[160:163], 0\n\t" "v_mfma_f32_16x16x32_bf16 v[48:51], a[48:51], v[104:107], v[48:51]\n\t" \
   "buffer_store_dwordx4 v[148:151], v18, %[rs], s50 offen sc1\n\t" "v_mfma_f32_16x16x32_bf16 v[52:55], a[52:55], v[104:107], v[52:55]\n\t" \
   "ds_read_b128 v[100:103], v26 offset:13312\n\t" "s_sub_i32 s49, s41, 1\n\t" \
   "v_mfma_f32_16x16x32_bf16 v[64:67], v[156:159], a[164:167], v[64:67]\n\t" "s_mul_i32 s49, s49, 1310720\n\t" \
   "v_mfma_f32_16x16x32_bf16 v[48:51], a[56:59], v[108:111], v[48:51]\n\t" "ds_read_b128 v[112:115], v27 offset:0\n\t" \
   "v_mfma_f32_16x16x32_bf16 v[52:55], a[60:63], v[108:111], v[52:55]\n\t" "ds_read_b128 v[104:107], v26 offset:14336\n\t" \
   "v_mfma_f32_16x16x32_bf16 v[68:71], v[172:175], a[164:167], v[68:71]\n\t" "s_waitcnt lgkmcnt(8)\n\t" \
   "v_mfma_f32_16x16x32_bf16 v[48:51], a[64:67], v[80:83], v[48:51]\n\t" "ds_read_b128 v[116:119], v27 offset:16\n\t" \
   "v_mfma_f32_16x16x32_bf16 v[52:55], a[68:71], v[80:83], v[52:55]\n\t" "ds_read_b128 v[108:111], v26 offset:15360\n\t" \
   "ds_read_b64 v[120:121], v27 offset:32\n\t" "s_waitcnt lgkmcnt(9)\n\t" "v_mfma_f32_16x16x32_bf16 v[64:67], v[160:163], a[168:171], v[64:67]\n\t" \
   "ds_read_b128 v[122:125], v27 offset:768\n\t" "v_mfma_f32_16x16x32_bf16 v[48:51], a[72:75], v[84:87], v[48:51]\n\t" \
   "ds_read_b128 v[126:129], v27 offset:784\n\t" "v_mfma_f32_16x16x32_bf16 v[52:55], a[76:79], v[84:87], v[52:55]\n\t" \
   "ds_read_b128 v[80:83], v26 offset:16384\n\t" "ds_read_b64 v[130:131], v27 offset:800\n\t" \
   "v_mfma_f32_16x16x32_bf16 v[68:71], v[176:179], a[168:171], v[68:71]\n\t" "v_add_u32 v28, s43, v22\n\t" "s_waitcnt lgkmcnt(11)\n\t" \
   "v_mfma_f32_16x16x32_bf16 v[48:51], a[80:83], v[88:91], v[48:51]\n\t" "buffer_load_dwordx4 v16, %[rs], s51 offen offset:1024 sc1 lds\n\t" \
   "v_mfma_f32_16x16x32_bf16 v[52:55], a[84:87], v[88:91], v[52:55]\n\t" "ds_read_b128 v[84:87], v26 offset:17408\n\t" \
   "v_mfma_f32_16x16x32_bf16 v[48:51], a[88:91], v[92:95], v[48:51]\n\t" "s_waitcnt lgkmcnt(5)\n\t" "v_add_f32 v112, v112, v114\n\t" \
   "v_mfma_f32_16x16x32_bf16 v[64:67], v[164:167], a[172:175], v[64:67]\n\t" "v_add_f32 v113, v113, v115\n\t" "v_add_f32 v116, v116, v118\n\t" \
   "v_mfma_f32_16x16x32_bf16 v[52:55], a[92:95], v[92:95], v[52:55]\n\t" "ds_read_b128 v[88:91], v26 offset:18432\n\t" \
   "v_mfma_f32_16x16x32_bf16 v[48:51], a[96:99], v[96:99], v[48:51]\n\t" "v_add_f32 v117, v117, v119\n\t" "v_add_f32 v112, v112, v120\n\t" \
   "v_mfma_f32_16x16x32_bf16 v[68:71], v[180:183], a[172:175], v[68:71]\n\t" "v_add_f32 v113, v113, v121\n\t" "v_add_f32 v112, v112, v116\n\t" \
   "v_mfma_f32_16x16x32_bf16 v[52:55], a[100:103], v[96:99], v[52:55]\n\t" "ds_read_b128 v[92:95], v26 offset:19456\n\t" \
   "v_mfma_f32_16x16x32_bf16 v[48:51], a[104:107], v[100:103], v[48:51]\n\t" "v_add_f32 v113, v113, v117\n\t" "v_mul_f32 v112, 0xbacccccd, v112\n\t" \
   "v_mfma_f32_16x16x32_bf16 v[72:75], v[152:155], a[176:179], 0\n\t" "v_cvt_pk_bf16_f32 v144, v64, v65\n\t" "v_mul_f32 v113, 0x3acccccd, v113\n\t" \
   "v_mfma_f32_16x16x32_bf16 v[52:55], a[108:111], v[100:103], v[52:55]\n\t" "ds_read_b128 v[96:99], v26 offset:20480\n\t" \
   "buffer_load_dwordx4 v16, %[rs], s51 offen offset:2048 sc1 lds\n\t" "v_mfma_f32_16x16x32_bf16 v[48:51], a[112:115], v[104:107], v[48:51]\n\t" \
   "v_cvt_pk_bf16_f32 v146, v66, v67\n\t" "v_cvt_pk_bf16_f32 v145, v68, v69\n\t" "v_mfma_f32_16x16x32_bf16 v[76:79], v[168:171], a[176:179], 0\n\t" \
   "v_fma_f32 v113, -v112, v112, v113\n\t" "v_cvt_pk_bf16_f32 v147, v70, v71\n\t" \
   "v_mfma_f32_16x16x32_bf16 v[52:55], a[116:119], v[104:107], v[52:55]\n\t" "ds_read_b128 v[100:103], v26 offset:21504\n\t" \
   "v_mfma_f32_16x16x32_bf16 v[48:51], a[120:123], v[108:111], v[48:51]\n\t" "s_add_u32 s50, s49, 0\n\t" "v_max_f32 v113, 0, v113\n\t" \
   "v_mfma_f32_16x16x32_bf16 v[72:75], v[156:159], a[180:183], v[72:75]\n\t" "buffer_store_dwordx4 v[144:147], v21, %[rs], s50 offen sc1\n\t" \
   "v_mfma_f32_16x16x32_bf16 v[52:55], a[124:127], v[108:111], v[52:55]\n\t" "ds_read_b128 v[104:107], v26 offset:22528\n\t" "s_waitcnt lgkmcnt(5)\n\t" \
   "v_mfma_f32_16x16x32_bf16 v[48:51], a[128:131], v[80:83], v[48:51]\n\t" "v_cvt_pk_bf16_f32 v113, v113, v112\n\t" \
   "v_and_b32 v8, 0xffff0000, v113\n\t" "v_mfma_f32_16x16x32_bf16 v[52:55], a[132:135], v[80:83], v[52:55]\n\t" \
   "ds_read_b128 v[108:111], v26 offset:23552\n\t" "v_mfma_f32_16x16x32_bf16 v[76:79], v[172:175], a[180:183], v[76:79]\n\t" \
   "v_lshlrev_b32 v113, 16, v113\n\t" "v_add_f32 v113, 0x3727c5ac, v113\n\t" "v_mfma_f32_16x16x32_bf16 v[48:51], a[136:139], v[84:87], v[48:51]\n\t" \
   "buffer_load_dwordx4 v16, %[rs], s51 offen offset:3072 sc1 lds\n\t" "s_add_u32 m0, m0, 4096\n\t" "s_add_u32 s51, s51, 4096\n\t" \
   "v_mfma_f32_16x16x32_bf16 v[52:55], a[140:143], v[84:87], v[52:55]\n\t" "ds_read_b128 v[80:83], v26 offset:24576\n\t" \
   "v_mfma_f32_16x16x32_bf16 v[72:75], v[160:163], a[184:187], v[72:75]\n\t" "v_cvt_pk_bf16_f32 v113, v113, v113\n\t" "s_waitcnt lgkmcnt(5)\n\t" \
   "v_mfma_f32_16x16x32_bf16 v[48:51], a[144:147], v[88:91], v[48:51]\n\t" "v_lshlrev_b32 v113, 16, v113\n\t" "v_add_u32 v29, s43, v23\n\t" \
   "v_mfma_f32_16x16x32_bf16 v[52:55], a[148:151], v[88:91], v[52:55]\n\t" "ds_read_b128 v[84:87], v26 offset:25600\n\t" \
   "v_mfma_f32_16x16x32_bf16 v[76:79], v[176:179], a[184:187], v[76:79]\n\t" "v_sqrt_f32 v113, v113\n\t" \
   "v_mfma_f32_16x16x32_bf16 v[48:51], a[152:155], v[92:95], v[48:51]\n\t" "s_nop 0\n\t" "v_cvt_pk_bf16_f32 v113, v113, v113\n\t" \
   "v_mfma_f32_16x16x32_bf16 v[52:55], a[156:159], v[92:95], v[52:55]\n\t" "ds_read_b128 v[88:91], v26 offset:26624\n\t" \
   "v_mfma_f32_16x16x32_bf16 v[72:75], v[164:167], a[188:191], v[72:75]\n\t" "v_lshlrev_b32 v113, 16, v113\n\t" "v_rcp_f32 v10, v113\n\t" \
   "s_waitcnt lgkmcnt(5)\n\t" "v_mfma_f32_16x16x32_bf16 v[56:59], a[0:3], v[96:99], 0\n\t" \
   "buffer_load_dwordx4 v16, %[rs], s51 offen offset:0 sc1 lds\n\t" "v_mfma_f32_16x16x32_bf16 v[60:63], a[4:7], v[96:99], 0\n\t" \
   "ds_read_b128 v[92:95], v26 offset:27648\n\t" "v_mfma_f32_16x16x32_bf16 v[76:79], v[180:183], a[188:191], v[76:79]\n\t" \
   "v_add_f32 v122, v122, v124\n\t" "v_mfma_f32_16x16x32_bf16 v[56:59], a[8:11], v[100:103], v[56:59]\n\t" "v_add_f32 v123, v123, v125\n\t" \
   "v_add_f32 v126, v126, v128\n\t" "v_mfma_f32_16x16x32_bf16 v[60:63], a[12:15], v[100:103], v[60:63]\n\t" \
   "ds_read_b128 v[96:99], v26 offset:28672\n\t" "s_waitcnt lgkmcnt(5)\n\t" "v_mfma_f32_16x16x32_bf16 v[56:59], a[16:19], v[104:107], v[56:59]\n\t" \
   "v_cvt_pk_bf16_f32 v148, v72, v73\n\t" "v_cvt_pk_bf16_f32 v150, v74, v75\n\t" "v_mfma_f32_16x16x32_bf16 v[64:67], v[152:155], a[192:195], 0\n\t" \
   "v_cvt_pk_bf16_f32 v149, v76, v77\n\t" "v_cvt_pk_bf16_f32 v151, v78, v79\n\t" \
   "v_mfma_f32_16x16x32_bf16 v[60:63], a[20:23], v[104:107], v[60:63]\n\t" "ds_read_b128 v[100:103], v26 offset:29696\n\t" \
   "v_mfma_f32_16x16x32_bf16 v[56:59], a[24:27], v[108:111], v[56:59]\n\t" "s_add_u32 s50, s49, 32768\n\t" "v_fmac_f32 v48, v32, v8\n\t" \
   "v_mfma_f32_16x16x32_bf16 v[68:71], v[168:171], a[192:195], 0\n\t" "buffer_store_dwordx4 v[148:151], v21, %[rs], s50 offen sc1\n\t" \
   "v_mfma_f32_16x16x32_bf16 v[60:63], a[28:31], v[108:111], v[60:63]\n\t" "ds_read_b128 v[104:107], v26 offset:30720\n\t" "s_waitcnt lgkmcnt(5)\n\t" \
   "v_mfma_f32_16x16x32_bf16 v[56:59], a[32:35], v[80:83], v[56:59]\n\t" "buffer_load_dwordx4 v16, %[rs], s51 offen offset:1024 sc1 lds\n\t" \
   "v_mfma_f32_16x16x32_bf16 v[64:67], v[156:159], a[196:199], v[64:67]\n\t" "v_add_f32 v127, v127, v129\n\t" "v_fmac_f32 v49, v33, v8\n\t" \
   "v_mfma_f32_16x16x32_bf16 v[60:63], a[36:39], v[80:83], v[60:63]\n\t" "ds_read_b128 v[108:111], v26 offset:31744\n\t" \
   "v_mfma_f32_16x16x32_bf16 v[56:59], a[40:43], v[84:87], v[56:59]\n\t" "v_add_f32 v122, v122, v130\n\t" "v_fmac_f32 v50, v34, v8\n\t" \
   "v_mfma_f32_16x16x32_bf16 v[68:71], v[172:175], a[196:199], v[68:71]\n\t" "v_add_f32 v123, v123, v131\n\t" "v_fmac_f32 v51, v35, v8\n\t" \
   "v_mfma_f32_16x16x32_bf16 v[60:63], a[44:47], v[84:87], v[60:63]\n\t" "ds_read_b128 v[80:83], v26 offset:32768\n\t" "s_waitcnt lgkmcnt(5)\n\t" \
   "v_mfma_f32_16x16x32_bf16 v[56:59], a[48:51], v[88:91], v[56:59]\n\t" "v_add_f32 v122, v122, v126\n\t" "v_fmac_f32 v52, v36, v8\n\t" \
   "v_mfma_f32_16x16x32_bf16 v[64:67], v[160:163], a[200:203], v[64:67]\n\t" "v_add_f32 v123, v123, v127\n\t" "v_fmac_f32 v53, v37, v8\n\t" \
   "v_mfma_f32_16x16x32_bf16 v[60:63], a[52:55], v[88:91], v[60:63]\n\t" "ds_read_b128 v[84:87], v26 offset:33792\n\t" \
   "v_mfma_f32_16x16x32_bf16 v[56:59], a[56:59], v[92:95], v[56:59]\n\t" "s_cmp_lt_i32 s40, 2\n\t" "s_cselect_b32 s47, 0x7ffffff0, 0\n\t" \
   "v_mfma_f32_16x16x32_bf16 v[68:71], v[176:179], a[200:203], v[68:71]\n\t" "v_or_b32 v30, s47, v20\n\t" "s_sub_i32 s47, s41, 2\n\t" \
   "buffer_load_dwordx4 v16, %[rs], s51 offen offset:2048 sc1 lds\n\t" "v_mfma_f32_16x16x32_bf16 v[60:63], a[60:63], v[92:95], v[60:63]\n\t" \
   "ds_read_b128 v[88:91], v26 offset:34816\n\t" "s_max_i32 s47, s47, 0\n\t" "s_waitcnt lgkmcnt(5)\n\t" \
   "v_mfma_f32_16x16x32_bf16 v[56:59], a[64:67], v[96:99], v[56:59]\n\t" "s_lshl_b32 s47, s47, 12\n\t" "s_waitcnt vmcnt(8)\n\t" \
   "v_mfma_f32_16x16x32_bf16 v[60:63], a[68:71], v[96:99], v[60:63]\n\t" "ds_read_b128 v[92:95], v26 offset:35840\n\t" \
   "buffer_store_dword v31, v30, %[rs], s47 offen sc1\n\t" "v_mfma_f32_16x16x32_bf16 v[64:67], v[164:167], a[204:207], v[64:67]\n\t" \
   "v_fmac_f32 v54, v38, v8\n\t" "v_mul_f32 v122, 0xbacccccd, v122\n\t" "v_mfma_f32_16x16x32_bf16 v[56:59], a[72:75], v[100:103], v[56:59]\n\t" \
   "v_mul_f32 v123, 0x3acccccd, v123\n\t" "v_fmac_f32 v55, v39, v8\n\t" "v_mfma_f32_16x16x32_bf16 v[60:63], a[76:79], v[100:103], v[60:63]\n\t" \
   "ds_read_b128 v[96:99], v26 offset:36864\n\t" "v_mfma_f32_16x16x32_bf16 v[68:71], v[180:183], a[204:207], v[68:71]\n\t" \
   "v_fma_f32 v123, -v122, v122, v123\n\t" "s_waitcnt lgkmcnt(5)\n\t" "v_mfma_f32_16x16x32_bf16 v[56:59], a[80:83], v[104:107], v[56:59]\n\t" \
   "v_mul_f32 v48, v48, v10\n\t" "v_max_f32 v123, 0, v123\n\t" "v_mfma_f32_16x16x32_bf16 v[60:63], a[84:87], v[104:107], v[60:63]\n\t" \
   "ds_read_b128 v[100:103], v26 offset:37888\n\t" "v_mfma_f32_16x16x32_bf16 v[72:75], v[152:155], a[208:211], 0\n\t" \
   "buffer_load_dwordx4 v16, %[rs], s51 offen offset:3072 sc1 lds\n\t" "s_add_u32 m0, m0, 4096\n\t" "s_add_u32 s51, s51, 4096\n\t" \
   "v_mfma_f32_16x16x32_bf16 v[56:59], a[88:91], v[108:111], v[56:59]\n\t" "v_cvt_pk_bf16_f32 v144, v64, v65\n\t" \
   "v_cvt_pk_bf16_f32 v146, v66, v67\n\t" "v_mfma_f32_16x16x32_bf16 v[60:63], a[92:95], v[108:111], v[60:63]\n\t" \
   "ds_read_b128 v[104:107], v26 offset:38912\n\t" "v_mfma_f32_16x16x32_bf16 v[76:79], v[168:171], a[208:211], 0\n\t" \
   "v_cvt_pk_bf16_f32 v145, v68, v69\n\t" "s_waitcnt lgkmcnt(5)\n\t" "v_mfma_f32_16x16x32_bf16 v[56:59], a[96:99], v[80:83], v[56:59]\n\t" \
   "v_cvt_pk_bf16_f32 v147, v70, v71\n\t" "s_add_u32 s50, s49, 65536\n\t" "v_mfma_f32_16x16x32_bf16 v[60:63], a[100:103], v[80:83], v[60:63]\n\t" \
   "ds_read_b128 v[108:111], v26 offset:39936\n\t" "v_mfma_f32_16x16x32_bf16 v[72:75], v[156:159], a[212:215], v[72:75]\n\t" \
   "buffer_store_dwordx4 v[144:147], v21, %[rs], s50 offen sc1\n\t" "v_mfma_f32_16x16x32_bf16 v[56:59], a[104:107], v[84:87], v[56:59]\n\t" \
   "v_mul_f32 v49, v49, v10\n\t" "v_mul_f32 v50, v50, v10\n\t" "v_mfma_f32_16x16x32_bf16 v[60:63], a[108:111], v[84:87], v[60:63]\n\t" \
   "v_cvt_pk_bf16_f32 v123, v123, v122\n\t" "s_waitcnt lgkmcnt(4)\n\t" "v_mfma_f32_16x16x32_bf16 v[56:59], a[112:115], v[88:91], v[56:59]\n\t" \
   "buffer_load_dwordx4 v16, %[rs], s51 offen offset:0 sc1 lds\n\t" "v_mfma_f32_16x16x32_bf16 v[76:79], v[172:175], a[212:215], v[76:79]\n\t" \
   "v_mul_f32 v51, v51, v10\n\t" "v_and_b32 v12, 0xffff0000, v123\n\t" "v_mfma_f32_16x16x32_bf16 v[60:63], a[116:119], v[88:91], v[60:63]\n\t" \
   "v_mul_f32 v52, v52, v10\n\t" "v_lshlrev_b32 v123, 16, v123\n\t" "v_mfma_f32_16x16x32_bf16 v[56:59], a[120:123], v[92:95], v[56:59]\n\t" \
   "v_mul_f32 v53, v53, v10\n\t" "v_add_f32 v123, 0x3727c5ac, v123\n\t" "v_mfma_f32_16x16x32_bf16 v[72:75], v[160:163], a[216:219], v[72:75]\n\t" \
   "v_mul_f32 v54, v54, v10\n\t" "v_cvt_pk_bf16_f32 v123, v123, v123\n\t" "v_mfma_f32_16x16x32_bf16 v[60:63], a[124:127], v[92:95], v[60:63]\n\t" \
   "v_mul_f32 v55, v55, v10\n\t" "s_waitcnt lgkmcnt(2)\n\t" "v_mfma_f32_16x16x32_bf16 v[56:59], a[128:131], v[96:99], v[56:59]\n\t" \
   "v_lshlrev_b32 v123, 16, v123\n\t" "v_cvt_pk_bf16_f32 v136, v48, v49\n\t" "v_mfma_f32_16x16x32_bf16 v[76:79], v[176:179], a[216:219], v[76:79]\n\t" \
   "v_sqrt_f32 v123, v123\n\t" "v_mfma_f32_16x16x32_bf16 v[60:63], a[132:135], v[96:99], v[60:63]\n\t" "v_cvt_pk_bf16_f32 v137, v50, v51\n\t" \
   "s_nop 0\n\t" "v_mfma_f32_16x16x32_bf16 v[56:59], a[136:139], v[100:103], v[56:59]\n\t" "v_cvt_pk_bf16_f32 v138, v52, v53\n\t" \
   "v_cvt_pk_bf16_f32 v123, v123, v123\n\t" "v_mfma_f32_16x16x32_bf16 v[72:75], v[164:167], a[220:223], v[72:75]\n\t" \
   "buffer_load_dwordx4 v16, %[rs], s51 offen offset:1024 sc1 lds\n\t" "s_add_u32 m0, s53, %[ld1]\n\t" "s_nop 0\n\t" \
   "v_mfma_f32_16x16x32_bf16 v[60:63], a[140:143], v[100:103], v[60:63]\n\t" "v_cvt_pk_bf16_f32 v139, v54, v55\n\t" "s_waitcnt lgkmcnt(0)\n\t" \
   "v_mfma_f32_16x16x32_bf16 v[56:59], a[144:147], v[104:107], v[56:59]\n\t" "v_lshlrev_b32 v123, 16, v123\n\t" \
   "v_mfma_f32_16x16x32_bf16 v[76:79], v[180:183], a[220:223], v[76:79]\n\t" "ds_write_b128 v14, v[136:139] offset:0\n\t" \
   "v_mfma_f32_16x16x32_bf16 v[60:63], a[148:151], v[104:107], v[60:63]\n\t" "v_rcp_f32 v132, v123\n\t" \
   "v_mfma_f32_16x16x32_bf16 v[56:59], a[152:155], v[108:111], v[56:59]\n\t" "buffer_load_dwordx4 v17, %[rs], s52 offen sc1 lds\n\t" \
   "v_mfma_f32_16x16x32_bf16 v[60:63], a[156:159], v[108:111], v[60:63]\n\t" "v_cvt_pk_bf16_f32 v148, v72, v73\n\t" \
   "v_cvt_pk_bf16_f32 v150, v74, v75\n\t" "v_mfma_f32_16x16x32_bf16 v[64:67], v[152:155], a[224:227], 0\n\t" "v_cvt_pk_bf16_f32 v149, v76, v77\n\t" \
   "v_cvt_pk_bf16_f32 v151, v78, v79\n\t" "v_mfma_f32_16x16x32_bf16 v[68:71], v[168:171], a[224:227], 0\n\t" "s_add_u32 s50, s49, 98304\n\t" \
   "v_mfma_f32_16x16x32_bf16 v[64:67], v[156:159], a[228:231], v[64:67]\n\t" "v_fmac_f32 v56, v32, v12\n\t" "v_fmac_f32 v57, v33, v12\n\t" \
   "v_mfma_f32_16x16x32_bf16 v[68:71], v[172:175], a[228:231], v[68:71]\n\t" "v_fmac_f32 v58, v34, v12\n\t" "v_fmac_f32 v59, v35, v12\n\t" \
   "v_mfma_f32_16x16x32_bf16 v[64:67], v[160:163], a[232:235], v[64:67]\n\t" "v_fmac_f32 v60, v36, v12\n\t" "v_fmac_f32 v61, v37, v12\n\t" \
   "v_mfma_f32_16x16x32_bf16 v[68:71], v[176:179], a[232:235], v[68:71]\n\t" "v_fmac_f32 v62, v38, v12\n\t" "v_fmac_f32 v63, v39, v12\n\t" \
   "buffer_store_dwordx4 v[148:151], v21, %[rs], s50 offen sc1\n\t" "v_mfma_f32_16x16x32_bf16 v[64:67], v[164:167], a[236:239], v[64:67]\n\t" \
   "v_mul_f32 v56, v56, v132\n\t" "v_mul_f32 v57, v57, v132\n\t" "v_mfma_f32_16x16x32_bf16 v[68:71], v[180:183], a[236:239], v[68:71]\n\t" \
   "v_mul_f32 v58, v58, v132\n\t" "v_mul_f32 v59, v59, v132\n\t" "v_mfma_f32_16x16x32_bf16 v[72:75], v[152:155], a[240:243], 0\n\t" \
   "v_mul_f32 v60, v60, v132\n\t" "v_mul_f32 v61, v61, v132\n\t" "v_mfma_f32_16x16x32_bf16 v[76:79], v[168:171], a[240:243], 0\n\t" \
   "v_mul_f32 v62, v62, v132\n\t" "v_mul_f32 v63, v63, v132\n\t" "v_mfma_f32_16x16x32_bf16 v[72:75], v[156:159], a[244:247], v[72:75]\n\t" \
   "v_cvt_pk_bf16_f32 v144, v64, v65\n\t" "v_cvt_pk_bf16_f32 v146, v66, v67\n\t" \
   "v_mfma_f32_16x16x32_bf16 v[76:79], v[172:175], a[244:247], v[76:79]\n\t" "v_cvt_pk_bf16_f32 v145, v68, v69\n\t" \
   "v_cvt_pk_bf16_f32 v140, v56, v57\n\t" "v_mfma_f32_16x16x32_bf16 v[72:75], v[160:163], a[248:251], v[72:75]\n\t" \
   "v_cvt_pk_bf16_f32 v147, v70, v71\n\t" "v_cvt_pk_bf16_f32 v141, v58, v59\n\t" \
   "v_mfma_f32_16x16x32_bf16 v[76:79], v[176:179], a[248:251], v[76:79]\n\t" "s_add_u32 s50, s49, 131072\n\t" "v_cvt_pk_bf16_f32 v142, v60, v61\n\t" \
   "v_mfma_f32_16x16x32_bf16 v[72:75], v[164:167], a[252:255], v[72:75]\n\t" "v_cvt_pk_bf16_f32 v143, v62, v63\n\t" \
   "v_mfma_f32_16x16x32_bf16 v[76:79], v[180:183], a[252:255], v[76:79]\n\t" "buffer_store_dwordx4 v[144:147], v21, %[rs], s50 offen sc1\n\t" \
   "v_mfma_f32_16x16x32_bf16 v[64:67], v[152:155], v[192:195], 0\n\t" "ds_write_b128 v14, v[140:143] offset:4096\n\t" \
   "v_mfma_f32_16x16x32_bf16 v[68:71], v[168:171], v[192:195], 0\n\t" "v_mfma_f32_16x16x32_bf16 v[64:67], v[156:159], v[196:199], v[64:67]\n\t" \
   "v_cvt_pk_bf16_f32 v148, v72, v73\n\t" "v_cvt_pk_bf16_f32 v150, v74, v75\n\t" \
   "v_mfma_f32_16x16x32_bf16 v[68:71], v[172:175], v[196:199], v[68:71]\n\t" "v_cvt_pk_bf16_f32 v149, v76, v77\n\t" \
   "v_cvt_pk_bf16_f32 v151, v78, v79\n\t" "s_waitcnt vmcnt(16) lgkmcnt(0)\n\t" "s_barrier\n\t" \
   "v_mfma_f32_16x16x32_bf16 v[64:67], v[160:163], v[200:203], v[64:67]\n\t" "s_add_u32 s45, s40, 3\n\t" "s_min_u32 s45, s45, 63\n\t" \
   "v_mfma_f32_16x16x32_bf16 v[68:71], v[176:179], v[200:203], v[68:71]\n\t" "s_add_u32 s50, s49, 163840\n\t" "s_mov_b32 s46, s42\n\t" \
   "v_mfma_f32_16x16x32_bf16 v[64:67], v[164:167], v[204:207], v[64:67]\n\t" "s_lshr_b32 s48, s45, 1\n\t" "s_bitcmp1_b32 s44, s48\n\t" \
   "s_cbranch_scc0 174f\n\t" "74:\n\t" "v_mfma_f32_16x16x32_bf16 v[68:71], v[180:183], v[204:207], v[68:71]\n\t" \
   "buffer_store_dwordx4 v[148:151], v21, %[rs], s50 offen sc1\n\t" "v_mfma_f32_16x16x32_bf16 v[72:75], v[152:155], v[208:211], 0\n\t" \
   "s_mul_i32 s52, s45, 1536\n\t" "s_mul_i32 s51, s45, 40960\n\t" "s_add_u32 m0, s46, %[ldx]\n\t" "s_mov_b32 s53, s46\n\t" \
   "v_mfma_f32_16x16x32_bf16 v[76:79], v[168:171], v[208:211], 0\n\t" "buffer_load_dwordx4 v16, %[rs], s51 offen offset:0 sc1 lds\n\t" \
   "v_mfma_f32_16x16x32_bf16 v[72:75], v[156:159], v[212:215], v[72:75]\n\t" "v_cvt_pk_bf16_f32 v144, v64, v65\n\t" \
   "v_cvt_pk_bf16_f32 v146, v66, v67\n\t" "v_mfma_f32_16x16x32_bf16 v[76:79], v[172:175], v[212:215], v[76:79]\n\t" \
   "v_cvt_pk_bf16_f32 v145, v68, v69\n\t" "v_cvt_pk_bf16_f32 v147, v70, v71\n\t" \
   "v_mfma_f32_16x16x32_bf16 v[72:75], v[160:163], v[216:219], v[72:75]\n\t" "ds_read_b128 v[80:83], v28 offset:0\n\t" \
   "v_mfma_f32_16x16x32_bf16 v[76:79], v[176:179], v[216:219], v[76:79]\n\t" "s_add_u32 s50, s49, 196608\n\t" "s_mov_b32 s42, s43\n\t" \
   "v_mfma_f32_16x16x32_bf16 v[72:75], v[164:167], v[220:223], v[72:75]\n\t" "ds_read_b128 v[84:87], v28 offset:1024\n\t" \
   "v_mfma_f32_16x16x32_bf16 v[76:79], v[180:183], v[220:223], v[76:79]\n\t" "buffer_store_dwordx4 v[144:147], v21, %[rs], s50 offen sc1\n\t" \
   "v_mfma_f32_16x16x32_bf16 v[64:67], v[152:155], v[224:227], 0\n\t" "ds_read_b128 v[88:91], v28 offset:2048\n\t" \
   "v_mfma_f32_16x16x32_bf16 v[68:71], v[168:171], v[224:227], 0\n\t" "s_add_u32 s43, s43, 44032\n\t" \
   "v_mfma_f32_16x16x32_bf16 v[64:67], v[156:159], v[228:231], v[64:67]\n\t" "v_cvt_pk_bf16_f32 v148, v72, v73\n\t" \
   "v_cvt_pk_bf16_f32 v150, v74, v75\n\t" "v_mfma_f32_16x16x32_bf16 v[68:71], v[172:175], v[228:231], v[68:71]\n\t" \
   "v_cvt_pk_bf16_f32 v149, v76, v77\n\t" "v_cvt_pk_bf16_f32 v151, v78, v79\n\t" \
   "v_mfma_f32_16x16x32_bf16 v[64:67], v[160:163], v[232:235], v[64:67]\n\t" "s_cmp_eq_u32 s43, 132096\n\t" "s_cselect_b32 s43, 0, s43\n\t" \
   "v_mfma_f32_16x16x32_bf16 v[68:71], v[176:179], v[232:235], v[68:71]\n\t" "ds_read_b128 v[92:95], v28 offset:3072\n\t" \
   "v_mfma_f32_16x16x32_bf16 v[64:67], v[164:167], v[236:239], v[64:67]\n\t" "s_add_u32 s50, s49, 229376\n\t" "s_add_u32 s41, s41, 1\n\t" \
   "v_mfma_f32_16x16x32_bf16 v[68:71], v[180:183], v[236:239], v[68:71]\n\t" "buffer_store_dwordx4 v[148:151], v21, %[rs], s50 offen sc1\n\t" \
   "v_mfma_f32_16x16x32_bf16 v[72:75], v[152:155], v[240:243], 0\n\t" "ds_read_b128 v[96:99], v28 offset:4096\n\t" \
   "v_mfma_f32_16x16x32_bf16 v[76:79], v[168:171], v[240:243], 0\n\t" "s_add_u32 s40, s40, 1\n\t" "v_xor_b32 v14, 0x2000, v14\n\t" \
   "v_mfma_f32_16x16x32_bf16 v[72:75], v[156:159], v[244:247], v[72:75]\n\t" "v_cvt_pk_bf16_f32 v144, v64, v65\n\t" \
   "v_cvt_pk_bf16_f32 v146, v66, v67\n\t" "v_mfma_f32_16x16x32_bf16 v[76:79], v[172:175], v[244:247], v[76:79]\n\t" \
   "v_cvt_pk_bf16_f32 v145, v68, v69\n\t" "v_cvt_pk_bf16_f32 v147, v70, v71\n\t" \
   "v_mfma_f32_16x16x32_bf16 v[72:75], v[160:163], v[248:251], v[72:75]\n\t" "ds_read_b128 v[100:103], v28 offset:5120\n\t" \
   "v_mfma_f32_16x16x32_bf16 v[76:79], v[176:179], v[248:251], v[76:79]\n\t" "s_add_u32 s50, s49, 262144\n\t" "v_xor_b32 v15, 0x2000, v15\n\t" \
   "v_mfma_f32_16x16x32_bf16 v[72:75], v[164:167], v[252:255], v[72:75]\n\t" "ds_read_b128 v[104:107], v28 offset:6144\n\t" \
   "buffer_store_dwordx4 v[144:147], v21, %[rs], s50 offen sc1\n\t" "v_mfma_f32_16x16x32_bf16 v[76:79], v[180:183], v[252:255], v[76:79]\n\t" \
   "v_mov_b32 v18, v21\n\t" "s_cmp_lt_u32 s40, 64\n\t" "s_cbranch_scc1 1b\n\t" "s_nop 7\n\t" "s_waitcnt lgkmcnt(0)\n\t" \
   "v_cvt_pk_bf16_f32 v148, v72, v73\n\t" "v_cvt_pk_bf16_f32 v150, v74, v75\n\t" "v_cvt_pk_bf16_f32 v149, v76, v77\n\t" \
   "v_cvt_pk_bf16_f32 v151, v78, v79\n\t" "s_add_u32 s50, s49, 294912\n\t" "buffer_store_dwordx4 v[148:151], v21, %[rs], s50 offen sc1\n\t" \
   "s_sub_i32 s49, s41, 1\n\t" "s_mul_i32 s49, s49, 1310720\n\t" "ds_read_b128 v[152:155], v15 offset:0\n\t" \
   "ds_read_b128 v[168:171], v15 offset:4096\n\t" "ds_read_b128 v[156:159], v15 offset:1024\n\t" "ds_read_b128 v[172:175], v15 offset:5120\n\t" \
   "ds_read_b128 v[160:163], v15 offset:2048\n\t" "ds_read_b128 v[176:179], v15 offset:6144\n\t" "ds_read_b128 v[164:167], v15 offset:3072\n\t" \
   "ds_read_b128 v[180:183], v15 offset:7168\n\t" "s_waitcnt lgkmcnt(4)\n\t" "v_mfma_f32_16x16x32_bf16 v[64:67], v[152:155], a[160:163], 0\n\t" \
   "v_mfma_f32_16x16x32_bf16 v[68:71], v[168:171], a[160:163], 0\n\t" "v_mfma_f32_16x16x32_bf16 v[64:67], v[156:159], a[164:167], v[64:67]\n\t" \
   "v_mfma_f32_16x16x32_bf16 v[68:71], v[172:175], a[164:167], v[68:71]\n\t" "s_waitcnt lgkmcnt(0)\n\t" \
   "v_mfma_f32_16x16x32_bf16 v[64:67], v[160:163], a[168:171], v[64:67]\n\t" "v_mfma_f32_16x16x32_bf16 v[68:71], v[176:179], a[168:171], v[68:71]\n\t" \
   "v_mfma_f32_16x16x32_bf16 v[64:67], v[164:167], a[172:175], v[64:67]\n\t" "v_mfma_f32_16x16x32_bf16 v[68:71], v[180:183], a[172:175], v[68:71]\n\t" \
   "v_mfma_f32_16x16x32_bf16 v[72:75], v[152:155], a[176:179], 0\n\t" "v_mfma_f32_16x16x32_bf16 v[76:79], v[168:171], a[176:179], 0\n\t" \
   "v_mfma_f32_16x16x32_bf16 v[72:75], v[156:159], a[180:183], v[72:75]\n\t" "v_cvt_pk_bf16_f32 v144, v64, v65\n\t" \
   "v_cvt_pk_bf16_f32 v146, v66, v67\n\t" "v_mfma_f32_16x16x32_bf16 v[76:79], v[172:175], a[180:183], v[76:79]\n\t" \
   "v_cvt_pk_bf16_f32 v145, v68, v69\n\t" "v_cvt_pk_bf16_f32 v147, v70, v71\n\t" \
   "v_mfma_f32_16x16x32_bf16 v[72:75], v[160:163], a[184:187], v[72:75]\n\t" "s_add_u32 s50, s49, 0\n\t" \
   "v_mfma_f32_16x16x32_bf16 v[76:79], v[176:179], a[184:187], v[76:79]\n\t" "buffer_store_dwordx4 v[144:147], v21, %[rs], s50 offen sc1\n\t" \
   "v_mfma_f32_16x16x32_bf16 v[72:75], v[164:167], a[188:191], v[72:75]\n\t" "v_mfma_f32_16x16x32_bf16 v[76:79], v[180:183], a[188:191], v[76:79]\n\t" \
   "v_mfma_f32_16x16x32_bf16 v[64:67], v[152:155], a[192:195], 0\n\t" "v_mfma_f32_16x16x32_bf16 v[68:71], v[168:171], a[192:195], 0\n\t" \
   "v_mfma_f32_16x16x32_bf16 v[64:67], v[156:159], a[196:199], v[64:67]\n\t" "v_cvt_pk_bf16_f32 v148, v72, v73\n\t" \
   "v_cvt_pk_bf16_f32 v150, v74, v75\n\t" "v_mfma_f32_16x16x32_bf16 v[68:71], v[172:175], a[196:199], v[68:71]\n\t" \
   "v_cvt_pk_bf16_f32 v149, v76, v77\n\t" "v_cvt_pk_bf16_f32 v151, v78, v79\n\t" \
   "v_mfma_f32_16x16x32_bf16 v[64:67], v[160:163], a[200:203], v[64:67]\n\t" "s_add_u32 s50, s49, 32768\n\t" \
   "v_mfma_f32_16x16x32_bf16 v[68:71], v[176:179], a[200:203], v[68:71]\n\t" "buffer_store_dwordx4 v[148:151], v21, %[rs], s50 offen sc1\n\t" \
   "v_mfma_f32_16x16x32_bf16 v[64:67], v[164:167], a[204:207], v[64:67]\n\t" "v_mfma_f32_16x16x32_bf16 v[68:71], v[180:183], a[204:207], v[68:71]\n\t" \
   "v_mfma_f32_16x16x32_bf16 v[72:75], v[152:155], a[208:211], 0\n\t" "v_mfma_f32_16x16x32_bf16 v[76:79], v[168:171], a[208:211], 0\n\t" \
   "v_mfma_f32_16x16x32_bf16 v[72:75], v[156:159], a[212:215], v[72:75]\n\t" "v_cvt_pk_bf16_f32 v144, v64, v65\n\t" \
   "v_cvt_pk_bf16_f32 v146, v66, v67\n\t" "v_mfma_f32_16x16x32_bf16 v[76:79], v[172:175], a[212:215], v[76:79]\n\t" \
   "v_cvt_pk_bf16_f32 v145, v68, v69\n\t" "v_cvt_pk_bf16_f32 v147, v70, v71\n\t" \
   "v_mfma_f32_16x16x32_bf16 v[72:75], v[160:163], a[216:219], v[72:75]\n\t" "s_add_u32 s50, s49, 65536\n\t" \
   "v_mfma_f32_16x16x32_bf16 v[76:79], v[176:179], a[216:219], v[76:79]\n\t" "buffer_store_dwordx4 v[144:147], v21, %[rs], s50 offen sc1\n\t" \
   "v_mfma_f32_16x16x32_bf16 v[72:75], v[164:167], a[220:223], v[72:75]\n\t" "v_mfma_f32_16x16x32_bf16 v[76:79], v[180:183], a[220:223], v[76:79]\n\t" \
   "v_mfma_f32_16x16x32_bf16 v[64:67], v[152:155], a[224:227], 0\n\t" "v_mfma_f32_16x16x32_bf16 v[68:71], v[168:171], a[224:227], 0\n\t" \
   "v_mfma_f32_16x16x32_bf16 v[64:67], v[156:159], a[228:231], v[64:67]\n\t" "v_cvt_pk_bf16_f32 v148, v72, v73\n\t" \
   "v_cvt_pk_bf16_f32 v150, v74, v75\n\t" "v_mfma_f32_16x16x32_bf16 v[68:71], v[172:175], a[228:231], v[68:71]\n\t" \
   "v_cvt_pk_bf16_f32 v149, v76, v77\n\t" "v_cvt_pk_bf16_f32 v151, v78, v79\n\t" \
   "v_mfma_f32_16x16x32_bf16 v[64:67], v[160:163], a[232:235], v[64:67]\n\t" "s_add_u32 s50, s49, 98304\n\t" \
   "v_mfma_f32_16x16x32_bf16 v[68:71], v[176:179], a[232:235], v[68:71]\n\t" "buffer_store_dwordx4 v[148:151], v21, %[rs], s50 offen sc1\n\t" \
   "v_mfma_f32_16x16x32_bf16 v[64:67], v[164:167], a[236:239], v[64:67]\n\t" "v_mfma_f32_16x16x32_bf16 v[68:71], v[180:183], a[236:239], v[68:71]\n\t" \
   "v_mfma_f32_16x16x32_bf16 v[72:75], v[152:155], a[240:243], 0\n\t" "v_mfma_f32_16x16x32_bf16 v[76:79], v[168:171], a[240:243], 0\n\t" \
   "v_mfma_f32_16x16x32_bf16 v[72:75], v[156:159], a[244:247], v[72:75]\n\t" "v_cvt_pk_bf16_f32 v144, v64, v65\n\t" \
   "v_cvt_pk_bf16_f32 v146, v66, v67\n\t" "v_mfma_f32_16x16x32_bf16 v[76:79], v[172:175], a[244:247], v[76:79]\n\t" \
   "v_cvt_pk_bf16_f32 v145, v68, v69\n\t" "v_cvt_pk_bf16_f32 v147, v70, v71\n\t" \
   "v_mfma_f32_16x16x32_bf16 v[72:75], v[160:163], a[248:251], v[72:75]\n\t" "s_add_u32 s50, s49, 131072\n\t" \
   "v_mfma_f32_16x16x32_bf16 v[76:79], v[176:179], a[248:251], v[76:79]\n\t" "buffer_store_dwordx4 v[144:147], v21, %[rs], s50 offen sc1\n\t" \
   "v_mfma_f32_16x16x32_bf16 v[72:75], v[164:167], a[252:255], v[72:75]\n\t" "v_mfma_f32_16x16x32_bf16 v[76:79], v[180:183], a[252:255], v[76:79]\n\t" \
   "v_mfma_f32_16x16x32_bf16 v[64:67], v[152:155], v[192:195], 0\n\t" "v_mfma_f32_16x16x32_bf16 v[68:71], v[168:171], v[192:195], 0\n\t" \
   "v_mfma_f32_16x16x32_bf16 v[64:67], v[156:159], v[196:199], v[64:67]\n\t" "v_cvt_pk_bf16_f32 v148, v72, v73\n\t" \
   "v_cvt_pk_bf16_f32 v150, v74, v75\n\t" "v_mfma_f32_16x16x32_bf16 v[68:71], v[172:175], v[196:199], v[68:71]\n\t" \
   "v_cvt_pk_bf16_f32 v149, v76, v77\n\t" "v_cvt_pk_bf16_f32 v151, v78, v79\n\t" \
   "v_mfma_f32_16x16x32_bf16 v[64:67], v[160:163], v[200:203], v[64:67]\n\t" "s_add_u32 s50, s49, 163840\n\t" \
   "v_mfma_f32_16x16x32_bf16 v[68:71], v[176:179], v[200:203], v[68:71]\n\t" "buffer_store_dwordx4 v[148:151], v21, %[rs], s50 offen sc1\n\t" \
   "v_mfma_f32_16x16x32_bf16 v[64:67], v[164:167], v[204:207], v[64:67]\n\t" "v_mfma_f32_16x16x32_bf16 v[68:71], v[180:183], v[204:207], v[68:71]\n\t" \
   "v_mfma_f32_16x16x32_bf16 v[72:75], v[152:155], v[208:211], 0\n\t" "v_mfma_f32_16x16x32_bf16 v[76:79], v[168:171], v[208:211], 0\n\t" \
   "v_mfma_f32_16x16x32_bf16 v[72:75], v[156:159], v[212:215], v[72:75]\n\t" "v_cvt_pk_bf16_f32 v144, v64, v65\n\t" \
   "v_cvt_pk_bf16_f32 v146, v66, v67\n\t" "v_mfma_f32_16x16x32_bf16 v[76:79], v[172:175], v[212:215], v[76:79]\n\t" \
   "v_cvt_pk_bf16_f32 v145, v68, v69\n\t" "v_cvt_pk_bf16_f32 v147, v70, v71\n\t" \
   "v_mfma_f32_16x16x32_bf16 v[72:75], v[160:163], v[216:219], v[72:75]\n\t" "s_add_u32 s50, s49, 196608\n\t" \
   "v_mfma_f32_16x16x32_bf16 v[76:79], v[176:179], v[216:219], v[76:79]\n\t" "buffer_store_dwordx4 v[144:147], v21, %[rs], s50 offen sc1\n\t" \
   "v_mfma_f32_16x16x32_bf16 v[72:75], v[164:167], v[220:223], v[72:75]\n\t" "v_mfma_f32_16x16x32_bf16 v[76:79], v[180:183], v[220:223], v[76:79]\n\t" \
   "v_mfma_f32_16x16x32_bf16 v[64:67], v[152:155], v[224:227], 0\n\t" "v_mfma_f32_16x16x32_bf16 v[68:71], v[168:171], v[224:227], 0\n\t" \
   "v_mfma_f32_16x16x32_bf16 v[64:67], v[156:159], v[228:231], v[64:67]\n\t" "v_cvt_pk_bf16_f32 v148, v72, v73\n\t" \
   "v_cvt_pk_bf16_f32 v150, v74, v75\n\t" "v_mfma_f32_16x16x32_bf16 v[68:71], v[172:175], v[228:231], v[68:71]\n\t" \
   "v_cvt_pk_bf16_f32 v149, v76, v77\n\t" "v_cvt_pk_bf16_f32 v151, v78, v79\n\t" \
   "v_mfma_f32_16x16x32_bf16 v[64:67], v[160:163], v[232:235], v[64:67]\n\t" "s_add_u32 s50, s49, 229376\n\t" \
   "v_mfma_f32_16x16x32_bf16 v[68:71], v[176:179], v[232:235], v[68:71]\n\t" "buffer_store_dwordx4 v[148:151], v21, %[rs], s50 offen sc1\n\t" \
   "v_mfma_f32_16x16x32_bf16 v[64:67], v[164:167], v[236:239], v[64:67]\n\t" "v_mfma_f32_16x16x32_bf16 v[68:71], v[180:183], v[236:239], v[68:71]\n\t" \
   "v_mfma_f32_16x16x32_bf16 v[72:75], v[152:155], v[240:243], 0\n\t" "v_mfma_f32_16x16x32_bf16 v[76:79], v[168:171], v[240:243], 0\n\t" \
   "v_mfma_f32_16x16x32_bf16 v[72:75], v[156:159], v[244:247], v[72:75]\n\t" "v_cvt_pk_bf16_f32 v144, v64, v65\n\t" \
   "v_cvt_pk_bf16_f32 v146, v66, v67\n\t" "v_mfma_f32_16x16x32_bf16 v[76:79], v[172:175], v[244:247], v[76:79]\n\t" \
   "v_cvt_pk_bf16_f32 v145, v68, v69\n\t" "v_cvt_pk_bf16_f32 v147, v70, v71\n\t" \
   "v_mfma_f32_16x16x32_bf16 v[72:75], v[160:163], v[248:251], v[72:75]\n\t" "s_add_u32 s50, s49, 262144\n\t" \
   "v_mfma_f32_16x16x32_bf16 v[76:79], v[176:179], v[248:251], v[76:79]\n\t" "buffer_store_dwordx4 v[144:147], v21, %[rs], s50 offen sc1\n\t" \
   "v_mfma_f32_16x16x32_bf16 v[72:75], v[164:167], v[252:255], v[72:75]\n\t" "v_mfma_f32_16x16x32_bf16 v[76:79], v[180:183], v[252:255], v[76:79]\n\t" \
   "s_nop 7\n\t" "v_cvt_pk_bf16_f32 v148, v72, v73\n\t" "v_cvt_pk_bf16_f32 v150, v74, v75\n\t" "v_cvt_pk_bf16_f32 v149, v76, v77\n\t" \
   "v_cvt_pk_bf16_f32 v151, v78, v79\n\t" "s_add_u32 s50, s49, 294912\n\t" "buffer_store_dwordx4 v[148:151], v21, %[rs], s50 offen sc1\n\t" \
   "s_waitcnt vmcnt(0)\n\t" "s_cmp_lt_i32 s40, 2\n\t" "s_cselect_b32 s47, 0x7ffffff0, 0\n\t" "v_or_b32 v30, s47, v20\n\t" "s_sub_i32 s47, s41, 2\n\t" \
   "s_max_i32 s47, s47, 0\n\t" "s_lshl_b32 s47, s47, 12\n\t" "buffer_store_dword v31, v30, %[rs], s47 offen sc1\n\t" "s_cmp_lt_i32 s40, 1\n\t" \
   "s_cselect_b32 s47, 0x7ffffff0, 0\n\t" "v_or_b32 v30, s47, v20\n\t" "s_sub_i32 s47, s41, 1\n\t" "s_max_i32 s47, s47, 0\n\t" \
   "s_lshl_b32 s47, s47, 12\n\t" "buffer_store_dword v31, v30, %[rs], s47 offen sc1\n\t" "s_branch 9f\n\t" "172:\n\t" "s_mov_b32 s47, 0\n\t" \
   "buffer_load_dword v30, v19, %[rs], 0 offen sc1\n\t" "s_waitcnt vmcnt(0)\n\t" "v_add_u32 v30, 1, v30\n\t" "v_cmp_lt_u32 vcc, 9, v30\n\t" \
   "s_mov_b32 s44, vcc_lo\n\t" "s_bitcmp1_b32 s44, s48\n\t" "s_cbranch_scc1 72b\n\t" "s_bitset1_b32 s44, s48\n\t" "s_lshl_b32 s48, s48, 7\n\t" \
   "s_add_u32 s48, s48, %[cnt]\n\t" "73:\n\t" "s_sleep 10\n\t" "buffer_load_dword v30, off, %[rs], s48 sc1\n\t" "s_add_u32 s47, s47, 1\n\t" \
   "s_waitcnt vmcnt(0)\n\t" "v_add_u32 v30, 1, v30\n\t" "v_cmp_lt_u32 vcc, 9, v30\n\t" "s_cmp_gt_u32 s47, 0x20000\n\t" "s_cbranch_scc1 72b\n\t" \
   "s_cbranch_vccz 73b\n\t" "s_branch 72b\n\t" "176:\n\t" "s_mov_b32 s47, 0\n\t" "buffer_load_dword v30, v19, %[rs], 0 offen sc1\n\t" \
   "s_waitcnt vmcnt(0)\n\t" "v_add_u32 v30, 1, v30\n\t" "v_cmp_lt_u32 vcc, 9, v30\n\t" "s_mov_b32 s44, vcc_lo\n\t" "s_bitcmp1_b32 s44, s48\n\t" \
   "s_cbranch_scc1 76b\n\t" "s_bitset1_b32 s44, s48\n\t" "s_lshl_b32 s48, s48, 7\n\t" "s_add_u32 s48, s48, %[cnt]\n\t" "77:\n\t" "s_sleep 10\n\t" \
   "buffer_load_dword v30, off, %[rs], s48 sc1\n\t" "s_add_u32 s47, s47, 1\n\t" "s_waitcnt vmcnt(0)\n\t" "v_add_u32 v30, 1, v30\n\t" \
   "v_cmp_lt_u32 vcc, 9, v30\n\t" "s_cmp_gt_u32 s47, 0x20000\n\t" "s_cbranch_scc1 76b\n\t" "s_cbranch_vccz 77b\n\t" "s_branch 76b\n\t" "174:\n\t" \
   "s_mov_b32 s47, 0\n\t" "buffer_load_dword v30, v19, %[rs], 0 offen sc1\n\t" "s_waitcnt vmcnt(0)\n\t" "v_add_u32 v30, 1, v30\n\t" \
   "v_cmp_lt_u32 vcc, 9, v30\n\t" "s_mov_b32 s44, vcc_lo\n\t" "s_bitcmp1_b32 s44, s48\n\t" "s_cbranch_scc1 74b\n\t" "s_bitset1_b32 s44, s48\n\t" \
   "s_lshl_b32 s48, s48, 7\n\t" "s_add_u32 s48, s48, %[cnt]\n\t" "75:\n\t" "s_sleep 10\n\t" "buffer_load_dword v30, off, %[rs], s48 sc1\n\t" \
   "s_add_u32 s47, s47, 1\n\t" "s_waitcnt vmcnt(0)\n\t" "v_add_u32 v30, 1, v30\n\t" "v_cmp_lt_u32 vcc, 9, v30\n\t" "s_cmp_gt_u32 s47, 0x20000\n\t" \
   "s_cbranch_scc1 74b\n\t" "s_cbranch_vccz 75b\n\t" "s_branch 74b\n\t" "9:\n\t" "s_waitcnt vmcnt(0)"
#define VU_CLOB "v8", "v9", "v10", "v11", "v12", "v13", "v14", "v15", "v32", "v33", "v34", "v35", "v36", "v37", "v38", "v39", "v40", "v41", "v42", "v43", "v44", "v45", "v46", "v47", "v48", "v49", "v50", "v51", "v52", "v53", "v54", "v55", "v56", "v57", "v58", "v59", "v60", "v61", "v62", "v63", "v64", "v65", "v66", "v67", "v68", "v69", "v70", "v71", "v72", "v73", "v74", "v75", "v76", "v77", "v78", "v79", "v80", "v81", "v82", "v83", "v84", "v85", "v86", "v87", "v88", "v89", "v90", "v91", "v92", "v93", "v94", "v95", "v96", "v97", "v98", "v99", "v100", "v101", "v102", "v103", "v104", "v105", "v106", "v107", "v108", "v109", "v110", "v111", "v112", "v113", "v114", "v115", "v116", "v117", "v118", "v119", "v120", "v121", "v122", "v123", "v124", "v125", "v126", "v127", "v128", "v129", "v130", "v131", "v132", "v133", "v134", "v135", "v136", "v137", "v138", "v139", "v140", "v141", "v142", "v143", "v144", "v145", "v146", "v147", "v148", "v149", "v150", "v151", "v152", "v153", "v154", "v155", "v156", "v157", "v158", "v159", "v160", "v161", "v162", "v163", "v164", "v165", "v166", "v167", "v168", "v169", "v170", "v171", "v172", "v173", "v174", "v175", "v176", "v177", "v178", "v179", "v180", "v181", "v182", "v183", "s40", "s41", "s42", "s43", "s44", "s45", "s46", "s47", "s48", "s49", "s50", "s51", "s52", "s53", "s54", "s55"
#define DF_ASM \
   "v_mov_b32 v80, 0x3f803f80\n\t" "v_mov_b32 v81, 0x3f803f80\n\t" "v_mov_b32 v82, 0x3f803f80\n\t" "v_mov_b32 v83, 0x3f803f80\n\t" \
   "s_mov_b32 s45, %[set]\n\t" "s_mov_b32 s46, 0\n\t" "v_mfma_f32_16x16x32_bf16 v[32:35], a[0:3], v[80:83], 0\n\t" \
   "v_mfma_f32_16x16x32_bf16 v[32:35], a[4:7], v[80:83], v[32:35]\n\t" "v_mfma_f32_16x16x32_bf16 v[32:35], a[8:11], v[80:83], v[32:35]\n\t" \
   "v_mfma_f32_16x16x32_bf16 v[32:35], a[12:15], v[80:83], v[32:35]\n\t" "v_mfma_f32_16x16x32_bf16 v[32:35], a[16:19], v[80:83], v[32:35]\n\t" \
   "v_mfma_f32_16x16x32_bf16 v[32:35], a[20:23], v[80:83], v[32:35]\n\t" "v_mfma_f32_16x16x32_bf16 v[32:35], a[24:27], v[80:83], v[32:35]\n\t" \
   "v_mfma_f32_16x16x32_bf16 v[32:35], a[28:31], v[80:83], v[32:35]\n\t" "v_mfma_f32_16x16x32_bf16 v[32:35], a[32:35], v[80:83], v[32:35]\n\t" \
   "v_mfma_f32_16x16x32_bf16 v[32:35], a[36:39], v[80:83], v[32:35]\n\t" "v_mfma_f32_16x16x32_bf16 v[32:35], a[40:43], v[80:83], v[32:35]\n\t" \
   "v_mfma_f32_16x16x32_bf16 v[32:35], a[44:47], v[80:83], v[32:35]\n\t" "v_mfma_f32_16x16x32_bf16 v[32:35], a[48:51], v[80:83], v[32:35]\n\t" \
   "v_mfma_f32_16x16x32_bf16 v[32:35], a[52:55], v[80:83], v[32:35]\n\t" "v_mfma_f32_16x16x32_bf16 v[32:35], a[56:59], v[80:83], v[32:35]\n\t" \
   "v_mfma_f32_16x16x32_bf16 v[32:35], a[60:63], v[80:83], v[32:35]\n\t" "v_mfma_f32_16x16x32_bf16 v[32:35], a[64:67], v[80:83], v[32:35]\n\t" \
   "v_mfma_f32_16x16x32_bf16 v[32:35], a[68:71], v[80:83], v[32:35]\n\t" "v_mfma_f32_16x16x32_bf16 v[32:35], a[72:75], v[80:83], v[32:35]\n\t" \
   "v_mfma_f32_16x16x32_bf16 v[32:35], a[76:79], v[80:83], v[32:35]\n\t" "v_mfma_f32_16x16x32_bf16 v[36:39], a[80:83], v[80:83], 0\n\t" \
   "v_mfma_f32_16x16x32_bf16 v[36:39], a[84:87], v[80:83], v[36:39]\n\t" "v_mfma_f32_16x16x32_bf16 v[36:39], a[88:91], v[80:83], v[36:39]\n\t" \
   "v_mfma_f32_16x16x32_bf16 v[36:39], a[92:95], v[80:83], v[36:39]\n\t" "v_mfma_f32_16x16x32_bf16 v[36:39], a[96:99], v[80:83], v[36:39]\n\t" \
   "v_mfma_f32_16x16x32_bf16 v[36:39], a[100:103], v[80:83], v[36:39]\n\t" "v_mfma_f32_16x16x32_bf16 v[36:39], a[104:107], v[80:83], v[36:39]\n\t" \
   "v_mfma_f32_16x16x32_bf16 v[36:39], a[108:111], v[80:83], v[36:39]\n\t" "v_mfma_f32_16x16x32_bf16 v[36:39], a[112:115], v[80:83], v[36:39]\n\t" \
   "v_mfma_f32_16x16x32_bf16 v[36:39], a[116:119], v[80:83], v[36:39]\n\t" "v_mfma_f32_16x16x32_bf16 v[36:39], a[120:123], v[80:83], v[36:39]\n\t" \
   "v_mfma_f32_16x16x32_bf16 v[36:39], a[124:127], v[80:83], v[36:39]\n\t" "v_mfma_f32_16x16x32_bf16 v[36:39], a[128:131], v[80:83], v[36:39]\n\t" \
   "v_mfma_f32_16x16x32_bf16 v[36:39], a[132:135], v[80:83], v[36:39]\n\t" "v_mfma_f32_16x16x32_bf16 v[36:39], a[136:139], v[80:83], v[36:39]\n\t" \
   "v_mfma_f32_16x16x32_bf16 v[36:39], a[140:143], v[80:83], v[36:39]\n\t" "v_mfma_f32_16x16x32_bf16 v[36:39], a[144:147], v[80:83], v[36:39]\n\t" \
   "v_mfma_f32_16x16x32_bf16 v[36:39], a[148:151], v[80:83], v[36:39]\n\t" "v_mfma_f32_16x16x32_bf16 v[36:39], a[152:155], v[80:83], v[36:39]\n\t" \
   "v_mfma_f32_16x16x32_bf16 v[36:39], a[156:159], v[80:83], v[36:39]\n\t" "1:\n\t" "s_lshl_b32 s48, s45, 7\n\t" "s_mov_b32 s47, 0\n\t" \
   "s_cmp_lg_u32 %[ldx], %[ld0]\n\t" "s_cbranch_scc1 3f\n\t" "2:\n\t" "buffer_load_dword v30, v19, %[rs], s48 offen sc1\n\t" \
   "s_add_u32 s47, s47, 1\n\t" "s_waitcnt vmcnt(0)\n\t" "v_readfirstlane_b32 s44, v30\n\t" "s_add_u32 s44, s44, 1\n\t" "s_cmp_ge_u32 s44, 5\n\t" \
   "s_cbranch_scc1 3f\n\t" "s_cmp_gt_u32 s47, 0x20000\n\t" "s_cbranch_scc1 3f\n\t" "s_sleep 3\n\t" "s_cmp_lg_u32 s44, 0\n\t" "s_cbranch_scc1 2b\n\t" \
   "s_sleep 8\n\t" "s_branch 2b\n\t" "3:\n\t" "s_barrier\n\t" "s_mul_i32 s52, s45, 1536\n\t" "s_mul_i32 s51, s45, 40960\n\t" \
   "s_add_u32 m0, s46, %[ldx]\n\t" "s_mov_b32 s53, s46\n\t" "buffer_load_dwordx4 v16, %[rs], s51 offen offset:0 sc1 lds\n\t" \
   "buffer_load_dwordx4 v16, %[rs], s51 offen offset:1024 sc1 lds\n\t" "buffer_load_dwordx4 v16, %[rs], s51 offen offset:2048 sc1 lds\n\t" \
   "buffer_load_dwordx4 v16, %[rs], s51 offen offset:3072 sc1 lds\n\t" "s_add_u32 m0, m0, 4096\n\t" "s_add_u32 s51, s51, 4096\n\t" \
   "buffer_load_dwordx4 v16, %[rs], s51 offen offset:0 sc1 lds\n\t" "buffer_load_dwordx4 v16, %[rs], s51 offen offset:1024 sc1 lds\n\t" \
   "buffer_load_dwordx4 v16, %[rs], s51 offen offset:2048 sc1 lds\n\t" "buffer_load_dwordx4 v16, %[rs], s51 offen offset:3072 sc1 lds\n\t" \
   "s_add_u32 m0, m0, 4096\n\t" "s_add_u32 s51, s51, 4096\n\t" "buffer_load_dwordx4 v16, %[rs], s51 offen offset:0 sc1 lds\n\t" \
   "buffer_load_dwordx4 v16, %[rs], s51 offen offset:1024 sc1 lds\n\t" "s_add_u32 m0, s53, %[ld1]\n\t" "s_nop 0\n\t" \
   "buffer_load_dwordx4 v17, %[rs], s52 offen sc1 lds\n\t" "v_add_u32 v26, s46, v22\n\t" "v_add_u32 v27, s46, v23\n\t" "s_waitcnt vmcnt(0)\n\t" \
   "s_barrier\n\t" "ds_read_b128 v[80:83], v26 offset:0\n\t" "ds_read_b128 v[84:87], v26 offset:1024\n\t" "ds_read_b128 v[88:91], v26 offset:2048\n\t" \
   "ds_read_b128 v[92:95], v26 offset:3072\n\t" "ds_read_b128 v[96:99], v26 offset:4096\n\t" "ds_read_b128 v[100:103], v26 offset:5120\n\t" \
   "ds_read_b128 v[104:107], v26 offset:6144\n\t" "s_waitcnt lgkmcnt(5)\n\t" "v_mfma_f32_16x16x32_bf16 v[52:55], a[80:83], v[80:83], 0\n\t" \
   "ds_read_b128 v[112:115], v27 offset:0\n\t" "v_mfma_f32_16x16x32_bf16 v[48:51], a[0:3], v[80:83], 0\n\t" \
   "ds_read_b128 v[108:111], v26 offset:7168\n\t" "ds_read_b128 v[116:119], v27 offset:16\n\t" \
   "v_mfma_f32_16x16x32_bf16 v[52:55], a[84:87], v[84:87], v[52:55]\n\t" "ds_read_b64 v[120:121], v27 offset:32\n\t" \
   "v_mfma_f32_16x16x32_bf16 v[48:51], a[4:7], v[84:87], v[48:51]\n\t" "ds_read_b128 v[80:83], v26 offset:8192\n\t" "s_waitcnt lgkmcnt(8)\n\t" \
   "v_mfma_f32_16x16x32_bf16 v[52:55], a[88:91], v[88:91], v[52:55]\n\t" "ds_read_b128 v[122:125], v27 offset:768\n\t" \
   "v_mfma_f32_16x16x32_bf16 v[48:51], a[8:11], v[88:91], v[48:51]\n\t" "ds_read_b128 v[84:87], v26 offset:9216\n\t" \
   "ds_read_b128 v[126:129], v27 offset:784\n\t" "v_mfma_f32_16x16x32_bf16 v[52:55], a[92:95], v[92:95], v[52:55]\n\t" \
   "v_mfma_f32_16x16x32_bf16 v[48:51], a[12:15], v[92:95], v[48:51]\n\t" "ds_read_b128 v[88:91], v26 offset:10240\n\t" \
   "ds_read_b64 v[130:131], v27 offset:800\n\t" "s_waitcnt lgkmcnt(11)\n\t" "v_mfma_f32_16x16x32_bf16 v[52:55], a[96:99], v[96:99], v[52:55]\n\t" \
   "s_lshl_b32 s48, s45, 8\n\t" "v_mfma_f32_16x16x32_bf16 v[48:51], a[16:19], v[96:99], v[48:51]\n\t" "ds_read_b128 v[92:95], v26 offset:11264\n\t" \
   "s_add_u32 s47, s48, 65536\n\t" "v_mfma_f32_16x16x32_bf16 v[52:55], a[100:103], v[100:103], v[52:55]\n\t" \
   "v_mfma_f32_16x16x32_bf16 v[48:51], a[20:23], v[100:103], v[48:51]\n\t" "ds_read_b128 v[96:99], v26 offset:12288\n\t" "s_waitcnt lgkmcnt(8)\n\t" \
   "v_add_f32 v112, v112, v114\n\t" "v_mfma_f32_16x16x32_bf16 v[52:55], a[104:107], v[104:107], v[52:55]\n\t" "v_add_f32 v113, v113, v115\n\t" \
   "v_mfma_f32_16x16x32_bf16 v[48:51], a[24:27], v[104:107], v[48:51]\n\t" "ds_read_b128 v[100:103], v26 offset:13312\n\t" \
   "v_mfma_f32_16x16x32_bf16 v[52:55], a[108:111], v[108:111], v[52:55]\n\t" "v_add_f32 v116, v116, v118\n\t" \
   "v_mfma_f32_16x16x32_bf16 v[48:51], a[28:31], v[108:111], v[48:51]\n\t" "ds_read_b128 v[104:107], v26 offset:14336\n\t" \
   "v_add_f32 v117, v117, v119\n\t" "s_waitcnt lgkmcnt(7)\n\t" "v_mfma_f32_16x16x32_bf16 v[52:55], a[112:115], v[80:83], v[52:55]\n\t" \
   "v_mfma_f32_16x16x32_bf16 v[48:51], a[32:35], v[80:83], v[48:51]\n\t" "ds_read_b128 v[108:111], v26 offset:15360\n\t" \
   "v_add_f32 v112, v112, v120\n\t" "v_mfma_f32_16x16x32_bf16 v[52:55], a[116:119], v[84:87], v[52:55]\n\t" "v_add_f32 v113, v113, v121\n\t" \
   "v_mfma_f32_16x16x32_bf16 v[48:51], a[36:39], v[84:87], v[48:51]\n\t" "ds_read_b128 v[80:83], v26 offset:16384\n\t" "v_add_f32 v112, v112, v116\n\t" \
   "s_waitcnt lgkmcnt(5)\n\t" "v_mfma_f32_16x16x32_bf16 v[52:55], a[120:123], v[88:91], v[52:55]\n\t" \
   "v_mfma_f32_16x16x32_bf16 v[48:51], a[40:43], v[88:91], v[48:51]\n\t" "ds_read_b128 v[84:87], v26 offset:17408\n\t" "v_add_f32 v113, v113, v117\n\t" \
   "v_mfma_f32_16x16x32_bf16 v[52:55], a[124:127], v[92:95], v[52:55]\n\t" "v_mul_f32 v112, 0xbacccccd, v112\n\t" \
   "v_mfma_f32_16x16x32_bf16 v[48:51], a[44:47], v[92:95], v[48:51]\n\t" "ds_read_b128 v[88:91], v26 offset:18432\n\t" "s_waitcnt lgkmcnt(5)\n\t" \
   "v_mfma_f32_16x16x32_bf16 v[52:55], a[128:131], v[96:99], v[52:55]\n\t" "v_mul_f32 v113, 0x3acccccd, v113\n\t" \
   "v_mfma_f32_16x16x32_bf16 v[48:51], a[48:51], v[96:99], v[48:51]\n\t" "ds_read_b128 v[92:95], v26 offset:19456\n\t" \
   "v_fma_f32 v113, -v112, v112, v113\n\t" "v_mfma_f32_16x16x32_bf16 v[52:55], a[132:135], v[100:103], v[52:55]\n\t" "v_max_f32 v113, 0, v113\n\t" \
   "v_mfma_f32_16x16x32_bf16 v[48:51], a[52:55], v[100:103], v[48:51]\n\t" "ds_read_b128 v[96:99], v26 offset:20480\n\t" "s_waitcnt lgkmcnt(5)\n\t" \
   "v_mfma_f32_16x16x32_bf16 v[52:55], a[136:139], v[104:107], v[52:55]\n\t" "v_cvt_pk_bf16_f32 v113, v113, v112\n\t" \
   "v_mfma_f32_16x16x32_bf16 v[48:51], a[56:59], v[104:107], v[48:51]\n\t" "ds_read_b128 v[100:103], v26 offset:21504\n\t" \
   "v_and_b32 v8, 0xffff0000, v113\n\t" "v_mfma_f32_16x16x32_bf16 v[52:55], a[140:143], v[108:111], v[52:55]\n\t" \
   "v_mfma_f32_16x16x32_bf16 v[48:51], a[60:63], v[108:111], v[48:51]\n\t" "ds_read_b128 v[104:107], v26 offset:22528\n\t" \
   "v_lshlrev_b32 v113, 16, v113\n\t" "s_waitcnt lgkmcnt(5)\n\t" "v_mfma_f32_16x16x32_bf16 v[52:55], a[144:147], v[80:83], v[52:55]\n\t" \
   "v_add_f32 v113, 0x3727c5ac, v113\n\t" "v_mfma_f32_16x16x32_bf16 v[48:51], a[64:67], v[80:83], v[48:51]\n\t" \
   "ds_read_b128 v[108:111], v26 offset:23552\n\t" "v_mfma_f32_16x16x32_bf16 v[52:55], a[148:151], v[84:87], v[52:55]\n\t" \
   "v_cvt_pk_bf16_f32 v113, v113, v113\n\t" "v_mfma_f32_16x16x32_bf16 v[48:51], a[68:71], v[84:87], v[48:51]\n\t" \
   "ds_read_b128 v[80:83], v26 offset:24576\n\t" "v_lshlrev_b32 v113, 16, v113\n\t" "s_waitcnt lgkmcnt(5)\n\t" \
   "v_mfma_f32_16x16x32_bf16 v[52:55], a[152:155], v[88:91], v[52:55]\n\t" "v_sqrt_f32 v113, v113\n\t" \
   "v_mfma_f32_16x16x32_bf16 v[48:51], a[72:75], v[88:91], v[48:51]\n\t" "ds_read_b128 v[84:87], v26 offset:25600\n\t" \
   "v_mfma_f32_16x16x32_bf16 v[52:55], a[156:159], v[92:95], v[52:55]\n\t" "s_nop 0\n\t" \
   "v_mfma_f32_16x16x32_bf16 v[48:51], a[76:79], v[92:95], v[48:51]\n\t" "ds_read_b128 v[88:91], v26 offset:26624\n\t" \
   "v_cvt_pk_bf16_f32 v113, v113, v113\n\t" "s_waitcnt lgkmcnt(5)\n\t" "v_mfma_f32_16x16x32_bf16 v[60:63], a[80:83], v[96:99], 0\n\t" \
   "v_mfma_f32_16x16x32_bf16 v[56:59], a[0:3], v[96:99], 0\n\t" "ds_read_b128 v[92:95], v26 offset:27648\n\t" "v_lshlrev_b32 v113, 16, v113\n\t" \
   "v_mfma_f32_16x16x32_bf16 v[60:63], a[84:87], v[100:103], v[60:63]\n\t" "v_rcp_f32 v10, v113\n\t" \
   "v_mfma_f32_16x16x32_bf16 v[56:59], a[4:7], v[100:103], v[56:59]\n\t" "ds_read_b128 v[96:99], v26 offset:28672\n\t" "s_waitcnt lgkmcnt(5)\n\t" \
   "v_mfma_f32_16x16x32_bf16 v[60:63], a[88:91], v[104:107], v[60:63]\n\t" "v_fmac_f32 v48, v32, v8\n\t" "v_fmac_f32 v49, v33, v8\n\t" \
   "v_mfma_f32_16x16x32_bf16 v[56:59], a[8:11], v[104:107], v[56:59]\n\t" "ds_read_b128 v[100:103], v26 offset:29696\n\t" "v_fmac_f32 v50, v34, v8\n\t" \
   "v_mfma_f32_16x16x32_bf16 v[60:63], a[92:95], v[108:111], v[60:63]\n\t" "v_fmac_f32 v51, v35, v8\n\t" \
   "v_mfma_f32_16x16x32_bf16 v[56:59], a[12:15], v[108:111], v[56:59]\n\t" "ds_read_b128 v[104:107], v26 offset:30720\n\t" \
   "v_fmac_f32 v52, v36, v8\n\t" "s_waitcnt lgkmcnt(5)\n\t" "v_mfma_f32_16x16x32_bf16 v[60:63], a[96:99], v[80:83], v[60:63]\n\t" \
   "v_fmac_f32 v53, v37, v8\n\t" "v_fmac_f32 v54, v38, v8\n\t" "v_mfma_f32_16x16x32_bf16 v[56:59], a[16:19], v[80:83], v[56:59]\n\t" \
   "ds_read_b128 v[108:111], v26 offset:31744\n\t" "v_fmac_f32 v55, v39, v8\n\t" \
   "v_mfma_f32_16x16x32_bf16 v[60:63], a[100:103], v[84:87], v[60:63]\n\t" "v_mul_f32 v48, v48, v10\n\t" \
   "v_mfma_f32_16x16x32_bf16 v[56:59], a[20:23], v[84:87], v[56:59]\n\t" "ds_read_b128 v[80:83], v26 offset:32768\n\t" "v_mul_f32 v49, v49, v10\n\t" \
   "s_waitcnt lgkmcnt(5)\n\t" "v_mfma_f32_16x16x32_bf16 v[60:63], a[104:107], v[88:91], v[60:63]\n\t" "v_mul_f32 v50, v50, v10\n\t" \
   "v_mul_f32 v51, v51, v10\n\t" "v_mfma_f32_16x16x32_bf16 v[56:59], a[24:27], v[88:91], v[56:59]\n\t" "ds_read_b128 v[84:87], v26 offset:33792\n\t" \
   "v_mul_f32 v52, v52, v10\n\t" "v_mfma_f32_16x16x32_bf16 v[60:63], a[108:111], v[92:95], v[60:63]\n\t" "v_mul_f32 v53, v53, v10\n\t" \
   "v_mfma_f32_16x16x32_bf16 v[56:59], a[28:31], v[92:95], v[56:59]\n\t" "ds_read_b128 v[88:91], v26 offset:34816\n\t" "v_mul_f32 v54, v54, v10\n\t" \
   "s_waitcnt lgkmcnt(5)\n\t" "v_mfma_f32_16x16x32_bf16 v[60:63], a[112:115], v[96:99], v[60:63]\n\t" "v_mul_f32 v55, v55, v10\n\t" \
   "v_cvt_pk_bf16_f32 v64, v48, v49\n\t" "v_mfma_f32_16x16x32_bf16 v[56:59], a[32:35], v[96:99], v[56:59]\n\t" \
   "ds_read_b128 v[92:95], v26 offset:35840\n\t" "v_cvt_pk_bf16_f32 v65, v50, v51\n\t" \
   "v_mfma_f32_16x16x32_bf16 v[60:63], a[116:119], v[100:103], v[60:63]\n\t" "v_cvt_pk_bf16_f32 v66, v52, v53\n\t" \
   "v_mfma_f32_16x16x32_bf16 v[56:59], a[36:39], v[100:103], v[56:59]\n\t" "ds_read_b128 v[96:99], v26 offset:36864\n\t" \
   "v_cvt_pk_bf16_f32 v67, v54, v55\n\t" "s_waitcnt lgkmcnt(5)\n\t" "v_mfma_f32_16x16x32_bf16 v[60:63], a[120:123], v[104:107], v[60:63]\n\t" \
   "buffer_store_dwordx2 v[64:65], v21, %[rs], s48 offen offset:0 sc1\n\t" "buffer_store_dwordx2 v[66:67], v21, %[rs], s47 offen offset:0 sc1\n\t" \
   "v_mfma_f32_16x16x32_bf16 v[56:59], a[40:43], v[104:107], v[56:59]\n\t" "ds_read_b128 v[100:103], v26 offset:37888\n\t" \
   "v_add_f32 v122, v122, v124\n\t" "v_mfma_f32_16x16x32_bf16 v[60:63], a[124:127], v[108:111], v[60:63]\n\t" "v_add_f32 v123, v123, v125\n\t" \
   "v_mfma_f32_16x16x32_bf16 v[56:59], a[44:47], v[108:111], v[56:59]\n\t" "ds_read_b128 v[104:107], v26 offset:38912\n\t" \
   "v_add_f32 v126, v126, v128\n\t" "s_waitcnt lgkmcnt(5)\n\t" "v_mfma_f32_16x16x32_bf16 v[60:63], a[128:131], v[80:83], v[60:63]\n\t" \
   "v_add_f32 v127, v127, v129\n\t" "v_add_f32 v122, v122, v130\n\t" "v_mfma_f32_16x16x32_bf16 v[56:59], a[48:51], v[80:83], v[56:59]\n\t" \
   "ds_read_b128 v[108:111], v26 offset:39936\n\t" "v_add_f32 v123, v123, v131\n\t" \
   "v_mfma_f32_16x16x32_bf16 v[60:63], a[132:135], v[84:87], v[60:63]\n\t" "v_add_f32 v122, v122, v126\n\t" \
   "v_mfma_f32_16x16x32_bf16 v[56:59], a[52:55], v[84:87], v[56:59]\n\t" "v_add_f32 v123, v123, v127\n\t" "s_waitcnt lgkmcnt(4)\n\t" \
   "v_mfma_f32_16x16x32_bf16 v[60:63], a[136:139], v[88:91], v[60:63]\n\t" "v_mul_f32 v122, 0xbacccccd, v122\n\t" \
   "v_mul_f32 v123, 0x3acccccd, v123\n\t" "v_mfma_f32_16x16x32_bf16 v[56:59], a[56:59], v[88:91], v[56:59]\n\t" "v_fma_f32 v123, -v122, v122, v123\n\t" \
   "v_mfma_f32_16x16x32_bf16 v[60:63], a[140:143], v[92:95], v[60:63]\n\t" "v_max_f32 v123, 0, v123\n\t" \
   "v_mfma_f32_16x16x32_bf16 v[56:59], a[60:63], v[92:95], v[56:59]\n\t" "v_cvt_pk_bf16_f32 v123, v123, v122\n\t" "s_waitcnt lgkmcnt(2)\n\t" \
   "v_mfma_f32_16x16x32_bf16 v[60:63], a[144:147], v[96:99], v[60:63]\n\t" "v_and_b32 v12, 0xffff0000, v123\n\t" "v_lshlrev_b32 v123, 16, v123\n\t" \
   "v_mfma_f32_16x16x32_bf16 v[56:59], a[64:67], v[96:99], v[56:59]\n\t" "v_add_f32 v123, 0x3727c5ac, v123\n\t" \
   "v_mfma_f32_16x16x32_bf16 v[60:63], a[148:151], v[100:103], v[60:63]\n\t" "v_cvt_pk_bf16_f32 v123, v123, v123\n\t" \
   "v_mfma_f32_16x16x32_bf16 v[56:59], a[68:71], v[100:103], v[56:59]\n\t" "v_lshlrev_b32 v123, 16, v123\n\t" "s_waitcnt lgkmcnt(0)\n\t" \
   "v_mfma_f32_16x16x32_bf16 v[60:63], a[152:155], v[104:107], v[60:63]\n\t" "v_sqrt_f32 v123, v123\n\t" "s_nop 0\n\t" \
   "v_mfma_f32_16x16x32_bf16 v[56:59], a[72:75], v[104:107], v[56:59]\n\t" "v_cvt_pk_bf16_f32 v123, v123, v123\n\t" \
   "v_mfma_f32_16x16x32_bf16 v[60:63], a[156:159], v[108:111], v[60:63]\n\t" "v_lshlrev_b32 v123, 16, v123\n\t" \
   "v_mfma_f32_16x16x32_bf16 v[56:59], a[76:79], v[108:111], v[56:59]\n\t" "v_rcp_f32 v132, v123\n\t" "s_xor_b32 s46, s46, 44032\n\t" \
   "s_add_u32 s45, s45, 4\n\t" "s_nop 5\n\t" "v_fmac_f32 v56, v32, v12\n\t" "v_fmac_f32 v57, v33, v12\n\t" "v_fmac_f32 v58, v34, v12\n\t" \
   "v_fmac_f32 v59, v35, v12\n\t" "v_fmac_f32 v60, v36, v12\n\t" "v_fmac_f32 v61, v37, v12\n\t" "v_fmac_f32 v62, v38, v12\n\t" \
   "v_fmac_f32 v63, v39, v12\n\t" "v_mul_f32 v56, v56, v132\n\t" "v_mul_f32 v57, v57, v132\n\t" "v_mul_f32 v58, v58, v132\n\t" \
   "v_mul_f32 v59, v59, v132\n\t" "v_mul_f32 v60, v60, v132\n\t" "v_mul_f32 v61, v61, v132\n\t" "v_mul_f32 v62, v62, v132\n\t" \
   "v_mul_f32 v63, v63, v132\n\t" "v_cvt_pk_bf16_f32 v68, v56, v57\n\t" "v_cvt_pk_bf16_f32 v69, v58, v59\n\t" "v_cvt_pk_bf16_f32 v70, v60, v61\n\t" \
   "v_cvt_pk_bf16_f32 v71, v62, v63\n\t" "buffer_store_dwordx2 v[68:69], v21, %[rs], s48 offen offset:128 sc1\n\t" \
   "buffer_store_dwordx2 v[70:71], v21, %[rs], s47 offen offset:128 sc1\n\t" "s_cmp_lt_u32 s45, 64\n\t" "s_cbranch_scc1 1b\n\t" \
   "s_waitcnt vmcnt(0) lgkmcnt(0)"
#define DF_CLOB "v8", "v9", "v10", "v11", "v12", "v13", "v14", "v15", "v32", "v33", "v34", "v35", "v36", "v37", "v38", "v39", "v40", "v41", "v42", "v43", "v44", "v45", "v46", "v47", "v48", "v49", "v50", "v51", "v52", "v53", "v54", "v55", "v56", "v57", "v58", "v59", "v60", "v61", "v62", "v63", "v64", "v65", "v66", "v67", "v68", "v69", "v70", "v71", "v72", "v73", "v74", "v75", "v76", "v77", "v78", "v79", "v80", "v81", "v82", "v83", "v84", "v85", "v86", "v87", "v88", "v89", "v90", "v91", "v92", "v93", "v94", "v95", "v96", "v97", "v98", "v99", "v100", "v101", "v102", "v103", "v104", "v105", "v106", "v107", "v108", "v109", "v110", "v111", "v112", "v113", "v114", "v115", "v116", "v117", "v118", "v119", "v120", "v121", "v122", "v123", "v124", "v125", "v126", "v127", "v128", "v129", "v130", "v131", "v132", "v133", "v134", "v135", "s40", "s41", "s42", "s43", "s44", "s45", "s46", "s47", "s48", "s49", "s50", "s51", "s52", "s53"
#define PRE_ASM \
   "s_mov_b32 s45, 0\n\t" "s_mov_b32 s46, 0\n\t" "s_mul_i32 s52, s45, 1536\n\t" "s_mul_i32 s51, s45, 40960\n\t" "s_add_u32 m0, s46, %[ldx]\n\t" \
   "s_mov_b32 s53, s46\n\t" "buffer_load_dwordx4 v16, %[rs], s51 offen offset:0 sc1 lds\n\t" \
   "buffer_load_dwordx4 v16, %[rs], s51 offen offset:1024 sc1 lds\n\t" "buffer_load_dwordx4 v16, %[rs], s51 offen offset:2048 sc1 lds\n\t" \
   "buffer_load_dwordx4 v16, %[rs], s51 offen offset:3072 sc1 lds\n\t" "s_add_u32 m0, m0, 4096\n\t" "s_add_u32 s51, s51, 4096\n\t" \
   "buffer_load_dwordx4 v16, %[rs], s51 offen offset:0 sc1 lds\n\t" "buffer_load_dwordx4 v16, %[rs], s51 offen offset:1024 sc1 lds\n\t" \
   "buffer_load_dwordx4 v16, %[rs], s51 offen offset:2048 sc1 lds\n\t" "buffer_load_dwordx4 v16, %[rs], s51 offen offset:3072 sc1 lds\n\t" \
   "s_add_u32 m0, m0, 4096\n\t" "s_add_u32 s51, s51, 4096\n\t" "buffer_load_dwordx4 v16, %[rs], s51 offen offset:0 sc1 lds\n\t" \
   "buffer_load_dwordx4 v16, %[rs], s51 offen offset:1024 sc1 lds\n\t" "s_add_u32 m0, s53, %[ld1]\n\t" "s_nop 0\n\t" \
   "buffer_load_dwordx4 v17, %[rs], s52 offen sc1 lds\n\t" "s_mov_b32 s45, 1\n\t" "s_mov_b32 s46, 44032\n\t" "s_mul_i32 s52, s45, 1536\n\t" \
   "s_mul_i32 s51, s45, 40960\n\t" "s_add_u32 m0, s46, %[ldx]\n\t" "s_mov_b32 s53, s46\n\t" \
   "buffer_load_dwordx4 v16, %[rs], s51 offen offset:0 sc1 lds\n\t" "buffer_load_dwordx4 v16, %[rs], s51 offen offset:1024 sc1 lds\n\t" \
   "buffer_load_dwordx4 v16, %[rs], s51 offen offset:2048 sc1 lds\n\t" "buffer_load_dwordx4 v16, %[rs], s51 offen offset:3072 sc1 lds\n\t" \
   "s_add_u32 m0, m0, 4096\n\t" "s_add_u32 s51, s51, 4096\n\t" "buffer_load_dwordx4 v16, %[rs], s51 offen offset:0 sc1 lds\n\t" \
   "buffer_load_dwordx4 v16, %[rs], s51 offen offset:1024 sc1 lds\n\t" "buffer_load_dwordx4 v16, %[rs], s51 offen offset:2048 sc1 lds\n\t" \
   "buffer_load_dwordx4 v16, %[rs], s51 offen offset:3072 sc1 lds\n\t" "s_add_u32 m0, m0, 4096\n\t" "s_add_u32 s51, s51, 4096\n\t" \
   "buffer_load_dwordx4 v16, %[rs], s51 offen offset:0 sc1 lds\n\t" "buffer_load_dwordx4 v16, %[rs], s51 offen offset:1024 sc1 lds\n\t" \
   "s_add_u32 m0, s53, %[ld1]\n\t" "s_nop 0\n\t" "buffer_load_dwordx4 v17, %[rs], s52 offen sc1 lds\n\t" ""
#define PRE_CLOB "s45", "s46", "s47", "s48", "s49", "s50", "s51", "s52", "s53"
__device__ __forceinline__ void pre_proj(int wv, rsrc_t rb) {
  const int lane = lane_id();
  const unsigned lds0 = (unsigned)(uintptr_t)(__attribute__((address_space(3))) unsigned char*)dyn_lds;
  asm volatile(PRE_ASM ::"{v16}"((int)OFF_XR + wv * 10240 + lane * 16), "{v17}"(wv * 64 + lane < 96 ? (int)OFF_ST + wv * 1024 + lane * 16 : OOB), [rs] "s"(rb),
               [ldx] "s"(lds0 + PJ_X + wv * 10240), [ld1] "s"(lds0 + PJ_X + 40960 + (wv < 2 ? wv : 2) * 1024) : PRE_CLOB, "scc", "m0", "memory");
}
template <bool VU>
__device__ __forceinline__ void job_proj(int wv_, const v32u (&w)[10], unsigned char* ws, const Pool& pool, int layer, int h) {
  const unsigned lds0 = (unsigned)(uintptr_t)(__attribute__((address_space(3))) unsigned char*)dyn_lds;
  const int lane = lane_id(), wv = wv_, g = lane >> 4, r = lane & 15;
  const v4u rs = make_rs(ws, (unsigned)OFF_END);
  v16u ad = (unsigned)lane;
  ad[0] = (int)OFF_XR + wv * 10240 + lane * 16;
  ad[1] = wv * 64 + lane < 96 ? (int)OFF_ST + wv * 1024 + lane * 16 : OOB;
  ad[3] = cnt_off(layer, CNT_IDFT, lane & 31);
  ad[4] = lane == 0 ? kqf_off(layer, 0, (VU ? 32 : 0) + h) + wv * 4 : OOB;
  ad[5] = (VU ? (int)OFF_UF + (wv * 320 + h) * 1024 : (h * RT * 4 + wv) * 1024) + lane * 16;
  ad[6] = lds0 + PJ_X + lane * 16;
  ad[7] = lds0 + PJ_X + 40960 + r * 48;
  ad[8] = lds0 + wv * 1024 + lane * 16;
  ad[9] = lds0 + 8192 + lane * 16;
  v4u rf = rs;
  v8u fo;
  if constexpr (!VU) {
    fo[0] = h * 16384 + wv * 2048 + lane * 16;
#pragma unroll
    for (int k = 0; k < 3; ++k) { const int u = h * 10 + wv + 4 * k; fo[1 + k] = wv + 4 * k < 10 ? (u >> 3) * 16384 + (u & 7) * 1024 + lane * 16 : OOB; }
    asm volatile("v_mov_b32 %0, -1" : "=v"(fo[4]));
    fo[5] = fo[6] = fo[7] = fo[4];
  }
  if (layer > 0) fan_wait<64, 60>(wv_, ws, fan_off(1, 1, 0), h);
  PH(2 + layer * 6)
#define PJ_OPS                                                                                                                      \
  : W_OPS, [rs] "s"(rs), [rdy] "s"((layer - 1) | 63), [ldx] "s"(lds0 + PJ_X + wv * 10240),                        \
    [ld1] "s"(lds0 + PJ_X + 40960 + (wv < 2 ? wv : 2) * 1024), [qf] "s"((int)OFF_QF), [kf] "s"((int)OFF_KF), [cnt] "s"(cnt_off(layer, CNT_IDFT, 0))
  if constexpr (VU) asm volatile(VU_ASM : "+{v[16:31]}"(ad) PJ_OPS : VU_CLOB, "vcc", "scc", "m0", "memory");
  else asm volatile(KQ_ASM : "+{v[16:31]}"(ad), "+&{s[56:59]}"(rf) PJ_OPS, "{v[152:159]}"(fo), [sel] "s"(pool.sel), [ysel] "s"(pool.ysel), [pool] "s"(pool.base),
                    [yb] "s"(pool.base + P_Y), [size] "s"((unsigned)OFF_END), [skip] "s"(h >= 20 && h < 30 ? 3ull << 62 : 2ull << 62)
                    : KQ_CLOB, "vcc", "scc", "m0", "memory");
  LDSBAR();
}

__device__ __forceinline__ void job_dft(int wv_, unsigned char* ws, int layer, int fg, int set) {
  const unsigned lds0 = (unsigned)(uintptr_t)(__attribute__((address_space(3))) unsigned char*)dyn_lds;
  const int lane = lane_id(), wv = wv_, g = lane >> 4, r = lane & 15;
  const v4u rs = make_rs(ws, (unsigned)OFF_END);
  const rsrc_t rb = ws_rsrc(ws);
  v32u w[5];
#pragma unroll
  for (int i = 0; i < 40; ++i) put4(w[i >> 3], i & 7, ldp(rb, (int)OFF_FWD + ((fg * 8 + wv * 2) * KT + i) * 1024 + lane * 16));
  v16u ad = (unsigned)lane;
  ad[0] = (int)OFF_XR + wv * 10240 + lane * 16;
  ad[1] = wv * 64 + lane < 96 ? (int)OFF_ST + wv * 1024 + lane * 16 : OOB;
  ad[3] = cnt_off(layer, CNT_OUT, 0);
  ad[5] = (int)OFF_X1T + (((fg * 8 + wv * 2) * 4 + g) * S_ + r) * 8;
  ad[6] = lds0 + lane * 16;
  ad[7] = lds0 + 40960 + r * 48;
  asm volatile(DF_ASM
               : "+{v[16:31]}"(ad)
               : "{a[0:31]}"(w[0]), "{a[32:63]}"(w[1]), "{a[64:95]}"(w[2]), "{a[96:127]}"(w[3]), "{a[128:159]}"(w[4]), [rs] "s"(rs), [set] "s"(set),
                 [ldx] "s"(lds0 + wv * 10240), [ld0] "s"(lds0), [ld1] "s"(lds0 + 40960 + (wv < 2 ? wv : 2) * 1024)
               : DF_CLOB, "vcc", "scc", "m0", "memory");
}

constexpr int NOUT = 44;
constexpr int MAIN_LDS = PJ_LDS;
static_assert(MAIN_LDS >= JOB_LDS && MAIN_LDS >= REC_LDS && MAIN_LDS <= 160 * 1024, "LDS");
__device__ __forceinline__ int take_role(int wv_, unsigned char* ws) {
  int* slot = (int*)dyn_lds;
  if (TIDX == 0) {
    const int x = (int)xcc_id();
    const int first = x;
    int role = 0;
#pragma unroll 1
    for (int i = 0; i < 8; ++i) {
      const int pl = first ^ i;
      const unsigned t = __hip_atomic_fetch_add((gu32*)(ws + tick_off(0, pl)), 1u, __ATOMIC_RELAXED, __HIP_MEMORY_SCOPE_AGENT) + 1u;
      if (t < 32u) { role = pl * 32 + (int)t; break; }
    }
    *slot = role;
  }
  __syncthreads();
  const int role = __builtin_amdgcn_readfirstlane(*slot);
  __syncthreads();
  return role;
}
#define TROB (24 * 256 + 16)
#define TC2 30
#define TVIS 32
#define TDLT 170
#define TM1 88
#define TM2 60
__global__ void __launch_bounds__(256, 1) k_main(unsigned char* ws, const u16* Wq, const u16* Wk, const u16* Wv, const u16* Wo, long nlayer
                                                ) {
  v4u* smem = (v4u*)dyn_lds;
  const int wv_ = __builtin_amdgcn_readfirstlane(threadIdx.x >> 6);
  const RecTune tn = {TM1, TM2, 4, TROB, TC2, TVIS, TDLT, 1};
  unsigned long long* dbg = nullptr;
#define RSTAMP_(i)
  const rsrc_t rs = ws_rsrc(ws);
  PH(1)
  v4i cal[18];
  pool_load(ws, cal);
  const int tk = take_role(wv_, ws), pl = tk >> 5, tt = tk & 31;
  Pool pool = pool_init(ws, cal);
  const int role = pl < 4 ? (tt < 16 ? (tt & 8) * 4 + pl * 8 + (tt & 7) : (tt < 26 ? 64 + pl * 10 + tt - 16 : 104 + pl * 6 + tt - 26)) : tk;
  if (role >= 104 && role < 128) return;
  if (role >= 64 && role < 104) {
#pragma unroll 1
    for (int layer = 0; layer < nlayer; ++layer) {
      if (layer > 0) {
        job_col(wv_, ws, rs, lds_base(), role + 57, fan_off(0, 1, 0));
        fan_signal<FSP>(wv_, ws, fan_off(1, 0, 0), role + 57);
        PH(7)
      }
      ((unsigned*)(dyn_lds + L_T))[lane_id() & 15] = 0u;
      __syncthreads();
      if (wv_ == 3) rec_fin(ws, pool, layer, role - 64, pl, tn, dbg);
      else if (wv_ == 0) rec_sched(ws, pool, layer, role - 64, pl, tn, dbg);
      __syncthreads();
    }
    return;
  }
  if (tt < 16) {
    v32u w[10];
    const int hd = (pl - 4) * 8 + (tt & 7);
    const unsigned lds0 = (unsigned)(uintptr_t)(__attribute__((address_space(3))) unsigned char*)dyn_lds;
    if (role < 64) {
      const bool pv = role >= 32;
      load_rows(w, pv ? 3 : 0, pv ? Wv : Wq, pv ? Wo : Wk, role & 31, wv_, lds0);
      LDSBAR();
    } else {
      pre_proj(wv_, rs);
      const bool vu = (tt & 8) != 0;
      load_rows(w, vu ? 2 : 1, vu ? Wv : Wq, vu ? Wo : Wk, hd, wv_, lds0 + PJ_X + 2 * PJ_BUF);
    }
#pragma unroll 1
    for (int layer = 0; layer < nlayer; ++layer) {
      int ro = role, xs = pl, wq = wv_, h2 = hd | (tt & 8) << 8;
      asm volatile("" : "+s"(ro), "+s"(xs), "+s"(wq), "+s"(h2), "+s"(ws), "+s"(pool.base));
      if (layer > 0) {
        const int jb = ro < 64 ? ro + 57 : (xs - 4) * 16 + (ro & 31);
        if (ro < 64 || jb < 57) {
          __syncthreads();
          job_col(wq, ws, ws_rsrc(ws), lds_base(), jb, fan_off(0, 1, 0));
          fan_signal<FSP>(wq, ws, fan_off(1, 0, 0), jb);
          RSTAMP_(5)
          PH(7)
        } else fan_wait<64, 20>(wq, ws, fan_off(0, 1, 0), jb);
      }
      if (ro < 32) { rec_p1(wq, w, ws, pool, layer, ro, xs, tn, dbg); PH(4 + layer * 6) }
      else if (ro < 64) rec_pv(wq, w, ws, pool, layer, ro - 32, xs, tn, dbg);
      else {
        if (h2 >> 8) job_proj<true>(wq, w, ws, pool, layer, h2 & 31);
        else job_proj<false>(wq, w, ws, pool, layer, h2 & 31);
        RSTAMP_(2)
      }
    }
    return;
  }
#pragma unroll 1
  for (int layer = 0; layer < nlayer; ++layer) {
    if (layer > 0 && tt < 31) {
      const int i = tt - 16;
      job_idft<false>(wv_, ws, fan_off(1, 0, 0), rs, smem, (i / 5) * 4 + pl - 4, 12, i % 5, nullptr, dbg + 60000 + role * 8);
    }
    RSTAMP_(2)
    __syncthreads();
    if (tt >= 27) {
      job_dft(wv_, ws, layer, tt - 27, pl - 4);
      if (layer == 0) fan_signal<64>(wv_, ws, fan_off(0, 1, 0), (pl - 4) * 5 + tt - 27);
    } else {
#pragma unroll 1
      for (int j = (pl - 4) * 11 + tt - 16; j < 320; j += NOUT) {
        flags32_wait(wv_, ws, kqf_off(layer, j / 5, 32));
        job_out(wv_, ws, rs, smem, pool.ysel, layer, j / 5, j % 5);
        dep_signal(wv_, ws, cnt_off(layer, CNT_OUT, j / 5));
      }
    }
    RSTAMP_(3)
    PH((tt >= 27 ? 6 : 5) + layer * 6)
  }
}

extern "C" void kernel_launch(void* const* d_in, const int* in_sizes, int n_in, void* d_out, int out_size, void* d_ws, size_t ws_size,
                              hipStream_t stream) {
  const u16* x = (const u16*)d_in[0];
  const u16* mem0 = (const u16*)d_in[1];
  const u16* Wq = (const u16*)d_in[2];
  const u16* Wk = (const u16*)d_in[3];
  const u16* Wv = (const u16*)d_in[4];
  const u16* Wo = (const u16*)d_in[5];
  const u16* wr = (const u16*)d_in[8];
  const u16* wi = (const u16*)d_in[9];
  unsigned char* ws = (unsigned char*)d_ws;
  u16* out = (u16*)d_out;
  (void)hipFuncSetAttribute((const void*)k_main, hipFuncAttributeMaxDynamicSharedMemorySize, MAIN_LDS);
  (void)hipFuncSetAttribute((const void*)k_fin, hipFuncAttributeMaxDynamicSharedMemorySize, FIN_LDS);
  k_prep<<<PB_TOTAL, 256, 0, stream>>>(x, mem0, Wq, Wk, Wv, Wo, wr, wi, ws);
  k_main<<<256, 256, MAIN_LDS, stream>>>(ws, Wq, Wk, Wv, Wo, 2);
  k_fin<<<NCOL + 320, 256, FIN_LDS, stream>>>(ws, out);
}
```

```cpp
#include <hip/hip_runtime.h>
#include <hip/hip_bf16.h>
#include <stdint.h>

typedef unsigned v4u __attribute__((ext_vector_type(4)));
typedef unsigned v2u __attribute__((ext_vector_type(2)));
typedef float f32x4 __attribute__((ext_vector_type(4)));
typedef float f32x2 __attribute__((ext_vector_type(2)));
typedef __bf16 bf16x8 __attribute__((ext_vector_type(8)));
typedef __bf16 bf16x2 __attribute__((ext_vector_type(2)));
typedef unsigned short u16;

constexpr int S_ = 2048, D_ = 640, NSEG = 64, KT = 20, RT = 128;
constexpr int NFT = 40;
constexpr int NFK = 20;
constexpr int WIN = 32768, REG = 16 * WIN;
constexpr int W_MEM = 10 * WIN, W_MISC = 15 * WIN;
__host__ __device__ constexpr int copy_off(int c) { return (c & 3) * 2048 + (c >> 2) * 16384; }
constexpr int M_STAMP = W_MISC + copy_off(4), M_TM = M_STAMP + 40 * 64, M_CAL = W_MISC + copy_off(3);
constexpr int PAGE = 4 * REG, NYP = 22, P_Y = NSEG * REG;
constexpr size_t SZ_POOL = (size_t)P_Y + (size_t)(NYP + 1) * PAGE;

constexpr size_t OFF_CNT = 0;
constexpr int CNT_IDFT = 0, CNT_OUT = 1;
constexpr size_t OFF_PROG = OFF_CNT + (size_t)3 * 32 * 128;
constexpr size_t OFF_KQF = OFF_PROG + (size_t)NSEG * 4096;
#define FNAP 6
#define FSP 16
#define FNG 32
constexpr int FAN_G = (161 * FSP + 127) / 128 * 128 > 3840 ? (161 * FSP + 127) / 128 * 128 : 3840;
constexpr size_t OFF_FAN = OFF_KQF + (size_t)NSEG * 4096;
constexpr size_t HO_LAYER = OFF_FAN + (size_t)2 * FNG * FAN_G;
constexpr size_t OFF_HO = SZ_POOL;
constexpr size_t HO_FIRST = OFF_FAN + (size_t)FNG * FAN_G;
constexpr size_t OFF_HS = OFF_HO + 2 * HO_LAYER;
constexpr int HS_GO = (int)OFF_HS, HS_PAY = HS_GO + 4096, HS_ASK = HS_PAY + 1024, HS_HI = HS_ASK + 2048, HS_CAL = HS_HI + 2048;
__host__ __device__ constexpr int prog_off(int layer, int rb, int slot) { return (int)(OFF_HO + layer * HO_LAYER + OFF_PROG) + (rb * 64 + slot) * 64; }
__host__ __device__ constexpr int kqf_off(int layer, int sg, int h) { return (int)(OFF_HO + layer * HO_LAYER + OFF_KQF) + (sg * 64 + h) * 64; }
__host__ __device__ constexpr int cnt_off(int layer, int kind, int rb) { return (int)(OFF_HO + layer * HO_LAYER + OFF_CNT) + (kind * 32 + rb) * 128; }
__host__ __device__ constexpr int fan_off(int layer, int set, int i) { return (int)(OFF_HO + layer * HO_LAYER + OFF_FAN) + ((set ^ layer ^ 1) * FNG + i) * FAN_G; }
constexpr size_t OFF_XR = (OFF_HS + 65536 + 4095) / 4096 * 4096;
constexpr size_t OFF_ST = OFF_XR + (size_t)RT * KT * 1024;
constexpr size_t OFF_QF = OFF_ST + (size_t)S_ * 48;
constexpr size_t OFF_KF = OFF_QF + (size_t)32 * RT * 4 * 1024;
constexpr size_t OFF_UF = OFF_KF + (size_t)32 * RT * 4 * 1024;
constexpr size_t OFF_PXF = OFF_UF + (size_t)NSEG * 80 * 16 * 1024;
constexpr size_t OFF_PXM = OFF_PXF + (size_t)NSEG * 32 * 2 * 1024;
constexpr size_t OFF_INV = OFF_PXM + (size_t)NSEG * 8 * 2 * 1024;
constexpr size_t OFF_X1T = OFF_INV + (size_t)40 * NFK * 1024;
constexpr size_t OFF_ZT = OFF_X1T + (size_t)160 * S_ * 8;
constexpr size_t OFF_WT = OFF_ZT + (size_t)161 * S_ * 8;
constexpr size_t OFF_DBG = OFF_WT + (size_t)321 * S_ * 4;
constexpr size_t OFF_END = OFF_DBG + (size_t)1048576;
static_assert(OFF_END <= ((size_t)256 << 20), "workspace too large");

constexpr int AUX_SC1 = 16;
#define PH(k)

__device__ __forceinline__ f32x4 mfma(v4u a, v4u b, f32x4 c) {
  return __builtin_amdgcn_mfma_f32_16x16x32_bf16(__builtin_bit_cast(bf16x8, a), __builtin_bit_cast(bf16x8, b), c, 0, 0, 0);
}
__device__ __forceinline__ unsigned pk(float a, float b) {
  f32x2 v = {a, b};
  return __builtin_bit_cast(unsigned, __builtin_convertvector(v, bf16x2));
}
__device__ __forceinline__ float lo_f(unsigned u) { return __builtin_bit_cast(float, u << 16); }
__device__ __forceinline__ float hi_f(unsigned u) { return __builtin_bit_cast(float, u & 0xFFFF0000u); }
__device__ __forceinline__ float bfr(float x) { return lo_f(pk(x, x)); }
__device__ __forceinline__ v4u pk8(f32x4 a, f32x4 b) { return v4u{pk(a.x, a.y), pk(a.z, a.w), pk(b.x, b.y), pk(b.z, b.w)}; }
__device__ __forceinline__ int sigma(int g, int jj) { return 16 * (jj >> 2) + 4 * g + (jj & 3); }
__device__ __forceinline__ int colmap(int c8, int jj) {
  const int p = c8 >> 3, ga = (c8 >> 1) & 3, half = c8 & 1;
  return 32 * (2 * p + (ga >> 1)) + 16 * ((jj >> 1) & 1) + 4 * (2 * (ga & 1) + (jj >> 2)) + 2 * half + (jj & 1);
}
__device__ __forceinline__ int pool_base(const void* ws) { return (int)((0u - (unsigned)(uintptr_t)ws) & (unsigned)(PAGE - 1)); }
__device__ __forceinline__ unsigned xcc_id() { return (unsigned)__builtin_amdgcn_s_getreg(20 | (3 << 11)) & 7u; }
__device__ __forceinline__ float shx(float v, int m) { return __shfl_xor(v, m); }
__device__ __forceinline__ unsigned nosent(unsigned x) { return x < 0xFFFFFFFEu ? x : 0xFFFFFFFEu; }

__device__ __forceinline__ f32x2 frag_stat(v4u c) {
  const float v[8] = {lo_f(c.x), hi_f(c.x), lo_f(c.y), hi_f(c.y), lo_f(c.z), hi_f(c.z), lo_f(c.w), hi_f(c.w)};
  float q = 0.f;
#pragma unroll
  for (int j = 0; j < 8; ++j) q = __builtin_fmaf(v[j], v[j], q);
  return f32x2{((v[0] + v[1]) + (v[2] + v[3])) + ((v[4] + v[5]) + (v[6] + v[7])), q};
}
__device__ __forceinline__ f32x2 rows_sum(f32x2 a) {
  a.x += shx(a.x, 16); a.y += shx(a.y, 16); a.x += shx(a.x, 32); a.y += shx(a.y, 32);
  return a;
}

typedef int v4i __attribute__((ext_vector_type(4)));
__device__ __forceinline__ int y_region(unsigned long long sel, int s) { return (s * 80 + (s * 43 >> 7) * 16 + (int)((sel >> s) & 1ull)) * 8192; }

__device__ __forceinline__ int lane_id() {
  int l;
  asm volatile("v_mbcnt_lo_u32_b32 %0, -1, 0\n\tv_mbcnt_hi_u32_b32 %0, -1, %0" : "=v"(l));
  return l;
}
#define TIDX (wv_ * 64 + lane_id())
typedef __amdgpu_buffer_rsrc_t rsrc_t;
__device__ __forceinline__ rsrc_t ws_rsrc(unsigned char* ws) { return __builtin_amdgcn_make_buffer_rsrc(ws, 0, (int)OFF_END, 0x00020000); }
__device__ __forceinline__ v4u ldc(rsrc_t rs, int off) { return __builtin_amdgcn_raw_buffer_load_b128(rs, off, 0, AUX_SC1); }
template <bool OLD> __device__ __forceinline__ v4u ldt(rsrc_t rs, int off) { return __builtin_amdgcn_raw_buffer_load_b128(rs, off, 0, OLD ? 0 : AUX_SC1); }
__device__ __forceinline__ void stc(rsrc_t rs, int off, v4u v) { __builtin_amdgcn_raw_buffer_store_b128(v, rs, off, 0, AUX_SC1); }

typedef __attribute__((address_space(1))) unsigned gu32;
__device__ __forceinline__ void prog_prewait(int wv_, unsigned char* ws, int off) {
  if (TIDX == 0) {
    gu32* p = (gu32*)(ws + off);
    for (unsigned spins = 0; spins < (1u << 16); ++spins) {
      if (__hip_atomic_load(p, __ATOMIC_RELAXED, __HIP_MEMORY_SCOPE_AGENT) != 0xFFFFFFFFu) break;
      __builtin_amdgcn_s_sleep(48);
    }
  }
}
__device__ __forceinline__ void prog_wait(int wv_, unsigned char* ws, int off) {
  if (wv_ == 0) {
    const int l = lane_id();
    gu32* p0 = (gu32*)(ws + off + l * 64);
    for (unsigned spins = 0; spins < (1u << 17); ++spins) {
      const unsigned a = __hip_atomic_load(p0, __ATOMIC_RELAXED, __HIP_MEMORY_SCOPE_AGENT);
      if (!__any(a == 0xFFFFFFFFu)) break;
      __builtin_amdgcn_s_sleep(8);
    }
  }
  __syncthreads();
}
__device__ __forceinline__ void flags32_wait(int wv_, unsigned char* ws, int off) {
  if (wv_ == 0) {
    const int l = lane_id();
    __attribute__((address_space(1))) unsigned long long* p0 = (__attribute__((address_space(1))) unsigned long long*)(ws + off + (l & 31) * 64 + (l >> 5) * 8);
    for (unsigned spins = 0; spins < (1u << 17); ++spins) {
      const unsigned long long v = __hip_atomic_load(p0, __ATOMIC_RELAXED, __HIP_MEMORY_SCOPE_AGENT);
      if (!__any(max((unsigned)v, (unsigned)(v >> 32)) == 0xFFFFFFFFu)) break;
      __builtin_amdgcn_s_sleep(16);
    }
  }
  __syncthreads();
}
__device__ __forceinline__ void dep_signal(int wv_, unsigned char* ws, int off, unsigned inc = 1u) {
  asm volatile("s_waitcnt vmcnt(0)" ::: "memory");
  __syncthreads();
  if (TIDX == 0) __hip_atomic_fetch_add((gu32*)(ws + off), inc, __ATOMIC_RELAXED, __HIP_MEMORY_SCOPE_AGENT);
}

template <int SP> __device__ __forceinline__ void fan_signal(int wv_, unsigned char* ws, int fan, int i) {
  asm volatile("s_waitcnt vmcnt(0)" ::: "memory");
  __syncthreads();
  const int l = lane_id();
  if (wv_ == 0 && l < FNG) __hip_atomic_store((gu32*)(ws + fan + l * FAN_G + i * SP), 1u, __ATOMIC_RELAXED, __HIP_MEMORY_SCOPE_AGENT);
}
template <int SP, int N> __device__ __forceinline__ void fan_wait(int wv_, unsigned char* ws, int fan, int salt) {
  if (wv_ * 64 < N) {
    gu32* p = (gu32*)(ws + fan + (((int)xcc_id() * (FNG / 8) + (salt & (FNG / 8 - 1))) * FAN_G + min(TIDX, N - 1) * SP));
    for (unsigned spins = 0; spins < (1u << 17); ++spins) {
      const unsigned long long m = __ballot(__hip_atomic_load(p, __ATOMIC_RELAXED, __HIP_MEMORY_SCOPE_AGENT) == 0xFFFFFFFFu);
      if (m == 0) break;
      __builtin_amdgcn_s_sleep(FNAP);
      if (N == 20 && (m & 0x3FF)) __builtin_amdgcn_s_sleep(40);
    }
  }
  __syncthreads();
}

__device__ __forceinline__ void stat_put(v4u* lds, int wv, int rt, int lane, v4u y) {
  const f32x2 st = rows_sum(frag_stat(y));
  if (lane < 16) ((f32x2*)(lds + 5120))[(wv * 2 + rt) * 16 + lane] = st;
}
__device__ __forceinline__ void stat_store(rsrc_t rs, v4u* lds, int wv, int lane, int sg, int cg) {
  __syncthreads();
  if (wv == 0) {
    const f32x2* sp = (const f32x2*)(lds + 5120) + (lane & 31);
    const f32x2 st = (sp[0] + sp[32]) + (sp[64] + sp[96]);
    __builtin_amdgcn_raw_buffer_store_b64(__builtin_bit_cast(v2u, st), rs, lane < 32 ? (int)OFF_ST + ((sg * 32 + lane) * 6 + cg) * 8 : 0x7FFFFFF0, 0, AUX_SC1);
  }
}

__device__ __forceinline__ void job_out(int wv_, unsigned char* ws, rsrc_t rs, v4u* lds, unsigned long long ysel, int layer, int s, int cg) {
  const int lane = lane_id(), wv = wv_, g = lane >> 4, r = lane & 15;
  const int kt = cg * 4 + wv;
  const int c16 = 2 * kt + (r >> 3), ci = (g * 16 + (r & 4) * 2 + (r & 3)) * 16;
  const int ubase = (int)OFF_UF + (s * 40 + c16) * 32768 + ci;
  const int ybase = pool_base(ws) + P_Y + y_region(ysel, s) + c16 * 16384 + ci;
  v4u z[40][2];
#pragma unroll
  for (int ks = 0; ks < 32; ++ks) { z[ks][0] = ldc(rs, ubase + ks * 1024); z[ks][1] = ldc(rs, ubase + ks * 1024 + 64); }
  const int xo = (int)OFF_XR + ((2 * s * KT + kt) * 64 + lane) * 16;
  const v4u x0 = ldc(rs, xo), x1 = ldc(rs, xo + KT * 1024);
  if (s > 1) prog_prewait(wv_, ws, prog_off(layer, s - 2, (s + cg * 7) & 31));
#define OSTAMP(i)
  OSTAMP(4)
  prog_wait(wv_, ws, prog_off(layer, s, 0));
  OSTAMP(0)
  {
    const unsigned l0 = (unsigned)(uintptr_t)(__attribute__((address_space(3))) v4u*)lds;
    const int vo = lane * 16;
#pragma unroll
    for (int q = 0; q < 5; ++q) {
      const unsigned d = l0 + (q < 4 ? wv * 16384 + q * 4096 : 65536 + wv * 4096);
      const int so = q < 4 ? (int)OFF_PXF + s * 65536 + wv * 16384 + q * 4096 : (int)OFF_PXM + s * 16384 + wv * 4096;
      asm volatile("s_mov_b32 m0, %0\n\ts_nop 0\n\tbuffer_load_dwordx4 %1, %2, %3 offen sc1 lds\n\t"
                   "buffer_load_dwordx4 %1, %2, %3 offen offset:1024 sc1 lds\n\tbuffer_load_dwordx4 %1, %2, %3 offen offset:2048 sc1 lds\n\t"
                   "buffer_load_dwordx4 %1, %2, %3 offen offset:3072 sc1 lds" ::"s"(d), "v"(vo), "s"(rs), "s"(so) : "m0", "memory");
    }
#pragma unroll
    for (int hq = 0; hq < 8; ++hq) { z[32 + hq][0] = ldc(rs, ybase + hq * 1024); z[32 + hq][1] = ldc(rs, ybase + hq * 1024 + 64); }
  }
  f32x4 acc[2][2] = {{{0, 0, 0, 0}, {0, 0, 0, 0}}, {{0, 0, 0, 0}, {0, 0, 0, 0}}};
  asm volatile("s_waitcnt vmcnt(16)\n\ts_barrier" ::: "memory");
  OSTAMP(1)
  v4u pf[2][8];
#pragma unroll
  for (int i = 0; i < 8; ++i) pf[0][i] = lds[i * 64 + lane];
#pragma unroll
  for (int b = 0; b < 10; ++b) {
    if (b < 9) {
#pragma unroll
      for (int i = 0; i < 8; ++i) pf[(b + 1) & 1][i] = lds[((b + 1) * 8 + i) * 64 + lane];
    }
    __builtin_amdgcn_sched_barrier(0);
#pragma unroll
    for (int i = 0; i < 8; ++i) {
      const v4u z0 = z[b * 4 + (i >> 1)][0], z1 = z[b * 4 + (i >> 1)][1];
      acc[0][i & 1] = mfma(b < 8 ? z0 : z0.xzyw, pf[b & 1][i], acc[0][i & 1]);
      acc[1][i & 1] = mfma(b < 8 ? z1 : z1.xzyw, pf[b & 1][i], acc[1][i & 1]);
    }
    __builtin_amdgcn_sched_barrier(0);
  }
#pragma unroll
  for (int qt = 0; qt < 2; ++qt) {
    const v4u x = qt ? x1 : x0;
    const auto s0 = __builtin_amdgcn_permlane16_swap(pk(acc[0][qt].x, acc[0][qt].y), pk(acc[1][qt].x, acc[1][qt].y), false, false);
    const auto s1 = __builtin_amdgcn_permlane16_swap(pk(acc[0][qt].z, acc[0][qt].w), pk(acc[1][qt].z, acc[1][qt].w), false, false);
    v4u y;
    y.x = pk(lo_f(x.x) + lo_f(s0[0]), hi_f(x.x) + hi_f(s0[0]));
    y.y = pk(lo_f(x.y) + lo_f(s0[1]), hi_f(x.y) + hi_f(s0[1]));
    y.z = pk(lo_f(x.z) + lo_f(s1[0]), hi_f(x.z) + hi_f(s1[0]));
    y.w = pk(lo_f(x.w) + lo_f(s1[1]), hi_f(x.w) + hi_f(s1[1]));
    stc(rs, xo + qt * KT * 1024, y);
    stat_put(lds, wv, qt, lane, y);
  }
  OSTAMP(2)
  stat_store(rs, lds, wv, lane, s, cg);
}

typedef unsigned v8u __attribute__((ext_vector_type(8)));
typedef unsigned v16u __attribute__((ext_vector_type(16)));
typedef unsigned v32u __attribute__((ext_vector_type(32)));
__device__ __forceinline__ float cosi(int i) { return __builtin_amdgcn_cosf((float)i * (1.f / 2048)); }
__device__ __forceinline__ float sini(int i) { return __builtin_amdgcn_sinf((float)i * (1.f / 2048)); }
__device__ __forceinline__ void cis8(v32u& d, int x0, int dx, int d4, unsigned m) {
#pragma unroll
  for (int q = 0; q < 4; ++q) {
    const int x = x0 + dx * 2 * (q & 1) + d4 * (q >> 1);
    d[q] = pk(cosi(x), cosi(x + dx)); d[4 + q] = d[q] ^ m;
    d[8 + q] = pk(sini(x), sini(x + dx)); d[12 + q] = d[8 + q] ^ m;
  }
}
#define LDS_V4(a) (*(__attribute__((address_space(3))) v4u*)(uintptr_t)(a))
#define LDS_U32(a) (*(__attribute__((address_space(3))) unsigned*)(uintptr_t)(a))
#define LDS_V2(a) (*(__attribute__((address_space(3))) v2u*)(uintptr_t)(a))
#define COL_ASM \
   "s_waitcnt lgkmcnt(0)\n\t" "s_barrier\n\t" "ds_read_b64_tr_b16 v[136:137], v16 offset:0\n\t" "ds_read_b64_tr_b16 v[138:139], v16 offset:1056\n\t" \
   "ds_read_b64_tr_b16 v[140:141], v16 offset:8448\n\t" "ds_read_b64_tr_b16 v[142:143], v16 offset:9504\n\t" \
   "ds_read_b64_tr_b16 v[144:145], v16 offset:8\n\t" "ds_read_b64_tr_b16 v[146:147], v16 offset:1064\n\t" \
   "ds_read_b64_tr_b16 v[148:149], v16 offset:8456\n\t" "ds_read_b64_tr_b16 v[150:151], v16 offset:9512\n\t" "s_waitcnt lgkmcnt(4)\n\t" \
   "v_mfma_f32_16x16x32_bf16 v[160:163], v[136:139], v[32:35], 0\n\t" "v_mfma_f32_16x16x32_bf16 v[164:167], v[136:139], v[40:43], 0\n\t" \
   "v_mfma_f32_16x16x32_bf16 v[160:163], v[140:143], v[36:39], v[160:163]\n\t" \
   "v_mfma_f32_16x16x32_bf16 v[164:167], v[140:143], v[44:47], v[164:167]\n\t" "ds_read_b64_tr_b16 v[152:153], v16 offset:16\n\t" \
   "ds_read_b64_tr_b16 v[154:155], v16 offset:1072\n\t" "ds_read_b64_tr_b16 v[156:157], v16 offset:8464\n\t" \
   "ds_read_b64_tr_b16 v[158:159], v16 offset:9520\n\t" "s_nop 3\n\t" "s_waitcnt lgkmcnt(4)\n\t" \
   "v_mfma_f32_16x16x32_bf16 v[168:171], v[144:147], v[32:35], 0\n\t" "v_add_f32 v160, v160, v165\n\t" "v_sub_f32 v161, v161, v164\n\t" \
   "v_mfma_f32_16x16x32_bf16 v[172:175], v[144:147], v[40:43], 0\n\t" "v_add_f32 v162, v162, v167\n\t" "v_sub_f32 v163, v163, v166\n\t" \
   "v_mfma_f32_16x16x32_bf16 v[168:171], v[148:151], v[36:39], v[168:171]\n\t" "v_mul_f32 v24, v161, v49\n\t" "v_mul_f32 v25, v161, v48\n\t" \
   "v_mfma_f32_16x16x32_bf16 v[172:175], v[148:151], v[44:47], v[172:175]\n\t" "v_mul_f32 v26, v163, v49\n\t" "v_mul_f32 v27, v163, v48\n\t" \
   "v_fma_f32 v177, v160, v49, v25\n\t" "v_fma_f32 v176, v160, v48, -v24\n\t" "v_fma_f32 v179, v162, v49, v27\n\t" \
   "v_fma_f32 v178, v162, v48, -v26\n\t" "ds_read_b64_tr_b16 v[136:137], v16 offset:24\n\t" "ds_read_b64_tr_b16 v[138:139], v16 offset:1080\n\t" \
   "ds_read_b64_tr_b16 v[140:141], v16 offset:8472\n\t" "ds_read_b64_tr_b16 v[142:143], v16 offset:9528\n\t" "s_waitcnt lgkmcnt(4)\n\t" \
   "v_mfma_f32_16x16x32_bf16 v[160:163], v[152:155], v[32:35], 0\n\t" "v_add_f32 v168, v168, v173\n\t" "v_sub_f32 v169, v169, v172\n\t" \
   "v_mfma_f32_16x16x32_bf16 v[164:167], v[152:155], v[40:43], 0\n\t" "v_add_f32 v170, v170, v175\n\t" "v_sub_f32 v171, v171, v174\n\t" \
   "v_mfma_f32_16x16x32_bf16 v[160:163], v[156:159], v[36:39], v[160:163]\n\t" "v_mul_f32 v24, v169, v51\n\t" "v_mul_f32 v25, v169, v50\n\t" \
   "v_mfma_f32_16x16x32_bf16 v[164:167], v[156:159], v[44:47], v[164:167]\n\t" "v_mul_f32 v26, v171, v51\n\t" "v_mul_f32 v27, v171, v50\n\t" \
   "v_fma_f32 v181, v168, v51, v25\n\t" "v_fma_f32 v180, v168, v50, -v24\n\t" "v_fma_f32 v183, v170, v51, v27\n\t" \
   "v_fma_f32 v182, v170, v50, -v26\n\t" "v_cvt_pk_bf16_f32 v12, v177, v181\n\t" "v_cvt_pk_bf16_f32 v8, v176, v180\n\t" \
   "v_cvt_pk_bf16_f32 v188, v179, v183\n\t" "v_cvt_pk_bf16_f32 v184, v178, v182\n\t" "ds_read_b64_tr_b16 v[144:145], v16 offset:128\n\t" \
   "ds_read_b64_tr_b16 v[146:147], v16 offset:1184\n\t" "ds_read_b64_tr_b16 v[148:149], v16 offset:8576\n\t" \
   "ds_read_b64_tr_b16 v[150:151], v16 offset:9632\n\t" "s_waitcnt lgkmcnt(4)\n\t" "v_mfma_f32_16x16x32_bf16 v[168:171], v[136:139], v[32:35], 0\n\t" \
   "v_add_f32 v160, v160, v165\n\t" "v_sub_f32 v161, v161, v164\n\t" "v_mfma_f32_16x16x32_bf16 v[172:175], v[136:139], v[40:43], 0\n\t" \
   "v_add_f32 v162, v162, v167\n\t" "v_sub_f32 v163, v163, v166\n\t" "v_mfma_f32_16x16x32_bf16 v[168:171], v[140:143], v[36:39], v[168:171]\n\t" \
   "v_mul_f32 v24, v161, v53\n\t" "v_mul_f32 v25, v161, v52\n\t" "v_mfma_f32_16x16x32_bf16 v[172:175], v[140:143], v[44:47], v[172:175]\n\t" \
   "v_mul_f32 v26, v163, v53\n\t" "v_mul_f32 v27, v163, v52\n\t" "v_fma_f32 v177, v160, v53, v25\n\t" "v_fma_f32 v176, v160, v52, -v24\n\t" \
   "v_fma_f32 v179, v162, v53, v27\n\t" "v_fma_f32 v178, v162, v52, -v26\n\t" "ds_read_b64_tr_b16 v[152:153], v16 offset:136\n\t" \
   "ds_read_b64_tr_b16 v[154:155], v16 offset:1192\n\t" "ds_read_b64_tr_b16 v[156:157], v16 offset:8584\n\t" \
   "ds_read_b64_tr_b16 v[158:159], v16 offset:9640\n\t" "s_waitcnt lgkmcnt(4)\n\t" "v_mfma_f32_16x16x32_bf16 v[160:163], v[144:147], v[32:35], 0\n\t" \
   "v_add_f32 v168, v168, v173\n\t" "v_sub_f32 v169, v169, v172\n\t" "v_mfma_f32_16x16x32_bf16 v[164:167], v[144:147], v[40:43], 0\n\t" \
   "v_add_f32 v170, v170, v175\n\t" "v_sub_f32 v171, v171, v174\n\t" "v_mfma_f32_16x16x32_bf16 v[160:163], v[148:151], v[36:39], v[160:163]\n\t" \
   "v_mul_f32 v24, v169, v55\n\t" "v_mul_f32 v25, v169, v54\n\t" "v_mfma_f32_16x16x32_bf16 v[164:167], v[148:151], v[44:47], v[164:167]\n\t" \
   "v_mul_f32 v26, v171, v55\n\t" "v_mul_f32 v27, v171, v54\n\t" "v_fma_f32 v181, v168, v55, v25\n\t" "v_fma_f32 v180, v168, v54, -v24\n\t" \
   "v_fma_f32 v183, v170, v55, v27\n\t" "v_fma_f32 v182, v170, v54, -v26\n\t" "v_cvt_pk_bf16_f32 v13, v177, v181\n\t" \
   "v_cvt_pk_bf16_f32 v9, v176, v180\n\t" "v_cvt_pk_bf16_f32 v189, v179, v183\n\t" "v_cvt_pk_bf16_f32 v185, v178, v182\n\t" \
   "ds_read_b64_tr_b16 v[136:137], v16 offset:144\n\t" "ds_read_b64_tr_b16 v[138:139], v16 offset:1200\n\t" \
   "ds_read_b64_tr_b16 v[140:141], v16 offset:8592\n\t" "ds_read_b64_tr_b16 v[142:143], v16 offset:9648\n\t" "s_waitcnt lgkmcnt(4)\n\t" \
   "v_mfma_f32_16x16x32_bf16 v[168:171], v[152:155], v[32:35], 0\n\t" "v_add_f32 v160, v160, v165\n\t" "v_sub_f32 v161, v161, v164\n\t" \
   "v_mfma_f32_16x16x32_bf16 v[172:175], v[152:155], v[40:43], 0\n\t" "v_add_f32 v162, v162, v167\n\t" "v_sub_f32 v163, v163, v166\n\t" \
   "v_mfma_f32_16x16x32_bf16 v[168:171], v[156:159], v[36:39], v[168:171]\n\t" "v_mul_f32 v24, v161, v57\n\t" "v_mul_f32 v25, v161, v56\n\t" \
   "v_mfma_f32_16x16x32_bf16 v[172:175], v[156:159], v[44:47], v[172:175]\n\t" "v_mul_f32 v26, v163, v57\n\t" "v_mul_f32 v27, v163, v56\n\t" \
   "v_fma_f32 v177, v160, v57, v25\n\t" "v_fma_f32 v176, v160, v56, -v24\n\t" "v_fma_f32 v179, v162, v57, v27\n\t" \
   "v_fma_f32 v178, v162, v56, -v26\n\t" "ds_read_b64_tr_b16 v[144:145], v16 offset:152\n\t" "ds_read_b64_tr_b16 v[146:147], v16 offset:1208\n\t" \
   "ds_read_b64_tr_b16 v[148:149], v16 offset:8600\n\t" "ds_read_b64_tr_b16 v[150:151], v16 offset:9656\n\t" "s_waitcnt lgkmcnt(4)\n\t" \
   "v_mfma_f32_16x16x32_bf16 v[160:163], v[136:139], v[32:35], 0\n\t" "v_add_f32 v168, v168, v173\n\t" "v_sub_f32 v169, v169, v172\n\t" \
   "v_mfma_f32_16x16x32_bf16 v[164:167], v[136:139], v[40:43], 0\n\t" "v_add_f32 v170, v170, v175\n\t" "v_sub_f32 v171, v171, v174\n\t" \
   "v_mfma_f32_16x16x32_bf16 v[160:163], v[140:143], v[36:39], v[160:163]\n\t" "v_mul_f32 v24, v169, v59\n\t" "v_mul_f32 v25, v169, v58\n\t" \
   "v_mfma_f32_16x16x32_bf16 v[164:167], v[140:143], v[44:47], v[164:167]\n\t" "v_mul_f32 v26, v171, v59\n\t" "v_mul_f32 v27, v171, v58\n\t" \
   "v_fma_f32 v181, v168, v59, v25\n\t" "v_fma_f32 v180, v168, v58, -v24\n\t" "v_fma_f32 v183, v170, v59, v27\n\t" \
   "v_fma_f32 v182, v170, v58, -v26\n\t" "v_cvt_pk_bf16_f32 v14, v177, v181\n\t" "v_cvt_pk_bf16_f32 v10, v176, v180\n\t" \
   "v_cvt_pk_bf16_f32 v190, v179, v183\n\t" "v_cvt_pk_bf16_f32 v186, v178, v182\n\t" "s_nop 1\n\t" "s_waitcnt lgkmcnt(0)\n\t" \
   "v_mfma_f32_16x16x32_bf16 v[168:171], v[144:147], v[32:35], 0\n\t" "v_add_f32 v160, v160, v165\n\t" "v_sub_f32 v161, v161, v164\n\t" \
   "v_mfma_f32_16x16x32_bf16 v[172:175], v[144:147], v[40:43], 0\n\t" "v_add_f32 v162, v162, v167\n\t" "v_sub_f32 v163, v163, v166\n\t" \
   "v_mfma_f32_16x16x32_bf16 v[168:171], v[148:151], v[36:39], v[168:171]\n\t" "v_mul_f32 v24, v161, v61\n\t" "v_mul_f32 v25, v161, v60\n\t" \
   "v_mfma_f32_16x16x32_bf16 v[172:175], v[148:151], v[44:47], v[172:175]\n\t" "v_mul_f32 v26, v163, v61\n\t" "v_mul_f32 v27, v163, v60\n\t" \
   "v_fma_f32 v177, v160, v61, v25\n\t" "v_fma_f32 v176, v160, v60, -v24\n\t" "v_fma_f32 v179, v162, v61, v27\n\t" \
   "v_fma_f32 v178, v162, v60, -v26\n\t" "s_nop 1\n\t" "v_add_f32 v168, v168, v173\n\t" "v_sub_f32 v169, v169, v172\n\t" "v_mul_f32 v24, v169, v63\n\t" \
   "v_mul_f32 v25, v169, v62\n\t" "v_fma_f32 v181, v168, v63, v25\n\t" "v_fma_f32 v180, v168, v62, -v24\n\t" "v_cvt_pk_bf16_f32 v15, v177, v181\n\t" \
   "v_cvt_pk_bf16_f32 v11, v176, v180\n\t" "s_nop 1\n\t" "v_mfma_f32_16x16x32_bf16 v[160:163], v[64:67], v[8:11], 0\n\t" \
   "v_add_f32 v170, v170, v175\n\t" "v_sub_f32 v171, v171, v174\n\t" "v_mfma_f32_16x16x32_bf16 v[164:167], v[64:67], v[12:15], 0\n\t" \
   "v_mul_f32 v24, v171, v63\n\t" "v_mfma_f32_16x16x32_bf16 v[160:163], v[72:75], v[12:15], v[160:163]\n\t" "v_mul_f32 v25, v171, v62\n\t" \
   "v_mfma_f32_16x16x32_bf16 v[164:167], v[128:131], v[8:11], v[164:167]\n\t" "v_fma_f32 v183, v170, v63, v25\n\t" \
   "v_fma_f32 v182, v170, v62, -v24\n\t" "v_cvt_pk_bf16_f32 v191, v179, v183\n\t" "v_cvt_pk_bf16_f32 v187, v178, v182\n\t" "s_nop 4\n\t" \
   "v_mfma_f32_16x16x32_bf16 v[168:171], v[68:71], v[8:11], 0\n\t" "v_mul_f32 v24, v164, v97\n\t" "v_mul_f32 v25, v164, v96\n\t" \
   "v_mul_f32 v26, v165, v99\n\t" "v_mfma_f32_16x16x32_bf16 v[172:175], v[68:71], v[12:15], 0\n\t" "v_mul_f32 v27, v165, v98\n\t" \
   "v_mul_f32 v28, v166, v101\n\t" "v_mul_f32 v29, v166, v100\n\t" "v_mfma_f32_16x16x32_bf16 v[168:171], v[76:79], v[12:15], v[168:171]\n\t" \
   "v_mul_f32 v30, v167, v103\n\t" "v_mul_f32 v31, v167, v102\n\t" "v_fma_f32 v164, v160, v97, v25\n\t" \
   "v_mfma_f32_16x16x32_bf16 v[172:175], v[132:135], v[8:11], v[172:175]\n\t" "v_fma_f32 v160, v160, v96, -v24\n\t" \
   "v_fma_f32 v165, v161, v99, v27\n\t" "v_fma_f32 v161, v161, v98, -v26\n\t" "v_fma_f32 v166, v162, v101, v29\n\t" \
   "v_fma_f32 v162, v162, v100, -v28\n\t" "v_fma_f32 v167, v163, v103, v31\n\t" "v_fma_f32 v163, v163, v102, -v30\n\t" \
   "v_cvt_pk_bf16_f32 v140, v164, v165\n\t" "v_cvt_pk_bf16_f32 v136, v160, v161\n\t" "v_cvt_pk_bf16_f32 v141, v166, v167\n\t" \
   "v_cvt_pk_bf16_f32 v137, v162, v163\n\t" "v_mfma_f32_16x16x32_bf16 v[160:163], v[64:67], v[184:187], 0\n\t" "v_mul_f32 v24, v172, v105\n\t" \
   "v_mul_f32 v25, v172, v104\n\t" "v_mul_f32 v26, v173, v107\n\t" "v_mfma_f32_16x16x32_bf16 v[164:167], v[64:67], v[188:191], 0\n\t" \
   "v_mul_f32 v27, v173, v106\n\t" "v_mul_f32 v28, v174, v109\n\t" "v_mul_f32 v29, v174, v108\n\t" \
   "v_mfma_f32_16x16x32_bf16 v[160:163], v[72:75], v[188:191], v[160:163]\n\t" "v_mul_f32 v30, v175, v111\n\t" "v_mul_f32 v31, v175, v110\n\t" \
   "v_fma_f32 v172, v168, v105, v25\n\t" "v_mfma_f32_16x16x32_bf16 v[164:167], v[128:131], v[184:187], v[164:167]\n\t" \
   "v_fma_f32 v168, v168, v104, -v24\n\t" "v_fma_f32 v173, v169, v107, v27\n\t" "v_fma_f32 v169, v169, v106, -v26\n\t" \
   "v_fma_f32 v174, v170, v109, v29\n\t" "v_fma_f32 v170, v170, v108, -v28\n\t" "v_fma_f32 v175, v171, v111, v31\n\t" \
   "v_fma_f32 v171, v171, v110, -v30\n\t" "v_cvt_pk_bf16_f32 v142, v172, v173\n\t" "v_cvt_pk_bf16_f32 v138, v168, v169\n\t" \
   "v_cvt_pk_bf16_f32 v143, v174, v175\n\t" "v_cvt_pk_bf16_f32 v139, v170, v171\n\t" "v_mfma_f32_16x16x32_bf16 v[168:171], v[68:71], v[184:187], 0\n\t" \
   "v_mul_f32 v24, v164, v113\n\t" "v_mul_f32 v25, v164, v112\n\t" "v_mul_f32 v26, v165, v115\n\t" \
   "v_mfma_f32_16x16x32_bf16 v[172:175], v[68:71], v[188:191], 0\n\t" "v_mul_f32 v27, v165, v114\n\t" "v_mul_f32 v28, v166, v117\n\t" \
   "v_mul_f32 v29, v166, v116\n\t" "v_mfma_f32_16x16x32_bf16 v[168:171], v[76:79], v[188:191], v[168:171]\n\t" "v_mul_f32 v30, v167, v119\n\t" \
   "v_mul_f32 v31, v167, v118\n\t" "v_fma_f32 v164, v160, v113, v25\n\t" "v_mfma_f32_16x16x32_bf16 v[172:175], v[132:135], v[184:187], v[172:175]\n\t" \
   "v_fma_f32 v160, v160, v112, -v24\n\t" "v_fma_f32 v165, v161, v115, v27\n\t" "v_fma_f32 v161, v161, v114, -v26\n\t" \
   "v_fma_f32 v166, v162, v117, v29\n\t" "v_fma_f32 v162, v162, v116, -v28\n\t" "v_fma_f32 v167, v163, v119, v31\n\t" \
   "v_fma_f32 v163, v163, v118, -v30\n\t" "v_cvt_pk_bf16_f32 v148, v164, v165\n\t" "v_cvt_pk_bf16_f32 v144, v160, v161\n\t" \
   "v_cvt_pk_bf16_f32 v149, v166, v167\n\t" "v_cvt_pk_bf16_f32 v145, v162, v163\n\t" "v_mfma_f32_16x16x32_bf16 v[160:163], v[136:139], v[64:67], 0\n\t" \
   "v_mul_f32 v24, v172, v121\n\t" "v_mul_f32 v25, v172, v120\n\t" "v_mul_f32 v26, v173, v123\n\t" \
   "v_mfma_f32_16x16x32_bf16 v[164:167], v[140:143], v[64:67], 0\n\t" "v_mul_f32 v27, v173, v122\n\t" "v_mul_f32 v28, v174, v125\n\t" \
   "v_mul_f32 v29, v174, v124\n\t" "v_mfma_f32_16x16x32_bf16 v[160:163], v[140:143], v[128:131], v[160:163]\n\t" "v_mul_f32 v30, v175, v127\n\t" \
   "v_mul_f32 v31, v175, v126\n\t" "v_fma_f32 v172, v168, v121, v25\n\t" "v_mfma_f32_16x16x32_bf16 v[164:167], v[136:139], v[72:75], v[164:167]\n\t" \
   "v_fma_f32 v168, v168, v120, -v24\n\t" "v_fma_f32 v173, v169, v123, v27\n\t" "v_fma_f32 v169, v169, v122, -v26\n\t" \
   "v_fma_f32 v174, v170, v125, v29\n\t" "v_fma_f32 v170, v170, v124, -v28\n\t" "v_fma_f32 v175, v171, v127, v31\n\t" \
   "v_fma_f32 v171, v171, v126, -v30\n\t" "v_cvt_pk_bf16_f32 v150, v172, v173\n\t" "v_cvt_pk_bf16_f32 v146, v168, v169\n\t" \
   "v_cvt_pk_bf16_f32 v151, v174, v175\n\t" "v_cvt_pk_bf16_f32 v147, v170, v171\n\t" "v_mfma_f32_16x16x32_bf16 v[168:171], v[136:139], v[68:71], 0\n\t" \
   "v_mul_f32 v24, v164, v81\n\t" "v_mul_f32 v25, v164, v80\n\t" "v_mul_f32 v26, v165, v83\n\t" \
   "v_mfma_f32_16x16x32_bf16 v[172:175], v[140:143], v[68:71], 0\n\t" "v_mul_f32 v27, v165, v82\n\t" "v_mul_f32 v28, v166, v85\n\t" \
   "v_mul_f32 v29, v166, v84\n\t" "v_mfma_f32_16x16x32_bf16 v[168:171], v[140:143], v[132:135], v[168:171]\n\t" "v_mul_f32 v30, v167, v87\n\t" \
   "v_mul_f32 v31, v167, v86\n\t" "v_fma_f32 v164, v160, v81, v25\n\t" "v_mfma_f32_16x16x32_bf16 v[172:175], v[136:139], v[76:79], v[172:175]\n\t" \
   "v_fma_f32 v160, v160, v80, -v24\n\t" "v_fma_f32 v165, v161, v83, v27\n\t" "v_fma_f32 v161, v161, v82, -v26\n\t" \
   "v_fma_f32 v166, v162, v85, v29\n\t" "v_fma_f32 v162, v162, v84, -v28\n\t" "v_fma_f32 v167, v163, v87, v31\n\t" \
   "v_fma_f32 v163, v163, v86, -v30\n\t" "v_cvt_pk_bf16_f32 v178, v164, v165\n\t" "v_cvt_pk_bf16_f32 v176, v160, v161\n\t" \
   "v_cvt_pk_bf16_f32 v179, v166, v167\n\t" "v_cvt_pk_bf16_f32 v177, v162, v163\n\t" "ds_write_b64 v17, v[178:179] offset:128\n\t" \
   "ds_write_b64 v17, v[176:177] offset:0\n\t" "v_mfma_f32_16x16x32_bf16 v[160:163], v[144:147], v[64:67], 0\n\t" "v_mul_f32 v24, v172, v89\n\t" \
   "v_mul_f32 v25, v172, v88\n\t" "v_mul_f32 v26, v173, v91\n\t" "v_mfma_f32_16x16x32_bf16 v[164:167], v[148:151], v[64:67], 0\n\t" \
   "v_mul_f32 v27, v173, v90\n\t" "v_mul_f32 v28, v174, v93\n\t" "v_mul_f32 v29, v174, v92\n\t" \
   "v_mfma_f32_16x16x32_bf16 v[160:163], v[148:151], v[128:131], v[160:163]\n\t" "v_mul_f32 v30, v175, v95\n\t" "v_mul_f32 v31, v175, v94\n\t" \
   "v_fma_f32 v172, v168, v89, v25\n\t" "v_mfma_f32_16x16x32_bf16 v[164:167], v[144:147], v[72:75], v[164:167]\n\t" \
   "v_fma_f32 v168, v168, v88, -v24\n\t" "v_fma_f32 v173, v169, v91, v27\n\t" "v_fma_f32 v169, v169, v90, -v26\n\t" \
   "v_fma_f32 v174, v170, v93, v29\n\t" "v_fma_f32 v170, v170, v92, -v28\n\t" "v_fma_f32 v175, v171, v95, v31\n\t" \
   "v_fma_f32 v171, v171, v94, -v30\n\t" "v_cvt_pk_bf16_f32 v182, v172, v173\n\t" "v_cvt_pk_bf16_f32 v180, v168, v169\n\t" \
   "v_cvt_pk_bf16_f32 v183, v174, v175\n\t" "v_cvt_pk_bf16_f32 v181, v170, v171\n\t" "ds_write_b64 v17, v[182:183] offset:8384\n\t" \
   "ds_write_b64 v17, v[180:181] offset:8256\n\t" "v_mfma_f32_16x16x32_bf16 v[168:171], v[144:147], v[68:71], 0\n\t" "v_mul_f32 v24, v164, v81\n\t" \
   "v_mul_f32 v25, v164, v80\n\t" "v_mul_f32 v26, v165, v83\n\t" "v_mfma_f32_16x16x32_bf16 v[172:175], v[148:151], v[68:71], 0\n\t" \
   "v_mul_f32 v27, v165, v82\n\t" "v_mul_f32 v28, v166, v85\n\t" "v_mul_f32 v29, v166, v84\n\t" \
   "v_mfma_f32_16x16x32_bf16 v[168:171], v[148:151], v[132:135], v[168:171]\n\t" "v_mul_f32 v30, v167, v87\n\t" "v_mul_f32 v31, v167, v86\n\t" \
   "v_fma_f32 v164, v160, v81, v25\n\t" "v_mfma_f32_16x16x32_bf16 v[172:175], v[144:147], v[76:79], v[172:175]\n\t" \
   "v_fma_f32 v160, v160, v80, -v24\n\t" "v_fma_f32 v165, v161, v83, v27\n\t" "v_fma_f32 v161, v161, v82, -v26\n\t" \
   "v_fma_f32 v166, v162, v85, v29\n\t" "v_fma_f32 v162, v162, v84, -v28\n\t" "v_fma_f32 v167, v163, v87, v31\n\t" \
   "v_fma_f32 v163, v163, v86, -v30\n\t" "v_cvt_pk_bf16_f32 v178, v164, v165\n\t" "v_cvt_pk_bf16_f32 v176, v160, v161\n\t" \
   "v_cvt_pk_bf16_f32 v179, v166, v167\n\t" "v_cvt_pk_bf16_f32 v177, v162, v163\n\t" "ds_write_b64 v18, v[178:179] offset:128\n\t" \
   "ds_write_b64 v18, v[176:177] offset:0\n\t" "v_mul_f32 v24, v172, v89\n\t" "v_mul_f32 v25, v172, v88\n\t" "v_mul_f32 v26, v173, v91\n\t" \
   "v_mul_f32 v27, v173, v90\n\t" "v_mul_f32 v28, v174, v93\n\t" "v_mul_f32 v29, v174, v92\n\t" "v_mul_f32 v30, v175, v95\n\t" \
   "v_mul_f32 v31, v175, v94\n\t" "v_fma_f32 v172, v168, v89, v25\n\t" "v_fma_f32 v168, v168, v88, -v24\n\t" "v_fma_f32 v173, v169, v91, v27\n\t" \
   "v_fma_f32 v169, v169, v90, -v26\n\t" "v_fma_f32 v174, v170, v93, v29\n\t" "v_fma_f32 v170, v170, v92, -v28\n\t" \
   "v_fma_f32 v175, v171, v95, v31\n\t" "v_fma_f32 v171, v171, v94, -v30\n\t" "v_cvt_pk_bf16_f32 v182, v172, v173\n\t" \
   "v_cvt_pk_bf16_f32 v180, v168, v169\n\t" "v_cvt_pk_bf16_f32 v183, v174, v175\n\t" "v_cvt_pk_bf16_f32 v181, v170, v171\n\t" \
   "ds_write_b64 v18, v[182:183] offset:8384\n\t" "ds_write_b64 v18, v[180:181] offset:8256\n\t" "s_waitcnt lgkmcnt(0)\n\t" "s_barrier\n\t" \
   "ds_read_b128 v[136:139], v19 offset:0\n\t" "ds_read_b128 v[140:143], v20 offset:0\n\t" "ds_read_b128 v[144:147], v19 offset:2064\n\t" \
   "ds_read_b128 v[148:151], v20 offset:2064\n\t" "s_waitcnt lgkmcnt(2)\n\t" "v_mfma_f32_16x16x32_bf16 v[160:163], v[136:139], v[32:35], 0\n\t" \
   "ds_read_b128 v[152:155], v19 offset:4128\n\t" "ds_read_b128 v[156:159], v20 offset:4128\n\t" \
   "v_mfma_f32_16x16x32_bf16 v[164:167], v[136:139], v[40:43], 0\n\t" "v_mfma_f32_16x16x32_bf16 v[160:163], v[140:143], v[36:39], v[160:163]\n\t" \
   "v_mfma_f32_16x16x32_bf16 v[164:167], v[140:143], v[44:47], v[164:167]\n\t" "s_waitcnt lgkmcnt(2)\n\t" \
   "v_mfma_f32_16x16x32_bf16 v[168:171], v[144:147], v[32:35], 0\n\t" "ds_read_b128 v[136:139], v19 offset:6192\n\t" \
   "ds_read_b128 v[140:143], v20 offset:6192\n\t" "v_mfma_f32_16x16x32_bf16 v[172:175], v[144:147], v[40:43], 0\n\t" "v_sub_f32 v160, v160, v165\n\t" \
   "v_add_f32 v161, v161, v164\n\t" "v_sub_f32 v162, v162, v167\n\t" "v_mfma_f32_16x16x32_bf16 v[168:171], v[148:151], v[36:39], v[168:171]\n\t" \
   "v_add_f32 v163, v163, v166\n\t" "v_cvt_pk_bf16_f32 v8, v160, v161\n\t" "v_cvt_pk_bf16_f32 v9, v162, v163\n\t" \
   "v_mfma_f32_16x16x32_bf16 v[172:175], v[148:151], v[44:47], v[172:175]\n\t" "s_waitcnt lgkmcnt(2)\n\t" \
   "v_mfma_f32_16x16x32_bf16 v[160:163], v[152:155], v[32:35], 0\n\t" "ds_read_b128 v[144:147], v19 offset:8256\n\t" \
   "ds_read_b128 v[148:151], v20 offset:8256\n\t" "v_mfma_f32_16x16x32_bf16 v[164:167], v[152:155], v[40:43], 0\n\t" "v_sub_f32 v168, v168, v173\n\t" \
   "v_add_f32 v169, v169, v172\n\t" "v_sub_f32 v170, v170, v175\n\t" "v_mfma_f32_16x16x32_bf16 v[160:163], v[156:159], v[36:39], v[160:163]\n\t" \
   "v_add_f32 v171, v171, v174\n\t" "v_cvt_pk_bf16_f32 v10, v168, v169\n\t" "v_cvt_pk_bf16_f32 v11, v170, v171\n\t" \
   "v_mfma_f32_16x16x32_bf16 v[164:167], v[156:159], v[44:47], v[164:167]\n\t" "buffer_store_dwordx4 v[8:11], v21, %[rs], 0 offen offset:0 sc1\n\t" \
   "s_waitcnt lgkmcnt(2)\n\t" "v_mfma_f32_16x16x32_bf16 v[168:171], v[136:139], v[32:35], 0\n\t" "ds_read_b128 v[152:155], v19 offset:10320\n\t" \
   "ds_read_b128 v[156:159], v20 offset:10320\n\t" "v_mfma_f32_16x16x32_bf16 v[172:175], v[136:139], v[40:43], 0\n\t" "v_sub_f32 v160, v160, v165\n\t" \
   "v_add_f32 v161, v161, v164\n\t" "v_sub_f32 v162, v162, v167\n\t" "v_mfma_f32_16x16x32_bf16 v[168:171], v[140:143], v[36:39], v[168:171]\n\t" \
   "v_add_f32 v163, v163, v166\n\t" "v_cvt_pk_bf16_f32 v12, v160, v161\n\t" "v_cvt_pk_bf16_f32 v13, v162, v163\n\t" \
   "v_mfma_f32_16x16x32_bf16 v[172:175], v[140:143], v[44:47], v[172:175]\n\t" "s_waitcnt lgkmcnt(2)\n\t" \
   "v_mfma_f32_16x16x32_bf16 v[160:163], v[144:147], v[32:35], 0\n\t" "ds_read_b128 v[136:139], v19 offset:12384\n\t" \
   "ds_read_b128 v[140:143], v20 offset:12384\n\t" "v_mfma_f32_16x16x32_bf16 v[164:167], v[144:147], v[40:43], 0\n\t" "v_sub_f32 v168, v168, v173\n\t" \
   "v_add_f32 v169, v169, v172\n\t" "v_sub_f32 v170, v170, v175\n\t" "v_mfma_f32_16x16x32_bf16 v[160:163], v[148:151], v[36:39], v[160:163]\n\t" \
   "v_add_f32 v171, v171, v174\n\t" "v_cvt_pk_bf16_f32 v14, v168, v169\n\t" "v_cvt_pk_bf16_f32 v15, v170, v171\n\t" \
   "v_mfma_f32_16x16x32_bf16 v[164:167], v[148:151], v[44:47], v[164:167]\n\t" "buffer_store_dwordx4 v[12:15], v21, %[rs], 0 offen offset:64 sc1\n\t" \
   "s_waitcnt lgkmcnt(2)\n\t" "v_mfma_f32_16x16x32_bf16 v[168:171], v[152:155], v[32:35], 0\n\t" "ds_read_b128 v[144:147], v19 offset:14448\n\t" \
   "ds_read_b128 v[148:151], v20 offset:14448\n\t" "v_mfma_f32_16x16x32_bf16 v[172:175], v[152:155], v[40:43], 0\n\t" "v_sub_f32 v160, v160, v165\n\t" \
   "v_add_f32 v161, v161, v164\n\t" "v_sub_f32 v162, v162, v167\n\t" "v_mfma_f32_16x16x32_bf16 v[168:171], v[156:159], v[36:39], v[168:171]\n\t" \
   "v_add_f32 v163, v163, v166\n\t" "v_cvt_pk_bf16_f32 v8, v160, v161\n\t" "v_cvt_pk_bf16_f32 v9, v162, v163\n\t" \
   "v_mfma_f32_16x16x32_bf16 v[172:175], v[156:159], v[44:47], v[172:175]\n\t" "s_waitcnt lgkmcnt(2)\n\t" \
   "v_mfma_f32_16x16x32_bf16 v[160:163], v[136:139], v[32:35], 0\n\t" "s_nop 0\n\t" "s_nop 0\n\t" \
   "v_mfma_f32_16x16x32_bf16 v[164:167], v[136:139], v[40:43], 0\n\t" "v_sub_f32 v168, v168, v173\n\t" "v_add_f32 v169, v169, v172\n\t" \
   "v_sub_f32 v170, v170, v175\n\t" "v_mfma_f32_16x16x32_bf16 v[160:163], v[140:143], v[36:39], v[160:163]\n\t" "v_add_f32 v171, v171, v174\n\t" \
   "v_cvt_pk_bf16_f32 v10, v168, v169\n\t" "v_cvt_pk_bf16_f32 v11, v170, v171\n\t" \
   "v_mfma_f32_16x16x32_bf16 v[164:167], v[140:143], v[44:47], v[164:167]\n\t" "buffer_store_dwordx4 v[8:11], v21, %[rs], 0 offen offset:128 sc1\n\t" \
   "s_waitcnt lgkmcnt(0)\n\t" "v_mfma_f32_16x16x32_bf16 v[168:171], v[144:147], v[32:35], 0\n\t" "s_nop 0\n\t" "s_nop 0\n\t" \
   "v_mfma_f32_16x16x32_bf16 v[172:175], v[144:147], v[40:43], 0\n\t" "v_sub_f32 v160, v160, v165\n\t" "v_add_f32 v161, v161, v164\n\t" \
   "v_sub_f32 v162, v162, v167\n\t" "v_mfma_f32_16x16x32_bf16 v[168:171], v[148:151], v[36:39], v[168:171]\n\t" "v_add_f32 v163, v163, v166\n\t" \
   "v_cvt_pk_bf16_f32 v12, v160, v161\n\t" "v_cvt_pk_bf16_f32 v13, v162, v163\n\t" \
   "v_mfma_f32_16x16x32_bf16 v[172:175], v[148:151], v[44:47], v[172:175]\n\t" "s_nop 7\n\t" "v_sub_f32 v168, v168, v173\n\t" \
   "v_add_f32 v169, v169, v172\n\t" "v_sub_f32 v170, v170, v175\n\t" "v_add_f32 v171, v171, v174\n\t" "v_cvt_pk_bf16_f32 v14, v168, v169\n\t" \
   "v_cvt_pk_bf16_f32 v15, v170, v171\n\t" "buffer_store_dwordx4 v[12:15], v21, %[rs], 0 offen offset:192 sc1\n\t" "s_nop 1"
#define COL_CLOB "v8", "v9", "v10", "v11", "v12", "v13", "v14", "v15", "v24", "v25", "v26", "v27", "v28", "v29", "v30", "v31", "v136", "v137", "v138", "v139", "v140", "v141", "v142", "v143", "v144", "v145", "v146", "v147", "v148", "v149", "v150", "v151", "v152", "v153", "v154", "v155", "v156", "v157", "v158", "v159", "v160", "v161", "v162", "v163", "v164", "v165", "v166", "v167", "v168", "v169", "v170", "v171", "v172", "v173", "v174", "v175", "v176", "v177", "v178", "v179", "v180", "v181", "v182", "v183", "v184", "v185", "v186", "v187", "v188", "v189", "v190", "v191"
constexpr int NCOL = 161, COL_I2 = 64 * 264, COL_LDS = COL_I2 + 8 * 2064;
__device__ __forceinline__ void job_col(int wv, unsigned char* ws, rsrc_t rs, unsigned lds, int jb, int go) {
  const int lane = lane_id(), g = lane >> 4, r = lane & 15, j = wv * 64 + lane;
  const int xin = (int)OFF_X1T + (jb == 160 ? 0 : jb) * S_ * 8 + j * 16;
  v4u x[4], wf[4];
  if (go < 0) {
#pragma unroll
    for (int q = 0; q < 4; ++q) x[q] = ldc(rs, xin + q * 4096);
  }
#pragma unroll
  for (int q = 0; q < 4; ++q) {
    const int o = (int)OFF_WT + (jb == 160 ? 320 : 2 * jb + (q >> 1)) * S_ * 4 + ((wv * 2 + (q & 1)) * 64 + lane) * 16;
    wf[q] = go < 0 ? ldt<true>(rs, o) : ldc(rs, o);
  }
  v32u k0, k1, k2;
  v8u k3, ad = (unsigned)lane;
  {
    const int n = 16 * wv + r;
    cis8(k0, n * (512 * (g & 1) + 256 * (g >> 1)), n * 32, n * 128, (r & 1) ? 0x80008000u : 0u);
    cis8(k1, r * g * 256, r * 64, r * 1024, 0x80000000u);
#pragma unroll
    for (int q = 0; q < 8; ++q) {
      const int i = -n * sigma(g, q), k = (r + 16 * (q >> 2)) * (16 * wv + 4 * g + (q & 3));
      const float sc = 1.0f / ((float)S_ * (float)D_);
      k0[16 + 2 * q] = __builtin_bit_cast(unsigned, cosi(i)); k0[17 + 2 * q] = __builtin_bit_cast(unsigned, sini(i));
      k1[16 + 2 * q] = __builtin_bit_cast(unsigned, cosi(k) * sc); k1[17 + 2 * q] = __builtin_bit_cast(unsigned, sini(k) * sc);
      k3[q] = k1[8 + q] ^ 0x80008000u;
    }
    ad[0] = lds + 264 * (16 * (g & 1) + 8 * (g >> 1) + (r >> 2)) + 32 * (r & 3);
    ad[1] = lds + COL_I2 + (2 * (r >> 3) + (r & 1)) * 2064 + ((r >> 1) & 3) * 512 + (g & 1) * 8;
    ad[2] = ad[1] + 256 + (((4 * (wv >> 1) + 2 * (g >> 1) + (wv & 1)) ^ ((r & 6) + 1)) << 4);
    ad[1] += ((4 * (wv >> 1) + 2 * (g >> 1) + (wv & 1)) ^ (r & 6)) << 4;
    ad[3] = lds + COL_I2 + r * 128 + ((g ^ (r >> 1)) << 4);
    ad[4] = lds + COL_I2 + r * 128 + (((4 + g) ^ (r >> 1)) << 4);
    ad[5] = (int)OFF_ZT + (jb * S_ + 32 * (16 * wv + r) + 2 * g) * 8;
    asm volatile("" : "+{v[32:63]}"(k0), "+{v[64:95]}"(k1), "+{v[128:135]}"(k3), "+{v[16:23]}"(ad));
  }
  if (go < 0) __builtin_amdgcn_sched_barrier(0);
  const unsigned sh = jb == 160 ? 16u : 0u, mk = jb == 0 ? 0xFFFFu : ~0u, d = lds + (j >> 4) * 264 + r * 16;
#pragma unroll
  for (int p = 0; p < 2; ++p) {
    if (p == (go < 0)) {
#pragma unroll
      for (int q = 0; q < 16; ++q) { k2[2 * q] = wf[q >> 2][q & 3] << 16; k2[2 * q + 1] = wf[q >> 2][q & 3] & 0xFFFF0000u; }
      asm volatile("" : "+{v[96:127]}"(k2));
    } else {
      if (go >= 0) {
        fan_wait<64, 20>(wv, ws, go, jb);
#pragma unroll
        for (int q = 0; q < 4; ++q) x[q] = ldc(rs, xin + q * 4096);
      }
#define CSTAMP(i)
      CSTAMP(0)
#pragma unroll
      for (int q = 0; q < 4; ++q) {
        LDS_V2(d + q * 16 * 264) = v2u{(x[q].x >> sh) & mk, x[q].y};
        LDS_V2(d + q * 16 * 264 + 8) = v2u{(x[q].z >> sh) & mk, x[q].w};
      }
    }
  }
  CSTAMP(1)
  asm volatile(COL_ASM ::"{v[32:63]}"(k0), "{v[64:95]}"(k1), "{v[96:127]}"(k2), "{v[128:135]}"(k3), "{v[16:23]}"(ad), [rs] "s"(rs) : COL_CLOB, "memory");
  CSTAMP(2)
  CSTAMP(3)
}
template <bool FINAL>
__device__ __forceinline__ void job_idft(int wv_, unsigned char* ws, int colcnt, rsrc_t rs, v4u* lds, int sg0, int sgstep, int dq, u16* out, unsigned long long* dbg) {
  const int lane = lane_id(), wv = wv_, g = lane >> 4, r = lane & 15;
  const int kt = dq * 4 + wv;
  v4u a[2][NFK];
#pragma unroll
  for (int fk = 0; fk < NFK; ++fk) {
    a[0][fk] = ldt<FINAL>(rs, (int)OFF_INV + ((2 * kt) * NFK + fk) * 1024 + lane * 16);
    a[1][fk] = ldt<FINAL>(rs, (int)OFF_INV + ((2 * kt + 1) * NFK + fk) * 1024 + lane * 16);
  }
#define ISTAMP(i)
  ISTAMP(0)
  if (!FINAL) fan_wait<64, 20>(wv_, ws, fan_off(0, 1, 0), dq);
  fan_wait<FSP, NCOL>(wv_, ws, colcnt, sg0 + dq);
  ISTAMP(1)
#pragma unroll 1
  for (int sg = sg0; sg < NSEG; sg += sgstep) {
    const int xo = (int)OFF_XR + ((sg * 2 * KT + kt) * 64 + lane) * 16;
    {
      const int jj = lane >> 3, rp = lane & 7, row = sg * 32 + (wv & 1) * 16 + 2 * rp;
      const int src = (int)OFF_ZT + (((wv >> 1) * 80 + jj) * S_ + row) * 8;
      v4u t[10];
#pragma unroll
      for (int i = 0; i < 10; ++i) t[i] = ldc(rs, src + i * 8 * S_ * 8);
      const v4u ny = ldc(rs, (int)OFF_ZT + (160 * S_ + row) * 8);
      if (wv < 2 && jj == 0) {
        t[0].x = (t[0].x & 0xFFFFu) | (ny.x << 16);
        t[0].z = (t[0].z & 0xFFFFu) | (ny.z << 16);
      }
      v2u* dst = (v2u*)(lds + ((wv & 1) * NFK + (wv >> 1) * 10) * 64 + (jj >> 1) * 16 + 2 * rp) + (jj & 1);
#pragma unroll
      for (int i = 0; i < 10; ++i) { dst[i * 128] = v2u{t[i].x, t[i].y}; dst[i * 128 + 2] = v2u{t[i].z, t[i].w}; }
    }
    const v4u x0 = ldc(rs, xo), x1 = ldc(rs, xo + KT * 1024);
    f32x4 acc[2][2] = {{{0, 0, 0, 0}, {0, 0, 0, 0}}, {{0, 0, 0, 0}, {0, 0, 0, 0}}};
    __syncthreads();
    ISTAMP(2)
#pragma unroll
    for (int k0 = 0; k0 < NFK; k0 += 5) {
      v4u b0[5], b1[5];
#pragma unroll
      for (int d = 0; d < 5; ++d) { b0[d] = lds[(k0 + d) * 64 + lane]; b1[d] = lds[(NFK + k0 + d) * 64 + lane]; }
#pragma unroll
      for (int d = 0; d < 5; ++d) {
        acc[0][0] = mfma(a[0][k0 + d], b0[d], acc[0][0]); acc[0][1] = mfma(a[0][k0 + d], b1[d], acc[0][1]);
        acc[1][0] = mfma(a[1][k0 + d], b0[d], acc[1][0]); acc[1][1] = mfma(a[1][k0 + d], b1[d], acc[1][1]);
      }
    }
#pragma unroll
    for (int rt = 0; rt < 2; ++rt) {
      const v4u x = rt ? x1 : x0;
      v4u y;
      y.x = pk(lo_f(x.x) + bfr(acc[0][rt].x), hi_f(x.x) + bfr(acc[0][rt].y));
      y.y = pk(lo_f(x.y) + bfr(acc[0][rt].z), hi_f(x.y) + bfr(acc[0][rt].w));
      y.z = pk(lo_f(x.z) + bfr(acc[1][rt].x), hi_f(x.z) + bfr(acc[1][rt].y));
      y.w = pk(lo_f(x.w) + bfr(acc[1][rt].z), hi_f(x.w) + bfr(acc[1][rt].w));
      if (FINAL) {
        u16* op = out + (size_t)(sg * 32 + rt * 16 + r) * D_ + 32 * kt + 4 * g;
        *(v2u*)op = v2u{y.x, y.y};
        *(v2u*)(op + 16) = v2u{y.z, y.w};
      } else {
        stc(rs, xo + rt * KT * 1024, y);
        stat_put(lds, wv, rt, lane, y);
      }
    }
    ISTAMP(3)
    if (!FINAL) {
      stat_store(rs, lds, wv, lane, sg, dq);
      if (sg < 12) fan_signal<64>(wv_, ws, fan_off(1, 1, 0), sg * 5 + dq);
      else dep_signal(wv_, ws, cnt_off(1, CNT_IDFT, sg >> 1));
    }
  }
}

constexpr int JOB_LDS = 4 * NFK * 1024;
extern __shared__ __attribute__((aligned(16))) unsigned char dyn_lds[];
constexpr int FIN_LDS = 2 * NFK * 1024;
static_assert(FIN_LDS >= COL_LDS, "LDS");
__device__ __forceinline__ unsigned lds_base() { return (unsigned)(uintptr_t)(__attribute__((address_space(3))) unsigned char*)dyn_lds; }
#define FIN_DELAY 1
__global__ void __launch_bounds__(256, 2) k_fin(unsigned char* ws, u16* out) {
  const int wv_ = __builtin_amdgcn_readfirstlane(threadIdx.x >> 6), b = blockIdx.x;
  const rsrc_t rs = ws_rsrc(ws);
  PH(14)
  if (b < NCOL) {
    job_col(wv_, ws, rs, lds_base(), b, -1);
    fan_signal<FSP>(wv_, ws, fan_off(0, 0, 0), b);
    PH(13)
  } else {
    for (int i = 0; i < FIN_DELAY; ++i) __builtin_amdgcn_s_sleep(32);
    job_idft<true>(wv_, ws, fan_off(0, 0, 0), rs, (v4u*)dyn_lds, (b - NCOL) / 5, NSEG, (b - NCOL) % 5, out, nullptr);
    PH(15)
  }
}

constexpr unsigned SPIN_CAP = 1u << 16;

#define VM_LD128(dst, off, so, IMM) asm volatile("buffer_load_dwordx4 %0, %1, %2, %3 offen offset:" #IMM " sc1" : "=v"(dst) : "v"(off), "s"(rs), "s"(so) : "memory")
#define VM_LD32(dst, off, so) asm volatile("buffer_load_dword %0, %1, %2, %3 offen sc1" : "=v"(dst) : "v"(off), "s"(rs), "s"(so) : "memory")
#define VM_ST32(val, off, so) asm volatile("buffer_store_dword %0, %1, %2, %3 offen sc1" ::"v"(val), "v"(off), "s"(rs), "s"(so) : "memory")
#define VM_ST128(val, off, so) asm volatile("buffer_store_dwordx4 %0, %1, %2, %3 offen sc1\n\ts_nop 1" ::"v"(val), "v"(off), "s"(rs), "s"(so) : "memory")
#define VM_WAIT(N) asm volatile("s_waitcnt vmcnt(" #N ")" ::: "memory")
#define VM_WAIT1(N, a) asm volatile("s_waitcnt vmcnt(" #N ")" : "+v"(a)::"memory")
__device__ __forceinline__ v4u make_rs(const void* p, unsigned bytes) {
  const unsigned long long a = (unsigned long long)p;
  v4u r;
  r.x = (unsigned)__builtin_amdgcn_readfirstlane((unsigned)a);
  r.y = (unsigned)__builtin_amdgcn_readfirstlane((unsigned)(a >> 32) & 0xFFFFu);
  r.z = bytes;
  r.w = 0x00020000u;
  return r;
}
__device__ __forceinline__ unsigned rtc() { return (unsigned)__builtin_amdgcn_s_memrealtime(); }
struct Clk0 { unsigned r, c; };
__device__ __forceinline__ Clk0 clk_sample() {
  unsigned long long r, c;
  asm volatile("s_memrealtime %0\n\ts_memtime %1\n\ts_waitcnt lgkmcnt(0)" : "=s"(r), "=s"(c)::"memory");
  return Clk0{(unsigned)r, (unsigned)c};
}
__device__ __forceinline__ v4u clk_init(Clk0 a) {
  const Clk0 b = clk_sample();
  unsigned k = 1400u;
  if (b.r - a.r >= 100u) k = (unsigned)((float)(b.r - a.r) * 32768.f / (float)(b.c - a.c));
  k = (unsigned)__builtin_amdgcn_readfirstlane((int)min(max(k, 1200u), 3000u));
  asm volatile("" : "+s"(k));
  return v4u{b.r * 8u + 16u, b.c, k, 0u};
}
#define LDSBAR() asm volatile("s_waitcnt lgkmcnt(0)\n\ts_barrier" ::: "memory")
#define MEMPIN() asm volatile("" ::: "memory")

__device__ __forceinline__ int wave_imax(int v) {
  asm volatile("s_nop 1\n\tv_max_i32_dpp %0, %0, %0 quad_perm:[1,0,3,2] row_mask:0xf bank_mask:0xf\n\t"
               "s_nop 1\n\tv_max_i32_dpp %0, %0, %0 quad_perm:[2,3,0,1] row_mask:0xf bank_mask:0xf\n\t"
               "s_nop 1\n\tv_max_i32_dpp %0, %0, %0 row_half_mirror row_mask:0xf bank_mask:0xf\n\t"
               "s_nop 1\n\tv_max_i32_dpp %0, %0, %0 row_mirror row_mask:0xf bank_mask:0xf\n\ts_nop 0"
               : "+v"(v));
  const int a = __builtin_amdgcn_readlane(v, 0), b = __builtin_amdgcn_readlane(v, 16), c = __builtin_amdgcn_readlane(v, 32),
            d = __builtin_amdgcn_readlane(v, 48);
  return max(max(a, b), max(c, d));
}

constexpr int OOB = 0x7FFFFFF0;
__device__ __forceinline__ int ladder(int x, int i) { return copy_off(((x + (i >> 1)) & 3) | ((i & 1) << 2)); }

struct RecTune { int m1, m2, dl1, rob, c2, vis, dlt, du; };

#define STAT(r)
__device__ __forceinline__ unsigned max4(v4u v) { return max(max(v.x, v.y), max(v.z, v.w)); }
template <int NAP = 6>
__device__ __forceinline__ unsigned flag_finish(const v4u rs, int off, unsigned cv) {
  for (unsigned spins = 0; __any(cv == 0xFFFFFFFFu) && spins < SPIN_CAP; ++spins) {
    __builtin_amdgcn_s_sleep(NAP);
    v4u t;
    VM_LD128(t, off, 0, 0);
    VM_WAIT1(0, t);
    cv = max4(t);
  }
  return cv;
}
struct Pool {
  unsigned long long sel, s3, ysel; int base;
  __device__ __forceinline__ int near(int s) const { return (int)((s3 >> s) & 1ull) * 16384; }
  __device__ __forceinline__ int region(int s) const { return base + s * REG + (int)((sel >> s) & 1ull) * 8192; }
};
constexpr int L_QT = 10240, L_KT = 12288, L_CL = 14336, L_KQ = 16384;
constexpr int L_T = 14336;
constexpr int L_M2 = L_KQ + 65536;
constexpr int REC_ST = L_M2 + 12288;
constexpr int REC_LDS = REC_ST + 4096 + 64;

#define RSTAMP(i)
#define YSTAMP(n)
#define RST_CLOB
#define P1TRACE
#define PVTRACE
#define FINTRACE
#define P1_ASM \
   "s_mov_b32 s64, -1\n\t" "s_mov_b32 s76, 0\n\t" "s_mov_b32 s77, 0\n\t" "s_mov_b32 s71, 63\n\t" "s_mov_b32 s47, 1300\n\t" "s_mov_b32 s49, 0\n\t" \
   "s_mov_b32 s51, 0\n\t" "s_mov_b32 s52, 0\n\t" "s_mov_b32 s70, 0\n\t" "s_mov_b32 s69, 0\n\t" "v_cmp_gt_i32 s[20:21], 0, v126\n\t" \
   "v_cmp_gt_i32 s[22:23], 1, v126\n\t" "v_cmp_gt_i32 s[24:25], 2, v126\n\t" "v_cmp_gt_i32 s[26:27], 3, v126\n\t" "v_cmp_gt_i32 s[28:29], 16, v126\n\t" \
   "v_cmp_gt_i32 s[30:31], 17, v126\n\t" "v_cmp_gt_i32 s[32:33], 18, v126\n\t" "v_cmp_gt_i32 s[34:35], 19, v126\n\t" "s_waitcnt vmcnt(0)\n\t" \
   "s_lshr_b64 s[72:73], %[sel], s71\n\t" "s_lshl_b32 s65, s71, 19\n\t" "s_and_b32 s72, s72, 1\n\t" "s_add_u32 s65, s65, %[pool]\n\t" \
   "s_lshl_b32 s72, s72, 13\n\t" "s_add_u32 s65, s65, s72\n\t" "s_lshr_b64 s[72:73], %[s3], s71\n\t" "s_and_b32 s54, s72, 1\n\t" \
   "s_lshl_b32 s54, s54, 14\n\t" "s_memtime s[62:63]\n\t" "s_memrealtime s[60:61]\n\t" "40:\n\t" "s_cmp_eq_u32 s64, 0\n\t" "s_cbranch_scc1 95f\n\t" \
   "96:\n\t" "s_add_i32 s72, s64, 2\n\t" "s_min_i32 s72, s72, 63\n\t" "s_lshl_b32 s59, s72, 12\n\t" "s_lshl_b32 s79, s72, 13\n\t" \
   "buffer_load_dword v108, v139, %[rs], s65 offen sc1\n\t" "s_cmp_gt_u32 %[wv], 1\n\t" "s_waitcnt vmcnt(4)\n\t" "s_cbranch_scc1 44f\n\t" \
   "buffer_load_dword v15, v140, %[rs], s59 offen sc1\n\t" "ds_write_b128 v127, v[16:19] offset:0\n\t" "ds_write_b128 v127, v[20:23] offset:1024\n\t" \
   "ds_write_b128 v127, v[24:27] offset:2048\n\t" "ds_write_b128 v127, v[28:31] offset:3072\n\t" "s_waitcnt lgkmcnt(0)\n\t" \
   "s_sub_u32 s74, s62, s57\n\t" "s_mul_i32 s74, s74, s58\n\t" "s_lshr_b32 s74, s74, 12\n\t" "s_add_u32 s74, s74, s56\n\t" "s_lshl_b32 s60, s60, 3\n\t" \
   "s_sub_u32 s75, s74, s60\n\t" "s_cmp_lt_u32 s75, 32\n\t" "s_cbranch_scc1 38f\n\t" "s_cmp_lt_i32 s75, 0\n\t" "s_cselect_b32 s61, 0, 31\n\t" \
   "s_add_u32 s74, s60, s61\n\t" "s_sub_i32 s75, s61, s75\n\t" "s_add_i32 s75, s75, 1\n\t" "s_ashr_i32 s75, s75, 1\n\t" "s_min_i32 s75, s75, 8\n\t" \
   "s_max_i32 s75, s75, -8\n\t" "s_mov_b32 s56, s74\n\t" "s_mov_b32 s57, s62\n\t" "s_add_u32 s58, s58, s75\n\t" "38:\n\t" "s_min_u32 s72, s70, 1\n\t" \
   "s_and_b32 s71, s74, -4\n\t" "s_or_b32 s71, s71, s72\n\t" "v_mov_b32 v111, s71\n\t" "buffer_store_dword v111, v131, %[rs], s65 offen sc1\n\t" \
   P1TRACE "s_barrier\n\t" "ds_read_b128 v[160:163], v122 offset:0\n\t" "ds_read_b128 v[164:167], v122 offset:1024\n\t" \
   "ds_read_b128 v[168:171], v122 offset:2048\n\t" "ds_read_b128 v[172:175], v122 offset:3072\n\t" "ds_read_b128 v[176:179], v122 offset:4096\n\t" \
   "ds_read_b128 v[180:183], v122 offset:5120\n\t" "ds_read_b128 v[184:187], v122 offset:6144\n\t" "ds_read_b128 v[188:191], v122 offset:7168\n\t" \
   "s_cmp_lt_i32 s64, 1\n\t" "s_cselect_b32 s72, 0x7ffffff0, 0\n\t" "s_max_i32 s71, s64, 1\n\t" "v_or_b32 v109, s72, v143\n\t" \
   "s_lshl_b32 s71, s71, 12\n\t" "v_mov_b32 v110, 1\n\t" "s_sub_u32 s71, s71, 4096\n\t" "buffer_store_dword v110, v109, %[rs], s71 offen sc1\n\t" \
   "s_add_u32 s48, s69, s47\n\t" "s_cmp_eq_u32 s70, 0\n\t" "s_cselect_b32 s48, s48, -1\n\t" "s_mov_b32 s66, s65\n\t" "s_add_i32 s64, s64, 1\n\t" \
   "s_and_b32 s71, s64, 63\n\t" "s_lshr_b64 s[72:73], %[sel], s71\n\t" "s_lshl_b32 s65, s71, 19\n\t" "s_and_b32 s72, s72, 1\n\t" \
   "s_add_u32 s65, s65, %[pool]\n\t" "s_lshl_b32 s72, s72, 13\n\t" "s_add_u32 s65, s65, s72\n\t" "s_xor_b32 s67, s54, %[lad0]\n\t" \
   "s_xor_b32 s68, s67, 0x4000\n\t" "s_add_u32 s67, s67, s66\n\t" "s_add_u32 s68, s68, s66\n\t" "s_lshr_b64 s[72:73], %[s3], s71\n\t" \
   "s_and_b32 s54, s72, 1\n\t" "s_lshl_b32 s54, s54, 14\n\t" "s_cmp_eq_u32 s64, 0\n\t" "s_cselect_b32 s70, 1, 0\n\t" "s_and_b32 s55, %[wv], 1\n\t" \
   "s_lshl_b32 s55, s55, 14\n\t" "s_xor_b32 s55, s55, s54\n\t" "s_xor_b32 s50, s55, 0x4000\n\t" "s_add_u32 s55, s55, s65\n\t" \
   "s_add_u32 s50, s50, s65\n\t" "v_mov_b32 v48, 0\n\t" "v_mov_b32 v49, 0\n\t" "v_mov_b32 v50, 0\n\t" "v_mov_b32 v51, 0\n\t" "v_mov_b32 v52, 0\n\t" \
   "v_mov_b32 v53, 0\n\t" "v_mov_b32 v54, 0\n\t" "v_mov_b32 v55, 0\n\t" "v_mov_b32 v56, 0\n\t" "v_mov_b32 v57, 0\n\t" "v_mov_b32 v58, 0\n\t" \
   "v_mov_b32 v59, 0\n\t" "v_mov_b32 v60, 0\n\t" "v_mov_b32 v61, 0\n\t" "v_mov_b32 v62, 0\n\t" "v_mov_b32 v63, 0\n\t" "s_waitcnt vmcnt(2)\n\t" \
   "v_cmp_eq_u32 vcc, -1, v15\n\t" "s_nop 0\n\t" "s_cbranch_vccnz 141f\n\t" "41:\n\t" "s_branch 48f\n\t" "44:\n\t" \
   "v_mfma_f32_16x16x32_bf16 v[32:35], v[160:163], v[16:19], 0\n\t" "s_max_i32 s51, s64, 0\n\t" "s_lshl_b32 s52, s51, 16\n\t" \
   "s_lshl_b32 s51, s51, 14\n\t" "v_mfma_f32_16x16x32_bf16 v[36:39], v[176:179], v[16:19], 0\n\t" "s_cmp_lt_i32 s64, 0\n\t" \
   "s_cselect_b32 s49, 0x7ffffff0, 0\n\t" "s_add_i32 s72, s64, 1\n\t" "v_mfma_f32_16x16x32_bf16 v[32:35], v[164:167], v[20:23], v[32:35]\n\t" \
   "s_min_i32 s72, s72, 63\n\t" "s_lshl_b32 s79, s72, 13\n\t" "v_mfma_f32_16x16x32_bf16 v[36:39], v[180:183], v[20:23], v[36:39]\n\t" \
   "v_mfma_f32_16x16x32_bf16 v[32:35], v[168:171], v[24:27], v[32:35]\n\t" "s_waitcnt lgkmcnt(0)\n\t" \
   "v_mfma_f32_16x16x32_bf16 v[36:39], v[184:187], v[24:27], v[36:39]\n\t" "s_sub_u32 s74, s62, s57\n\t" "s_mul_i32 s74, s74, s58\n\t" \
   "s_lshr_b32 s74, s74, 12\n\t" "v_mfma_f32_16x16x32_bf16 v[32:35], v[172:175], v[28:31], v[32:35]\n\t" "s_add_u32 s74, s74, s56\n\t" \
   "s_lshl_b32 s60, s60, 3\n\t" "s_sub_u32 s75, s74, s60\n\t" "v_mfma_f32_16x16x32_bf16 v[36:39], v[188:191], v[28:31], v[36:39]\n\t" \
   "s_cmp_lt_u32 s75, 32\n\t" "s_cbranch_scc1 39f\n\t" "s_cmp_lt_i32 s75, 0\n\t" "s_cselect_b32 s61, 0, 31\n\t" "s_add_u32 s74, s60, s61\n\t" \
   "s_sub_i32 s75, s61, s75\n\t" "s_add_i32 s75, s75, 1\n\t" "s_ashr_i32 s75, s75, 1\n\t" "s_min_i32 s75, s75, 8\n\t" "s_max_i32 s75, s75, -8\n\t" \
   "s_mov_b32 s56, s74\n\t" "s_mov_b32 s57, s62\n\t" "s_add_u32 s58, s58, s75\n\t" "39:\n\t" "s_barrier\n\t" \
   "v_mfma_f32_16x16x32_bf16 v[40:43], v[144:147], v[16:19], 0\n\t" "s_min_u32 s72, s70, 1\n\t" "s_and_b32 s71, s74, -4\n\t" \
   "s_or_b32 s71, s71, s72\n\t" "v_mov_b32 v111, s71\n\t" "v_mfma_f32_16x16x32_bf16 v[40:43], v[148:151], v[20:23], v[40:43]\n\t" \
   "buffer_store_dword v111, v131, %[rs], s65 offen sc1\n\t" P1TRACE "v_mov_b32 v48, 0\n\t" "v_mov_b32 v49, 0\n\t" \
   "v_mfma_f32_16x16x32_bf16 v[40:43], v[152:155], v[24:27], v[40:43]\n\t" "v_mov_b32 v50, 0\n\t" "v_mov_b32 v51, 0\n\t" "v_mov_b32 v52, 0\n\t" \
   "v_mov_b32 v53, 0\n\t" "v_mfma_f32_16x16x32_bf16 v[40:43], v[156:159], v[28:31], v[40:43]\n\t" "v_mov_b32 v54, 0\n\t" "v_mov_b32 v55, 0\n\t" \
   "v_cndmask_b32 v32, v32, v125, s[20:21]\n\t" "v_cndmask_b32 v33, v33, v125, s[22:23]\n\t" "v_cndmask_b32 v34, v34, v125, s[24:25]\n\t" \
   "v_cndmask_b32 v35, v35, v125, s[26:27]\n\t" "v_cndmask_b32 v36, v36, v125, s[28:29]\n\t" "v_cndmask_b32 v37, v37, v125, s[30:31]\n\t" \
   "v_cndmask_b32 v38, v38, v125, s[32:33]\n\t" "v_cndmask_b32 v39, v39, v125, s[34:35]\n\t" "v_exp_f32 v64, v32\n\t" "s_add_u32 s48, s69, s47\n\t" \
   "s_cmp_eq_u32 s70, 0\n\t" "s_cselect_b32 s48, s48, -1\n\t" "v_exp_f32 v65, v33\n\t" "s_mov_b32 s66, s65\n\t" "s_add_i32 s64, s64, 1\n\t" \
   "s_and_b32 s71, s64, 63\n\t" "v_exp_f32 v66, v34\n\t" "s_lshr_b64 s[72:73], %[sel], s71\n\t" "s_lshl_b32 s65, s71, 19\n\t" \
   "s_and_b32 s72, s72, 1\n\t" "v_exp_f32 v67, v35\n\t" "s_add_u32 s65, s65, %[pool]\n\t" "s_lshl_b32 s72, s72, 13\n\t" "s_add_u32 s65, s65, s72\n\t" \
   "v_exp_f32 v68, v36\n\t" "s_xor_b32 s67, s54, %[lad0]\n\t" "s_xor_b32 s68, s67, 0x4000\n\t" "s_add_u32 s67, s67, s66\n\t" "v_exp_f32 v69, v37\n\t" \
   "s_add_u32 s68, s68, s66\n\t" "s_lshr_b64 s[72:73], %[s3], s71\n\t" "s_and_b32 s54, s72, 1\n\t" "v_exp_f32 v70, v38\n\t" \
   "s_lshl_b32 s54, s54, 14\n\t" "s_cmp_eq_u32 s64, 0\n\t" "s_cselect_b32 s70, 1, 0\n\t" "v_exp_f32 v71, v39\n\t" "s_and_b32 s55, %[wv], 1\n\t" \
   "s_lshl_b32 s55, s55, 14\n\t" "s_xor_b32 s55, s55, s54\n\t" "v_exp_f32 v72, v40\n\t" "s_xor_b32 s50, s55, 0x4000\n\t" "s_add_u32 s55, s55, s65\n\t" \
   "s_add_u32 s50, s50, s65\n\t" "v_exp_f32 v73, v41\n\t" "v_mov_b32 v56, 0\n\t" "v_mov_b32 v57, 0\n\t" "v_mov_b32 v58, 0\n\t" "v_exp_f32 v74, v42\n\t" \
   "v_mov_b32 v59, 0\n\t" "v_mov_b32 v60, 0\n\t" "v_mov_b32 v61, 0\n\t" "v_exp_f32 v75, v43\n\t" "v_mov_b32 v62, 0\n\t" "v_mov_b32 v63, 0\n\t" \
   "v_add_f32 v80, v64, v65\n\t" "v_add_f32 v81, v66, v67\n\t" "v_add_f32 v82, v68, v69\n\t" "v_add_f32 v83, v70, v71\n\t" \
   "v_add_f32 v84, v72, v73\n\t" "v_add_f32 v85, v74, v75\n\t" "v_add_f32 v80, v80, v81\n\t" "v_add_f32 v82, v82, v83\n\t" \
   "v_add_f32 v84, v84, v85\n\t" "v_add_f32 v80, v80, v82\n\t" "v_fma_f32 v82, v80, 2.0, v84\n\t" "v_fma_f32 v80, v80, 2.0, v84\n\t" \
   "ds_read_b128 v[160:163], v122 offset:0\n\t" "ds_read_b128 v[164:167], v122 offset:1024\n\t" "v_permlane16_swap_b32 v80, v82\n\t" \
   "v_add_f32 v81, v80, v82\n\t" "v_add_f32 v80, v80, v82\n\t" "ds_read_b128 v[168:171], v122 offset:2048\n\t" \
   "ds_read_b128 v[172:175], v122 offset:3072\n\t" "v_permlane32_swap_b32 v80, v81\n\t" "v_add_f32 v80, v80, v81\n\t" "v_rcp_f32 v90, v80\n\t" \
   "v_add_u32 v81, 0xe0800000, v80\n\t" "v_add_f32 v90, v90, v90\n\t" "v_cmp_lt_u32 vcc, 0x40000000, v81\n\t" \
   "ds_read_b128 v[176:179], v122 offset:4096\n\t" "ds_read_b128 v[180:183], v122 offset:5120\n\t" "ds_read_b128 v[184:187], v122 offset:6144\n\t" \
   "ds_read_b128 v[188:191], v122 offset:7168\n\t" "s_cbranch_vccnz 74f\n\t" "75:\n\t" "v_pk_mul_f32 v[64:65], v[64:65], v[90:91] op_sel_hi:[1,0]\n\t" \
   "v_pk_mul_f32 v[66:67], v[66:67], v[90:91] op_sel_hi:[1,0]\n\t" "v_pk_mul_f32 v[68:69], v[68:69], v[90:91] op_sel_hi:[1,0]\n\t" \
   "v_pk_mul_f32 v[70:71], v[70:71], v[90:91] op_sel_hi:[1,0]\n\t" "v_pk_mul_f32 v[72:73], v[72:73], v[90:91] op_sel_hi:[1,0]\n\t" \
   "v_pk_mul_f32 v[74:75], v[74:75], v[90:91] op_sel_hi:[1,0]\n\t" "v_cvt_pk_bf16_f32 v44, v64, v65\n\t" "v_cvt_pk_bf16_f32 v46, v66, v67\n\t" \
   "v_cvt_pk_bf16_f32 v45, v68, v69\n\t" "v_cvt_pk_bf16_f32 v47, v70, v71\n\t" "v_cvt_pk_bf16_f32 v32, v72, v73\n\t" \
   "v_cvt_pk_bf16_f32 v33, v74, v75\n\t" "s_waitcnt vmcnt(1)\n\t" "48:\n\t" "v_readlane_b32 s69, v108, 0\n\t" "v_readlane_b32 s71, v108, 1\n\t" \
   "s_cmp_eq_u32 s64, 64\n\t" "s_cbranch_scc1 90f\n\t" "s_cmp_eq_u32 s69, -1\n\t" "s_cbranch_scc1 63f\n\t" "s_cmp_eq_u32 s71, -1\n\t" \
   "s_cselect_b32 s47, s47, s71\n\t" "50:\n\t" "s_add_u32 s73, s69, %[m1]\n\t" RSTAMP(5) "51:\n\t" "s_memrealtime s[60:61]\n\t" \
   "s_memtime s[62:63]\n\t" "s_waitcnt lgkmcnt(0)\n\t" "s_sub_u32 s74, s62, s57\n\t" "s_mul_i32 s74, s74, s58\n\t" "s_lshr_b32 s74, s74, 12\n\t" \
   "s_add_u32 s74, s74, s56\n\t" "s_lshl_b32 s60, s60, 3\n\t" "s_sub_u32 s75, s74, s60\n\t" "s_cmp_lt_u32 s75, 32\n\t" "s_cbranch_scc1 52f\n\t" \
   "s_cmp_lt_i32 s75, 0\n\t" "s_cselect_b32 s61, 0, 31\n\t" "s_add_u32 s74, s60, s61\n\t" "s_sub_i32 s75, s61, s75\n\t" "s_add_i32 s75, s75, 1\n\t" \
   "s_ashr_i32 s75, s75, 1\n\t" "s_min_i32 s75, s75, 8\n\t" "s_max_i32 s75, s75, -8\n\t" "s_mov_b32 s56, s74\n\t" "s_mov_b32 s57, s62\n\t" \
   "s_add_u32 s58, s58, s75\n\t" "52:\n\t" "s_sub_i32 s74, s73, s74\n\t" "s_cmp_le_i32 s74, 150\n\t" "s_cbranch_scc1 53f\n\t" \
   "s_cmp_lt_i32 s74, 260\n\t" "s_cbranch_scc1 51b\n\t" "s_sleep 2\n\t" "s_cmp_lt_i32 s74, 32000\n\t" "s_cbranch_scc1 51b\n\t" "s_mov_b32 s74, 0\n\t" \
   "53:\n\t" "s_sub_i32 s74, s74, 56\n\t" "s_max_i32 s74, s74, 0\n\t" "s_mul_i32 s74, s74, 23\n\t" "s_lshr_b32 s74, s74, 5\n\t" \
   "s_and_b32 s74, s74, -4\n\t" "s_getpc_b64 s[60:61]\n\t" "54:\n\t" "s_sub_u32 s74, 55f-54b, s74\n\t" "s_add_u32 s60, s60, s74\n\t" \
   "s_addc_u32 s61, s61, 0\n\t" "s_setpc_b64 s[60:61]\n\t" "s_nop 3\n\t" "s_nop 3\n\t" "s_nop 3\n\t" "s_nop 3\n\t" "s_nop 3\n\t" "s_nop 3\n\t" \
   "s_nop 3\n\t" "s_nop 3\n\t" "s_nop 3\n\t" "s_nop 3\n\t" "s_nop 3\n\t" "s_nop 3\n\t" "s_nop 3\n\t" "s_nop 3\n\t" "s_nop 3\n\t" "s_nop 3\n\t" \
   "s_nop 3\n\t" "s_nop 3\n\t" "s_nop 3\n\t" "s_nop 3\n\t" "s_nop 3\n\t" "s_nop 3\n\t" "s_nop 3\n\t" "s_nop 3\n\t" "55:\n\t" "20:\n\t" RSTAMP(4) \
   "buffer_load_dwordx4 v[64:67], v128, %[rs], s67 offen sc1\n\t" "buffer_load_dwordx4 v[68:71], v129, %[rs], s67 offen sc1\n\t" \
   "buffer_load_dwordx4 v[72:75], v130, %[rs], s67 offen sc1\n\t" "s_nop 7\n\t" "s_nop 7\n\t" "s_nop 7\n\t" "s_cmp_lg_u32 s70, 0\n\t" \
   "s_cbranch_scc1 27f\n\t" "buffer_load_dwordx4 v[16:19], v138, %[rs], s79 offen offset:0 sc1\n\t" \
   "buffer_load_dwordx4 v[20:23], v138, %[rs], s79 offen offset:1024 sc1\n\t" \
   "buffer_load_dwordx4 v[24:27], v138, %[rs], s79 offen offset:2048 sc1\n\t" \
   "buffer_load_dwordx4 v[28:31], v138, %[rs], s79 offen offset:3072 sc1\n\t" "v_or_b32 v108, s49, v141\n\t" "v_or_b32 v109, s49, v142\n\t" \
   "buffer_store_dwordx4 v[44:47], v108, %[rs], s52 offen sc1\n\t" "buffer_store_dwordx2 v[32:33], v109, %[rs], s51 offen sc1\n\t" "28:\n\t" RSTAMP(3) \
   "s_waitcnt vmcnt(8)\n\t" "ds_write_b128 v112, v[64:67]\n\t" "v_smfmac_f32_16x16x64_bf16 v[48:51], v[64:67], a[0:7], v134\n\t" \
   "s_waitcnt vmcnt(7)\n\t" "ds_write_b128 v113, v[68:71]\n\t" "s_waitcnt vmcnt(6)\n\t" "ds_write_b128 v114, v[72:75]\n\t" RSTAMP(7) "2:\n\t" \
   "v_smfmac_f32_16x16x64_bf16 v[52:55], v[64:67], a[64:71], v134\n\t" "v_max3_u32 v110, v64, v66, v68\n\t" "v_max3_u32 v111, v70, v72, v74\n\t" \
   "v_smfmac_f32_16x16x64_bf16 v[48:51], v[68:71], a[8:15], v134\n\t" "v_max_u32 v110, v110, v111\n\t" "v_cmp_eq_u32 vcc, -1, v110\n\t" \
   "v_smfmac_f32_16x16x64_bf16 v[52:55], v[68:71], a[72:79], v134\n\t" "s_cbranch_vccnz 8f\n\t" \
   "v_smfmac_f32_16x16x64_bf16 v[48:51], v[72:75], a[16:23], v134\n\t" "s_waitcnt lgkmcnt(0)\n\t" "s_barrier\n\t" RSTAMP(0) \
   "ds_read_b128 v[76:79], v115\n\t" "ds_read_b128 v[80:83], v116\n\t" "v_smfmac_f32_16x16x64_bf16 v[52:55], v[72:75], a[80:87], v134\n\t" \
   "ds_read_b128 v[84:87], v117\n\t" "ds_read_b128 v[88:91], v118\n\t" "v_smfmac_f32_16x16x64_bf16 v[56:59], v[64:67], a[128:135], v134\n\t" \
   "ds_read_b128 v[92:95], v119\n\t" "ds_read_b128 v[96:99], v120\n\t" "v_smfmac_f32_16x16x64_bf16 v[60:63], v[64:67], a[192:199], v134\n\t" \
   "ds_read_b128 v[100:103], v121\n\t" "v_smfmac_f32_16x16x64_bf16 v[56:59], v[68:71], a[136:143], v134\n\t" \
   "v_smfmac_f32_16x16x64_bf16 v[60:63], v[68:71], a[200:207], v134\n\t" "s_waitcnt lgkmcnt(6)\n\t" \
   "v_smfmac_f32_16x16x64_bf16 v[48:51], v[76:79], a[24:31], v134\n\t" "v_smfmac_f32_16x16x64_bf16 v[52:55], v[76:79], a[88:95], v134\n\t" \
   "s_waitcnt lgkmcnt(5)\n\t" "v_smfmac_f32_16x16x64_bf16 v[48:51], v[80:83], a[32:39], v134\n\t" \
   "v_smfmac_f32_16x16x64_bf16 v[52:55], v[80:83], a[96:103], v134\n\t" "s_waitcnt lgkmcnt(4)\n\t" \
   "v_smfmac_f32_16x16x64_bf16 v[48:51], v[84:87], a[40:47], v134\n\t" "v_smfmac_f32_16x16x64_bf16 v[52:55], v[84:87], a[104:111], v134\n\t" \
   "s_waitcnt lgkmcnt(3)\n\t" "v_smfmac_f32_16x16x64_bf16 v[48:51], v[88:91], a[48:55], v134\n\t" \
   "v_smfmac_f32_16x16x64_bf16 v[52:55], v[88:91], a[112:119], v134\n\t" "s_waitcnt lgkmcnt(2)\n\t" \
   "v_smfmac_f32_16x16x64_bf16 v[48:51], v[92:95], a[56:63], v134\n\t" "v_smfmac_f32_16x16x64_bf16 v[52:55], v[92:95], a[120:127], v134\n\t" \
   "s_waitcnt lgkmcnt(0)\n\t" "v_smfmac_f32_16x16x64_bf16 v[48:51], v[96:99], v[192:199], v134\n\t" \
   "v_smfmac_f32_16x16x64_bf16 v[48:51], v[100:103], v[200:207], v134\n\t" "v_smfmac_f32_16x16x64_bf16 v[52:55], v[96:99], v[208:215], v134\n\t" \
   "v_smfmac_f32_16x16x64_bf16 v[52:55], v[100:103], v[216:223], v134\n\t" "v_smfmac_f32_16x16x64_bf16 v[56:59], v[72:75], a[144:151], v134\n\t" \
   "v_add_f32 v48, v48, v49\n\t" "v_add_f32 v49, v50, v51\n\t" "v_smfmac_f32_16x16x64_bf16 v[60:63], v[72:75], a[208:215], v134\n\t" \
   "v_mul_f32 v48, 0x3e0293ee, v48\n\t" "v_mul_f32 v49, 0x3e0293ee, v49\n\t" "v_smfmac_f32_16x16x64_bf16 v[56:59], v[76:79], a[152:159], v134\n\t" \
   "v_add_f32 v52, v52, v53\n\t" "v_add_f32 v53, v54, v55\n\t" "v_smfmac_f32_16x16x64_bf16 v[56:59], v[80:83], a[160:167], v134\n\t" \
   "v_mul_f32 v52, 0x3e0293ee, v52\n\t" "v_mul_f32 v53, 0x3e0293ee, v53\n\t" "v_smfmac_f32_16x16x64_bf16 v[56:59], v[84:87], a[168:175], v134\n\t" \
   "v_cvt_pk_bf16_f32 v48, v48, v49\n\t" "ds_write_b32 v135, v48 offset:10240\n\t" \
   "v_smfmac_f32_16x16x64_bf16 v[56:59], v[88:91], a[176:183], v134\n\t" "v_cvt_pk_bf16_f32 v52, v52, v53\n\t" \
   "ds_write_b32 v135, v52 offset:10496\n\t" "v_smfmac_f32_16x16x64_bf16 v[56:59], v[92:95], a[184:191], v134\n\t" \
   "v_smfmac_f32_16x16x64_bf16 v[56:59], v[96:99], v[224:231], v134\n\t" "v_smfmac_f32_16x16x64_bf16 v[56:59], v[100:103], v[232:239], v134\n\t" \
   "s_waitcnt lgkmcnt(0)\n\t" "s_barrier\n\t" "ds_read_b64_tr_b16 v[64:65], v136 offset:10240\n\t" "ds_read_b64_tr_b16 v[66:67], v136 offset:10496\n\t" \
   "v_smfmac_f32_16x16x64_bf16 v[60:63], v[76:79], a[216:223], v134\n\t" "ds_read_b64_tr_b16 v[68:69], v136 offset:10752\n\t" \
   "v_smfmac_f32_16x16x64_bf16 v[60:63], v[80:83], a[224:231], v134\n\t" "ds_read_b64_tr_b16 v[70:71], v136 offset:11008\n\t" \
   "v_smfmac_f32_16x16x64_bf16 v[60:63], v[84:87], a[232:239], v134\n\t" "ds_read_b64_tr_b16 v[72:73], v136 offset:11264\n\t" \
   "v_smfmac_f32_16x16x64_bf16 v[60:63], v[88:91], a[240:247], v134\n\t" "ds_read_b64_tr_b16 v[74:75], v136 offset:11520\n\t" \
   "v_add_f32 v56, v56, v57\n\t" "v_add_f32 v57, v58, v59\n\t" "v_smfmac_f32_16x16x64_bf16 v[60:63], v[92:95], a[248:255], v134\n\t" \
   "ds_read_b64_tr_b16 v[104:105], v136 offset:11776\n\t" "ds_read_b64_tr_b16 v[106:107], v136 offset:12032\n\t" \
   "v_smfmac_f32_16x16x64_bf16 v[60:63], v[96:99], v[240:247], v134\n\t" "v_cvt_pk_bf16_f32 v56, v56, v57\n\t" \
   "ds_write_b32 v135, v56 offset:12288\n\t" "v_smfmac_f32_16x16x64_bf16 v[60:63], v[100:103], v[248:255], v134\n\t" RSTAMP(6) \
   "s_waitcnt lgkmcnt(7)\n\t" "v_mfma_f32_16x16x32_bf16 v[48:51], v[160:163], v[64:67], 0\n\t" \
   "v_mfma_f32_16x16x32_bf16 v[52:55], v[176:179], v[64:67], 0\n\t" "s_waitcnt lgkmcnt(3)\n\t" "s_nop 0\n\t" "v_add_f32 v60, v60, v61\n\t" \
   "v_add_f32 v61, v62, v63\n\t" "v_cvt_pk_bf16_f32 v60, v60, v61\n\t" "ds_write_b32 v135, v60 offset:12544\n\t" \
   "v_mfma_f32_16x16x32_bf16 v[48:51], v[164:167], v[68:71], v[48:51]\n\t" "v_mfma_f32_16x16x32_bf16 v[52:55], v[180:183], v[68:71], v[52:55]\n\t" \
   "v_mfma_f32_16x16x32_bf16 v[48:51], v[168:171], v[72:75], v[48:51]\n\t" "v_mfma_f32_16x16x32_bf16 v[52:55], v[184:187], v[72:75], v[52:55]\n\t" \
   "s_waitcnt lgkmcnt(2)\n\t" "v_mfma_f32_16x16x32_bf16 v[48:51], v[172:175], v[104:107], v[48:51]\n\t" \
   "v_mfma_f32_16x16x32_bf16 v[52:55], v[188:191], v[104:107], v[52:55]\n\t" "s_waitcnt lgkmcnt(0)\n\t" "s_barrier\n\t" \
   "ds_read_b64_tr_b16 v[144:145], v136 offset:12288\n\t" "ds_read_b64_tr_b16 v[146:147], v136 offset:12544\n\t" \
   "ds_read_b64_tr_b16 v[148:149], v136 offset:12800\n\t" "ds_read_b64_tr_b16 v[150:151], v136 offset:13056\n\t" \
   "ds_read_b64_tr_b16 v[152:153], v136 offset:13312\n\t" "ds_read_b64_tr_b16 v[154:155], v136 offset:13568\n\t" \
   "ds_read_b64_tr_b16 v[156:157], v136 offset:13824\n\t" "ds_read_b64_tr_b16 v[158:159], v136 offset:14080\n\t" "s_cmp_eq_u32 s64, 0\n\t" \
   "s_cbranch_scc1 92f\n\t" "93:\n\t" RSTAMP(1) "v_cndmask_b32 v48, v48, v52, %[hi8]\n\t" "v_cndmask_b32 v49, v49, v53, %[hi8]\n\t" \
   "v_cndmask_b32 v50, v50, v54, %[hi8]\n\t" "v_cndmask_b32 v51, v51, v55, %[hi8]\n\t" "v_exp_f32 v80, v48\n\t" "v_exp_f32 v81, v49\n\t" \
   "s_waitcnt lgkmcnt(6)\n\t" "v_mfma_f32_16x16x32_bf16 v[76:79], v[144:147], v[64:67], 0\n\t" "v_exp_f32 v82, v50\n\t" "v_exp_f32 v83, v51\n\t" \
   "s_waitcnt lgkmcnt(4)\n\t" "v_mfma_f32_16x16x32_bf16 v[76:79], v[148:151], v[68:71], v[76:79]\n\t" "v_add_f32 v96, v80, v81\n\t" \
   "v_add_f32 v103, v82, v83\n\t" "s_waitcnt lgkmcnt(2)\n\t" "v_mfma_f32_16x16x32_bf16 v[76:79], v[152:155], v[72:75], v[76:79]\n\t" \
   "v_add_f32 v94, v96, v103\n\t" "v_add_f32 v96, v96, v103\n\t" "s_waitcnt lgkmcnt(0)\n\t" \
   "v_mfma_f32_16x16x32_bf16 v[76:79], v[156:159], v[104:107], v[76:79]\n\t" "v_permlane32_swap_b32 v96, v94\n\t" "v_add_f32 v96, v96, v94\n\t" \
   "s_nop 1\n\t" "v_add_f32_dpp v96, v96, v96 row_ror:8 row_mask:0xf bank_mask:0xf\n\t" "s_nop 2\n\t" "v_exp_f32 v88, v76\n\t" "v_exp_f32 v89, v77\n\t" \
   "v_add_f32 v103, v96, v88\n\t" "v_exp_f32 v90, v78\n\t" "v_add_f32 v103, v103, v89\n\t" "v_exp_f32 v91, v79\n\t" "v_add_f32 v103, v103, v90\n\t" \
   "v_add_f32 v97, v103, v91\n\t" "v_add_f32 v103, v103, v91\n\t" "s_nop 1\n\t" "v_permlane16_swap_b32 v103, v97\n\t" "v_add_f32 v103, v103, v97\n\t" \
   "v_rcp_f32 v100, v103\n\t" "v_add_u32 v94, 0xe0800000, v103\n\t" "v_pk_mul_f32 v[80:81], v[80:81], v[100:101] op_sel_hi:[1,0]\n\t" \
   "v_cmp_lt_u32 vcc, 0x40000000, v94\n\t" "v_pk_mul_f32 v[82:83], v[82:83], v[100:101] op_sel_hi:[1,0]\n\t" "s_cbranch_vccnz 70f\n\t" \
   "v_cvt_pk_bf16_f32 v48, v80, v81\n\t" "v_cvt_pk_bf16_f32 v49, v82, v83\n\t" "buffer_store_dwordx2 v[48:49], v132, %[rs], s55 offen sc1\n\t" \
   "v_pk_mul_f32 v[88:89], v[88:89], v[100:101] op_sel_hi:[1,0]\n\t" "v_pk_mul_f32 v[90:91], v[90:91], v[100:101] op_sel_hi:[1,0]\n\t" \
   "v_cvt_pk_bf16_f32 v52, v88, v89\n\t" "v_cvt_pk_bf16_f32 v53, v90, v91\n\t" "71:\n\t" \
   "buffer_store_dwordx2 v[52:53], v133, %[rs], s65 offen sc1\n\t" "s_memtime s[62:63]\n\t" "s_memrealtime s[60:61]\n\t" \
   "buffer_store_dwordx2 v[48:49], v132, %[rs], s50 offen sc1\n\t" RSTAMP(2) "s_branch 40b\n\t" "70:\n\t" "v_max3_f32 v94, v48, v49, v50\n\t" \
   "v_max3_f32 v102, v76, v77, v78\n\t" "v_max3_f32 v94, v94, v51, v79\n\t" "v_max_f32 v92, v94, v102\n\t" "s_nop 1\n\t" \
   "v_max_f32_dpp v102, v92, v92 row_ror:8 row_mask:0xf bank_mask:0xf\n\t" "v_max_f32_dpp v94, v92, v92 row_ror:8 row_mask:0xf bank_mask:0xf\n\t" \
   "s_nop 1\n\t" "v_permlane32_swap_b32 v102, v94\n\t" "v_max_f32 v95, v102, v94\n\t" "v_max_f32 v102, v102, v94\n\t" "s_nop 1\n\t" \
   "v_permlane16_swap_b32 v102, v95\n\t" "v_max_f32 v102, v102, v95\n\t" "v_sub_f32 v80, v48, v102\n\t" "v_sub_f32 v88, v76, v102\n\t" \
   "v_sub_f32 v81, v49, v102\n\t" "v_sub_f32 v89, v77, v102\n\t" "v_sub_f32 v82, v50, v102\n\t" "v_sub_f32 v90, v78, v102\n\t" \
   "v_sub_f32 v83, v51, v102\n\t" "v_sub_f32 v91, v79, v102\n\t" "v_exp_f32 v80, v80\n\t" "v_exp_f32 v88, v88\n\t" "v_exp_f32 v81, v81\n\t" \
   "v_exp_f32 v89, v89\n\t" "v_exp_f32 v82, v82\n\t" "v_exp_f32 v90, v90\n\t" "v_exp_f32 v83, v83\n\t" "v_exp_f32 v91, v91\n\t" "s_nop 0\n\t" \
   "v_add_f32 v96, v80, v81\n\t" "v_add_f32 v103, v82, v83\n\t" "v_add_f32 v94, v96, v103\n\t" "v_add_f32 v96, v96, v103\n\t" "s_nop 1\n\t" \
   "v_permlane32_swap_b32 v96, v94\n\t" "v_add_f32 v96, v96, v94\n\t" "s_nop 1\n\t" \
   "v_add_f32_dpp v96, v96, v96 row_ror:8 row_mask:0xf bank_mask:0xf\n\t" "v_add_f32 v103, v96, v88\n\t" "v_add_f32 v103, v103, v89\n\t" \
   "v_add_f32 v103, v103, v90\n\t" "v_add_f32 v97, v103, v91\n\t" "v_add_f32 v103, v103, v91\n\t" "s_nop 1\n\t" "v_permlane16_swap_b32 v103, v97\n\t" \
   "v_add_f32 v103, v103, v97\n\t" "v_rcp_f32 v100, v103\n\t" "s_nop 0\n\t" "v_pk_mul_f32 v[80:81], v[80:81], v[100:101] op_sel_hi:[1,0]\n\t" \
   "v_pk_mul_f32 v[82:83], v[82:83], v[100:101] op_sel_hi:[1,0]\n\t" "v_pk_mul_f32 v[88:89], v[88:89], v[100:101] op_sel_hi:[1,0]\n\t" \
   "v_pk_mul_f32 v[90:91], v[90:91], v[100:101] op_sel_hi:[1,0]\n\t" "v_cvt_pk_bf16_f32 v48, v80, v81\n\t" "v_cvt_pk_bf16_f32 v49, v82, v83\n\t" \
   "v_cvt_pk_bf16_f32 v52, v88, v89\n\t" "v_cvt_pk_bf16_f32 v53, v90, v91\n\t" "v_min_u32 v48, -2, v48\n\t" "v_min_u32 v52, -2, v52\n\t" \
   "buffer_store_dwordx2 v[48:49], v132, %[rs], s55 offen sc1\n\t" "s_branch 71b\n\t" "74:\n\t" "v_max3_f32 v80, v32, v33, v34\n\t" \
   "v_max3_f32 v81, v35, v36, v37\n\t" "v_max3_f32 v82, v38, v39, v40\n\t" "v_max3_f32 v83, v41, v42, v43\n\t" "v_max3_f32 v80, v80, v81, v82\n\t" \
   "v_max_f32 v81, v80, v83\n\t" "v_max_f32 v80, v80, v83\n\t" "s_nop 1\n\t" "v_permlane16_swap_b32 v80, v81\n\t" "v_max_f32 v82, v80, v81\n\t" \
   "v_max_f32 v80, v80, v81\n\t" "s_nop 1\n\t" "v_permlane32_swap_b32 v80, v82\n\t" "v_max_f32 v80, v80, v82\n\t" "v_sub_f32 v64, v32, v80\n\t" \
   "v_sub_f32 v65, v33, v80\n\t" "v_sub_f32 v66, v34, v80\n\t" "v_sub_f32 v67, v35, v80\n\t" "v_sub_f32 v68, v36, v80\n\t" \
   "v_sub_f32 v69, v37, v80\n\t" "v_sub_f32 v70, v38, v80\n\t" "v_sub_f32 v71, v39, v80\n\t" "v_sub_f32 v72, v40, v80\n\t" \
   "v_sub_f32 v73, v41, v80\n\t" "v_sub_f32 v74, v42, v80\n\t" "v_sub_f32 v75, v43, v80\n\t" "v_exp_f32 v64, v64\n\t" "v_exp_f32 v65, v65\n\t" \
   "v_exp_f32 v66, v66\n\t" "v_exp_f32 v67, v67\n\t" "v_exp_f32 v68, v68\n\t" "v_exp_f32 v69, v69\n\t" "v_exp_f32 v70, v70\n\t" \
   "v_exp_f32 v71, v71\n\t" "v_exp_f32 v72, v72\n\t" "v_exp_f32 v73, v73\n\t" "v_exp_f32 v74, v74\n\t" "v_exp_f32 v75, v75\n\t" "s_nop 0\n\t" \
   "v_add_f32 v80, v64, v65\n\t" "v_add_f32 v81, v66, v67\n\t" "v_add_f32 v82, v68, v69\n\t" "v_add_f32 v83, v70, v71\n\t" \
   "v_add_f32 v84, v72, v73\n\t" "v_add_f32 v85, v74, v75\n\t" "v_add_f32 v80, v80, v81\n\t" "v_add_f32 v82, v82, v83\n\t" \
   "v_add_f32 v84, v84, v85\n\t" "v_add_f32 v80, v80, v82\n\t" "v_fma_f32 v82, v80, 2.0, v84\n\t" "v_fma_f32 v80, v80, 2.0, v84\n\t" "s_nop 0\n\t" \
   "s_nop 0\n\t" "v_permlane16_swap_b32 v80, v82\n\t" "v_add_f32 v81, v80, v82\n\t" "v_add_f32 v80, v80, v82\n\t" "s_nop 0\n\t" "s_nop 0\n\t" \
   "v_permlane32_swap_b32 v80, v81\n\t" "v_add_f32 v80, v80, v81\n\t" "v_rcp_f32 v90, v80\n\t" "s_nop 0\n\t" "v_add_f32 v90, v90, v90\n\t" \
   "s_branch 75b\n\t" "63:\n\t" "s_cmp_lg_u32 s64, 0\n\t" "s_cbranch_scc1 64f\n\t" "s_sub_u32 s69, s56, 0x10000\n\t" "s_and_b32 s69, s69, -2\n\t" \
   "s_branch 50b\n\t" "64:\n\t" "s_mov_b32 s69, s48\n\t" "s_cmp_lt_u32 s64, 3\n\t" "s_cselect_b32 s69, -1, s69\n\t" "s_cmp_lg_u32 s69, -1\n\t" \
   "s_cbranch_scc1 50b\n\t" "s_add_u32 s76, s76, 0x1000000\n\t" "s_mov_b32 s73, 0\n\t" "65:\n\t" "s_add_u32 s73, s73, 1\n\t" \
   "s_min_u32 s72, s73, 7\n\t" "s_mul_i32 s72, s72, 5\n\t" "s_add_u32 s72, s72, %[xs]\n\t" "s_and_b32 s72, s72, 7\n\t" "s_lshl_b32 s71, %[xs], 7\n\t" \
   "v_subrev_u32 v109, s71, v139\n\t" "s_lshl_b32 s72, s72, 7\n\t" "s_add_u32 s72, s72, s66\n\t" "s_cmp_lt_u32 s73, 7\n\t" "s_cbranch_scc1 66f\n\t" \
   "s_sleep 4\n\t" "66:\n\t" "buffer_load_dword v108, v109, %[rs], s72 offen sc1\n\t" "s_waitcnt vmcnt(0)\n\t" "v_readfirstlane_b32 s69, v108\n\t" \
   "s_cmp_gt_u32 s73, 0x10000\n\t" "s_cbranch_scc1 50b\n\t" "s_cmp_eq_u32 s69, -1\n\t" "s_cbranch_scc1 65b\n\t" "s_branch 50b\n\t" "8:\n\t" \
   "s_nop 7\n\t" "s_waitcnt vmcnt(6) lgkmcnt(0)\n\t" "s_add_u32 s70, s70, 1\n\t" "s_cmp_gt_u32 s70, 2\n\t" "s_cbranch_scc1 81f\n\t" \
   "s_lshl_b32 s71, s70, 3\n\t" "s_lshl_b32 s71, 1, s71\n\t" "s_add_u32 s76, s76, s71\n\t" "81:\n\t" "v_mov_b32 v48, 0\n\t" "v_mov_b32 v49, 0\n\t" \
   "v_mov_b32 v50, 0\n\t" "v_mov_b32 v51, 0\n\t" "v_mov_b32 v52, 0\n\t" "v_mov_b32 v53, 0\n\t" "v_mov_b32 v54, 0\n\t" "v_mov_b32 v55, 0\n\t" \
   "v_mov_b32 v56, 0\n\t" "v_mov_b32 v57, 0\n\t" "v_mov_b32 v58, 0\n\t" "v_mov_b32 v59, 0\n\t" "v_mov_b32 v60, 0\n\t" "v_mov_b32 v61, 0\n\t" \
   "v_mov_b32 v62, 0\n\t" "v_mov_b32 v63, 0\n\t" "s_min_u32 s71, s70, 7\n\t" "s_lshr_b32 s67, s71, 1\n\t" "s_add_u32 s67, s67, %[xs]\n\t" \
   "s_and_b32 s67, s67, 3\n\t" "s_lshl_b32 s67, s67, 11\n\t" "s_and_b32 s72, s71, 1\n\t" "s_lshl_b32 s72, s72, 14\n\t" "s_add_u32 s67, s67, s72\n\t" \
   "s_add_u32 s67, s67, s66\n\t" "s_cmp_eq_u32 s70, 1\n\t" "s_cselect_b32 s67, s68, s67\n\t" "s_cmp_lt_u32 s70, 7\n\t" "s_cbranch_scc1 20b\n\t" \
   "s_sleep 3\n\t" "s_cmp_lt_u32 s70, 0x20000\n\t" "s_cbranch_scc1 20b\n\t" "s_mov_b32 s67, %[esc]\n\t" "s_mov_b32 s68, %[esc]\n\t" "s_branch 20b\n\t" \
   "27:\n\t" "v_mov_b32 v98, 0x7ffffff0\n\t" "buffer_store_dword v98, v98, %[rs], 0 offen\n\t" "buffer_store_dword v98, v98, %[rs], 0 offen\n\t" \
   "buffer_store_dword v98, v98, %[rs], 0 offen\n\t" "buffer_store_dword v98, v98, %[rs], 0 offen\n\t" \
   "buffer_store_dword v98, v98, %[rs], 0 offen\n\t" "buffer_store_dword v98, v98, %[rs], 0 offen\n\t" "s_branch 28b\n\t" "141:\n\t" \
   "s_mov_b32 s74, 0\n\t" "s_cmp_eq_u32 s64, 0\n\t" "s_cbranch_scc1 41b\n\t" "42:\n\t" "buffer_load_dword v15, v140, %[rs], s59 offen sc1\n\t" \
   "s_add_u32 s74, s74, 1\n\t" "s_waitcnt vmcnt(0)\n\t" "v_cmp_eq_u32 vcc, -1, v15\n\t" "s_cmp_gt_u32 s74, 0x10000\n\t" "s_cbranch_scc1 41b\n\t" \
   "s_cbranch_vccz 41b\n\t" "s_sleep 2\n\t" "s_branch 42b\n\t" "95:\n\t" "s_movk_i32 s59, 0x1000\n\t" "s_branch 197f\n\t" "97:\n\t" \
   "buffer_load_dwordx4 v[16:19], v138, %[rs], s79 offen offset:0 sc1\n\t" "buffer_load_dwordx4 v[20:23], v138, %[rs], s79 offen offset:1024 sc1\n\t" \
   "buffer_load_dwordx4 v[24:27], v138, %[rs], s79 offen offset:2048 sc1\n\t" \
   "buffer_load_dwordx4 v[28:31], v138, %[rs], s79 offen offset:3072 sc1\n\t" "v_or_b32 v108, s49, v141\n\t" "v_or_b32 v109, s49, v142\n\t" \
   "buffer_store_dwordx4 v[44:47], v108, %[rs], s52 offen sc1\n\t" "buffer_store_dwordx2 v[32:33], v109, %[rs], s51 offen sc1\n\t" \
   "s_waitcnt vmcnt(0)\n\t" "s_branch 96b\n\t" "197:\n\t" "s_mov_b32 s74, 0\n\t" "98:\n\t" "buffer_load_dword v15, v140, %[rs], s59 offen sc1\n\t" \
   "s_add_u32 s74, s74, 1\n\t" "s_waitcnt vmcnt(0)\n\t" "v_cmp_eq_u32 vcc, -1, v15\n\t" "s_cmp_gt_u32 s74, 0x10000\n\t" "s_cbranch_scc1 97b\n\t" \
   "s_cbranch_vccz 97b\n\t" "s_sleep 2\n\t" "s_branch 98b\n\t" "92:\n\t" "s_mov_b32 s59, 0\n\t" "s_branch 145f\n\t" "45:\n\t" \
   "v_mbcnt_lo_u32_b32 v98, -1, 0\n\t" "v_mbcnt_hi_u32_b32 v98, -1, v98\n\t" "v_lshlrev_b32 v98, 4, v98\n\t" "s_add_u32 s72, %[kf0], 4096\n\t" \
   "buffer_load_dwordx4 v[160:163], v98, %[rs], %[kf0] offen offset:0 sc1\n\t" \
   "buffer_load_dwordx4 v[164:167], v98, %[rs], %[kf0] offen offset:1024 sc1\n\t" \
   "buffer_load_dwordx4 v[168:171], v98, %[rs], %[kf0] offen offset:2048 sc1\n\t" \
   "buffer_load_dwordx4 v[172:175], v98, %[rs], %[kf0] offen offset:3072 sc1\n\t" \
   "buffer_load_dwordx4 v[176:179], v98, %[rs], s72 offen offset:0 sc1\n\t" "buffer_load_dwordx4 v[180:183], v98, %[rs], s72 offen offset:1024 sc1\n\t" \
   "buffer_load_dwordx4 v[184:187], v98, %[rs], s72 offen offset:2048 sc1\n\t" \
   "buffer_load_dwordx4 v[188:191], v98, %[rs], s72 offen offset:3072 sc1\n\t" "s_waitcnt vmcnt(0)\n\t" \
   "v_mfma_f32_16x16x32_bf16 v[48:51], v[160:163], v[64:67], 0\n\t" "v_mfma_f32_16x16x32_bf16 v[52:55], v[176:179], v[64:67], 0\n\t" \
   "v_mfma_f32_16x16x32_bf16 v[48:51], v[164:167], v[68:71], v[48:51]\n\t" "v_mfma_f32_16x16x32_bf16 v[52:55], v[180:183], v[68:71], v[52:55]\n\t" \
   "v_mfma_f32_16x16x32_bf16 v[48:51], v[168:171], v[72:75], v[48:51]\n\t" "v_mfma_f32_16x16x32_bf16 v[52:55], v[184:187], v[72:75], v[52:55]\n\t" \
   "v_mfma_f32_16x16x32_bf16 v[48:51], v[172:175], v[104:107], v[48:51]\n\t" "v_mfma_f32_16x16x32_bf16 v[52:55], v[188:191], v[104:107], v[52:55]\n\t" \
   "s_nop 7\n\t" "s_branch 93b\n\t" "145:\n\t" "s_mov_b32 s74, 0\n\t" "46:\n\t" "buffer_load_dword v15, v140, %[rs], s59 offen sc1\n\t" \
   "s_add_u32 s74, s74, 1\n\t" "s_waitcnt vmcnt(0)\n\t" "v_cmp_eq_u32 vcc, -1, v15\n\t" "s_cmp_gt_u32 s74, 0x10000\n\t" "s_cbranch_scc1 45b\n\t" \
   "s_cbranch_vccz 45b\n\t" "s_sleep 2\n\t" "s_branch 46b\n\t" "90:\n\t" "v_or_b32 v108, s49, v141\n\t" "v_or_b32 v109, s49, v142\n\t" \
   "buffer_store_dwordx4 v[44:47], v108, %[rs], s52 offen sc1\n\t" "buffer_store_dwordx2 v[32:33], v109, %[rs], s51 offen sc1\n\t" \
   "s_waitcnt vmcnt(0) lgkmcnt(0)"
#define P1_CLOB "v15", "v16", "v17", "v18", "v19", "v20", "v21", "v22", "v23", "v24", "v25", "v26", "v27", "v28", "v29", "v30", "v31", "v32", "v33", "v34", "v35", "v36", "v37", "v38", "v39", "v40", "v41", "v42", "v43", "v44", "v45", "v46", "v47", "v48", "v49", "v50", "v51", "v52", "v53", "v54", "v55", "v56", "v57", "v58", "v59", "v60", "v61", "v62", "v63", "v64", "v65", "v66", "v67", "v68", "v69", "v70", "v71", "v72", "v73", "v74", "v75", "v76", "v77", "v78", "v79", "v80", "v81", "v82", "v83", "v84", "v85", "v86", "v87", "v88", "v89", "v90", "v91", "v92", "v93", "v94", "v95", "v96", "v97", "v98", "v99", "v100", "v101", "v102", "v103", "v104", "v105", "v106", "v107", "v108", "v109", "v110", "v111", "v144", "v145", "v146", "v147", "v148", "v149", "v150", "v151", "v152", "v153", "v154", "v155", "v156", "v157", "v158", "v159", "v160", "v161", "v162", "v163", "v164", "v165", "v166", "v167", "v168", "v169", "v170", "v171", "v172", "v173", "v174", "v175", "v176", "v177", "v178", "v179", "v180", "v181", "v182", "v183", "v184", "v185", "v186", "v187", "v188", "v189", "v190", "v191", "s60", "s61", "s62", "s63", "s64", "s65", "s66", "s67", "s68", "s69", "s70", "s71", "s72", "s73", "s74", "s75", "s47", "s48", "s49", "s50", "s51", "s52", "s54", "s55", "s79", "s20", "s21", "s22", "s23", "s24", "s25", "s26", "s27", "s28", "s29", "s30", "s31", "s32", "s33", "s34", "s35"
#define PV_ASM \
   "s_mov_b32 s64, -1\n\t" "s_mov_b32 s76, 0\n\t" "s_mov_b32 s77, 0\n\t" "s_mov_b32 s71, 63\n\t" "s_mov_b32 s47, 1300\n\t" "s_mov_b32 s70, 0\n\t" \
   "s_mov_b32 s69, 0\n\t" "s_lshr_b64 s[72:73], %[sel], s71\n\t" "s_lshl_b32 s65, s71, 19\n\t" "s_and_b32 s72, s72, 1\n\t" \
   "s_add_u32 s65, s65, %[pool]\n\t" "s_lshl_b32 s72, s72, 13\n\t" "s_add_u32 s65, s65, s72\n\t" "s_lshr_b64 s[72:73], %[s3], s71\n\t" \
   "s_and_b32 s54, s72, 1\n\t" "s_lshl_b32 s54, s54, 14\n\t" "40:\n\t" "s_cmp_lt_i32 s64, 0\n\t" "s_cselect_b32 s72, 0x7ffffff0, 0\n\t" \
   "s_max_i32 s71, s64, 0\n\t" "v_or_b32 v109, s72, v143\n\t" "s_lshl_b32 s71, s71, 12\n\t" "v_mov_b32 v110, 1\n\t" "s_cmp_eq_u32 s64, 63\n\t" \
   "s_cbranch_scc1 70f\n\t" "71:\n\t" "s_waitcnt vmcnt(0)\n\t" RSTAMP(3) "s_barrier\n\t" "buffer_load_dword v108, v139, %[rs], s65 offen sc1\n\t" \
   "buffer_store_dword v110, v109, %[rs], s71 offen sc1\n\t" PVTRACE "s_add_u32 s48, s69, s47\n\t" "s_cmp_eq_u32 s70, 0\n\t" \
   "s_cselect_b32 s48, s48, -1\n\t" "s_mov_b32 s66, s65\n\t" "s_add_i32 s64, s64, 1\n\t" "s_and_b32 s71, s64, 63\n\t" \
   "s_lshr_b64 s[72:73], %[sel], s71\n\t" "s_lshl_b32 s65, s71, 19\n\t" "s_and_b32 s72, s72, 1\n\t" "s_add_u32 s65, s65, %[pool]\n\t" \
   "s_lshl_b32 s72, s72, 13\n\t" "s_add_u32 s65, s65, s72\n\t" "s_xor_b32 s67, s54, %[lad0]\n\t" "s_xor_b32 s68, s67, 0x4000\n\t" \
   "s_add_u32 s67, s67, s66\n\t" "s_add_u32 s68, s68, s66\n\t" "s_lshr_b64 s[72:73], %[s3], s71\n\t" "s_and_b32 s54, s72, 1\n\t" \
   "s_lshl_b32 s54, s54, 14\n\t" "s_mov_b32 s70, 0\n\t" "s_lshr_b64 s[72:73], %[ysel], s71\n\t" "s_mul_i32 s73, s71, 43\n\t" \
   "s_mul_i32 s49, s71, 80\n\t" "s_and_b32 s72, s72, 1\n\t" "s_lshr_b32 s73, s73, 7\n\t" "s_add_u32 s49, s49, s72\n\t" \
   "s_lshl4_add_u32 s49, s73, s49\n\t" "s_lshl_b32 s49, s49, 13\n\t" "s_add_u32 s49, s49, %[yb]\n\t" "s_add_u32 s50, s49, 32768\n\t" \
   "s_add_u32 s51, s49, 65536\n\t" "s_add_u32 s52, s49, 98304\n\t" "s_add_u32 s53, s49, 131072\n\t" "v_mov_b32 v48, 0\n\t" "v_mov_b32 v49, 0\n\t" \
   "v_mov_b32 v50, 0\n\t" "v_mov_b32 v51, 0\n\t" "v_mov_b32 v52, 0\n\t" "v_mov_b32 v53, 0\n\t" "v_mov_b32 v54, 0\n\t" "v_mov_b32 v55, 0\n\t" \
   "v_mov_b32 v144, 0\n\t" "v_mov_b32 v145, 0\n\t" "v_mov_b32 v146, 0\n\t" "v_mov_b32 v147, 0\n\t" "v_mov_b32 v148, 0\n\t" "v_mov_b32 v149, 0\n\t" \
   "v_mov_b32 v150, 0\n\t" "v_mov_b32 v151, 0\n\t" "v_mov_b32 v152, 0\n\t" "v_mov_b32 v153, 0\n\t" "v_mov_b32 v154, 0\n\t" "v_mov_b32 v155, 0\n\t" \
   "v_mov_b32 v156, 0\n\t" "v_mov_b32 v157, 0\n\t" "v_mov_b32 v158, 0\n\t" "v_mov_b32 v159, 0\n\t" "v_mov_b32 v160, 0\n\t" "v_mov_b32 v161, 0\n\t" \
   "v_mov_b32 v162, 0\n\t" "v_mov_b32 v163, 0\n\t" "s_cmp_eq_u32 s64, 64\n\t" "s_cbranch_scc1 90f\n\t" "s_waitcnt vmcnt(1)\n\t" \
   "v_readlane_b32 s69, v108, 0\n\t" "v_readlane_b32 s71, v108, 1\n\t" "s_cmp_eq_u32 s69, -1\n\t" "s_cbranch_scc1 63f\n\t" "s_cmp_eq_u32 s71, -1\n\t" \
   "s_cselect_b32 s47, s47, s71\n\t" "50:\n\t" "s_add_u32 s73, s69, %[m1c]\n\t" RSTAMP(5) "51:\n\t" "s_memrealtime s[60:61]\n\t" \
   "s_memtime s[62:63]\n\t" "s_waitcnt lgkmcnt(0)\n\t" "s_sub_u32 s74, s62, s57\n\t" "s_mul_i32 s74, s74, s58\n\t" "s_lshr_b32 s74, s74, 12\n\t" \
   "s_add_u32 s74, s74, s56\n\t" "s_lshl_b32 s60, s60, 3\n\t" "s_sub_u32 s75, s74, s60\n\t" "s_cmp_lt_u32 s75, 32\n\t" "s_cbranch_scc1 52f\n\t" \
   "s_cmp_lt_i32 s75, 0\n\t" "s_cselect_b32 s61, 0, 31\n\t" "s_add_u32 s74, s60, s61\n\t" "s_sub_i32 s75, s61, s75\n\t" "s_add_i32 s75, s75, 1\n\t" \
   "s_ashr_i32 s75, s75, 1\n\t" "s_min_i32 s75, s75, 8\n\t" "s_max_i32 s75, s75, -8\n\t" "s_mov_b32 s56, s74\n\t" "s_mov_b32 s57, s62\n\t" \
   "s_add_u32 s58, s58, s75\n\t" "52:\n\t" "s_sub_i32 s74, s73, s74\n\t" "s_cmp_le_i32 s74, 150\n\t" "s_cbranch_scc1 53f\n\t" \
   "s_cmp_lt_i32 s74, 260\n\t" "s_cbranch_scc1 51b\n\t" "s_sleep 2\n\t" "s_cmp_lt_i32 s74, 32000\n\t" "s_cbranch_scc1 51b\n\t" "s_mov_b32 s74, 0\n\t" \
   "53:\n\t" "s_sub_i32 s74, s74, 56\n\t" "s_max_i32 s74, s74, 0\n\t" "s_mul_i32 s74, s74, 23\n\t" "s_lshr_b32 s74, s74, 5\n\t" \
   "s_and_b32 s74, s74, -4\n\t" "s_getpc_b64 s[60:61]\n\t" "54:\n\t" "s_sub_u32 s74, 55f-54b, s74\n\t" "s_add_u32 s60, s60, s74\n\t" \
   "s_addc_u32 s61, s61, 0\n\t" "s_setpc_b64 s[60:61]\n\t" "s_nop 3\n\t" "s_nop 3\n\t" "s_nop 3\n\t" "s_nop 3\n\t" "s_nop 3\n\t" "s_nop 3\n\t" \
   "s_nop 3\n\t" "s_nop 3\n\t" "s_nop 3\n\t" "s_nop 3\n\t" "s_nop 3\n\t" "s_nop 3\n\t" "s_nop 3\n\t" "s_nop 3\n\t" "s_nop 3\n\t" "s_nop 3\n\t" \
   "s_nop 3\n\t" "s_nop 3\n\t" "s_nop 3\n\t" "s_nop 3\n\t" "s_nop 3\n\t" "s_nop 3\n\t" "s_nop 3\n\t" "s_nop 3\n\t" "55:\n\t" "20:\n\t" RSTAMP(4) \
   "buffer_load_dwordx4 v[64:67], v128, %[rs], s67 offen sc1\n\t" "buffer_load_dwordx4 v[68:71], v129, %[rs], s67 offen sc1\n\t" \
   "buffer_load_dwordx4 v[72:75], v130, %[rs], s67 offen sc1\n\t" "s_waitcnt vmcnt(2)\n\t" "ds_write_b128 v112, v[64:67]\n\t" \
   "v_smfmac_f32_16x16x64_bf16 v[48:51], v[64:67], a[0:7], v134\n\t" "s_waitcnt vmcnt(1)\n\t" "ds_write_b128 v113, v[68:71]\n\t" \
   "s_waitcnt vmcnt(0)\n\t" "ds_write_b128 v114, v[72:75]\n\t" RSTAMP(7) "v_smfmac_f32_16x16x64_bf16 v[52:55], v[64:67], a[64:71], v134\n\t" \
   "v_max3_u32 v110, v64, v66, v68\n\t" "v_max3_u32 v111, v70, v72, v74\n\t" "v_smfmac_f32_16x16x64_bf16 v[48:51], v[68:71], a[8:15], v134\n\t" \
   "v_max_u32 v110, v110, v111\n\t" "v_cmp_eq_u32 vcc, -1, v110\n\t" "v_smfmac_f32_16x16x64_bf16 v[52:55], v[68:71], a[72:79], v134\n\t" \
   "s_cbranch_vccnz 8f\n\t" "s_waitcnt lgkmcnt(0)\n\t" "s_barrier\n\t" RSTAMP(0) "ds_read_b128 v[76:79], v115\n\t" "ds_read_b128 v[80:83], v116\n\t" \
   "ds_read_b128 v[84:87], v117\n\t" "v_smfmac_f32_16x16x64_bf16 v[48:51], v[72:75], a[16:23], v134\n\t" "ds_read_b128 v[88:91], v118\n\t" \
   "ds_read_b128 v[92:95], v119\n\t" "v_smfmac_f32_16x16x64_bf16 v[52:55], v[72:75], a[80:87], v134\n\t" "ds_read_b128 v[96:99], v120\n\t" \
   "ds_read_b128 v[100:103], v121\n\t" "s_waitcnt lgkmcnt(6)\n\t" "v_smfmac_f32_16x16x64_bf16 v[48:51], v[76:79], a[24:31], v134\n\t" \
   "v_smfmac_f32_16x16x64_bf16 v[52:55], v[76:79], a[88:95], v134\n\t" "s_waitcnt lgkmcnt(5)\n\t" \
   "v_smfmac_f32_16x16x64_bf16 v[48:51], v[80:83], a[32:39], v134\n\t" "v_smfmac_f32_16x16x64_bf16 v[52:55], v[80:83], a[96:103], v134\n\t" \
   "s_waitcnt lgkmcnt(4)\n\t" "v_smfmac_f32_16x16x64_bf16 v[48:51], v[84:87], a[40:47], v134\n\t" \
   "v_smfmac_f32_16x16x64_bf16 v[52:55], v[84:87], a[104:111], v134\n\t" "s_waitcnt lgkmcnt(3)\n\t" \
   "v_smfmac_f32_16x16x64_bf16 v[48:51], v[88:91], a[48:55], v134\n\t" "v_smfmac_f32_16x16x64_bf16 v[52:55], v[88:91], a[112:119], v134\n\t" \
   "s_waitcnt lgkmcnt(0)\n\t" "v_smfmac_f32_16x16x64_bf16 v[48:51], v[92:95], a[56:63], v134\n\t" \
   "v_smfmac_f32_16x16x64_bf16 v[48:51], v[96:99], v[192:199], v134\n\t" "v_smfmac_f32_16x16x64_bf16 v[48:51], v[100:103], v[200:207], v134\n\t" \
   "v_smfmac_f32_16x16x64_bf16 v[52:55], v[92:95], a[120:127], v134\n\t" "v_smfmac_f32_16x16x64_bf16 v[52:55], v[96:99], v[208:215], v134\n\t" \
   "v_smfmac_f32_16x16x64_bf16 v[52:55], v[100:103], v[216:223], v134\n\t" "v_add_f32 v48, v48, v49\n\t" "v_add_f32 v49, v50, v51\n\t" \
   "v_cvt_pk_bf16_f32 v48, v48, v49\n\t" "ds_write_b32 v135, v48 offset:10240\n\t" "s_nop 3\n\t" "v_add_f32 v52, v52, v53\n\t" \
   "v_add_f32 v53, v54, v55\n\t" "v_cvt_pk_bf16_f32 v52, v52, v53\n\t" "ds_write_b32 v135, v52 offset:10496\n\t" "s_waitcnt lgkmcnt(0)\n\t" \
   "s_barrier\n\t" RSTAMP(6) "ds_read_b64_tr_b16 v[16:17], v136 offset:10240\n\t" "ds_read_b64_tr_b16 v[18:19], v136 offset:10496\n\t" \
   "ds_read_b64_tr_b16 v[20:21], v136 offset:10752\n\t" "ds_read_b64_tr_b16 v[22:23], v136 offset:11008\n\t" \
   "ds_read_b64_tr_b16 v[24:25], v136 offset:11264\n\t" "ds_read_b64_tr_b16 v[26:27], v136 offset:11520\n\t" \
   "ds_read_b64_tr_b16 v[28:29], v136 offset:11776\n\t" "ds_read_b64_tr_b16 v[30:31], v136 offset:12032\n\t" "s_waitcnt lgkmcnt(6)\n\t" \
   "v_smfmac_f32_16x16x64_bf16 v[144:147], v[16:19], a[128:135], v131\n\t" "v_smfmac_f32_16x16x64_bf16 v[148:151], v[16:19], a[160:167], v131\n\t" \
   "v_smfmac_f32_16x16x64_bf16 v[152:155], v[16:19], a[192:199], v131\n\t" "v_smfmac_f32_16x16x64_bf16 v[156:159], v[16:19], a[224:231], v131\n\t" \
   "v_smfmac_f32_16x16x64_bf16 v[160:163], v[16:19], v[224:231], v131\n\t" "s_waitcnt lgkmcnt(0)\n\t" \
   "v_smfmac_f32_16x16x64_bf16 v[144:147], v[20:23], a[136:143], v131\n\t" "v_smfmac_f32_16x16x64_bf16 v[144:147], v[24:27], a[144:151], v131\n\t" \
   "v_smfmac_f32_16x16x64_bf16 v[144:147], v[28:31], a[152:159], v131\n\t" "v_smfmac_f32_16x16x64_bf16 v[148:151], v[20:23], a[168:175], v131\n\t" \
   "v_smfmac_f32_16x16x64_bf16 v[148:151], v[24:27], a[176:183], v131\n\t" "v_smfmac_f32_16x16x64_bf16 v[148:151], v[28:31], a[184:191], v131\n\t" \
   "v_smfmac_f32_16x16x64_bf16 v[152:155], v[20:23], a[200:207], v131\n\t" "v_cvt_pk_bf16_f32 v164, v144, v145\n\t" \
   "v_cvt_pk_bf16_f32 v165, v146, v147\n\t" "v_smfmac_f32_16x16x64_bf16 v[152:155], v[24:27], a[208:215], v131\n\t" "v_min_u32 v164, -2, v164\n\t" \
   "buffer_store_dwordx2 v[164:165], v132, %[rs], s49 offen sc1\n\t" "v_smfmac_f32_16x16x64_bf16 v[152:155], v[28:31], a[216:223], v131\n\t" \
   "v_smfmac_f32_16x16x64_bf16 v[156:159], v[20:23], a[232:239], v131\n\t" "v_cvt_pk_bf16_f32 v166, v148, v149\n\t" \
   "v_cvt_pk_bf16_f32 v167, v150, v151\n\t" "v_smfmac_f32_16x16x64_bf16 v[156:159], v[24:27], a[240:247], v131\n\t" "v_min_u32 v166, -2, v166\n\t" \
   "buffer_store_dwordx2 v[166:167], v132, %[rs], s50 offen sc1\n\t" "v_smfmac_f32_16x16x64_bf16 v[156:159], v[28:31], a[248:255], v131\n\t" \
   "v_smfmac_f32_16x16x64_bf16 v[160:163], v[20:23], v[232:239], v131\n\t" "v_cvt_pk_bf16_f32 v168, v152, v153\n\t" \
   "v_cvt_pk_bf16_f32 v169, v154, v155\n\t" "v_smfmac_f32_16x16x64_bf16 v[160:163], v[24:27], v[240:247], v131\n\t" "v_min_u32 v168, -2, v168\n\t" \
   "buffer_store_dwordx2 v[168:169], v132, %[rs], s51 offen sc1\n\t" "v_smfmac_f32_16x16x64_bf16 v[160:163], v[28:31], v[248:255], v131\n\t" \
   "v_cvt_pk_bf16_f32 v170, v156, v157\n\t" "v_cvt_pk_bf16_f32 v171, v158, v159\n\t" "v_min_u32 v170, -2, v170\n\t" \
   "buffer_store_dwordx2 v[170:171], v132, %[rs], s52 offen sc1\n\t" "s_nop 3\n\t" "v_cvt_pk_bf16_f32 v172, v160, v161\n\t" \
   "v_cvt_pk_bf16_f32 v173, v162, v163\n\t" "v_min_u32 v172, -2, v172\n\t" "buffer_store_dwordx2 v[172:173], v132, %[rs], s53 offen sc1\n\t" RSTAMP(2) \
   "s_branch 40b\n\t" "70:\n\t" "s_mov_b32 s46, 0\n\t" "s_lshr_b64 s[72:73], %[sel], s46\n\t" "s_lshl_b32 s45, s46, 19\n\t" "s_and_b32 s72, s72, 1\n\t" \
   "s_add_u32 s45, s45, %[pool]\n\t" "s_lshl_b32 s72, s72, 13\n\t" "s_add_u32 s45, s45, s72\n\t" "v_mov_b32 v76, -1\n\t" "v_mov_b32 v77, -1\n\t" \
   "v_mov_b32 v78, -1\n\t" "v_mov_b32 v79, -1\n\t" "buffer_store_dwordx4 v[76:79], v133, %[rs], s45 offen sc1\n\t" \
   "buffer_store_dwordx4 v[76:79], v133, %[rs], s45 offen offset:1024 sc1\n\t" "s_branch 71b\n\t" "63:\n\t" "s_cmp_lg_u32 s64, 0\n\t" \
   "s_cbranch_scc1 64f\n\t" "s_sub_u32 s69, s56, 0x10000\n\t" "s_and_b32 s69, s69, -2\n\t" "s_branch 50b\n\t" "64:\n\t" "s_mov_b32 s69, s48\n\t" \
   "s_cmp_lt_u32 s64, 3\n\t" "s_cselect_b32 s69, -1, s69\n\t" "s_cmp_lg_u32 s69, -1\n\t" "s_cbranch_scc1 50b\n\t" "s_add_u32 s76, s76, 0x1000000\n\t" \
   "s_mov_b32 s73, 0\n\t" "65:\n\t" "s_add_u32 s73, s73, 1\n\t" "s_sleep 6\n\t" "s_and_b32 s72, s73, 3\n\t" "s_add_u32 s72, s72, 8\n\t" \
   "s_sub_u32 s72, s72, %[xs]\n\t" "s_lshl_b32 s72, s72, 7\n\t" "s_add_u32 s72, s72, s66\n\t" "buffer_load_dword v108, v139, %[rs], s72 offen sc1\n\t" \
   "s_waitcnt vmcnt(0)\n\t" "v_readfirstlane_b32 s69, v108\n\t" "s_cmp_gt_u32 s73, 0x10000\n\t" "s_cbranch_scc1 50b\n\t" "s_cmp_eq_u32 s69, -1\n\t" \
   "s_cbranch_scc1 65b\n\t" "s_branch 50b\n\t" "8:\n\t" "s_nop 7\n\t" "s_waitcnt lgkmcnt(0)\n\t" "s_add_u32 s70, s70, 1\n\t" "s_cmp_gt_u32 s70, 2\n\t" \
   "s_cbranch_scc1 81f\n\t" "s_lshl_b32 s71, s70, 3\n\t" "s_lshl_b32 s71, 1, s71\n\t" "s_add_u32 s76, s76, s71\n\t" "81:\n\t" "v_mov_b32 v48, 0\n\t" \
   "v_mov_b32 v49, 0\n\t" "v_mov_b32 v50, 0\n\t" "v_mov_b32 v51, 0\n\t" "v_mov_b32 v52, 0\n\t" "v_mov_b32 v53, 0\n\t" "v_mov_b32 v54, 0\n\t" \
   "v_mov_b32 v55, 0\n\t" "s_add_u32 s71, s70, 1\n\t" "s_min_u32 s71, s71, 7\n\t" "s_lshr_b32 s67, s71, 1\n\t" "s_add_u32 s67, s67, %[xs]\n\t" \
   "s_and_b32 s67, s67, 3\n\t" "s_lshl_b32 s67, s67, 11\n\t" "s_and_b32 s72, s71, 1\n\t" "s_lshl_b32 s72, s72, 14\n\t" "s_add_u32 s67, s67, s72\n\t" \
   "s_add_u32 s67, s67, s66\n\t" "s_cmp_lt_u32 s70, 6\n\t" "s_cbranch_scc1 20b\n\t" "s_sleep 3\n\t" "s_cmp_lt_u32 s70, 0x20000\n\t" \
   "s_cbranch_scc1 20b\n\t" "s_mov_b32 s67, %[esc]\n\t" "s_branch 20b\n\t" "90:\n\t" "s_waitcnt vmcnt(0) lgkmcnt(0)"
#define PV_CLOB "v16", "v17", "v18", "v19", "v20", "v21", "v22", "v23", "v24", "v25", "v26", "v27", "v28", "v29", "v30", "v31", "v48", "v49", "v50", "v51", "v52", "v53", "v54", "v55", "v56", "v57", "v58", "v59", "v60", "v61", "v62", "v63", "v64", "v65", "v66", "v67", "v68", "v69", "v70", "v71", "v72", "v73", "v74", "v75", "v76", "v77", "v78", "v79", "v80", "v81", "v82", "v83", "v84", "v85", "v86", "v87", "v88", "v89", "v90", "v91", "v92", "v93", "v94", "v95", "v96", "v97", "v98", "v99", "v100", "v101", "v102", "v103", "v104", "v105", "v106", "v107", "v108", "v109", "v110", "v111", "v144", "v145", "v146", "v147", "v148", "v149", "v150", "v151", "v152", "v153", "v154", "v155", "v156", "v157", "v158", "v159", "v160", "v161", "v162", "v163", "v164", "v165", "v166", "v167", "v168", "v169", "v170", "v171", "v172", "v173", "v174", "v175", "s60", "s61", "s62", "s63", "s64", "s65", "s66", "s67", "s68", "s69", "s70", "s71", "s72", "s73", "s74", "s75", "s45", "s46", "s47", "s48", "s49", "s50", "s51", "s52", "s53", "s54"
#define WAIT_ASM \
   "11:\n\t" "s_memrealtime s[60:61]\n\t" "s_memtime s[62:63]\n\t" "s_waitcnt lgkmcnt(0)\n\t" "s_sub_u32 s74, s62, s57\n\t" \
   "s_mul_i32 s74, s74, s58\n\t" "s_lshr_b32 s74, s74, 12\n\t" "s_add_u32 s74, s74, s56\n\t" "s_lshl_b32 s60, s60, 3\n\t" "s_sub_u32 s75, s74, s60\n\t" \
   "s_cmp_lt_u32 s75, 32\n\t" "s_cbranch_scc1 12f\n\t" "s_cmp_lt_i32 s75, 0\n\t" "s_cselect_b32 s61, 0, 31\n\t" "s_add_u32 s74, s60, s61\n\t" \
   "s_sub_i32 s75, s61, s75\n\t" "s_add_i32 s75, s75, 1\n\t" "s_ashr_i32 s75, s75, 1\n\t" "s_min_i32 s75, s75, 8\n\t" "s_max_i32 s75, s75, -8\n\t" \
   "s_mov_b32 s56, s74\n\t" "s_mov_b32 s57, s62\n\t" "s_add_u32 s58, s58, s75\n\t" "12:\n\t" "s_sub_i32 s74, %[ta], s74\n\t" \
   "s_cmp_le_i32 s74, 150\n\t" "s_cbranch_scc1 13f\n\t" "s_cmp_lt_i32 s74, 260\n\t" "s_cbranch_scc1 11b\n\t" "s_sleep 2\n\t" \
   "s_cmp_lt_i32 s74, 32000\n\t" "s_cbranch_scc1 11b\n\t" "s_mov_b32 s74, 0\n\t" "13:\n\t" "s_sub_i32 s74, s74, 56\n\t" "s_max_i32 s74, s74, 0\n\t" \
   "s_mul_i32 s74, s74, 23\n\t" "s_lshr_b32 s74, s74, 5\n\t" "s_and_b32 s74, s74, -4\n\t" "s_getpc_b64 s[60:61]\n\t" "14:\n\t" \
   "s_sub_u32 s74, 15f-14b, s74\n\t" "s_add_u32 s60, s60, s74\n\t" "s_addc_u32 s61, s61, 0\n\t" "s_setpc_b64 s[60:61]\n\t" "s_nop 3\n\t" "s_nop 3\n\t" \
   "s_nop 3\n\t" "s_nop 3\n\t" "s_nop 3\n\t" "s_nop 3\n\t" "s_nop 3\n\t" "s_nop 3\n\t" "s_nop 3\n\t" "s_nop 3\n\t" "s_nop 3\n\t" "s_nop 3\n\t" \
   "s_nop 3\n\t" "s_nop 3\n\t" "s_nop 3\n\t" "s_nop 3\n\t" "s_nop 3\n\t" "s_nop 3\n\t" "s_nop 3\n\t" "s_nop 3\n\t" "s_nop 3\n\t" "s_nop 3\n\t" \
   "s_nop 3\n\t" "s_nop 3\n\t" "15:\n\t" ""
#define WAIT_CLOB "s60", "s61", "s62", "s63", "s74", "s75"
#define FIN_ASM \
   "s_mov_b32 s40, 0\n\t" "s_mov_b32 s76, 0\n\t" "s_mov_b32 s77, 0\n\t" "s_sub_u32 s45, s56, 0x10000\n\t" "v_mov_b32 v13, -2\n\t" \
   "v_mov_b32 v11, -1\n\t" "30:\n\t" "s_lshr_b64 s[72:73], %[sel], s40\n\t" "s_lshl_b32 s41, s40, 19\n\t" "s_and_b32 s72, s72, 1\n\t" \
   "s_add_u32 s41, s41, %[pool]\n\t" "s_lshl_b32 s72, s72, 13\n\t" "s_add_u32 s41, s41, s72\n\t" "s_lshr_b64 s[72:73], %[s3], s40\n\t" \
   "s_and_b32 s47, s72, 1\n\t" "s_lshl_b32 s47, s47, 14\n\t" "s_mul_i32 s49, s40, 1310720\n\t" "s_add_u32 s49, s49, %[su]\n\t" \
   "s_lshr_b64 s[72:73], %[ysel], s40\n\t" "s_mul_i32 s73, s40, 43\n\t" "s_mul_i32 s50, s40, 80\n\t" "s_and_b32 s72, s72, 1\n\t" \
   "s_lshr_b32 s73, s73, 7\n\t" "s_add_u32 s50, s50, s72\n\t" "s_lshl4_add_u32 s50, s73, s50\n\t" "s_lshl_b32 s50, s50, 13\n\t" \
   "s_add_u32 s50, s50, %[yb]\n\t" "s_add_u32 s51, s50, 4096\n\t" "s_lshl_b32 s52, s40, 12\n\t" "s_add_u32 s52, s52, %[uf]\n\t" \
   "s_add_u32 s69, s45, %[tu]\n\t" "v_max3_u32 v15, v8, v9, v10\n\t" "v_max_u32 v15, v15, v11\n\t" "v_cmp_eq_u32 vcc, -1, v15\n\t" \
   "s_add_u32 s40, s40, 1\n\t" "s_cbranch_vccnz 95f\n\t" "96:\n\t" "11:\n\t" "s_memrealtime s[60:61]\n\t" "s_memtime s[62:63]\n\t" \
   "s_waitcnt lgkmcnt(0)\n\t" "s_sub_u32 s74, s62, s57\n\t" "s_mul_i32 s74, s74, s58\n\t" "s_lshr_b32 s74, s74, 12\n\t" "s_add_u32 s74, s74, s56\n\t" \
   "s_lshl_b32 s60, s60, 3\n\t" "s_sub_u32 s75, s74, s60\n\t" "s_cmp_lt_u32 s75, 32\n\t" "s_cbranch_scc1 12f\n\t" "s_cmp_lt_i32 s75, 0\n\t" \
   "s_cselect_b32 s61, 0, 31\n\t" "s_add_u32 s74, s60, s61\n\t" "s_sub_i32 s75, s61, s75\n\t" "s_add_i32 s75, s75, 1\n\t" "s_ashr_i32 s75, s75, 1\n\t" \
   "s_min_i32 s75, s75, 8\n\t" "s_max_i32 s75, s75, -8\n\t" "s_mov_b32 s56, s74\n\t" "s_mov_b32 s57, s62\n\t" "s_add_u32 s58, s58, s75\n\t" "12:\n\t" \
   "s_sub_i32 s74, s69, s74\n\t" "s_cmp_le_i32 s74, 150\n\t" "s_cbranch_scc1 13f\n\t" "s_cmp_lt_i32 s74, 260\n\t" "s_cbranch_scc1 11b\n\t" \
   "s_sleep 2\n\t" "s_cmp_lt_i32 s74, 32000\n\t" "s_cbranch_scc1 11b\n\t" "s_mov_b32 s74, 0\n\t" "13:\n\t" "s_sub_i32 s74, s74, 56\n\t" \
   "s_max_i32 s74, s74, 0\n\t" "s_mul_i32 s74, s74, 23\n\t" "s_lshr_b32 s74, s74, 5\n\t" "s_and_b32 s74, s74, -4\n\t" "s_getpc_b64 s[60:61]\n\t" \
   "14:\n\t" "s_sub_u32 s74, 15f-14b, s74\n\t" "s_add_u32 s60, s60, s74\n\t" "s_addc_u32 s61, s61, 0\n\t" "s_setpc_b64 s[60:61]\n\t" "s_nop 3\n\t" \
   "s_nop 3\n\t" "s_nop 3\n\t" "s_nop 3\n\t" "s_nop 3\n\t" "s_nop 3\n\t" "s_nop 3\n\t" "s_nop 3\n\t" "s_nop 3\n\t" "s_nop 3\n\t" "s_nop 3\n\t" \
   "s_nop 3\n\t" "s_nop 3\n\t" "s_nop 3\n\t" "s_nop 3\n\t" "s_nop 3\n\t" "s_nop 3\n\t" "s_nop 3\n\t" "s_nop 3\n\t" "s_nop 3\n\t" "s_nop 3\n\t" \
   "s_nop 3\n\t" "s_nop 3\n\t" "s_nop 3\n\t" "15:\n\t" RSTAMP(4) "buffer_load_dwordx4 a[0:3], v16, %[rs], s49 offen offset:0 sc1\n\t" "s_nop 0\n\t" \
   "buffer_load_dwordx4 a[4:7], v16, %[rs], s49 offen offset:1024 sc1\n\t" "s_nop 0\n\t" \
   "buffer_load_dwordx4 a[8:11], v16, %[rs], s49 offen offset:2048 sc1\n\t" "s_nop 0\n\t" \
   "buffer_load_dwordx4 a[12:15], v16, %[rs], s49 offen offset:3072 sc1\n\t" "s_nop 0\n\t" "s_add_u32 s49, s49, 4096\n\t" \
   "buffer_load_dwordx4 a[16:19], v16, %[rs], s49 offen offset:0 sc1\n\t" "s_nop 0\n\t" \
   "buffer_load_dwordx4 a[20:23], v16, %[rs], s49 offen offset:1024 sc1\n\t" "s_nop 0\n\t" \
   "buffer_load_dwordx4 a[24:27], v16, %[rs], s49 offen offset:2048 sc1\n\t" "s_nop 0\n\t" \
   "buffer_load_dwordx4 a[28:31], v16, %[rs], s49 offen offset:3072 sc1\n\t" "s_nop 0\n\t" "s_add_u32 s49, s49, 4096\n\t" \
   "buffer_load_dwordx4 a[32:35], v16, %[rs], s49 offen offset:0 sc1\n\t" "s_nop 0\n\t" \
   "buffer_load_dwordx4 a[36:39], v16, %[rs], s49 offen offset:1024 sc1\n\t" "s_nop 0\n\t" \
   "buffer_load_dwordx4 a[40:43], v16, %[rs], s49 offen offset:2048 sc1\n\t" "s_nop 0\n\t" \
   "buffer_load_dwordx4 a[44:47], v16, %[rs], s49 offen offset:3072 sc1\n\t" "s_nop 0\n\t" "s_add_u32 s49, s49, 4096\n\t" \
   "buffer_load_dwordx4 a[48:51], v16, %[rs], s49 offen offset:0 sc1\n\t" "s_nop 0\n\t" \
   "buffer_load_dwordx4 a[52:55], v16, %[rs], s49 offen offset:1024 sc1\n\t" "s_nop 0\n\t" \
   "buffer_load_dwordx4 a[56:59], v16, %[rs], s49 offen offset:2048 sc1\n\t" "s_nop 0\n\t" \
   "buffer_load_dwordx4 a[60:63], v16, %[rs], s49 offen offset:3072 sc1\n\t" "s_nop 0\n\t" "s_add_u32 s49, s49, 4096\n\t" \
   "buffer_load_dwordx4 a[64:67], v16, %[rs], s49 offen offset:0 sc1\n\t" "s_nop 0\n\t" \
   "buffer_load_dwordx4 a[68:71], v16, %[rs], s49 offen offset:1024 sc1\n\t" "s_nop 0\n\t" \
   "buffer_load_dwordx4 a[72:75], v16, %[rs], s49 offen offset:2048 sc1\n\t" "s_nop 0\n\t" \
   "buffer_load_dwordx4 a[76:79], v16, %[rs], s49 offen offset:3072 sc1\n\t" "s_nop 0\n\t" "s_add_u32 s49, s49, 4096\n\t" \
   "buffer_load_dwordx4 a[80:83], v16, %[rs], s49 offen offset:0 sc1\n\t" "s_nop 0\n\t" \
   "buffer_load_dwordx4 a[84:87], v16, %[rs], s49 offen offset:1024 sc1\n\t" "s_nop 0\n\t" \
   "buffer_load_dwordx4 a[88:91], v16, %[rs], s49 offen offset:2048 sc1\n\t" "s_nop 0\n\t" \
   "buffer_load_dwordx4 a[92:95], v16, %[rs], s49 offen offset:3072 sc1\n\t" "s_nop 0\n\t" "s_add_u32 s49, s49, 4096\n\t" \
   "buffer_load_dwordx4 a[96:99], v16, %[rs], s49 offen offset:0 sc1\n\t" "s_nop 0\n\t" \
   "buffer_load_dwordx4 a[100:103], v16, %[rs], s49 offen offset:1024 sc1\n\t" "s_nop 0\n\t" \
   "buffer_load_dwordx4 a[104:107], v16, %[rs], s49 offen offset:2048 sc1\n\t" "s_nop 0\n\t" \
   "buffer_load_dwordx4 a[108:111], v16, %[rs], s49 offen offset:3072 sc1\n\t" "s_nop 0\n\t" "s_add_u32 s49, s49, 4096\n\t" \
   "buffer_load_dwordx4 a[112:115], v16, %[rs], s49 offen offset:0 sc1\n\t" "s_nop 0\n\t" \
   "buffer_load_dwordx4 a[116:119], v16, %[rs], s49 offen offset:1024 sc1\n\t" "s_nop 0\n\t" \
   "buffer_load_dwordx4 a[120:123], v16, %[rs], s49 offen offset:2048 sc1\n\t" "s_nop 0\n\t" \
   "buffer_load_dwordx4 a[124:127], v16, %[rs], s49 offen offset:3072 sc1\n\t" "s_nop 0\n\t" "s_add_u32 s52, s52, 4096\n\t" \
   "buffer_load_dwordx4 v[8:11], v19, %[rs], s52 offen sc1\n\t" RSTAMP(0) "s_mov_b32 s46, 0\n\t" "s_xor_b32 s64, s47, %[lad0]\n\t" \
   "s_and_b32 s65, s64, 0x4000\n\t" "s_xor_b32 s64, s64, s65\n\t" "v_or_b32 v224, s65, v16\n\t" "v_or_b32 v225, s65, v17\n\t" \
   "v_xor_b32 v226, 0x4000, v224\n\t" "v_xor_b32 v227, 0x4000, v225\n\t" "s_add_u32 s64, s64, s41\n\t" "v_xor_b32 v14, s47, v20\n\t" \
   "v_xor_b32 v15, 0x4000, v14\n\t" "s_add_u32 s65, s64, 32768\n\t" "s_add_u32 s66, s64, 65536\n\t" "s_add_u32 s67, s64, 98304\n\t" \
   "s_add_u32 s68, s64, 131072\n\t" "s_add_u32 s69, s64, 163840\n\t" "s_add_u32 s70, s64, 196608\n\t" "s_add_u32 s71, s64, 229376\n\t" \
   "s_add_u32 s72, s64, 262144\n\t" "s_add_u32 s73, s64, 294912\n\t" "s_add_u32 s53, s41, 4096\n\t" "v_mov_b32 v28, 0\n\t" "v_mov_b32 v29, 0\n\t" \
   "v_mov_b32 v30, 0\n\t" "v_mov_b32 v31, 0\n\t" "19:\n\t" "ds_read_b128 v[24:27], v23\n\t" "s_waitcnt lgkmcnt(0)\n\t" \
   "v_readfirstlane_b32 s74, v26\n\t" "v_readfirstlane_b32 s44, v24\n\t" "v_readfirstlane_b32 s45, v25\n\t" "s_sub_u32 s48, s44, 110\n\t" \
   "s_cmp_lt_u32 s74, s40\n\t" "s_cbranch_scc1 18f\n\t" RSTAMP(7) "21:\n\t" "s_memrealtime s[60:61]\n\t" "s_memtime s[62:63]\n\t" \
   "s_waitcnt lgkmcnt(0)\n\t" "s_sub_u32 s74, s62, s57\n\t" "s_mul_i32 s74, s74, s58\n\t" "s_lshr_b32 s74, s74, 12\n\t" "s_add_u32 s74, s74, s56\n\t" \
   "s_lshl_b32 s60, s60, 3\n\t" "s_sub_u32 s75, s74, s60\n\t" "s_cmp_lt_u32 s75, 32\n\t" "s_cbranch_scc1 22f\n\t" "s_cmp_lt_i32 s75, 0\n\t" \
   "s_cselect_b32 s61, 0, 31\n\t" "s_add_u32 s74, s60, s61\n\t" "s_sub_i32 s75, s61, s75\n\t" "s_add_i32 s75, s75, 1\n\t" "s_ashr_i32 s75, s75, 1\n\t" \
   "s_min_i32 s75, s75, 8\n\t" "s_max_i32 s75, s75, -8\n\t" "s_mov_b32 s56, s74\n\t" "s_mov_b32 s57, s62\n\t" "s_add_u32 s58, s58, s75\n\t" "22:\n\t" \
   "s_sub_i32 s74, s48, s74\n\t" "s_cmp_le_i32 s74, 150\n\t" "s_cbranch_scc1 23f\n\t" "s_cmp_lt_i32 s74, 260\n\t" "s_cbranch_scc1 21b\n\t" \
   "s_sleep 2\n\t" "s_cmp_lt_i32 s74, 32000\n\t" "s_cbranch_scc1 21b\n\t" "s_mov_b32 s74, 0\n\t" "23:\n\t" "s_sub_i32 s74, s74, 56\n\t" \
   "s_max_i32 s74, s74, 0\n\t" "s_mul_i32 s74, s74, 23\n\t" "s_lshr_b32 s74, s74, 5\n\t" "s_and_b32 s74, s74, -4\n\t" "s_getpc_b64 s[60:61]\n\t" \
   "24:\n\t" "s_sub_u32 s74, 25f-24b, s74\n\t" "s_add_u32 s60, s60, s74\n\t" "s_addc_u32 s61, s61, 0\n\t" "s_setpc_b64 s[60:61]\n\t" "s_nop 3\n\t" \
   "s_nop 3\n\t" "s_nop 3\n\t" "s_nop 3\n\t" "s_nop 3\n\t" "s_nop 3\n\t" "s_nop 3\n\t" "s_nop 3\n\t" "s_nop 3\n\t" "s_nop 3\n\t" "s_nop 3\n\t" \
   "s_nop 3\n\t" "s_nop 3\n\t" "s_nop 3\n\t" "s_nop 3\n\t" "s_nop 3\n\t" "s_nop 3\n\t" "s_nop 3\n\t" "s_nop 3\n\t" "s_nop 3\n\t" "s_nop 3\n\t" \
   "s_nop 3\n\t" "s_nop 3\n\t" "s_nop 3\n\t" "25:\n\t" "buffer_load_dwordx4 v[32:35], v16, %[rs], s50 offen offset:0 sc1\n\t" \
   "buffer_load_dwordx4 v[36:39], v16, %[rs], s50 offen offset:1024 sc1\n\t" "buffer_load_dwordx4 v[40:43], v16, %[rs], s50 offen offset:2048 sc1\n\t" \
   "buffer_load_dwordx4 v[44:47], v16, %[rs], s50 offen offset:3072 sc1\n\t" "buffer_load_dwordx4 v[48:51], v16, %[rs], s51 offen offset:0 sc1\n\t" \
   "buffer_load_dwordx4 v[52:55], v16, %[rs], s51 offen offset:1024 sc1\n\t" "buffer_load_dwordx4 v[56:59], v16, %[rs], s51 offen offset:2048 sc1\n\t" \
   "buffer_load_dwordx4 v[60:63], v16, %[rs], s51 offen offset:3072 sc1\n\t" "s_nop 3\n\t" "s_nop 3\n\t" "s_nop 3\n\t" "s_nop 3\n\t" "s_nop 3\n\t" \
   "s_nop 3\n\t" "s_nop 3\n\t" "s_nop 3\n\t" "s_nop 3\n\t" "s_nop 3\n\t" "s_nop 3\n\t" "s_nop 3\n\t" "s_nop 3\n\t" "s_nop 3\n\t" "s_nop 3\n\t" \
   "s_nop 3\n\t" RSTAMP(6) "buffer_load_dwordx4 v[64:67], v224, %[rs], s64 offen offset:0 sc1\n\t" \
   "buffer_load_dwordx4 v[68:71], v224, %[rs], s64 offen offset:1024 sc1\n\t" "buffer_load_dwordx4 v[72:75], v224, %[rs], s65 offen offset:0 sc1\n\t" \
   "buffer_load_dwordx4 v[76:79], v224, %[rs], s65 offen offset:1024 sc1\n\t" "buffer_load_dwordx4 v[80:83], v224, %[rs], s66 offen offset:0 sc1\n\t" \
   "buffer_load_dwordx4 v[84:87], v224, %[rs], s66 offen offset:1024 sc1\n\t" "buffer_load_dwordx4 v[88:91], v224, %[rs], s67 offen offset:0 sc1\n\t" \
   "buffer_load_dwordx4 v[92:95], v224, %[rs], s67 offen offset:1024 sc1\n\t" "buffer_load_dwordx4 v[96:99], v224, %[rs], s68 offen offset:0 sc1\n\t" \
   "buffer_load_dwordx4 v[100:103], v224, %[rs], s68 offen offset:1024 sc1\n\t" \
   "buffer_load_dwordx4 v[104:107], v224, %[rs], s69 offen offset:0 sc1\n\t" \
   "buffer_load_dwordx4 v[108:111], v224, %[rs], s69 offen offset:1024 sc1\n\t" \
   "buffer_load_dwordx4 v[112:115], v224, %[rs], s70 offen offset:0 sc1\n\t" \
   "buffer_load_dwordx4 v[116:119], v224, %[rs], s70 offen offset:1024 sc1\n\t" \
   "buffer_load_dwordx4 v[120:123], v224, %[rs], s71 offen offset:0 sc1\n\t" \
   "buffer_load_dwordx4 v[124:127], v224, %[rs], s71 offen offset:1024 sc1\n\t" \
   "buffer_load_dwordx2 v[128:129], v225, %[rs], s72 offen offset:0 sc1\n\t" \
   "buffer_load_dwordx2 v[130:131], v225, %[rs], s72 offen offset:128 sc1\n\t" \
   "buffer_load_dwordx2 v[132:133], v225, %[rs], s72 offen offset:1024 sc1\n\t" \
   "buffer_load_dwordx2 v[134:135], v225, %[rs], s72 offen offset:1152 sc1\n\t" \
   "buffer_load_dwordx2 v[136:137], v225, %[rs], s73 offen offset:0 sc1\n\t" \
   "buffer_load_dwordx2 v[138:139], v225, %[rs], s73 offen offset:128 sc1\n\t" \
   "buffer_load_dwordx2 v[140:141], v225, %[rs], s73 offen offset:1024 sc1\n\t" \
   "buffer_load_dwordx2 v[142:143], v225, %[rs], s73 offen offset:1152 sc1\n\t" YSTAMP(24) "s_waitcnt vmcnt(23)\n\t" RSTAMP(1) \
   "v_smfmac_f32_16x16x64_bf16 v[28:31], v[64:67], a[0:7], v22\n\t" "s_waitcnt vmcnt(22)\n\t" \
   "v_smfmac_f32_16x16x64_bf16 v[28:31], v[68:71], a[8:15], v22\n\t" "s_waitcnt vmcnt(21)\n\t" \
   "v_smfmac_f32_16x16x64_bf16 v[28:31], v[72:75], a[16:23], v22\n\t" "s_waitcnt vmcnt(20)\n\t" \
   "v_smfmac_f32_16x16x64_bf16 v[28:31], v[76:79], a[24:31], v22\n\t" "s_waitcnt vmcnt(19)\n\t" \
   "v_smfmac_f32_16x16x64_bf16 v[28:31], v[80:83], a[32:39], v22\n\t" "s_waitcnt vmcnt(18)\n\t" \
   "v_smfmac_f32_16x16x64_bf16 v[28:31], v[84:87], a[40:47], v22\n\t" "s_waitcnt vmcnt(17)\n\t" \
   "v_smfmac_f32_16x16x64_bf16 v[28:31], v[88:91], a[48:55], v22\n\t" "s_waitcnt vmcnt(16)\n\t" \
   "v_smfmac_f32_16x16x64_bf16 v[28:31], v[92:95], a[56:63], v22\n\t" "s_waitcnt vmcnt(15)\n\t" \
   "v_smfmac_f32_16x16x64_bf16 v[28:31], v[96:99], a[64:71], v22\n\t" "s_waitcnt vmcnt(14)\n\t" \
   "v_smfmac_f32_16x16x64_bf16 v[28:31], v[100:103], a[72:79], v22\n\t" "s_waitcnt vmcnt(13)\n\t" \
   "v_smfmac_f32_16x16x64_bf16 v[28:31], v[104:107], a[80:87], v22\n\t" "s_waitcnt vmcnt(12)\n\t" \
   "v_smfmac_f32_16x16x64_bf16 v[28:31], v[108:111], a[88:95], v22\n\t" "s_waitcnt vmcnt(11)\n\t" \
   "v_smfmac_f32_16x16x64_bf16 v[28:31], v[112:115], a[96:103], v22\n\t" "s_waitcnt vmcnt(10)\n\t" \
   "v_smfmac_f32_16x16x64_bf16 v[28:31], v[116:119], a[104:111], v22\n\t" "s_waitcnt vmcnt(9)\n\t" \
   "v_smfmac_f32_16x16x64_bf16 v[28:31], v[120:123], a[112:119], v22\n\t" "s_waitcnt vmcnt(8)\n\t" \
   "v_smfmac_f32_16x16x64_bf16 v[28:31], v[124:127], a[120:127], v22\n\t" "s_waitcnt vmcnt(6)\n\t" \
   "v_smfmac_f32_16x16x64_bf16 v[28:31], v[128:131], v[32:39], v22\n\t" "s_waitcnt vmcnt(4)\n\t" \
   "v_smfmac_f32_16x16x64_bf16 v[28:31], v[132:135], v[40:47], v22\n\t" "s_waitcnt vmcnt(2)\n\t" \
   "v_smfmac_f32_16x16x64_bf16 v[28:31], v[136:139], v[48:55], v22\n\t" "s_waitcnt vmcnt(0)\n\t" \
   "v_smfmac_f32_16x16x64_bf16 v[28:31], v[140:143], v[56:63], v22\n\t" "s_nop 7\n\t" "v_add_f32 v28, v28, v29\n\t" "v_add_f32 v30, v30, v31\n\t" \
   "v_cvt_pk_bf16_f32 v24, v28, v30\n\t" "v_cmp_u_f32 vcc, v28, v30\n\t" "s_nop 0\n\t" \
   "v_mov_b32_dpp v25, v24 quad_perm:[1,0,3,2] row_mask:0xf bank_mask:0xf\n\t" "s_cbranch_vccnz 8f\n\t" "2:\n\t" RSTAMP(2) \
   "v_perm_b32 v26, v25, v24, v18\n\t" "s_nop 1\n\t" "v_min_u32_dpp v24, v26, v13 quad_perm:[0,1,0,1] row_mask:0xf bank_mask:0xf\n\t" \
   "v_mov_b32_dpp v25, v26 quad_perm:[2,3,2,3] row_mask:0xf bank_mask:0xf\n\t" "buffer_store_dwordx2 v[24:25], v14, %[rs], s41 offen sc1\n\t" \
   "buffer_store_dwordx2 v[24:25], v14, %[rs], s53 offen sc1\n\t" "s_memtime s[62:63]\n\t" "s_memrealtime s[60:61]\n\t" \
   "buffer_store_dwordx2 v[24:25], v15, %[rs], s41 offen sc1\n\t" "buffer_store_dwordx2 v[24:25], v15, %[rs], s53 offen sc1\n\t" RSTAMP(3) \
   "s_waitcnt lgkmcnt(0)\n\t" "s_sub_u32 s74, s62, s57\n\t" "s_mul_i32 s74, s74, s58\n\t" "s_lshr_b32 s74, s74, 12\n\t" "s_add_u32 s74, s74, s56\n\t" \
   "s_lshl_b32 s60, s60, 3\n\t" "s_sub_u32 s75, s74, s60\n\t" "s_cmp_lt_u32 s75, 32\n\t" "s_cbranch_scc1 38f\n\t" "s_cmp_lt_i32 s75, 0\n\t" \
   "s_cselect_b32 s61, 0, 31\n\t" "s_add_u32 s74, s60, s61\n\t" "s_sub_i32 s75, s61, s75\n\t" "s_add_i32 s75, s75, 1\n\t" "s_ashr_i32 s75, s75, 1\n\t" \
   "s_min_i32 s75, s75, 8\n\t" "s_max_i32 s75, s75, -8\n\t" "s_mov_b32 s56, s74\n\t" "s_mov_b32 s57, s62\n\t" "s_add_u32 s58, s58, s75\n\t" "38:\n\t" \
   "s_min_u32 s75, s46, 1\n\t" "s_and_b32 s74, s74, -4\n\t" "s_or_b32 s74, s74, s75\n\t" "v_mov_b32 v26, s74\n\t" \
   "buffer_store_dword v26, v21, %[rs], s41 offen sc1\n\t" FINTRACE "v_xor_b32 v23, 32, v23\n\t" "s_cmp_lt_u32 s40, 63\n\t" "s_cbranch_scc1 30b\n\t" \
   "s_branch 90f\n\t" "18:\n\t" "s_sleep 2\n\t" "s_branch 19b\n\t" "8:\n\t" "s_mov_b32 s74, 0\n\t" "87:\n\t" "v_max3_u32 v12, v32, v34, v36\n\t" \
   "v_max3_u32 v12, v12, v38, v40\n\t" "v_max3_u32 v12, v12, v42, v44\n\t" "v_max3_u32 v12, v12, v46, v48\n\t" "v_max3_u32 v12, v12, v50, v52\n\t" \
   "v_max3_u32 v12, v12, v54, v56\n\t" "v_max3_u32 v12, v12, v58, v60\n\t" "v_max_u32 v12, v12, v62\n\t" "v_cmp_eq_u32 vcc, -1, v12\n\t" \
   "s_cmp_gt_u32 s74, 0x10000\n\t" "s_cbranch_scc1 86f\n\t" "s_cbranch_vccz 86f\n\t" "s_cmp_eq_u32 s74, 0\n\t" "s_cselect_b32 s75, 0x100, 0\n\t" \
   "s_cbranch_scc1 88f\n\t" "s_sleep 3\n\t" "88:\n\t" "buffer_load_dwordx4 v[32:35], v16, %[rs], s50 offen offset:0 sc1\n\t" \
   "buffer_load_dwordx4 v[36:39], v16, %[rs], s50 offen offset:1024 sc1\n\t" "buffer_load_dwordx4 v[40:43], v16, %[rs], s50 offen offset:2048 sc1\n\t" \
   "buffer_load_dwordx4 v[44:47], v16, %[rs], s50 offen offset:3072 sc1\n\t" "buffer_load_dwordx4 v[48:51], v16, %[rs], s51 offen offset:0 sc1\n\t" \
   "buffer_load_dwordx4 v[52:55], v16, %[rs], s51 offen offset:1024 sc1\n\t" "buffer_load_dwordx4 v[56:59], v16, %[rs], s51 offen offset:2048 sc1\n\t" \
   "buffer_load_dwordx4 v[60:63], v16, %[rs], s51 offen offset:3072 sc1\n\t" "s_add_u32 s76, s76, s75\n\t" "s_add_u32 s74, s74, 1\n\t" \
   "s_waitcnt vmcnt(0)\n\t" "s_branch 87b\n\t" "86:\n\t" "v_mov_b32 v27, 0\n\t" "v_mov_b32 v28, 0\n\t" "v_mov_b32 v29, 0\n\t" "v_mov_b32 v30, 0\n\t" \
   "v_mov_b32 v31, 0\n\t" "v_max3_u32 v27, v27, v64, v66\n\t" "v_max3_u32 v27, v27, v68, v70\n\t" "v_max3_u32 v27, v27, v72, v74\n\t" \
   "v_max3_u32 v27, v27, v76, v78\n\t" "v_max3_u32 v27, v27, v80, v82\n\t" "v_max3_u32 v27, v27, v84, v86\n\t" "v_max3_u32 v27, v27, v88, v90\n\t" \
   "v_max3_u32 v27, v27, v92, v94\n\t" "v_max3_u32 v27, v27, v96, v98\n\t" "v_max3_u32 v27, v27, v100, v102\n\t" "v_max3_u32 v27, v27, v104, v106\n\t" \
   "v_max3_u32 v27, v27, v108, v110\n\t" "v_max3_u32 v27, v27, v112, v114\n\t" "v_max3_u32 v27, v27, v116, v118\n\t" \
   "v_max3_u32 v27, v27, v120, v122\n\t" "v_max3_u32 v27, v27, v124, v126\n\t" "v_max3_u32 v27, v27, v128, v130\n\t" \
   "v_max3_u32 v27, v27, v132, v134\n\t" "v_max3_u32 v27, v27, v136, v138\n\t" "v_max3_u32 v27, v27, v140, v142\n\t" "v_cmp_eq_u32 vcc, -1, v27\n\t" \
   "s_nop 0\n\t" "s_cbranch_vccnz 85f\n\t" "v_smfmac_f32_16x16x64_bf16 v[28:31], v[64:67], a[0:7], v22\n\t" \
   "v_smfmac_f32_16x16x64_bf16 v[28:31], v[68:71], a[8:15], v22\n\t" "v_smfmac_f32_16x16x64_bf16 v[28:31], v[72:75], a[16:23], v22\n\t" \
   "v_smfmac_f32_16x16x64_bf16 v[28:31], v[76:79], a[24:31], v22\n\t" "v_smfmac_f32_16x16x64_bf16 v[28:31], v[80:83], a[32:39], v22\n\t" \
   "v_smfmac_f32_16x16x64_bf16 v[28:31], v[84:87], a[40:47], v22\n\t" "v_smfmac_f32_16x16x64_bf16 v[28:31], v[88:91], a[48:55], v22\n\t" \
   "v_smfmac_f32_16x16x64_bf16 v[28:31], v[92:95], a[56:63], v22\n\t" "v_smfmac_f32_16x16x64_bf16 v[28:31], v[96:99], a[64:71], v22\n\t" \
   "v_smfmac_f32_16x16x64_bf16 v[28:31], v[100:103], a[72:79], v22\n\t" "v_smfmac_f32_16x16x64_bf16 v[28:31], v[104:107], a[80:87], v22\n\t" \
   "v_smfmac_f32_16x16x64_bf16 v[28:31], v[108:111], a[88:95], v22\n\t" "v_smfmac_f32_16x16x64_bf16 v[28:31], v[112:115], a[96:103], v22\n\t" \
   "v_smfmac_f32_16x16x64_bf16 v[28:31], v[116:119], a[104:111], v22\n\t" "v_smfmac_f32_16x16x64_bf16 v[28:31], v[120:123], a[112:119], v22\n\t" \
   "v_smfmac_f32_16x16x64_bf16 v[28:31], v[124:127], a[120:127], v22\n\t" "v_smfmac_f32_16x16x64_bf16 v[28:31], v[128:131], v[32:39], v22\n\t" \
   "v_smfmac_f32_16x16x64_bf16 v[28:31], v[132:135], v[40:47], v22\n\t" "v_smfmac_f32_16x16x64_bf16 v[28:31], v[136:139], v[48:55], v22\n\t" \
   "v_smfmac_f32_16x16x64_bf16 v[28:31], v[140:143], v[56:63], v22\n\t" "84:\n\t" "v_cmp_eq_u32 vcc, -1, v27\n\t" "s_cmp_gt_u32 s46, 0x10000\n\t" \
   "s_cbranch_scc1 83f\n\t" "s_cbranch_vccz 83f\n\t" "85:\n\t" "s_add_u32 s46, s46, 1\n\t" "s_cmp_gt_u32 s46, 2\n\t" "s_cbranch_scc1 81f\n\t" \
   "s_lshl_b32 s74, s46, 3\n\t" "s_lshl_b32 s74, 1, s74\n\t" "s_add_u32 s77, s77, s74\n\t" "81:\n\t" "s_min_u32 s74, s46, 7\n\t" \
   "s_lshr_b32 s64, s74, 1\n\t" "s_add_u32 s64, s64, %[xs]\n\t" "s_and_b32 s64, s64, 3\n\t" "s_lshl_b32 s64, s64, 11\n\t" "s_and_b32 s75, s74, 1\n\t" \
   "s_lshl_b32 s75, s75, 14\n\t" "s_xor_b32 s75, s75, s47\n\t" "s_xor_b32 s75, s75, %[lad0]\n\t" "s_and_b32 s75, s75, 0x4000\n\t" \
   "s_add_u32 s64, s64, s75\n\t" "s_add_u32 s64, s64, s41\n\t" "s_add_u32 s65, s64, 32768\n\t" "s_add_u32 s66, s64, 65536\n\t" \
   "s_add_u32 s67, s64, 98304\n\t" "s_add_u32 s68, s64, 131072\n\t" "s_add_u32 s69, s64, 163840\n\t" "s_add_u32 s70, s64, 196608\n\t" \
   "s_add_u32 s71, s64, 229376\n\t" "s_add_u32 s72, s64, 262144\n\t" "s_add_u32 s73, s64, 294912\n\t" "v_mov_b32 v28, 0\n\t" "v_mov_b32 v29, 0\n\t" \
   "v_mov_b32 v30, 0\n\t" "v_mov_b32 v31, 0\n\t" "v_mov_b32 v27, 0\n\t" "s_cmp_lt_u32 s46, 7\n\t" "s_cbranch_scc1 82f\n\t" "s_sleep 3\n\t" "82:\n\t" \
   RSTAMP(6) "buffer_load_dwordx4 v[64:67], v16, %[rs], s64 offen offset:0 sc1\n\t" \
   "buffer_load_dwordx4 v[68:71], v16, %[rs], s64 offen offset:1024 sc1\n\t" "buffer_load_dwordx4 v[72:75], v16, %[rs], s65 offen offset:0 sc1\n\t" \
   "buffer_load_dwordx4 v[76:79], v16, %[rs], s65 offen offset:1024 sc1\n\t" "buffer_load_dwordx4 v[80:83], v16, %[rs], s66 offen offset:0 sc1\n\t" \
   "buffer_load_dwordx4 v[84:87], v16, %[rs], s66 offen offset:1024 sc1\n\t" "buffer_load_dwordx4 v[88:91], v16, %[rs], s67 offen offset:0 sc1\n\t" \
   "buffer_load_dwordx4 v[92:95], v16, %[rs], s67 offen offset:1024 sc1\n\t" "buffer_load_dwordx4 v[96:99], v16, %[rs], s68 offen offset:0 sc1\n\t" \
   "buffer_load_dwordx4 v[100:103], v16, %[rs], s68 offen offset:1024 sc1\n\t" "buffer_load_dwordx4 v[104:107], v16, %[rs], s69 offen offset:0 sc1\n\t" \
   "buffer_load_dwordx4 v[108:111], v16, %[rs], s69 offen offset:1024 sc1\n\t" "buffer_load_dwordx4 v[112:115], v16, %[rs], s70 offen offset:0 sc1\n\t" \
   "buffer_load_dwordx4 v[116:119], v16, %[rs], s70 offen offset:1024 sc1\n\t" "buffer_load_dwordx4 v[120:123], v16, %[rs], s71 offen offset:0 sc1\n\t" \
   "buffer_load_dwordx4 v[124:127], v16, %[rs], s71 offen offset:1024 sc1\n\t" "buffer_load_dwordx2 v[128:129], v17, %[rs], s72 offen offset:0 sc1\n\t" \
   "buffer_load_dwordx2 v[130:131], v17, %[rs], s72 offen offset:128 sc1\n\t" \
   "buffer_load_dwordx2 v[132:133], v17, %[rs], s72 offen offset:1024 sc1\n\t" \
   "buffer_load_dwordx2 v[134:135], v17, %[rs], s72 offen offset:1152 sc1\n\t" "buffer_load_dwordx2 v[136:137], v17, %[rs], s73 offen offset:0 sc1\n\t" \
   "buffer_load_dwordx2 v[138:139], v17, %[rs], s73 offen offset:128 sc1\n\t" \
   "buffer_load_dwordx2 v[140:141], v17, %[rs], s73 offen offset:1024 sc1\n\t" \
   "buffer_load_dwordx2 v[142:143], v17, %[rs], s73 offen offset:1152 sc1\n\t" "s_waitcnt vmcnt(23)\n\t" \
   "v_smfmac_f32_16x16x64_bf16 v[28:31], v[64:67], a[0:7], v22\n\t" "v_max3_u32 v27, v27, v64, v66\n\t" "s_waitcnt vmcnt(22)\n\t" \
   "v_smfmac_f32_16x16x64_bf16 v[28:31], v[68:71], a[8:15], v22\n\t" "v_max3_u32 v27, v27, v68, v70\n\t" "s_waitcnt vmcnt(21)\n\t" \
   "v_smfmac_f32_16x16x64_bf16 v[28:31], v[72:75], a[16:23], v22\n\t" "v_max3_u32 v27, v27, v72, v74\n\t" "s_waitcnt vmcnt(20)\n\t" \
   "v_smfmac_f32_16x16x64_bf16 v[28:31], v[76:79], a[24:31], v22\n\t" "v_max3_u32 v27, v27, v76, v78\n\t" "s_waitcnt vmcnt(19)\n\t" \
   "v_smfmac_f32_16x16x64_bf16 v[28:31], v[80:83], a[32:39], v22\n\t" "v_max3_u32 v27, v27, v80, v82\n\t" "s_waitcnt vmcnt(18)\n\t" \
   "v_smfmac_f32_16x16x64_bf16 v[28:31], v[84:87], a[40:47], v22\n\t" "v_max3_u32 v27, v27, v84, v86\n\t" "s_waitcnt vmcnt(17)\n\t" \
   "v_smfmac_f32_16x16x64_bf16 v[28:31], v[88:91], a[48:55], v22\n\t" "v_max3_u32 v27, v27, v88, v90\n\t" "s_waitcnt vmcnt(16)\n\t" \
   "v_smfmac_f32_16x16x64_bf16 v[28:31], v[92:95], a[56:63], v22\n\t" "v_max3_u32 v27, v27, v92, v94\n\t" "s_waitcnt vmcnt(15)\n\t" \
   "v_smfmac_f32_16x16x64_bf16 v[28:31], v[96:99], a[64:71], v22\n\t" "v_max3_u32 v27, v27, v96, v98\n\t" "s_waitcnt vmcnt(14)\n\t" \
   "v_smfmac_f32_16x16x64_bf16 v[28:31], v[100:103], a[72:79], v22\n\t" "v_max3_u32 v27, v27, v100, v102\n\t" "s_waitcnt vmcnt(13)\n\t" \
   "v_smfmac_f32_16x16x64_bf16 v[28:31], v[104:107], a[80:87], v22\n\t" "v_max3_u32 v27, v27, v104, v106\n\t" "s_waitcnt vmcnt(12)\n\t" \
   "v_smfmac_f32_16x16x64_bf16 v[28:31], v[108:111], a[88:95], v22\n\t" "v_max3_u32 v27, v27, v108, v110\n\t" "s_waitcnt vmcnt(11)\n\t" \
   "v_smfmac_f32_16x16x64_bf16 v[28:31], v[112:115], a[96:103], v22\n\t" "v_max3_u32 v27, v27, v112, v114\n\t" "s_waitcnt vmcnt(10)\n\t" \
   "v_smfmac_f32_16x16x64_bf16 v[28:31], v[116:119], a[104:111], v22\n\t" "v_max3_u32 v27, v27, v116, v118\n\t" "s_waitcnt vmcnt(9)\n\t" \
   "v_smfmac_f32_16x16x64_bf16 v[28:31], v[120:123], a[112:119], v22\n\t" "v_max3_u32 v27, v27, v120, v122\n\t" "s_waitcnt vmcnt(8)\n\t" \
   "v_smfmac_f32_16x16x64_bf16 v[28:31], v[124:127], a[120:127], v22\n\t" "v_max3_u32 v27, v27, v124, v126\n\t" "s_waitcnt vmcnt(6)\n\t" \
   "v_smfmac_f32_16x16x64_bf16 v[28:31], v[128:131], v[32:39], v22\n\t" "v_max3_u32 v27, v27, v128, v130\n\t" "s_waitcnt vmcnt(4)\n\t" \
   "v_smfmac_f32_16x16x64_bf16 v[28:31], v[132:135], v[40:47], v22\n\t" "v_max3_u32 v27, v27, v132, v134\n\t" "s_waitcnt vmcnt(2)\n\t" \
   "v_smfmac_f32_16x16x64_bf16 v[28:31], v[136:139], v[48:55], v22\n\t" "v_max3_u32 v27, v27, v136, v138\n\t" "s_waitcnt vmcnt(0)\n\t" \
   "v_smfmac_f32_16x16x64_bf16 v[28:31], v[140:143], v[56:63], v22\n\t" "v_max3_u32 v27, v27, v140, v142\n\t" "s_branch 84b\n\t" "83:\n\t" \
   "s_or_b32 s46, s46, 0x10000000\n\t" "s_nop 4\n\t" "v_add_f32 v28, v28, v29\n\t" "v_add_f32 v30, v30, v31\n\t" "v_cvt_pk_bf16_f32 v24, v28, v30\n\t" \
   "s_nop 1\n\t" "v_mov_b32_dpp v25, v24 quad_perm:[1,0,3,2] row_mask:0xf bank_mask:0xf\n\t" "s_branch 2b\n\t" "95:\n\t" "s_mov_b32 s74, 0\n\t" \
   "97:\n\t" "buffer_load_dwordx4 v[8:11], v19, %[rs], s52 offen sc1\n\t" "s_add_u32 s74, s74, 1\n\t" "s_waitcnt vmcnt(0)\n\t" \
   "v_max3_u32 v15, v8, v9, v10\n\t" "v_max_u32 v15, v15, v11\n\t" "v_cmp_eq_u32 vcc, -1, v15\n\t" "s_cmp_gt_u32 s74, 0x10000\n\t" \
   "s_cbranch_scc1 96b\n\t" "s_cbranch_vccz 96b\n\t" "s_sleep 2\n\t" "s_branch 97b\n\t" "90:\n\t" "s_waitcnt vmcnt(0) lgkmcnt(0)"
#define FIN_CLOB "v8", "v9", "v10", "v11", "v12", "v13", "v14", "v15", "v24", "v25", "v26", "v27", "v28", "v29", "v30", "v31", "v32", "v33", "v34", "v35", "v36", "v37", "v38", "v39", "v40", "v41", "v42", "v43", "v44", "v45", "v46", "v47", "v48", "v49", "v50", "v51", "v52", "v53", "v54", "v55", "v56", "v57", "v58", "v59", "v60", "v61", "v62", "v63", "v64", "v65", "v66", "v67", "v68", "v69", "v70", "v71", "v72", "v73", "v74", "v75", "v76", "v77", "v78", "v79", "v80", "v81", "v82", "v83", "v84", "v85", "v86", "v87", "v88", "v89", "v90", "v91", "v92", "v93", "v94", "v95", "v96", "v97", "v98", "v99", "v100", "v101", "v102", "v103", "v104", "v105", "v106", "v107", "v108", "v109", "v110", "v111", "v112", "v113", "v114", "v115", "v116", "v117", "v118", "v119", "v120", "v121", "v122", "v123", "v124", "v125", "v126", "v127", "v128", "v129", "v130", "v131", "v132", "v133", "v134", "v135", "v136", "v137", "v138", "v139", "v140", "v141", "v142", "v143", "v144", "v145", "v146", "v147", "v148", "v149", "v150", "v151", "v152", "v153", "v154", "v155", "v156", "v157", "v158", "v159", "v160", "v161", "v162", "v163", "v164", "v165", "v166", "v167", "v168", "v169", "v170", "v171", "v172", "v173", "v174", "v175", "v176", "v177", "v178", "v179", "v180", "v181", "v182", "v183", "v184", "v185", "v186", "v187", "v188", "v189", "v190", "v191", "v192", "v193", "v194", "v195", "v196", "v197", "v198", "v199", "v200", "v201", "v202", "v203", "v204", "v205", "v206", "v207", "v208", "v209", "v210", "v211", "v212", "v213", "v214", "v215", "v216", "v217", "v218", "v219", "v220", "v221", "v222", "v223", "v224", "v225", "v226", "v227", "s40", "s41", "s42", "s43", "s44", "s45", "s46", "s47", "s48", "s49", "s50", "s51", "s52", "s53", "s60", "s61", "s62", "s63", "s64", "s65", "s66", "s67", "s68", "s69", "s70", "s71", "s72", "s73", "s74", "s75", "a0", "a1", "a2", "a3", "a4", "a5", "a6", "a7", "a8", "a9", "a10", "a11", "a12", "a13", "a14", "a15", "a16", "a17", "a18", "a19", "a20", "a21", "a22", "a23", "a24", "a25", "a26", "a27", "a28", "a29", "a30", "a31", "a32", "a33", "a34", "a35", "a36", "a37", "a38", "a39", "a40", "a41", "a42", "a43", "a44", "a45", "a46", "a47", "a48", "a49", "a50", "a51", "a52", "a53", "a54", "a55", "a56", "a57", "a58", "a59", "a60", "a61", "a62", "a63", "a64", "a65", "a66", "a67", "a68", "a69", "a70", "a71", "a72", "a73", "a74", "a75", "a76", "a77", "a78", "a79", "a80", "a81", "a82", "a83", "a84", "a85", "a86", "a87", "a88", "a89", "a90", "a91", "a92", "a93", "a94", "a95", "a96", "a97", "a98", "a99", "a100", "a101", "a102", "a103", "a104", "a105", "a106", "a107", "a108", "a109", "a110", "a111", "a112", "a113", "a114", "a115", "a116", "a117", "a118", "a119", "a120", "a121", "a122", "a123", "a124", "a125", "a126", "a127"
__device__ __forceinline__ void put4(v32u& d, int i, v4u v) { d[4 * i] = v.x; d[4 * i + 1] = v.y; d[4 * i + 2] = v.z; d[4 * i + 3] = v.w; }
#define LDP_ASM \
   "buffer_load_dword v26, v113, %[rw], 0 offen sc1\n\t" "s_mov_b32 s41, 0\n\t" "buffer_load_dwordx4 a[0:3], v16, %[ra], s41 offen\n\t" \
   "s_mov_b32 s41, 131072\n\t" "buffer_load_dwordx4 a[64:67], v16, %[ra], s41 offen\n\t" "s_mov_b32 s41, 524288\n\t" \
   "buffer_load_dwordx4 a[8:11], v16, %[ra], s41 offen\n\t" "s_mov_b32 s41, 655360\n\t" "buffer_load_dwordx4 a[72:75], v16, %[ra], s41 offen\n\t" \
   "s_mov_b32 s41, 1048576\n\t" "buffer_load_dwordx4 a[16:19], v16, %[ra], s41 offen\n\t" "s_mov_b32 s41, 1179648\n\t" \
   "buffer_load_dwordx4 a[80:83], v16, %[ra], s41 offen\n\t" "s_mov_b32 s41, 1572864\n\t" "buffer_load_dwordx4 a[24:27], v16, %[ra], s41 offen\n\t" \
   "s_mov_b32 s41, 1703936\n\t" "buffer_load_dwordx4 a[88:91], v16, %[ra], s41 offen\n\t" "s_mov_b32 s41, 2097152\n\t" \
   "buffer_load_dwordx4 a[32:35], v16, %[ra], s41 offen\n\t" "s_mov_b32 s41, 2228224\n\t" "buffer_load_dwordx4 a[96:99], v16, %[ra], s41 offen\n\t" \
   "s_mov_b32 s41, 2621440\n\t" "buffer_load_dwordx4 a[40:43], v16, %[ra], s41 offen\n\t" "s_mov_b32 s41, 2752512\n\t" \
   "buffer_load_dwordx4 a[104:107], v16, %[ra], s41 offen\n\t" "s_mov_b32 s41, 3145728\n\t" "buffer_load_dwordx4 a[48:51], v16, %[ra], s41 offen\n\t" \
   "s_mov_b32 s41, 3276800\n\t" "buffer_load_dwordx4 a[112:115], v16, %[ra], s41 offen\n\t" "s_mov_b32 s41, 3670016\n\t" \
   "buffer_load_dwordx4 a[56:59], v16, %[ra], s41 offen\n\t" "s_mov_b32 s41, 3801088\n\t" "buffer_load_dwordx4 a[120:123], v16, %[ra], s41 offen\n\t" \
   "s_waitcnt vmcnt(16)\n\t" "v_add_u32 v26, 1, v26\n\t" "buffer_store_dword v26, v114, %[rw], 0 offen sc1\n\t" "ds_write_b32 v115, v26\n\t" LDSTAMP(0) \
   "s_waitcnt vmcnt(15)\n\t" LDSTAMP(1) "ds_write_b128 v20, a[0:3] offset:0\n\t" "s_mov_b32 s41, 4194304\n\t" \
   "buffer_load_dwordx4 v[192:195], v16, %[ra], s41 offen\n\t" "s_waitcnt vmcnt(15)\n\t" "ds_write_b128 v20, a[64:67] offset:4096\n\t" \
   "s_mov_b32 s41, 4325376\n\t" "buffer_load_dwordx4 v[208:211], v16, %[ra], s41 offen\n\t" "s_waitcnt vmcnt(15)\n\t" \
   "ds_write_b128 v20, a[8:11] offset:8192\n\t" "s_mov_b32 s41, 4718592\n\t" "buffer_load_dwordx4 v[200:203], v16, %[ra], s41 offen\n\t" \
   "s_waitcnt vmcnt(15)\n\t" "ds_write_b128 v20, a[72:75] offset:12288\n\t" "s_mov_b32 s41, 4849664\n\t" \
   "buffer_load_dwordx4 v[216:219], v16, %[ra], s41 offen\n\t" "s_waitcnt vmcnt(15)\n\t" "ds_write_b128 v20, a[16:19] offset:16384\n\t" \
   "s_mov_b32 s41, 0\n\t" "buffer_load_dwordx4 a[128:131], v16, %[rb], s41 offen\n\t" "s_waitcnt vmcnt(15)\n\t" \
   "ds_write_b128 v20, a[80:83] offset:20480\n\t" "s_mov_b32 s41, 131072\n\t" "buffer_load_dwordx4 a[192:195], v16, %[rb], s41 offen\n\t" \
   "s_waitcnt vmcnt(15)\n\t" "ds_write_b128 v20, a[24:27] offset:24576\n\t" "s_mov_b32 s41, 524288\n\t" \
   "buffer_load_dwordx4 a[136:139], v16, %[rb], s41 offen\n\t" "s_waitcnt vmcnt(15)\n\t" "ds_write_b128 v20, a[88:91] offset:28672\n\t" \
   "s_mov_b32 s41, 655360\n\t" "buffer_load_dwordx4 a[200:203], v16, %[rb], s41 offen\n\t" "s_waitcnt vmcnt(15)\n\t" \
   "ds_write_b128 v20, a[32:35] offset:32768\n\t" "s_mov_b32 s41, 1048576\n\t" "buffer_load_dwordx4 a[144:147], v16, %[rb], s41 offen\n\t" \
   "s_waitcnt vmcnt(15)\n\t" "ds_write_b128 v20, a[96:99] offset:36864\n\t" "s_mov_b32 s41, 1179648\n\t" \
   "buffer_load_dwordx4 a[208:211], v16, %[rb], s41 offen\n\t" "s_waitcnt vmcnt(15)\n\t" "ds_write_b128 v20, a[40:43] offset:40960\n\t" \
   "s_mov_b32 s41, 1572864\n\t" "buffer_load_dwordx4 a[152:155], v16, %[rb], s41 offen\n\t" "s_waitcnt vmcnt(15)\n\t" \
   "ds_write_b128 v20, a[104:107] offset:45056\n\t" "s_mov_b32 s41, 1703936\n\t" "buffer_load_dwordx4 a[216:219], v16, %[rb], s41 offen\n\t" \
   "s_waitcnt vmcnt(15)\n\t" "ds_write_b128 v20, a[48:51] offset:49152\n\t" "s_mov_b32 s41, 2097152\n\t" \
   "buffer_load_dwordx4 a[160:163], v16, %[rb], s41 offen\n\t" "s_waitcnt vmcnt(15)\n\t" "ds_write_b128 v20, a[112:115] offset:53248\n\t" \
   "s_mov_b32 s41, 2228224\n\t" "buffer_load_dwordx4 a[224:227], v16, %[rb], s41 offen\n\t" "s_waitcnt vmcnt(15)\n\t" \
   "ds_write_b128 v20, a[56:59] offset:57344\n\t" "s_mov_b32 s41, 2621440\n\t" "buffer_load_dwordx4 a[168:171], v16, %[rb], s41 offen\n\t" \
   "s_waitcnt vmcnt(15)\n\t" "ds_write_b128 v20, a[120:123] offset:61440\n\t" "s_mov_b32 s41, 2752512\n\t" \
   "buffer_load_dwordx4 a[232:235], v16, %[rb], s41 offen\n\t" "s_waitcnt vmcnt(15)\n\t" "ds_write_b128 v23, v[192:195] offset:0\n\t" \
   "s_mov_b32 s41, 3145728\n\t" "buffer_load_dwordx4 a[176:179], v16, %[rb], s41 offen\n\t" "s_waitcnt vmcnt(15)\n\t" \
   "ds_write_b128 v23, v[208:211] offset:4096\n\t" "s_mov_b32 s41, 3276800\n\t" "buffer_load_dwordx4 a[240:243], v16, %[rb], s41 offen\n\t" \
   "s_waitcnt vmcnt(15)\n\t" "ds_write_b128 v23, v[200:203] offset:8192\n\t" "s_mov_b32 s41, 3670016\n\t" \
   "buffer_load_dwordx4 a[184:187], v16, %[rb], s41 offen\n\t" "s_waitcnt vmcnt(15)\n\t" "ds_write_b128 v23, v[216:219] offset:12288\n\t" \
   "s_mov_b32 s41, 3801088\n\t" "buffer_load_dwordx4 a[248:251], v16, %[rb], s41 offen\n\t" "s_waitcnt lgkmcnt(0)\n\t" "s_barrier\n\t" LDSTAMP(2) \
   "s_lshl_b32 s40, %[p0], 13\n\t" "v_add_u32 v24, s40, v18\n\t" "v_add_u32 v25, s40, v19\n\t" "s_add_u32 s40, s40, 8192\n\t" \
   "s_cmp_eq_u32 s40, 81920\n\t" "s_cselect_b32 s40, 0, s40\n\t" "ds_read_b64_tr_b16 a[0:1], v24 offset:0\n\t" \
   "ds_read_b64_tr_b16 a[2:3], v24 offset:4096\n\t" "ds_read_b64_tr_b16 a[64:65], v25 offset:0\n\t" "ds_read_b64_tr_b16 a[66:67], v25 offset:4096\n\t" \
   "v_add_u32 v24, s40, v18\n\t" "v_add_u32 v25, s40, v19\n\t" "s_add_u32 s40, s40, 8192\n\t" "s_cmp_eq_u32 s40, 81920\n\t" \
   "s_cselect_b32 s40, 0, s40\n\t" "ds_read_b64_tr_b16 a[8:9], v24 offset:0\n\t" "ds_read_b64_tr_b16 a[10:11], v24 offset:4096\n\t" \
   "ds_read_b64_tr_b16 a[72:73], v25 offset:0\n\t" "ds_read_b64_tr_b16 a[74:75], v25 offset:4096\n\t" "v_add_u32 v24, s40, v18\n\t" \
   "v_add_u32 v25, s40, v19\n\t" "s_add_u32 s40, s40, 8192\n\t" "s_cmp_eq_u32 s40, 81920\n\t" "s_cselect_b32 s40, 0, s40\n\t" \
   "ds_read_b64_tr_b16 a[16:17], v24 offset:0\n\t" "ds_read_b64_tr_b16 a[18:19], v24 offset:4096\n\t" "ds_read_b64_tr_b16 a[80:81], v25 offset:0\n\t" \
   "ds_read_b64_tr_b16 a[82:83], v25 offset:4096\n\t" "v_add_u32 v24, s40, v18\n\t" "v_add_u32 v25, s40, v19\n\t" "s_add_u32 s40, s40, 8192\n\t" \
   "s_cmp_eq_u32 s40, 81920\n\t" "s_cselect_b32 s40, 0, s40\n\t" "ds_read_b64_tr_b16 a[24:25], v24 offset:0\n\t" \
   "ds_read_b64_tr_b16 a[26:27], v24 offset:4096\n\t" "ds_read_b64_tr_b16 a[88:89], v25 offset:0\n\t" \
   "ds_read_b64_tr_b16 a[90:91], v25 offset:4096\n\t" "v_add_u32 v24, s40, v18\n\t" "v_add_u32 v25, s40, v19\n\t" "s_add_u32 s40, s40, 8192\n\t" \
   "s_cmp_eq_u32 s40, 81920\n\t" "s_cselect_b32 s40, 0, s40\n\t" "ds_read_b64_tr_b16 a[32:33], v24 offset:0\n\t" \
   "ds_read_b64_tr_b16 a[34:35], v24 offset:4096\n\t" "ds_read_b64_tr_b16 a[96:97], v25 offset:0\n\t" \
   "ds_read_b64_tr_b16 a[98:99], v25 offset:4096\n\t" "v_add_u32 v24, s40, v18\n\t" "v_add_u32 v25, s40, v19\n\t" "s_add_u32 s40, s40, 8192\n\t" \
   "s_cmp_eq_u32 s40, 81920\n\t" "s_cselect_b32 s40, 0, s40\n\t" "ds_read_b64_tr_b16 a[40:41], v24 offset:0\n\t" \
   "ds_read_b64_tr_b16 a[42:43], v24 offset:4096\n\t" "ds_read_b64_tr_b16 a[104:105], v25 offset:0\n\t" \
   "ds_read_b64_tr_b16 a[106:107], v25 offset:4096\n\t" "v_add_u32 v24, s40, v18\n\t" "v_add_u32 v25, s40, v19\n\t" "s_add_u32 s40, s40, 8192\n\t" \
   "s_cmp_eq_u32 s40, 81920\n\t" "s_cselect_b32 s40, 0, s40\n\t" "ds_read_b64_tr_b16 a[48:49], v24 offset:0\n\t" \
   "ds_read_b64_tr_b16 a[50:51], v24 offset:4096\n\t" "ds_read_b64_tr_b16 a[112:113], v25 offset:0\n\t" \
   "ds_read_b64_tr_b16 a[114:115], v25 offset:4096\n\t" "v_add_u32 v24, s40, v18\n\t" "v_add_u32 v25, s40, v19\n\t" "s_add_u32 s40, s40, 8192\n\t" \
   "s_cmp_eq_u32 s40, 81920\n\t" "s_cselect_b32 s40, 0, s40\n\t" "ds_read_b64_tr_b16 a[56:57], v24 offset:0\n\t" \
   "ds_read_b64_tr_b16 a[58:59], v24 offset:4096\n\t" "ds_read_b64_tr_b16 a[120:121], v25 offset:0\n\t" \
   "ds_read_b64_tr_b16 a[122:123], v25 offset:4096\n\t" "v_add_u32 v24, s40, v18\n\t" "v_add_u32 v25, s40, v19\n\t" "s_add_u32 s40, s40, 8192\n\t" \
   "s_cmp_eq_u32 s40, 81920\n\t" "s_cselect_b32 s40, 0, s40\n\t" "ds_read_b64_tr_b16 v[192:193], v24 offset:0\n\t" \
   "ds_read_b64_tr_b16 v[194:195], v24 offset:4096\n\t" "ds_read_b64_tr_b16 v[208:209], v25 offset:0\n\t" \
   "ds_read_b64_tr_b16 v[210:211], v25 offset:4096\n\t" "v_add_u32 v24, s40, v18\n\t" "v_add_u32 v25, s40, v19\n\t" "s_add_u32 s40, s40, 8192\n\t" \
   "s_cmp_eq_u32 s40, 81920\n\t" "s_cselect_b32 s40, 0, s40\n\t" "ds_read_b64_tr_b16 v[200:201], v24 offset:0\n\t" \
   "ds_read_b64_tr_b16 v[202:203], v24 offset:4096\n\t" "ds_read_b64_tr_b16 v[216:217], v25 offset:0\n\t" \
   "ds_read_b64_tr_b16 v[218:219], v25 offset:4096\n\t" "s_waitcnt vmcnt(15)\n\t" "s_waitcnt lgkmcnt(0)\n\t" "s_barrier\n\t" \
   "ds_write_b128 v20, a[128:131] offset:0\n\t" "s_mov_b32 s41, 4194304\n\t" "buffer_load_dwordx4 v[224:227], v16, %[rb], s41 offen\n\t" \
   "s_waitcnt vmcnt(15)\n\t" "ds_write_b128 v20, a[192:195] offset:4096\n\t" "s_mov_b32 s41, 4325376\n\t" \
   "buffer_load_dwordx4 v[240:243], v16, %[rb], s41 offen\n\t" "s_waitcnt vmcnt(15)\n\t" "ds_write_b128 v20, a[136:139] offset:8192\n\t" \
   "s_mov_b32 s41, 4718592\n\t" "buffer_load_dwordx4 v[232:235], v16, %[rb], s41 offen\n\t" "s_waitcnt vmcnt(15)\n\t" \
   "ds_write_b128 v20, a[200:203] offset:12288\n\t" "s_mov_b32 s41, 4849664\n\t" "buffer_load_dwordx4 v[248:251], v16, %[rb], s41 offen\n\t" \
   "s_waitcnt vmcnt(15)\n\t" "ds_write_b128 v20, a[144:147] offset:16384\n\t" "s_mov_b32 s41, 262144\n\t" \
   "buffer_load_dwordx4 a[4:7], v16, %[ra], s41 offen\n\t" "s_waitcnt vmcnt(15)\n\t" "ds_write_b128 v20, a[208:211] offset:20480\n\t" \
   "s_mov_b32 s41, 393216\n\t" "buffer_load_dwordx4 a[68:71], v16, %[ra], s41 offen\n\t" "s_waitcnt vmcnt(15)\n\t" \
   "ds_write_b128 v20, a[152:155] offset:24576\n\t" "s_mov_b32 s41, 786432\n\t" "buffer_load_dwordx4 a[12:15], v16, %[ra], s41 offen\n\t" \
   "s_waitcnt vmcnt(15)\n\t" "ds_write_b128 v20, a[216:219] offset:28672\n\t" "s_mov_b32 s41, 917504\n\t" \
   "buffer_load_dwordx4 a[76:79], v16, %[ra], s41 offen\n\t" "s_waitcnt vmcnt(15)\n\t" "ds_write_b128 v20, a[160:163] offset:32768\n\t" \
   "s_mov_b32 s41, 1310720\n\t" "buffer_load_dwordx4 a[20:23], v16, %[ra], s41 offen\n\t" "s_waitcnt vmcnt(15)\n\t" \
   "ds_write_b128 v20, a[224:227] offset:36864\n\t" "s_mov_b32 s41, 1441792\n\t" "buffer_load_dwordx4 a[84:87], v16, %[ra], s41 offen\n\t" \
   "s_waitcnt vmcnt(15)\n\t" "ds_write_b128 v20, a[168:171] offset:40960\n\t" "s_mov_b32 s41, 1835008\n\t" \
   "buffer_load_dwordx4 a[28:31], v16, %[ra], s41 offen\n\t" "s_waitcnt vmcnt(15)\n\t" "ds_write_b128 v20, a[232:235] offset:45056\n\t" \
   "s_mov_b32 s41, 1966080\n\t" "buffer_load_dwordx4 a[92:95], v16, %[ra], s41 offen\n\t" "s_waitcnt vmcnt(15)\n\t" \
   "ds_write_b128 v20, a[176:179] offset:49152\n\t" "s_mov_b32 s41, 2359296\n\t" "buffer_load_dwordx4 a[36:39], v16, %[ra], s41 offen\n\t" \
   "s_waitcnt vmcnt(15)\n\t" "ds_write_b128 v20, a[240:243] offset:53248\n\t" "s_mov_b32 s41, 2490368\n\t" \
   "buffer_load_dwordx4 a[100:103], v16, %[ra], s41 offen\n\t" "s_waitcnt vmcnt(15)\n\t" "ds_write_b128 v20, a[184:187] offset:57344\n\t" \
   "s_mov_b32 s41, 2883584\n\t" "buffer_load_dwordx4 a[44:47], v16, %[ra], s41 offen\n\t" "s_waitcnt vmcnt(15)\n\t" \
   "ds_write_b128 v20, a[248:251] offset:61440\n\t" "s_mov_b32 s41, 3014656\n\t" "buffer_load_dwordx4 a[108:111], v16, %[ra], s41 offen\n\t" \
   "s_waitcnt vmcnt(15)\n\t" "ds_write_b128 v23, v[224:227] offset:0\n\t" "s_mov_b32 s41, 3407872\n\t" \
   "buffer_load_dwordx4 a[52:55], v16, %[ra], s41 offen\n\t" "s_waitcnt vmcnt(15)\n\t" "ds_write_b128 v23, v[240:243] offset:4096\n\t" \
   "s_mov_b32 s41, 3538944\n\t" "buffer_load_dwordx4 a[116:119], v16, %[ra], s41 offen\n\t" "s_waitcnt vmcnt(15)\n\t" \
   "ds_write_b128 v23, v[232:235] offset:8192\n\t" "s_mov_b32 s41, 3932160\n\t" "buffer_load_dwordx4 a[60:63], v16, %[ra], s41 offen\n\t" \
   "s_waitcnt vmcnt(15)\n\t" "ds_write_b128 v23, v[248:251] offset:12288\n\t" "s_mov_b32 s41, 4063232\n\t" \
   "buffer_load_dwordx4 a[124:127], v16, %[ra], s41 offen\n\t" "s_waitcnt lgkmcnt(0)\n\t" "s_barrier\n\t" LDSTAMP(3) "s_lshl_b32 s40, %[p0], 13\n\t" \
   "v_add_u32 v24, s40, v18\n\t" "v_add_u32 v25, s40, v19\n\t" "s_add_u32 s40, s40, 8192\n\t" "s_cmp_eq_u32 s40, 81920\n\t" \
   "s_cselect_b32 s40, 0, s40\n\t" "ds_read_b64_tr_b16 a[128:129], v24 offset:0\n\t" "ds_read_b64_tr_b16 a[130:131], v24 offset:4096\n\t" \
   "ds_read_b64_tr_b16 a[192:193], v25 offset:0\n\t" "ds_read_b64_tr_b16 a[194:195], v25 offset:4096\n\t" "v_add_u32 v24, s40, v18\n\t" \
   "v_add_u32 v25, s40, v19\n\t" "s_add_u32 s40, s40, 8192\n\t" "s_cmp_eq_u32 s40, 81920\n\t" "s_cselect_b32 s40, 0, s40\n\t" \
   "ds_read_b64_tr_b16 a[136:137], v24 offset:0\n\t" "ds_read_b64_tr_b16 a[138:139], v24 offset:4096\n\t" \
   "ds_read_b64_tr_b16 a[200:201], v25 offset:0\n\t" "ds_read_b64_tr_b16 a[202:203], v25 offset:4096\n\t" "v_add_u32 v24, s40, v18\n\t" \
   "v_add_u32 v25, s40, v19\n\t" "s_add_u32 s40, s40, 8192\n\t" "s_cmp_eq_u32 s40, 81920\n\t" "s_cselect_b32 s40, 0, s40\n\t" \
   "ds_read_b64_tr_b16 a[144:145], v24 offset:0\n\t" "ds_read_b64_tr_b16 a[146:147], v24 offset:4096\n\t" \
   "ds_read_b64_tr_b16 a[208:209], v25 offset:0\n\t" "ds_read_b64_tr_b16 a[210:211], v25 offset:4096\n\t" "v_add_u32 v24, s40, v18\n\t" \
   "v_add_u32 v25, s40, v19\n\t" "s_add_u32 s40, s40, 8192\n\t" "s_cmp_eq_u32 s40, 81920\n\t" "s_cselect_b32 s40, 0, s40\n\t" \
   "ds_read_b64_tr_b16 a[152:153], v24 offset:0\n\t" "ds_read_b64_tr_b16 a[154:155], v24 offset:4096\n\t" \
   "ds_read_b64_tr_b16 a[216:217], v25 offset:0\n\t" "ds_read_b64_tr_b16 a[218:219], v25 offset:4096\n\t" "v_add_u32 v24, s40, v18\n\t" \
   "v_add_u32 v25, s40, v19\n\t" "s_add_u32 s40, s40, 8192\n\t" "s_cmp_eq_u32 s40, 81920\n\t" "s_cselect_b32 s40, 0, s40\n\t" \
   "ds_read_b64_tr_b16 a[160:161], v24 offset:0\n\t" "ds_read_b64_tr_b16 a[162:163], v24 offset:4096\n\t" \
   "ds_read_b64_tr_b16 a[224:225], v25 offset:0\n\t" "ds_read_b64_tr_b16 a[226:227], v25 offset:4096\n\t" "v_add_u32 v24, s40, v18\n\t" \
   "v_add_u32 v25, s40, v19\n\t" "s_add_u32 s40, s40, 8192\n\t" "s_cmp_eq_u32 s40, 81920\n\t" "s_cselect_b32 s40, 0, s40\n\t" \
   "ds_read_b64_tr_b16 a[168:169], v24 offset:0\n\t" "ds_read_b64_tr_b16 a[170:171], v24 offset:4096\n\t" \
   "ds_read_b64_tr_b16 a[232:233], v25 offset:0\n\t" "ds_read_b64_tr_b16 a[234:235], v25 offset:4096\n\t" "v_add_u32 v24, s40, v18\n\t" \
   "v_add_u32 v25, s40, v19\n\t" "s_add_u32 s40, s40, 8192\n\t" "s_cmp_eq_u32 s40, 81920\n\t" "s_cselect_b32 s40, 0, s40\n\t" \
   "ds_read_b64_tr_b16 a[176:177], v24 offset:0\n\t" "ds_read_b64_tr_b16 a[178:179], v24 offset:4096\n\t" \
   "ds_read_b64_tr_b16 a[240:241], v25 offset:0\n\t" "ds_read_b64_tr_b16 a[242:243], v25 offset:4096\n\t" "v_add_u32 v24, s40, v18\n\t" \
   "v_add_u32 v25, s40, v19\n\t" "s_add_u32 s40, s40, 8192\n\t" "s_cmp_eq_u32 s40, 81920\n\t" "s_cselect_b32 s40, 0, s40\n\t" \
   "ds_read_b64_tr_b16 a[184:185], v24 offset:0\n\t" "ds_read_b64_tr_b16 a[186:187], v24 offset:4096\n\t" \
   "ds_read_b64_tr_b16 a[248:249], v25 offset:0\n\t" "ds_read_b64_tr_b16 a[250:251], v25 offset:4096\n\t" "v_add_u32 v24, s40, v18\n\t" \
   "v_add_u32 v25, s40, v19\n\t" "s_add_u32 s40, s40, 8192\n\t" "s_cmp_eq_u32 s40, 81920\n\t" "s_cselect_b32 s40, 0, s40\n\t" \
   "ds_read_b64_tr_b16 v[224:225], v24 offset:0\n\t" "ds_read_b64_tr_b16 v[226:227], v24 offset:4096\n\t" \
   "ds_read_b64_tr_b16 v[240:241], v25 offset:0\n\t" "ds_read_b64_tr_b16 v[242:243], v25 offset:4096\n\t" "v_add_u32 v24, s40, v18\n\t" \
   "v_add_u32 v25, s40, v19\n\t" "s_add_u32 s40, s40, 8192\n\t" "s_cmp_eq_u32 s40, 81920\n\t" "s_cselect_b32 s40, 0, s40\n\t" \
   "ds_read_b64_tr_b16 v[232:233], v24 offset:0\n\t" "ds_read_b64_tr_b16 v[234:235], v24 offset:4096\n\t" \
   "ds_read_b64_tr_b16 v[248:249], v25 offset:0\n\t" "ds_read_b64_tr_b16 v[250:251], v25 offset:4096\n\t" "s_waitcnt vmcnt(15)\n\t" \
   "s_waitcnt lgkmcnt(0)\n\t" "s_barrier\n\t" "ds_write_b128 v20, a[4:7] offset:0\n\t" "s_mov_b32 s41, 4456448\n\t" \
   "buffer_load_dwordx4 v[196:199], v16, %[ra], s41 offen\n\t" "s_waitcnt vmcnt(15)\n\t" "ds_write_b128 v20, a[68:71] offset:4096\n\t" \
   "s_mov_b32 s41, 4587520\n\t" "buffer_load_dwordx4 v[212:215], v16, %[ra], s41 offen\n\t" "s_waitcnt vmcnt(15)\n\t" \
   "ds_write_b128 v20, a[12:15] offset:8192\n\t" "s_mov_b32 s41, 4980736\n\t" "buffer_load_dwordx4 v[204:207], v16, %[ra], s41 offen\n\t" \
   "s_waitcnt vmcnt(15)\n\t" "ds_write_b128 v20, a[76:79] offset:12288\n\t" "s_mov_b32 s41, 5111808\n\t" \
   "buffer_load_dwordx4 v[220:223], v16, %[ra], s41 offen\n\t" "s_waitcnt vmcnt(15)\n\t" "ds_write_b128 v20, a[20:23] offset:16384\n\t" \
   "s_mov_b32 s41, 262144\n\t" "buffer_load_dwordx4 a[132:135], v16, %[rb], s41 offen\n\t" "s_waitcnt vmcnt(15)\n\t" \
   "ds_write_b128 v20, a[84:87] offset:20480\n\t" "s_mov_b32 s41, 393216\n\t" "buffer_load_dwordx4 a[196:199], v16, %[rb], s41 offen\n\t" \
   "s_waitcnt vmcnt(15)\n\t" "ds_write_b128 v20, a[28:31] offset:24576\n\t" "s_mov_b32 s41, 786432\n\t" \
   "buffer_load_dwordx4 a[140:143], v16, %[rb], s41 offen\n\t" "s_waitcnt vmcnt(15)\n\t" "ds_write_b128 v20, a[92:95] offset:28672\n\t" \
   "s_mov_b32 s41, 917504\n\t" "buffer_load_dwordx4 a[204:207], v16, %[rb], s41 offen\n\t" "s_waitcnt vmcnt(15)\n\t" \
   "ds_write_b128 v20, a[36:39] offset:32768\n\t" "s_mov_b32 s41, 1310720\n\t" "buffer_load_dwordx4 a[148:151], v16, %[rb], s41 offen\n\t" \
   "s_waitcnt vmcnt(15)\n\t" "ds_write_b128 v20, a[100:103] offset:36864\n\t" "s_mov_b32 s41, 1441792\n\t" \
   "buffer_load_dwordx4 a[212:215], v16, %[rb], s41 offen\n\t" "s_waitcnt vmcnt(15)\n\t" "ds_write_b128 v20, a[44:47] offset:40960\n\t" \
   "s_mov_b32 s41, 1835008\n\t" "buffer_load_dwordx4 a[156:159], v16, %[rb], s41 offen\n\t" "s_waitcnt vmcnt(15)\n\t" \
   "ds_write_b128 v20, a[108:111] offset:45056\n\t" "s_mov_b32 s41, 1966080\n\t" "buffer_load_dwordx4 a[220:223], v16, %[rb], s41 offen\n\t" \
   "s_waitcnt vmcnt(15)\n\t" "ds_write_b128 v20, a[52:55] offset:49152\n\t" "s_mov_b32 s41, 2359296\n\t" \
   "buffer_load_dwordx4 a[164:167], v16, %[rb], s41 offen\n\t" "s_waitcnt vmcnt(15)\n\t" "ds_write_b128 v20, a[116:119] offset:53248\n\t" \
   "s_mov_b32 s41, 2490368\n\t" "buffer_load_dwordx4 a[228:231], v16, %[rb], s41 offen\n\t" "s_waitcnt vmcnt(15)\n\t" \
   "ds_write_b128 v20, a[60:63] offset:57344\n\t" "s_mov_b32 s41, 2883584\n\t" "buffer_load_dwordx4 a[172:175], v16, %[rb], s41 offen\n\t" \
   "s_waitcnt vmcnt(15)\n\t" "ds_write_b128 v20, a[124:127] offset:61440\n\t" "s_mov_b32 s41, 3014656\n\t" \
   "buffer_load_dwordx4 a[236:239], v16, %[rb], s41 offen\n\t" "s_waitcnt vmcnt(15)\n\t" "ds_write_b128 v23, v[196:199] offset:0\n\t" \
   "s_mov_b32 s41, 3407872\n\t" "buffer_load_dwordx4 a[180:183], v16, %[rb], s41 offen\n\t" "s_waitcnt vmcnt(15)\n\t" \
   "ds_write_b128 v23, v[212:215] offset:4096\n\t" "s_mov_b32 s41, 3538944\n\t" "buffer_load_dwordx4 a[244:247], v16, %[rb], s41 offen\n\t" \
   "s_waitcnt vmcnt(15)\n\t" "ds_write_b128 v23, v[204:207] offset:8192\n\t" "s_mov_b32 s41, 3932160\n\t" \
   "buffer_load_dwordx4 a[188:191], v16, %[rb], s41 offen\n\t" "s_waitcnt vmcnt(15)\n\t" "ds_write_b128 v23, v[220:223] offset:12288\n\t" \
   "s_mov_b32 s41, 4063232\n\t" "buffer_load_dwordx4 a[252:255], v16, %[rb], s41 offen\n\t" "s_waitcnt lgkmcnt(0)\n\t" "s_barrier\n\t" LDSTAMP(4) \
   "s_lshl_b32 s40, %[p0], 13\n\t" "v_add_u32 v24, s40, v18\n\t" "v_add_u32 v25, s40, v19\n\t" "s_add_u32 s40, s40, 8192\n\t" \
   "s_cmp_eq_u32 s40, 81920\n\t" "s_cselect_b32 s40, 0, s40\n\t" "ds_read_b64_tr_b16 a[4:5], v24 offset:0\n\t" \
   "ds_read_b64_tr_b16 a[6:7], v24 offset:4096\n\t" "ds_read_b64_tr_b16 a[68:69], v25 offset:0\n\t" "ds_read_b64_tr_b16 a[70:71], v25 offset:4096\n\t" \
   "v_add_u32 v24, s40, v18\n\t" "v_add_u32 v25, s40, v19\n\t" "s_add_u32 s40, s40, 8192\n\t" "s_cmp_eq_u32 s40, 81920\n\t" \
   "s_cselect_b32 s40, 0, s40\n\t" "ds_read_b64_tr_b16 a[12:13], v24 offset:0\n\t" "ds_read_b64_tr_b16 a[14:15], v24 offset:4096\n\t" \
   "ds_read_b64_tr_b16 a[76:77], v25 offset:0\n\t" "ds_read_b64_tr_b16 a[78:79], v25 offset:4096\n\t" "v_add_u32 v24, s40, v18\n\t" \
   "v_add_u32 v25, s40, v19\n\t" "s_add_u32 s40, s40, 8192\n\t" "s_cmp_eq_u32 s40, 81920\n\t" "s_cselect_b32 s40, 0, s40\n\t" \
   "ds_read_b64_tr_b16 a[20:21], v24 offset:0\n\t" "ds_read_b64_tr_b16 a[22:23], v24 offset:4096\n\t" "ds_read_b64_tr_b16 a[84:85], v25 offset:0\n\t" \
   "ds_read_b64_tr_b16 a[86:87], v25 offset:4096\n\t" "v_add_u32 v24, s40, v18\n\t" "v_add_u32 v25, s40, v19\n\t" "s_add_u32 s40, s40, 8192\n\t" \
   "s_cmp_eq_u32 s40, 81920\n\t" "s_cselect_b32 s40, 0, s40\n\t" "ds_read_b64_tr_b16 a[28:29], v24 offset:0\n\t" \
   "ds_read_b64_tr_b16 a[30:31], v24 offset:4096\n\t" "ds_read_b64_tr_b16 a[92:93], v25 offset:0\n\t" \
   "ds_read_b64_tr_b16 a[94:95], v25 offset:4096\n\t" "v_add_u32 v24, s40, v18\n\t" "v_add_u32 v25, s40, v19\n\t" "s_add_u32 s40, s40, 8192\n\t" \
   "s_cmp_eq_u32 s40, 81920\n\t" "s_cselect_b32 s40, 0, s40\n\t" "ds_read_b64_tr_b16 a[36:37], v24 offset:0\n\t" \
   "ds_read_b64_tr_b16 a[38:39], v24 offset:4096\n\t" "ds_read_b64_tr_b16 a[100:101], v25 offset:0\n\t" \
   "ds_read_b64_tr_b16 a[102:103], v25 offset:4096\n\t" "v_add_u32 v24, s40, v18\n\t" "v_add_u32 v25, s40, v19\n\t" "s_add_u32 s40, s40, 8192\n\t" \
   "s_cmp_eq_u32 s40, 81920\n\t" "s_cselect_b32 s40, 0, s40\n\t" "ds_read_b64_tr_b16 a[44:45], v24 offset:0\n\t" \
   "ds_read_b64_tr_b16 a[46:47], v24 offset:4096\n\t" "ds_read_b64_tr_b16 a[108:109], v25 offset:0\n\t" \
   "ds_read_b64_tr_b16 a[110:111], v25 offset:4096\n\t" "v_add_u32 v24, s40, v18\n\t" "v_add_u32 v25, s40, v19\n\t" "s_add_u32 s40, s40, 8192\n\t" \
   "s_cmp_eq_u32 s40, 81920\n\t" "s_cselect_b32 s40, 0, s40\n\t" "ds_read_b64_tr_b16 a[52:53], v24 offset:0\n\t" \
   "ds_read_b64_tr_b16 a[54:55], v24 offset:4096\n\t" "ds_read_b64_tr_b16 a[116:117], v25 offset:0\n\t" \
   "ds_read_b64_tr_b16 a[118:119], v25 offset:4096\n\t" "v_add_u32 v24, s40, v18\n\t" "v_add_u32 v25, s40, v19\n\t" "s_add_u32 s40, s40, 8192\n\t" \
   "s_cmp_eq_u32 s40, 81920\n\t" "s_cselect_b32 s40, 0, s40\n\t" "ds_read_b64_tr_b16 a[60:61], v24 offset:0\n\t" \
   "ds_read_b64_tr_b16 a[62:63], v24 offset:4096\n\t" "ds_read_b64_tr_b16 a[124:125], v25 offset:0\n\t" \
   "ds_read_b64_tr_b16 a[126:127], v25 offset:4096\n\t" "v_add_u32 v24, s40, v18\n\t" "v_add_u32 v25, s40, v19\n\t" "s_add_u32 s40, s40, 8192\n\t" \
   "s_cmp_eq_u32 s40, 81920\n\t" "s_cselect_b32 s40, 0, s40\n\t" "ds_read_b64_tr_b16 v[196:197], v24 offset:0\n\t" \
   "ds_read_b64_tr_b16 v[198:199], v24 offset:4096\n\t" "ds_read_b64_tr_b16 v[212:213], v25 offset:0\n\t" \
   "ds_read_b64_tr_b16 v[214:215], v25 offset:4096\n\t" "v_add_u32 v24, s40, v18\n\t" "v_add_u32 v25, s40, v19\n\t" "s_add_u32 s40, s40, 8192\n\t" \
   "s_cmp_eq_u32 s40, 81920\n\t" "s_cselect_b32 s40, 0, s40\n\t" "ds_read_b64_tr_b16 v[204:205], v24 offset:0\n\t" \
   "ds_read_b64_tr_b16 v[206:207], v24 offset:4096\n\t" "ds_read_b64_tr_b16 v[220:221], v25 offset:0\n\t" \
   "ds_read_b64_tr_b16 v[222:223], v25 offset:4096\n\t" "s_waitcnt vmcnt(15)\n\t" "s_waitcnt lgkmcnt(0)\n\t" "s_barrier\n\t" \
   "ds_write_b128 v20, a[132:135] offset:0\n\t" "s_mov_b32 s41, 4456448\n\t" "buffer_load_dwordx4 v[228:231], v16, %[rb], s41 offen\n\t" \
   "s_waitcnt vmcnt(15)\n\t" "ds_write_b128 v20, a[196:199] offset:4096\n\t" "s_mov_b32 s41, 4587520\n\t" \
   "buffer_load_dwordx4 v[244:247], v16, %[rb], s41 offen\n\t" "s_waitcnt vmcnt(15)\n\t" "ds_write_b128 v20, a[140:143] offset:8192\n\t" \
   "s_mov_b32 s41, 4980736\n\t" "buffer_load_dwordx4 v[236:239], v16, %[rb], s41 offen\n\t" "s_waitcnt vmcnt(15)\n\t" \
   "ds_write_b128 v20, a[204:207] offset:12288\n\t" "s_mov_b32 s41, 5111808\n\t" "buffer_load_dwordx4 v[252:255], v16, %[rb], s41 offen\n\t" \
   "s_waitcnt vmcnt(15)\n\t" "ds_write_b128 v20, a[148:151] offset:16384\n\t" "s_waitcnt vmcnt(14)\n\t" \
   "ds_write_b128 v20, a[212:215] offset:20480\n\t" "s_waitcnt vmcnt(13)\n\t" "ds_write_b128 v20, a[156:159] offset:24576\n\t" \
   "s_waitcnt vmcnt(12)\n\t" "ds_write_b128 v20, a[220:223] offset:28672\n\t" "s_waitcnt vmcnt(11)\n\t" \
   "ds_write_b128 v20, a[164:167] offset:32768\n\t" "s_waitcnt vmcnt(10)\n\t" "ds_write_b128 v20, a[228:231] offset:36864\n\t" \
   "buffer_load_dwordx4 v[28:31], v112, %[rw], 0 offen sc1\n\t" "s_waitcnt vmcnt(10)\n\t" "ds_write_b128 v20, a[172:175] offset:40960\n\t" \
   "s_waitcnt vmcnt(9)\n\t" "ds_write_b128 v20, a[236:239] offset:45056\n\t" "s_waitcnt vmcnt(8)\n\t" "ds_write_b128 v20, a[180:183] offset:49152\n\t" \
   "s_waitcnt vmcnt(7)\n\t" "ds_write_b128 v20, a[244:247] offset:53248\n\t" "s_waitcnt vmcnt(6)\n\t" "ds_write_b128 v20, a[188:191] offset:57344\n\t" \
   "s_waitcnt vmcnt(5)\n\t" "ds_write_b128 v20, a[252:255] offset:61440\n\t" "s_waitcnt vmcnt(4)\n\t" "ds_write_b128 v23, v[228:231] offset:0\n\t" \
   "s_waitcnt vmcnt(3)\n\t" "ds_write_b128 v23, v[244:247] offset:4096\n\t" "s_waitcnt vmcnt(2)\n\t" "ds_write_b128 v23, v[236:239] offset:8192\n\t" \
   "s_waitcnt vmcnt(1)\n\t" "ds_write_b128 v23, v[252:255] offset:12288\n\t" "s_waitcnt lgkmcnt(0)\n\t" "s_barrier\n\t" LDSTAMP(5) \
   "s_lshl_b32 s40, %[p0], 13\n\t" "v_add_u32 v24, s40, v18\n\t" "v_add_u32 v25, s40, v19\n\t" "s_add_u32 s40, s40, 8192\n\t" \
   "s_cmp_eq_u32 s40, 81920\n\t" "s_cselect_b32 s40, 0, s40\n\t" "ds_read_b64_tr_b16 a[132:133], v24 offset:0\n\t" \
   "ds_read_b64_tr_b16 a[134:135], v24 offset:4096\n\t" "ds_read_b64_tr_b16 a[196:197], v25 offset:0\n\t" \
   "ds_read_b64_tr_b16 a[198:199], v25 offset:4096\n\t" "v_add_u32 v24, s40, v18\n\t" "v_add_u32 v25, s40, v19\n\t" "s_add_u32 s40, s40, 8192\n\t" \
   "s_cmp_eq_u32 s40, 81920\n\t" "s_cselect_b32 s40, 0, s40\n\t" "ds_read_b64_tr_b16 a[140:141], v24 offset:0\n\t" \
   "ds_read_b64_tr_b16 a[142:143], v24 offset:4096\n\t" "ds_read_b64_tr_b16 a[204:205], v25 offset:0\n\t" \
   "ds_read_b64_tr_b16 a[206:207], v25 offset:4096\n\t" "v_add_u32 v24, s40, v18\n\t" "v_add_u32 v25, s40, v19\n\t" "s_add_u32 s40, s40, 8192\n\t" \
   "s_cmp_eq_u32 s40, 81920\n\t" "s_cselect_b32 s40, 0, s40\n\t" "ds_read_b64_tr_b16 a[148:149], v24 offset:0\n\t" \
   "ds_read_b64_tr_b16 a[150:151], v24 offset:4096\n\t" "ds_read_b64_tr_b16 a[212:213], v25 offset:0\n\t" \
   "ds_read_b64_tr_b16 a[214:215], v25 offset:4096\n\t" "v_add_u32 v24, s40, v18\n\t" "v_add_u32 v25, s40, v19\n\t" "s_add_u32 s40, s40, 8192\n\t" \
   "s_cmp_eq_u32 s40, 81920\n\t" "s_cselect_b32 s40, 0, s40\n\t" "ds_read_b64_tr_b16 a[156:157], v24 offset:0\n\t" \
   "ds_read_b64_tr_b16 a[158:159], v24 offset:4096\n\t" "ds_read_b64_tr_b16 a[220:221], v25 offset:0\n\t" \
   "ds_read_b64_tr_b16 a[222:223], v25 offset:4096\n\t" "v_add_u32 v24, s40, v18\n\t" "v_add_u32 v25, s40, v19\n\t" "s_add_u32 s40, s40, 8192\n\t" \
   "s_cmp_eq_u32 s40, 81920\n\t" "s_cselect_b32 s40, 0, s40\n\t" "ds_read_b64_tr_b16 a[164:165], v24 offset:0\n\t" \
   "ds_read_b64_tr_b16 a[166:167], v24 offset:4096\n\t" "ds_read_b64_tr_b16 a[228:229], v25 offset:0\n\t" \
   "ds_read_b64_tr_b16 a[230:231], v25 offset:4096\n\t" "v_add_u32 v24, s40, v18\n\t" "v_add_u32 v25, s40, v19\n\t" "s_add_u32 s40, s40, 8192\n\t" \
   "s_cmp_eq_u32 s40, 81920\n\t" "s_cselect_b32 s40, 0, s40\n\t" "ds_read_b64_tr_b16 a[172:173], v24 offset:0\n\t" \
   "ds_read_b64_tr_b16 a[174:175], v24 offset:4096\n\t" "ds_read_b64_tr_b16 a[236:237], v25 offset:0\n\t" \
   "ds_read_b64_tr_b16 a[238:239], v25 offset:4096\n\t" "v_add_u32 v24, s40, v18\n\t" "v_add_u32 v25, s40, v19\n\t" "s_add_u32 s40, s40, 8192\n\t" \
   "s_cmp_eq_u32 s40, 81920\n\t" "s_cselect_b32 s40, 0, s40\n\t" "ds_read_b64_tr_b16 a[180:181], v24 offset:0\n\t" \
   "ds_read_b64_tr_b16 a[182:183], v24 offset:4096\n\t" "ds_read_b64_tr_b16 a[244:245], v25 offset:0\n\t" \
   "ds_read_b64_tr_b16 a[246:247], v25 offset:4096\n\t" "v_add_u32 v24, s40, v18\n\t" "v_add_u32 v25, s40, v19\n\t" "s_add_u32 s40, s40, 8192\n\t" \
   "s_cmp_eq_u32 s40, 81920\n\t" "s_cselect_b32 s40, 0, s40\n\t" "ds_read_b64_tr_b16 a[188:189], v24 offset:0\n\t" \
   "ds_read_b64_tr_b16 a[190:191], v24 offset:4096\n\t" "ds_read_b64_tr_b16 a[252:253], v25 offset:0\n\t" \
   "ds_read_b64_tr_b16 a[254:255], v25 offset:4096\n\t" "v_add_u32 v24, s40, v18\n\t" "v_add_u32 v25, s40, v19\n\t" "s_add_u32 s40, s40, 8192\n\t" \
   "s_cmp_eq_u32 s40, 81920\n\t" "s_cselect_b32 s40, 0, s40\n\t" "ds_read_b64_tr_b16 v[228:229], v24 offset:0\n\t" \
   "ds_read_b64_tr_b16 v[230:231], v24 offset:4096\n\t" "ds_read_b64_tr_b16 v[244:245], v25 offset:0\n\t" \
   "ds_read_b64_tr_b16 v[246:247], v25 offset:4096\n\t" "v_add_u32 v24, s40, v18\n\t" "v_add_u32 v25, s40, v19\n\t" "s_add_u32 s40, s40, 8192\n\t" \
   "s_cmp_eq_u32 s40, 81920\n\t" "s_cselect_b32 s40, 0, s40\n\t" "ds_read_b64_tr_b16 v[236:237], v24 offset:0\n\t" \
   "ds_read_b64_tr_b16 v[238:239], v24 offset:4096\n\t" "ds_read_b64_tr_b16 v[252:253], v25 offset:0\n\t" \
   "ds_read_b64_tr_b16 v[254:255], v25 offset:4096\n\t" "s_waitcnt vmcnt(0) lgkmcnt(0)\n\t" "s_barrier"
#define LDP_CLOB "v24", "v25", "v26", "v32", "v33", "v34", "v35", "v36", "v37", "v38", "v39", "v40", "v41", "v42", "v43", "v44", "v45", "v46", "v47", "v48", "v49", "v50", "v51", "v52", "v53", "v54", "v55", "v56", "v57", "v58", "v59", "v60", "v61", "v62", "v63", "v64", "v65", "v66", "v67", "v68", "v69", "v70", "v71", "v72", "v73", "v74", "v75", "v76", "v77", "v78", "v79", "v80", "v81", "v82", "v83", "v84", "v85", "v86", "v87", "v88", "v89", "v90", "v91", "v92", "v93", "v94", "v95", "v96", "v97", "v98", "v99", "v100", "v101", "v102", "v103", "v104", "v105", "v106", "v107", "v108", "v109", "v110", "v111", "v120", "v121", "v122", "v123", "s40", "s41"
#define LDK_ASM \
   "buffer_load_dword v26, v113, %[rw], 0 offen sc1\n\t" "s_mov_b32 s41, 0\n\t" "buffer_load_dwordx4 v[32:35], v116, %[rx], s41 offen\n\t" \
   "s_mov_b32 s41, 10240\n\t" "buffer_load_dwordx4 v[36:39], v116, %[rx], s41 offen\n\t" "s_mov_b32 s41, 128\n\t" \
   "buffer_load_dwordx4 v[40:43], v116, %[rx], s41 offen\n\t" "s_mov_b32 s41, 10368\n\t" "buffer_load_dwordx4 v[44:47], v116, %[rx], s41 offen\n\t" \
   "s_mov_b32 s41, 256\n\t" "buffer_load_dwordx4 v[48:51], v116, %[rx], s41 offen\n\t" "s_mov_b32 s41, 10496\n\t" \
   "buffer_load_dwordx4 v[52:55], v116, %[rx], s41 offen\n\t" "s_mov_b32 s41, 384\n\t" "buffer_load_dwordx4 v[56:59], v116, %[rx], s41 offen\n\t" \
   "s_mov_b32 s41, 10624\n\t" "buffer_load_dwordx4 v[60:63], v116, %[rx], s41 offen\n\t" "s_mov_b32 s41, 512\n\t" \
   "buffer_load_dwordx4 v[64:67], v116, %[rx], s41 offen\n\t" "s_mov_b32 s41, 10752\n\t" "buffer_load_dwordx4 v[68:71], v116, %[rx], s41 offen\n\t" \
   "s_mov_b32 s41, 640\n\t" "buffer_load_dwordx4 v[72:75], v116, %[rx], s41 offen\n\t" "s_mov_b32 s41, 10880\n\t" \
   "buffer_load_dwordx4 v[76:79], v116, %[rx], s41 offen\n\t" "s_mov_b32 s41, 768\n\t" "buffer_load_dwordx4 v[80:83], v116, %[rx], s41 offen\n\t" \
   "s_mov_b32 s41, 11008\n\t" "buffer_load_dwordx4 v[84:87], v116, %[rx], s41 offen\n\t" "s_mov_b32 s41, 896\n\t" \
   "buffer_load_dwordx4 v[88:91], v116, %[rx], s41 offen\n\t" "s_mov_b32 s41, 11136\n\t" "buffer_load_dwordx4 v[92:95], v116, %[rx], s41 offen\n\t" \
   "s_mov_b32 s40, 0x3f803f80\n\t" "v_mov_b32 v120, 0\n\t" "v_mov_b32 v121, 0\n\t" "v_mov_b32 v122, 0\n\t" "v_mov_b32 v123, 0\n\t" \
   "s_waitcnt vmcnt(16)\n\t" "v_add_u32 v26, 1, v26\n\t" "buffer_store_dword v26, v114, %[rw], 0 offen sc1\n\t" "ds_write_b32 v115, v26\n\t" LDSTAMP(0) \
   "s_waitcnt vmcnt(15)\n\t" LDSTAMP(1) "ds_write_b64 v117, v[32:33] offset:0\n\t" "ds_write_b64 v117, v[34:35] offset:256\n\t" \
   "s_mov_b32 s41, 1024\n\t" "buffer_load_dwordx4 v[96:99], v116, %[rx], s41 offen\n\t" "v_dot2c_f32_bf16 v120, s40, v32\n\t" \
   "v_dot2c_f32_bf16 v121, v32, v32\n\t" "v_dot2c_f32_bf16 v120, s40, v33\n\t" "v_dot2c_f32_bf16 v121, v33, v33\n\t" \
   "v_dot2c_f32_bf16 v120, s40, v34\n\t" "v_dot2c_f32_bf16 v121, v34, v34\n\t" "v_dot2c_f32_bf16 v120, s40, v35\n\t" \
   "v_dot2c_f32_bf16 v121, v35, v35\n\t" "s_waitcnt vmcnt(15)\n\t" "ds_write_b64 v117, v[36:37] offset:128\n\t" \
   "ds_write_b64 v117, v[38:39] offset:384\n\t" "s_mov_b32 s41, 11264\n\t" "buffer_load_dwordx4 v[100:103], v116, %[rx], s41 offen\n\t" \
   "v_dot2c_f32_bf16 v122, s40, v36\n\t" "v_dot2c_f32_bf16 v123, v36, v36\n\t" "v_dot2c_f32_bf16 v122, s40, v37\n\t" \
   "v_dot2c_f32_bf16 v123, v37, v37\n\t" "v_dot2c_f32_bf16 v122, s40, v38\n\t" "v_dot2c_f32_bf16 v123, v38, v38\n\t" \
   "v_dot2c_f32_bf16 v122, s40, v39\n\t" "v_dot2c_f32_bf16 v123, v39, v39\n\t" "s_waitcnt vmcnt(15)\n\t" "ds_write_b64 v117, v[40:41] offset:2048\n\t" \
   "ds_write_b64 v117, v[42:43] offset:2304\n\t" "s_mov_b32 s41, 1152\n\t" "buffer_load_dwordx4 v[104:107], v116, %[rx], s41 offen\n\t" \
   "v_dot2c_f32_bf16 v120, s40, v40\n\t" "v_dot2c_f32_bf16 v121, v40, v40\n\t" "v_dot2c_f32_bf16 v120, s40, v41\n\t" \
   "v_dot2c_f32_bf16 v121, v41, v41\n\t" "v_dot2c_f32_bf16 v120, s40, v42\n\t" "v_dot2c_f32_bf16 v121, v42, v42\n\t" \
   "v_dot2c_f32_bf16 v120, s40, v43\n\t" "v_dot2c_f32_bf16 v121, v43, v43\n\t" "s_waitcnt vmcnt(15)\n\t" "ds_write_b64 v117, v[44:45] offset:2176\n\t" \
   "ds_write_b64 v117, v[46:47] offset:2432\n\t" "s_mov_b32 s41, 11392\n\t" "buffer_load_dwordx4 v[108:111], v116, %[rx], s41 offen\n\t" \
   "v_dot2c_f32_bf16 v122, s40, v44\n\t" "v_dot2c_f32_bf16 v123, v44, v44\n\t" "v_dot2c_f32_bf16 v122, s40, v45\n\t" \
   "v_dot2c_f32_bf16 v123, v45, v45\n\t" "v_dot2c_f32_bf16 v122, s40, v46\n\t" "v_dot2c_f32_bf16 v123, v46, v46\n\t" \
   "v_dot2c_f32_bf16 v122, s40, v47\n\t" "v_dot2c_f32_bf16 v123, v47, v47\n\t" "s_waitcnt vmcnt(15)\n\t" "ds_write_b64 v117, v[48:49] offset:4096\n\t" \
   "ds_write_b64 v117, v[50:51] offset:4352\n\t" "s_mov_b32 s41, 0\n\t" "buffer_load_dwordx4 a[0:3], v16, %[ra], s41 offen\n\t" \
   "v_dot2c_f32_bf16 v120, s40, v48\n\t" "v_dot2c_f32_bf16 v121, v48, v48\n\t" "v_dot2c_f32_bf16 v120, s40, v49\n\t" \
   "v_dot2c_f32_bf16 v121, v49, v49\n\t" "v_dot2c_f32_bf16 v120, s40, v50\n\t" "v_dot2c_f32_bf16 v121, v50, v50\n\t" \
   "v_dot2c_f32_bf16 v120, s40, v51\n\t" "v_dot2c_f32_bf16 v121, v51, v51\n\t" "s_waitcnt vmcnt(15)\n\t" "ds_write_b64 v117, v[52:53] offset:4224\n\t" \
   "ds_write_b64 v117, v[54:55] offset:4480\n\t" "s_mov_b32 s41, 131072\n\t" "buffer_load_dwordx4 a[4:7], v16, %[ra], s41 offen\n\t" \
   "v_dot2c_f32_bf16 v122, s40, v52\n\t" "v_dot2c_f32_bf16 v123, v52, v52\n\t" "v_dot2c_f32_bf16 v122, s40, v53\n\t" \
   "v_dot2c_f32_bf16 v123, v53, v53\n\t" "v_dot2c_f32_bf16 v122, s40, v54\n\t" "v_dot2c_f32_bf16 v123, v54, v54\n\t" \
   "v_dot2c_f32_bf16 v122, s40, v55\n\t" "v_dot2c_f32_bf16 v123, v55, v55\n\t" "s_waitcnt vmcnt(15)\n\t" "ds_write_b64 v117, v[56:57] offset:6144\n\t" \
   "ds_write_b64 v117, v[58:59] offset:6400\n\t" "s_mov_b32 s41, 262144\n\t" "buffer_load_dwordx4 a[16:19], v16, %[ra], s41 offen\n\t" \
   "v_dot2c_f32_bf16 v120, s40, v56\n\t" "v_dot2c_f32_bf16 v121, v56, v56\n\t" "v_dot2c_f32_bf16 v120, s40, v57\n\t" \
   "v_dot2c_f32_bf16 v121, v57, v57\n\t" "v_dot2c_f32_bf16 v120, s40, v58\n\t" "v_dot2c_f32_bf16 v121, v58, v58\n\t" \
   "v_dot2c_f32_bf16 v120, s40, v59\n\t" "v_dot2c_f32_bf16 v121, v59, v59\n\t" "s_waitcnt vmcnt(15)\n\t" "ds_write_b64 v117, v[60:61] offset:6272\n\t" \
   "ds_write_b64 v117, v[62:63] offset:6528\n\t" "s_mov_b32 s41, 393216\n\t" "buffer_load_dwordx4 a[20:23], v16, %[ra], s41 offen\n\t" \
   "v_dot2c_f32_bf16 v122, s40, v60\n\t" "v_dot2c_f32_bf16 v123, v60, v60\n\t" "v_dot2c_f32_bf16 v122, s40, v61\n\t" \
   "v_dot2c_f32_bf16 v123, v61, v61\n\t" "v_dot2c_f32_bf16 v122, s40, v62\n\t" "v_dot2c_f32_bf16 v123, v62, v62\n\t" \
   "v_dot2c_f32_bf16 v122, s40, v63\n\t" "v_dot2c_f32_bf16 v123, v63, v63\n\t" "s_waitcnt vmcnt(15)\n\t" "ds_write_b64 v117, v[64:65] offset:8192\n\t" \
   "ds_write_b64 v117, v[66:67] offset:8448\n\t" "s_mov_b32 s41, 524288\n\t" "buffer_load_dwordx4 a[32:35], v16, %[ra], s41 offen\n\t" \
   "v_dot2c_f32_bf16 v120, s40, v64\n\t" "v_dot2c_f32_bf16 v121, v64, v64\n\t" "v_dot2c_f32_bf16 v120, s40, v65\n\t" \
   "v_dot2c_f32_bf16 v121, v65, v65\n\t" "v_dot2c_f32_bf16 v120, s40, v66\n\t" "v_dot2c_f32_bf16 v121, v66, v66\n\t" \
   "v_dot2c_f32_bf16 v120, s40, v67\n\t" "v_dot2c_f32_bf16 v121, v67, v67\n\t" "s_waitcnt vmcnt(15)\n\t" "ds_write_b64 v117, v[68:69] offset:8320\n\t" \
   "ds_write_b64 v117, v[70:71] offset:8576\n\t" "s_mov_b32 s41, 655360\n\t" "buffer_load_dwordx4 a[36:39], v16, %[ra], s41 offen\n\t" \
   "v_dot2c_f32_bf16 v122, s40, v68\n\t" "v_dot2c_f32_bf16 v123, v68, v68\n\t" "v_dot2c_f32_bf16 v122, s40, v69\n\t" \
   "v_dot2c_f32_bf16 v123, v69, v69\n\t" "v_dot2c_f32_bf16 v122, s40, v70\n\t" "v_dot2c_f32_bf16 v123, v70, v70\n\t" \
   "v_dot2c_f32_bf16 v122, s40, v71\n\t" "v_dot2c_f32_bf16 v123, v71, v71\n\t" "s_waitcnt vmcnt(15)\n\t" "ds_write_b64 v117, v[72:73] offset:10240\n\t" \
   "ds_write_b64 v117, v[74:75] offset:10496\n\t" "s_mov_b32 s41, 786432\n\t" "buffer_load_dwordx4 a[48:51], v16, %[ra], s41 offen\n\t" \
   "v_dot2c_f32_bf16 v120, s40, v72\n\t" "v_dot2c_f32_bf16 v121, v72, v72\n\t" "v_dot2c_f32_bf16 v120, s40, v73\n\t" \
   "v_dot2c_f32_bf16 v121, v73, v73\n\t" "v_dot2c_f32_bf16 v120, s40, v74\n\t" "v_dot2c_f32_bf16 v121, v74, v74\n\t" \
   "v_dot2c_f32_bf16 v120, s40, v75\n\t" "v_dot2c_f32_bf16 v121, v75, v75\n\t" "s_waitcnt vmcnt(15)\n\t" "ds_write_b64 v117, v[76:77] offset:10368\n\t" \
   "ds_write_b64 v117, v[78:79] offset:10624\n\t" "s_mov_b32 s41, 917504\n\t" "buffer_load_dwordx4 a[52:55], v16, %[ra], s41 offen\n\t" \
   "v_dot2c_f32_bf16 v122, s40, v76\n\t" "v_dot2c_f32_bf16 v123, v76, v76\n\t" "v_dot2c_f32_bf16 v122, s40, v77\n\t" \
   "v_dot2c_f32_bf16 v123, v77, v77\n\t" "v_dot2c_f32_bf16 v122, s40, v78\n\t" "v_dot2c_f32_bf16 v123, v78, v78\n\t" \
   "v_dot2c_f32_bf16 v122, s40, v79\n\t" "v_dot2c_f32_bf16 v123, v79, v79\n\t" "s_waitcnt vmcnt(15)\n\t" "ds_write_b64 v117, v[80:81] offset:12288\n\t" \
   "ds_write_b64 v117, v[82:83] offset:12544\n\t" "s_mov_b32 s41, 1048576\n\t" "buffer_load_dwordx4 a[64:67], v16, %[ra], s41 offen\n\t" \
   "v_dot2c_f32_bf16 v120, s40, v80\n\t" "v_dot2c_f32_bf16 v121, v80, v80\n\t" "v_dot2c_f32_bf16 v120, s40, v81\n\t" \
   "v_dot2c_f32_bf16 v121, v81, v81\n\t" "v_dot2c_f32_bf16 v120, s40, v82\n\t" "v_dot2c_f32_bf16 v121, v82, v82\n\t" \
   "v_dot2c_f32_bf16 v120, s40, v83\n\t" "v_dot2c_f32_bf16 v121, v83, v83\n\t" "s_waitcnt vmcnt(15)\n\t" "ds_write_b64 v117, v[84:85] offset:12416\n\t" \
   "ds_write_b64 v117, v[86:87] offset:12672\n\t" "s_mov_b32 s41, 1179648\n\t" "buffer_load_dwordx4 a[68:71], v16, %[ra], s41 offen\n\t" \
   "v_dot2c_f32_bf16 v122, s40, v84\n\t" "v_dot2c_f32_bf16 v123, v84, v84\n\t" "v_dot2c_f32_bf16 v122, s40, v85\n\t" \
   "v_dot2c_f32_bf16 v123, v85, v85\n\t" "v_dot2c_f32_bf16 v122, s40, v86\n\t" "v_dot2c_f32_bf16 v123, v86, v86\n\t" \
   "v_dot2c_f32_bf16 v122, s40, v87\n\t" "v_dot2c_f32_bf16 v123, v87, v87\n\t" "s_waitcnt vmcnt(15)\n\t" "ds_write_b64 v117, v[88:89] offset:14336\n\t" \
   "ds_write_b64 v117, v[90:91] offset:14592\n\t" "s_mov_b32 s41, 0\n\t" "buffer_load_dwordx4 a[8:11], v16, %[rb], s41 offen\n\t" \
   "v_dot2c_f32_bf16 v120, s40, v88\n\t" "v_dot2c_f32_bf16 v121, v88, v88\n\t" "v_dot2c_f32_bf16 v120, s40, v89\n\t" \
   "v_dot2c_f32_bf16 v121, v89, v89\n\t" "v_dot2c_f32_bf16 v120, s40, v90\n\t" "v_dot2c_f32_bf16 v121, v90, v90\n\t" \
   "v_dot2c_f32_bf16 v120, s40, v91\n\t" "v_dot2c_f32_bf16 v121, v91, v91\n\t" "s_waitcnt vmcnt(15)\n\t" "ds_write_b64 v117, v[92:93] offset:14464\n\t" \
   "ds_write_b64 v117, v[94:95] offset:14720\n\t" "s_mov_b32 s41, 131072\n\t" "buffer_load_dwordx4 a[12:15], v16, %[rb], s41 offen\n\t" \
   "v_dot2c_f32_bf16 v122, s40, v92\n\t" "v_dot2c_f32_bf16 v123, v92, v92\n\t" "v_dot2c_f32_bf16 v122, s40, v93\n\t" \
   "v_dot2c_f32_bf16 v123, v93, v93\n\t" "v_dot2c_f32_bf16 v122, s40, v94\n\t" "v_dot2c_f32_bf16 v123, v94, v94\n\t" \
   "v_dot2c_f32_bf16 v122, s40, v95\n\t" "v_dot2c_f32_bf16 v123, v95, v95\n\t" "s_waitcnt vmcnt(15)\n\t" "ds_write_b64 v117, v[96:97] offset:16384\n\t" \
   "ds_write_b64 v117, v[98:99] offset:16640\n\t" "s_mov_b32 s41, 262144\n\t" "buffer_load_dwordx4 a[24:27], v16, %[rb], s41 offen\n\t" \
   "v_dot2c_f32_bf16 v120, s40, v96\n\t" "v_dot2c_f32_bf16 v121, v96, v96\n\t" "v_dot2c_f32_bf16 v120, s40, v97\n\t" \
   "v_dot2c_f32_bf16 v121, v97, v97\n\t" "v_dot2c_f32_bf16 v120, s40, v98\n\t" "v_dot2c_f32_bf16 v121, v98, v98\n\t" \
   "v_dot2c_f32_bf16 v120, s40, v99\n\t" "v_dot2c_f32_bf16 v121, v99, v99\n\t" "s_waitcnt vmcnt(15)\n\t" \
   "ds_write_b64 v117, v[100:101] offset:16512\n\t" "ds_write_b64 v117, v[102:103] offset:16768\n\t" "s_mov_b32 s41, 393216\n\t" \
   "buffer_load_dwordx4 a[28:31], v16, %[rb], s41 offen\n\t" "v_dot2c_f32_bf16 v122, s40, v100\n\t" "v_dot2c_f32_bf16 v123, v100, v100\n\t" \
   "v_dot2c_f32_bf16 v122, s40, v101\n\t" "v_dot2c_f32_bf16 v123, v101, v101\n\t" "v_dot2c_f32_bf16 v122, s40, v102\n\t" \
   "v_dot2c_f32_bf16 v123, v102, v102\n\t" "v_dot2c_f32_bf16 v122, s40, v103\n\t" "v_dot2c_f32_bf16 v123, v103, v103\n\t" "s_waitcnt vmcnt(15)\n\t" \
   "ds_write_b64 v117, v[104:105] offset:18432\n\t" "ds_write_b64 v117, v[106:107] offset:18688\n\t" "s_mov_b32 s41, 524288\n\t" \
   "buffer_load_dwordx4 a[40:43], v16, %[rb], s41 offen\n\t" "v_dot2c_f32_bf16 v120, s40, v104\n\t" "v_dot2c_f32_bf16 v121, v104, v104\n\t" \
   "v_dot2c_f32_bf16 v120, s40, v105\n\t" "v_dot2c_f32_bf16 v121, v105, v105\n\t" "v_dot2c_f32_bf16 v120, s40, v106\n\t" \
   "v_dot2c_f32_bf16 v121, v106, v106\n\t" "v_dot2c_f32_bf16 v120, s40, v107\n\t" "v_dot2c_f32_bf16 v121, v107, v107\n\t" "s_waitcnt vmcnt(15)\n\t" \
   "ds_write_b64 v117, v[108:109] offset:18560\n\t" "ds_write_b64 v117, v[110:111] offset:18816\n\t" "s_mov_b32 s41, 655360\n\t" \
   "buffer_load_dwordx4 a[44:47], v16, %[rb], s41 offen\n\t" "v_dot2c_f32_bf16 v122, s40, v108\n\t" "v_dot2c_f32_bf16 v123, v108, v108\n\t" \
   "v_dot2c_f32_bf16 v122, s40, v109\n\t" "v_dot2c_f32_bf16 v123, v109, v109\n\t" "v_dot2c_f32_bf16 v122, s40, v110\n\t" \
   "v_dot2c_f32_bf16 v123, v110, v110\n\t" "v_dot2c_f32_bf16 v122, s40, v111\n\t" "v_dot2c_f32_bf16 v123, v111, v111\n\t" "s_nop 3\n\t" \
   "v_add_f32_dpp v120, v120, v120 quad_perm:[1,0,3,2] row_mask:0xf bank_mask:0xf\n\t" \
   "v_add_f32_dpp v121, v121, v121 quad_perm:[1,0,3,2] row_mask:0xf bank_mask:0xf\n\t" \
   "v_add_f32_dpp v122, v122, v122 quad_perm:[1,0,3,2] row_mask:0xf bank_mask:0xf\n\t" \
   "v_add_f32_dpp v123, v123, v123 quad_perm:[1,0,3,2] row_mask:0xf bank_mask:0xf\n\t" \
   "v_add_f32_dpp v120, v120, v120 quad_perm:[2,3,0,1] row_mask:0xf bank_mask:0xf\n\t" \
   "v_add_f32_dpp v121, v121, v121 quad_perm:[2,3,0,1] row_mask:0xf bank_mask:0xf\n\t" \
   "v_add_f32_dpp v122, v122, v122 quad_perm:[2,3,0,1] row_mask:0xf bank_mask:0xf\n\t" \
   "v_add_f32_dpp v123, v123, v123 quad_perm:[2,3,0,1] row_mask:0xf bank_mask:0xf\n\t" \
   "v_add_f32_dpp v120, v120, v120 row_half_mirror row_mask:0xf bank_mask:0xf\n\t" \
   "v_add_f32_dpp v121, v121, v121 row_half_mirror row_mask:0xf bank_mask:0xf\n\t" \
   "v_add_f32_dpp v122, v122, v122 row_half_mirror row_mask:0xf bank_mask:0xf\n\t" \
   "v_add_f32_dpp v123, v123, v123 row_half_mirror row_mask:0xf bank_mask:0xf\n\t" "v_and_b32 v120, v119, v120\n\t" "v_and_b32 v121, v119, v121\n\t" \
   "v_and_b32 v122, v119, v122\n\t" "v_and_b32 v123, v119, v123\n\t" "ds_write_b64 v118, v[120:121]\n\t" "ds_write_b64 v118, v[122:123] offset:384\n\t" \
   "s_waitcnt vmcnt(15)\n\t" "ds_write_b128 v20, a[0:3] offset:0\n\t" "s_mov_b32 s41, 786432\n\t" \
   "buffer_load_dwordx4 a[56:59], v16, %[rb], s41 offen\n\t" "s_waitcnt vmcnt(15)\n\t" "ds_write_b128 v20, a[4:7] offset:4096\n\t" \
   "s_mov_b32 s41, 917504\n\t" "buffer_load_dwordx4 a[60:63], v16, %[rb], s41 offen\n\t" "s_waitcnt vmcnt(15)\n\t" \
   "ds_write_b128 v20, a[16:19] offset:8192\n\t" "s_mov_b32 s41, 1048576\n\t" "buffer_load_dwordx4 a[72:75], v16, %[rb], s41 offen\n\t" \
   "s_waitcnt vmcnt(15)\n\t" "ds_write_b128 v20, a[20:23] offset:12288\n\t" "s_mov_b32 s41, 1179648\n\t" \
   "buffer_load_dwordx4 a[76:79], v16, %[rb], s41 offen\n\t" "s_waitcnt vmcnt(15)\n\t" "ds_write_b128 v20, a[32:35] offset:16384\n\t" \
   "s_mov_b32 s41, 1310720\n\t" "buffer_load_dwordx4 a[80:83], v16, %[ra], s41 offen\n\t" "s_waitcnt vmcnt(15)\n\t" \
   "ds_write_b128 v20, a[36:39] offset:20480\n\t" "s_mov_b32 s41, 1441792\n\t" "buffer_load_dwordx4 a[84:87], v16, %[ra], s41 offen\n\t" \
   "s_waitcnt vmcnt(15)\n\t" "ds_write_b128 v20, a[48:51] offset:24576\n\t" "s_mov_b32 s41, 1572864\n\t" \
   "buffer_load_dwordx4 a[96:99], v16, %[ra], s41 offen\n\t" "s_waitcnt vmcnt(15)\n\t" "ds_write_b128 v20, a[52:55] offset:28672\n\t" \
   "s_mov_b32 s41, 1703936\n\t" "buffer_load_dwordx4 a[100:103], v16, %[ra], s41 offen\n\t" "s_waitcnt vmcnt(15)\n\t" \
   "ds_write_b128 v20, a[64:67] offset:32768\n\t" "s_mov_b32 s41, 1835008\n\t" "buffer_load_dwordx4 a[112:115], v16, %[ra], s41 offen\n\t" \
   "s_waitcnt vmcnt(15)\n\t" "ds_write_b128 v20, a[68:71] offset:36864\n\t" "s_mov_b32 s41, 1966080\n\t" \
   "buffer_load_dwordx4 a[116:119], v16, %[ra], s41 offen\n\t" "s_waitcnt lgkmcnt(0)\n\t" "s_barrier\n\t" LDSTAMP(2) \
   "ds_read_b64_tr_b16 a[0:1], v18 offset:0\n\t" "ds_read_b64_tr_b16 a[2:3], v18 offset:4096\n\t" "ds_read_b64_tr_b16 a[4:5], v19 offset:0\n\t" \
   "ds_read_b64_tr_b16 a[6:7], v19 offset:4096\n\t" "ds_read_b64_tr_b16 a[16:17], v18 offset:8192\n\t" \
   "ds_read_b64_tr_b16 a[18:19], v18 offset:12288\n\t" "ds_read_b64_tr_b16 a[20:21], v19 offset:8192\n\t" \
   "ds_read_b64_tr_b16 a[22:23], v19 offset:12288\n\t" "ds_read_b64_tr_b16 a[32:33], v18 offset:16384\n\t" \
   "ds_read_b64_tr_b16 a[34:35], v18 offset:20480\n\t" "ds_read_b64_tr_b16 a[36:37], v19 offset:16384\n\t" \
   "ds_read_b64_tr_b16 a[38:39], v19 offset:20480\n\t" "ds_read_b64_tr_b16 a[48:49], v18 offset:24576\n\t" \
   "ds_read_b64_tr_b16 a[50:51], v18 offset:28672\n\t" "ds_read_b64_tr_b16 a[52:53], v19 offset:24576\n\t" \
   "ds_read_b64_tr_b16 a[54:55], v19 offset:28672\n\t" "ds_read_b64_tr_b16 a[64:65], v18 offset:32768\n\t" \
   "ds_read_b64_tr_b16 a[66:67], v18 offset:36864\n\t" "ds_read_b64_tr_b16 a[68:69], v19 offset:32768\n\t" \
   "ds_read_b64_tr_b16 a[70:71], v19 offset:36864\n\t" "s_waitcnt vmcnt(15)\n\t" "s_waitcnt lgkmcnt(0)\n\t" "s_barrier\n\t" \
   "ds_write_b128 v20, a[8:11] offset:0\n\t" "s_mov_b32 s41, 2097152\n\t" "buffer_load_dwordx4 a[128:131], v16, %[ra], s41 offen\n\t" \
   "s_waitcnt vmcnt(15)\n\t" "ds_write_b128 v20, a[12:15] offset:4096\n\t" "s_mov_b32 s41, 2228224\n\t" \
   "buffer_load_dwordx4 a[132:135], v16, %[ra], s41 offen\n\t" "s_waitcnt vmcnt(15)\n\t" "ds_write_b128 v20, a[24:27] offset:8192\n\t" \
   "s_mov_b32 s41, 2359296\n\t" "buffer_load_dwordx4 a[144:147], v16, %[ra], s41 offen\n\t" "s_waitcnt vmcnt(15)\n\t" \
   "ds_write_b128 v20, a[28:31] offset:12288\n\t" "s_mov_b32 s41, 2490368\n\t" "buffer_load_dwordx4 a[148:151], v16, %[ra], s41 offen\n\t" \
   "s_waitcnt vmcnt(15)\n\t" "ds_write_b128 v20, a[40:43] offset:16384\n\t" "s_mov_b32 s41, 1310720\n\t" \
   "buffer_load_dwordx4 a[88:91], v16, %[rb], s41 offen\n\t" "s_waitcnt vmcnt(15)\n\t" "ds_write_b128 v20, a[44:47] offset:20480\n\t" \
   "s_mov_b32 s41, 1441792\n\t" "buffer_load_dwordx4 a[92:95], v16, %[rb], s41 offen\n\t" "s_waitcnt vmcnt(15)\n\t" \
   "ds_write_b128 v20, a[56:59] offset:24576\n\t" "s_mov_b32 s41, 1572864\n\t" "buffer_load_dwordx4 a[104:107], v16, %[rb], s41 offen\n\t" \
   "s_waitcnt vmcnt(15)\n\t" "ds_write_b128 v20, a[60:63] offset:28672\n\t" "s_mov_b32 s41, 1703936\n\t" \
   "buffer_load_dwordx4 a[108:111], v16, %[rb], s41 offen\n\t" "s_waitcnt vmcnt(15)\n\t" "ds_write_b128 v20, a[72:75] offset:32768\n\t" \
   "s_mov_b32 s41, 1835008\n\t" "buffer_load_dwordx4 a[120:123], v16, %[rb], s41 offen\n\t" "s_waitcnt vmcnt(15)\n\t" \
   "ds_write_b128 v20, a[76:79] offset:36864\n\t" "s_mov_b32 s41, 1966080\n\t" "buffer_load_dwordx4 a[124:127], v16, %[rb], s41 offen\n\t" \
   "s_waitcnt lgkmcnt(0)\n\t" "s_barrier\n\t" LDSTAMP(3) "ds_read_b64_tr_b16 a[8:9], v18 offset:0\n\t" \
   "ds_read_b64_tr_b16 a[10:11], v18 offset:4096\n\t" "ds_read_b64_tr_b16 a[12:13], v19 offset:0\n\t" \
   "ds_read_b64_tr_b16 a[14:15], v19 offset:4096\n\t" "ds_read_b64_tr_b16 a[24:25], v18 offset:8192\n\t" \
   "ds_read_b64_tr_b16 a[26:27], v18 offset:12288\n\t" "ds_read_b64_tr_b16 a[28:29], v19 offset:8192\n\t" \
   "ds_read_b64_tr_b16 a[30:31], v19 offset:12288\n\t" "ds_read_b64_tr_b16 a[40:41], v18 offset:16384\n\t" \
   "ds_read_b64_tr_b16 a[42:43], v18 offset:20480\n\t" "ds_read_b64_tr_b16 a[44:45], v19 offset:16384\n\t" \
   "ds_read_b64_tr_b16 a[46:47], v19 offset:20480\n\t" "ds_read_b64_tr_b16 a[56:57], v18 offset:24576\n\t" \
   "ds_read_b64_tr_b16 a[58:59], v18 offset:28672\n\t" "ds_read_b64_tr_b16 a[60:61], v19 offset:24576\n\t" \
   "ds_read_b64_tr_b16 a[62:63], v19 offset:28672\n\t" "ds_read_b64_tr_b16 a[72:73], v18 offset:32768\n\t" \
   "ds_read_b64_tr_b16 a[74:75], v18 offset:36864\n\t" "ds_read_b64_tr_b16 a[76:77], v19 offset:32768\n\t" \
   "ds_read_b64_tr_b16 a[78:79], v19 offset:36864\n\t" "s_waitcnt vmcnt(15)\n\t" "s_waitcnt lgkmcnt(0)\n\t" "s_barrier\n\t" \
   "ds_write_b128 v20, a[80:83] offset:0\n\t" "s_mov_b32 s41, 2097152\n\t" "buffer_load_dwordx4 a[136:139], v16, %[rb], s41 offen\n\t" \
   "s_waitcnt vmcnt(15)\n\t" "ds_write_b128 v20, a[84:87] offset:4096\n\t" "s_mov_b32 s41, 2228224\n\t" \
   "buffer_load_dwordx4 a[140:143], v16, %[rb], s41 offen\n\t" "s_waitcnt vmcnt(15)\n\t" "ds_write_b128 v20, a[96:99] offset:8192\n\t" \
   "s_mov_b32 s41, 2359296\n\t" "buffer_load_dwordx4 a[152:155], v16, %[rb], s41 offen\n\t" "s_waitcnt vmcnt(15)\n\t" \
   "ds_write_b128 v20, a[100:103] offset:12288\n\t" "s_mov_b32 s41, 2490368\n\t" "buffer_load_dwordx4 a[156:159], v16, %[rb], s41 offen\n\t" \
   "s_waitcnt vmcnt(15)\n\t" "ds_write_b128 v20, a[112:115] offset:16384\n\t" "s_mov_b32 s41, 2621440\n\t" \
   "buffer_load_dwordx4 a[160:163], v16, %[ra], s41 offen\n\t" "s_waitcnt vmcnt(15)\n\t" "ds_write_b128 v20, a[116:119] offset:20480\n\t" \
   "s_mov_b32 s41, 2752512\n\t" "buffer_load_dwordx4 a[164:167], v16, %[ra], s41 offen\n\t" "s_waitcnt vmcnt(15)\n\t" \
   "ds_write_b128 v20, a[128:131] offset:24576\n\t" "s_mov_b32 s41, 2883584\n\t" "buffer_load_dwordx4 a[176:179], v16, %[ra], s41 offen\n\t" \
   "s_waitcnt vmcnt(15)\n\t" "ds_write_b128 v20, a[132:135] offset:28672\n\t" "s_mov_b32 s41, 3014656\n\t" \
   "buffer_load_dwordx4 a[180:183], v16, %[ra], s41 offen\n\t" "s_waitcnt vmcnt(15)\n\t" "ds_write_b128 v20, a[144:147] offset:32768\n\t" \
   "s_mov_b32 s41, 3145728\n\t" "buffer_load_dwordx4 a[192:195], v16, %[ra], s41 offen\n\t" "s_waitcnt vmcnt(15)\n\t" \
   "ds_write_b128 v20, a[148:151] offset:36864\n\t" "s_mov_b32 s41, 3276800\n\t" "buffer_load_dwordx4 a[196:199], v16, %[ra], s41 offen\n\t" \
   "s_waitcnt lgkmcnt(0)\n\t" "s_barrier\n\t" LDSTAMP(4) "ds_read_b64_tr_b16 a[80:81], v18 offset:0\n\t" \
   "ds_read_b64_tr_b16 a[82:83], v18 offset:4096\n\t" "ds_read_b64_tr_b16 a[84:85], v19 offset:0\n\t" \
   "ds_read_b64_tr_b16 a[86:87], v19 offset:4096\n\t" "ds_read_b64_tr_b16 a[96:97], v18 offset:8192\n\t" \
   "ds_read_b64_tr_b16 a[98:99], v18 offset:12288\n\t" "ds_read_b64_tr_b16 a[100:101], v19 offset:8192\n\t" \
   "ds_read_b64_tr_b16 a[102:103], v19 offset:12288\n\t" "ds_read_b64_tr_b16 a[112:113], v18 offset:16384\n\t" \
   "ds_read_b64_tr_b16 a[114:115], v18 offset:20480\n\t" "ds_read_b64_tr_b16 a[116:117], v19 offset:16384\n\t" \
   "ds_read_b64_tr_b16 a[118:119], v19 offset:20480\n\t" "ds_read_b64_tr_b16 a[128:129], v18 offset:24576\n\t" \
   "ds_read_b64_tr_b16 a[130:131], v18 offset:28672\n\t" "ds_read_b64_tr_b16 a[132:133], v19 offset:24576\n\t" \
   "ds_read_b64_tr_b16 a[134:135], v19 offset:28672\n\t" "ds_read_b64_tr_b16 a[144:145], v18 offset:32768\n\t" \
   "ds_read_b64_tr_b16 a[146:147], v18 offset:36864\n\t" "ds_read_b64_tr_b16 a[148:149], v19 offset:32768\n\t" \
   "ds_read_b64_tr_b16 a[150:151], v19 offset:36864\n\t" "s_waitcnt vmcnt(15)\n\t" "s_waitcnt lgkmcnt(0)\n\t" "s_barrier\n\t" \
   "ds_write_b128 v20, a[88:91] offset:0\n\t" "s_mov_b32 s41, 3407872\n\t" "buffer_load_dwordx4 a[208:211], v16, %[ra], s41 offen\n\t" \
   "s_waitcnt vmcnt(15)\n\t" "ds_write_b128 v20, a[92:95] offset:4096\n\t" "s_mov_b32 s41, 3538944\n\t" \
   "buffer_load_dwordx4 a[212:215], v16, %[ra], s41 offen\n\t" "s_waitcnt vmcnt(15)\n\t" "ds_write_b128 v20, a[104:107] offset:8192\n\t" \
   "s_mov_b32 s41, 3670016\n\t" "buffer_load_dwordx4 a[224:227], v16, %[ra], s41 offen\n\t" "s_waitcnt vmcnt(15)\n\t" \
   "ds_write_b128 v20, a[108:111] offset:12288\n\t" "s_mov_b32 s41, 3801088\n\t" "buffer_load_dwordx4 a[228:231], v16, %[ra], s41 offen\n\t" \
   "s_waitcnt vmcnt(15)\n\t" "ds_write_b128 v20, a[120:123] offset:16384\n\t" "s_mov_b32 s41, 2621440\n\t" \
   "buffer_load_dwordx4 a[168:171], v16, %[rb], s41 offen\n\t" "s_waitcnt vmcnt(15)\n\t" "ds_write_b128 v20, a[124:127] offset:20480\n\t" \
   "s_mov_b32 s41, 2752512\n\t" "buffer_load_dwordx4 a[172:175], v16, %[rb], s41 offen\n\t" "s_waitcnt vmcnt(15)\n\t" \
   "ds_write_b128 v20, a[136:139] offset:24576\n\t" "s_mov_b32 s41, 2883584\n\t" "buffer_load_dwordx4 a[184:187], v16, %[rb], s41 offen\n\t" \
   "s_waitcnt vmcnt(15)\n\t" "ds_write_b128 v20, a[140:143] offset:28672\n\t" "s_mov_b32 s41, 3014656\n\t" \
   "buffer_load_dwordx4 a[188:191], v16, %[rb], s41 offen\n\t" "s_waitcnt vmcnt(15)\n\t" "ds_write_b128 v20, a[152:155] offset:32768\n\t" \
   "s_mov_b32 s41, 3145728\n\t" "buffer_load_dwordx4 a[200:203], v16, %[rb], s41 offen\n\t" "s_waitcnt vmcnt(15)\n\t" \
   "ds_write_b128 v20, a[156:159] offset:36864\n\t" "s_mov_b32 s41, 3276800\n\t" "buffer_load_dwordx4 a[204:207], v16, %[rb], s41 offen\n\t" \
   "s_waitcnt lgkmcnt(0)\n\t" "s_barrier\n\t" LDSTAMP(5) "ds_read_b64_tr_b16 a[88:89], v18 offset:0\n\t" \
   "ds_read_b64_tr_b16 a[90:91], v18 offset:4096\n\t" "ds_read_b64_tr_b16 a[92:93], v19 offset:0\n\t" \
   "ds_read_b64_tr_b16 a[94:95], v19 offset:4096\n\t" "ds_read_b64_tr_b16 a[104:105], v18 offset:8192\n\t" \
   "ds_read_b64_tr_b16 a[106:107], v18 offset:12288\n\t" "ds_read_b64_tr_b16 a[108:109], v19 offset:8192\n\t" \
   "ds_read_b64_tr_b16 a[110:111], v19 offset:12288\n\t" "ds_read_b64_tr_b16 a[120:121], v18 offset:16384\n\t" \
   "ds_read_b64_tr_b16 a[122:123], v18 offset:20480\n\t" "ds_read_b64_tr_b16 a[124:125], v19 offset:16384\n\t" \
   "ds_read_b64_tr_b16 a[126:127], v19 offset:20480\n\t" "ds_read_b64_tr_b16 a[136:137], v18 offset:24576\n\t" \
   "ds_read_b64_tr_b16 a[138:139], v18 offset:28672\n\t" "ds_read_b64_tr_b16 a[140:141], v19 offset:24576\n\t" \
   "ds_read_b64_tr_b16 a[142:143], v19 offset:28672\n\t" "ds_read_b64_tr_b16 a[152:153], v18 offset:32768\n\t" \
   "ds_read_b64_tr_b16 a[154:155], v18 offset:36864\n\t" "ds_read_b64_tr_b16 a[156:157], v19 offset:32768\n\t" \
   "ds_read_b64_tr_b16 a[158:159], v19 offset:36864\n\t" "s_waitcnt vmcnt(15)\n\t" "s_waitcnt lgkmcnt(0)\n\t" "s_barrier\n\t" \
   "ds_write_b128 v20, a[160:163] offset:0\n\t" "s_mov_b32 s41, 3407872\n\t" "buffer_load_dwordx4 a[216:219], v16, %[rb], s41 offen\n\t" \
   "s_waitcnt vmcnt(15)\n\t" "ds_write_b128 v20, a[164:167] offset:4096\n\t" "s_mov_b32 s41, 3538944\n\t" \
   "buffer_load_dwordx4 a[220:223], v16, %[rb], s41 offen\n\t" "s_waitcnt vmcnt(15)\n\t" "ds_write_b128 v20, a[176:179] offset:8192\n\t" \
   "s_mov_b32 s41, 3670016\n\t" "buffer_load_dwordx4 a[232:235], v16, %[rb], s41 offen\n\t" "s_waitcnt vmcnt(15)\n\t" \
   "ds_write_b128 v20, a[180:183] offset:12288\n\t" "s_mov_b32 s41, 3801088\n\t" "buffer_load_dwordx4 a[236:239], v16, %[rb], s41 offen\n\t" \
   "s_waitcnt vmcnt(15)\n\t" "ds_write_b128 v20, a[192:195] offset:16384\n\t" "s_mov_b32 s41, 3932160\n\t" \
   "buffer_load_dwordx4 a[240:243], v16, %[ra], s41 offen\n\t" "s_waitcnt vmcnt(15)\n\t" "ds_write_b128 v20, a[196:199] offset:20480\n\t" \
   "s_mov_b32 s41, 4063232\n\t" "buffer_load_dwordx4 a[244:247], v16, %[ra], s41 offen\n\t" "s_waitcnt vmcnt(15)\n\t" \
   "ds_write_b128 v20, a[208:211] offset:24576\n\t" "s_mov_b32 s41, 4194304\n\t" "buffer_load_dwordx4 v[192:195], v16, %[ra], s41 offen\n\t" \
   "s_waitcnt vmcnt(15)\n\t" "ds_write_b128 v20, a[212:215] offset:28672\n\t" "s_mov_b32 s41, 4325376\n\t" \
   "buffer_load_dwordx4 v[196:199], v16, %[ra], s41 offen\n\t" "s_waitcnt vmcnt(15)\n\t" "ds_write_b128 v20, a[224:227] offset:32768\n\t" \
   "s_mov_b32 s41, 4456448\n\t" "buffer_load_dwordx4 v[208:211], v16, %[ra], s41 offen\n\t" "s_waitcnt vmcnt(15)\n\t" \
   "ds_write_b128 v20, a[228:231] offset:36864\n\t" "s_mov_b32 s41, 4587520\n\t" "buffer_load_dwordx4 v[212:215], v16, %[ra], s41 offen\n\t" \
   "s_waitcnt lgkmcnt(0)\n\t" "s_barrier\n\t" LDSTAMP(6) "ds_read_b64_tr_b16 a[160:161], v18 offset:0\n\t" \
   "ds_read_b64_tr_b16 a[162:163], v18 offset:4096\n\t" "ds_read_b64_tr_b16 a[164:165], v19 offset:0\n\t" \
   "ds_read_b64_tr_b16 a[166:167], v19 offset:4096\n\t" "ds_read_b64_tr_b16 a[176:177], v18 offset:8192\n\t" \
   "ds_read_b64_tr_b16 a[178:179], v18 offset:12288\n\t" "ds_read_b64_tr_b16 a[180:181], v19 offset:8192\n\t" \
   "ds_read_b64_tr_b16 a[182:183], v19 offset:12288\n\t" "ds_read_b64_tr_b16 a[192:193], v18 offset:16384\n\t" \
   "ds_read_b64_tr_b16 a[194:195], v18 offset:20480\n\t" "ds_read_b64_tr_b16 a[196:197], v19 offset:16384\n\t" \
   "ds_read_b64_tr_b16 a[198:199], v19 offset:20480\n\t" "ds_read_b64_tr_b16 a[208:209], v18 offset:24576\n\t" \
   "ds_read_b64_tr_b16 a[210:211], v18 offset:28672\n\t" "ds_read_b64_tr_b16 a[212:213], v19 offset:24576\n\t" \
   "ds_read_b64_tr_b16 a[214:215], v19 offset:28672\n\t" "ds_read_b64_tr_b16 a[224:225], v18 offset:32768\n\t" \
   "ds_read_b64_tr_b16 a[226:227], v18 offset:36864\n\t" "ds_read_b64_tr_b16 a[228:229], v19 offset:32768\n\t" \
   "ds_read_b64_tr_b16 a[230:231], v19 offset:36864\n\t" "s_waitcnt vmcnt(15)\n\t" "s_waitcnt lgkmcnt(0)\n\t" "s_barrier\n\t" \
   "ds_write_b128 v20, a[168:171] offset:0\n\t" "s_mov_b32 s41, 4718592\n\t" "buffer_load_dwordx4 v[224:227], v16, %[ra], s41 offen\n\t" \
   "s_waitcnt vmcnt(15)\n\t" "ds_write_b128 v20, a[172:175] offset:4096\n\t" "s_mov_b32 s41, 4849664\n\t" \
   "buffer_load_dwordx4 v[228:231], v16, %[ra], s41 offen\n\t" "s_waitcnt vmcnt(15)\n\t" "ds_write_b128 v20, a[184:187] offset:8192\n\t" \
   "s_mov_b32 s41, 4980736\n\t" "buffer_load_dwordx4 v[240:243], v16, %[ra], s41 offen\n\t" "s_waitcnt vmcnt(15)\n\t" \
   "ds_write_b128 v20, a[188:191] offset:12288\n\t" "s_mov_b32 s41, 5111808\n\t" "buffer_load_dwordx4 v[244:247], v16, %[ra], s41 offen\n\t" \
   "s_waitcnt vmcnt(15)\n\t" "ds_write_b128 v20, a[200:203] offset:16384\n\t" "s_mov_b32 s41, 3932160\n\t" \
   "buffer_load_dwordx4 a[248:251], v16, %[rb], s41 offen\n\t" "s_waitcnt vmcnt(15)\n\t" "ds_write_b128 v20, a[204:207] offset:20480\n\t" \
   "s_mov_b32 s41, 4063232\n\t" "buffer_load_dwordx4 a[252:255], v16, %[rb], s41 offen\n\t" "s_waitcnt vmcnt(15)\n\t" \
   "ds_write_b128 v20, a[216:219] offset:24576\n\t" "s_mov_b32 s41, 4194304\n\t" "buffer_load_dwordx4 v[200:203], v16, %[rb], s41 offen\n\t" \
   "s_waitcnt vmcnt(15)\n\t" "ds_write_b128 v20, a[220:223] offset:28672\n\t" "s_mov_b32 s41, 4325376\n\t" \
   "buffer_load_dwordx4 v[204:207], v16, %[rb], s41 offen\n\t" "s_waitcnt vmcnt(15)\n\t" "ds_write_b128 v20, a[232:235] offset:32768\n\t" \
   "s_mov_b32 s41, 4456448\n\t" "buffer_load_dwordx4 v[216:219], v16, %[rb], s41 offen\n\t" "s_waitcnt vmcnt(15)\n\t" \
   "ds_write_b128 v20, a[236:239] offset:36864\n\t" "s_mov_b32 s41, 4587520\n\t" "buffer_load_dwordx4 v[220:223], v16, %[rb], s41 offen\n\t" \
   "s_waitcnt lgkmcnt(0)\n\t" "s_barrier\n\t" LDSTAMP(7) "ds_read_b64_tr_b16 a[168:169], v18 offset:0\n\t" \
   "ds_read_b64_tr_b16 a[170:171], v18 offset:4096\n\t" "ds_read_b64_tr_b16 a[172:173], v19 offset:0\n\t" \
   "ds_read_b64_tr_b16 a[174:175], v19 offset:4096\n\t" "ds_read_b64_tr_b16 a[184:185], v18 offset:8192\n\t" \
   "ds_read_b64_tr_b16 a[186:187], v18 offset:12288\n\t" "ds_read_b64_tr_b16 a[188:189], v19 offset:8192\n\t" \
   "ds_read_b64_tr_b16 a[190:191], v19 offset:12288\n\t" "ds_read_b64_tr_b16 a[200:201], v18 offset:16384\n\t" \
   "ds_read_b64_tr_b16 a[202:203], v18 offset:20480\n\t" "ds_read_b64_tr_b16 a[204:205], v19 offset:16384\n\t" \
   "ds_read_b64_tr_b16 a[206:207], v19 offset:20480\n\t" "ds_read_b64_tr_b16 a[216:217], v18 offset:24576\n\t" \
   "ds_read_b64_tr_b16 a[218:219], v18 offset:28672\n\t" "ds_read_b64_tr_b16 a[220:221], v19 offset:24576\n\t" \
   "ds_read_b64_tr_b16 a[222:223], v19 offset:28672\n\t" "ds_read_b64_tr_b16 a[232:233], v18 offset:32768\n\t" \
   "ds_read_b64_tr_b16 a[234:235], v18 offset:36864\n\t" "ds_read_b64_tr_b16 a[236:237], v19 offset:32768\n\t" \
   "ds_read_b64_tr_b16 a[238:239], v19 offset:36864\n\t" "s_waitcnt vmcnt(15)\n\t" "s_waitcnt lgkmcnt(0)\n\t" "s_barrier\n\t" \
   "ds_write_b128 v20, a[240:243] offset:0\n\t" "s_mov_b32 s41, 4718592\n\t" "buffer_load_dwordx4 v[232:235], v16, %[rb], s41 offen\n\t" \
   "s_waitcnt vmcnt(15)\n\t" "ds_write_b128 v20, a[244:247] offset:4096\n\t" "s_mov_b32 s41, 4849664\n\t" \
   "buffer_load_dwordx4 v[236:239], v16, %[rb], s41 offen\n\t" "s_waitcnt vmcnt(15)\n\t" "ds_write_b128 v20, v[192:195] offset:8192\n\t" \
   "s_mov_b32 s41, 4980736\n\t" "buffer_load_dwordx4 v[248:251], v16, %[rb], s41 offen\n\t" "s_waitcnt vmcnt(15)\n\t" \
   "ds_write_b128 v20, v[196:199] offset:12288\n\t" "s_mov_b32 s41, 5111808\n\t" "buffer_load_dwordx4 v[252:255], v16, %[rb], s41 offen\n\t" \
   "s_waitcnt vmcnt(15)\n\t" "ds_write_b128 v20, v[208:211] offset:16384\n\t" "s_waitcnt vmcnt(14)\n\t" \
   "ds_write_b128 v20, v[212:215] offset:20480\n\t" "s_waitcnt vmcnt(13)\n\t" "ds_write_b128 v20, v[224:227] offset:24576\n\t" \
   "s_waitcnt vmcnt(12)\n\t" "ds_write_b128 v20, v[228:231] offset:28672\n\t" "s_waitcnt vmcnt(11)\n\t" \
   "ds_write_b128 v20, v[240:243] offset:32768\n\t" "s_waitcnt vmcnt(10)\n\t" "ds_write_b128 v20, v[244:247] offset:36864\n\t" \
   "buffer_load_dwordx4 v[28:31], v112, %[rw], 0 offen sc1\n\t" "s_waitcnt lgkmcnt(0)\n\t" "s_barrier\n\t" LDSTAMP(8) \
   "ds_read_b64_tr_b16 a[240:241], v18 offset:0\n\t" "ds_read_b64_tr_b16 a[242:243], v18 offset:4096\n\t" \
   "ds_read_b64_tr_b16 a[244:245], v19 offset:0\n\t" "ds_read_b64_tr_b16 a[246:247], v19 offset:4096\n\t" \
   "ds_read_b64_tr_b16 v[192:193], v18 offset:8192\n\t" "ds_read_b64_tr_b16 v[194:195], v18 offset:12288\n\t" \
   "ds_read_b64_tr_b16 v[196:197], v19 offset:8192\n\t" "ds_read_b64_tr_b16 v[198:199], v19 offset:12288\n\t" \
   "ds_read_b64_tr_b16 v[208:209], v18 offset:16384\n\t" "ds_read_b64_tr_b16 v[210:211], v18 offset:20480\n\t" \
   "ds_read_b64_tr_b16 v[212:213], v19 offset:16384\n\t" "ds_read_b64_tr_b16 v[214:215], v19 offset:20480\n\t" \
   "ds_read_b64_tr_b16 v[224:225], v18 offset:24576\n\t" "ds_read_b64_tr_b16 v[226:227], v18 offset:28672\n\t" \
   "ds_read_b64_tr_b16 v[228:229], v19 offset:24576\n\t" "ds_read_b64_tr_b16 v[230:231], v19 offset:28672\n\t" \
   "ds_read_b64_tr_b16 v[240:241], v18 offset:32768\n\t" "ds_read_b64_tr_b16 v[242:243], v18 offset:36864\n\t" \
   "ds_read_b64_tr_b16 v[244:245], v19 offset:32768\n\t" "ds_read_b64_tr_b16 v[246:247], v19 offset:36864\n\t" "s_waitcnt vmcnt(10)\n\t" \
   "s_waitcnt lgkmcnt(0)\n\t" "s_barrier\n\t" "ds_write_b128 v20, a[248:251] offset:0\n\t" "s_waitcnt vmcnt(9)\n\t" \
   "ds_write_b128 v20, a[252:255] offset:4096\n\t" "s_waitcnt vmcnt(8)\n\t" "ds_write_b128 v20, v[200:203] offset:8192\n\t" "s_waitcnt vmcnt(7)\n\t" \
   "ds_write_b128 v20, v[204:207] offset:12288\n\t" "s_waitcnt vmcnt(6)\n\t" "ds_write_b128 v20, v[216:219] offset:16384\n\t" "s_waitcnt vmcnt(5)\n\t" \
   "ds_write_b128 v20, v[220:223] offset:20480\n\t" "s_waitcnt vmcnt(4)\n\t" "ds_write_b128 v20, v[232:235] offset:24576\n\t" "s_waitcnt vmcnt(3)\n\t" \
   "ds_write_b128 v20, v[236:239] offset:28672\n\t" "s_waitcnt vmcnt(2)\n\t" "ds_write_b128 v20, v[248:251] offset:32768\n\t" "s_waitcnt vmcnt(1)\n\t" \
   "ds_write_b128 v20, v[252:255] offset:36864\n\t" "s_waitcnt lgkmcnt(0)\n\t" "s_barrier\n\t" LDSTAMP(9) \
   "ds_read_b64_tr_b16 a[248:249], v18 offset:0\n\t" "ds_read_b64_tr_b16 a[250:251], v18 offset:4096\n\t" \
   "ds_read_b64_tr_b16 a[252:253], v19 offset:0\n\t" "ds_read_b64_tr_b16 a[254:255], v19 offset:4096\n\t" \
   "ds_read_b64_tr_b16 v[200:201], v18 offset:8192\n\t" "ds_read_b64_tr_b16 v[202:203], v18 offset:12288\n\t" \
   "ds_read_b64_tr_b16 v[204:205], v19 offset:8192\n\t" "ds_read_b64_tr_b16 v[206:207], v19 offset:12288\n\t" \
   "ds_read_b64_tr_b16 v[216:217], v18 offset:16384\n\t" "ds_read_b64_tr_b16 v[218:219], v18 offset:20480\n\t" \
   "ds_read_b64_tr_b16 v[220:221], v19 offset:16384\n\t" "ds_read_b64_tr_b16 v[222:223], v19 offset:20480\n\t" \
   "ds_read_b64_tr_b16 v[232:233], v18 offset:24576\n\t" "ds_read_b64_tr_b16 v[234:235], v18 offset:28672\n\t" \
   "ds_read_b64_tr_b16 v[236:237], v19 offset:24576\n\t" "ds_read_b64_tr_b16 v[238:239], v19 offset:28672\n\t" \
   "ds_read_b64_tr_b16 v[248:249], v18 offset:32768\n\t" "ds_read_b64_tr_b16 v[250:251], v18 offset:36864\n\t" \
   "ds_read_b64_tr_b16 v[252:253], v19 offset:32768\n\t" "ds_read_b64_tr_b16 v[254:255], v19 offset:36864\n\t" "s_waitcnt vmcnt(0) lgkmcnt(0)\n\t" \
   "s_barrier"
#define LDK_CLOB "v24", "v25", "v26", "v32", "v33", "v34", "v35", "v36", "v37", "v38", "v39", "v40", "v41", "v42", "v43", "v44", "v45", "v46", "v47", "v48", "v49", "v50", "v51", "v52", "v53", "v54", "v55", "v56", "v57", "v58", "v59", "v60", "v61", "v62", "v63", "v64", "v65", "v66", "v67", "v68", "v69", "v70", "v71", "v72", "v73", "v74", "v75", "v76", "v77", "v78", "v79", "v80", "v81", "v82", "v83", "v84", "v85", "v86", "v87", "v88", "v89", "v90", "v91", "v92", "v93", "v94", "v95", "v96", "v97", "v98", "v99", "v100", "v101", "v102", "v103", "v104", "v105", "v106", "v107", "v108", "v109", "v110", "v111", "v120", "v121", "v122", "v123", "s40", "s41"
#define LDV_ASM \
   "buffer_load_dword v26, v113, %[rw], 0 offen sc1\n\t" "s_mov_b32 s41, 0\n\t" "buffer_load_dwordx4 v[32:35], v116, %[rx], s41 offen\n\t" \
   "s_mov_b32 s41, 10240\n\t" "buffer_load_dwordx4 v[36:39], v116, %[rx], s41 offen\n\t" "s_mov_b32 s41, 128\n\t" \
   "buffer_load_dwordx4 v[40:43], v116, %[rx], s41 offen\n\t" "s_mov_b32 s41, 10368\n\t" "buffer_load_dwordx4 v[44:47], v116, %[rx], s41 offen\n\t" \
   "s_mov_b32 s41, 256\n\t" "buffer_load_dwordx4 v[48:51], v116, %[rx], s41 offen\n\t" "s_mov_b32 s41, 10496\n\t" \
   "buffer_load_dwordx4 v[52:55], v116, %[rx], s41 offen\n\t" "s_mov_b32 s41, 384\n\t" "buffer_load_dwordx4 v[56:59], v116, %[rx], s41 offen\n\t" \
   "s_mov_b32 s41, 10624\n\t" "buffer_load_dwordx4 v[60:63], v116, %[rx], s41 offen\n\t" "s_mov_b32 s41, 512\n\t" \
   "buffer_load_dwordx4 v[64:67], v116, %[rx], s41 offen\n\t" "s_mov_b32 s41, 10752\n\t" "buffer_load_dwordx4 v[68:71], v116, %[rx], s41 offen\n\t" \
   "s_mov_b32 s41, 640\n\t" "buffer_load_dwordx4 v[72:75], v116, %[rx], s41 offen\n\t" "s_mov_b32 s41, 10880\n\t" \
   "buffer_load_dwordx4 v[76:79], v116, %[rx], s41 offen\n\t" "s_mov_b32 s41, 768\n\t" "buffer_load_dwordx4 v[80:83], v116, %[rx], s41 offen\n\t" \
   "s_mov_b32 s41, 11008\n\t" "buffer_load_dwordx4 v[84:87], v116, %[rx], s41 offen\n\t" "s_mov_b32 s41, 896\n\t" \
   "buffer_load_dwordx4 v[88:91], v116, %[rx], s41 offen\n\t" "s_mov_b32 s41, 11136\n\t" "buffer_load_dwordx4 v[92:95], v116, %[rx], s41 offen\n\t" \
   "s_mov_b32 s40, 0x3f803f80\n\t" "v_mov_b32 v120, 0\n\t" "v_mov_b32 v121, 0\n\t" "v_mov_b32 v122, 0\n\t" "v_mov_b32 v123, 0\n\t" \
   "s_waitcnt vmcnt(16)\n\t" "v_add_u32 v26, 1, v26\n\t" "buffer_store_dword v26, v114, %[rw], 0 offen sc1\n\t" "ds_write_b32 v115, v26\n\t" LDSTAMP(0) \
   "s_waitcnt vmcnt(15)\n\t" LDSTAMP(1) "ds_write_b64 v117, v[32:33] offset:0\n\t" "ds_write_b64 v117, v[34:35] offset:256\n\t" \
   "s_mov_b32 s41, 1024\n\t" "buffer_load_dwordx4 v[96:99], v116, %[rx], s41 offen\n\t" "v_dot2c_f32_bf16 v120, s40, v32\n\t" \
   "v_dot2c_f32_bf16 v121, v32, v32\n\t" "v_dot2c_f32_bf16 v120, s40, v33\n\t" "v_dot2c_f32_bf16 v121, v33, v33\n\t" \
   "v_dot2c_f32_bf16 v120, s40, v34\n\t" "v_dot2c_f32_bf16 v121, v34, v34\n\t" "v_dot2c_f32_bf16 v120, s40, v35\n\t" \
   "v_dot2c_f32_bf16 v121, v35, v35\n\t" "s_waitcnt vmcnt(15)\n\t" "ds_write_b64 v117, v[36:37] offset:128\n\t" \
   "ds_write_b64 v117, v[38:39] offset:384\n\t" "s_mov_b32 s41, 11264\n\t" "buffer_load_dwordx4 v[100:103], v116, %[rx], s41 offen\n\t" \
   "v_dot2c_f32_bf16 v122, s40, v36\n\t" "v_dot2c_f32_bf16 v123, v36, v36\n\t" "v_dot2c_f32_bf16 v122, s40, v37\n\t" \
   "v_dot2c_f32_bf16 v123, v37, v37\n\t" "v_dot2c_f32_bf16 v122, s40, v38\n\t" "v_dot2c_f32_bf16 v123, v38, v38\n\t" \
   "v_dot2c_f32_bf16 v122, s40, v39\n\t" "v_dot2c_f32_bf16 v123, v39, v39\n\t" "s_waitcnt vmcnt(15)\n\t" "ds_write_b64 v117, v[40:41] offset:2048\n\t" \
   "ds_write_b64 v117, v[42:43] offset:2304\n\t" "s_mov_b32 s41, 1152\n\t" "buffer_load_dwordx4 v[104:107], v116, %[rx], s41 offen\n\t" \
   "v_dot2c_f32_bf16 v120, s40, v40\n\t" "v_dot2c_f32_bf16 v121, v40, v40\n\t" "v_dot2c_f32_bf16 v120, s40, v41\n\t" \
   "v_dot2c_f32_bf16 v121, v41, v41\n\t" "v_dot2c_f32_bf16 v120, s40, v42\n\t" "v_dot2c_f32_bf16 v121, v42, v42\n\t" \
   "v_dot2c_f32_bf16 v120, s40, v43\n\t" "v_dot2c_f32_bf16 v121, v43, v43\n\t" "s_waitcnt vmcnt(15)\n\t" "ds_write_b64 v117, v[44:45] offset:2176\n\t" \
   "ds_write_b64 v117, v[46:47] offset:2432\n\t" "s_mov_b32 s41, 11392\n\t" "buffer_load_dwordx4 v[108:111], v116, %[rx], s41 offen\n\t" \
   "v_dot2c_f32_bf16 v122, s40, v44\n\t" "v_dot2c_f32_bf16 v123, v44, v44\n\t" "v_dot2c_f32_bf16 v122, s40, v45\n\t" \
   "v_dot2c_f32_bf16 v123, v45, v45\n\t" "v_dot2c_f32_bf16 v122, s40, v46\n\t" "v_dot2c_f32_bf16 v123, v46, v46\n\t" \
   "v_dot2c_f32_bf16 v122, s40, v47\n\t" "v_dot2c_f32_bf16 v123, v47, v47\n\t" "s_waitcnt vmcnt(15)\n\t" "ds_write_b64 v117, v[48:49] offset:4096\n\t" \
   "ds_write_b64 v117, v[50:51] offset:4352\n\t" "s_mov_b32 s41, 0\n\t" "buffer_load_dwordx4 a[0:3], v16, %[ra], s41 offen\n\t" \
   "v_dot2c_f32_bf16 v120, s40, v48\n\t" "v_dot2c_f32_bf16 v121, v48, v48\n\t" "v_dot2c_f32_bf16 v120, s40, v49\n\t" \
   "v_dot2c_f32_bf16 v121, v49, v49\n\t" "v_dot2c_f32_bf16 v120, s40, v50\n\t" "v_dot2c_f32_bf16 v121, v50, v50\n\t" \
   "v_dot2c_f32_bf16 v120, s40, v51\n\t" "v_dot2c_f32_bf16 v121, v51, v51\n\t" "s_waitcnt vmcnt(15)\n\t" "ds_write_b64 v117, v[52:53] offset:4224\n\t" \
   "ds_write_b64 v117, v[54:55] offset:4480\n\t" "s_mov_b32 s41, 131072\n\t" "buffer_load_dwordx4 a[4:7], v16, %[ra], s41 offen\n\t" \
   "v_dot2c_f32_bf16 v122, s40, v52\n\t" "v_dot2c_f32_bf16 v123, v52, v52\n\t" "v_dot2c_f32_bf16 v122, s40, v53\n\t" \
   "v_dot2c_f32_bf16 v123, v53, v53\n\t" "v_dot2c_f32_bf16 v122, s40, v54\n\t" "v_dot2c_f32_bf16 v123, v54, v54\n\t" \
   "v_dot2c_f32_bf16 v122, s40, v55\n\t" "v_dot2c_f32_bf16 v123, v55, v55\n\t" "s_waitcnt vmcnt(15)\n\t" "ds_write_b64 v117, v[56:57] offset:6144\n\t" \
   "ds_write_b64 v117, v[58:59] offset:6400\n\t" "s_mov_b32 s41, 262144\n\t" "buffer_load_dwordx4 a[8:11], v16, %[ra], s41 offen\n\t" \
   "v_dot2c_f32_bf16 v120, s40, v56\n\t" "v_dot2c_f32_bf16 v121, v56, v56\n\t" "v_dot2c_f32_bf16 v120, s40, v57\n\t" \
   "v_dot2c_f32_bf16 v121, v57, v57\n\t" "v_dot2c_f32_bf16 v120, s40, v58\n\t" "v_dot2c_f32_bf16 v121, v58, v58\n\t" \
   "v_dot2c_f32_bf16 v120, s40, v59\n\t" "v_dot2c_f32_bf16 v121, v59, v59\n\t" "s_waitcnt vmcnt(15)\n\t" "ds_write_b64 v117, v[60:61] offset:6272\n\t" \
   "ds_write_b64 v117, v[62:63] offset:6528\n\t" "s_mov_b32 s41, 393216\n\t" "buffer_load_dwordx4 a[12:15], v16, %[ra], s41 offen\n\t" \
   "v_dot2c_f32_bf16 v122, s40, v60\n\t" "v_dot2c_f32_bf16 v123, v60, v60\n\t" "v_dot2c_f32_bf16 v122, s40, v61\n\t" \
   "v_dot2c_f32_bf16 v123, v61, v61\n\t" "v_dot2c_f32_bf16 v122, s40, v62\n\t" "v_dot2c_f32_bf16 v123, v62, v62\n\t" \
   "v_dot2c_f32_bf16 v122, s40, v63\n\t" "v_dot2c_f32_bf16 v123, v63, v63\n\t" "s_waitcnt vmcnt(15)\n\t" "ds_write_b64 v117, v[64:65] offset:8192\n\t" \
   "ds_write_b64 v117, v[66:67] offset:8448\n\t" "s_mov_b32 s41, 524288\n\t" "buffer_load_dwordx4 a[16:19], v16, %[ra], s41 offen\n\t" \
   "v_dot2c_f32_bf16 v120, s40, v64\n\t" "v_dot2c_f32_bf16 v121, v64, v64\n\t" "v_dot2c_f32_bf16 v120, s40, v65\n\t" \
   "v_dot2c_f32_bf16 v121, v65, v65\n\t" "v_dot2c_f32_bf16 v120, s40, v66\n\t" "v_dot2c_f32_bf16 v121, v66, v66\n\t" \
   "v_dot2c_f32_bf16 v120, s40, v67\n\t" "v_dot2c_f32_bf16 v121, v67, v67\n\t" "s_waitcnt vmcnt(15)\n\t" "ds_write_b64 v117, v[68:69] offset:8320\n\t" \
   "ds_write_b64 v117, v[70:71] offset:8576\n\t" "s_mov_b32 s41, 655360\n\t" "buffer_load_dwordx4 a[20:23], v16, %[ra], s41 offen\n\t" \
   "v_dot2c_f32_bf16 v122, s40, v68\n\t" "v_dot2c_f32_bf16 v123, v68, v68\n\t" "v_dot2c_f32_bf16 v122, s40, v69\n\t" \
   "v_dot2c_f32_bf16 v123, v69, v69\n\t" "v_dot2c_f32_bf16 v122, s40, v70\n\t" "v_dot2c_f32_bf16 v123, v70, v70\n\t" \
   "v_dot2c_f32_bf16 v122, s40, v71\n\t" "v_dot2c_f32_bf16 v123, v71, v71\n\t" "s_waitcnt vmcnt(15)\n\t" "ds_write_b64 v117, v[72:73] offset:10240\n\t" \
   "ds_write_b64 v117, v[74:75] offset:10496\n\t" "s_mov_b32 s41, 786432\n\t" "buffer_load_dwordx4 a[24:27], v16, %[ra], s41 offen\n\t" \
   "v_dot2c_f32_bf16 v120, s40, v72\n\t" "v_dot2c_f32_bf16 v121, v72, v72\n\t" "v_dot2c_f32_bf16 v120, s40, v73\n\t" \
   "v_dot2c_f32_bf16 v121, v73, v73\n\t" "v_dot2c_f32_bf16 v120, s40, v74\n\t" "v_dot2c_f32_bf16 v121, v74, v74\n\t" \
   "v_dot2c_f32_bf16 v120, s40, v75\n\t" "v_dot2c_f32_bf16 v121, v75, v75\n\t" "s_waitcnt vmcnt(15)\n\t" "ds_write_b64 v117, v[76:77] offset:10368\n\t" \
   "ds_write_b64 v117, v[78:79] offset:10624\n\t" "s_mov_b32 s41, 917504\n\t" "buffer_load_dwordx4 a[28:31], v16, %[ra], s41 offen\n\t" \
   "v_dot2c_f32_bf16 v122, s40, v76\n\t" "v_dot2c_f32_bf16 v123, v76, v76\n\t" "v_dot2c_f32_bf16 v122, s40, v77\n\t" \
   "v_dot2c_f32_bf16 v123, v77, v77\n\t" "v_dot2c_f32_bf16 v122, s40, v78\n\t" "v_dot2c_f32_bf16 v123, v78, v78\n\t" \
   "v_dot2c_f32_bf16 v122, s40, v79\n\t" "v_dot2c_f32_bf16 v123, v79, v79\n\t" "s_waitcnt vmcnt(15)\n\t" "ds_write_b64 v117, v[80:81] offset:12288\n\t" \
   "ds_write_b64 v117, v[82:83] offset:12544\n\t" "s_mov_b32 s41, 1048576\n\t" "buffer_load_dwordx4 a[32:35], v16, %[ra], s41 offen\n\t" \
   "v_dot2c_f32_bf16 v120, s40, v80\n\t" "v_dot2c_f32_bf16 v121, v80, v80\n\t" "v_dot2c_f32_bf16 v120, s40, v81\n\t" \
   "v_dot2c_f32_bf16 v121, v81, v81\n\t" "v_dot2c_f32_bf16 v120, s40, v82\n\t" "v_dot2c_f32_bf16 v121, v82, v82\n\t" \
   "v_dot2c_f32_bf16 v120, s40, v83\n\t" "v_dot2c_f32_bf16 v121, v83, v83\n\t" "s_waitcnt vmcnt(15)\n\t" "ds_write_b64 v117, v[84:85] offset:12416\n\t" \
   "ds_write_b64 v117, v[86:87] offset:12672\n\t" "s_mov_b32 s41, 1179648\n\t" "buffer_load_dwordx4 a[36:39], v16, %[ra], s41 offen\n\t" \
   "v_dot2c_f32_bf16 v122, s40, v84\n\t" "v_dot2c_f32_bf16 v123, v84, v84\n\t" "v_dot2c_f32_bf16 v122, s40, v85\n\t" \
   "v_dot2c_f32_bf16 v123, v85, v85\n\t" "v_dot2c_f32_bf16 v122, s40, v86\n\t" "v_dot2c_f32_bf16 v123, v86, v86\n\t" \
   "v_dot2c_f32_bf16 v122, s40, v87\n\t" "v_dot2c_f32_bf16 v123, v87, v87\n\t" "s_waitcnt vmcnt(15)\n\t" "ds_write_b64 v117, v[88:89] offset:14336\n\t" \
   "ds_write_b64 v117, v[90:91] offset:14592\n\t" "s_mov_b32 s41, 1310720\n\t" "buffer_load_dwordx4 a[40:43], v16, %[ra], s41 offen\n\t" \
   "v_dot2c_f32_bf16 v120, s40, v88\n\t" "v_dot2c_f32_bf16 v121, v88, v88\n\t" "v_dot2c_f32_bf16 v120, s40, v89\n\t" \
   "v_dot2c_f32_bf16 v121, v89, v89\n\t" "v_dot2c_f32_bf16 v120, s40, v90\n\t" "v_dot2c_f32_bf16 v121, v90, v90\n\t" \
   "v_dot2c_f32_bf16 v120, s40, v91\n\t" "v_dot2c_f32_bf16 v121, v91, v91\n\t" "s_waitcnt vmcnt(15)\n\t" "ds_write_b64 v117, v[92:93] offset:14464\n\t" \
   "ds_write_b64 v117, v[94:95] offset:14720\n\t" "s_mov_b32 s41, 1441792\n\t" "buffer_load_dwordx4 a[44:47], v16, %[ra], s41 offen\n\t" \
   "v_dot2c_f32_bf16 v122, s40, v92\n\t" "v_dot2c_f32_bf16 v123, v92, v92\n\t" "v_dot2c_f32_bf16 v122, s40, v93\n\t" \
   "v_dot2c_f32_bf16 v123, v93, v93\n\t" "v_dot2c_f32_bf16 v122, s40, v94\n\t" "v_dot2c_f32_bf16 v123, v94, v94\n\t" \
   "v_dot2c_f32_bf16 v122, s40, v95\n\t" "v_dot2c_f32_bf16 v123, v95, v95\n\t" "s_waitcnt vmcnt(15)\n\t" "ds_write_b64 v117, v[96:97] offset:16384\n\t" \
   "ds_write_b64 v117, v[98:99] offset:16640\n\t" "s_mov_b32 s41, 1572864\n\t" "buffer_load_dwordx4 a[48:51], v16, %[ra], s41 offen\n\t" \
   "v_dot2c_f32_bf16 v120, s40, v96\n\t" "v_dot2c_f32_bf16 v121, v96, v96\n\t" "v_dot2c_f32_bf16 v120, s40, v97\n\t" \
   "v_dot2c_f32_bf16 v121, v97, v97\n\t" "v_dot2c_f32_bf16 v120, s40, v98\n\t" "v_dot2c_f32_bf16 v121, v98, v98\n\t" \
   "v_dot2c_f32_bf16 v120, s40, v99\n\t" "v_dot2c_f32_bf16 v121, v99, v99\n\t" "s_waitcnt vmcnt(15)\n\t" \
   "ds_write_b64 v117, v[100:101] offset:16512\n\t" "ds_write_b64 v117, v[102:103] offset:16768\n\t" "s_mov_b32 s41, 1703936\n\t" \
   "buffer_load_dwordx4 a[52:55], v16, %[ra], s41 offen\n\t" "v_dot2c_f32_bf16 v122, s40, v100\n\t" "v_dot2c_f32_bf16 v123, v100, v100\n\t" \
   "v_dot2c_f32_bf16 v122, s40, v101\n\t" "v_dot2c_f32_bf16 v123, v101, v101\n\t" "v_dot2c_f32_bf16 v122, s40, v102\n\t" \
   "v_dot2c_f32_bf16 v123, v102, v102\n\t" "v_dot2c_f32_bf16 v122, s40, v103\n\t" "v_dot2c_f32_bf16 v123, v103, v103\n\t" "s_waitcnt vmcnt(15)\n\t" \
   "ds_write_b64 v117, v[104:105] offset:18432\n\t" "ds_write_b64 v117, v[106:107] offset:18688\n\t" "s_mov_b32 s41, 1835008\n\t" \
   "buffer_load_dwordx4 a[56:59], v16, %[ra], s41 offen\n\t" "v_dot2c_f32_bf16 v120, s40, v104\n\t" "v_dot2c_f32_bf16 v121, v104, v104\n\t" \
   "v_dot2c_f32_bf16 v120, s40, v105\n\t" "v_dot2c_f32_bf16 v121, v105, v105\n\t" "v_dot2c_f32_bf16 v120, s40, v106\n\t" \
   "v_dot2c_f32_bf16 v121, v106, v106\n\t" "v_dot2c_f32_bf16 v120, s40, v107\n\t" "v_dot2c_f32_bf16 v121, v107, v107\n\t" "s_waitcnt vmcnt(15)\n\t" \
   "ds_write_b64 v117, v[108:109] offset:18560\n\t" "ds_write_b64 v117, v[110:111] offset:18816\n\t" "s_mov_b32 s41, 1966080\n\t" \
   "buffer_load_dwordx4 a[60:63], v16, %[ra], s41 offen\n\t" "v_dot2c_f32_bf16 v122, s40, v108\n\t" "v_dot2c_f32_bf16 v123, v108, v108\n\t" \
   "v_dot2c_f32_bf16 v122, s40, v109\n\t" "v_dot2c_f32_bf16 v123, v109, v109\n\t" "v_dot2c_f32_bf16 v122, s40, v110\n\t" \
   "v_dot2c_f32_bf16 v123, v110, v110\n\t" "v_dot2c_f32_bf16 v122, s40, v111\n\t" "v_dot2c_f32_bf16 v123, v111, v111\n\t" "s_nop 3\n\t" \
   "v_add_f32_dpp v120, v120, v120 quad_perm:[1,0,3,2] row_mask:0xf bank_mask:0xf\n\t" \
   "v_add_f32_dpp v121, v121, v121 quad_perm:[1,0,3,2] row_mask:0xf bank_mask:0xf\n\t" \
   "v_add_f32_dpp v122, v122, v122 quad_perm:[1,0,3,2] row_mask:0xf bank_mask:0xf\n\t" \
   "v_add_f32_dpp v123, v123, v123 quad_perm:[1,0,3,2] row_mask:0xf bank_mask:0xf\n\t" \
   "v_add_f32_dpp v120, v120, v120 quad_perm:[2,3,0,1] row_mask:0xf bank_mask:0xf\n\t" \
   "v_add_f32_dpp v121, v121, v121 quad_perm:[2,3,0,1] row_mask:0xf bank_mask:0xf\n\t" \
   "v_add_f32_dpp v122, v122, v122 quad_perm:[2,3,0,1] row_mask:0xf bank_mask:0xf\n\t" \
   "v_add_f32_dpp v123, v123, v123 quad_perm:[2,3,0,1] row_mask:0xf bank_mask:0xf\n\t" \
   "v_add_f32_dpp v120, v120, v120 row_half_mirror row_mask:0xf bank_mask:0xf\n\t" \
   "v_add_f32_dpp v121, v121, v121 row_half_mirror row_mask:0xf bank_mask:0xf\n\t" \
   "v_add_f32_dpp v122, v122, v122 row_half_mirror row_mask:0xf bank_mask:0xf\n\t" \
   "v_add_f32_dpp v123, v123, v123 row_half_mirror row_mask:0xf bank_mask:0xf\n\t" "v_and_b32 v120, v119, v120\n\t" "v_and_b32 v121, v119, v121\n\t" \
   "v_and_b32 v122, v119, v122\n\t" "v_and_b32 v123, v119, v123\n\t" "ds_write_b64 v118, v[120:121]\n\t" "ds_write_b64 v118, v[122:123] offset:384\n\t" \
   "s_waitcnt vmcnt(15)\n\t" "ds_write_b128 v20, a[0:3] offset:0\n\t" "s_mov_b32 s41, 2097152\n\t" \
   "buffer_load_dwordx4 a[64:67], v16, %[ra], s41 offen\n\t" "s_waitcnt vmcnt(15)\n\t" "ds_write_b128 v20, a[4:7] offset:4096\n\t" \
   "s_mov_b32 s41, 2228224\n\t" "buffer_load_dwordx4 a[68:71], v16, %[ra], s41 offen\n\t" "s_waitcnt vmcnt(15)\n\t" \
   "ds_write_b128 v20, a[8:11] offset:8192\n\t" "s_mov_b32 s41, 2359296\n\t" "buffer_load_dwordx4 a[72:75], v16, %[ra], s41 offen\n\t" \
   "s_waitcnt vmcnt(15)\n\t" "ds_write_b128 v20, a[12:15] offset:12288\n\t" "s_mov_b32 s41, 2490368\n\t" \
   "buffer_load_dwordx4 a[76:79], v16, %[ra], s41 offen\n\t" "s_waitcnt vmcnt(15)\n\t" "ds_write_b128 v20, a[16:19] offset:16384\n\t" \
   "s_mov_b32 s41, 2621440\n\t" "buffer_load_dwordx4 a[80:83], v16, %[ra], s41 offen\n\t" "s_waitcnt vmcnt(15)\n\t" \
   "ds_write_b128 v20, a[20:23] offset:20480\n\t" "s_mov_b32 s41, 2752512\n\t" "buffer_load_dwordx4 a[84:87], v16, %[ra], s41 offen\n\t" \
   "s_waitcnt vmcnt(15)\n\t" "ds_write_b128 v20, a[24:27] offset:24576\n\t" "s_mov_b32 s41, 2883584\n\t" \
   "buffer_load_dwordx4 a[88:91], v16, %[ra], s41 offen\n\t" "s_waitcnt vmcnt(15)\n\t" "ds_write_b128 v20, a[28:31] offset:28672\n\t" \
   "s_mov_b32 s41, 3014656\n\t" "buffer_load_dwordx4 a[92:95], v16, %[ra], s41 offen\n\t" "s_waitcnt vmcnt(15)\n\t" \
   "ds_write_b128 v20, a[32:35] offset:32768\n\t" "s_mov_b32 s41, 3145728\n\t" "buffer_load_dwordx4 a[96:99], v16, %[ra], s41 offen\n\t" \
   "s_waitcnt vmcnt(15)\n\t" "ds_write_b128 v20, a[36:39] offset:36864\n\t" "s_mov_b32 s41, 3276800\n\t" \
   "buffer_load_dwordx4 a[100:103], v16, %[ra], s41 offen\n\t" "s_waitcnt lgkmcnt(0)\n\t" "s_barrier\n\t" LDSTAMP(2) \
   "ds_read_b64_tr_b16 a[0:1], v18 offset:0\n\t" "ds_read_b64_tr_b16 a[2:3], v18 offset:4096\n\t" "ds_read_b64_tr_b16 a[4:5], v19 offset:0\n\t" \
   "ds_read_b64_tr_b16 a[6:7], v19 offset:4096\n\t" "ds_read_b64_tr_b16 a[8:9], v18 offset:8192\n\t" \
   "ds_read_b64_tr_b16 a[10:11], v18 offset:12288\n\t" "ds_read_b64_tr_b16 a[12:13], v19 offset:8192\n\t" \
   "ds_read_b64_tr_b16 a[14:15], v19 offset:12288\n\t" "ds_read_b64_tr_b16 a[16:17], v18 offset:16384\n\t" \
   "ds_read_b64_tr_b16 a[18:19], v18 offset:20480\n\t" "ds_read_b64_tr_b16 a[20:21], v19 offset:16384\n\t" \
   "ds_read_b64_tr_b16 a[22:23], v19 offset:20480\n\t" "ds_read_b64_tr_b16 a[24:25], v18 offset:24576\n\t" \
   "ds_read_b64_tr_b16 a[26:27], v18 offset:28672\n\t" "ds_read_b64_tr_b16 a[28:29], v19 offset:24576\n\t" \
   "ds_read_b64_tr_b16 a[30:31], v19 offset:28672\n\t" "ds_read_b64_tr_b16 a[32:33], v18 offset:32768\n\t" \
   "ds_read_b64_tr_b16 a[34:35], v18 offset:36864\n\t" "ds_read_b64_tr_b16 a[36:37], v19 offset:32768\n\t" \
   "ds_read_b64_tr_b16 a[38:39], v19 offset:36864\n\t" "s_waitcnt vmcnt(15)\n\t" "s_waitcnt lgkmcnt(0)\n\t" "s_barrier\n\t" \
   "ds_write_b128 v20, a[40:43] offset:0\n\t" "s_mov_b32 s41, 3407872\n\t" "buffer_load_dwordx4 a[104:107], v16, %[ra], s41 offen\n\t" \
   "s_waitcnt vmcnt(15)\n\t" "ds_write_b128 v20, a[44:47] offset:4096\n\t" "s_mov_b32 s41, 3538944\n\t" \
   "buffer_load_dwordx4 a[108:111], v16, %[ra], s41 offen\n\t" "s_waitcnt vmcnt(15)\n\t" "ds_write_b128 v20, a[48:51] offset:8192\n\t" \
   "s_mov_b32 s41, 3670016\n\t" "buffer_load_dwordx4 a[112:115], v16, %[ra], s41 offen\n\t" "s_waitcnt vmcnt(15)\n\t" \
   "ds_write_b128 v20, a[52:55] offset:12288\n\t" "s_mov_b32 s41, 3801088\n\t" "buffer_load_dwordx4 a[116:119], v16, %[ra], s41 offen\n\t" \
   "s_waitcnt vmcnt(15)\n\t" "ds_write_b128 v20, a[56:59] offset:16384\n\t" "s_mov_b32 s41, 3932160\n\t" \
   "buffer_load_dwordx4 a[120:123], v16, %[ra], s41 offen\n\t" "s_waitcnt vmcnt(15)\n\t" "ds_write_b128 v20, a[60:63] offset:20480\n\t" \
   "s_mov_b32 s41, 4063232\n\t" "buffer_load_dwordx4 a[124:127], v16, %[ra], s41 offen\n\t" "s_waitcnt vmcnt(15)\n\t" \
   "ds_write_b128 v20, a[64:67] offset:24576\n\t" "s_mov_b32 s41, 4194304\n\t" "buffer_load_dwordx4 a[128:131], v16, %[ra], s41 offen\n\t" \
   "s_waitcnt vmcnt(15)\n\t" "ds_write_b128 v20, a[68:71] offset:28672\n\t" "s_mov_b32 s41, 4325376\n\t" \
   "buffer_load_dwordx4 a[132:135], v16, %[ra], s41 offen\n\t" "s_waitcnt vmcnt(15)\n\t" "ds_write_b128 v20, a[72:75] offset:32768\n\t" \
   "s_mov_b32 s41, 4456448\n\t" "buffer_load_dwordx4 a[136:139], v16, %[ra], s41 offen\n\t" "s_waitcnt vmcnt(15)\n\t" \
   "ds_write_b128 v20, a[76:79] offset:36864\n\t" "s_mov_b32 s41, 4587520\n\t" "buffer_load_dwordx4 a[140:143], v16, %[ra], s41 offen\n\t" \
   "s_waitcnt lgkmcnt(0)\n\t" "s_barrier\n\t" LDSTAMP(3) "ds_read_b64_tr_b16 a[40:41], v18 offset:0\n\t" \
   "ds_read_b64_tr_b16 a[42:43], v18 offset:4096\n\t" "ds_read_b64_tr_b16 a[44:45], v19 offset:0\n\t" \
   "ds_read_b64_tr_b16 a[46:47], v19 offset:4096\n\t" "ds_read_b64_tr_b16 a[48:49], v18 offset:8192\n\t" \
   "ds_read_b64_tr_b16 a[50:51], v18 offset:12288\n\t" "ds_read_b64_tr_b16 a[52:53], v19 offset:8192\n\t" \
   "ds_read_b64_tr_b16 a[54:55], v19 offset:12288\n\t" "ds_read_b64_tr_b16 a[56:57], v18 offset:16384\n\t" \
   "ds_read_b64_tr_b16 a[58:59], v18 offset:20480\n\t" "ds_read_b64_tr_b16 a[60:61], v19 offset:16384\n\t" \
   "ds_read_b64_tr_b16 a[62:63], v19 offset:20480\n\t" "ds_read_b64_tr_b16 a[64:65], v18 offset:24576\n\t" \
   "ds_read_b64_tr_b16 a[66:67], v18 offset:28672\n\t" "ds_read_b64_tr_b16 a[68:69], v19 offset:24576\n\t" \
   "ds_read_b64_tr_b16 a[70:71], v19 offset:28672\n\t" "ds_read_b64_tr_b16 a[72:73], v18 offset:32768\n\t" \
   "ds_read_b64_tr_b16 a[74:75], v18 offset:36864\n\t" "ds_read_b64_tr_b16 a[76:77], v19 offset:32768\n\t" \
   "ds_read_b64_tr_b16 a[78:79], v19 offset:36864\n\t" "s_waitcnt vmcnt(15)\n\t" "s_waitcnt lgkmcnt(0)\n\t" "s_barrier\n\t" \
   "ds_write_b128 v20, a[80:83] offset:0\n\t" "s_mov_b32 s41, 4718592\n\t" "buffer_load_dwordx4 a[144:147], v16, %[ra], s41 offen\n\t" \
   "s_waitcnt vmcnt(15)\n\t" "ds_write_b128 v20, a[84:87] offset:4096\n\t" "s_mov_b32 s41, 4849664\n\t" \
   "buffer_load_dwordx4 a[148:151], v16, %[ra], s41 offen\n\t" "s_waitcnt vmcnt(15)\n\t" "ds_write_b128 v20, a[88:91] offset:8192\n\t" \
   "s_mov_b32 s41, 4980736\n\t" "buffer_load_dwordx4 a[152:155], v16, %[ra], s41 offen\n\t" "s_waitcnt vmcnt(15)\n\t" \
   "ds_write_b128 v20, a[92:95] offset:12288\n\t" "s_mov_b32 s41, 5111808\n\t" "buffer_load_dwordx4 a[156:159], v16, %[ra], s41 offen\n\t" \
   "s_waitcnt vmcnt(15)\n\t" "ds_write_b128 v20, a[96:99] offset:16384\n\t" "s_mov_b32 s41, 0\n\t" \
   "buffer_load_dwordx4 a[160:163], v17, %[rb], s41 offen\n\t" "s_waitcnt vmcnt(15)\n\t" "ds_write_b128 v20, a[100:103] offset:20480\n\t" \
   "s_mov_b32 s41, 256\n\t" "buffer_load_dwordx4 a[176:179], v17, %[rb], s41 offen\n\t" "s_waitcnt vmcnt(15)\n\t" \
   "ds_write_b128 v20, a[104:107] offset:24576\n\t" "s_mov_b32 s41, 512\n\t" "buffer_load_dwordx4 a[192:195], v17, %[rb], s41 offen\n\t" \
   "s_waitcnt vmcnt(15)\n\t" "ds_write_b128 v20, a[108:111] offset:28672\n\t" "s_mov_b32 s41, 768\n\t" \
   "buffer_load_dwordx4 a[208:211], v17, %[rb], s41 offen\n\t" "s_waitcnt vmcnt(15)\n\t" "ds_write_b128 v20, a[112:115] offset:32768\n\t" \
   "s_mov_b32 s41, 1024\n\t" "buffer_load_dwordx4 a[224:227], v17, %[rb], s41 offen\n\t" "s_waitcnt vmcnt(15)\n\t" \
   "ds_write_b128 v20, a[116:119] offset:36864\n\t" "s_mov_b32 s41, 20480\n\t" "buffer_load_dwordx4 a[240:243], v17, %[rb], s41 offen\n\t" \
   "s_waitcnt lgkmcnt(0)\n\t" "s_barrier\n\t" LDSTAMP(4) "ds_read_b64_tr_b16 a[80:81], v18 offset:0\n\t" \
   "ds_read_b64_tr_b16 a[82:83], v18 offset:4096\n\t" "ds_read_b64_tr_b16 a[84:85], v19 offset:0\n\t" \
   "ds_read_b64_tr_b16 a[86:87], v19 offset:4096\n\t" "ds_read_b64_tr_b16 a[88:89], v18 offset:8192\n\t" \
   "ds_read_b64_tr_b16 a[90:91], v18 offset:12288\n\t" "ds_read_b64_tr_b16 a[92:93], v19 offset:8192\n\t" \
   "ds_read_b64_tr_b16 a[94:95], v19 offset:12288\n\t" "ds_read_b64_tr_b16 a[96:97], v18 offset:16384\n\t" \
   "ds_read_b64_tr_b16 a[98:99], v18 offset:20480\n\t" "ds_read_b64_tr_b16 a[100:101], v19 offset:16384\n\t" \
   "ds_read_b64_tr_b16 a[102:103], v19 offset:20480\n\t" "ds_read_b64_tr_b16 a[104:105], v18 offset:24576\n\t" \
   "ds_read_b64_tr_b16 a[106:107], v18 offset:28672\n\t" "ds_read_b64_tr_b16 a[108:109], v19 offset:24576\n\t" \
   "ds_read_b64_tr_b16 a[110:111], v19 offset:28672\n\t" "ds_read_b64_tr_b16 a[112:113], v18 offset:32768\n\t" \
   "ds_read_b64_tr_b16 a[114:115], v18 offset:36864\n\t" "ds_read_b64_tr_b16 a[116:117], v19 offset:32768\n\t" \
   "ds_read_b64_tr_b16 a[118:119], v19 offset:36864\n\t" "s_waitcnt vmcnt(15)\n\t" "s_waitcnt lgkmcnt(0)\n\t" "s_barrier\n\t" \
   "ds_write_b128 v20, a[120:123] offset:0\n\t" "s_mov_b32 s41, 20736\n\t" "buffer_load_dwordx4 v[192:195], v17, %[rb], s41 offen\n\t" \
   "s_waitcnt vmcnt(15)\n\t" "ds_write_b128 v20, a[124:127] offset:4096\n\t" "s_mov_b32 s41, 20992\n\t" \
   "buffer_load_dwordx4 v[208:211], v17, %[rb], s41 offen\n\t" "s_waitcnt vmcnt(15)\n\t" "ds_write_b128 v20, a[128:131] offset:8192\n\t" \
   "s_mov_b32 s41, 21248\n\t" "buffer_load_dwordx4 v[224:227], v17, %[rb], s41 offen\n\t" "s_waitcnt vmcnt(15)\n\t" \
   "ds_write_b128 v20, a[132:135] offset:12288\n\t" "s_mov_b32 s41, 21504\n\t" "buffer_load_dwordx4 v[240:243], v17, %[rb], s41 offen\n\t" \
   "s_waitcnt vmcnt(15)\n\t" "ds_write_b128 v20, a[136:139] offset:16384\n\t" "s_mov_b32 s41, 40960\n\t" \
   "buffer_load_dwordx4 a[164:167], v17, %[rb], s41 offen\n\t" "s_waitcnt vmcnt(15)\n\t" "ds_write_b128 v20, a[140:143] offset:20480\n\t" \
   "s_mov_b32 s41, 41216\n\t" "buffer_load_dwordx4 a[180:183], v17, %[rb], s41 offen\n\t" "s_waitcnt vmcnt(15)\n\t" \
   "ds_write_b128 v20, a[144:147] offset:24576\n\t" "s_mov_b32 s41, 41472\n\t" "buffer_load_dwordx4 a[196:199], v17, %[rb], s41 offen\n\t" \
   "s_waitcnt vmcnt(15)\n\t" "ds_write_b128 v20, a[148:151] offset:28672\n\t" "s_mov_b32 s41, 41728\n\t" \
   "buffer_load_dwordx4 a[212:215], v17, %[rb], s41 offen\n\t" "s_waitcnt vmcnt(15)\n\t" "ds_write_b128 v20, a[152:155] offset:32768\n\t" \
   "s_mov_b32 s41, 41984\n\t" "buffer_load_dwordx4 a[228:231], v17, %[rb], s41 offen\n\t" "s_waitcnt vmcnt(15)\n\t" \
   "ds_write_b128 v20, a[156:159] offset:36864\n\t" "s_mov_b32 s41, 61440\n\t" "buffer_load_dwordx4 a[244:247], v17, %[rb], s41 offen\n\t" \
   "s_waitcnt lgkmcnt(0)\n\t" "s_barrier\n\t" LDSTAMP(5) "ds_read_b64_tr_b16 a[120:121], v18 offset:0\n\t" \
   "ds_read_b64_tr_b16 a[122:123], v18 offset:4096\n\t" "ds_read_b64_tr_b16 a[124:125], v19 offset:0\n\t" \
   "ds_read_b64_tr_b16 a[126:127], v19 offset:4096\n\t" "ds_read_b64_tr_b16 a[128:129], v18 offset:8192\n\t" \
   "ds_read_b64_tr_b16 a[130:131], v18 offset:12288\n\t" "ds_read_b64_tr_b16 a[132:133], v19 offset:8192\n\t" \
   "ds_read_b64_tr_b16 a[134:135], v19 offset:12288\n\t" "ds_read_b64_tr_b16 a[136:137], v18 offset:16384\n\t" \
   "ds_read_b64_tr_b16 a[138:139], v18 offset:20480\n\t" "ds_read_b64_tr_b16 a[140:141], v19 offset:16384\n\t" \
   "ds_read_b64_tr_b16 a[142:143], v19 offset:20480\n\t" "ds_read_b64_tr_b16 a[144:145], v18 offset:24576\n\t" \
   "ds_read_b64_tr_b16 a[146:147], v18 offset:28672\n\t" "ds_read_b64_tr_b16 a[148:149], v19 offset:24576\n\t" \
   "ds_read_b64_tr_b16 a[150:151], v19 offset:28672\n\t" "ds_read_b64_tr_b16 a[152:153], v18 offset:32768\n\t" \
   "ds_read_b64_tr_b16 a[154:155], v18 offset:36864\n\t" "ds_read_b64_tr_b16 a[156:157], v19 offset:32768\n\t" \
   "ds_read_b64_tr_b16 a[158:159], v19 offset:36864\n\t" "s_waitcnt vmcnt(15)\n\t" "s_waitcnt lgkmcnt(0)\n\t" "s_barrier\n\t" \
   "ds_write_b32 v21, a160 offset:0\n\t" "ds_write_b32 v21, a161 offset:21120\n\t" "ds_write_b32 v21, a162 offset:10560\n\t" \
   "ds_write_b32 v21, a163 offset:31680\n\t" "s_mov_b32 s41, 61696\n\t" "buffer_load_dwordx4 v[196:199], v17, %[rb], s41 offen\n\t" \
   "s_waitcnt vmcnt(15)\n\t" "ds_write_b32 v21, a176 offset:64\n\t" "ds_write_b32 v21, a177 offset:21184\n\t" "ds_write_b32 v21, a178 offset:10624\n\t" \
   "ds_write_b32 v21, a179 offset:31744\n\t" "s_mov_b32 s41, 61952\n\t" "buffer_load_dwordx4 v[212:215], v17, %[rb], s41 offen\n\t" \
   "s_waitcnt vmcnt(15)\n\t" "ds_write_b32 v21, a192 offset:128\n\t" "ds_write_b32 v21, a193 offset:21248\n\t" \
   "ds_write_b32 v21, a194 offset:10688\n\t" "ds_write_b32 v21, a195 offset:31808\n\t" "s_mov_b32 s41, 62208\n\t" \
   "buffer_load_dwordx4 v[228:231], v17, %[rb], s41 offen\n\t" "s_waitcnt vmcnt(15)\n\t" "ds_write_b32 v21, a208 offset:192\n\t" \
   "ds_write_b32 v21, a209 offset:21312\n\t" "ds_write_b32 v21, a210 offset:10752\n\t" "ds_write_b32 v21, a211 offset:31872\n\t" \
   "s_mov_b32 s41, 62464\n\t" "buffer_load_dwordx4 v[244:247], v17, %[rb], s41 offen\n\t" "s_waitcnt vmcnt(15)\n\t" \
   "ds_write_b32 v21, a224 offset:256\n\t" "ds_write_b32 v21, a225 offset:21376\n\t" "ds_write_b32 v21, a226 offset:10816\n\t" \
   "ds_write_b32 v21, a227 offset:31936\n\t" "s_mov_b32 s41, 81920\n\t" "buffer_load_dwordx4 a[168:171], v17, %[rb], s41 offen\n\t" \
   "s_waitcnt vmcnt(15)\n\t" "ds_write_b32 v21, a240 offset:5248\n\t" "ds_write_b32 v21, a241 offset:26368\n\t" \
   "ds_write_b32 v21, a242 offset:15808\n\t" "ds_write_b32 v21, a243 offset:36928\n\t" "s_mov_b32 s41, 82176\n\t" \
   "buffer_load_dwordx4 a[184:187], v17, %[rb], s41 offen\n\t" "s_waitcnt vmcnt(15)\n\t" "ds_write_b32 v21, v192 offset:5312\n\t" \
   "ds_write_b32 v21, v193 offset:26432\n\t" "ds_write_b32 v21, v194 offset:15872\n\t" "ds_write_b32 v21, v195 offset:36992\n\t" \
   "s_mov_b32 s41, 82432\n\t" "buffer_load_dwordx4 a[200:203], v17, %[rb], s41 offen\n\t" "s_waitcnt vmcnt(15)\n\t" \
   "ds_write_b32 v21, v208 offset:5376\n\t" "ds_write_b32 v21, v209 offset:26496\n\t" "ds_write_b32 v21, v210 offset:15936\n\t" \
   "ds_write_b32 v21, v211 offset:37056\n\t" "s_mov_b32 s41, 82688\n\t" "buffer_load_dwordx4 a[216:219], v17, %[rb], s41 offen\n\t" \
   "s_waitcnt vmcnt(15)\n\t" "ds_write_b32 v21, v224 offset:5440\n\t" "ds_write_b32 v21, v225 offset:26560\n\t" \
   "ds_write_b32 v21, v226 offset:16000\n\t" "ds_write_b32 v21, v227 offset:37120\n\t" "s_mov_b32 s41, 82944\n\t" \
   "buffer_load_dwordx4 a[232:235], v17, %[rb], s41 offen\n\t" "s_waitcnt vmcnt(15)\n\t" "ds_write_b32 v21, v240 offset:5504\n\t" \
   "ds_write_b32 v21, v241 offset:26624\n\t" "ds_write_b32 v21, v242 offset:16064\n\t" "ds_write_b32 v21, v243 offset:37184\n\t" \
   "s_mov_b32 s41, 102400\n\t" "buffer_load_dwordx4 a[248:251], v17, %[rb], s41 offen\n\t" "s_waitcnt lgkmcnt(0)\n\t" "s_barrier\n\t" LDSTAMP(6) \
   "ds_read_b64_tr_b16 a[160:161], v22 offset:0\n\t" "ds_read_b64_tr_b16 a[162:163], v22 offset:5248\n\t" \
   "ds_read_b64_tr_b16 a[176:177], v22 offset:8\n\t" "ds_read_b64_tr_b16 a[178:179], v22 offset:5256\n\t" \
   "ds_read_b64_tr_b16 a[192:193], v22 offset:16\n\t" "ds_read_b64_tr_b16 a[194:195], v22 offset:5264\n\t" \
   "ds_read_b64_tr_b16 a[208:209], v22 offset:24\n\t" "ds_read_b64_tr_b16 a[210:211], v22 offset:5272\n\t" \
   "ds_read_b64_tr_b16 a[224:225], v22 offset:32\n\t" "ds_read_b64_tr_b16 a[226:227], v22 offset:5280\n\t" \
   "ds_read_b64_tr_b16 a[240:241], v22 offset:40\n\t" "ds_read_b64_tr_b16 a[242:243], v22 offset:5288\n\t" \
   "ds_read_b64_tr_b16 v[192:193], v22 offset:48\n\t" "ds_read_b64_tr_b16 v[194:195], v22 offset:5296\n\t" \
   "ds_read_b64_tr_b16 v[208:209], v22 offset:56\n\t" "ds_read_b64_tr_b16 v[210:211], v22 offset:5304\n\t" \
   "ds_read_b64_tr_b16 v[224:225], v22 offset:64\n\t" "ds_read_b64_tr_b16 v[226:227], v22 offset:5312\n\t" \
   "ds_read_b64_tr_b16 v[240:241], v22 offset:72\n\t" "ds_read_b64_tr_b16 v[242:243], v22 offset:5320\n\t" "s_waitcnt vmcnt(15)\n\t" \
   "s_waitcnt lgkmcnt(0)\n\t" "s_barrier\n\t" "ds_write_b32 v21, a164 offset:0\n\t" "ds_write_b32 v21, a165 offset:21120\n\t" \
   "ds_write_b32 v21, a166 offset:10560\n\t" "ds_write_b32 v21, a167 offset:31680\n\t" "s_mov_b32 s41, 102656\n\t" \
   "buffer_load_dwordx4 v[200:203], v17, %[rb], s41 offen\n\t" "s_waitcnt vmcnt(15)\n\t" "ds_write_b32 v21, a180 offset:64\n\t" \
   "ds_write_b32 v21, a181 offset:21184\n\t" "ds_write_b32 v21, a182 offset:10624\n\t" "ds_write_b32 v21, a183 offset:31744\n\t" \
   "s_mov_b32 s41, 102912\n\t" "buffer_load_dwordx4 v[216:219], v17, %[rb], s41 offen\n\t" "s_waitcnt vmcnt(15)\n\t" \
   "ds_write_b32 v21, a196 offset:128\n\t" "ds_write_b32 v21, a197 offset:21248\n\t" "ds_write_b32 v21, a198 offset:10688\n\t" \
   "ds_write_b32 v21, a199 offset:31808\n\t" "s_mov_b32 s41, 103168\n\t" "buffer_load_dwordx4 v[232:235], v17, %[rb], s41 offen\n\t" \
   "s_waitcnt vmcnt(15)\n\t" "ds_write_b32 v21, a212 offset:192\n\t" "ds_write_b32 v21, a213 offset:21312\n\t" \
   "ds_write_b32 v21, a214 offset:10752\n\t" "ds_write_b32 v21, a215 offset:31872\n\t" "s_mov_b32 s41, 103424\n\t" \
   "buffer_load_dwordx4 v[248:251], v17, %[rb], s41 offen\n\t" "s_waitcnt vmcnt(15)\n\t" "ds_write_b32 v21, a228 offset:256\n\t" \
   "ds_write_b32 v21, a229 offset:21376\n\t" "ds_write_b32 v21, a230 offset:10816\n\t" "ds_write_b32 v21, a231 offset:31936\n\t" \
   "s_mov_b32 s41, 122880\n\t" "buffer_load_dwordx4 a[172:175], v17, %[rb], s41 offen\n\t" "s_waitcnt vmcnt(15)\n\t" \
   "ds_write_b32 v21, a244 offset:5248\n\t" "ds_write_b32 v21, a245 offset:26368\n\t" "ds_write_b32 v21, a246 offset:15808\n\t" \
   "ds_write_b32 v21, a247 offset:36928\n\t" "s_mov_b32 s41, 123136\n\t" "buffer_load_dwordx4 a[188:191], v17, %[rb], s41 offen\n\t" \
   "s_waitcnt vmcnt(15)\n\t" "ds_write_b32 v21, v196 offset:5312\n\t" "ds_write_b32 v21, v197 offset:26432\n\t" \
   "ds_write_b32 v21, v198 offset:15872\n\t" "ds_write_b32 v21, v199 offset:36992\n\t" "s_mov_b32 s41, 123392\n\t" \
   "buffer_load_dwordx4 a[204:207], v17, %[rb], s41 offen\n\t" "s_waitcnt vmcnt(15)\n\t" "ds_write_b32 v21, v212 offset:5376\n\t" \
   "ds_write_b32 v21, v213 offset:26496\n\t" "ds_write_b32 v21, v214 offset:15936\n\t" "ds_write_b32 v21, v215 offset:37056\n\t" \
   "s_mov_b32 s41, 123648\n\t" "buffer_load_dwordx4 a[220:223], v17, %[rb], s41 offen\n\t" "s_waitcnt vmcnt(15)\n\t" \
   "ds_write_b32 v21, v228 offset:5440\n\t" "ds_write_b32 v21, v229 offset:26560\n\t" "ds_write_b32 v21, v230 offset:16000\n\t" \
   "ds_write_b32 v21, v231 offset:37120\n\t" "s_mov_b32 s41, 123904\n\t" "buffer_load_dwordx4 a[236:239], v17, %[rb], s41 offen\n\t" \
   "s_waitcnt vmcnt(15)\n\t" "ds_write_b32 v21, v244 offset:5504\n\t" "ds_write_b32 v21, v245 offset:26624\n\t" \
   "ds_write_b32 v21, v246 offset:16064\n\t" "ds_write_b32 v21, v247 offset:37184\n\t" "s_mov_b32 s41, 143360\n\t" \
   "buffer_load_dwordx4 a[252:255], v17, %[rb], s41 offen\n\t" "s_waitcnt lgkmcnt(0)\n\t" "s_barrier\n\t" LDSTAMP(7) \
   "ds_read_b64_tr_b16 a[164:165], v22 offset:0\n\t" "ds_read_b64_tr_b16 a[166:167], v22 offset:5248\n\t" \
   "ds_read_b64_tr_b16 a[180:181], v22 offset:8\n\t" "ds_read_b64_tr_b16 a[182:183], v22 offset:5256\n\t" \
   "ds_read_b64_tr_b16 a[196:197], v22 offset:16\n\t" "ds_read_b64_tr_b16 a[198:199], v22 offset:5264\n\t" \
   "ds_read_b64_tr_b16 a[212:213], v22 offset:24\n\t" "ds_read_b64_tr_b16 a[214:215], v22 offset:5272\n\t" \
   "ds_read_b64_tr_b16 a[228:229], v22 offset:32\n\t" "ds_read_b64_tr_b16 a[230:231], v22 offset:5280\n\t" \
   "ds_read_b64_tr_b16 a[244:245], v22 offset:40\n\t" "ds_read_b64_tr_b16 a[246:247], v22 offset:5288\n\t" \
   "ds_read_b64_tr_b16 v[196:197], v22 offset:48\n\t" "ds_read_b64_tr_b16 v[198:199], v22 offset:5296\n\t" \
   "ds_read_b64_tr_b16 v[212:213], v22 offset:56\n\t" "ds_read_b64_tr_b16 v[214:215], v22 offset:5304\n\t" \
   "ds_read_b64_tr_b16 v[228:229], v22 offset:64\n\t" "ds_read_b64_tr_b16 v[230:231], v22 offset:5312\n\t" \
   "ds_read_b64_tr_b16 v[244:245], v22 offset:72\n\t" "ds_read_b64_tr_b16 v[246:247], v22 offset:5320\n\t" "s_waitcnt vmcnt(15)\n\t" \
   "s_waitcnt lgkmcnt(0)\n\t" "s_barrier\n\t" "ds_write_b32 v21, a168 offset:0\n\t" "ds_write_b32 v21, a169 offset:21120\n\t" \
   "ds_write_b32 v21, a170 offset:10560\n\t" "ds_write_b32 v21, a171 offset:31680\n\t" "s_mov_b32 s41, 143616\n\t" \
   "buffer_load_dwordx4 v[204:207], v17, %[rb], s41 offen\n\t" "s_waitcnt vmcnt(15)\n\t" "ds_write_b32 v21, a184 offset:64\n\t" \
   "ds_write_b32 v21, a185 offset:21184\n\t" "ds_write_b32 v21, a186 offset:10624\n\t" "ds_write_b32 v21, a187 offset:31744\n\t" \
   "s_mov_b32 s41, 143872\n\t" "buffer_load_dwordx4 v[220:223], v17, %[rb], s41 offen\n\t" "s_waitcnt vmcnt(15)\n\t" \
   "ds_write_b32 v21, a200 offset:128\n\t" "ds_write_b32 v21, a201 offset:21248\n\t" "ds_write_b32 v21, a202 offset:10688\n\t" \
   "ds_write_b32 v21, a203 offset:31808\n\t" "s_mov_b32 s41, 144128\n\t" "buffer_load_dwordx4 v[236:239], v17, %[rb], s41 offen\n\t" \
   "s_waitcnt vmcnt(15)\n\t" "ds_write_b32 v21, a216 offset:192\n\t" "ds_write_b32 v21, a217 offset:21312\n\t" \
   "ds_write_b32 v21, a218 offset:10752\n\t" "ds_write_b32 v21, a219 offset:31872\n\t" "s_mov_b32 s41, 144384\n\t" \
   "buffer_load_dwordx4 v[252:255], v17, %[rb], s41 offen\n\t" "s_waitcnt vmcnt(15)\n\t" "ds_write_b32 v21, a232 offset:256\n\t" \
   "ds_write_b32 v21, a233 offset:21376\n\t" "ds_write_b32 v21, a234 offset:10816\n\t" "ds_write_b32 v21, a235 offset:31936\n\t" \
   "s_waitcnt vmcnt(14)\n\t" "ds_write_b32 v21, a248 offset:5248\n\t" "ds_write_b32 v21, a249 offset:26368\n\t" \
   "ds_write_b32 v21, a250 offset:15808\n\t" "ds_write_b32 v21, a251 offset:36928\n\t" "s_waitcnt vmcnt(13)\n\t" \
   "ds_write_b32 v21, v200 offset:5312\n\t" "ds_write_b32 v21, v201 offset:26432\n\t" "ds_write_b32 v21, v202 offset:15872\n\t" \
   "ds_write_b32 v21, v203 offset:36992\n\t" "s_waitcnt vmcnt(12)\n\t" "ds_write_b32 v21, v216 offset:5376\n\t" \
   "ds_write_b32 v21, v217 offset:26496\n\t" "ds_write_b32 v21, v218 offset:15936\n\t" "ds_write_b32 v21, v219 offset:37056\n\t" \
   "s_waitcnt vmcnt(11)\n\t" "ds_write_b32 v21, v232 offset:5440\n\t" "ds_write_b32 v21, v233 offset:26560\n\t" \
   "ds_write_b32 v21, v234 offset:16000\n\t" "ds_write_b32 v21, v235 offset:37120\n\t" "s_waitcnt vmcnt(10)\n\t" \
   "ds_write_b32 v21, v248 offset:5504\n\t" "ds_write_b32 v21, v249 offset:26624\n\t" "ds_write_b32 v21, v250 offset:16064\n\t" \
   "ds_write_b32 v21, v251 offset:37184\n\t" "buffer_load_dwordx4 v[28:31], v112, %[rw], 0 offen sc1\n\t" "s_waitcnt lgkmcnt(0)\n\t" "s_barrier\n\t" \
   LDSTAMP(8) "ds_read_b64_tr_b16 a[168:169], v22 offset:0\n\t" "ds_read_b64_tr_b16 a[170:171], v22 offset:5248\n\t" \
   "ds_read_b64_tr_b16 a[184:185], v22 offset:8\n\t" "ds_read_b64_tr_b16 a[186:187], v22 offset:5256\n\t" \
   "ds_read_b64_tr_b16 a[200:201], v22 offset:16\n\t" "ds_read_b64_tr_b16 a[202:203], v22 offset:5264\n\t" \
   "ds_read_b64_tr_b16 a[216:217], v22 offset:24\n\t" "ds_read_b64_tr_b16 a[218:219], v22 offset:5272\n\t" \
   "ds_read_b64_tr_b16 a[232:233], v22 offset:32\n\t" "ds_read_b64_tr_b16 a[234:235], v22 offset:5280\n\t" \
   "ds_read_b64_tr_b16 a[248:249], v22 offset:40\n\t" "ds_read_b64_tr_b16 a[250:251], v22 offset:5288\n\t" \
   "ds_read_b64_tr_b16 v[200:201], v22 offset:48\n\t" "ds_read_b64_tr_b16 v[202:203], v22 offset:5296\n\t" \
   "ds_read_b64_tr_b16 v[216:217], v22 offset:56\n\t" "ds_read_b64_tr_b16 v[218:219], v22 offset:5304\n\t" \
   "ds_read_b64_tr_b16 v[232:233], v22 offset:64\n\t" "ds_read_b64_tr_b16 v[234:235], v22 offset:5312\n\t" \
   "ds_read_b64_tr_b16 v[248:249], v22 offset:72\n\t" "ds_read_b64_tr_b16 v[250:251], v22 offset:5320\n\t" "s_waitcnt vmcnt(10)\n\t" \
   "s_waitcnt lgkmcnt(0)\n\t" "s_barrier\n\t" "ds_write_b32 v21, a172 offset:0\n\t" "ds_write_b32 v21, a173 offset:21120\n\t" \
   "ds_write_b32 v21, a174 offset:10560\n\t" "ds_write_b32 v21, a175 offset:31680\n\t" "s_waitcnt vmcnt(9)\n\t" "ds_write_b32 v21, a188 offset:64\n\t" \
   "ds_write_b32 v21, a189 offset:21184\n\t" "ds_write_b32 v21, a190 offset:10624\n\t" "ds_write_b32 v21, a191 offset:31744\n\t" \
   "s_waitcnt vmcnt(8)\n\t" "ds_write_b32 v21, a204 offset:128\n\t" "ds_write_b32 v21, a205 offset:21248\n\t" "ds_write_b32 v21, a206 offset:10688\n\t" \
   "ds_write_b32 v21, a207 offset:31808\n\t" "s_waitcnt vmcnt(7)\n\t" "ds_write_b32 v21, a220 offset:192\n\t" "ds_write_b32 v21, a221 offset:21312\n\t" \
   "ds_write_b32 v21, a222 offset:10752\n\t" "ds_write_b32 v21, a223 offset:31872\n\t" "s_waitcnt vmcnt(6)\n\t" "ds_write_b32 v21, a236 offset:256\n\t" \
   "ds_write_b32 v21, a237 offset:21376\n\t" "ds_write_b32 v21, a238 offset:10816\n\t" "ds_write_b32 v21, a239 offset:31936\n\t" \
   "s_waitcnt vmcnt(5)\n\t" "ds_write_b32 v21, a252 offset:5248\n\t" "ds_write_b32 v21, a253 offset:26368\n\t" \
   "ds_write_b32 v21, a254 offset:15808\n\t" "ds_write_b32 v21, a255 offset:36928\n\t" "s_waitcnt vmcnt(4)\n\t" \
   "ds_write_b32 v21, v204 offset:5312\n\t" "ds_write_b32 v21, v205 offset:26432\n\t" "ds_write_b32 v21, v206 offset:15872\n\t" \
   "ds_write_b32 v21, v207 offset:36992\n\t" "s_waitcnt vmcnt(3)\n\t" "ds_write_b32 v21, v220 offset:5376\n\t" \
   "ds_write_b32 v21, v221 offset:26496\n\t" "ds_write_b32 v21, v222 offset:15936\n\t" "ds_write_b32 v21, v223 offset:37056\n\t" \
   "s_waitcnt vmcnt(2)\n\t" "ds_write_b32 v21, v236 offset:5440\n\t" "ds_write_b32 v21, v237 offset:26560\n\t" \
   "ds_write_b32 v21, v238 offset:16000\n\t" "ds_write_b32 v21, v239 offset:37120\n\t" "s_waitcnt vmcnt(1)\n\t" \
   "ds_write_b32 v21, v252 offset:5504\n\t" "ds_write_b32 v21, v253 offset:26624\n\t" "ds_write_b32 v21, v254 offset:16064\n\t" \
   "ds_write_b32 v21, v255 offset:37184\n\t" "s_waitcnt lgkmcnt(0)\n\t" "s_barrier\n\t" LDSTAMP(9) "ds_read_b64_tr_b16 a[172:173], v22 offset:0\n\t" \
   "ds_read_b64_tr_b16 a[174:175], v22 offset:5248\n\t" "ds_read_b64_tr_b16 a[188:189], v22 offset:8\n\t" \
   "ds_read_b64_tr_b16 a[190:191], v22 offset:5256\n\t" "ds_read_b64_tr_b16 a[204:205], v22 offset:16\n\t" \
   "ds_read_b64_tr_b16 a[206:207], v22 offset:5264\n\t" "ds_read_b64_tr_b16 a[220:221], v22 offset:24\n\t" \
   "ds_read_b64_tr_b16 a[222:223], v22 offset:5272\n\t" "ds_read_b64_tr_b16 a[236:237], v22 offset:32\n\t" \
   "ds_read_b64_tr_b16 a[238:239], v22 offset:5280\n\t" "ds_read_b64_tr_b16 a[252:253], v22 offset:40\n\t" \
   "ds_read_b64_tr_b16 a[254:255], v22 offset:5288\n\t" "ds_read_b64_tr_b16 v[204:205], v22 offset:48\n\t" \
   "ds_read_b64_tr_b16 v[206:207], v22 offset:5296\n\t" "ds_read_b64_tr_b16 v[220:221], v22 offset:56\n\t" \
   "ds_read_b64_tr_b16 v[222:223], v22 offset:5304\n\t" "ds_read_b64_tr_b16 v[236:237], v22 offset:64\n\t" \
   "ds_read_b64_tr_b16 v[238:239], v22 offset:5312\n\t" "ds_read_b64_tr_b16 v[252:253], v22 offset:72\n\t" \
   "ds_read_b64_tr_b16 v[254:255], v22 offset:5320\n\t" "s_waitcnt vmcnt(0) lgkmcnt(0)\n\t" "s_barrier"
#define LDV_CLOB "v24", "v25", "v26", "v32", "v33", "v34", "v35", "v36", "v37", "v38", "v39", "v40", "v41", "v42", "v43", "v44", "v45", "v46", "v47", "v48", "v49", "v50", "v51", "v52", "v53", "v54", "v55", "v56", "v57", "v58", "v59", "v60", "v61", "v62", "v63", "v64", "v65", "v66", "v67", "v68", "v69", "v70", "v71", "v72", "v73", "v74", "v75", "v76", "v77", "v78", "v79", "v80", "v81", "v82", "v83", "v84", "v85", "v86", "v87", "v88", "v89", "v90", "v91", "v92", "v93", "v94", "v95", "v96", "v97", "v98", "v99", "v100", "v101", "v102", "v103", "v104", "v105", "v106", "v107", "v108", "v109", "v110", "v111", "v120", "v121", "v122", "v123", "s40", "s41"
#define LDY_ASM \
   "buffer_load_dword v26, v113, %[rw], 0 offen sc1\n\t" "s_mov_b32 s41, 0\n\t" "buffer_load_dwordx4 a[0:3], v16, %[ra], s41 offen\n\t" \
   "s_mov_b32 s41, 131072\n\t" "buffer_load_dwordx4 a[64:67], v16, %[ra], s41 offen\n\t" "s_mov_b32 s41, 524288\n\t" \
   "buffer_load_dwordx4 a[8:11], v16, %[ra], s41 offen\n\t" "s_mov_b32 s41, 655360\n\t" "buffer_load_dwordx4 a[72:75], v16, %[ra], s41 offen\n\t" \
   "s_mov_b32 s41, 1048576\n\t" "buffer_load_dwordx4 a[16:19], v16, %[ra], s41 offen\n\t" "s_mov_b32 s41, 1179648\n\t" \
   "buffer_load_dwordx4 a[80:83], v16, %[ra], s41 offen\n\t" "s_mov_b32 s41, 1572864\n\t" "buffer_load_dwordx4 a[24:27], v16, %[ra], s41 offen\n\t" \
   "s_mov_b32 s41, 1703936\n\t" "buffer_load_dwordx4 a[88:91], v16, %[ra], s41 offen\n\t" "s_mov_b32 s41, 2097152\n\t" \
   "buffer_load_dwordx4 a[32:35], v16, %[ra], s41 offen\n\t" "s_mov_b32 s41, 2228224\n\t" "buffer_load_dwordx4 a[96:99], v16, %[ra], s41 offen\n\t" \
   "s_mov_b32 s41, 2621440\n\t" "buffer_load_dwordx4 a[40:43], v16, %[ra], s41 offen\n\t" "s_mov_b32 s41, 2752512\n\t" \
   "buffer_load_dwordx4 a[104:107], v16, %[ra], s41 offen\n\t" "s_mov_b32 s41, 3145728\n\t" "buffer_load_dwordx4 a[48:51], v16, %[ra], s41 offen\n\t" \
   "s_mov_b32 s41, 3276800\n\t" "buffer_load_dwordx4 a[112:115], v16, %[ra], s41 offen\n\t" "s_mov_b32 s41, 3670016\n\t" \
   "buffer_load_dwordx4 a[56:59], v16, %[ra], s41 offen\n\t" "s_mov_b32 s41, 3801088\n\t" "buffer_load_dwordx4 a[120:123], v16, %[ra], s41 offen\n\t" \
   "s_waitcnt vmcnt(16)\n\t" "v_add_u32 v26, 1, v26\n\t" "buffer_store_dword v26, v114, %[rw], 0 offen sc1\n\t" "ds_write_b32 v115, v26\n\t" LDSTAMP(0) \
   "s_waitcnt vmcnt(15)\n\t" LDSTAMP(1) "ds_write_b128 v20, a[0:3] offset:0\n\t" "s_mov_b32 s41, 4194304\n\t" \
   "buffer_load_dwordx4 v[192:195], v16, %[ra], s41 offen\n\t" "s_waitcnt vmcnt(15)\n\t" "ds_write_b128 v20, a[64:67] offset:4096\n\t" \
   "s_mov_b32 s41, 4325376\n\t" "buffer_load_dwordx4 v[208:211], v16, %[ra], s41 offen\n\t" "s_waitcnt vmcnt(15)\n\t" \
   "ds_write_b128 v20, a[8:11] offset:8192\n\t" "s_mov_b32 s41, 4718592\n\t" "buffer_load_dwordx4 v[200:203], v16, %[ra], s41 offen\n\t" \
   "s_waitcnt vmcnt(15)\n\t" "ds_write_b128 v20, a[72:75] offset:12288\n\t" "s_mov_b32 s41, 4849664\n\t" \
   "buffer_load_dwordx4 v[216:219], v16, %[ra], s41 offen\n\t" "s_waitcnt vmcnt(15)\n\t" "ds_write_b128 v20, a[16:19] offset:16384\n\t" \
   "s_mov_b32 s41, 262144\n\t" "buffer_load_dwordx4 a[4:7], v16, %[ra], s41 offen\n\t" "s_waitcnt vmcnt(15)\n\t" \
   "ds_write_b128 v20, a[80:83] offset:20480\n\t" "s_mov_b32 s41, 393216\n\t" "buffer_load_dwordx4 a[68:71], v16, %[ra], s41 offen\n\t" \
   "s_waitcnt vmcnt(15)\n\t" "ds_write_b128 v20, a[24:27] offset:24576\n\t" "s_mov_b32 s41, 786432\n\t" \
   "buffer_load_dwordx4 a[12:15], v16, %[ra], s41 offen\n\t" "s_waitcnt vmcnt(15)\n\t" "ds_write_b128 v20, a[88:91] offset:28672\n\t" \
   "s_mov_b32 s41, 917504\n\t" "buffer_load_dwordx4 a[76:79], v16, %[ra], s41 offen\n\t" "s_waitcnt vmcnt(15)\n\t" \
   "ds_write_b128 v20, a[32:35] offset:32768\n\t" "s_mov_b32 s41, 1310720\n\t" "buffer_load_dwordx4 a[20:23], v16, %[ra], s41 offen\n\t" \
   "s_waitcnt vmcnt(15)\n\t" "ds_write_b128 v20, a[96:99] offset:36864\n\t" "s_mov_b32 s41, 1441792\n\t" \
   "buffer_load_dwordx4 a[84:87], v16, %[ra], s41 offen\n\t" "s_waitcnt vmcnt(15)\n\t" "ds_write_b128 v20, a[40:43] offset:40960\n\t" \
   "s_mov_b32 s41, 1835008\n\t" "buffer_load_dwordx4 a[28:31], v16, %[ra], s41 offen\n\t" "s_waitcnt vmcnt(15)\n\t" \
   "ds_write_b128 v20, a[104:107] offset:45056\n\t" "s_mov_b32 s41, 1966080\n\t" "buffer_load_dwordx4 a[92:95], v16, %[ra], s41 offen\n\t" \
   "s_waitcnt vmcnt(15)\n\t" "ds_write_b128 v20, a[48:51] offset:49152\n\t" "s_mov_b32 s41, 2359296\n\t" \
   "buffer_load_dwordx4 a[36:39], v16, %[ra], s41 offen\n\t" "s_waitcnt vmcnt(15)\n\t" "ds_write_b128 v20, a[112:115] offset:53248\n\t" \
   "s_mov_b32 s41, 2490368\n\t" "buffer_load_dwordx4 a[100:103], v16, %[ra], s41 offen\n\t" "s_waitcnt vmcnt(15)\n\t" \
   "ds_write_b128 v20, a[56:59] offset:57344\n\t" "s_mov_b32 s41, 2883584\n\t" "buffer_load_dwordx4 a[44:47], v16, %[ra], s41 offen\n\t" \
   "s_waitcnt vmcnt(15)\n\t" "ds_write_b128 v20, a[120:123] offset:61440\n\t" "s_mov_b32 s41, 3014656\n\t" \
   "buffer_load_dwordx4 a[108:111], v16, %[ra], s41 offen\n\t" "s_waitcnt vmcnt(15)\n\t" "ds_write_b128 v23, v[192:195] offset:0\n\t" \
   "s_mov_b32 s41, 3407872\n\t" "buffer_load_dwordx4 a[52:55], v16, %[ra], s41 offen\n\t" "s_waitcnt vmcnt(15)\n\t" \
   "ds_write_b128 v23, v[208:211] offset:4096\n\t" "s_mov_b32 s41, 3538944\n\t" "buffer_load_dwordx4 a[116:119], v16, %[ra], s41 offen\n\t" \
   "s_waitcnt vmcnt(15)\n\t" "ds_write_b128 v23, v[200:203] offset:8192\n\t" "s_mov_b32 s41, 3932160\n\t" \
   "buffer_load_dwordx4 a[60:63], v16, %[ra], s41 offen\n\t" "s_waitcnt vmcnt(15)\n\t" "ds_write_b128 v23, v[216:219] offset:12288\n\t" \
   "s_mov_b32 s41, 4063232\n\t" "buffer_load_dwordx4 a[124:127], v16, %[ra], s41 offen\n\t" "s_waitcnt lgkmcnt(0)\n\t" "s_barrier\n\t" LDSTAMP(2) \
   "s_lshl_b32 s40, %[p0], 13\n\t" "v_add_u32 v24, s40, v18\n\t" "v_add_u32 v25, s40, v19\n\t" "s_add_u32 s40, s40, 8192\n\t" \
   "s_cmp_eq_u32 s40, 81920\n\t" "s_cselect_b32 s40, 0, s40\n\t" "ds_read_b64_tr_b16 a[0:1], v24 offset:0\n\t" \
   "ds_read_b64_tr_b16 a[2:3], v24 offset:4096\n\t" "ds_read_b64_tr_b16 a[64:65], v25 offset:0\n\t" "ds_read_b64_tr_b16 a[66:67], v25 offset:4096\n\t" \
   "v_add_u32 v24, s40, v18\n\t" "v_add_u32 v25, s40, v19\n\t" "s_add_u32 s40, s40, 8192\n\t" "s_cmp_eq_u32 s40, 81920\n\t" \
   "s_cselect_b32 s40, 0, s40\n\t" "ds_read_b64_tr_b16 a[8:9], v24 offset:0\n\t" "ds_read_b64_tr_b16 a[10:11], v24 offset:4096\n\t" \
   "ds_read_b64_tr_b16 a[72:73], v25 offset:0\n\t" "ds_read_b64_tr_b16 a[74:75], v25 offset:4096\n\t" "v_add_u32 v24, s40, v18\n\t" \
   "v_add_u32 v25, s40, v19\n\t" "s_add_u32 s40, s40, 8192\n\t" "s_cmp_eq_u32 s40, 81920\n\t" "s_cselect_b32 s40, 0, s40\n\t" \
   "ds_read_b64_tr_b16 a[16:17], v24 offset:0\n\t" "ds_read_b64_tr_b16 a[18:19], v24 offset:4096\n\t" "ds_read_b64_tr_b16 a[80:81], v25 offset:0\n\t" \
   "ds_read_b64_tr_b16 a[82:83], v25 offset:4096\n\t" "v_add_u32 v24, s40, v18\n\t" "v_add_u32 v25, s40, v19\n\t" "s_add_u32 s40, s40, 8192\n\t" \
   "s_cmp_eq_u32 s40, 81920\n\t" "s_cselect_b32 s40, 0, s40\n\t" "ds_read_b64_tr_b16 a[24:25], v24 offset:0\n\t" \
   "ds_read_b64_tr_b16 a[26:27], v24 offset:4096\n\t" "ds_read_b64_tr_b16 a[88:89], v25 offset:0\n\t" \
   "ds_read_b64_tr_b16 a[90:91], v25 offset:4096\n\t" "v_add_u32 v24, s40, v18\n\t" "v_add_u32 v25, s40, v19\n\t" "s_add_u32 s40, s40, 8192\n\t" \
   "s_cmp_eq_u32 s40, 81920\n\t" "s_cselect_b32 s40, 0, s40\n\t" "ds_read_b64_tr_b16 a[32:33], v24 offset:0\n\t" \
   "ds_read_b64_tr_b16 a[34:35], v24 offset:4096\n\t" "ds_read_b64_tr_b16 a[96:97], v25 offset:0\n\t" \
   "ds_read_b64_tr_b16 a[98:99], v25 offset:4096\n\t" "v_add_u32 v24, s40, v18\n\t" "v_add_u32 v25, s40, v19\n\t" "s_add_u32 s40, s40, 8192\n\t" \
   "s_cmp_eq_u32 s40, 81920\n\t" "s_cselect_b32 s40, 0, s40\n\t" "ds_read_b64_tr_b16 a[40:41], v24 offset:0\n\t" \
   "ds_read_b64_tr_b16 a[42:43], v24 offset:4096\n\t" "ds_read_b64_tr_b16 a[104:105], v25 offset:0\n\t" \
   "ds_read_b64_tr_b16 a[106:107], v25 offset:4096\n\t" "v_add_u32 v24, s40, v18\n\t" "v_add_u32 v25, s40, v19\n\t" "s_add_u32 s40, s40, 8192\n\t" \
   "s_cmp_eq_u32 s40, 81920\n\t" "s_cselect_b32 s40, 0, s40\n\t" "ds_read_b64_tr_b16 a[48:49], v24 offset:0\n\t" \
   "ds_read_b64_tr_b16 a[50:51], v24 offset:4096\n\t" "ds_read_b64_tr_b16 a[112:113], v25 offset:0\n\t" \
   "ds_read_b64_tr_b16 a[114:115], v25 offset:4096\n\t" "v_add_u32 v24, s40, v18\n\t" "v_add_u32 v25, s40, v19\n\t" "s_add_u32 s40, s40, 8192\n\t" \
   "s_cmp_eq_u32 s40, 81920\n\t" "s_cselect_b32 s40, 0, s40\n\t" "ds_read_b64_tr_b16 a[56:57], v24 offset:0\n\t" \
   "ds_read_b64_tr_b16 a[58:59], v24 offset:4096\n\t" "ds_read_b64_tr_b16 a[120:121], v25 offset:0\n\t" \
   "ds_read_b64_tr_b16 a[122:123], v25 offset:4096\n\t" "v_add_u32 v24, s40, v18\n\t" "v_add_u32 v25, s40, v19\n\t" "s_add_u32 s40, s40, 8192\n\t" \
   "s_cmp_eq_u32 s40, 81920\n\t" "s_cselect_b32 s40, 0, s40\n\t" "ds_read_b64_tr_b16 v[192:193], v24 offset:0\n\t" \
   "ds_read_b64_tr_b16 v[194:195], v24 offset:4096\n\t" "ds_read_b64_tr_b16 v[208:209], v25 offset:0\n\t" \
   "ds_read_b64_tr_b16 v[210:211], v25 offset:4096\n\t" "v_add_u32 v24, s40, v18\n\t" "v_add_u32 v25, s40, v19\n\t" "s_add_u32 s40, s40, 8192\n\t" \
   "s_cmp_eq_u32 s40, 81920\n\t" "s_cselect_b32 s40, 0, s40\n\t" "ds_read_b64_tr_b16 v[200:201], v24 offset:0\n\t" \
   "ds_read_b64_tr_b16 v[202:203], v24 offset:4096\n\t" "ds_read_b64_tr_b16 v[216:217], v25 offset:0\n\t" \
   "ds_read_b64_tr_b16 v[218:219], v25 offset:4096\n\t" "s_waitcnt vmcnt(15)\n\t" "s_waitcnt lgkmcnt(0)\n\t" "s_barrier\n\t" \
   "ds_write_b128 v20, a[4:7] offset:0\n\t" "s_mov_b32 s41, 4456448\n\t" "buffer_load_dwordx4 v[196:199], v16, %[ra], s41 offen\n\t" \
   "s_waitcnt vmcnt(15)\n\t" "ds_write_b128 v20, a[68:71] offset:4096\n\t" "s_mov_b32 s41, 4587520\n\t" \
   "buffer_load_dwordx4 v[212:215], v16, %[ra], s41 offen\n\t" "s_waitcnt vmcnt(15)\n\t" "ds_write_b128 v20, a[12:15] offset:8192\n\t" \
   "s_mov_b32 s41, 4980736\n\t" "buffer_load_dwordx4 v[204:207], v16, %[ra], s41 offen\n\t" "s_waitcnt vmcnt(15)\n\t" \
   "ds_write_b128 v20, a[76:79] offset:12288\n\t" "s_mov_b32 s41, 5111808\n\t" "buffer_load_dwordx4 v[220:223], v16, %[ra], s41 offen\n\t" \
   "s_waitcnt vmcnt(15)\n\t" "ds_write_b128 v20, a[20:23] offset:16384\n\t" "s_mov_b32 s41, 0\n\t" \
   "buffer_load_dwordx4 a[128:131], v17, %[rb], s41 offen\n\t" "s_waitcnt vmcnt(15)\n\t" "ds_write_b128 v20, a[84:87] offset:20480\n\t" \
   "s_mov_b32 s41, 256\n\t" "buffer_load_dwordx4 a[132:135], v17, %[rb], s41 offen\n\t" "s_waitcnt vmcnt(15)\n\t" \
   "ds_write_b128 v20, a[28:31] offset:24576\n\t" "s_mov_b32 s41, 512\n\t" "buffer_load_dwordx4 a[160:163], v17, %[rb], s41 offen\n\t" \
   "s_waitcnt vmcnt(15)\n\t" "ds_write_b128 v20, a[92:95] offset:28672\n\t" "s_mov_b32 s41, 768\n\t" \
   "buffer_load_dwordx4 a[164:167], v17, %[rb], s41 offen\n\t" "s_waitcnt vmcnt(15)\n\t" "ds_write_b128 v20, a[36:39] offset:32768\n\t" \
   "s_mov_b32 s41, 1024\n\t" "buffer_load_dwordx4 a[192:195], v17, %[rb], s41 offen\n\t" "s_waitcnt vmcnt(15)\n\t" \
   "ds_write_b128 v20, a[100:103] offset:36864\n\t" "s_mov_b32 s41, 20480\n\t" "buffer_load_dwordx4 a[196:199], v17, %[rb], s41 offen\n\t" \
   "s_waitcnt vmcnt(15)\n\t" "ds_write_b128 v20, a[44:47] offset:40960\n\t" "s_mov_b32 s41, 20736\n\t" \
   "buffer_load_dwordx4 a[224:227], v17, %[rb], s41 offen\n\t" "s_waitcnt vmcnt(15)\n\t" "ds_write_b128 v20, a[108:111] offset:45056\n\t" \
   "s_mov_b32 s41, 20992\n\t" "buffer_load_dwordx4 a[228:231], v17, %[rb], s41 offen\n\t" "s_waitcnt vmcnt(15)\n\t" \
   "ds_write_b128 v20, a[52:55] offset:49152\n\t" "s_mov_b32 s41, 21248\n\t" "buffer_load_dwordx4 v[224:227], v17, %[rb], s41 offen\n\t" \
   "s_waitcnt vmcnt(15)\n\t" "ds_write_b128 v20, a[116:119] offset:53248\n\t" "s_mov_b32 s41, 21504\n\t" \
   "buffer_load_dwordx4 v[228:231], v17, %[rb], s41 offen\n\t" "s_waitcnt vmcnt(15)\n\t" "ds_write_b128 v20, a[60:63] offset:57344\n\t" \
   "s_mov_b32 s41, 40960\n\t" "buffer_load_dwordx4 a[136:139], v17, %[rb], s41 offen\n\t" "s_waitcnt vmcnt(15)\n\t" \
   "ds_write_b128 v20, a[124:127] offset:61440\n\t" "s_mov_b32 s41, 41216\n\t" "buffer_load_dwordx4 a[140:143], v17, %[rb], s41 offen\n\t" \
   "s_waitcnt vmcnt(15)\n\t" "ds_write_b128 v23, v[196:199] offset:0\n\t" "s_mov_b32 s41, 41472\n\t" \
   "buffer_load_dwordx4 a[168:171], v17, %[rb], s41 offen\n\t" "s_waitcnt vmcnt(15)\n\t" "ds_write_b128 v23, v[212:215] offset:4096\n\t" \
   "s_mov_b32 s41, 41728\n\t" "buffer_load_dwordx4 a[172:175], v17, %[rb], s41 offen\n\t" "s_waitcnt vmcnt(15)\n\t" \
   "ds_write_b128 v23, v[204:207] offset:8192\n\t" "s_mov_b32 s41, 41984\n\t" "buffer_load_dwordx4 a[200:203], v17, %[rb], s41 offen\n\t" \
   "s_waitcnt vmcnt(15)\n\t" "ds_write_b128 v23, v[220:223] offset:12288\n\t" "s_mov_b32 s41, 61440\n\t" \
   "buffer_load_dwordx4 a[204:207], v17, %[rb], s41 offen\n\t" "s_waitcnt lgkmcnt(0)\n\t" "s_barrier\n\t" LDSTAMP(3) "s_lshl_b32 s40, %[p0], 13\n\t" \
   "v_add_u32 v24, s40, v18\n\t" "v_add_u32 v25, s40, v19\n\t" "s_add_u32 s40, s40, 8192\n\t" "s_cmp_eq_u32 s40, 81920\n\t" \
   "s_cselect_b32 s40, 0, s40\n\t" "ds_read_b64_tr_b16 a[4:5], v24 offset:0\n\t" "ds_read_b64_tr_b16 a[6:7], v24 offset:4096\n\t" \
   "ds_read_b64_tr_b16 a[68:69], v25 offset:0\n\t" "ds_read_b64_tr_b16 a[70:71], v25 offset:4096\n\t" "v_add_u32 v24, s40, v18\n\t" \
   "v_add_u32 v25, s40, v19\n\t" "s_add_u32 s40, s40, 8192\n\t" "s_cmp_eq_u32 s40, 81920\n\t" "s_cselect_b32 s40, 0, s40\n\t" \
   "ds_read_b64_tr_b16 a[12:13], v24 offset:0\n\t" "ds_read_b64_tr_b16 a[14:15], v24 offset:4096\n\t" "ds_read_b64_tr_b16 a[76:77], v25 offset:0\n\t" \
   "ds_read_b64_tr_b16 a[78:79], v25 offset:4096\n\t" "v_add_u32 v24, s40, v18\n\t" "v_add_u32 v25, s40, v19\n\t" "s_add_u32 s40, s40, 8192\n\t" \
   "s_cmp_eq_u32 s40, 81920\n\t" "s_cselect_b32 s40, 0, s40\n\t" "ds_read_b64_tr_b16 a[20:21], v24 offset:0\n\t" \
   "ds_read_b64_tr_b16 a[22:23], v24 offset:4096\n\t" "ds_read_b64_tr_b16 a[84:85], v25 offset:0\n\t" \
   "ds_read_b64_tr_b16 a[86:87], v25 offset:4096\n\t" "v_add_u32 v24, s40, v18\n\t" "v_add_u32 v25, s40, v19\n\t" "s_add_u32 s40, s40, 8192\n\t" \
   "s_cmp_eq_u32 s40, 81920\n\t" "s_cselect_b32 s40, 0, s40\n\t" "ds_read_b64_tr_b16 a[28:29], v24 offset:0\n\t" \
   "ds_read_b64_tr_b16 a[30:31], v24 offset:4096\n\t" "ds_read_b64_tr_b16 a[92:93], v25 offset:0\n\t" \
   "ds_read_b64_tr_b16 a[94:95], v25 offset:4096\n\t" "v_add_u32 v24, s40, v18\n\t" "v_add_u32 v25, s40, v19\n\t" "s_add_u32 s40, s40, 8192\n\t" \
   "s_cmp_eq_u32 s40, 81920\n\t" "s_cselect_b32 s40, 0, s40\n\t" "ds_read_b64_tr_b16 a[36:37], v24 offset:0\n\t" \
   "ds_read_b64_tr_b16 a[38:39], v24 offset:4096\n\t" "ds_read_b64_tr_b16 a[100:101], v25 offset:0\n\t" \
   "ds_read_b64_tr_b16 a[102:103], v25 offset:4096\n\t" "v_add_u32 v24, s40, v18\n\t" "v_add_u32 v25, s40, v19\n\t" "s_add_u32 s40, s40, 8192\n\t" \
   "s_cmp_eq_u32 s40, 81920\n\t" "s_cselect_b32 s40, 0, s40\n\t" "ds_read_b64_tr_b16 a[44:45], v24 offset:0\n\t" \
   "ds_read_b64_tr_b16 a[46:47], v24 offset:4096\n\t" "ds_read_b64_tr_b16 a[108:109], v25 offset:0\n\t" \
   "ds_read_b64_tr_b16 a[110:111], v25 offset:4096\n\t" "v_add_u32 v24, s40, v18\n\t" "v_add_u32 v25, s40, v19\n\t" "s_add_u32 s40, s40, 8192\n\t" \
   "s_cmp_eq_u32 s40, 81920\n\t" "s_cselect_b32 s40, 0, s40\n\t" "ds_read_b64_tr_b16 a[52:53], v24 offset:0\n\t" \
   "ds_read_b64_tr_b16 a[54:55], v24 offset:4096\n\t" "ds_read_b64_tr_b16 a[116:117], v25 offset:0\n\t" \
   "ds_read_b64_tr_b16 a[118:119], v25 offset:4096\n\t" "v_add_u32 v24, s40, v18\n\t" "v_add_u32 v25, s40, v19\n\t" "s_add_u32 s40, s40, 8192\n\t" \
   "s_cmp_eq_u32 s40, 81920\n\t" "s_cselect_b32 s40, 0, s40\n\t" "ds_read_b64_tr_b16 a[60:61], v24 offset:0\n\t" \
   "ds_read_b64_tr_b16 a[62:63], v24 offset:4096\n\t" "ds_read_b64_tr_b16 a[124:125], v25 offset:0\n\t" \
   "ds_read_b64_tr_b16 a[126:127], v25 offset:4096\n\t" "v_add_u32 v24, s40, v18\n\t" "v_add_u32 v25, s40, v19\n\t" "s_add_u32 s40, s40, 8192\n\t" \
   "s_cmp_eq_u32 s40, 81920\n\t" "s_cselect_b32 s40, 0, s40\n\t" "ds_read_b64_tr_b16 v[196:197], v24 offset:0\n\t" \
   "ds_read_b64_tr_b16 v[198:199], v24 offset:4096\n\t" "ds_read_b64_tr_b16 v[212:213], v25 offset:0\n\t" \
   "ds_read_b64_tr_b16 v[214:215], v25 offset:4096\n\t" "v_add_u32 v24, s40, v18\n\t" "v_add_u32 v25, s40, v19\n\t" "s_add_u32 s40, s40, 8192\n\t" \
   "s_cmp_eq_u32 s40, 81920\n\t" "s_cselect_b32 s40, 0, s40\n\t" "ds_read_b64_tr_b16 v[204:205], v24 offset:0\n\t" \
   "ds_read_b64_tr_b16 v[206:207], v24 offset:4096\n\t" "ds_read_b64_tr_b16 v[220:221], v25 offset:0\n\t" \
   "ds_read_b64_tr_b16 v[222:223], v25 offset:4096\n\t" "s_waitcnt vmcnt(15)\n\t" "s_waitcnt lgkmcnt(0)\n\t" "s_barrier\n\t" \
   "ds_write_b32 v21, a128 offset:0\n\t" "ds_write_b32 v21, a129 offset:21120\n\t" "ds_write_b32 v21, a130 offset:10560\n\t" \
   "ds_write_b32 v21, a131 offset:31680\n\t" "s_mov_b32 s41, 61696\n\t" "buffer_load_dwordx4 a[232:235], v17, %[rb], s41 offen\n\t" \
   "s_waitcnt vmcnt(15)\n\t" "ds_write_b32 v21, a132 offset:64\n\t" "ds_write_b32 v21, a133 offset:21184\n\t" "ds_write_b32 v21, a134 offset:10624\n\t" \
   "ds_write_b32 v21, a135 offset:31744\n\t" "s_mov_b32 s41, 61952\n\t" "buffer_load_dwordx4 a[236:239], v17, %[rb], s41 offen\n\t" \
   "s_waitcnt vmcnt(15)\n\t" "ds_write_b32 v21, a160 offset:128\n\t" "ds_write_b32 v21, a161 offset:21248\n\t" \
   "ds_write_b32 v21, a162 offset:10688\n\t" "ds_write_b32 v21, a163 offset:31808\n\t" "s_mov_b32 s41, 62208\n\t" \
   "buffer_load_dwordx4 v[232:235], v17, %[rb], s41 offen\n\t" "s_waitcnt vmcnt(15)\n\t" "ds_write_b32 v21, a164 offset:192\n\t" \
   "ds_write_b32 v21, a165 offset:21312\n\t" "ds_write_b32 v21, a166 offset:10752\n\t" "ds_write_b32 v21, a167 offset:31872\n\t" \
   "s_mov_b32 s41, 62464\n\t" "buffer_load_dwordx4 v[236:239], v17, %[rb], s41 offen\n\t" "s_waitcnt vmcnt(15)\n\t" \
   "ds_write_b32 v21, a192 offset:256\n\t" "ds_write_b32 v21, a193 offset:21376\n\t" "ds_write_b32 v21, a194 offset:10816\n\t" \
   "ds_write_b32 v21, a195 offset:31936\n\t" "s_mov_b32 s41, 81920\n\t" "buffer_load_dwordx4 a[144:147], v17, %[rb], s41 offen\n\t" \
   "s_waitcnt vmcnt(15)\n\t" "ds_write_b32 v21, a196 offset:5248\n\t" "ds_write_b32 v21, a197 offset:26368\n\t" \
   "ds_write_b32 v21, a198 offset:15808\n\t" "ds_write_b32 v21, a199 offset:36928\n\t" "s_mov_b32 s41, 82176\n\t" \
   "buffer_load_dwordx4 a[148:151], v17, %[rb], s41 offen\n\t" "s_waitcnt vmcnt(15)\n\t" "ds_write_b32 v21, a224 offset:5312\n\t" \
   "ds_write_b32 v21, a225 offset:26432\n\t" "ds_write_b32 v21, a226 offset:15872\n\t" "ds_write_b32 v21, a227 offset:36992\n\t" \
   "s_mov_b32 s41, 82432\n\t" "buffer_load_dwordx4 a[176:179], v17, %[rb], s41 offen\n\t" "s_waitcnt vmcnt(15)\n\t" \
   "ds_write_b32 v21, a228 offset:5376\n\t" "ds_write_b32 v21, a229 offset:26496\n\t" "ds_write_b32 v21, a230 offset:15936\n\t" \
   "ds_write_b32 v21, a231 offset:37056\n\t" "s_mov_b32 s41, 82688\n\t" "buffer_load_dwordx4 a[180:183], v17, %[rb], s41 offen\n\t" \
   "s_waitcnt vmcnt(15)\n\t" "ds_write_b32 v21, v224 offset:5440\n\t" "ds_write_b32 v21, v225 offset:26560\n\t" \
   "ds_write_b32 v21, v226 offset:16000\n\t" "ds_write_b32 v21, v227 offset:37120\n\t" "s_mov_b32 s41, 82944\n\t" \
   "buffer_load_dwordx4 a[208:211], v17, %[rb], s41 offen\n\t" "s_waitcnt vmcnt(15)\n\t" "ds_write_b32 v21, v228 offset:5504\n\t" \
   "ds_write_b32 v21, v229 offset:26624\n\t" "ds_write_b32 v21, v230 offset:16064\n\t" "ds_write_b32 v21, v231 offset:37184\n\t" \
   "s_mov_b32 s41, 102400\n\t" "buffer_load_dwordx4 a[212:215], v17, %[rb], s41 offen\n\t" "s_waitcnt lgkmcnt(0)\n\t" "s_barrier\n\t" LDSTAMP(4) \
   "ds_read_b64_tr_b16 a[128:129], v22 offset:0\n\t" "ds_read_b64_tr_b16 a[130:131], v22 offset:656\n\t" \
   "ds_read_b64_tr_b16 a[132:133], v22 offset:2624\n\t" "ds_read_b64_tr_b16 a[134:135], v22 offset:3280\n\t" \
   "ds_read_b64_tr_b16 a[160:161], v22 offset:16\n\t" "ds_read_b64_tr_b16 a[162:163], v22 offset:672\n\t" \
   "ds_read_b64_tr_b16 a[164:165], v22 offset:2640\n\t" "ds_read_b64_tr_b16 a[166:167], v22 offset:3296\n\t" \
   "ds_read_b64_tr_b16 a[192:193], v22 offset:32\n\t" "ds_read_b64_tr_b16 a[194:195], v22 offset:688\n\t" \
   "ds_read_b64_tr_b16 a[196:197], v22 offset:2656\n\t" "ds_read_b64_tr_b16 a[198:199], v22 offset:3312\n\t" \
   "ds_read_b64_tr_b16 a[224:225], v22 offset:48\n\t" "ds_read_b64_tr_b16 a[226:227], v22 offset:704\n\t" \
   "ds_read_b64_tr_b16 a[228:229], v22 offset:2672\n\t" "ds_read_b64_tr_b16 a[230:231], v22 offset:3328\n\t" \
   "ds_read_b64_tr_b16 v[224:225], v22 offset:64\n\t" "ds_read_b64_tr_b16 v[226:227], v22 offset:720\n\t" \
   "ds_read_b64_tr_b16 v[228:229], v22 offset:2688\n\t" "ds_read_b64_tr_b16 v[230:231], v22 offset:3344\n\t" "s_waitcnt vmcnt(15)\n\t" \
   "s_waitcnt lgkmcnt(0)\n\t" "s_barrier\n\t" "ds_write_b32 v21, a136 offset:0\n\t" "ds_write_b32 v21, a137 offset:21120\n\t" \
   "ds_write_b32 v21, a138 offset:10560\n\t" "ds_write_b32 v21, a139 offset:31680\n\t" "s_mov_b32 s41, 102656\n\t" \
   "buffer_load_dwordx4 a[240:243], v17, %[rb], s41 offen\n\t" "s_waitcnt vmcnt(15)\n\t" "ds_write_b32 v21, a140 offset:64\n\t" \
   "ds_write_b32 v21, a141 offset:21184\n\t" "ds_write_b32 v21, a142 offset:10624\n\t" "ds_write_b32 v21, a143 offset:31744\n\t" \
   "s_mov_b32 s41, 102912\n\t" "buffer_load_dwordx4 a[244:247], v17, %[rb], s41 offen\n\t" "s_waitcnt vmcnt(15)\n\t" \
   "ds_write_b32 v21, a168 offset:128\n\t" "ds_write_b32 v21, a169 offset:21248\n\t" "ds_write_b32 v21, a170 offset:10688\n\t" \
   "ds_write_b32 v21, a171 offset:31808\n\t" "s_mov_b32 s41, 103168\n\t" "buffer_load_dwordx4 v[240:243], v17, %[rb], s41 offen\n\t" \
   "s_waitcnt vmcnt(15)\n\t" "ds_write_b32 v21, a172 offset:192\n\t" "ds_write_b32 v21, a173 offset:21312\n\t" \
   "ds_write_b32 v21, a174 offset:10752\n\t" "ds_write_b32 v21, a175 offset:31872\n\t" "s_mov_b32 s41, 103424\n\t" \
   "buffer_load_dwordx4 v[244:247], v17, %[rb], s41 offen\n\t" "s_waitcnt vmcnt(15)\n\t" "ds_write_b32 v21, a200 offset:256\n\t" \
   "ds_write_b32 v21, a201 offset:21376\n\t" "ds_write_b32 v21, a202 offset:10816\n\t" "ds_write_b32 v21, a203 offset:31936\n\t" \
   "s_mov_b32 s41, 122880\n\t" "buffer_load_dwordx4 a[152:155], v17, %[rb], s41 offen\n\t" "s_waitcnt vmcnt(15)\n\t" \
   "ds_write_b32 v21, a204 offset:5248\n\t" "ds_write_b32 v21, a205 offset:26368\n\t" "ds_write_b32 v21, a206 offset:15808\n\t" \
   "ds_write_b32 v21, a207 offset:36928\n\t" "s_mov_b32 s41, 123136\n\t" "buffer_load_dwordx4 a[156:159], v17, %[rb], s41 offen\n\t" \
   "s_waitcnt vmcnt(15)\n\t" "ds_write_b32 v21, a232 offset:5312\n\t" "ds_write_b32 v21, a233 offset:26432\n\t" \
   "ds_write_b32 v21, a234 offset:15872\n\t" "ds_write_b32 v21, a235 offset:36992\n\t" "s_mov_b32 s41, 123392\n\t" \
   "buffer_load_dwordx4 a[184:187], v17, %[rb], s41 offen\n\t" "s_waitcnt vmcnt(15)\n\t" "ds_write_b32 v21, a236 offset:5376\n\t" \
   "ds_write_b32 v21, a237 offset:26496\n\t" "ds_write_b32 v21, a238 offset:15936\n\t" "ds_write_b32 v21, a239 offset:37056\n\t" \
   "s_mov_b32 s41, 123648\n\t" "buffer_load_dwordx4 a[188:191], v17, %[rb], s41 offen\n\t" "s_waitcnt vmcnt(15)\n\t" \
   "ds_write_b32 v21, v232 offset:5440\n\t" "ds_write_b32 v21, v233 offset:26560\n\t" "ds_write_b32 v21, v234 offset:16000\n\t" \
   "ds_write_b32 v21, v235 offset:37120\n\t" "s_mov_b32 s41, 123904\n\t" "buffer_load_dwordx4 a[216:219], v17, %[rb], s41 offen\n\t" \
   "s_waitcnt vmcnt(15)\n\t" "ds_write_b32 v21, v236 offset:5504\n\t" "ds_write_b32 v21, v237 offset:26624\n\t" \
   "ds_write_b32 v21, v238 offset:16064\n\t" "ds_write_b32 v21, v239 offset:37184\n\t" "s_mov_b32 s41, 143360\n\t" \
   "buffer_load_dwordx4 a[220:223], v17, %[rb], s41 offen\n\t" "s_waitcnt lgkmcnt(0)\n\t" "s_barrier\n\t" LDSTAMP(5) \
   "ds_read_b64_tr_b16 a[136:137], v22 offset:0\n\t" "ds_read_b64_tr_b16 a[138:139], v22 offset:656\n\t" \
   "ds_read_b64_tr_b16 a[140:141], v22 offset:2624\n\t" "ds_read_b64_tr_b16 a[142:143], v22 offset:3280\n\t" \
   "ds_read_b64_tr_b16 a[168:169], v22 offset:16\n\t" "ds_read_b64_tr_b16 a[170:171], v22 offset:672\n\t" \
   "ds_read_b64_tr_b16 a[172:173], v22 offset:2640\n\t" "ds_read_b64_tr_b16 a[174:175], v22 offset:3296\n\t" \
   "ds_read_b64_tr_b16 a[200:201], v22 offset:32\n\t" "ds_read_b64_tr_b16 a[202:203], v22 offset:688\n\t" \
   "ds_read_b64_tr_b16 a[204:205], v22 offset:2656\n\t" "ds_read_b64_tr_b16 a[206:207], v22 offset:3312\n\t" \
   "ds_read_b64_tr_b16 a[232:233], v22 offset:48\n\t" "ds_read_b64_tr_b16 a[234:235], v22 offset:704\n\t" \
   "ds_read_b64_tr_b16 a[236:237], v22 offset:2672\n\t" "ds_read_b64_tr_b16 a[238:239], v22 offset:3328\n\t" \
   "ds_read_b64_tr_b16 v[232:233], v22 offset:64\n\t" "ds_read_b64_tr_b16 v[234:235], v22 offset:720\n\t" \
   "ds_read_b64_tr_b16 v[236:237], v22 offset:2688\n\t" "ds_read_b64_tr_b16 v[238:239], v22 offset:3344\n\t" "s_waitcnt vmcnt(15)\n\t" \
   "s_waitcnt lgkmcnt(0)\n\t" "s_barrier\n\t" "ds_write_b32 v21, a144 offset:0\n\t" "ds_write_b32 v21, a145 offset:21120\n\t" \
   "ds_write_b32 v21, a146 offset:10560\n\t" "ds_write_b32 v21, a147 offset:31680\n\t" "s_mov_b32 s41, 143616\n\t" \
   "buffer_load_dwordx4 a[248:251], v17, %[rb], s41 offen\n\t" "s_waitcnt vmcnt(15)\n\t" "ds_write_b32 v21, a148 offset:64\n\t" \
   "ds_write_b32 v21, a149 offset:21184\n\t" "ds_write_b32 v21, a150 offset:10624\n\t" "ds_write_b32 v21, a151 offset:31744\n\t" \
   "s_mov_b32 s41, 143872\n\t" "buffer_load_dwordx4 a[252:255], v17, %[rb], s41 offen\n\t" "s_waitcnt vmcnt(15)\n\t" \
   "ds_write_b32 v21, a176 offset:128\n\t" "ds_write_b32 v21, a177 offset:21248\n\t" "ds_write_b32 v21, a178 offset:10688\n\t" \
   "ds_write_b32 v21, a179 offset:31808\n\t" "s_mov_b32 s41, 144128\n\t" "buffer_load_dwordx4 v[248:251], v17, %[rb], s41 offen\n\t" \
   "s_waitcnt vmcnt(15)\n\t" "ds_write_b32 v21, a180 offset:192\n\t" "ds_write_b32 v21, a181 offset:21312\n\t" \
   "ds_write_b32 v21, a182 offset:10752\n\t" "ds_write_b32 v21, a183 offset:31872\n\t" "s_mov_b32 s41, 144384\n\t" \
   "buffer_load_dwordx4 v[252:255], v17, %[rb], s41 offen\n\t" "s_waitcnt vmcnt(15)\n\t" "ds_write_b32 v21, a208 offset:256\n\t" \
   "ds_write_b32 v21, a209 offset:21376\n\t" "ds_write_b32 v21, a210 offset:10816\n\t" "ds_write_b32 v21, a211 offset:31936\n\t" \
   "s_waitcnt vmcnt(14)\n\t" "ds_write_b32 v21, a212 offset:5248\n\t" "ds_write_b32 v21, a213 offset:26368\n\t" \
   "ds_write_b32 v21, a214 offset:15808\n\t" "ds_write_b32 v21, a215 offset:36928\n\t" "s_waitcnt vmcnt(13)\n\t" \
   "ds_write_b32 v21, a240 offset:5312\n\t" "ds_write_b32 v21, a241 offset:26432\n\t" "ds_write_b32 v21, a242 offset:15872\n\t" \
   "ds_write_b32 v21, a243 offset:36992\n\t" "s_waitcnt vmcnt(12)\n\t" "ds_write_b32 v21, a244 offset:5376\n\t" \
   "ds_write_b32 v21, a245 offset:26496\n\t" "ds_write_b32 v21, a246 offset:15936\n\t" "ds_write_b32 v21, a247 offset:37056\n\t" \
   "s_waitcnt vmcnt(11)\n\t" "ds_write_b32 v21, v240 offset:5440\n\t" "ds_write_b32 v21, v241 offset:26560\n\t" \
   "ds_write_b32 v21, v242 offset:16000\n\t" "ds_write_b32 v21, v243 offset:37120\n\t" "s_waitcnt vmcnt(10)\n\t" \
   "ds_write_b32 v21, v244 offset:5504\n\t" "ds_write_b32 v21, v245 offset:26624\n\t" "ds_write_b32 v21, v246 offset:16064\n\t" \
   "ds_write_b32 v21, v247 offset:37184\n\t" "buffer_load_dwordx4 v[28:31], v112, %[rw], 0 offen sc1\n\t" "s_waitcnt lgkmcnt(0)\n\t" "s_barrier\n\t" \
   LDSTAMP(6) "ds_read_b64_tr_b16 a[144:145], v22 offset:0\n\t" "ds_read_b64_tr_b16 a[146:147], v22 offset:656\n\t" \
   "ds_read_b64_tr_b16 a[148:149], v22 offset:2624\n\t" "ds_read_b64_tr_b16 a[150:151], v22 offset:3280\n\t" \
   "ds_read_b64_tr_b16 a[176:177], v22 offset:16\n\t" "ds_read_b64_tr_b16 a[178:179], v22 offset:672\n\t" \
   "ds_read_b64_tr_b16 a[180:181], v22 offset:2640\n\t" "ds_read_b64_tr_b16 a[182:183], v22 offset:3296\n\t" \
   "ds_read_b64_tr_b16 a[208:209], v22 offset:32\n\t" "ds_read_b64_tr_b16 a[210:211], v22 offset:688\n\t" \
   "ds_read_b64_tr_b16 a[212:213], v22 offset:2656\n\t" "ds_read_b64_tr_b16 a[214:215], v22 offset:3312\n\t" \
   "ds_read_b64_tr_b16 a[240:241], v22 offset:48\n\t" "ds_read_b64_tr_b16 a[242:243], v22 offset:704\n\t" \
   "ds_read_b64_tr_b16 a[244:245], v22 offset:2672\n\t" "ds_read_b64_tr_b16 a[246:247], v22 offset:3328\n\t" \
   "ds_read_b64_tr_b16 v[240:241], v22 offset:64\n\t" "ds_read_b64_tr_b16 v[242:243], v22 offset:720\n\t" \
   "ds_read_b64_tr_b16 v[244:245], v22 offset:2688\n\t" "ds_read_b64_tr_b16 v[246:247], v22 offset:3344\n\t" "s_waitcnt vmcnt(10)\n\t" \
   "s_waitcnt lgkmcnt(0)\n\t" "s_barrier\n\t" "ds_write_b32 v21, a152 offset:0\n\t" "ds_write_b32 v21, a153 offset:21120\n\t" \
   "ds_write_b32 v21, a154 offset:10560\n\t" "ds_write_b32 v21, a155 offset:31680\n\t" "s_waitcnt vmcnt(9)\n\t" "ds_write_b32 v21, a156 offset:64\n\t" \
   "ds_write_b32 v21, a157 offset:21184\n\t" "ds_write_b32 v21, a158 offset:10624\n\t" "ds_write_b32 v21, a159 offset:31744\n\t" \
   "s_waitcnt vmcnt(8)\n\t" "ds_write_b32 v21, a184 offset:128\n\t" "ds_write_b32 v21, a185 offset:21248\n\t" "ds_write_b32 v21, a186 offset:10688\n\t" \
   "ds_write_b32 v21, a187 offset:31808\n\t" "s_waitcnt vmcnt(7)\n\t" "ds_write_b32 v21, a188 offset:192\n\t" "ds_write_b32 v21, a189 offset:21312\n\t" \
   "ds_write_b32 v21, a190 offset:10752\n\t" "ds_write_b32 v21, a191 offset:31872\n\t" "s_waitcnt vmcnt(6)\n\t" "ds_write_b32 v21, a216 offset:256\n\t" \
   "ds_write_b32 v21, a217 offset:21376\n\t" "ds_write_b32 v21, a218 offset:10816\n\t" "ds_write_b32 v21, a219 offset:31936\n\t" \
   "s_waitcnt vmcnt(5)\n\t" "ds_write_b32 v21, a220 offset:5248\n\t" "ds_write_b32 v21, a221 offset:26368\n\t" \
   "ds_write_b32 v21, a222 offset:15808\n\t" "ds_write_b32 v21, a223 offset:36928\n\t" "s_waitcnt vmcnt(4)\n\t" \
   "ds_write_b32 v21, a248 offset:5312\n\t" "ds_write_b32 v21, a249 offset:26432\n\t" "ds_write_b32 v21, a250 offset:15872\n\t" \
   "ds_write_b32 v21, a251 offset:36992\n\t" "s_waitcnt vmcnt(3)\n\t" "ds_write_b32 v21, a252 offset:5376\n\t" \
   "ds_write_b32 v21, a253 offset:26496\n\t" "ds_write_b32 v21, a254 offset:15936\n\t" "ds_write_b32 v21, a255 offset:37056\n\t" \
   "s_waitcnt vmcnt(2)\n\t" "ds_write_b32 v21, v248 offset:5440\n\t" "ds_write_b32 v21, v249 offset:26560\n\t" \
   "ds_write_b32 v21, v250 offset:16000\n\t" "ds_write_b32 v21, v251 offset:37120\n\t" "s_waitcnt vmcnt(1)\n\t" \
   "ds_write_b32 v21, v252 offset:5504\n\t" "ds_write_b32 v21, v253 offset:26624\n\t" "ds_write_b32 v21, v254 offset:16064\n\t" \
   "ds_write_b32 v21, v255 offset:37184\n\t" "s_waitcnt lgkmcnt(0)\n\t" "s_barrier\n\t" LDSTAMP(7) "ds_read_b64_tr_b16 a[152:153], v22 offset:0\n\t" \
   "ds_read_b64_tr_b16 a[154:155], v22 offset:656\n\t" "ds_read_b64_tr_b16 a[156:157], v22 offset:2624\n\t" \
   "ds_read_b64_tr_b16 a[158:159], v22 offset:3280\n\t" "ds_read_b64_tr_b16 a[184:185], v22 offset:16\n\t" \
   "ds_read_b64_tr_b16 a[186:187], v22 offset:672\n\t" "ds_read_b64_tr_b16 a[188:189], v22 offset:2640\n\t" \
   "ds_read_b64_tr_b16 a[190:191], v22 offset:3296\n\t" "ds_read_b64_tr_b16 a[216:217], v22 offset:32\n\t" \
   "ds_read_b64_tr_b16 a[218:219], v22 offset:688\n\t" "ds_read_b64_tr_b16 a[220:221], v22 offset:2656\n\t" \
   "ds_read_b64_tr_b16 a[222:223], v22 offset:3312\n\t" "ds_read_b64_tr_b16 a[248:249], v22 offset:48\n\t" \
   "ds_read_b64_tr_b16 a[250:251], v22 offset:704\n\t" "ds_read_b64_tr_b16 a[252:253], v22 offset:2672\n\t" \
   "ds_read_b64_tr_b16 a[254:255], v22 offset:3328\n\t" "ds_read_b64_tr_b16 v[248:249], v22 offset:64\n\t" \
   "ds_read_b64_tr_b16 v[250:251], v22 offset:720\n\t" "ds_read_b64_tr_b16 v[252:253], v22 offset:2688\n\t" \
   "ds_read_b64_tr_b16 v[254:255], v22 offset:3344\n\t" "s_waitcnt vmcnt(0) lgkmcnt(0)\n\t" "s_barrier"
#define LDY_CLOB "v24", "v25", "v26", "v32", "v33", "v34", "v35", "v36", "v37", "v38", "v39", "v40", "v41", "v42", "v43", "v44", "v45", "v46", "v47", "v48", "v49", "v50", "v51", "v52", "v53", "v54", "v55", "v56", "v57", "v58", "v59", "v60", "v61", "v62", "v63", "v64", "v65", "v66", "v67", "v68", "v69", "v70", "v71", "v72", "v73", "v74", "v75", "v76", "v77", "v78", "v79", "v80", "v81", "v82", "v83", "v84", "v85", "v86", "v87", "v88", "v89", "v90", "v91", "v92", "v93", "v94", "v95", "v96", "v97", "v98", "v99", "v100", "v101", "v102", "v103", "v104", "v105", "v106", "v107", "v108", "v109", "v110", "v111", "v120", "v121", "v122", "v123", "s40", "s41"
#define W_OPS "{a[0:31]}"(w[0]), "{a[32:63]}"(w[1]), "{a[64:95]}"(w[2]), "{a[96:127]}"(w[3]), "{a[128:159]}"(w[4]), "{a[160:191]}"(w[5]), \
              "{a[192:223]}"(w[6]), "{a[224:255]}"(w[7]), "{v[192:223]}"(w[8]), "{v[224:255]}"(w[9])

__device__ __forceinline__ int mem_lane(int lane) { return lane * 16; }
__device__ __forceinline__ int pair_of(int g) { return (g & 1) * 2 + (g >> 1); }

__device__ __forceinline__ void rec_p1(int wv_, const v32u (&w)[10], unsigned char* ws, const Pool& pool, int layer, int h, int xs, RecTune tn, unsigned long long* stamps) {
  const Clk0 c0 = clk_sample();
  const unsigned lds0 = (unsigned)(uintptr_t)(__attribute__((address_space(3))) unsigned char*)dyn_lds;
  const int lane = lane_id(), wv = wv_, g = lane >> 4, r = lane & 15;
  const v4u rs = make_rs(ws, (unsigned)OFF_END);
  const int p0 = wv < 3 ? 3 * wv : 8;
  v16u ad, la = (unsigned)lane;
  const int kqo = (int)(wv < 2 ? OFF_KF : OFF_QF) + ((h * RT + (wv & 1)) * 4 * 64 + lane) * 16;
  const int progo = (wv == 0 && lane == 0) ? prog_off(layer, 0, h) : OOB;
  {
    const int p2 = wv < 3 ? p0 + 2 : 0;
    ad[0] = W_MEM + (p0 >> 1) * WIN + (p0 & 1) * 1024 + mem_lane(lane);
    ad[1] = W_MEM + ((p0 + 1) >> 1) * WIN + ((p0 + 1) & 1) * 1024 + mem_lane(lane);
    ad[2] = W_MEM + (p2 >> 1) * WIN + (p2 & 1) * 1024 + mem_lane(lane);
    ad[3] = lane == 0 ? W_MISC + h * 64 + wv * 4 : OOB;
    ad[4] = copy_off(wv) + (h >> 2) * WIN + (h & 3) * 512 + (g >> 1) * 256 + (2 * (r & 7) + (r >> 3)) * 16 + (g & 1) * 8;
    ad[5] = g < 2 ? copy_off(wv + 4 * (r >> 3)) + (8 + (h >> 4)) * WIN + ((h >> 3) & 1) * 1024 + (((h >> 1) & 3) * 16 + (h & 1) * 8 + (r & 7)) * 16 + g * 8 : OOB;
    ad[6] = (r & 1) ? 0xEEEEu : 0x4444u;
    ad[7] = lds0 + (32 * wv + r) * 16 + g * 4;
    ad[8] = lds0 + (4 * g + (r >> 2)) * 16 + (r & 1) * 8;
    ad[9] = lds0 + L_M2 + wv * 3072 + lane * 16;
    ad[10] = kqo;
    ad[11] = M_TM + xs * 128 + (lane & 1) * 12;
    ad[12] = kqf_off(layer, 0, h) + (lane & 3) * 4;
    ad[13] = wv > 1 ? (int)OFF_PXF + ((h * 2 + (wv & 1)) * 64 + lane) * 16 : OOB;
    ad[14] = (g < 2 && wv > 1) ? (int)OFF_PXM + (((h >> 2) * 2 + (wv & 1)) * 64 + (h & 3) * 16 + r) * 16 + g * 8 : OOB;
    ad[15] = progo;
#pragma unroll
    for (int i = 0; i < 10; ++i) { int p = p0 + i; p = p >= 10 ? p - 10 : p; la[i] = lds0 + p * 1024 + lane * 16; }
    la[10] = lds0 + L_KQ + lane * 16;
    la[12] = lds0 + REC_ST + wv * 8;
    asm volatile("v_mov_b32 %0, 0xceabfb8f" : "=v"(la[13]));
    la[14] = 16 * (wv & 1) + r - 4 * g;
    la[15] = lds0 + L_KQ + (wv & 1) * 4096 + lane * 16;
  }
  __builtin_amdgcn_s_waitcnt(0x0F70);
  if (layer) __builtin_amdgcn_s_sleep(64);
  v4u clk = clk_init(c0);
  unsigned long long stat;
  asm volatile(P1_ASM
               : "=&{s[76:77]}"(stat), "+{s[56:59]}"(clk)
               : W_OPS, "{v[128:143]}"(ad), "{v[112:127]}"(la), [rs] "s"(rs), [sel] "s"(pool.sel), [pool] "s"(pool.base), [xs] "s"(xs),
                 [lad0] "s"(ladder(xs, xs & 1)), [s3] "s"(pool.s3), [m1] "s"(tn.m1), [l2s] "s"(lds0 + L_M2 + wv * 3072),
                 [hi8] "s"(0xFF00FF00FF00FF00ull), [wv] "s"(wv), [kf0] "s"((int)OFF_KF + h * RT * 4096),
                 [esc] "s"(pool.base + (NSEG - 1) * REG)
               : P1_CLOB RST_CLOB, "vcc", "scc", "m0", "memory");
  LDSBAR();
  {
    const unsigned one = 1u;
    const int so = (NSEG - 1) * 4096;
    VM_ST32(one, progo, so);
    VM_WAIT(0);
  }
}

__device__ __forceinline__ void rec_pv(int wv_, const v32u (&w)[10], unsigned char* ws, const Pool& pool, int layer, int h, int xs, RecTune tn, unsigned long long* stamps) {
  const Clk0 c0 = clk_sample();
  const unsigned lds0 = (unsigned)(uintptr_t)(__attribute__((address_space(3))) unsigned char*)dyn_lds;
  const int lane = lane_id(), wv = wv_, g = lane >> 4, r = lane & 15;
  const v4u rs = make_rs(ws, (unsigned)OFF_END);
  const int p0 = wv < 3 ? 3 * wv : 8, p2 = wv < 3 ? p0 + 2 : 0;
  v16u ad = (unsigned)lane, la = (unsigned)lane;
  ad[0] = W_MEM + (p0 >> 1) * WIN + (p0 & 1) * 1024 + mem_lane(lane);
  ad[1] = W_MEM + ((p0 + 1) >> 1) * WIN + ((p0 + 1) & 1) * 1024 + mem_lane(lane);
  ad[2] = W_MEM + (p2 >> 1) * WIN + (p2 & 1) * 1024 + mem_lane(lane);
  ad[3] = (r & 8) ? 0xEEEEu : 0x4444u;
  ad[4] = (wv * 10 + (g >> 1)) * 16384 + h * 256 + r * 16 + (g & 1) * 8;
  ad[5] = h * 16384 + wv * 2048 + lane * 16;
  ad[6] = (r & 1) ? 0xEEEEu : 0x4444u;
  ad[7] = lds0 + (32 * wv + r) * 16 + pair_of(g) * 4;
  ad[8] = lds0 + (4 * g + (r >> 2)) * 16 + (r & 1) * 8;
  ad[11] = M_TM + (4 + xs) * 128 + (lane & 1) * 12;
  ad[15] = (wv == 0 && lane == 0) ? prog_off(layer, 0, 32 + h) : OOB;
#pragma unroll
  for (int i = 0; i < 10; ++i) { int p = p0 + i; p = p >= 10 ? p - 10 : p; la[i] = lds0 + p * 1024 + lane * 16; }
  la[12] = lds0 + REC_ST + wv * 8;
  __builtin_amdgcn_s_waitcnt(0x0F70);
  if (layer) __builtin_amdgcn_s_sleep(64);
  v4u clk = clk_init(c0);
  unsigned long long stat;
  asm volatile(PV_ASM
               : "=&{s[76:77]}"(stat), "+{s[56:59]}"(clk)
               : W_OPS, "{v[128:143]}"(ad), "{v[112:127]}"(la), [rs] "s"(rs), [sel] "s"(pool.sel), [pool] "s"(pool.base), [xs] "s"(xs),
                 [lad0] "s"(ladder(xs, xs & 1)), [s3] "s"(pool.s3), [m1c] "s"(tn.m1 + tn.c2), [esc] "s"(pool.base + (NSEG - 1) * REG),
                 [ysel] "s"(pool.ysel), [yb] "s"(pool.base + P_Y)
               : PV_CLOB RST_CLOB, "vcc", "scc", "memory");
}

#define TSTART 5000
#define TD1 900u
#define TR2 400u
#define TEMA 1
#define TEMB 2
#define TBUP 48
#define TBDN 6
#define TLATE 520
#define TSEC 50
__device__ __forceinline__ unsigned track_dur(unsigned e, int m, int rob, int s) {
  const int lo = (int)e - (s < 8 ? 64 : (rob & 255)), hi = (int)e + (rob >> 8);
  if (s >= 8) m = (int)e + ((m - (int)e + (m > (int)e ? (1 << TEMA) - 1 : 0)) >> (m > (int)e ? TEMA : TEMB));
  return (unsigned)min(max(m, lo), hi);
}
#define TTS 200
#define TTU 240
__device__ __forceinline__ void rec_sched(unsigned char* ws, const Pool& pool, int layer, int c16, int xs, RecTune tn, unsigned long long* stamps) {
  const Clk0 c0 = clk_sample();
  const unsigned lds0 = (unsigned)(uintptr_t)(__attribute__((address_space(3))) unsigned char*)dyn_lds;
  const int lane = lane_id();
  const v4u rs = make_rs(ws, (unsigned)OFF_END);
  unsigned long long stat = 0;
  const int sm = M_STAMP + min(lane, 39) * 64, sp = W_MISC + (lane & 31) * 64;
  const int yo = M_TM + xs * 128, oc = (c16 == 0 && lane < 16) ? M_TM + lane * 128 : OOB;
  const unsigned cv = flag_finish<2>(rs, kqf_off(layer, 0, 32 + (lane & 31)), 0xFFFFFFFFu);
  v4u clk = clk_init(c0);
  PH(3 + layer * 6)
  unsigned r2a = TR2, d1a = TD1, dd = TD1 + tn.m2, tp = 0, tm = 0, anchor = clk.x - dd;
  unsigned rr = (unsigned)min(max(wave_imax((int)(cv * 8u - clk.x)) + TSTART, 300), 4000);
  int bonus = 0;
  bool clean = false;
#pragma unroll 1
  for (int s = 0; s < NSEG - 1; ++s) {
    const int rgm = pool.region((s + NSEG - 1) & (NSEG - 1)), rg = pool.region(s);
    const unsigned slot = lds0 + L_T + (s & 1) * 32, ao = anchor;
    unsigned b2 = 1u;
    v4u b1 = {1u, 1u, 1u, 1u};
    if (s > 0) {
      asm volatile(WAIT_ASM : "+{s[56:59]}"(clk) : [ta] "s"(tm + TTS) : WAIT_CLOB, "scc", "memory");
      VM_LD32(b2, sm, rgm);
      VM_LD128(b1, sp, rgm, 0);
      asm volatile("s_waitcnt vmcnt(0)" : "+v"(b2), "+v"(b1)::"memory");
      anchor = tm;
      if (__builtin_expect(__any(b2 == 0xFFFFFFFFu || (int)(b2 - (tm + tn.m1 - tn.vis)) > 0), 0)) {
        STAT(3);
        for (unsigned spins = 0; __any(b2 == 0xFFFFFFFFu) && spins < SPIN_CAP; ++spins) {
          __builtin_amdgcn_s_sleep(2);
          VM_LD32(b2, sm, rgm);
          VM_WAIT1(0, b2);
        }
        const int d = wave_imax((int)(b2 - tm)) + tn.vis - tn.m1;
        anchor = tm + (d <= 0 ? 0 : (d <= tn.dlt ? tn.dlt + TSEC : d + TLATE));
      }
    }
    const unsigned tpo = tp, tmo = tm;
    tp = anchor + dd;
    tm = nosent(tp + rr);
    LDS_V4(slot) = v4u{tp, tm, (unsigned)(s + 1), anchor};
    if (anchor != tmo && s > 2) bonus = TBUP; else bonus = max(bonus - TBDN, 0);
    dd = d1a + tn.m2 + bonus;
    rr = r2a + bonus;
    {
      const v4u wq = v4u{tm, dd, rr, dd + rr};
      VM_ST128(wq, oc, rg);
    }
    if (s > 1 && !__any((b2 & 1u) != 0u)) r2a = track_dur(r2a, wave_imax((int)(b2 - tpo)), tn.rob, s);
    if (s > 1 && clean && !__any(((b1.x | b1.y | b1.z | b1.w) & 1u) != 0u))
      d1a = track_dur(d1a, wave_imax(max(max((int)(b1.x - ao), (int)(b1.y - ao)), max((int)(b1.z - ao), (int)(b1.w - ao)))), tn.rob, s);
    clean = anchor == tmo;
    while ((int)(tp + 40u - rtc() * 8u) > 0 && (int)(tp - rtc() * 8u) < 32000) __builtin_amdgcn_s_sleep(1);
    v4u lw;
    VM_LD128(lw, yo, rg, 0);
    VM_WAIT1(0, lw);
    const unsigned l0 = (unsigned)__builtin_amdgcn_readfirstlane(lw.x), l1 = (unsigned)__builtin_amdgcn_readfirstlane(lw.y),
                   l2 = (unsigned)__builtin_amdgcn_readfirstlane(lw.z);
    if (max(max(l0, l1), l2) != 0xFFFFFFFFu) { tm = l0; dd = l1; rr = l2; }
  }
}

__device__ __forceinline__ void rec_fin(unsigned char* ws, const Pool& pool, int layer, int c16, int xs, RecTune tn, unsigned long long* stamps) {
  const Clk0 c0 = clk_sample();
  const unsigned lds0 = (unsigned)(uintptr_t)(__attribute__((address_space(3))) unsigned char*)dyn_lds;
  const int lane = lane_id(), g = lane >> 4, r = lane & 15;
  const v4u rs = make_rs(ws, (unsigned)OFF_END);
  v8u ad;
  ad[0] = lane * 16;
  ad[1] = g * 256 + r * 8;
  ad[2] = (r & 1) ? 0x03020706u : 0x05040100u;
  ad[3] = (lane & 31) * 64;
  ad[4] = W_MEM + copy_off(((r >> 1) & 1) * 5) + (c16 >> 3) * WIN + (c16 & 7) * 256 + (2 * (2 * g + (r & 1)) + (r >> 3)) * 16 + ((r >> 2) & 1) * 8;
  ad[5] = lane == 0 ? M_STAMP + c16 * 64 : OOB;
  ad[6] = (r & 1) ? 0xEEEEu : 0x4444u;
  ad[7] = lds0 + L_T;
  __builtin_amdgcn_s_sleep(64);
  v4u clk = clk_init(c0);
  unsigned long long stat;
  asm volatile(FIN_ASM
               : "=&{s[76:77]}"(stat), "+{s[56:59]}"(clk), "+{v[16:23]}"(ad)
               : [rs] "s"(rs), [sel] "s"(pool.sel), [pool] "s"(pool.base), [xs] "s"(xs), [lad0] "s"(ladder(xs, xs & 1)), [s3] "s"(pool.s3),
                 [su] "s"((int)OFF_UF + c16 * 32768), [yb] "s"(pool.base + P_Y + c16 * 16384), [ysel] "s"(pool.ysel), [tu] "s"(TTU),
                 [stl] "s"(lds0 + REC_ST - 32), [uf] "s"(kqf_off(layer, 0, 32))
               : FIN_CLOB RST_CLOB, "vcc", "scc", "memory");
}

constexpr int PJ_BUF = 43 * 1024, PJ_X = 16384, PJ_LDS = PJ_X + 3 * PJ_BUF;
#define KQ_ASM \
   "s_mov_b32 s44, %[rdy]\n\t" "s_mov_b32 s45, 0\n\t" "s_mov_b32 s46, 0\n\t" "s_cmp_eq_u32 s44, -1\n\t" "s_cbranch_scc1 68f\n\t" \
   "s_mul_i32 s52, s45, 1536\n\t" "s_mul_i32 s51, s45, 40960\n\t" "s_add_u32 m0, s46, %[ldx]\n\t" "s_mov_b32 s53, s46\n\t" \
   "buffer_load_dwordx4 v16, %[rs], s51 offen offset:0 sc1 lds\n\t" "buffer_load_dwordx4 v16, %[rs], s51 offen offset:1024 sc1 lds\n\t" \
   "buffer_load_dwordx4 v16, %[rs], s51 offen offset:2048 sc1 lds\n\t" "buffer_load_dwordx4 v16, %[rs], s51 offen offset:3072 sc1 lds\n\t" \
   "s_add_u32 m0, m0, 4096\n\t" "s_add_u32 s51, s51, 4096\n\t" "buffer_load_dwordx4 v16, %[rs], s51 offen offset:0 sc1 lds\n\t" \
   "buffer_load_dwordx4 v16, %[rs], s51 offen offset:1024 sc1 lds\n\t" "buffer_load_dwordx4 v16, %[rs], s51 offen offset:2048 sc1 lds\n\t" \
   "buffer_load_dwordx4 v16, %[rs], s51 offen offset:3072 sc1 lds\n\t" "s_add_u32 m0, m0, 4096\n\t" "s_add_u32 s51, s51, 4096\n\t" \
   "buffer_load_dwordx4 v16, %[rs], s51 offen offset:0 sc1 lds\n\t" "buffer_load_dwordx4 v16, %[rs], s51 offen offset:1024 sc1 lds\n\t" \
   "s_add_u32 m0, s53, %[ld1]\n\t" "s_nop 0\n\t" "buffer_load_dwordx4 v17, %[rs], s52 offen sc1 lds\n\t" "s_mov_b32 s45, 1\n\t" \
   "s_mov_b32 s46, 44032\n\t" "s_mul_i32 s52, s45, 1536\n\t" "s_mul_i32 s51, s45, 40960\n\t" "s_add_u32 m0, s46, %[ldx]\n\t" "s_mov_b32 s53, s46\n\t" \
   "buffer_load_dwordx4 v16, %[rs], s51 offen offset:0 sc1 lds\n\t" "buffer_load_dwordx4 v16, %[rs], s51 offen offset:1024 sc1 lds\n\t" \
   "buffer_load_dwordx4 v16, %[rs], s51 offen offset:2048 sc1 lds\n\t" "buffer_load_dwordx4 v16, %[rs], s51 offen offset:3072 sc1 lds\n\t" \
   "s_add_u32 m0, m0, 4096\n\t" "s_add_u32 s51, s51, 4096\n\t" "buffer_load_dwordx4 v16, %[rs], s51 offen offset:0 sc1 lds\n\t" \
   "buffer_load_dwordx4 v16, %[rs], s51 offen offset:1024 sc1 lds\n\t" "buffer_load_dwordx4 v16, %[rs], s51 offen offset:2048 sc1 lds\n\t" \
   "buffer_load_dwordx4 v16, %[rs], s51 offen offset:3072 sc1 lds\n\t" "s_add_u32 m0, m0, 4096\n\t" "s_add_u32 s51, s51, 4096\n\t" \
   "buffer_load_dwordx4 v16, %[rs], s51 offen offset:0 sc1 lds\n\t" "buffer_load_dwordx4 v16, %[rs], s51 offen offset:1024 sc1 lds\n\t" \
   "s_add_u32 m0, s53, %[ld1]\n\t" "s_nop 0\n\t" "buffer_load_dwordx4 v17, %[rs], s52 offen sc1 lds\n\t" "68:\n\t" "v_mov_b32 v80, 0x3f803f80\n\t" \
   "v_mov_b32 v81, 0x3f803f80\n\t" "v_mov_b32 v82, 0x3f803f80\n\t" "v_mov_b32 v83, 0x3f803f80\n\t" "v_mov_b32 v31, 1\n\t" "s_nop 1\n\t" \
   "v_mfma_f32_16x16x32_bf16 v[32:35], a[0:3], v[80:83], 0\n\t" "v_mfma_f32_16x16x32_bf16 v[36:39], a[4:7], v[80:83], 0\n\t" \
   "v_mfma_f32_16x16x32_bf16 v[40:43], a[8:11], v[80:83], 0\n\t" "v_mfma_f32_16x16x32_bf16 v[44:47], a[12:15], v[80:83], 0\n\t" \
   "v_mfma_f32_16x16x32_bf16 v[32:35], a[16:19], v[80:83], v[32:35]\n\t" "v_mfma_f32_16x16x32_bf16 v[36:39], a[20:23], v[80:83], v[36:39]\n\t" \
   "v_mfma_f32_16x16x32_bf16 v[40:43], a[24:27], v[80:83], v[40:43]\n\t" "v_mfma_f32_16x16x32_bf16 v[44:47], a[28:31], v[80:83], v[44:47]\n\t" \
   "v_mfma_f32_16x16x32_bf16 v[32:35], a[32:35], v[80:83], v[32:35]\n\t" "v_mfma_f32_16x16x32_bf16 v[36:39], a[36:39], v[80:83], v[36:39]\n\t" \
   "v_mfma_f32_16x16x32_bf16 v[40:43], a[40:43], v[80:83], v[40:43]\n\t" "v_mfma_f32_16x16x32_bf16 v[44:47], a[44:47], v[80:83], v[44:47]\n\t" \
   "v_mfma_f32_16x16x32_bf16 v[32:35], a[48:51], v[80:83], v[32:35]\n\t" "v_mfma_f32_16x16x32_bf16 v[36:39], a[52:55], v[80:83], v[36:39]\n\t" \
   "v_mfma_f32_16x16x32_bf16 v[40:43], a[56:59], v[80:83], v[40:43]\n\t" "v_mfma_f32_16x16x32_bf16 v[44:47], a[60:63], v[80:83], v[44:47]\n\t" \
   "v_mfma_f32_16x16x32_bf16 v[32:35], a[64:67], v[80:83], v[32:35]\n\t" "v_mfma_f32_16x16x32_bf16 v[36:39], a[68:71], v[80:83], v[36:39]\n\t" \
   "v_mfma_f32_16x16x32_bf16 v[40:43], a[72:75], v[80:83], v[40:43]\n\t" "v_mfma_f32_16x16x32_bf16 v[44:47], a[76:79], v[80:83], v[44:47]\n\t" \
   "v_mfma_f32_16x16x32_bf16 v[32:35], a[80:83], v[80:83], v[32:35]\n\t" "v_mfma_f32_16x16x32_bf16 v[36:39], a[84:87], v[80:83], v[36:39]\n\t" \
   "v_mfma_f32_16x16x32_bf16 v[40:43], a[88:91], v[80:83], v[40:43]\n\t" "v_mfma_f32_16x16x32_bf16 v[44:47], a[92:95], v[80:83], v[44:47]\n\t" \
   "v_mfma_f32_16x16x32_bf16 v[32:35], a[96:99], v[80:83], v[32:35]\n\t" "v_mfma_f32_16x16x32_bf16 v[36:39], a[100:103], v[80:83], v[36:39]\n\t" \
   "v_mfma_f32_16x16x32_bf16 v[40:43], a[104:107], v[80:83], v[40:43]\n\t" "v_mfma_f32_16x16x32_bf16 v[44:47], a[108:111], v[80:83], v[44:47]\n\t" \
   "v_mfma_f32_16x16x32_bf16 v[32:35], a[112:115], v[80:83], v[32:35]\n\t" "v_mfma_f32_16x16x32_bf16 v[36:39], a[116:119], v[80:83], v[36:39]\n\t" \
   "v_mfma_f32_16x16x32_bf16 v[40:43], a[120:123], v[80:83], v[40:43]\n\t" "v_mfma_f32_16x16x32_bf16 v[44:47], a[124:127], v[80:83], v[44:47]\n\t" \
   "v_mfma_f32_16x16x32_bf16 v[32:35], a[128:131], v[80:83], v[32:35]\n\t" "v_mfma_f32_16x16x32_bf16 v[36:39], a[132:135], v[80:83], v[36:39]\n\t" \
   "v_mfma_f32_16x16x32_bf16 v[40:43], a[136:139], v[80:83], v[40:43]\n\t" "v_mfma_f32_16x16x32_bf16 v[44:47], a[140:143], v[80:83], v[44:47]\n\t" \
   "v_mfma_f32_16x16x32_bf16 v[32:35], a[144:147], v[80:83], v[32:35]\n\t" "v_mfma_f32_16x16x32_bf16 v[36:39], a[148:151], v[80:83], v[36:39]\n\t" \
   "v_mfma_f32_16x16x32_bf16 v[40:43], a[152:155], v[80:83], v[40:43]\n\t" "v_mfma_f32_16x16x32_bf16 v[44:47], a[156:159], v[80:83], v[44:47]\n\t" \
   "v_mfma_f32_16x16x32_bf16 v[32:35], a[160:163], v[80:83], v[32:35]\n\t" "v_mfma_f32_16x16x32_bf16 v[36:39], a[164:167], v[80:83], v[36:39]\n\t" \
   "v_mfma_f32_16x16x32_bf16 v[40:43], a[168:171], v[80:83], v[40:43]\n\t" "v_mfma_f32_16x16x32_bf16 v[44:47], a[172:175], v[80:83], v[44:47]\n\t" \
   "v_mfma_f32_16x16x32_bf16 v[32:35], a[176:179], v[80:83], v[32:35]\n\t" "v_mfma_f32_16x16x32_bf16 v[36:39], a[180:183], v[80:83], v[36:39]\n\t" \
   "v_mfma_f32_16x16x32_bf16 v[40:43], a[184:187], v[80:83], v[40:43]\n\t" "v_mfma_f32_16x16x32_bf16 v[44:47], a[188:191], v[80:83], v[44:47]\n\t" \
   "v_mfma_f32_16x16x32_bf16 v[32:35], a[192:195], v[80:83], v[32:35]\n\t" "v_mfma_f32_16x16x32_bf16 v[36:39], a[196:199], v[80:83], v[36:39]\n\t" \
   "v_mfma_f32_16x16x32_bf16 v[40:43], a[200:203], v[80:83], v[40:43]\n\t" "v_mfma_f32_16x16x32_bf16 v[44:47], a[204:207], v[80:83], v[44:47]\n\t" \
   "v_mfma_f32_16x16x32_bf16 v[32:35], a[208:211], v[80:83], v[32:35]\n\t" "v_mfma_f32_16x16x32_bf16 v[36:39], a[212:215], v[80:83], v[36:39]\n\t" \
   "v_mfma_f32_16x16x32_bf16 v[40:43], a[216:219], v[80:83], v[40:43]\n\t" "v_mfma_f32_16x16x32_bf16 v[44:47], a[220:223], v[80:83], v[44:47]\n\t" \
   "v_mfma_f32_16x16x32_bf16 v[32:35], a[224:227], v[80:83], v[32:35]\n\t" "v_mfma_f32_16x16x32_bf16 v[36:39], a[228:231], v[80:83], v[36:39]\n\t" \
   "v_mfma_f32_16x16x32_bf16 v[40:43], a[232:235], v[80:83], v[40:43]\n\t" "v_mfma_f32_16x16x32_bf16 v[44:47], a[236:239], v[80:83], v[44:47]\n\t" \
   "v_mfma_f32_16x16x32_bf16 v[32:35], a[240:243], v[80:83], v[32:35]\n\t" "v_mfma_f32_16x16x32_bf16 v[36:39], a[244:247], v[80:83], v[36:39]\n\t" \
   "v_mfma_f32_16x16x32_bf16 v[40:43], a[248:251], v[80:83], v[40:43]\n\t" "v_mfma_f32_16x16x32_bf16 v[44:47], a[252:255], v[80:83], v[44:47]\n\t" \
   "v_mfma_f32_16x16x32_bf16 v[32:35], v[192:195], v[80:83], v[32:35]\n\t" "v_mfma_f32_16x16x32_bf16 v[36:39], v[196:199], v[80:83], v[36:39]\n\t" \
   "v_mfma_f32_16x16x32_bf16 v[40:43], v[200:203], v[80:83], v[40:43]\n\t" "v_mfma_f32_16x16x32_bf16 v[44:47], v[204:207], v[80:83], v[44:47]\n\t" \
   "v_mfma_f32_16x16x32_bf16 v[32:35], v[208:211], v[80:83], v[32:35]\n\t" "v_mfma_f32_16x16x32_bf16 v[36:39], v[212:215], v[80:83], v[36:39]\n\t" \
   "v_mfma_f32_16x16x32_bf16 v[40:43], v[216:219], v[80:83], v[40:43]\n\t" "v_mfma_f32_16x16x32_bf16 v[44:47], v[220:223], v[80:83], v[44:47]\n\t" \
   "v_mfma_f32_16x16x32_bf16 v[32:35], v[224:227], v[80:83], v[32:35]\n\t" "v_mfma_f32_16x16x32_bf16 v[36:39], v[228:231], v[80:83], v[36:39]\n\t" \
   "v_mfma_f32_16x16x32_bf16 v[40:43], v[232:235], v[80:83], v[40:43]\n\t" "v_mfma_f32_16x16x32_bf16 v[44:47], v[236:239], v[80:83], v[44:47]\n\t" \
   "v_mfma_f32_16x16x32_bf16 v[32:35], v[240:243], v[80:83], v[32:35]\n\t" "v_mfma_f32_16x16x32_bf16 v[36:39], v[244:247], v[80:83], v[36:39]\n\t" \
   "v_mfma_f32_16x16x32_bf16 v[40:43], v[248:251], v[80:83], v[40:43]\n\t" "v_mfma_f32_16x16x32_bf16 v[44:47], v[252:255], v[80:83], v[44:47]\n\t" \
   "s_mov_b32 s40, 0\n\t" "s_mov_b32 s41, 0\n\t" "s_mov_b32 s42, 0\n\t" "s_mov_b32 s43, 44032\n\t" "v_mov_b32 v28, v22\n\t" "v_mov_b32 v29, v23\n\t" \
   "s_mov_b32 s45, 2\n\t" "s_mov_b32 s46, 88064\n\t" "s_waitcnt vmcnt(11)\n\t" "s_barrier\n\t" "s_lshr_b32 s48, s45, 1\n\t" \
   "s_bitcmp1_b32 s44, s48\n\t" "s_cbranch_scc0 172f\n\t" "72:\n\t" "s_mul_i32 s52, s45, 1536\n\t" "s_mul_i32 s51, s45, 40960\n\t" \
   "s_add_u32 m0, s46, %[ldx]\n\t" "s_mov_b32 s53, s46\n\t" "buffer_load_dwordx4 v16, %[rs], s51 offen offset:0 sc1 lds\n\t" \
   "ds_read_b128 v[80:83], v28 offset:0\n\t" "ds_read_b128 v[84:87], v28 offset:1024\n\t" "ds_read_b128 v[88:91], v28 offset:2048\n\t" \
   "ds_read_b128 v[92:95], v28 offset:3072\n\t" "1:\n\t" "v_mov_b32 v26, v28\n\t" "v_mov_b32 v27, v29\n\t" "s_waitcnt lgkmcnt(2)\n\t" \
   "v_mfma_f32_16x16x32_bf16 v[48:51], a[0:3], v[80:83], 0\n\t" "ds_read_b128 v[112:115], v27 offset:0\n\t" \
   "v_mfma_f32_16x16x32_bf16 v[52:55], a[4:7], v[80:83], 0\n\t" "ds_read_b128 v[96:99], v26 offset:4096\n\t" \
   "v_mfma_f32_16x16x32_bf16 v[56:59], a[8:11], v[80:83], 0\n\t" "ds_read_b128 v[116:119], v27 offset:16\n\t" \
   "v_mfma_f32_16x16x32_bf16 v[60:63], a[12:15], v[80:83], 0\n\t" "v_mul_f32 v133, 0x3e0293ee, v132\n\t" "v_fmac_f32 v64, v32, v12\n\t" \
   "v_mfma_f32_16x16x32_bf16 v[48:51], a[16:19], v[84:87], v[48:51]\n\t" "v_fmac_f32 v65, v33, v12\n\t" "v_fmac_f32 v66, v34, v12\n\t" \
   "v_mfma_f32_16x16x32_bf16 v[52:55], a[20:23], v[84:87], v[52:55]\n\t" "ds_read_b128 v[100:103], v26 offset:5120\n\t" \
   "v_mfma_f32_16x16x32_bf16 v[56:59], a[24:27], v[84:87], v[56:59]\n\t" "v_fmac_f32 v67, v35, v12\n\t" "v_fmac_f32 v68, v36, v12\n\t" \
   "v_mfma_f32_16x16x32_bf16 v[60:63], a[28:31], v[84:87], v[60:63]\n\t" "ds_read_b64 v[120:121], v27 offset:32\n\t" "s_waitcnt lgkmcnt(5)\n\t" \
   "v_mfma_f32_16x16x32_bf16 v[48:51], a[32:35], v[88:91], v[48:51]\n\t" "ds_read_b128 v[122:125], v27 offset:768\n\t" \
   "v_mfma_f32_16x16x32_bf16 v[52:55], a[36:39], v[88:91], v[52:55]\n\t" "ds_read_b128 v[104:107], v26 offset:6144\n\t" \
   "ds_read_b128 v[126:129], v27 offset:784\n\t" "v_mfma_f32_16x16x32_bf16 v[56:59], a[40:43], v[88:91], v[56:59]\n\t" \
   "ds_read_b64 v[130:131], v27 offset:800\n\t" "v_mfma_f32_16x16x32_bf16 v[60:63], a[44:47], v[88:91], v[60:63]\n\t" "v_fmac_f32 v69, v37, v12\n\t" \
   "v_fmac_f32 v70, v38, v12\n\t" "v_mfma_f32_16x16x32_bf16 v[48:51], a[48:51], v[92:95], v[48:51]\n\t" "v_fmac_f32 v71, v39, v12\n\t" \
   "v_fmac_f32 v72, v40, v12\n\t" "v_mfma_f32_16x16x32_bf16 v[52:55], a[52:55], v[92:95], v[52:55]\n\t" "ds_read_b128 v[108:111], v26 offset:7168\n\t" \
   "v_mfma_f32_16x16x32_bf16 v[56:59], a[56:59], v[92:95], v[56:59]\n\t" "v_fmac_f32 v73, v41, v12\n\t" "v_fmac_f32 v74, v42, v12\n\t" \
   "v_mfma_f32_16x16x32_bf16 v[60:63], a[60:63], v[92:95], v[60:63]\n\t" "v_fmac_f32 v75, v43, v12\n\t" "s_waitcnt lgkmcnt(6)\n\t" \
   "v_mfma_f32_16x16x32_bf16 v[48:51], a[64:67], v[96:99], v[48:51]\n\t" "v_fmac_f32 v76, v44, v12\n\t" "v_fmac_f32 v77, v45, v12\n\t" \
   "v_mfma_f32_16x16x32_bf16 v[52:55], a[68:71], v[96:99], v[52:55]\n\t" "ds_read_b128 v[80:83], v26 offset:8192\n\t" \
   "v_mfma_f32_16x16x32_bf16 v[56:59], a[72:75], v[96:99], v[56:59]\n\t" "v_fmac_f32 v78, v46, v12\n\t" "v_fmac_f32 v79, v47, v12\n\t" \
   "v_mfma_f32_16x16x32_bf16 v[60:63], a[76:79], v[96:99], v[60:63]\n\t" "v_mul_f32 v64, v64, v133\n\t" "v_mul_f32 v65, v65, v133\n\t" \
   "v_mfma_f32_16x16x32_bf16 v[48:51], a[80:83], v[100:103], v[48:51]\n\t" "v_mul_f32 v66, v66, v133\n\t" "v_mul_f32 v67, v67, v133\n\t" \
   "v_mfma_f32_16x16x32_bf16 v[52:55], a[84:87], v[100:103], v[52:55]\n\t" "ds_read_b128 v[84:87], v26 offset:9216\n\t" \
   "v_mfma_f32_16x16x32_bf16 v[56:59], a[88:91], v[100:103], v[56:59]\n\t" "v_mul_f32 v68, v68, v133\n\t" "v_mul_f32 v69, v69, v133\n\t" \
   "v_mfma_f32_16x16x32_bf16 v[60:63], a[92:95], v[100:103], v[60:63]\n\t" "v_mul_f32 v70, v70, v133\n\t" "s_waitcnt lgkmcnt(2)\n\t" \
   "v_mfma_f32_16x16x32_bf16 v[48:51], a[96:99], v[104:107], v[48:51]\n\t" "v_mul_f32 v71, v71, v133\n\t" "v_mul_f32 v72, v72, v132\n\t" \
   "v_mfma_f32_16x16x32_bf16 v[52:55], a[100:103], v[104:107], v[52:55]\n\t" "ds_read_b128 v[88:91], v26 offset:10240\n\t" \
   "v_mfma_f32_16x16x32_bf16 v[56:59], a[104:107], v[104:107], v[56:59]\n\t" "v_mul_f32 v73, v73, v132\n\t" "v_mul_f32 v74, v74, v132\n\t" \
   "v_mfma_f32_16x16x32_bf16 v[60:63], a[108:111], v[104:107], v[60:63]\n\t" "v_mul_f32 v75, v75, v132\n\t" "v_mul_f32 v76, v76, v132\n\t" \
   "v_mfma_f32_16x16x32_bf16 v[48:51], a[112:115], v[108:111], v[48:51]\n\t" "v_mul_f32 v77, v77, v132\n\t" "v_mul_f32 v78, v78, v132\n\t" \
   "v_mfma_f32_16x16x32_bf16 v[52:55], a[116:119], v[108:111], v[52:55]\n\t" "ds_read_b128 v[92:95], v26 offset:11264\n\t" \
   "v_mfma_f32_16x16x32_bf16 v[56:59], a[120:123], v[108:111], v[56:59]\n\t" "v_mul_f32 v79, v79, v132\n\t" "v_cvt_pk_bf16_f32 v136, v64, v65\n\t" \
   "v_mfma_f32_16x16x32_bf16 v[60:63], a[124:127], v[108:111], v[60:63]\n\t" "v_cvt_pk_bf16_f32 v137, v66, v67\n\t" "s_waitcnt lgkmcnt(2)\n\t" \
   "v_mfma_f32_16x16x32_bf16 v[48:51], a[128:131], v[80:83], v[48:51]\n\t" "v_cvt_pk_bf16_f32 v138, v68, v69\n\t" \
   "v_cvt_pk_bf16_f32 v139, v70, v71\n\t" "v_mfma_f32_16x16x32_bf16 v[52:55], a[132:135], v[80:83], v[52:55]\n\t" \
   "ds_read_b128 v[96:99], v26 offset:12288\n\t" "v_mfma_f32_16x16x32_bf16 v[56:59], a[136:139], v[80:83], v[56:59]\n\t" \
   "v_cvt_pk_bf16_f32 v140, v72, v73\n\t" "v_cvt_pk_bf16_f32 v141, v74, v75\n\t" \
   "v_mfma_f32_16x16x32_bf16 v[60:63], a[140:143], v[80:83], v[60:63]\n\t" "v_cvt_pk_bf16_f32 v142, v76, v77\n\t" \
   "v_cvt_pk_bf16_f32 v143, v78, v79\n\t" "v_mfma_f32_16x16x32_bf16 v[48:51], a[144:147], v[84:87], v[48:51]\n\t" "s_lshl_b32 s47, s41, 13\n\t" \
   "s_add_i32 s47, s47, -4096\n\t" "v_mfma_f32_16x16x32_bf16 v[52:55], a[148:151], v[84:87], v[52:55]\n\t" \
   "ds_read_b128 v[100:103], v26 offset:13312\n\t" "v_mfma_f32_16x16x32_bf16 v[56:59], a[152:155], v[84:87], v[56:59]\n\t" "s_add_u32 s54, s41, 1\n\t" \
   "s_max_i32 s47, s47, 0\n\t" "v_mfma_f32_16x16x32_bf16 v[60:63], a[156:159], v[84:87], v[60:63]\n\t" "s_lshr_b64 s[60:61], %[sel], s54\n\t" \
   "s_cmp_eq_u32 s40, 0\n\t" "s_cselect_b32 s48, 0x7ffffff0, 0\n\t" "s_waitcnt lgkmcnt(2)\n\t" \
   "v_mfma_f32_16x16x32_bf16 v[48:51], a[160:163], v[88:91], v[48:51]\n\t" "s_lshl_b32 s55, s54, 19\n\t" "s_and_b32 s60, s60, 1\n\t" \
   "v_or_b32 v30, s48, v21\n\t" "s_add_u32 s55, s55, %[pool]\n\t" "v_mfma_f32_16x16x32_bf16 v[52:55], a[164:167], v[88:91], v[52:55]\n\t" \
   "ds_read_b128 v[104:107], v26 offset:14336\n\t" "s_add_u32 s48, s47, %[qf]\n\t" "s_lshl_b32 s60, s60, 13\n\t" \
   "buffer_store_dwordx4 v[136:139], v30, %[rs], s48 offen sc1\n\t" "v_mfma_f32_16x16x32_bf16 v[56:59], a[168:171], v[88:91], v[56:59]\n\t" \
   "s_add_u32 s55, s55, s60\n\t" "s_add_u32 s48, s47, %[kf]\n\t" "s_bitcmp1_b64 %[skip], s41\n\t" "s_cselect_b32 s58, 0, %[size]\n\t" \
   "v_mfma_f32_16x16x32_bf16 v[60:63], a[172:175], v[88:91], v[60:63]\n\t" "buffer_store_dwordx4 v[140:143], v30, %[rs], s48 offen sc1\n\t" \
   "v_mfma_f32_16x16x32_bf16 v[48:51], a[176:179], v[92:95], v[48:51]\n\t" "v_add_f32 v112, v112, v114\n\t" \
   "v_mfma_f32_16x16x32_bf16 v[52:55], a[180:183], v[92:95], v[52:55]\n\t" "ds_read_b128 v[108:111], v26 offset:15360\n\t" \
   "v_mfma_f32_16x16x32_bf16 v[56:59], a[184:187], v[92:95], v[56:59]\n\t" "v_add_f32 v113, v113, v115\n\t" "v_add_f32 v116, v116, v118\n\t" \
   "v_mfma_f32_16x16x32_bf16 v[60:63], a[188:191], v[92:95], v[60:63]\n\t" \
   "buffer_store_dwordx4 v[156:159], v152, s[56:59], s55 offen offset:0 sc1\n\t" "s_waitcnt lgkmcnt(2)\n\t" \
   "v_mfma_f32_16x16x32_bf16 v[48:51], a[192:195], v[96:99], v[48:51]\n\t" "v_add_f32 v117, v117, v119\n\t" "v_add_f32 v112, v112, v120\n\t" \
   "v_mfma_f32_16x16x32_bf16 v[52:55], a[196:199], v[96:99], v[52:55]\n\t" "ds_read_b128 v[80:83], v26 offset:16384\n\t" \
   "v_mfma_f32_16x16x32_bf16 v[56:59], a[200:203], v[96:99], v[56:59]\n\t" "v_add_f32 v113, v113, v121\n\t" "v_add_f32 v112, v112, v116\n\t" \
   "v_mfma_f32_16x16x32_bf16 v[60:63], a[204:207], v[96:99], v[60:63]\n\t" "v_add_f32 v113, v113, v117\n\t" "v_mul_f32 v112, 0xbacccccd, v112\n\t" \
   "v_mfma_f32_16x16x32_bf16 v[48:51], a[208:211], v[100:103], v[48:51]\n\t" \
   "buffer_store_dwordx4 v[156:159], v152, s[56:59], s55 offen offset:1024 sc1\n\t" \
   "v_mfma_f32_16x16x32_bf16 v[52:55], a[212:215], v[100:103], v[52:55]\n\t" "ds_read_b128 v[84:87], v26 offset:17408\n\t" \
   "v_mfma_f32_16x16x32_bf16 v[56:59], a[216:219], v[100:103], v[56:59]\n\t" "v_mul_f32 v113, 0x3acccccd, v113\n\t" \
   "v_fma_f32 v113, -v112, v112, v113\n\t" "v_mfma_f32_16x16x32_bf16 v[60:63], a[220:223], v[100:103], v[60:63]\n\t" "v_max_f32 v113, 0, v113\n\t" \
   "s_waitcnt lgkmcnt(2)\n\t" "v_mfma_f32_16x16x32_bf16 v[48:51], a[224:227], v[104:107], v[48:51]\n\t" "v_cvt_pk_bf16_f32 v113, v113, v112\n\t" \
   "v_and_b32 v8, 0xffff0000, v113\n\t" "v_mfma_f32_16x16x32_bf16 v[52:55], a[228:231], v[104:107], v[52:55]\n\t" \
   "ds_read_b128 v[88:91], v26 offset:18432\n\t" "v_mfma_f32_16x16x32_bf16 v[56:59], a[232:235], v[104:107], v[56:59]\n\t" \
   "v_lshlrev_b32 v113, 16, v113\n\t" "v_add_f32 v113, 0x3727c5ac, v113\n\t" "v_mfma_f32_16x16x32_bf16 v[60:63], a[236:239], v[104:107], v[60:63]\n\t" \
   "v_cvt_pk_bf16_f32 v113, v113, v113\n\t" "v_lshlrev_b32 v113, 16, v113\n\t" \
   "v_mfma_f32_16x16x32_bf16 v[48:51], a[240:243], v[108:111], v[48:51]\n\t" "v_sqrt_f32 v113, v113\n\t" \
   "v_mfma_f32_16x16x32_bf16 v[52:55], a[244:247], v[108:111], v[52:55]\n\t" "ds_read_b128 v[92:95], v26 offset:19456\n\t" \
   "v_mfma_f32_16x16x32_bf16 v[56:59], a[248:251], v[108:111], v[56:59]\n\t" "buffer_load_dwordx4 v16, %[rs], s51 offen offset:1024 sc1 lds\n\t" \
   "v_mfma_f32_16x16x32_bf16 v[60:63], a[252:255], v[108:111], v[60:63]\n\t" "s_lshr_b64 s[60:61], %[ysel], s54\n\t" "s_waitcnt lgkmcnt(2)\n\t" \
   "v_mfma_f32_16x16x32_bf16 v[48:51], v[192:195], v[80:83], v[48:51]\n\t" "s_nop 0\n\t" "s_mul_i32 s61, s54, 43\n\t" \
   "v_mfma_f32_16x16x32_bf16 v[52:55], v[196:199], v[80:83], v[52:55]\n\t" "ds_read_b128 v[96:99], v26 offset:20480\n\t" \
   "v_mfma_f32_16x16x32_bf16 v[56:59], v[200:203], v[80:83], v[56:59]\n\t" "v_add_f32 v122, v122, v124\n\t" \
   "v_mfma_f32_16x16x32_bf16 v[60:63], v[204:207], v[80:83], v[60:63]\n\t" "v_cvt_pk_bf16_f32 v113, v113, v113\n\t" "v_add_f32 v123, v123, v125\n\t" \
   "v_mfma_f32_16x16x32_bf16 v[48:51], v[208:211], v[84:87], v[48:51]\n\t" "s_mul_i32 s55, s54, 80\n\t" "v_add_f32 v126, v126, v128\n\t" \
   "v_mfma_f32_16x16x32_bf16 v[52:55], v[212:215], v[84:87], v[52:55]\n\t" "ds_read_b128 v[100:103], v26 offset:21504\n\t" \
   "v_mfma_f32_16x16x32_bf16 v[56:59], v[216:219], v[84:87], v[56:59]\n\t" "buffer_load_dwordx4 v16, %[rs], s51 offen offset:2048 sc1 lds\n\t" \
   "v_mfma_f32_16x16x32_bf16 v[60:63], v[220:223], v[84:87], v[60:63]\n\t" "v_lshlrev_b32 v113, 16, v113\n\t" "s_waitcnt lgkmcnt(2)\n\t" \
   "v_mfma_f32_16x16x32_bf16 v[48:51], v[224:227], v[88:91], v[48:51]\n\t" "s_and_b32 s60, s60, 1\n\t" "v_add_f32 v127, v127, v129\n\t" \
   "v_mfma_f32_16x16x32_bf16 v[52:55], v[228:231], v[88:91], v[52:55]\n\t" "ds_read_b128 v[104:107], v26 offset:22528\n\t" \
   "v_mfma_f32_16x16x32_bf16 v[56:59], v[232:235], v[88:91], v[56:59]\n\t" "v_rcp_f32 v10, v113\n\t" \
   "v_mfma_f32_16x16x32_bf16 v[60:63], v[236:239], v[88:91], v[60:63]\n\t" "s_lshr_b32 s61, s61, 7\n\t" "v_add_f32 v122, v122, v130\n\t" \
   "v_mfma_f32_16x16x32_bf16 v[48:51], v[240:243], v[92:95], v[48:51]\n\t" "buffer_load_dwordx4 v16, %[rs], s51 offen offset:3072 sc1 lds\n\t" \
   "s_add_u32 m0, m0, 4096\n\t" "s_add_u32 s51, s51, 4096\n\t" "v_mfma_f32_16x16x32_bf16 v[52:55], v[244:247], v[92:95], v[52:55]\n\t" \
   "ds_read_b128 v[108:111], v26 offset:23552\n\t" "v_mfma_f32_16x16x32_bf16 v[56:59], v[248:251], v[92:95], v[56:59]\n\t" \
   "s_add_u32 s55, s55, s60\n\t" "v_add_f32 v123, v123, v131\n\t" "v_mfma_f32_16x16x32_bf16 v[60:63], v[252:255], v[92:95], v[60:63]\n\t" \
   "s_lshl4_add_u32 s55, s61, s55\n\t" "s_waitcnt lgkmcnt(2)\n\t" "v_mfma_f32_16x16x32_bf16 v[64:67], a[0:3], v[96:99], 0\n\t" \
   "v_add_f32 v122, v122, v126\n\t" "v_add_f32 v123, v123, v127\n\t" "v_mfma_f32_16x16x32_bf16 v[68:71], a[4:7], v[96:99], 0\n\t" \
   "ds_read_b128 v[80:83], v26 offset:24576\n\t" "v_mfma_f32_16x16x32_bf16 v[72:75], a[8:11], v[96:99], 0\n\t" "s_lshl_b32 s55, s55, 13\n\t" \
   "v_mul_f32 v122, 0xbacccccd, v122\n\t" "v_mfma_f32_16x16x32_bf16 v[76:79], a[12:15], v[96:99], 0\n\t" "v_mul_f32 v11, 0x3e0293ee, v10\n\t" \
   "v_fmac_f32 v48, v32, v8\n\t" "v_mfma_f32_16x16x32_bf16 v[64:67], a[16:19], v[100:103], v[64:67]\n\t" "v_fmac_f32 v49, v33, v8\n\t" \
   "v_fmac_f32 v50, v34, v8\n\t" "v_mfma_f32_16x16x32_bf16 v[68:71], a[20:23], v[100:103], v[68:71]\n\t" "ds_read_b128 v[84:87], v26 offset:25600\n\t" \
   "v_mfma_f32_16x16x32_bf16 v[72:75], a[24:27], v[100:103], v[72:75]\n\t" "s_cmp_lt_i32 s40, 1\n\t" "s_cselect_b32 s49, 0x7ffffff0, 0\n\t" \
   "v_mfma_f32_16x16x32_bf16 v[76:79], a[28:31], v[100:103], v[76:79]\n\t" "v_or_b32 v9, s49, v20\n\t" "s_waitcnt lgkmcnt(2)\n\t" \
   "v_mfma_f32_16x16x32_bf16 v[64:67], a[32:35], v[104:107], v[64:67]\n\t" "s_sub_i32 s49, s41, 1\n\t" "s_max_i32 s49, s49, 0\n\t" \
   "buffer_load_dwordx4 v16, %[rs], s51 offen offset:0 sc1 lds\n\t" "v_mfma_f32_16x16x32_bf16 v[68:71], a[36:39], v[104:107], v[68:71]\n\t" \
   "ds_read_b128 v[88:91], v26 offset:26624\n\t" "v_mfma_f32_16x16x32_bf16 v[72:75], a[40:43], v[104:107], v[72:75]\n\t" "s_lshl_b32 s49, s49, 12\n\t" \
   "s_waitcnt vmcnt(6)\n\t" "v_mfma_f32_16x16x32_bf16 v[76:79], a[44:47], v[104:107], v[76:79]\n\t" \
   "buffer_store_dword v31, v9, %[rs], s49 offen sc1\n\t" "v_mfma_f32_16x16x32_bf16 v[64:67], a[48:51], v[108:111], v[64:67]\n\t" \
   "s_add_u32 s55, s55, %[yb]\n\t" "v_fmac_f32 v51, v35, v8\n\t" "v_mfma_f32_16x16x32_bf16 v[68:71], a[52:55], v[108:111], v[68:71]\n\t" \
   "ds_read_b128 v[92:95], v26 offset:27648\n\t" "s_cmp_eq_u32 s41, 63\n\t" "s_cselect_b32 s58, 0, %[size]\n\t" \
   "v_mfma_f32_16x16x32_bf16 v[72:75], a[56:59], v[108:111], v[72:75]\n\t" "v_fmac_f32 v52, v36, v8\n\t" "v_fmac_f32 v53, v37, v8\n\t" \
   "v_mfma_f32_16x16x32_bf16 v[76:79], a[60:63], v[108:111], v[76:79]\n\t" "v_fmac_f32 v54, v38, v8\n\t" "s_waitcnt lgkmcnt(2)\n\t" \
   "v_mfma_f32_16x16x32_bf16 v[64:67], a[64:67], v[80:83], v[64:67]\n\t" "buffer_load_dwordx4 v16, %[rs], s51 offen offset:1024 sc1 lds\n\t" \
   "v_mfma_f32_16x16x32_bf16 v[68:71], a[68:71], v[80:83], v[68:71]\n\t" "ds_read_b128 v[96:99], v26 offset:28672\n\t" \
   "v_mfma_f32_16x16x32_bf16 v[72:75], a[72:75], v[80:83], v[72:75]\n\t" "buffer_store_dwordx4 v[156:159], v153, s[56:59], s55 offen sc1\n\t" \
   "v_mfma_f32_16x16x32_bf16 v[76:79], a[76:79], v[80:83], v[76:79]\n\t" "v_fmac_f32 v55, v39, v8\n\t" "v_fmac_f32 v56, v40, v8\n\t" \
   "v_mfma_f32_16x16x32_bf16 v[64:67], a[80:83], v[84:87], v[64:67]\n\t" "v_fmac_f32 v57, v41, v8\n\t" "v_fmac_f32 v58, v42, v8\n\t" \
   "v_mfma_f32_16x16x32_bf16 v[68:71], a[84:87], v[84:87], v[68:71]\n\t" "ds_read_b128 v[100:103], v26 offset:29696\n\t" \
   "v_mfma_f32_16x16x32_bf16 v[72:75], a[88:91], v[84:87], v[72:75]\n\t" "v_fmac_f32 v59, v43, v8\n\t" "v_fmac_f32 v60, v44, v8\n\t" \
   "v_mfma_f32_16x16x32_bf16 v[76:79], a[92:95], v[84:87], v[76:79]\n\t" "v_fmac_f32 v61, v45, v8\n\t" "s_waitcnt lgkmcnt(2)\n\t" \
   "v_mfma_f32_16x16x32_bf16 v[64:67], a[96:99], v[88:91], v[64:67]\n\t" "buffer_load_dwordx4 v16, %[rs], s51 offen offset:2048 sc1 lds\n\t" \
   "v_mfma_f32_16x16x32_bf16 v[68:71], a[100:103], v[88:91], v[68:71]\n\t" "ds_read_b128 v[104:107], v26 offset:30720\n\t" \
   "v_mfma_f32_16x16x32_bf16 v[72:75], a[104:107], v[88:91], v[72:75]\n\t" "buffer_store_dwordx4 v[156:159], v154, s[56:59], s55 offen sc1\n\t" \
   "v_mfma_f32_16x16x32_bf16 v[76:79], a[108:111], v[88:91], v[76:79]\n\t" "v_fmac_f32 v62, v46, v8\n\t" "v_fmac_f32 v63, v47, v8\n\t" \
   "v_mfma_f32_16x16x32_bf16 v[64:67], a[112:115], v[92:95], v[64:67]\n\t" "v_mul_f32 v48, v48, v11\n\t" "v_mul_f32 v49, v49, v11\n\t" \
   "v_mfma_f32_16x16x32_bf16 v[68:71], a[116:119], v[92:95], v[68:71]\n\t" "ds_read_b128 v[108:111], v26 offset:31744\n\t" \
   "v_mfma_f32_16x16x32_bf16 v[72:75], a[120:123], v[92:95], v[72:75]\n\t" "v_mul_f32 v50, v50, v11\n\t" "v_mul_f32 v51, v51, v11\n\t" \
   "v_mfma_f32_16x16x32_bf16 v[76:79], a[124:127], v[92:95], v[76:79]\n\t" "v_mul_f32 v52, v52, v11\n\t" "s_waitcnt lgkmcnt(2)\n\t" \
   "v_mfma_f32_16x16x32_bf16 v[64:67], a[128:131], v[96:99], v[64:67]\n\t" "buffer_load_dwordx4 v16, %[rs], s51 offen offset:3072 sc1 lds\n\t" \
   "s_add_u32 m0, m0, 4096\n\t" "s_add_u32 s51, s51, 4096\n\t" "v_mfma_f32_16x16x32_bf16 v[68:71], a[132:135], v[96:99], v[68:71]\n\t" \
   "ds_read_b128 v[80:83], v26 offset:32768\n\t" "v_mfma_f32_16x16x32_bf16 v[72:75], a[136:139], v[96:99], v[72:75]\n\t" "v_mul_f32 v53, v53, v11\n\t" \
   "buffer_store_dwordx4 v[156:159], v155, s[56:59], s55 offen sc1\n\t" "v_mfma_f32_16x16x32_bf16 v[76:79], a[140:143], v[96:99], v[76:79]\n\t" \
   "v_mul_f32 v54, v54, v11\n\t" "v_mul_f32 v55, v55, v11\n\t" "v_mfma_f32_16x16x32_bf16 v[64:67], a[144:147], v[100:103], v[64:67]\n\t" \
   "v_mul_f32 v56, v56, v10\n\t" "v_mul_f32 v57, v57, v10\n\t" "v_mfma_f32_16x16x32_bf16 v[68:71], a[148:151], v[100:103], v[68:71]\n\t" \
   "ds_read_b128 v[84:87], v26 offset:33792\n\t" "v_mfma_f32_16x16x32_bf16 v[72:75], a[152:155], v[100:103], v[72:75]\n\t" \
   "v_mul_f32 v58, v58, v10\n\t" "v_mul_f32 v59, v59, v10\n\t" "v_mfma_f32_16x16x32_bf16 v[76:79], a[156:159], v[100:103], v[76:79]\n\t" \
   "v_mul_f32 v60, v60, v10\n\t" "s_waitcnt lgkmcnt(2)\n\t" "v_mfma_f32_16x16x32_bf16 v[64:67], a[160:163], v[104:107], v[64:67]\n\t" \
   "buffer_load_dwordx4 v16, %[rs], s51 offen offset:0 sc1 lds\n\t" "v_mfma_f32_16x16x32_bf16 v[68:71], a[164:167], v[104:107], v[68:71]\n\t" \
   "ds_read_b128 v[88:91], v26 offset:34816\n\t" "v_mfma_f32_16x16x32_bf16 v[72:75], a[168:171], v[104:107], v[72:75]\n\t" \
   "v_mul_f32 v61, v61, v10\n\t" "v_mul_f32 v62, v62, v10\n\t" "v_mfma_f32_16x16x32_bf16 v[76:79], a[172:175], v[104:107], v[76:79]\n\t" \
   "v_mul_f32 v63, v63, v10\n\t" "v_mul_f32 v123, 0x3acccccd, v123\n\t" "v_mfma_f32_16x16x32_bf16 v[64:67], a[176:179], v[108:111], v[64:67]\n\t" \
   "v_cvt_pk_bf16_f32 v136, v48, v49\n\t" "v_fma_f32 v123, -v122, v122, v123\n\t" \
   "v_mfma_f32_16x16x32_bf16 v[68:71], a[180:183], v[108:111], v[68:71]\n\t" "ds_read_b128 v[92:95], v26 offset:35840\n\t" \
   "v_mfma_f32_16x16x32_bf16 v[72:75], a[184:187], v[108:111], v[72:75]\n\t" "v_cvt_pk_bf16_f32 v137, v50, v51\n\t" "v_max_f32 v123, 0, v123\n\t" \
   "v_mfma_f32_16x16x32_bf16 v[76:79], a[188:191], v[108:111], v[76:79]\n\t" "v_cvt_pk_bf16_f32 v138, v52, v53\n\t" "s_waitcnt lgkmcnt(2)\n\t" \
   "v_mfma_f32_16x16x32_bf16 v[64:67], a[192:195], v[80:83], v[64:67]\n\t" "v_cvt_pk_bf16_f32 v123, v123, v122\n\t" \
   "v_cvt_pk_bf16_f32 v139, v54, v55\n\t" "buffer_load_dwordx4 v16, %[rs], s51 offen offset:1024 sc1 lds\n\t" "s_add_u32 m0, s53, %[ld1]\n\t" \
   "s_nop 0\n\t" "v_mfma_f32_16x16x32_bf16 v[68:71], a[196:199], v[80:83], v[68:71]\n\t" "ds_read_b128 v[96:99], v26 offset:36864\n\t" \
   "v_mfma_f32_16x16x32_bf16 v[72:75], a[200:203], v[80:83], v[72:75]\n\t" "v_and_b32 v12, 0xffff0000, v123\n\t" "v_cvt_pk_bf16_f32 v140, v56, v57\n\t" \
   "v_mfma_f32_16x16x32_bf16 v[76:79], a[204:207], v[80:83], v[76:79]\n\t" "v_lshlrev_b32 v123, 16, v123\n\t" "v_cvt_pk_bf16_f32 v141, v58, v59\n\t" \
   "v_mfma_f32_16x16x32_bf16 v[64:67], a[208:211], v[84:87], v[64:67]\n\t" "v_add_f32 v123, 0x3727c5ac, v123\n\t" \
   "v_cvt_pk_bf16_f32 v142, v60, v61\n\t" "v_mfma_f32_16x16x32_bf16 v[68:71], a[212:215], v[84:87], v[68:71]\n\t" \
   "ds_read_b128 v[100:103], v26 offset:37888\n\t" "v_mfma_f32_16x16x32_bf16 v[72:75], a[216:219], v[84:87], v[72:75]\n\t" \
   "v_cvt_pk_bf16_f32 v123, v123, v123\n\t" "v_cvt_pk_bf16_f32 v143, v62, v63\n\t" \
   "v_mfma_f32_16x16x32_bf16 v[76:79], a[220:223], v[84:87], v[76:79]\n\t" "v_lshlrev_b32 v123, 16, v123\n\t" "s_waitcnt lgkmcnt(2)\n\t" \
   "v_mfma_f32_16x16x32_bf16 v[64:67], a[224:227], v[88:91], v[64:67]\n\t" "s_lshl_b32 s47, s41, 13\n\t" "v_sqrt_f32 v123, v123\n\t" \
   "s_add_i32 s47, s47, 0\n\t" "v_mfma_f32_16x16x32_bf16 v[68:71], a[228:231], v[88:91], v[68:71]\n\t" "ds_read_b128 v[104:107], v26 offset:38912\n\t" \
   "buffer_load_dwordx4 v17, %[rs], s52 offen sc1 lds\n\t" "v_mfma_f32_16x16x32_bf16 v[72:75], a[232:235], v[88:91], v[72:75]\n\t" \
   "s_add_u32 s48, s47, %[qf]\n\t" "s_nop 0\n\t" "buffer_store_dwordx4 v[136:139], v21, %[rs], s48 offen sc1\n\t" \
   "v_mfma_f32_16x16x32_bf16 v[76:79], a[236:239], v[88:91], v[76:79]\n\t" "v_cvt_pk_bf16_f32 v123, v123, v123\n\t" "v_lshlrev_b32 v123, 16, v123\n\t" \
   "s_add_u32 s48, s47, %[kf]\n\t" "v_rcp_f32 v132, v123\n\t" "v_mfma_f32_16x16x32_bf16 v[64:67], a[240:243], v[92:95], v[64:67]\n\t" \
   "buffer_store_dwordx4 v[140:143], v21, %[rs], s48 offen sc1\n\t" "v_add_u32 v28, s43, v22\n\t" "v_add_u32 v29, s43, v23\n\t" \
   "v_mfma_f32_16x16x32_bf16 v[68:71], a[244:247], v[92:95], v[68:71]\n\t" "ds_read_b128 v[108:111], v26 offset:39936\n\t" \
   "v_mfma_f32_16x16x32_bf16 v[72:75], a[248:251], v[92:95], v[72:75]\n\t" "v_mfma_f32_16x16x32_bf16 v[76:79], a[252:255], v[92:95], v[76:79]\n\t" \
   "s_waitcnt vmcnt(18) lgkmcnt(0)\n\t" "s_barrier\n\t" "v_mfma_f32_16x16x32_bf16 v[64:67], v[192:195], v[96:99], v[64:67]\n\t" \
   "s_add_u32 s45, s40, 3\n\t" "s_min_u32 s45, s45, 63\n\t" "v_mfma_f32_16x16x32_bf16 v[68:71], v[196:199], v[96:99], v[68:71]\n\t" \
   "ds_read_b128 v[80:83], v28 offset:0\n\t" "v_mfma_f32_16x16x32_bf16 v[72:75], v[200:203], v[96:99], v[72:75]\n\t" "s_mov_b32 s46, s42\n\t" \
   "v_mfma_f32_16x16x32_bf16 v[76:79], v[204:207], v[96:99], v[76:79]\n\t" "s_lshr_b32 s48, s45, 1\n\t" "s_bitcmp1_b32 s44, s48\n\t" \
   "s_cbranch_scc0 174f\n\t" "74:\n\t" "v_mfma_f32_16x16x32_bf16 v[64:67], v[208:211], v[100:103], v[64:67]\n\t" "s_mul_i32 s52, s45, 1536\n\t" \
   "s_mul_i32 s51, s45, 40960\n\t" "s_add_u32 m0, s46, %[ldx]\n\t" "s_mov_b32 s53, s46\n\t" \
   "v_mfma_f32_16x16x32_bf16 v[68:71], v[212:215], v[100:103], v[68:71]\n\t" "ds_read_b128 v[84:87], v28 offset:1024\n\t" \
   "v_mfma_f32_16x16x32_bf16 v[72:75], v[216:219], v[100:103], v[72:75]\n\t" "buffer_load_dwordx4 v16, %[rs], s51 offen offset:0 sc1 lds\n\t" \
   "v_mfma_f32_16x16x32_bf16 v[76:79], v[220:223], v[100:103], v[76:79]\n\t" "s_mov_b32 s42, s43\n\t" \
   "v_mfma_f32_16x16x32_bf16 v[64:67], v[224:227], v[104:107], v[64:67]\n\t" "s_add_u32 s43, s43, 44032\n\t" \
   "v_mfma_f32_16x16x32_bf16 v[68:71], v[228:231], v[104:107], v[68:71]\n\t" "ds_read_b128 v[88:91], v28 offset:2048\n\t" \
   "v_mfma_f32_16x16x32_bf16 v[72:75], v[232:235], v[104:107], v[72:75]\n\t" "s_cmp_eq_u32 s43, 132096\n\t" "s_cselect_b32 s43, 0, s43\n\t" \
   "v_mfma_f32_16x16x32_bf16 v[76:79], v[236:239], v[104:107], v[76:79]\n\t" "s_add_u32 s41, s41, 1\n\t" "s_add_u32 s40, s40, 1\n\t" \
   "v_mfma_f32_16x16x32_bf16 v[64:67], v[240:243], v[108:111], v[64:67]\n\t" "v_mfma_f32_16x16x32_bf16 v[68:71], v[244:247], v[108:111], v[68:71]\n\t" \
   "ds_read_b128 v[92:95], v28 offset:3072\n\t" "v_mfma_f32_16x16x32_bf16 v[72:75], v[248:251], v[108:111], v[72:75]\n\t" \
   "v_mfma_f32_16x16x32_bf16 v[76:79], v[252:255], v[108:111], v[76:79]\n\t" "s_cmp_lt_u32 s40, 64\n\t" "s_cbranch_scc1 1b\n\t" "s_nop 7\n\t" \
   "v_mul_f32 v133, 0x3e0293ee, v132\n\t" "v_fmac_f32 v64, v32, v12\n\t" "v_fmac_f32 v65, v33, v12\n\t" "v_fmac_f32 v66, v34, v12\n\t" \
   "v_fmac_f32 v67, v35, v12\n\t" "v_fmac_f32 v68, v36, v12\n\t" "v_fmac_f32 v69, v37, v12\n\t" "v_fmac_f32 v70, v38, v12\n\t" \
   "v_fmac_f32 v71, v39, v12\n\t" "v_fmac_f32 v72, v40, v12\n\t" "v_fmac_f32 v73, v41, v12\n\t" "v_fmac_f32 v74, v42, v12\n\t" \
   "v_fmac_f32 v75, v43, v12\n\t" "v_fmac_f32 v76, v44, v12\n\t" "v_fmac_f32 v77, v45, v12\n\t" "v_fmac_f32 v78, v46, v12\n\t" \
   "v_fmac_f32 v79, v47, v12\n\t" "v_mul_f32 v64, v64, v133\n\t" "v_mul_f32 v65, v65, v133\n\t" "v_mul_f32 v66, v66, v133\n\t" \
   "v_mul_f32 v67, v67, v133\n\t" "v_mul_f32 v68, v68, v133\n\t" "v_mul_f32 v69, v69, v133\n\t" "v_mul_f32 v70, v70, v133\n\t" \
   "v_mul_f32 v71, v71, v133\n\t" "v_mul_f32 v72, v72, v132\n\t" "v_mul_f32 v73, v73, v132\n\t" "v_mul_f32 v74, v74, v132\n\t" \
   "v_mul_f32 v75, v75, v132\n\t" "v_mul_f32 v76, v76, v132\n\t" "v_mul_f32 v77, v77, v132\n\t" "v_mul_f32 v78, v78, v132\n\t" \
   "v_mul_f32 v79, v79, v132\n\t" "v_cvt_pk_bf16_f32 v136, v64, v65\n\t" "v_cvt_pk_bf16_f32 v137, v66, v67\n\t" "v_cvt_pk_bf16_f32 v138, v68, v69\n\t" \
   "v_cvt_pk_bf16_f32 v139, v70, v71\n\t" "v_cvt_pk_bf16_f32 v140, v72, v73\n\t" "v_cvt_pk_bf16_f32 v141, v74, v75\n\t" \
   "v_cvt_pk_bf16_f32 v142, v76, v77\n\t" "v_cvt_pk_bf16_f32 v143, v78, v79\n\t" "s_lshl_b32 s47, s41, 13\n\t" "s_add_i32 s47, s47, -4096\n\t" \
   "s_max_i32 s47, s47, 0\n\t" "s_cmp_eq_u32 s40, 0\n\t" "s_cselect_b32 s48, 0x7ffffff0, 0\n\t" "v_or_b32 v30, s48, v21\n\t" \
   "s_add_u32 s48, s47, %[qf]\n\t" "buffer_store_dwordx4 v[136:139], v30, %[rs], s48 offen sc1\n\t" "s_add_u32 s48, s47, %[kf]\n\t" \
   "buffer_store_dwordx4 v[140:143], v30, %[rs], s48 offen sc1\n\t" "s_waitcnt vmcnt(0) lgkmcnt(0)\n\t" "s_cmp_lt_i32 s40, 1\n\t" \
   "s_cselect_b32 s47, 0x7ffffff0, 0\n\t" "v_or_b32 v30, s47, v20\n\t" "s_sub_i32 s47, s41, 1\n\t" "s_max_i32 s47, s47, 0\n\t" \
   "s_lshl_b32 s47, s47, 12\n\t" "buffer_store_dword v31, v30, %[rs], s47 offen sc1\n\t" "s_branch 9f\n\t" "172:\n\t" "s_mov_b32 s47, 0\n\t" \
   "buffer_load_dword v30, v19, %[rs], 0 offen sc1\n\t" "s_waitcnt vmcnt(0)\n\t" "v_add_u32 v30, 1, v30\n\t" "v_cmp_lt_u32 vcc, 9, v30\n\t" \
   "s_mov_b32 s44, vcc_lo\n\t" "s_bitcmp1_b32 s44, s48\n\t" "s_cbranch_scc1 72b\n\t" "s_bitset1_b32 s44, s48\n\t" "s_lshl_b32 s48, s48, 7\n\t" \
   "s_add_u32 s48, s48, %[cnt]\n\t" "73:\n\t" "s_sleep 10\n\t" "buffer_load_dword v30, off, %[rs], s48 sc1\n\t" "s_add_u32 s47, s47, 1\n\t" \
   "s_waitcnt vmcnt(0)\n\t" "v_add_u32 v30, 1, v30\n\t" "v_cmp_lt_u32 vcc, 9, v30\n\t" "s_cmp_gt_u32 s47, 0x20000\n\t" "s_cbranch_scc1 72b\n\t" \
   "s_cbranch_vccz 73b\n\t" "s_branch 72b\n\t" "174:\n\t" "s_mov_b32 s47, 0\n\t" "buffer_load_dword v30, v19, %[rs], 0 offen sc1\n\t" \
   "s_waitcnt vmcnt(0)\n\t" "v_add_u32 v30, 1, v30\n\t" "v_cmp_lt_u32 vcc, 9, v30\n\t" "s_mov_b32 s44, vcc_lo\n\t" "s_bitcmp1_b32 s44, s48\n\t" \
   "s_cbranch_scc1 74b\n\t" "s_bitset1_b32 s44, s48\n\t" "s_lshl_b32 s48, s48, 7\n\t" "s_add_u32 s48, s48, %[cnt]\n\t" "75:\n\t" "s_sleep 10\n\t" \
   "buffer_load_dword v30, off, %[rs], s48 sc1\n\t" "s_add_u32 s47, s47, 1\n\t" "s_waitcnt vmcnt(0)\n\t" "v_add_u32 v30, 1, v30\n\t" \
   "v_cmp_lt_u32 vcc, 9, v30\n\t" "s_cmp_gt_u32 s47, 0x20000\n\t" "s_cbranch_scc1 74b\n\t" "s_cbranch_vccz 75b\n\t" "s_branch 74b\n\t" "9:\n\t" \
   "s_waitcnt vmcnt(0)"
#define KQ_CLOB "v8", "v9", "v10", "v11", "v12", "v13", "v14", "v15", "v32", "v33", "v34", "v35", "v36", "v37", "v38", "v39", "v40", "v41", "v42", "v43", "v44", "v45", "v46", "v47", "v48", "v49", "v50", "v51", "v52", "v53", "v54", "v55", "v56", "v57", "v58", "v59", "v60", "v61", "v62", "v63", "v64", "v65", "v66", "v67", "v68", "v69", "v70", "v71", "v72", "v73", "v74", "v75", "v76", "v77", "v78", "v79", "v80", "v81", "v82", "v83", "v84", "v85", "v86", "v87", "v88", "v89", "v90", "v91", "v92", "v93", "v94", "v95", "v96", "v97", "v98", "v99", "v100", "v101", "v102", "v103", "v104", "v105", "v106", "v107", "v108", "v109", "v110", "v111", "v112", "v113", "v114", "v115", "v116", "v117", "v118", "v119", "v120", "v121", "v122", "v123", "v124", "v125", "v126", "v127", "v128", "v129", "v130", "v131", "v132", "v133", "v134", "v135", "v136", "v137", "v138", "v139", "v140", "v141", "v142", "v143", "v144", "v145", "v146", "v147", "v148", "v149", "v150", "v151", "s40", "s41", "s42", "s43", "s44", "s45", "s46", "s47", "s48", "s49", "s50", "s51", "s52", "s53", "s54", "s55", "s60", "s61"
#define VU_ASM \
   "s_mov_b32 s44, %[rdy]\n\t" "s_mov_b32 s45, 0\n\t" "s_mov_b32 s46, 0\n\t" "s_cmp_eq_u32 s44, -1\n\t" "s_cbranch_scc1 68f\n\t" \
   "s_mul_i32 s52, s45, 1536\n\t" "s_mul_i32 s51, s45, 40960\n\t" "s_add_u32 m0, s46, %[ldx]\n\t" "s_mov_b32 s53, s46\n\t" \
   "buffer_load_dwordx4 v16, %[rs], s51 offen offset:0 sc1 lds\n\t" "buffer_load_dwordx4 v16, %[rs], s51 offen offset:1024 sc1 lds\n\t" \
   "buffer_load_dwordx4 v16, %[rs], s51 offen offset:2048 sc1 lds\n\t" "buffer_load_dwordx4 v16, %[rs], s51 offen offset:3072 sc1 lds\n\t" \
   "s_add_u32 m0, m0, 4096\n\t" "s_add_u32 s51, s51, 4096\n\t" "buffer_load_dwordx4 v16, %[rs], s51 offen offset:0 sc1 lds\n\t" \
   "buffer_load_dwordx4 v16, %[rs], s51 offen offset:1024 sc1 lds\n\t" "buffer_load_dwordx4 v16, %[rs], s51 offen offset:2048 sc1 lds\n\t" \
   "buffer_load_dwordx4 v16, %[rs], s51 offen offset:3072 sc1 lds\n\t" "s_add_u32 m0, m0, 4096\n\t" "s_add_u32 s51, s51, 4096\n\t" \
   "buffer_load_dwordx4 v16, %[rs], s51 offen offset:0 sc1 lds\n\t" "buffer_load_dwordx4 v16, %[rs], s51 offen offset:1024 sc1 lds\n\t" \
   "s_add_u32 m0, s53, %[ld1]\n\t" "s_nop 0\n\t" "buffer_load_dwordx4 v17, %[rs], s52 offen sc1 lds\n\t" "s_mov_b32 s45, 1\n\t" \
   "s_mov_b32 s46, 44032\n\t" "s_mul_i32 s52, s45, 1536\n\t" "s_mul_i32 s51, s45, 40960\n\t" "s_add_u32 m0, s46, %[ldx]\n\t" "s_mov_b32 s53, s46\n\t" \
   "buffer_load_dwordx4 v16, %[rs], s51 offen offset:0 sc1 lds\n\t" "buffer_load_dwordx4 v16, %[rs], s51 offen offset:1024 sc1 lds\n\t" \
   "buffer_load_dwordx4 v16, %[rs], s51 offen offset:2048 sc1 lds\n\t" "buffer_load_dwordx4 v16, %[rs], s51 offen offset:3072 sc1 lds\n\t" \
   "s_add_u32 m0, m0, 4096\n\t" "s_add_u32 s51, s51, 4096\n\t" "buffer_load_dwordx4 v16, %[rs], s51 offen offset:0 sc1 lds\n\t" \
   "buffer_load_dwordx4 v16, %[rs], s51 offen offset:1024 sc1 lds\n\t" "buffer_load_dwordx4 v16, %[rs], s51 offen offset:2048 sc1 lds\n\t" \
   "buffer_load_dwordx4 v16, %[rs], s51 offen offset:3072 sc1 lds\n\t" "s_add_u32 m0, m0, 4096\n\t" "s_add_u32 s51, s51, 4096\n\t" \
   "buffer_load_dwordx4 v16, %[rs], s51 offen offset:0 sc1 lds\n\t" "buffer_load_dwordx4 v16, %[rs], s51 offen offset:1024 sc1 lds\n\t" \
   "s_add_u32 m0, s53, %[ld1]\n\t" "s_nop 0\n\t" "buffer_load_dwordx4 v17, %[rs], s52 offen sc1 lds\n\t" "68:\n\t" "v_mov_b32 v80, 0x3f803f80\n\t" \
   "v_mov_b32 v81, 0x3f803f80\n\t" "v_mov_b32 v82, 0x3f803f80\n\t" "v_mov_b32 v83, 0x3f803f80\n\t" "v_mov_b32 v31, 1\n\t" "s_nop 1\n\t" \
   "v_mfma_f32_16x16x32_bf16 v[32:35], a[0:3], v[80:83], 0\n\t" "v_mfma_f32_16x16x32_bf16 v[36:39], a[4:7], v[80:83], 0\n\t" \
   "v_mfma_f32_16x16x32_bf16 v[32:35], a[8:11], v[80:83], v[32:35]\n\t" "v_mfma_f32_16x16x32_bf16 v[36:39], a[12:15], v[80:83], v[36:39]\n\t" \
   "v_mfma_f32_16x16x32_bf16 v[32:35], a[16:19], v[80:83], v[32:35]\n\t" "v_mfma_f32_16x16x32_bf16 v[36:39], a[20:23], v[80:83], v[36:39]\n\t" \
   "v_mfma_f32_16x16x32_bf16 v[32:35], a[24:27], v[80:83], v[32:35]\n\t" "v_mfma_f32_16x16x32_bf16 v[36:39], a[28:31], v[80:83], v[36:39]\n\t" \
   "v_mfma_f32_16x16x32_bf16 v[32:35], a[32:35], v[80:83], v[32:35]\n\t" "v_mfma_f32_16x16x32_bf16 v[36:39], a[36:39], v[80:83], v[36:39]\n\t" \
   "v_mfma_f32_16x16x32_bf16 v[32:35], a[40:43], v[80:83], v[32:35]\n\t" "v_mfma_f32_16x16x32_bf16 v[36:39], a[44:47], v[80:83], v[36:39]\n\t" \
   "v_mfma_f32_16x16x32_bf16 v[32:35], a[48:51], v[80:83], v[32:35]\n\t" "v_mfma_f32_16x16x32_bf16 v[36:39], a[52:55], v[80:83], v[36:39]\n\t" \
   "v_mfma_f32_16x16x32_bf16 v[32:35], a[56:59], v[80:83], v[32:35]\n\t" "v_mfma_f32_16x16x32_bf16 v[36:39], a[60:63], v[80:83], v[36:39]\n\t" \
   "v_mfma_f32_16x16x32_bf16 v[32:35], a[64:67], v[80:83], v[32:35]\n\t" "v_mfma_f32_16x16x32_bf16 v[36:39], a[68:71], v[80:83], v[36:39]\n\t" \
   "v_mfma_f32_16x16x32_bf16 v[32:35], a[72:75], v[80:83], v[32:35]\n\t" "v_mfma_f32_16x16x32_bf16 v[36:39], a[76:79], v[80:83], v[36:39]\n\t" \
   "v_mfma_f32_16x16x32_bf16 v[32:35], a[80:83], v[80:83], v[32:35]\n\t" "v_mfma_f32_16x16x32_bf16 v[36:39], a[84:87], v[80:83], v[36:39]\n\t" \
   "v_mfma_f32_16x16x32_bf16 v[32:35], a[88:91], v[80:83], v[32:35]\n\t" "v_mfma_f32_16x16x32_bf16 v[36:39], a[92:95], v[80:83], v[36:39]\n\t" \
   "v_mfma_f32_16x16x32_bf16 v[32:35], a[96:99], v[80:83], v[32:35]\n\t" "v_mfma_f32_16x16x32_bf16 v[36:39], a[100:103], v[80:83], v[36:39]\n\t" \
   "v_mfma_f32_16x16x32_bf16 v[32:35], a[104:107], v[80:83], v[32:35]\n\t" "v_mfma_f32_16x16x32_bf16 v[36:39], a[108:111], v[80:83], v[36:39]\n\t" \
   "v_mfma_f32_16x16x32_bf16 v[32:35], a[112:115], v[80:83], v[32:35]\n\t" "v_mfma_f32_16x16x32_bf16 v[36:39], a[116:119], v[80:83], v[36:39]\n\t" \
   "v_mfma_f32_16x16x32_bf16 v[32:35], a[120:123], v[80:83], v[32:35]\n\t" "v_mfma_f32_16x16x32_bf16 v[36:39], a[124:127], v[80:83], v[36:39]\n\t" \
   "v_mfma_f32_16x16x32_bf16 v[32:35], a[128:131], v[80:83], v[32:35]\n\t" "v_mfma_f32_16x16x32_bf16 v[36:39], a[132:135], v[80:83], v[36:39]\n\t" \
   "v_mfma_f32_16x16x32_bf16 v[32:35], a[136:139], v[80:83], v[32:35]\n\t" "v_mfma_f32_16x16x32_bf16 v[36:39], a[140:143], v[80:83], v[36:39]\n\t" \
   "v_mfma_f32_16x16x32_bf16 v[32:35], a[144:147], v[80:83], v[32:35]\n\t" "v_mfma_f32_16x16x32_bf16 v[36:39], a[148:151], v[80:83], v[36:39]\n\t" \
   "v_mfma_f32_16x16x32_bf16 v[32:35], a[152:155], v[80:83], v[32:35]\n\t" "v_mfma_f32_16x16x32_bf16 v[36:39], a[156:159], v[80:83], v[36:39]\n\t" \
   "s_mov_b32 s40, 0\n\t" "s_mov_b32 s41, 0\n\t" "s_mov_b32 s42, 0\n\t" "s_mov_b32 s43, 44032\n\t" "v_mov_b32 v28, v22\n\t" "v_mov_b32 v29, v23\n\t" \
   "s_mov_b32 s45, 2\n\t" "s_mov_b32 s46, 88064\n\t" "s_waitcnt vmcnt(11)\n\t" "s_barrier\n\t" "s_lshr_b32 s48, s45, 1\n\t" \
   "s_bitcmp1_b32 s44, s48\n\t" "s_cbranch_scc0 172f\n\t" "72:\n\t" "s_mul_i32 s52, s45, 1536\n\t" "s_mul_i32 s51, s45, 40960\n\t" \
   "s_add_u32 m0, s46, %[ldx]\n\t" "s_mov_b32 s53, s46\n\t" "buffer_load_dwordx4 v16, %[rs], s51 offen offset:0 sc1 lds\n\t" \
   "ds_read_b128 v[80:83], v28 offset:0\n\t" "ds_read_b128 v[84:87], v28 offset:1024\n\t" "ds_read_b128 v[88:91], v28 offset:2048\n\t" \
   "ds_read_b128 v[92:95], v28 offset:3072\n\t" "ds_read_b128 v[96:99], v28 offset:4096\n\t" "ds_read_b128 v[100:103], v28 offset:5120\n\t" \
   "ds_read_b128 v[104:107], v28 offset:6144\n\t" "v_mov_b32 v14, v24\n\t" "v_mov_b32 v15, v25\n\t" "v_mov_b32 v18, 0x7ffffff0\n\t" \
   "s_mov_b32 s49, 0\n\t" "v_mov_b32 v26, v28\n\t" "v_mov_b32 v27, v29\n\t" "s_waitcnt lgkmcnt(5)\n\t" \
   "v_mfma_f32_16x16x32_bf16 v[48:51], a[0:3], v[80:83], 0\n\t" "ds_read_b128 v[112:115], v27 offset:0\n\t" \
   "v_mfma_f32_16x16x32_bf16 v[52:55], a[4:7], v[80:83], 0\n\t" "ds_read_b128 v[108:111], v26 offset:7168\n\t" \
   "v_mfma_f32_16x16x32_bf16 v[48:51], a[8:11], v[84:87], v[48:51]\n\t" "ds_read_b128 v[116:119], v27 offset:16\n\t" \
   "v_mfma_f32_16x16x32_bf16 v[52:55], a[12:15], v[84:87], v[52:55]\n\t" "ds_read_b128 v[80:83], v26 offset:8192\n\t" "s_waitcnt lgkmcnt(7)\n\t" \
   "v_mfma_f32_16x16x32_bf16 v[48:51], a[16:19], v[88:91], v[48:51]\n\t" "ds_read_b64 v[120:121], v27 offset:32\n\t" \
   "v_mfma_f32_16x16x32_bf16 v[52:55], a[20:23], v[88:91], v[52:55]\n\t" "ds_read_b128 v[84:87], v26 offset:9216\n\t" \
   "v_mfma_f32_16x16x32_bf16 v[48:51], a[24:27], v[92:95], v[48:51]\n\t" "ds_read_b128 v[122:125], v27 offset:768\n\t" \
   "v_mfma_f32_16x16x32_bf16 v[52:55], a[28:31], v[92:95], v[52:55]\n\t" "ds_read_b128 v[88:91], v26 offset:10240\n\t" "s_waitcnt lgkmcnt(9)\n\t" \
   "v_mfma_f32_16x16x32_bf16 v[48:51], a[32:35], v[96:99], v[48:51]\n\t" "buffer_load_dwordx4 v16, %[rs], s51 offen offset:1024 sc1 lds\n\t" \
   "v_mfma_f32_16x16x32_bf16 v[52:55], a[36:39], v[96:99], v[52:55]\n\t" "ds_read_b128 v[92:95], v26 offset:11264\n\t" \
   "v_mfma_f32_16x16x32_bf16 v[48:51], a[40:43], v[100:103], v[48:51]\n\t" "ds_read_b128 v[126:129], v27 offset:784\n\t" \
   "v_mfma_f32_16x16x32_bf16 v[52:55], a[44:47], v[100:103], v[52:55]\n\t" "ds_read_b128 v[96:99], v26 offset:12288\n\t" "s_waitcnt lgkmcnt(9)\n\t" \
   "v_mfma_f32_16x16x32_bf16 v[48:51], a[48:51], v[104:107], v[48:51]\n\t" "ds_read_b64 v[130:131], v27 offset:800\n\t" \
   "v_mfma_f32_16x16x32_bf16 v[52:55], a[52:55], v[104:107], v[52:55]\n\t" "ds_read_b128 v[100:103], v26 offset:13312\n\t" \
   "v_mfma_f32_16x16x32_bf16 v[48:51], a[56:59], v[108:111], v[48:51]\n\t" "s_waitcnt lgkmcnt(8)\n\t" "v_add_f32 v112, v112, v114\n\t" \
   "v_mfma_f32_16x16x32_bf16 v[52:55], a[60:63], v[108:111], v[52:55]\n\t" "ds_read_b128 v[104:107], v26 offset:14336\n\t" "s_waitcnt lgkmcnt(8)\n\t" \
   "v_mfma_f32_16x16x32_bf16 v[48:51], a[64:67], v[80:83], v[48:51]\n\t" "buffer_load_dwordx4 v16, %[rs], s51 offen offset:2048 sc1 lds\n\t" \
   "v_mfma_f32_16x16x32_bf16 v[52:55], a[68:71], v[80:83], v[52:55]\n\t" "ds_read_b128 v[108:111], v26 offset:15360\n\t" \
   "v_mfma_f32_16x16x32_bf16 v[48:51], a[72:75], v[84:87], v[48:51]\n\t" "v_add_f32 v113, v113, v115\n\t" "v_add_f32 v116, v116, v118\n\t" \
   "v_mfma_f32_16x16x32_bf16 v[52:55], a[76:79], v[84:87], v[52:55]\n\t" "ds_read_b128 v[80:83], v26 offset:16384\n\t" "s_waitcnt lgkmcnt(7)\n\t" \
   "v_mfma_f32_16x16x32_bf16 v[48:51], a[80:83], v[88:91], v[48:51]\n\t" "v_add_f32 v117, v117, v119\n\t" "v_add_f32 v112, v112, v120\n\t" \
   "v_mfma_f32_16x16x32_bf16 v[52:55], a[84:87], v[88:91], v[52:55]\n\t" "ds_read_b128 v[84:87], v26 offset:17408\n\t" \
   "v_mfma_f32_16x16x32_bf16 v[48:51], a[88:91], v[92:95], v[48:51]\n\t" "v_add_f32 v113, v113, v121\n\t" "v_add_f32 v112, v112, v116\n\t" \
   "v_mfma_f32_16x16x32_bf16 v[52:55], a[92:95], v[92:95], v[52:55]\n\t" "ds_read_b128 v[88:91], v26 offset:18432\n\t" \
   "buffer_load_dwordx4 v16, %[rs], s51 offen offset:3072 sc1 lds\n\t" "s_add_u32 m0, m0, 4096\n\t" "s_add_u32 s51, s51, 4096\n\t" \
   "s_waitcnt lgkmcnt(5)\n\t" "v_mfma_f32_16x16x32_bf16 v[48:51], a[96:99], v[96:99], v[48:51]\n\t" "v_add_f32 v113, v113, v117\n\t" \
   "v_mul_f32 v112, 0xbacccccd, v112\n\t" "v_mfma_f32_16x16x32_bf16 v[52:55], a[100:103], v[96:99], v[52:55]\n\t" \
   "ds_read_b128 v[92:95], v26 offset:19456\n\t" "v_mfma_f32_16x16x32_bf16 v[48:51], a[104:107], v[100:103], v[48:51]\n\t" \
   "v_mul_f32 v113, 0x3acccccd, v113\n\t" "v_fma_f32 v113, -v112, v112, v113\n\t" \
   "v_mfma_f32_16x16x32_bf16 v[52:55], a[108:111], v[100:103], v[52:55]\n\t" "ds_read_b128 v[96:99], v26 offset:20480\n\t" "s_waitcnt lgkmcnt(5)\n\t" \
   "v_mfma_f32_16x16x32_bf16 v[48:51], a[112:115], v[104:107], v[48:51]\n\t" "v_max_f32 v113, 0, v113\n\t" "v_cvt_pk_bf16_f32 v113, v113, v112\n\t" \
   "v_mfma_f32_16x16x32_bf16 v[52:55], a[116:119], v[104:107], v[52:55]\n\t" "ds_read_b128 v[100:103], v26 offset:21504\n\t" \
   "v_mfma_f32_16x16x32_bf16 v[48:51], a[120:123], v[108:111], v[48:51]\n\t" "buffer_load_dwordx4 v16, %[rs], s51 offen offset:0 sc1 lds\n\t" \
   "v_mfma_f32_16x16x32_bf16 v[52:55], a[124:127], v[108:111], v[52:55]\n\t" "ds_read_b128 v[104:107], v26 offset:22528\n\t" "s_waitcnt lgkmcnt(5)\n\t" \
   "v_mfma_f32_16x16x32_bf16 v[48:51], a[128:131], v[80:83], v[48:51]\n\t" "v_and_b32 v8, 0xffff0000, v113\n\t" "v_lshlrev_b32 v113, 16, v113\n\t" \
   "v_mfma_f32_16x16x32_bf16 v[52:55], a[132:135], v[80:83], v[52:55]\n\t" "ds_read_b128 v[108:111], v26 offset:23552\n\t" \
   "v_mfma_f32_16x16x32_bf16 v[48:51], a[136:139], v[84:87], v[48:51]\n\t" "v_add_f32 v113, 0x3727c5ac, v113\n\t" \
   "v_cvt_pk_bf16_f32 v113, v113, v113\n\t" "v_mfma_f32_16x16x32_bf16 v[52:55], a[140:143], v[84:87], v[52:55]\n\t" \
   "ds_read_b128 v[80:83], v26 offset:24576\n\t" "s_waitcnt lgkmcnt(5)\n\t" "v_mfma_f32_16x16x32_bf16 v[48:51], a[144:147], v[88:91], v[48:51]\n\t" \
   "v_lshlrev_b32 v113, 16, v113\n\t" "v_add_u32 v28, s43, v22\n\t" "v_mfma_f32_16x16x32_bf16 v[52:55], a[148:151], v[88:91], v[52:55]\n\t" \
   "ds_read_b128 v[84:87], v26 offset:25600\n\t" "buffer_load_dwordx4 v16, %[rs], s51 offen offset:1024 sc1 lds\n\t" \
   "v_mfma_f32_16x16x32_bf16 v[48:51], a[152:155], v[92:95], v[48:51]\n\t" "v_sqrt_f32 v113, v113\n\t" \
   "v_mfma_f32_16x16x32_bf16 v[52:55], a[156:159], v[92:95], v[52:55]\n\t" "ds_read_b128 v[88:91], v26 offset:26624\n\t" "s_nop 0\n\t" \
   "s_waitcnt lgkmcnt(5)\n\t" "v_mfma_f32_16x16x32_bf16 v[56:59], a[0:3], v[96:99], 0\n\t" "v_cvt_pk_bf16_f32 v113, v113, v113\n\t" \
   "v_lshlrev_b32 v113, 16, v113\n\t" "v_mfma_f32_16x16x32_bf16 v[60:63], a[4:7], v[96:99], 0\n\t" "ds_read_b128 v[92:95], v26 offset:27648\n\t" \
   "v_rcp_f32 v10, v113\n\t" "v_mfma_f32_16x16x32_bf16 v[56:59], a[8:11], v[100:103], v[56:59]\n\t" "v_add_f32 v122, v122, v124\n\t" \
   "v_mfma_f32_16x16x32_bf16 v[60:63], a[12:15], v[100:103], v[60:63]\n\t" "ds_read_b128 v[96:99], v26 offset:28672\n\t" "s_waitcnt lgkmcnt(5)\n\t" \
   "v_mfma_f32_16x16x32_bf16 v[56:59], a[16:19], v[104:107], v[56:59]\n\t" "buffer_load_dwordx4 v16, %[rs], s51 offen offset:2048 sc1 lds\n\t" \
   "v_mfma_f32_16x16x32_bf16 v[60:63], a[20:23], v[104:107], v[60:63]\n\t" "ds_read_b128 v[100:103], v26 offset:29696\n\t" \
   "v_mfma_f32_16x16x32_bf16 v[56:59], a[24:27], v[108:111], v[56:59]\n\t" "v_add_f32 v123, v123, v125\n\t" "v_add_f32 v126, v126, v128\n\t" \
   "v_mfma_f32_16x16x32_bf16 v[60:63], a[28:31], v[108:111], v[60:63]\n\t" "ds_read_b128 v[104:107], v26 offset:30720\n\t" "s_waitcnt lgkmcnt(5)\n\t" \
   "v_mfma_f32_16x16x32_bf16 v[56:59], a[32:35], v[80:83], v[56:59]\n\t" "v_fmac_f32 v48, v32, v8\n\t" "v_add_f32 v127, v127, v129\n\t" \
   "v_mfma_f32_16x16x32_bf16 v[60:63], a[36:39], v[80:83], v[60:63]\n\t" "ds_read_b128 v[108:111], v26 offset:31744\n\t" \
   "v_mfma_f32_16x16x32_bf16 v[56:59], a[40:43], v[84:87], v[56:59]\n\t" "v_fmac_f32 v49, v33, v8\n\t" "v_add_f32 v122, v122, v130\n\t" \
   "v_mfma_f32_16x16x32_bf16 v[60:63], a[44:47], v[84:87], v[60:63]\n\t" "ds_read_b128 v[80:83], v26 offset:32768\n\t" \
   "buffer_load_dwordx4 v16, %[rs], s51 offen offset:3072 sc1 lds\n\t" "s_add_u32 m0, m0, 4096\n\t" "s_add_u32 s51, s51, 4096\n\t" \
   "s_waitcnt lgkmcnt(5)\n\t" "v_mfma_f32_16x16x32_bf16 v[56:59], a[48:51], v[88:91], v[56:59]\n\t" "v_fmac_f32 v50, v34, v8\n\t" \
   "v_add_f32 v123, v123, v131\n\t" "v_mfma_f32_16x16x32_bf16 v[60:63], a[52:55], v[88:91], v[60:63]\n\t" "ds_read_b128 v[84:87], v26 offset:33792\n\t" \
   "v_mfma_f32_16x16x32_bf16 v[56:59], a[56:59], v[92:95], v[56:59]\n\t" "v_fmac_f32 v51, v35, v8\n\t" "v_add_f32 v122, v122, v126\n\t" \
   "v_mfma_f32_16x16x32_bf16 v[60:63], a[60:63], v[92:95], v[60:63]\n\t" "ds_read_b128 v[88:91], v26 offset:34816\n\t" "s_waitcnt lgkmcnt(5)\n\t" \
   "v_mfma_f32_16x16x32_bf16 v[56:59], a[64:67], v[96:99], v[56:59]\n\t" "v_fmac_f32 v52, v36, v8\n\t" "v_add_f32 v123, v123, v127\n\t" \
   "v_mfma_f32_16x16x32_bf16 v[60:63], a[68:71], v[96:99], v[60:63]\n\t" "ds_read_b128 v[92:95], v26 offset:35840\n\t" \
   "v_mfma_f32_16x16x32_bf16 v[56:59], a[72:75], v[100:103], v[56:59]\n\t" "buffer_load_dwordx4 v16, %[rs], s51 offen offset:0 sc1 lds\n\t" \
   "v_mfma_f32_16x16x32_bf16 v[60:63], a[76:79], v[100:103], v[60:63]\n\t" "ds_read_b128 v[96:99], v26 offset:36864\n\t" "s_waitcnt lgkmcnt(5)\n\t" \
   "v_mfma_f32_16x16x32_bf16 v[56:59], a[80:83], v[104:107], v[56:59]\n\t" "v_fmac_f32 v53, v37, v8\n\t" "v_mul_f32 v122, 0xbacccccd, v122\n\t" \
   "v_mfma_f32_16x16x32_bf16 v[60:63], a[84:87], v[104:107], v[60:63]\n\t" "ds_read_b128 v[100:103], v26 offset:37888\n\t" \
   "v_mfma_f32_16x16x32_bf16 v[56:59], a[88:91], v[108:111], v[56:59]\n\t" "v_fmac_f32 v54, v38, v8\n\t" "v_mul_f32 v123, 0x3acccccd, v123\n\t" \
   "v_mfma_f32_16x16x32_bf16 v[60:63], a[92:95], v[108:111], v[60:63]\n\t" "ds_read_b128 v[104:107], v26 offset:38912\n\t" "s_waitcnt lgkmcnt(5)\n\t" \
   "v_mfma_f32_16x16x32_bf16 v[56:59], a[96:99], v[80:83], v[56:59]\n\t" "v_fmac_f32 v55, v39, v8\n\t" "v_fma_f32 v123, -v122, v122, v123\n\t" \
   "v_mfma_f32_16x16x32_bf16 v[60:63], a[100:103], v[80:83], v[60:63]\n\t" "ds_read_b128 v[108:111], v26 offset:39936\n\t" \
   "buffer_load_dwordx4 v16, %[rs], s51 offen offset:1024 sc1 lds\n\t" "s_add_u32 m0, s53, %[ld1]\n\t" "s_nop 0\n\t" \
   "v_mfma_f32_16x16x32_bf16 v[56:59], a[104:107], v[84:87], v[56:59]\n\t" "v_mul_f32 v48, v48, v10\n\t" "v_max_f32 v123, 0, v123\n\t" \
   "v_mfma_f32_16x16x32_bf16 v[60:63], a[108:111], v[84:87], v[60:63]\n\t" "v_mul_f32 v49, v49, v10\n\t" "s_waitcnt lgkmcnt(4)\n\t" \
   "v_mfma_f32_16x16x32_bf16 v[56:59], a[112:115], v[88:91], v[56:59]\n\t" "v_cvt_pk_bf16_f32 v123, v123, v122\n\t" "v_mul_f32 v50, v50, v10\n\t" \
   "v_mfma_f32_16x16x32_bf16 v[60:63], a[116:119], v[88:91], v[60:63]\n\t" "v_and_b32 v12, 0xffff0000, v123\n\t" "v_mul_f32 v51, v51, v10\n\t" \
   "v_mfma_f32_16x16x32_bf16 v[56:59], a[120:123], v[92:95], v[56:59]\n\t" "v_lshlrev_b32 v123, 16, v123\n\t" "v_mul_f32 v52, v52, v10\n\t" \
   "v_mfma_f32_16x16x32_bf16 v[60:63], a[124:127], v[92:95], v[60:63]\n\t" "v_add_f32 v123, 0x3727c5ac, v123\n\t" "s_waitcnt lgkmcnt(2)\n\t" \
   "v_mfma_f32_16x16x32_bf16 v[56:59], a[128:131], v[96:99], v[56:59]\n\t" "v_mul_f32 v53, v53, v10\n\t" "v_cvt_pk_bf16_f32 v123, v123, v123\n\t" \
   "buffer_load_dwordx4 v17, %[rs], s52 offen sc1 lds\n\t" "v_mfma_f32_16x16x32_bf16 v[60:63], a[132:135], v[96:99], v[60:63]\n\t" \
   "v_mul_f32 v54, v54, v10\n\t" "v_lshlrev_b32 v123, 16, v123\n\t" "v_mfma_f32_16x16x32_bf16 v[56:59], a[136:139], v[100:103], v[56:59]\n\t" \
   "v_mul_f32 v55, v55, v10\n\t" "v_cvt_pk_bf16_f32 v136, v48, v49\n\t" "v_mfma_f32_16x16x32_bf16 v[60:63], a[140:143], v[100:103], v[60:63]\n\t" \
   "v_sqrt_f32 v123, v123\n\t" "s_waitcnt lgkmcnt(0)\n\t" "v_mfma_f32_16x16x32_bf16 v[56:59], a[144:147], v[104:107], v[56:59]\n\t" \
   "v_cvt_pk_bf16_f32 v137, v50, v51\n\t" "s_nop 0\n\t" "v_mfma_f32_16x16x32_bf16 v[60:63], a[148:151], v[104:107], v[60:63]\n\t" \
   "v_cvt_pk_bf16_f32 v138, v52, v53\n\t" "v_cvt_pk_bf16_f32 v123, v123, v123\n\t" \
   "v_mfma_f32_16x16x32_bf16 v[56:59], a[152:155], v[108:111], v[56:59]\n\t" "v_cvt_pk_bf16_f32 v139, v54, v55\n\t" "v_lshlrev_b32 v123, 16, v123\n\t" \
   "ds_write_b128 v14, v[136:139] offset:0\n\t" "v_mfma_f32_16x16x32_bf16 v[60:63], a[156:159], v[108:111], v[60:63]\n\t" "v_rcp_f32 v132, v123\n\t" \
   "v_add_u32 v29, s43, v23\n\t" "s_memrealtime s[54:55]\n\t" "s_nop 6\n\t" "v_fmac_f32 v56, v32, v12\n\t" "v_fmac_f32 v57, v33, v12\n\t" \
   "v_fmac_f32 v58, v34, v12\n\t" "v_fmac_f32 v59, v35, v12\n\t" "v_fmac_f32 v60, v36, v12\n\t" "v_fmac_f32 v61, v37, v12\n\t" \
   "v_fmac_f32 v62, v38, v12\n\t" "v_fmac_f32 v63, v39, v12\n\t" "v_mul_f32 v56, v56, v132\n\t" "v_mul_f32 v57, v57, v132\n\t" \
   "v_mul_f32 v58, v58, v132\n\t" "v_mul_f32 v59, v59, v132\n\t" "v_mul_f32 v60, v60, v132\n\t" "v_mul_f32 v61, v61, v132\n\t" \
   "v_mul_f32 v62, v62, v132\n\t" "v_mul_f32 v63, v63, v132\n\t" "v_cvt_pk_bf16_f32 v140, v56, v57\n\t" "v_cvt_pk_bf16_f32 v141, v58, v59\n\t" \
   "v_cvt_pk_bf16_f32 v142, v60, v61\n\t" "v_cvt_pk_bf16_f32 v143, v62, v63\n\t" "ds_write_b128 v14, v[140:143] offset:4096\n\t" \
   "s_waitcnt vmcnt(11) lgkmcnt(0)\n\t" "s_barrier\n\t" "ds_read_b128 v[80:83], v28 offset:0\n\t" "ds_read_b128 v[84:87], v28 offset:1024\n\t" \
   "ds_read_b128 v[88:91], v28 offset:2048\n\t" "ds_read_b128 v[92:95], v28 offset:3072\n\t" "ds_read_b128 v[96:99], v28 offset:4096\n\t" \
   "ds_read_b128 v[100:103], v28 offset:5120\n\t" "ds_read_b128 v[104:107], v28 offset:6144\n\t" "s_add_u32 s45, s40, 3\n\t" \
   "s_min_u32 s45, s45, 63\n\t" "s_mov_b32 s46, s42\n\t" "s_lshr_b32 s48, s45, 1\n\t" "s_bitcmp1_b32 s44, s48\n\t" "s_cbranch_scc0 176f\n\t" "76:\n\t" \
   "s_mul_i32 s52, s45, 1536\n\t" "s_mul_i32 s51, s45, 40960\n\t" "s_add_u32 m0, s46, %[ldx]\n\t" "s_mov_b32 s53, s46\n\t" \
   "buffer_load_dwordx4 v16, %[rs], s51 offen offset:0 sc1 lds\n\t" "s_mov_b32 s42, s43\n\t" "s_add_u32 s43, s43, 44032\n\t" \
   "s_cmp_eq_u32 s43, 132096\n\t" "s_cselect_b32 s43, 0, s43\n\t" "s_add_u32 s41, s41, 1\n\t" "s_add_u32 s40, s40, 1\n\t" \
   "v_xor_b32 v14, 0x2000, v14\n\t" "v_xor_b32 v15, 0x2000, v15\n\t" "v_mov_b32 v31, s54\n\t" "v_min_u32 v31, -2, v31\n\t" "1:\n\t" \
   "v_mov_b32 v26, v28\n\t" "v_mov_b32 v27, v29\n\t" "s_waitcnt lgkmcnt(5)\n\t" "v_mfma_f32_16x16x32_bf16 v[48:51], a[0:3], v[80:83], 0\n\t" \
   "ds_read_b128 v[152:155], v15 offset:0\n\t" "ds_read_b128 v[168:171], v15 offset:4096\n\t" \
   "v_mfma_f32_16x16x32_bf16 v[52:55], a[4:7], v[80:83], 0\n\t" "ds_read_b128 v[108:111], v26 offset:7168\n\t" \
   "ds_read_b128 v[156:159], v15 offset:1024\n\t" "v_mfma_f32_16x16x32_bf16 v[48:51], a[8:11], v[84:87], v[48:51]\n\t" \
   "ds_read_b128 v[172:175], v15 offset:5120\n\t" "v_mfma_f32_16x16x32_bf16 v[52:55], a[12:15], v[84:87], v[52:55]\n\t" \
   "ds_read_b128 v[80:83], v26 offset:8192\n\t" "ds_read_b128 v[160:163], v15 offset:2048\n\t" "s_waitcnt lgkmcnt(10)\n\t" \
   "v_mfma_f32_16x16x32_bf16 v[48:51], a[16:19], v[88:91], v[48:51]\n\t" "ds_read_b128 v[176:179], v15 offset:6144\n\t" \
   "v_mfma_f32_16x16x32_bf16 v[52:55], a[20:23], v[88:91], v[52:55]\n\t" "ds_read_b128 v[84:87], v26 offset:9216\n\t" \
   "ds_read_b128 v[164:167], v15 offset:3072\n\t" "v_mfma_f32_16x16x32_bf16 v[48:51], a[24:27], v[92:95], v[48:51]\n\t" \
   "ds_read_b128 v[180:183], v15 offset:7168\n\t" "v_mfma_f32_16x16x32_bf16 v[52:55], a[28:31], v[92:95], v[52:55]\n\t" \
   "ds_read_b128 v[88:91], v26 offset:10240\n\t" "s_waitcnt lgkmcnt(13)\n\t" "v_mfma_f32_16x16x32_bf16 v[48:51], a[32:35], v[96:99], v[48:51]\n\t" \
   "v_cvt_pk_bf16_f32 v148, v72, v73\n\t" "v_cvt_pk_bf16_f32 v150, v74, v75\n\t" "v_mfma_f32_16x16x32_bf16 v[52:55], a[36:39], v[96:99], v[52:55]\n\t" \
   "ds_read_b128 v[92:95], v26 offset:11264\n\t" "s_waitcnt lgkmcnt(8)\n\t" "v_mfma_f32_16x16x32_bf16 v[64:67], v[152:155], a[160:163], 0\n\t" \
   "v_cvt_pk_bf16_f32 v149, v76, v77\n\t" "v_cvt_pk_bf16_f32 v151, v78, v79\n\t" \
   "v_mfma_f32_16x16x32_bf16 v[48:51], a[40:43], v[100:103], v[48:51]\n\t" "s_add_u32 s50, s49, 294912\n\t" \
   "v_mfma_f32_16x16x32_bf16 v[52:55], a[44:47], v[100:103], v[52:55]\n\t" "ds_read_b128 v[96:99], v26 offset:12288\n\t" \
   "v_mfma_f32_16x16x32_bf16 v[68:71], v[168:171], a[160:163], 0\n\t" "v_mfma_f32_16x16x32_bf16 v[48:51], a[48:51], v[104:107], v[48:51]\n\t" \
   "buffer_store_dwordx4 v[148:151], v18, %[rs], s50 offen sc1\n\t" "v_mfma_f32_16x16x32_bf16 v[52:55], a[52:55], v[104:107], v[52:55]\n\t" \
   "ds_read_b128 v[100:103], v26 offset:13312\n\t" "s_sub_i32 s49, s41, 1\n\t" \
   "v_mfma_f32_16x16x32_bf16 v[64:67], v[156:159], a[164:167], v[64:67]\n\t" "s_mul_i32 s49, s49, 1310720\n\t" \
   "v_mfma_f32_16x16x32_bf16 v[48:51], a[56:59], v[108:111], v[48:51]\n\t" "ds_read_b128 v[112:115], v27 offset:0\n\t" \
   "v_mfma_f32_16x16x32_bf16 v[52:55], a[60:63], v[108:111], v[52:55]\n\t" "ds_read_b128 v[104:107], v26 offset:14336\n\t" \
   "v_mfma_f32_16x16x32_bf16 v[68:71], v[172:175], a[164:167], v[68:71]\n\t" "s_waitcnt lgkmcnt(8)\n\t" \
   "v_mfma_f32_16x16x32_bf16 v[48:51], a[64:67], v[80:83], v[48:51]\n\t" "ds_read_b128 v[116:119], v27 offset:16\n\t" \
   "v_mfma_f32_16x16x32_bf16 v[52:55], a[68:71], v[80:83], v[52:55]\n\t" "ds_read_b128 v[108:111], v26 offset:15360\n\t" \
   "ds_read_b64 v[120:121], v27 offset:32\n\t" "s_waitcnt lgkmcnt(9)\n\t" "v_mfma_f32_16x16x32_bf16 v[64:67], v[160:163], a[168:171], v[64:67]\n\t" \
   "ds_read_b128 v[122:125], v27 offset:768\n\t" "v_mfma_f32_16x16x32_bf16 v[48:51], a[72:75], v[84:87], v[48:51]\n\t" \
   "ds_read_b128 v[126:129], v27 offset:784\n\t" "v_mfma_f32_16x16x32_bf16 v[52:55], a[76:79], v[84:87], v[52:55]\n\t" \
   "ds_read_b128 v[80:83], v26 offset:16384\n\t" "ds_read_b64 v[130:131], v27 offset:800\n\t" \
   "v_mfma_f32_16x16x32_bf16 v[68:71], v[176:179], a[168:171], v[68:71]\n\t" "v_add_u32 v28, s43, v22\n\t" "s_waitcnt lgkmcnt(11)\n\t" \
   "v_mfma_f32_16x16x32_bf16 v[48:51], a[80:83], v[88:91], v[48:51]\n\t" "buffer_load_dwordx4 v16, %[rs], s51 offen offset:1024 sc1 lds\n\t" \
   "v_mfma_f32_16x16x32_bf16 v[52:55], a[84:87], v[88:91], v[52:55]\n\t" "ds_read_b128 v[84:87], v26 offset:17408\n\t" \
   "v_mfma_f32_16x16x32_bf16 v[48:51], a[88:91], v[92:95], v[48:51]\n\t" "s_waitcnt lgkmcnt(5)\n\t" "v_add_f32 v112, v112, v114\n\t" \
   "v_mfma_f32_16x16x32_bf16 v[64:67], v[164:167], a[172:175], v[64:67]\n\t" "v_add_f32 v113, v113, v115\n\t" "v_add_f32 v116, v116, v118\n\t" \
   "v_mfma_f32_16x16x32_bf16 v[52:55], a[92:95], v[92:95], v[52:55]\n\t" "ds_read_b128 v[88:91], v26 offset:18432\n\t" \
   "v_mfma_f32_16x16x32_bf16 v[48:51], a[96:99], v[96:99], v[48:51]\n\t" "v_add_f32 v117, v117, v119\n\t" "v_add_f32 v112, v112, v120\n\t" \
   "v_mfma_f32_16x16x32_bf16 v[68:71], v[180:183], a[172:175], v[68:71]\n\t" "v_add_f32 v113, v113, v121\n\t" "v_add_f32 v112, v112, v116\n\t" \
   "v_mfma_f32_16x16x32_bf16 v[52:55], a[100:103], v[96:99], v[52:55]\n\t" "ds_read_b128 v[92:95], v26 offset:19456\n\t" \
   "v_mfma_f32_16x16x32_bf16 v[48:51], a[104:107], v[100:103], v[48:51]\n\t" "v_add_f32 v113, v113, v117\n\t" "v_mul_f32 v112, 0xbacccccd, v112\n\t" \
   "v_mfma_f32_16x16x32_bf16 v[72:75], v[152:155], a[176:179], 0\n\t" "v_cvt_pk_bf16_f32 v144, v64, v65\n\t" "v_mul_f32 v113, 0x3acccccd, v113\n\t" \
   "v_mfma_f32_16x16x32_bf16 v[52:55], a[108:111], v[100:103], v[52:55]\n\t" "ds_read_b128 v[96:99], v26 offset:20480\n\t" \
   "buffer_load_dwordx4 v16, %[rs], s51 offen offset:2048 sc1 lds\n\t" "v_mfma_f32_16x16x32_bf16 v[48:51], a[112:115], v[104:107], v[48:51]\n\t" \
   "v_cvt_pk_bf16_f32 v146, v66, v67\n\t" "v_cvt_pk_bf16_f32 v145, v68, v69\n\t" "v_mfma_f32_16x16x32_bf16 v[76:79], v[168:171], a[176:179], 0\n\t" \
   "v_fma_f32 v113, -v112, v112, v113\n\t" "v_cvt_pk_bf16_f32 v147, v70, v71\n\t" \
   "v_mfma_f32_16x16x32_bf16 v[52:55], a[116:119], v[104:107], v[52:55]\n\t" "ds_read_b128 v[100:103], v26 offset:21504\n\t" \
   "v_mfma_f32_16x16x32_bf16 v[48:51], a[120:123], v[108:111], v[48:51]\n\t" "s_add_u32 s50, s49, 0\n\t" "v_max_f32 v113, 0, v113\n\t" \
   "v_mfma_f32_16x16x32_bf16 v[72:75], v[156:159], a[180:183], v[72:75]\n\t" "buffer_store_dwordx4 v[144:147], v21, %[rs], s50 offen sc1\n\t" \
   "v_mfma_f32_16x16x32_bf16 v[52:55], a[124:127], v[108:111], v[52:55]\n\t" "ds_read_b128 v[104:107], v26 offset:22528\n\t" "s_waitcnt lgkmcnt(5)\n\t" \
   "v_mfma_f32_16x16x32_bf16 v[48:51], a[128:131], v[80:83], v[48:51]\n\t" "v_cvt_pk_bf16_f32 v113, v113, v112\n\t" \
   "v_and_b32 v8, 0xffff0000, v113\n\t" "v_mfma_f32_16x16x32_bf16 v[52:55], a[132:135], v[80:83], v[52:55]\n\t" \
   "ds_read_b128 v[108:111], v26 offset:23552\n\t" "v_mfma_f32_16x16x32_bf16 v[76:79], v[172:175], a[180:183], v[76:79]\n\t" \
   "v_lshlrev_b32 v113, 16, v113\n\t" "v_add_f32 v113, 0x3727c5ac, v113\n\t" "v_mfma_f32_16x16x32_bf16 v[48:51], a[136:139], v[84:87], v[48:51]\n\t" \
   "buffer_load_dwordx4 v16, %[rs], s51 offen offset:3072 sc1 lds\n\t" "s_add_u32 m0, m0, 4096\n\t" "s_add_u32 s51, s51, 4096\n\t" \
   "v_mfma_f32_16x16x32_bf16 v[52:55], a[140:143], v[84:87], v[52:55]\n\t" "ds_read_b128 v[80:83], v26 offset:24576\n\t" \
   "v_mfma_f32_16x16x32_bf16 v[72:75], v[160:163], a[184:187], v[72:75]\n\t" "v_cvt_pk_bf16_f32 v113, v113, v113\n\t" "s_waitcnt lgkmcnt(5)\n\t" \
   "v_mfma_f32_16x16x32_bf16 v[48:51], a[144:147], v[88:91], v[48:51]\n\t" "v_lshlrev_b32 v113, 16, v113\n\t" "v_add_u32 v29, s43, v23\n\t" \
   "v_mfma_f32_16x16x32_bf16 v[52:55], a[148:151], v[88:91], v[52:55]\n\t" "ds_read_b128 v[84:87], v26 offset:25600\n\t" \
   "v_mfma_f32_16x16x32_bf16 v[76:79], v[176:179], a[184:187], v[76:79]\n\t" "v_sqrt_f32 v113, v113\n\t" \
   "v_mfma_f32_16x16x32_bf16 v[48:51], a[152:155], v[92:95], v[48:51]\n\t" "s_nop 0\n\t" "v_cvt_pk_bf16_f32 v113, v113, v113\n\t" \
   "v_mfma_f32_16x16x32_bf16 v[52:55], a[156:159], v[92:95], v[52:55]\n\t" "ds_read_b128 v[88:91], v26 offset:26624\n\t" \
   "v_mfma_f32_16x16x32_bf16 v[72:75], v[164:167], a[188:191], v[72:75]\n\t" "v_lshlrev_b32 v113, 16, v113\n\t" "v_rcp_f32 v10, v113\n\t" \
   "s_waitcnt lgkmcnt(5)\n\t" "v_mfma_f32_16x16x32_bf16 v[56:59], a[0:3], v[96:99], 0\n\t" \
   "buffer_load_dwordx4 v16, %[rs], s51 offen offset:0 sc1 lds\n\t" "v_mfma_f32_16x16x32_bf16 v[60:63], a[4:7], v[96:99], 0\n\t" \
   "ds_read_b128 v[92:95], v26 offset:27648\n\t" "v_mfma_f32_16x16x32_bf16 v[76:79], v[180:183], a[188:191], v[76:79]\n\t" \
   "v_add_f32 v122, v122, v124\n\t" "v_mfma_f32_16x16x32_bf16 v[56:59], a[8:11], v[100:103], v[56:59]\n\t" "v_add_f32 v123, v123, v125\n\t" \
   "v_add_f32 v126, v126, v128\n\t" "v_mfma_f32_16x16x32_bf16 v[60:63], a[12:15], v[100:103], v[60:63]\n\t" \
   "ds_read_b128 v[96:99], v26 offset:28672\n\t" "s_waitcnt lgkmcnt(5)\n\t" "v_mfma_f32_16x16x32_bf16 v[56:59], a[16:19], v[104:107], v[56:59]\n\t" \
   "v_cvt_pk_bf16_f32 v148, v72, v73\n\t" "v_cvt_pk_bf16_f32 v150, v74, v75\n\t" "v_mfma_f32_16x16x32_bf16 v[64:67], v[152:155], a[192:195], 0\n\t" \
   "v_cvt_pk_bf16_f32 v149, v76, v77\n\t" "v_cvt_pk_bf16_f32 v151, v78, v79\n\t" \
   "v_mfma_f32_16x16x32_bf16 v[60:63], a[20:23], v[104:107], v[60:63]\n\t" "ds_read_b128 v[100:103], v26 offset:29696\n\t" \
   "v_mfma_f32_16x16x32_bf16 v[56:59], a[24:27], v[108:111], v[56:59]\n\t" "s_add_u32 s50, s49, 32768\n\t" "v_fmac_f32 v48, v32, v8\n\t" \
   "v_mfma_f32_16x16x32_bf16 v[68:71], v[168:171], a[192:195], 0\n\t" "buffer_store_dwordx4 v[148:151], v21, %[rs], s50 offen sc1\n\t" \
   "v_mfma_f32_16x16x32_bf16 v[60:63], a[28:31], v[108:111], v[60:63]\n\t" "ds_read_b128 v[104:107], v26 offset:30720\n\t" "s_waitcnt lgkmcnt(5)\n\t" \
   "v_mfma_f32_16x16x32_bf16 v[56:59], a[32:35], v[80:83], v[56:59]\n\t" "buffer_load_dwordx4 v16, %[rs], s51 offen offset:1024 sc1 lds\n\t" \
   "v_mfma_f32_16x16x32_bf16 v[64:67], v[156:159], a[196:199], v[64:67]\n\t" "v_add_f32 v127, v127, v129\n\t" "v_fmac_f32 v49, v33, v8\n\t" \
   "v_mfma_f32_16x16x32_bf16 v[60:63], a[36:39], v[80:83], v[60:63]\n\t" "ds_read_b128 v[108:111], v26 offset:31744\n\t" \
   "v_mfma_f32_16x16x32_bf16 v[56:59], a[40:43], v[84:87], v[56:59]\n\t" "v_add_f32 v122, v122, v130\n\t" "v_fmac_f32 v50, v34, v8\n\t" \
   "v_mfma_f32_16x16x32_bf16 v[68:71], v[172:175], a[196:199], v[68:71]\n\t" "v_add_f32 v123, v123, v131\n\t" "v_fmac_f32 v51, v35, v8\n\t" \
   "v_mfma_f32_16x16x32_bf16 v[60:63], a[44:47], v[84:87], v[60:63]\n\t" "ds_read_b128 v[80:83], v26 offset:32768\n\t" "s_waitcnt lgkmcnt(5)\n\t" \
   "v_mfma_f32_16x16x32_bf16 v[56:59], a[48:51], v[88:91], v[56:59]\n\t" "v_add_f32 v122, v122, v126\n\t" "v_fmac_f32 v52, v36, v8\n\t" \
   "v_mfma_f32_16x16x32_bf16 v[64:67], v[160:163], a[200:203], v[64:67]\n\t" "v_add_f32 v123, v123, v127\n\t" "v_fmac_f32 v53, v37, v8\n\t" \
   "v_mfma_f32_16x16x32_bf16 v[60:63], a[52:55], v[88:91], v[60:63]\n\t" "ds_read_b128 v[84:87], v26 offset:33792\n\t" \
   "v_mfma_f32_16x16x32_bf16 v[56:59], a[56:59], v[92:95], v[56:59]\n\t" "s_cmp_lt_i32 s40, 2\n\t" "s_cselect_b32 s47, 0x7ffffff0, 0\n\t" \
   "v_mfma_f32_16x16x32_bf16 v[68:71], v[176:179], a[200:203], v[68:71]\n\t" "v_or_b32 v30, s47, v20\n\t" "s_sub_i32 s47, s41, 2\n\t" \
   "buffer_load_dwordx4 v16, %[rs], s51 offen offset:2048 sc1 lds\n\t" "v_mfma_f32_16x16x32_bf16 v[60:63], a[60:63], v[92:95], v[60:63]\n\t" \
   "ds_read_b128 v[88:91], v26 offset:34816\n\t" "s_max_i32 s47, s47, 0\n\t" "s_waitcnt lgkmcnt(5)\n\t" \
   "v_mfma_f32_16x16x32_bf16 v[56:59], a[64:67], v[96:99], v[56:59]\n\t" "s_lshl_b32 s47, s47, 12\n\t" "s_waitcnt vmcnt(8)\n\t" \
   "v_mfma_f32_16x16x32_bf16 v[60:63], a[68:71], v[96:99], v[60:63]\n\t" "ds_read_b128 v[92:95], v26 offset:35840\n\t" \
   "buffer_store_dword v31, v30, %[rs], s47 offen sc1\n\t" "v_mfma_f32_16x16x32_bf16 v[64:67], v[164:167], a[204:207], v[64:67]\n\t" \
   "v_fmac_f32 v54, v38, v8\n\t" "v_mul_f32 v122, 0xbacccccd, v122\n\t" "v_mfma_f32_16x16x32_bf16 v[56:59], a[72:75], v[100:103], v[56:59]\n\t" \
   "v_mul_f32 v123, 0x3acccccd, v123\n\t" "v_fmac_f32 v55, v39, v8\n\t" "v_mfma_f32_16x16x32_bf16 v[60:63], a[76:79], v[100:103], v[60:63]\n\t" \
   "ds_read_b128 v[96:99], v26 offset:36864\n\t" "v_mfma_f32_16x16x32_bf16 v[68:71], v[180:183], a[204:207], v[68:71]\n\t" \
   "v_fma_f32 v123, -v122, v122, v123\n\t" "s_waitcnt lgkmcnt(5)\n\t" "v_mfma_f32_16x16x32_bf16 v[56:59], a[80:83], v[104:107], v[56:59]\n\t" \
   "v_mul_f32 v48, v48, v10\n\t" "v_max_f32 v123, 0, v123\n\t" "v_mfma_f32_16x16x32_bf16 v[60:63], a[84:87], v[104:107], v[60:63]\n\t" \
   "ds_read_b128 v[100:103], v26 offset:37888\n\t" "v_mfma_f32_16x16x32_bf16 v[72:75], v[152:155], a[208:211], 0\n\t" \
   "buffer_load_dwordx4 v16, %[rs], s51 offen offset:3072 sc1 lds\n\t" "s_add_u32 m0, m0, 4096\n\t" "s_add_u32 s51, s51, 4096\n\t" \
   "v_mfma_f32_16x16x32_bf16 v[56:59], a[88:91], v[108:111], v[56:59]\n\t" "v_cvt_pk_bf16_f32 v144, v64, v65\n\t" \
   "v_cvt_pk_bf16_f32 v146, v66, v67\n\t" "v_mfma_f32_16x16x32_bf16 v[60:63], a[92:95], v[108:111], v[60:63]\n\t" \
   "ds_read_b128 v[104:107], v26 offset:38912\n\t" "v_mfma_f32_16x16x32_bf16 v[76:79], v[168:171], a[208:211], 0\n\t" \
   "v_cvt_pk_bf16_f32 v145, v68, v69\n\t" "s_waitcnt lgkmcnt(5)\n\t" "v_mfma_f32_16x16x32_bf16 v[56:59], a[96:99], v[80:83], v[56:59]\n\t" \
   "v_cvt_pk_bf16_f32 v147, v70, v71\n\t" "s_add_u32 s50, s49, 65536\n\t" "v_mfma_f32_16x16x32_bf16 v[60:63], a[100:103], v[80:83], v[60:63]\n\t" \
   "ds_read_b128 v[108:111], v26 offset:39936\n\t" "v_mfma_f32_16x16x32_bf16 v[72:75], v[156:159], a[212:215], v[72:75]\n\t" \
   "buffer_store_dwordx4 v[144:147], v21, %[rs], s50 offen sc1\n\t" "v_mfma_f32_16x16x32_bf16 v[56:59], a[104:107], v[84:87], v[56:59]\n\t" \
   "v_mul_f32 v49, v49, v10\n\t" "v_mul_f32 v50, v50, v10\n\t" "v_mfma_f32_16x16x32_bf16 v[60:63], a[108:111], v[84:87], v[60:63]\n\t" \
   "v_cvt_pk_bf16_f32 v123, v123, v122\n\t" "s_waitcnt lgkmcnt(4)\n\t" "v_mfma_f32_16x16x32_bf16 v[56:59], a[112:115], v[88:91], v[56:59]\n\t" \
   "buffer_load_dwordx4 v16, %[rs], s51 offen offset:0 sc1 lds\n\t" "v_mfma_f32_16x16x32_bf16 v[76:79], v[172:175], a[212:215], v[76:79]\n\t" \
   "v_mul_f32 v51, v51, v10\n\t" "v_and_b32 v12, 0xffff0000, v123\n\t" "v_mfma_f32_16x16x32_bf16 v[60:63], a[116:119], v[88:91], v[60:63]\n\t" \
   "v_mul_f32 v52, v52, v10\n\t" "v_lshlrev_b32 v123, 16, v123\n\t" "v_mfma_f32_16x16x32_bf16 v[56:59], a[120:123], v[92:95], v[56:59]\n\t" \
   "v_mul_f32 v53, v53, v10\n\t" "v_add_f32 v123, 0x3727c5ac, v123\n\t" "v_mfma_f32_16x16x32_bf16 v[72:75], v[160:163], a[216:219], v[72:75]\n\t" \
   "v_mul_f32 v54, v54, v10\n\t" "v_cvt_pk_bf16_f32 v123, v123, v123\n\t" "v_mfma_f32_16x16x32_bf16 v[60:63], a[124:127], v[92:95], v[60:63]\n\t" \
   "v_mul_f32 v55, v55, v10\n\t" "s_waitcnt lgkmcnt(2)\n\t" "v_mfma_f32_16x16x32_bf16 v[56:59], a[128:131], v[96:99], v[56:59]\n\t" \
   "v_lshlrev_b32 v123, 16, v123\n\t" "v_cvt_pk_bf16_f32 v136, v48, v49\n\t" "v_mfma_f32_16x16x32_bf16 v[76:79], v[176:179], a[216:219], v[76:79]\n\t" \
   "v_sqrt_f32 v123, v123\n\t" "v_mfma_f32_16x16x32_bf16 v[60:63], a[132:135], v[96:99], v[60:63]\n\t" "v_cvt_pk_bf16_f32 v137, v50, v51\n\t" \
   "s_nop 0\n\t" "v_mfma_f32_16x16x32_bf16 v[56:59], a[136:139], v[100:103], v[56:59]\n\t" "v_cvt_pk_bf16_f32 v138, v52, v53\n\t" \
   "v_cvt_pk_bf16_f32 v123, v123, v123\n\t" "v_mfma_f32_16x16x32_bf16 v[72:75], v[164:167], a[220:223], v[72:75]\n\t" \
   "buffer_load_dwordx4 v16, %[rs], s51 offen offset:1024 sc1 lds\n\t" "s_add_u32 m0, s53, %[ld1]\n\t" "s_nop 0\n\t" \
   "v_mfma_f32_16x16x32_bf16 v[60:63], a[140:143], v[100:103], v[60:63]\n\t" "v_cvt_pk_bf16_f32 v139, v54, v55\n\t" "s_waitcnt lgkmcnt(0)\n\t" \
   "v_mfma_f32_16x16x32_bf16 v[56:59], a[144:147], v[104:107], v[56:59]\n\t" "v_lshlrev_b32 v123, 16, v123\n\t" \
   "v_mfma_f32_16x16x32_bf16 v[76:79], v[180:183], a[220:223], v[76:79]\n\t" "ds_write_b128 v14, v[136:139] offset:0\n\t" \
   "v_mfma_f32_16x16x32_bf16 v[60:63], a[148:151], v[104:107], v[60:63]\n\t" "v_rcp_f32 v132, v123\n\t" \
   "v_mfma_f32_16x16x32_bf16 v[56:59], a[152:155], v[108:111], v[56:59]\n\t" "buffer_load_dwordx4 v17, %[rs], s52 offen sc1 lds\n\t" \
   "v_mfma_f32_16x16x32_bf16 v[60:63], a[156:159], v[108:111], v[60:63]\n\t" "v_cvt_pk_bf16_f32 v148, v72, v73\n\t" \
   "v_cvt_pk_bf16_f32 v150, v74, v75\n\t" "v_mfma_f32_16x16x32_bf16 v[64:67], v[152:155], a[224:227], 0\n\t" "v_cvt_pk_bf16_f32 v149, v76, v77\n\t" \
   "v_cvt_pk_bf16_f32 v151, v78, v79\n\t" "v_mfma_f32_16x16x32_bf16 v[68:71], v[168:171], a[224:227], 0\n\t" "s_add_u32 s50, s49, 98304\n\t" \
   "v_mfma_f32_16x16x32_bf16 v[64:67], v[156:159], a[228:231], v[64:67]\n\t" "v_fmac_f32 v56, v32, v12\n\t" "v_fmac_f32 v57, v33, v12\n\t" \
   "v_mfma_f32_16x16x32_bf16 v[68:71], v[172:175], a[228:231], v[68:71]\n\t" "v_fmac_f32 v58, v34, v12\n\t" "v_fmac_f32 v59, v35, v12\n\t" \
   "v_mfma_f32_16x16x32_bf16 v[64:67], v[160:163], a[232:235], v[64:67]\n\t" "v_fmac_f32 v60, v36, v12\n\t" "v_fmac_f32 v61, v37, v12\n\t" \
   "v_mfma_f32_16x16x32_bf16 v[68:71], v[176:179], a[232:235], v[68:71]\n\t" "v_fmac_f32 v62, v38, v12\n\t" "v_fmac_f32 v63, v39, v12\n\t" \
   "buffer_store_dwordx4 v[148:151], v21, %[rs], s50 offen sc1\n\t" "v_mfma_f32_16x16x32_bf16 v[64:67], v[164:167], a[236:239], v[64:67]\n\t" \
   "v_mul_f32 v56, v56, v132\n\t" "v_mul_f32 v57, v57, v132\n\t" "v_mfma_f32_16x16x32_bf16 v[68:71], v[180:183], a[236:239], v[68:71]\n\t" \
   "v_mul_f32 v58, v58, v132\n\t" "v_mul_f32 v59, v59, v132\n\t" "v_mfma_f32_16x16x32_bf16 v[72:75], v[152:155], a[240:243], 0\n\t" \
   "v_mul_f32 v60, v60, v132\n\t" "v_mul_f32 v61, v61, v132\n\t" "v_mfma_f32_16x16x32_bf16 v[76:79], v[168:171], a[240:243], 0\n\t" \
   "v_mul_f32 v62, v62, v132\n\t" "v_mul_f32 v63, v63, v132\n\t" "v_mfma_f32_16x16x32_bf16 v[72:75], v[156:159], a[244:247], v[72:75]\n\t" \
   "v_cvt_pk_bf16_f32 v144, v64, v65\n\t" "v_cvt_pk_bf16_f32 v146, v66, v67\n\t" \
   "v_mfma_f32_16x16x32_bf16 v[76:79], v[172:175], a[244:247], v[76:79]\n\t" "v_cvt_pk_bf16_f32 v145, v68, v69\n\t" \
   "v_cvt_pk_bf16_f32 v140, v56, v57\n\t" "v_mfma_f32_16x16x32_bf16 v[72:75], v[160:163], a[248:251], v[72:75]\n\t" \
   "v_cvt_pk_bf16_f32 v147, v70, v71\n\t" "v_cvt_pk_bf16_f32 v141, v58, v59\n\t" \
   "v_mfma_f32_16x16x32_bf16 v[76:79], v[176:179], a[248:251], v[76:79]\n\t" "s_add_u32 s50, s49, 131072\n\t" "v_cvt_pk_bf16_f32 v142, v60, v61\n\t" \
   "v_mfma_f32_16x16x32_bf16 v[72:75], v[164:167], a[252:255], v[72:75]\n\t" "v_cvt_pk_bf16_f32 v143, v62, v63\n\t" \
   "v_mfma_f32_16x16x32_bf16 v[76:79], v[180:183], a[252:255], v[76:79]\n\t" "buffer_store_dwordx4 v[144:147], v21, %[rs], s50 offen sc1\n\t" \
   "v_mfma_f32_16x16x32_bf16 v[64:67], v[152:155], v[192:195], 0\n\t" "ds_write_b128 v14, v[140:143] offset:4096\n\t" \
   "v_mfma_f32_16x16x32_bf16 v[68:71], v[168:171], v[192:195], 0\n\t" "v_mfma_f32_16x16x32_bf16 v[64:67], v[156:159], v[196:199], v[64:67]\n\t" \
   "v_cvt_pk_bf16_f32 v148, v72, v73\n\t" "v_cvt_pk_bf16_f32 v150, v74, v75\n\t" \
   "v_mfma_f32_16x16x32_bf16 v[68:71], v[172:175], v[196:199], v[68:71]\n\t" "v_cvt_pk_bf16_f32 v149, v76, v77\n\t" \
   "v_cvt_pk_bf16_f32 v151, v78, v79\n\t" "s_waitcnt vmcnt(16) lgkmcnt(0)\n\t" "s_barrier\n\t" \
   "v_mfma_f32_16x16x32_bf16 v[64:67], v[160:163], v[200:203], v[64:67]\n\t" "s_add_u32 s45, s40, 3\n\t" "s_min_u32 s45, s45, 63\n\t" \
   "v_mfma_f32_16x16x32_bf16 v[68:71], v[176:179], v[200:203], v[68:71]\n\t" "s_add_u32 s50, s49, 163840\n\t" "s_mov_b32 s46, s42\n\t" \
   "v_mfma_f32_16x16x32_bf16 v[64:67], v[164:167], v[204:207], v[64:67]\n\t" "s_lshr_b32 s48, s45, 1\n\t" "s_bitcmp1_b32 s44, s48\n\t" \
   "s_cbranch_scc0 174f\n\t" "74:\n\t" "v_mfma_f32_16x16x32_bf16 v[68:71], v[180:183], v[204:207], v[68:71]\n\t" \
   "buffer_store_dwordx4 v[148:151], v21, %[rs], s50 offen sc1\n\t" "v_mfma_f32_16x16x32_bf16 v[72:75], v[152:155], v[208:211], 0\n\t" \
   "s_mul_i32 s52, s45, 1536\n\t" "s_mul_i32 s51, s45, 40960\n\t" "s_add_u32 m0, s46, %[ldx]\n\t" "s_mov_b32 s53, s46\n\t" \
   "v_mfma_f32_16x16x32_bf16 v[76:79], v[168:171], v[208:211], 0\n\t" "buffer_load_dwordx4 v16, %[rs], s51 offen offset:0 sc1 lds\n\t" \
   "v_mfma_f32_16x16x32_bf16 v[72:75], v[156:159], v[212:215], v[72:75]\n\t" "v_cvt_pk_bf16_f32 v144, v64, v65\n\t" \
   "v_cvt_pk_bf16_f32 v146, v66, v67\n\t" "v_mfma_f32_16x16x32_bf16 v[76:79], v[172:175], v[212:215], v[76:79]\n\t" \
   "v_cvt_pk_bf16_f32 v145, v68, v69\n\t" "v_cvt_pk_bf16_f32 v147, v70, v71\n\t" \
   "v_mfma_f32_16x16x32_bf16 v[72:75], v[160:163], v[216:219], v[72:75]\n\t" "ds_read_b128 v[80:83], v28 offset:0\n\t" \
   "v_mfma_f32_16x16x32_bf16 v[76:79], v[176:179], v[216:219], v[76:79]\n\t" "s_add_u32 s50, s49, 196608\n\t" "s_mov_b32 s42, s43\n\t" \
   "v_mfma_f32_16x16x32_bf16 v[72:75], v[164:167], v[220:223], v[72:75]\n\t" "ds_read_b128 v[84:87], v28 offset:1024\n\t" \
   "v_mfma_f32_16x16x32_bf16 v[76:79], v[180:183], v[220:223], v[76:79]\n\t" "buffer_store_dwordx4 v[144:147], v21, %[rs], s50 offen sc1\n\t" \
   "v_mfma_f32_16x16x32_bf16 v[64:67], v[152:155], v[224:227], 0\n\t" "ds_read_b128 v[88:91], v28 offset:2048\n\t" \
   "v_mfma_f32_16x16x32_bf16 v[68:71], v[168:171], v[224:227], 0\n\t" "s_add_u32 s43, s43, 44032\n\t" \
   "v_mfma_f32_16x16x32_bf16 v[64:67], v[156:159], v[228:231], v[64:67]\n\t" "v_cvt_pk_bf16_f32 v148, v72, v73\n\t" \
   "v_cvt_pk_bf16_f32 v150, v74, v75\n\t" "v_mfma_f32_16x16x32_bf16 v[68:71], v[172:175], v[228:231], v[68:71]\n\t" \
   "v_cvt_pk_bf16_f32 v149, v76, v77\n\t" "v_cvt_pk_bf16_f32 v151, v78, v79\n\t" \
   "v_mfma_f32_16x16x32_bf16 v[64:67], v[160:163], v[232:235], v[64:67]\n\t" "s_cmp_eq_u32 s43, 132096\n\t" "s_cselect_b32 s43, 0, s43\n\t" \
   "v_mfma_f32_16x16x32_bf16 v[68:71], v[176:179], v[232:235], v[68:71]\n\t" "ds_read_b128 v[92:95], v28 offset:3072\n\t" \
   "v_mfma_f32_16x16x32_bf16 v[64:67], v[164:167], v[236:239], v[64:67]\n\t" "s_add_u32 s50, s49, 229376\n\t" "s_add_u32 s41, s41, 1\n\t" \
   "v_mfma_f32_16x16x32_bf16 v[68:71], v[180:183], v[236:239], v[68:71]\n\t" "buffer_store_dwordx4 v[148:151], v21, %[rs], s50 offen sc1\n\t" \
   "v_mfma_f32_16x16x32_bf16 v[72:75], v[152:155], v[240:243], 0\n\t" "ds_read_b128 v[96:99], v28 offset:4096\n\t" \
   "v_mfma_f32_16x16x32_bf16 v[76:79], v[168:171], v[240:243], 0\n\t" "s_add_u32 s40, s40, 1\n\t" "v_xor_b32 v14, 0x2000, v14\n\t" \
   "v_mfma_f32_16x16x32_bf16 v[72:75], v[156:159], v[244:247], v[72:75]\n\t" "v_cvt_pk_bf16_f32 v144, v64, v65\n\t" \
   "v_cvt_pk_bf16_f32 v146, v66, v67\n\t" "v_mfma_f32_16x16x32_bf16 v[76:79], v[172:175], v[244:247], v[76:79]\n\t" \
   "v_cvt_pk_bf16_f32 v145, v68, v69\n\t" "v_cvt_pk_bf16_f32 v147, v70, v71\n\t" \
   "v_mfma_f32_16x16x32_bf16 v[72:75], v[160:163], v[248:251], v[72:75]\n\t" "ds_read_b128 v[100:103], v28 offset:5120\n\t" \
   "v_mfma_f32_16x16x32_bf16 v[76:79], v[176:179], v[248:251], v[76:79]\n\t" "s_add_u32 s50, s49, 262144\n\t" "v_xor_b32 v15, 0x2000, v15\n\t" \
   "v_mfma_f32_16x16x32_bf16 v[72:75], v[164:167], v[252:255], v[72:75]\n\t" "ds_read_b128 v[104:107], v28 offset:6144\n\t" \
   "buffer_store_dwordx4 v[144:147], v21, %[rs], s50 offen sc1\n\t" "v_mfma_f32_16x16x32_bf16 v[76:79], v[180:183], v[252:255], v[76:79]\n\t" \
   "v_mov_b32 v18, v21\n\t" "s_cmp_lt_u32 s40, 64\n\t" "s_cbranch_scc1 1b\n\t" "s_nop 7\n\t" "s_waitcnt lgkmcnt(0)\n\t" \
   "v_cvt_pk_bf16_f32 v148, v72, v73\n\t" "v_cvt_pk_bf16_f32 v150, v74, v75\n\t" "v_cvt_pk_bf16_f32 v149, v76, v77\n\t" \
   "v_cvt_pk_bf16_f32 v151, v78, v79\n\t" "s_add_u32 s50, s49, 294912\n\t" "buffer_store_dwordx4 v[148:151], v21, %[rs], s50 offen sc1\n\t" \
   "s_sub_i32 s49, s41, 1\n\t" "s_mul_i32 s49, s49, 1310720\n\t" "ds_read_b128 v[152:155], v15 offset:0\n\t" \
   "ds_read_b128 v[168:171], v15 offset:4096\n\t" "ds_read_b128 v[156:159], v15 offset:1024\n\t" "ds_read_b128 v[172:175], v15 offset:5120\n\t" \
   "ds_read_b128 v[160:163], v15 offset:2048\n\t" "ds_read_b128 v[176:179], v15 offset:6144\n\t" "ds_read_b128 v[164:167], v15 offset:3072\n\t" \
   "ds_read_b128 v[180:183], v15 offset:7168\n\t" "s_waitcnt lgkmcnt(4)\n\t" "v_mfma_f32_16x16x32_bf16 v[64:67], v[152:155], a[160:163], 0\n\t" \
   "v_mfma_f32_16x16x32_bf16 v[68:71], v[168:171], a[160:163], 0\n\t" "v_mfma_f32_16x16x32_bf16 v[64:67], v[156:159], a[164:167], v[64:67]\n\t" \
   "v_mfma_f32_16x16x32_bf16 v[68:71], v[172:175], a[164:167], v[68:71]\n\t" "s_waitcnt lgkmcnt(0)\n\t" \
   "v_mfma_f32_16x16x32_bf16 v[64:67], v[160:163], a[168:171], v[64:67]\n\t" "v_mfma_f32_16x16x32_bf16 v[68:71], v[176:179], a[168:171], v[68:71]\n\t" \
   "v_mfma_f32_16x16x32_bf16 v[64:67], v[164:167], a[172:175], v[64:67]\n\t" "v_mfma_f32_16x16x32_bf16 v[68:71], v[180:183], a[172:175], v[68:71]\n\t" \
   "v_mfma_f32_16x16x32_bf16 v[72:75], v[152:155], a[176:179], 0\n\t" "v_mfma_f32_16x16x32_bf16 v[76:79], v[168:171], a[176:179], 0\n\t" \
   "v_mfma_f32_16x16x32_bf16 v[72:75], v[156:159], a[180:183], v[72:75]\n\t" "v_cvt_pk_bf16_f32 v144, v64, v65\n\t" \
   "v_cvt_pk_bf16_f32 v146, v66, v67\n\t" "v_mfma_f32_16x16x32_bf16 v[76:79], v[172:175], a[180:183], v[76:79]\n\t" \
   "v_cvt_pk_bf16_f32 v145, v68, v69\n\t" "v_cvt_pk_bf16_f32 v147, v70, v71\n\t" \
   "v_mfma_f32_16x16x32_bf16 v[72:75], v[160:163], a[184:187], v[72:75]\n\t" "s_add_u32 s50, s49, 0\n\t" \
   "v_mfma_f32_16x16x32_bf16 v[76:79], v[176:179], a[184:187], v[76:79]\n\t" "buffer_store_dwordx4 v[144:147], v21, %[rs], s50 offen sc1\n\t" \
   "v_mfma_f32_16x16x32_bf16 v[72:75], v[164:167], a[188:191], v[72:75]\n\t" "v_mfma_f32_16x16x32_bf16 v[76:79], v[180:183], a[188:191], v[76:79]\n\t" \
   "v_mfma_f32_16x16x32_bf16 v[64:67], v[152:155], a[192:195], 0\n\t" "v_mfma_f32_16x16x32_bf16 v[68:71], v[168:171], a[192:195], 0\n\t" \
   "v_mfma_f32_16x16x32_bf16 v[64:67], v[156:159], a[196:199], v[64:67]\n\t" "v_cvt_pk_bf16_f32 v148, v72, v73\n\t" \
   "v_cvt_pk_bf16_f32 v150, v74, v75\n\t" "v_mfma_f32_16x16x32_bf16 v[68:71], v[172:175], a[196:199], v[68:71]\n\t" \
   "v_cvt_pk_bf16_f32 v149, v76, v77\n\t" "v_cvt_pk_bf16_f32 v151, v78, v79\n\t" \
   "v_mfma_f32_16x16x32_bf16 v[64:67], v[160:163], a[200:203], v[64:67]\n\t" "s_add_u32 s50, s49, 32768\n\t" \
   "v_mfma_f32_16x16x32_bf16 v[68:71], v[176:179], a[200:203], v[68:71]\n\t" "buffer_store_dwordx4 v[148:151], v21, %[rs], s50 offen sc1\n\t" \
   "v_mfma_f32_16x16x32_bf16 v[64:67], v[164:167], a[204:207], v[64:67]\n\t" "v_mfma_f32_16x16x32_bf16 v[68:71], v[180:183], a[204:207], v[68:71]\n\t" \
   "v_mfma_f32_16x16x32_bf16 v[72:75], v[152:155], a[208:211], 0\n\t" "v_mfma_f32_16x16x32_bf16 v[76:79], v[168:171], a[208:211], 0\n\t" \
   "v_mfma_f32_16x16x32_bf16 v[72:75], v[156:159], a[212:215], v[72:75]\n\t" "v_cvt_pk_bf16_f32 v144, v64, v65\n\t" \
   "v_cvt_pk_bf16_f32 v146, v66, v67\n\t" "v_mfma_f32_16x16x32_bf16 v[76:79], v[172:175], a[212:215], v[76:79]\n\t" \
   "v_cvt_pk_bf16_f32 v145, v68, v69\n\t" "v_cvt_pk_bf16_f32 v147, v70, v71\n\t" \
   "v_mfma_f32_16x16x32_bf16 v[72:75], v[160:163], a[216:219], v[72:75]\n\t" "s_add_u32 s50, s49, 65536\n\t" \
   "v_mfma_f32_16x16x32_bf16 v[76:79], v[176:179], a[216:219], v[76:79]\n\t" "buffer_store_dwordx4 v[144:147], v21, %[rs], s50 offen sc1\n\t" \
   "v_mfma_f32_16x16x32_bf16 v[72:75], v[164:167], a[220:223], v[72:75]\n\t" "v_mfma_f32_16x16x32_bf16 v[76:79], v[180:183], a[220:223], v[76:79]\n\t" \
   "v_mfma_f32_16x16x32_bf16 v[64:67], v[152:155], a[224:227], 0\n\t" "v_mfma_f32_16x16x32_bf16 v[68:71], v[168:171], a[224:227], 0\n\t" \
   "v_mfma_f32_16x16x32_bf16 v[64:67], v[156:159], a[228:231], v[64:67]\n\t" "v_cvt_pk_bf16_f32 v148, v72, v73\n\t" \
   "v_cvt_pk_bf16_f32 v150, v74, v75\n\t" "v_mfma_f32_16x16x32_bf16 v[68:71], v[172:175], a[228:231], v[68:71]\n\t" \
   "v_cvt_pk_bf16_f32 v149, v76, v77\n\t" "v_cvt_pk_bf16_f32 v151, v78, v79\n\t" \
   "v_mfma_f32_16x16x32_bf16 v[64:67], v[160:163], a[232:235], v[64:67]\n\t" "s_add_u32 s50, s49, 98304\n\t" \
   "v_mfma_f32_16x16x32_bf16 v[68:71], v[176:179], a[232:235], v[68:71]\n\t" "buffer_store_dwordx4 v[148:151], v21, %[rs], s50 offen sc1\n\t" \
   "v_mfma_f32_16x16x32_bf16 v[64:67], v[164:167], a[236:239], v[64:67]\n\t" "v_mfma_f32_16x16x32_bf16 v[68:71], v[180:183], a[236:239], v[68:71]\n\t" \
   "v_mfma_f32_16x16x32_bf16 v[72:75], v[152:155], a[240:243], 0\n\t" "v_mfma_f32_16x16x32_bf16 v[76:79], v[168:171], a[240:243], 0\n\t" \
   "v_mfma_f32_16x16x32_bf16 v[72:75], v[156:159], a[244:247], v[72:75]\n\t" "v_cvt_pk_bf16_f32 v144, v64, v65\n\t" \
   "v_cvt_pk_bf16_f32 v146, v66, v67\n\t" "v_mfma_f32_16x16x32_bf16 v[76:79], v[172:175], a[244:247], v[76:79]\n\t" \
   "v_cvt_pk_bf16_f32 v145, v68, v69\n\t" "v_cvt_pk_bf16_f32 v147, v70, v71\n\t" \
   "v_mfma_f32_16x16x32_bf16 v[72:75], v[160:163], a[248:251], v[72:75]\n\t" "s_add_u32 s50, s49, 131072\n\t" \
   "v_mfma_f32_16x16x32_bf16 v[76:79], v[176:179], a[248:251], v[76:79]\n\t" "buffer_store_dwordx4 v[144:147], v21, %[rs], s50 offen sc1\n\t" \
   "v_mfma_f32_16x16x32_bf16 v[72:75], v[164:167], a[252:255], v[72:75]\n\t" "v_mfma_f32_16x16x32_bf16 v[76:79], v[180:183], a[252:255], v[76:79]\n\t" \
   "v_mfma_f32_16x16x32_bf16 v[64:67], v[152:155], v[192:195], 0\n\t" "v_mfma_f32_16x16x32_bf16 v[68:71], v[168:171], v[192:195], 0\n\t" \
   "v_mfma_f32_16x16x32_bf16 v[64:67], v[156:159], v[196:199], v[64:67]\n\t" "v_cvt_pk_bf16_f32 v148, v72, v73\n\t" \
   "v_cvt_pk_bf16_f32 v150, v74, v75\n\t" "v_mfma_f32_16x16x32_bf16 v[68:71], v[172:175], v[196:199], v[68:71]\n\t" \
   "v_cvt_pk_bf16_f32 v149, v76, v77\n\t" "v_cvt_pk_bf16_f32 v151, v78, v79\n\t" \
   "v_mfma_f32_16x16x32_bf16 v[64:67], v[160:163], v[200:203], v[64:67]\n\t" "s_add_u32 s50, s49, 163840\n\t" \
   "v_mfma_f32_16x16x32_bf16 v[68:71], v[176:179], v[200:203], v[68:71]\n\t" "buffer_store_dwordx4 v[148:151], v21, %[rs], s50 offen sc1\n\t" \
   "v_mfma_f32_16x16x32_bf16 v[64:67], v[164:167], v[204:207], v[64:67]\n\t" "v_mfma_f32_16x16x32_bf16 v[68:71], v[180:183], v[204:207], v[68:71]\n\t" \
   "v_mfma_f32_16x16x32_bf16 v[72:75], v[152:155], v[208:211], 0\n\t" "v_mfma_f32_16x16x32_bf16 v[76:79], v[168:171], v[208:211], 0\n\t" \
   "v_mfma_f32_16x16x32_bf16 v[72:75], v[156:159], v[212:215], v[72:75]\n\t" "v_cvt_pk_bf16_f32 v144, v64, v65\n\t" \
   "v_cvt_pk_bf16_f32 v146, v66, v67\n\t" "v_mfma_f32_16x16x32_bf16 v[76:79], v[172:175], v[212:215], v[76:79]\n\t" \
   "v_cvt_pk_bf16_f32 v145, v68, v69\n\t" "v_cvt_pk_bf16_f32 v147, v70, v71\n\t" \
   "v_mfma_f32_16x16x32_bf16 v[72:75], v[160:163], v[216:219], v[72:75]\n\t" "s_add_u32 s50, s49, 196608\n\t" \
   "v_mfma_f32_16x16x32_bf16 v[76:79], v[176:179], v[216:219], v[76:79]\n\t" "buffer_store_dwordx4 v[144:147], v21, %[rs], s50 offen sc1\n\t" \
   "v_mfma_f32_16x16x32_bf16 v[72:75], v[164:167], v[220:223], v[72:75]\n\t" "v_mfma_f32_16x16x32_bf16 v[76:79], v[180:183], v[220:223], v[76:79]\n\t" \
   "v_mfma_f32_16x16x32_bf16 v[64:67], v[152:155], v[224:227], 0\n\t" "v_mfma_f32_16x16x32_bf16 v[68:71], v[168:171], v[224:227], 0\n\t" \
   "v_mfma_f32_16x16x32_bf16 v[64:67], v[156:159], v[228:231], v[64:67]\n\t" "v_cvt_pk_bf16_f32 v148, v72, v73\n\t" \
   "v_cvt_pk_bf16_f32 v150, v74, v75\n\t" "v_mfma_f32_16x16x32_bf16 v[68:71], v[172:175], v[228:231], v[68:71]\n\t" \
   "v_cvt_pk_bf16_f32 v149, v76, v77\n\t" "v_cvt_pk_bf16_f32 v151, v78, v79\n\t" \
   "v_mfma_f32_16x16x32_bf16 v[64:67], v[160:163], v[232:235], v[64:67]\n\t" "s_add_u32 s50, s49, 229376\n\t" \
   "v_mfma_f32_16x16x32_bf16 v[68:71], v[176:179], v[232:235], v[68:71]\n\t" "buffer_store_dwordx4 v[148:151], v21, %[rs], s50 offen sc1\n\t" \
   "v_mfma_f32_16x16x32_bf16 v[64:67], v[164:167], v[236:239], v[64:67]\n\t" "v_mfma_f32_16x16x32_bf16 v[68:71], v[180:183], v[236:239], v[68:71]\n\t" \
   "v_mfma_f32_16x16x32_bf16 v[72:75], v[152:155], v[240:243], 0\n\t" "v_mfma_f32_16x16x32_bf16 v[76:79], v[168:171], v[240:243], 0\n\t" \
   "v_mfma_f32_16x16x32_bf16 v[72:75], v[156:159], v[244:247], v[72:75]\n\t" "v_cvt_pk_bf16_f32 v144, v64, v65\n\t" \
   "v_cvt_pk_bf16_f32 v146, v66, v67\n\t" "v_mfma_f32_16x16x32_bf16 v[76:79], v[172:175], v[244:247], v[76:79]\n\t" \
   "v_cvt_pk_bf16_f32 v145, v68, v69\n\t" "v_cvt_pk_bf16_f32 v147, v70, v71\n\t" \
   "v_mfma_f32_16x16x32_bf16 v[72:75], v[160:163], v[248:251], v[72:75]\n\t" "s_add_u32 s50, s49, 262144\n\t" \
   "v_mfma_f32_16x16x32_bf16 v[76:79], v[176:179], v[248:251], v[76:79]\n\t" "buffer_store_dwordx4 v[144:147], v21, %[rs], s50 offen sc1\n\t" \
   "v_mfma_f32_16x16x32_bf16 v[72:75], v[164:167], v[252:255], v[72:75]\n\t" "v_mfma_f32_16x16x32_bf16 v[76:79], v[180:183], v[252:255], v[76:79]\n\t" \
   "s_nop 7\n\t" "v_cvt_pk_bf16_f32 v148, v72, v73\n\t" "v_cvt_pk_bf16_f32 v150, v74, v75\n\t" "v_cvt_pk_bf16_f32 v149, v76, v77\n\t" \
   "v_cvt_pk_bf16_f32 v151, v78, v79\n\t" "s_add_u32 s50, s49, 294912\n\t" "buffer_store_dwordx4 v[148:151], v21, %[rs], s50 offen sc1\n\t" \
   "s_waitcnt vmcnt(0)\n\t" "s_cmp_lt_i32 s40, 2\n\t" "s_cselect_b32 s47, 0x7ffffff0, 0\n\t" "v_or_b32 v30, s47, v20\n\t" "s_sub_i32 s47, s41, 2\n\t" \
   "s_max_i32 s47, s47, 0\n\t" "s_lshl_b32 s47, s47, 12\n\t" "buffer_store_dword v31, v30, %[rs], s47 offen sc1\n\t" "s_cmp_lt_i32 s40, 1\n\t" \
   "s_cselect_b32 s47, 0x7ffffff0, 0\n\t" "v_or_b32 v30, s47, v20\n\t" "s_sub_i32 s47, s41, 1\n\t" "s_max_i32 s47, s47, 0\n\t" \
   "s_lshl_b32 s47, s47, 12\n\t" "buffer_store_dword v31, v30, %[rs], s47 offen sc1\n\t" "s_branch 9f\n\t" "172:\n\t" "s_mov_b32 s47, 0\n\t" \
   "buffer_load_dword v30, v19, %[rs], 0 offen sc1\n\t" "s_waitcnt vmcnt(0)\n\t" "v_add_u32 v30, 1, v30\n\t" "v_cmp_lt_u32 vcc, 9, v30\n\t" \
   "s_mov_b32 s44, vcc_lo\n\t" "s_bitcmp1_b32 s44, s48\n\t" "s_cbranch_scc1 72b\n\t" "s_bitset1_b32 s44, s48\n\t" "s_lshl_b32 s48, s48, 7\n\t" \
   "s_add_u32 s48, s48, %[cnt]\n\t" "73:\n\t" "s_sleep 10\n\t" "buffer_load_dword v30, off, %[rs], s48 sc1\n\t" "s_add_u32 s47, s47, 1\n\t" \
   "s_waitcnt vmcnt(0)\n\t" "v_add_u32 v30, 1, v30\n\t" "v_cmp_lt_u32 vcc, 9, v30\n\t" "s_cmp_gt_u32 s47, 0x20000\n\t" "s_cbranch_scc1 72b\n\t" \
   "s_cbranch_vccz 73b\n\t" "s_branch 72b\n\t" "176:\n\t" "s_mov_b32 s47, 0\n\t" "buffer_load_dword v30, v19, %[rs], 0 offen sc1\n\t" \
   "s_waitcnt vmcnt(0)\n\t" "v_add_u32 v30, 1, v30\n\t" "v_cmp_lt_u32 vcc, 9, v30\n\t" "s_mov_b32 s44, vcc_lo\n\t" "s_bitcmp1_b32 s44, s48\n\t" \
   "s_cbranch_scc1 76b\n\t" "s_bitset1_b32 s44, s48\n\t" "s_lshl_b32 s48, s48, 7\n\t" "s_add_u32 s48, s48, %[cnt]\n\t" "77:\n\t" "s_sleep 10\n\t" \
   "buffer_load_dword v30, off, %[rs], s48 sc1\n\t" "s_add_u32 s47, s47, 1\n\t" "s_waitcnt vmcnt(0)\n\t" "v_add_u32 v30, 1, v30\n\t" \
   "v_cmp_lt_u32 vcc, 9, v30\n\t" "s_cmp_gt_u32 s47, 0x20000\n\t" "s_cbranch_scc1 76b\n\t" "s_cbranch_vccz 77b\n\t" "s_branch 76b\n\t" "174:\n\t" \
   "s_mov_b32 s47, 0\n\t" "buffer_load_dword v30, v19, %[rs], 0 offen sc1\n\t" "s_waitcnt vmcnt(0)\n\t" "v_add_u32 v30, 1, v30\n\t" \
   "v_cmp_lt_u32 vcc, 9, v30\n\t" "s_mov_b32 s44, vcc_lo\n\t" "s_bitcmp1_b32 s44, s48\n\t" "s_cbranch_scc1 74b\n\t" "s_bitset1_b32 s44, s48\n\t" \
   "s_lshl_b32 s48, s48, 7\n\t" "s_add_u32 s48, s48, %[cnt]\n\t" "75:\n\t" "s_sleep 10\n\t" "buffer_load_dword v30, off, %[rs], s48 sc1\n\t" \
   "s_add_u32 s47, s47, 1\n\t" "s_waitcnt vmcnt(0)\n\t" "v_add_u32 v30, 1, v30\n\t" "v_cmp_lt_u32 vcc, 9, v30\n\t" "s_cmp_gt_u32 s47, 0x20000\n\t" \
   "s_cbranch_scc1 74b\n\t" "s_cbranch_vccz 75b\n\t" "s_branch 74b\n\t" "9:\n\t" "s_waitcnt vmcnt(0)"
#define VU_CLOB "v8", "v9", "v10", "v11", "v12", "v13", "v14", "v15", "v32", "v33", "v34", "v35", "v36", "v37", "v38", "v39", "v40", "v41", "v42", "v43", "v44", "v45", "v46", "v47", "v48", "v49", "v50", "v51", "v52", "v53", "v54", "v55", "v56", "v57", "v58", "v59", "v60", "v61", "v62", "v63", "v64", "v65", "v66", "v67", "v68", "v69", "v70", "v71", "v72", "v73", "v74", "v75", "v76", "v77", "v78", "v79", "v80", "v81", "v82", "v83", "v84", "v85", "v86", "v87", "v88", "v89", "v90", "v91", "v92", "v93", "v94", "v95", "v96", "v97", "v98", "v99", "v100", "v101", "v102", "v103", "v104", "v105", "v106", "v107", "v108", "v109", "v110", "v111", "v112", "v113", "v114", "v115", "v116", "v117", "v118", "v119", "v120", "v121", "v122", "v123", "v124", "v125", "v126", "v127", "v128", "v129", "v130", "v131", "v132", "v133", "v134", "v135", "v136", "v137", "v138", "v139", "v140", "v141", "v142", "v143", "v144", "v145", "v146", "v147", "v148", "v149", "v150", "v151", "v152", "v153", "v154", "v155", "v156", "v157", "v158", "v159", "v160", "v161", "v162", "v163", "v164", "v165", "v166", "v167", "v168", "v169", "v170", "v171", "v172", "v173", "v174", "v175", "v176", "v177", "v178", "v179", "v180", "v181", "v182", "v183", "s40", "s41", "s42", "s43", "s44", "s45", "s46", "s47", "s48", "s49", "s50", "s51", "s52", "s53", "s54", "s55"
#define DF_ASM \
   "v_mov_b32 v80, 0x3f803f80\n\t" "v_mov_b32 v81, 0x3f803f80\n\t" "v_mov_b32 v82, 0x3f803f80\n\t" "v_mov_b32 v83, 0x3f803f80\n\t" \
   "s_mov_b32 s45, %[set]\n\t" "s_mov_b32 s46, 0\n\t" "v_mfma_f32_16x16x32_bf16 v[32:35], a[0:3], v[80:83], 0\n\t" \
   "v_mfma_f32_16x16x32_bf16 v[32:35], a[4:7], v[80:83], v[32:35]\n\t" "v_mfma_f32_16x16x32_bf16 v[32:35], a[8:11], v[80:83], v[32:35]\n\t" \
   "v_mfma_f32_16x16x32_bf16 v[32:35], a[12:15], v[80:83], v[32:35]\n\t" "v_mfma_f32_16x16x32_bf16 v[32:35], a[16:19], v[80:83], v[32:35]\n\t" \
   "v_mfma_f32_16x16x32_bf16 v[32:35], a[20:23], v[80:83], v[32:35]\n\t" "v_mfma_f32_16x16x32_bf16 v[32:35], a[24:27], v[80:83], v[32:35]\n\t" \
   "v_mfma_f32_16x16x32_bf16 v[32:35], a[28:31], v[80:83], v[32:35]\n\t" "v_mfma_f32_16x16x32_bf16 v[32:35], a[32:35], v[80:83], v[32:35]\n\t" \
   "v_mfma_f32_16x16x32_bf16 v[32:35], a[36:39], v[80:83], v[32:35]\n\t" "v_mfma_f32_16x16x32_bf16 v[32:35], a[40:43], v[80:83], v[32:35]\n\t" \
   "v_mfma_f32_16x16x32_bf16 v[32:35], a[44:47], v[80:83], v[32:35]\n\t" "v_mfma_f32_16x16x32_bf16 v[32:35], a[48:51], v[80:83], v[32:35]\n\t" \
   "v_mfma_f32_16x16x32_bf16 v[32:35], a[52:55], v[80:83], v[32:35]\n\t" "v_mfma_f32_16x16x32_bf16 v[32:35], a[56:59], v[80:83], v[32:35]\n\t" \
   "v_mfma_f32_16x16x32_bf16 v[32:35], a[60:63], v[80:83], v[32:35]\n\t" "v_mfma_f32_16x16x32_bf16 v[32:35], a[64:67], v[80:83], v[32:35]\n\t" \
   "v_mfma_f32_16x16x32_bf16 v[32:35], a[68:71], v[80:83], v[32:35]\n\t" "v_mfma_f32_16x16x32_bf16 v[32:35], a[72:75], v[80:83], v[32:35]\n\t" \
   "v_mfma_f32_16x16x32_bf16 v[32:35], a[76:79], v[80:83], v[32:35]\n\t" "v_mfma_f32_16x16x32_bf16 v[36:39], a[80:83], v[80:83], 0\n\t" \
   "v_mfma_f32_16x16x32_bf16 v[36:39], a[84:87], v[80:83], v[36:39]\n\t" "v_mfma_f32_16x16x32_bf16 v[36:39], a[88:91], v[80:83], v[36:39]\n\t" \
   "v_mfma_f32_16x16x32_bf16 v[36:39], a[92:95], v[80:83], v[36:39]\n\t" "v_mfma_f32_16x16x32_bf16 v[36:39], a[96:99], v[80:83], v[36:39]\n\t" \
   "v_mfma_f32_16x16x32_bf16 v[36:39], a[100:103], v[80:83], v[36:39]\n\t" "v_mfma_f32_16x16x32_bf16 v[36:39], a[104:107], v[80:83], v[36:39]\n\t" \
   "v_mfma_f32_16x16x32_bf16 v[36:39], a[108:111], v[80:83], v[36:39]\n\t" "v_mfma_f32_16x16x32_bf16 v[36:39], a[112:115], v[80:83], v[36:39]\n\t" \
   "v_mfma_f32_16x16x32_bf16 v[36:39], a[116:119], v[80:83], v[36:39]\n\t" "v_mfma_f32_16x16x32_bf16 v[36:39], a[120:123], v[80:83], v[36:39]\n\t" \
   "v_mfma_f32_16x16x32_bf16 v[36:39], a[124:127], v[80:83], v[36:39]\n\t" "v_mfma_f32_16x16x32_bf16 v[36:39], a[128:131], v[80:83], v[36:39]\n\t" \
   "v_mfma_f32_16x16x32_bf16 v[36:39], a[132:135], v[80:83], v[36:39]\n\t" "v_mfma_f32_16x16x32_bf16 v[36:39], a[136:139], v[80:83], v[36:39]\n\t" \
   "v_mfma_f32_16x16x32_bf16 v[36:39], a[140:143], v[80:83], v[36:39]\n\t" "v_mfma_f32_16x16x32_bf16 v[36:39], a[144:147], v[80:83], v[36:39]\n\t" \
   "v_mfma_f32_16x16x32_bf16 v[36:39], a[148:151], v[80:83], v[36:39]\n\t" "v_mfma_f32_16x16x32_bf16 v[36:39], a[152:155], v[80:83], v[36:39]\n\t" \
   "v_mfma_f32_16x16x32_bf16 v[36:39], a[156:159], v[80:83], v[36:39]\n\t" "1:\n\t" "s_lshl_b32 s48, s45, 7\n\t" "s_mov_b32 s47, 0\n\t" \
   "s_cmp_lg_u32 %[ldx], %[ld0]\n\t" "s_cbranch_scc1 3f\n\t" "2:\n\t" "buffer_load_dword v30, v19, %[rs], s48 offen sc1\n\t" \
   "s_add_u32 s47, s47, 1\n\t" "s_waitcnt vmcnt(0)\n\t" "v_readfirstlane_b32 s44, v30\n\t" "s_add_u32 s44, s44, 1\n\t" "s_cmp_ge_u32 s44, 5\n\t" \
   "s_cbranch_scc1 3f\n\t" "s_cmp_gt_u32 s47, 0x20000\n\t" "s_cbranch_scc1 3f\n\t" "s_sleep 3\n\t" "s_cmp_lg_u32 s44, 0\n\t" "s_cbranch_scc1 2b\n\t" \
   "s_sleep 8\n\t" "s_branch 2b\n\t" "3:\n\t" "s_barrier\n\t" "s_mul_i32 s52, s45, 1536\n\t" "s_mul_i32 s51, s45, 40960\n\t" \
   "s_add_u32 m0, s46, %[ldx]\n\t" "s_mov_b32 s53, s46\n\t" "buffer_load_dwordx4 v16, %[rs], s51 offen offset:0 sc1 lds\n\t" \
   "buffer_load_dwordx4 v16, %[rs], s51 offen offset:1024 sc1 lds\n\t" "buffer_load_dwordx4 v16, %[rs], s51 offen offset:2048 sc1 lds\n\t" \
   "buffer_load_dwordx4 v16, %[rs], s51 offen offset:3072 sc1 lds\n\t" "s_add_u32 m0, m0, 4096\n\t" "s_add_u32 s51, s51, 4096\n\t" \
   "buffer_load_dwordx4 v16, %[rs], s51 offen offset:0 sc1 lds\n\t" "buffer_load_dwordx4 v16, %[rs], s51 offen offset:1024 sc1 lds\n\t" \
   "buffer_load_dwordx4 v16, %[rs], s51 offen offset:2048 sc1 lds\n\t" "buffer_load_dwordx4 v16, %[rs], s51 offen offset:3072 sc1 lds\n\t" \
   "s_add_u32 m0, m0, 4096\n\t" "s_add_u32 s51, s51, 4096\n\t" "buffer_load_dwordx4 v16, %[rs], s51 offen offset:0 sc1 lds\n\t" \
   "buffer_load_dwordx4 v16, %[rs], s51 offen offset:1024 sc1 lds\n\t" "s_add_u32 m0, s53, %[ld1]\n\t" "s_nop 0\n\t" \
   "buffer_load_dwordx4 v17, %[rs], s52 offen sc1 lds\n\t" "v_add_u32 v26, s46, v22\n\t" "v_add_u32 v27, s46, v23\n\t" "s_waitcnt vmcnt(0)\n\t" \
   "s_barrier\n\t" "ds_read_b128 v[80:83], v26 offset:0\n\t" "ds_read_b128 v[84:87], v26 offset:1024\n\t" "ds_read_b128 v[88:91], v26 offset:2048\n\t" \
   "ds_read_b128 v[92:95], v26 offset:3072\n\t" "ds_read_b128 v[96:99], v26 offset:4096\n\t" "ds_read_b128 v[100:103], v26 offset:5120\n\t" \
   "ds_read_b128 v[104:107], v26 offset:6144\n\t" "s_waitcnt lgkmcnt(5)\n\t" "v_mfma_f32_16x16x32_bf16 v[52:55], a[80:83], v[80:83], 0\n\t" \
   "ds_read_b128 v[112:115], v27 offset:0\n\t" "v_mfma_f32_16x16x32_bf16 v[48:51], a[0:3], v[80:83], 0\n\t" \
   "ds_read_b128 v[108:111], v26 offset:7168\n\t" "ds_read_b128 v[116:119], v27 offset:16\n\t" \
   "v_mfma_f32_16x16x32_bf16 v[52:55], a[84:87], v[84:87], v[52:55]\n\t" "ds_read_b64 v[120:121], v27 offset:32\n\t" \
   "v_mfma_f32_16x16x32_bf16 v[48:51], a[4:7], v[84:87], v[48:51]\n\t" "ds_read_b128 v[80:83], v26 offset:8192\n\t" "s_waitcnt lgkmcnt(8)\n\t" \
   "v_mfma_f32_16x16x32_bf16 v[52:55], a[88:91], v[88:91], v[52:55]\n\t" "ds_read_b128 v[122:125], v27 offset:768\n\t" \
   "v_mfma_f32_16x16x32_bf16 v[48:51], a[8:11], v[88:91], v[48:51]\n\t" "ds_read_b128 v[84:87], v26 offset:9216\n\t" \
   "ds_read_b128 v[126:129], v27 offset:784\n\t" "v_mfma_f32_16x16x32_bf16 v[52:55], a[92:95], v[92:95], v[52:55]\n\t" \
   "v_mfma_f32_16x16x32_bf16 v[48:51], a[12:15], v[92:95], v[48:51]\n\t" "ds_read_b128 v[88:91], v26 offset:10240\n\t" \
   "ds_read_b64 v[130:131], v27 offset:800\n\t" "s_waitcnt lgkmcnt(11)\n\t" "v_mfma_f32_16x16x32_bf16 v[52:55], a[96:99], v[96:99], v[52:55]\n\t" \
   "s_lshl_b32 s48, s45, 8\n\t" "v_mfma_f32_16x16x32_bf16 v[48:51], a[16:19], v[96:99], v[48:51]\n\t" "ds_read_b128 v[92:95], v26 offset:11264\n\t" \
   "s_add_u32 s47, s48, 65536\n\t" "v_mfma_f32_16x16x32_bf16 v[52:55], a[100:103], v[100:103], v[52:55]\n\t" \
   "v_mfma_f32_16x16x32_bf16 v[48:51], a[20:23], v[100:103], v[48:51]\n\t" "ds_read_b128 v[96:99], v26 offset:12288\n\t" "s_waitcnt lgkmcnt(8)\n\t" \
   "v_add_f32 v112, v112, v114\n\t" "v_mfma_f32_16x16x32_bf16 v[52:55], a[104:107], v[104:107], v[52:55]\n\t" "v_add_f32 v113, v113, v115\n\t" \
   "v_mfma_f32_16x16x32_bf16 v[48:51], a[24:27], v[104:107], v[48:51]\n\t" "ds_read_b128 v[100:103], v26 offset:13312\n\t" \
   "v_mfma_f32_16x16x32_bf16 v[52:55], a[108:111], v[108:111], v[52:55]\n\t" "v_add_f32 v116, v116, v118\n\t" \
   "v_mfma_f32_16x16x32_bf16 v[48:51], a[28:31], v[108:111], v[48:51]\n\t" "ds_read_b128 v[104:107], v26 offset:14336\n\t" \
   "v_add_f32 v117, v117, v119\n\t" "s_waitcnt lgkmcnt(7)\n\t" "v_mfma_f32_16x16x32_bf16 v[52:55], a[112:115], v[80:83], v[52:55]\n\t" \
   "v_mfma_f32_16x16x32_bf16 v[48:51], a[32:35], v[80:83], v[48:51]\n\t" "ds_read_b128 v[108:111], v26 offset:15360\n\t" \
   "v_add_f32 v112, v112, v120\n\t" "v_mfma_f32_16x16x32_bf16 v[52:55], a[116:119], v[84:87], v[52:55]\n\t" "v_add_f32 v113, v113, v121\n\t" \
   "v_mfma_f32_16x16x32_bf16 v[48:51], a[36:39], v[84:87], v[48:51]\n\t" "ds_read_b128 v[80:83], v26 offset:16384\n\t" "v_add_f32 v112, v112, v116\n\t" \
   "s_waitcnt lgkmcnt(5)\n\t" "v_mfma_f32_16x16x32_bf16 v[52:55], a[120:123], v[88:91], v[52:55]\n\t" \
   "v_mfma_f32_16x16x32_bf16 v[48:51], a[40:43], v[88:91], v[48:51]\n\t" "ds_read_b128 v[84:87], v26 offset:17408\n\t" "v_add_f32 v113, v113, v117\n\t" \
   "v_mfma_f32_16x16x32_bf16 v[52:55], a[124:127], v[92:95], v[52:55]\n\t" "v_mul_f32 v112, 0xbacccccd, v112\n\t" \
   "v_mfma_f32_16x16x32_bf16 v[48:51], a[44:47], v[92:95], v[48:51]\n\t" "ds_read_b128 v[88:91], v26 offset:18432\n\t" "s_waitcnt lgkmcnt(5)\n\t" \
   "v_mfma_f32_16x16x32_bf16 v[52:55], a[128:131], v[96:99], v[52:55]\n\t" "v_mul_f32 v113, 0x3acccccd, v113\n\t" \
   "v_mfma_f32_16x16x32_bf16 v[48:51], a[48:51], v[96:99], v[48:51]\n\t" "ds_read_b128 v[92:95], v26 offset:19456\n\t" \
   "v_fma_f32 v113, -v112, v112, v113\n\t" "v_mfma_f32_16x16x32_bf16 v[52:55], a[132:135], v[100:103], v[52:55]\n\t" "v_max_f32 v113, 0, v113\n\t" \
   "v_mfma_f32_16x16x32_bf16 v[48:51], a[52:55], v[100:103], v[48:51]\n\t" "ds_read_b128 v[96:99], v26 offset:20480\n\t" "s_waitcnt lgkmcnt(5)\n\t" \
   "v_mfma_f32_16x16x32_bf16 v[52:55], a[136:139], v[104:107], v[52:55]\n\t" "v_cvt_pk_bf16_f32 v113, v113, v112\n\t" \
   "v_mfma_f32_16x16x32_bf16 v[48:51], a[56:59], v[104:107], v[48:51]\n\t" "ds_read_b128 v[100:103], v26 offset:21504\n\t" \
   "v_and_b32 v8, 0xffff0000, v113\n\t" "v_mfma_f32_16x16x32_bf16 v[52:55], a[140:143], v[108:111], v[52:55]\n\t" \
   "v_mfma_f32_16x16x32_bf16 v[48:51], a[60:63], v[108:111], v[48:51]\n\t" "ds_read_b128 v[104:107], v26 offset:22528\n\t" \
   "v_lshlrev_b32 v113, 16, v113\n\t" "s_waitcnt lgkmcnt(5)\n\t" "v_mfma_f32_16x16x32_bf16 v[52:55], a[144:147], v[80:83], v[52:55]\n\t" \
   "v_add_f32 v113, 0x3727c5ac, v113\n\t" "v_mfma_f32_16x16x32_bf16 v[48:51], a[64:67], v[80:83], v[48:51]\n\t" \
   "ds_read_b128 v[108:111], v26 offset:23552\n\t" "v_mfma_f32_16x16x32_bf16 v[52:55], a[148:151], v[84:87], v[52:55]\n\t" \
   "v_cvt_pk_bf16_f32 v113, v113, v113\n\t" "v_mfma_f32_16x16x32_bf16 v[48:51], a[68:71], v[84:87], v[48:51]\n\t" \
   "ds_read_b128 v[80:83], v26 offset:24576\n\t" "v_lshlrev_b32 v113, 16, v113\n\t" "s_waitcnt lgkmcnt(5)\n\t" \
   "v_mfma_f32_16x16x32_bf16 v[52:55], a[152:155], v[88:91], v[52:55]\n\t" "v_sqrt_f32 v113, v113\n\t" \
   "v_mfma_f32_16x16x32_bf16 v[48:51], a[72:75], v[88:91], v[48:51]\n\t" "ds_read_b128 v[84:87], v26 offset:25600\n\t" \
   "v_mfma_f32_16x16x32_bf16 v[52:55], a[156:159], v[92:95], v[52:55]\n\t" "s_nop 0\n\t" \
   "v_mfma_f32_16x16x32_bf16 v[48:51], a[76:79], v[92:95], v[48:51]\n\t" "ds_read_b128 v[88:91], v26 offset:26624\n\t" \
   "v_cvt_pk_bf16_f32 v113, v113, v113\n\t" "s_waitcnt lgkmcnt(5)\n\t" "v_mfma_f32_16x16x32_bf16 v[60:63], a[80:83], v[96:99], 0\n\t" \
   "v_mfma_f32_16x16x32_bf16 v[56:59], a[0:3], v[96:99], 0\n\t" "ds_read_b128 v[92:95], v26 offset:27648\n\t" "v_lshlrev_b32 v113, 16, v113\n\t" \
   "v_mfma_f32_16x16x32_bf16 v[60:63], a[84:87], v[100:103], v[60:63]\n\t" "v_rcp_f32 v10, v113\n\t" \
   "v_mfma_f32_16x16x32_bf16 v[56:59], a[4:7], v[100:103], v[56:59]\n\t" "ds_read_b128 v[96:99], v26 offset:28672\n\t" "s_waitcnt lgkmcnt(5)\n\t" \
   "v_mfma_f32_16x16x32_bf16 v[60:63], a[88:91], v[104:107], v[60:63]\n\t" "v_fmac_f32 v48, v32, v8\n\t" "v_fmac_f32 v49, v33, v8\n\t" \
   "v_mfma_f32_16x16x32_bf16 v[56:59], a[8:11], v[104:107], v[56:59]\n\t" "ds_read_b128 v[100:103], v26 offset:29696\n\t" "v_fmac_f32 v50, v34, v8\n\t" \
   "v_mfma_f32_16x16x32_bf16 v[60:63], a[92:95], v[108:111], v[60:63]\n\t" "v_fmac_f32 v51, v35, v8\n\t" \
   "v_mfma_f32_16x16x32_bf16 v[56:59], a[12:15], v[108:111], v[56:59]\n\t" "ds_read_b128 v[104:107], v26 offset:30720\n\t" \
   "v_fmac_f32 v52, v36, v8\n\t" "s_waitcnt lgkmcnt(5)\n\t" "v_mfma_f32_16x16x32_bf16 v[60:63], a[96:99], v[80:83], v[60:63]\n\t" \
   "v_fmac_f32 v53, v37, v8\n\t" "v_fmac_f32 v54, v38, v8\n\t" "v_mfma_f32_16x16x32_bf16 v[56:59], a[16:19], v[80:83], v[56:59]\n\t" \
   "ds_read_b128 v[108:111], v26 offset:31744\n\t" "v_fmac_f32 v55, v39, v8\n\t" \
   "v_mfma_f32_16x16x32_bf16 v[60:63], a[100:103], v[84:87], v[60:63]\n\t" "v_mul_f32 v48, v48, v10\n\t" \
   "v_mfma_f32_16x16x32_bf16 v[56:59], a[20:23], v[84:87], v[56:59]\n\t" "ds_read_b128 v[80:83], v26 offset:32768\n\t" "v_mul_f32 v49, v49, v10\n\t" \
   "s_waitcnt lgkmcnt(5)\n\t" "v_mfma_f32_16x16x32_bf16 v[60:63], a[104:107], v[88:91], v[60:63]\n\t" "v_mul_f32 v50, v50, v10\n\t" \
   "v_mul_f32 v51, v51, v10\n\t" "v_mfma_f32_16x16x32_bf16 v[56:59], a[24:27], v[88:91], v[56:59]\n\t" "ds_read_b128 v[84:87], v26 offset:33792\n\t" \
   "v_mul_f32 v52, v52, v10\n\t" "v_mfma_f32_16x16x32_bf16 v[60:63], a[108:111], v[92:95], v[60:63]\n\t" "v_mul_f32 v53, v53, v10\n\t" \
   "v_mfma_f32_16x16x32_bf16 v[56:59], a[28:31], v[92:95], v[56:59]\n\t" "ds_read_b128 v[88:91], v26 offset:34816\n\t" "v_mul_f32 v54, v54, v10\n\t" \
   "s_waitcnt lgkmcnt(5)\n\t" "v_mfma_f32_16x16x32_bf16 v[60:63], a[112:115], v[96:99], v[60:63]\n\t" "v_mul_f32 v55, v55, v10\n\t" \
   "v_cvt_pk_bf16_f32 v64, v48, v49\n\t" "v_mfma_f32_16x16x32_bf16 v[56:59], a[32:35], v[96:99], v[56:59]\n\t" \
   "ds_read_b128 v[92:95], v26 offset:35840\n\t" "v_cvt_pk_bf16_f32 v65, v50, v51\n\t" \
   "v_mfma_f32_16x16x32_bf16 v[60:63], a[116:119], v[100:103], v[60:63]\n\t" "v_cvt_pk_bf16_f32 v66, v52, v53\n\t" \
   "v_mfma_f32_16x16x32_bf16 v[56:59], a[36:39], v[100:103], v[56:59]\n\t" "ds_read_b128 v[96:99], v26 offset:36864\n\t" \
   "v_cvt_pk_bf16_f32 v67, v54, v55\n\t" "s_waitcnt lgkmcnt(5)\n\t" "v_mfma_f32_16x16x32_bf16 v[60:63], a[120:123], v[104:107], v[60:63]\n\t" \
   "buffer_store_dwordx2 v[64:65], v21, %[rs], s48 offen offset:0 sc1\n\t" "buffer_store_dwordx2 v[66:67], v21, %[rs], s47 offen offset:0 sc1\n\t" \
   "v_mfma_f32_16x16x32_bf16 v[56:59], a[40:43], v[104:107], v[56:59]\n\t" "ds_read_b128 v[100:103], v26 offset:37888\n\t" \
   "v_add_f32 v122, v122, v124\n\t" "v_mfma_f32_16x16x32_bf16 v[60:63], a[124:127], v[108:111], v[60:63]\n\t" "v_add_f32 v123, v123, v125\n\t" \
   "v_mfma_f32_16x16x32_bf16 v[56:59], a[44:47], v[108:111], v[56:59]\n\t" "ds_read_b128 v[104:107], v26 offset:38912\n\t" \
   "v_add_f32 v126, v126, v128\n\t" "s_waitcnt lgkmcnt(5)\n\t" "v_mfma_f32_16x16x32_bf16 v[60:63], a[128:131], v[80:83], v[60:63]\n\t" \
   "v_add_f32 v127, v127, v129\n\t" "v_add_f32 v122, v122, v130\n\t" "v_mfma_f32_16x16x32_bf16 v[56:59], a[48:51], v[80:83], v[56:59]\n\t" \
   "ds_read_b128 v[108:111], v26 offset:39936\n\t" "v_add_f32 v123, v123, v131\n\t" \
   "v_mfma_f32_16x16x32_bf16 v[60:63], a[132:135], v[84:87], v[60:63]\n\t" "v_add_f32 v122, v122, v126\n\t" \
   "v_mfma_f32_16x16x32_bf16 v[56:59], a[52:55], v[84:87], v[56:59]\n\t" "v_add_f32 v123, v123, v127\n\t" "s_waitcnt lgkmcnt(4)\n\t" \
   "v_mfma_f32_16x16x32_bf16 v[60:63], a[136:139], v[88:91], v[60:63]\n\t" "v_mul_f32 v122, 0xbacccccd, v122\n\t" \
   "v_mul_f32 v123, 0x3acccccd, v123\n\t" "v_mfma_f32_16x16x32_bf16 v[56:59], a[56:59], v[88:91], v[56:59]\n\t" "v_fma_f32 v123, -v122, v122, v123\n\t" \
   "v_mfma_f32_16x16x32_bf16 v[60:63], a[140:143], v[92:95], v[60:63]\n\t" "v_max_f32 v123, 0, v123\n\t" \
   "v_mfma_f32_16x16x32_bf16 v[56:59], a[60:63], v[92:95], v[56:59]\n\t" "v_cvt_pk_bf16_f32 v123, v123, v122\n\t" "s_waitcnt lgkmcnt(2)\n\t" \
   "v_mfma_f32_16x16x32_bf16 v[60:63], a[144:147], v[96:99], v[60:63]\n\t" "v_and_b32 v12, 0xffff0000, v123\n\t" "v_lshlrev_b32 v123, 16, v123\n\t" \
   "v_mfma_f32_16x16x32_bf16 v[56:59], a[64:67], v[96:99], v[56:59]\n\t" "v_add_f32 v123, 0x3727c5ac, v123\n\t" \
   "v_mfma_f32_16x16x32_bf16 v[60:63], a[148:151], v[100:103], v[60:63]\n\t" "v_cvt_pk_bf16_f32 v123, v123, v123\n\t" \
   "v_mfma_f32_16x16x32_bf16 v[56:59], a[68:71], v[100:103], v[56:59]\n\t" "v_lshlrev_b32 v123, 16, v123\n\t" "s_waitcnt lgkmcnt(0)\n\t" \
   "v_mfma_f32_16x16x32_bf16 v[60:63], a[152:155], v[104:107], v[60:63]\n\t" "v_sqrt_f32 v123, v123\n\t" "s_nop 0\n\t" \
   "v_mfma_f32_16x16x32_bf16 v[56:59], a[72:75], v[104:107], v[56:59]\n\t" "v_cvt_pk_bf16_f32 v123, v123, v123\n\t" \
   "v_mfma_f32_16x16x32_bf16 v[60:63], a[156:159], v[108:111], v[60:63]\n\t" "v_lshlrev_b32 v123, 16, v123\n\t" \
   "v_mfma_f32_16x16x32_bf16 v[56:59], a[76:79], v[108:111], v[56:59]\n\t" "v_rcp_f32 v132, v123\n\t" "s_xor_b32 s46, s46, 44032\n\t" \
   "s_add_u32 s45, s45, 4\n\t" "s_nop 5\n\t" "v_fmac_f32 v56, v32, v12\n\t" "v_fmac_f32 v57, v33, v12\n\t" "v_fmac_f32 v58, v34, v12\n\t" \
   "v_fmac_f32 v59, v35, v12\n\t" "v_fmac_f32 v60, v36, v12\n\t" "v_fmac_f32 v61, v37, v12\n\t" "v_fmac_f32 v62, v38, v12\n\t" \
   "v_fmac_f32 v63, v39, v12\n\t" "v_mul_f32 v56, v56, v132\n\t" "v_mul_f32 v57, v57, v132\n\t" "v_mul_f32 v58, v58, v132\n\t" \
   "v_mul_f32 v59, v59, v132\n\t" "v_mul_f32 v60, v60, v132\n\t" "v_mul_f32 v61, v61, v132\n\t" "v_mul_f32 v62, v62, v132\n\t" \
   "v_mul_f32 v63, v63, v132\n\t" "v_cvt_pk_bf16_f32 v68, v56, v57\n\t" "v_cvt_pk_bf16_f32 v69, v58, v59\n\t" "v_cvt_pk_bf16_f32 v70, v60, v61\n\t" \
   "v_cvt_pk_bf16_f32 v71, v62, v63\n\t" "buffer_store_dwordx2 v[68:69], v21, %[rs], s48 offen offset:128 sc1\n\t" \
   "buffer_store_dwordx2 v[70:71], v21, %[rs], s47 offen offset:128 sc1\n\t" "s_cmp_lt_u32 s45, 64\n\t" "s_cbranch_scc1 1b\n\t" \
   "s_waitcnt vmcnt(0) lgkmcnt(0)"
#define DF_CLOB "v8", "v9", "v10", "v11", "v12", "v13", "v14", "v15", "v32", "v33", "v34", "v35", "v36", "v37", "v38", "v39", "v40", "v41", "v42", "v43", "v44", "v45", "v46", "v47", "v48", "v49", "v50", "v51", "v52", "v53", "v54", "v55", "v56", "v57", "v58", "v59", "v60", "v61", "v62", "v63", "v64", "v65", "v66", "v67", "v68", "v69", "v70", "v71", "v72", "v73", "v74", "v75", "v76", "v77", "v78", "v79", "v80", "v81", "v82", "v83", "v84", "v85", "v86", "v87", "v88", "v89", "v90", "v91", "v92", "v93", "v94", "v95", "v96", "v97", "v98", "v99", "v100", "v101", "v102", "v103", "v104", "v105", "v106", "v107", "v108", "v109", "v110", "v111", "v112", "v113", "v114", "v115", "v116", "v117", "v118", "v119", "v120", "v121", "v122", "v123", "v124", "v125", "v126", "v127", "v128", "v129", "v130", "v131", "v132", "v133", "v134", "v135", "s40", "s41", "s42", "s43", "s44", "s45", "s46", "s47", "s48", "s49", "s50", "s51", "s52", "s53"
template <bool VU>
__device__ __forceinline__ void job_proj(int wv_, const v32u (&w)[10], unsigned char* ws, const Pool& pool, int layer, int h) {
  const unsigned lds0 = (unsigned)(uintptr_t)(__attribute__((address_space(3))) unsigned char*)dyn_lds;
  const int lane = lane_id(), wv = wv_, g = lane >> 4, r = lane & 15;
  const v4u rs = make_rs(ws, (unsigned)OFF_END);
  v16u ad = (unsigned)lane;
  ad[0] = (int)OFF_XR + wv * 10240 + lane * 16;
  ad[1] = wv * 64 + lane < 96 ? (int)OFF_ST + wv * 1024 + lane * 16 : OOB;
  ad[3] = cnt_off(layer, CNT_IDFT, lane & 31);
  ad[4] = lane == 0 ? kqf_off(layer, 0, (VU ? 32 : 0) + h) + wv * 4 : OOB;
  ad[5] = (VU ? (int)OFF_UF + (wv * 320 + h) * 1024 : (h * RT * 4 + wv) * 1024) + lane * 16;
  ad[6] = lds0 + PJ_X + lane * 16;
  ad[7] = lds0 + PJ_X + 40960 + r * 48;
  ad[8] = lds0 + wv * 1024 + lane * 16;
  ad[9] = lds0 + 8192 + lane * 16;
  v4u rf = rs;
  v8u fo;
  if constexpr (!VU) {
    fo[0] = h * 16384 + wv * 2048 + lane * 16;
#pragma unroll
    for (int k = 0; k < 3; ++k) { const int u = h * 10 + wv + 4 * k; fo[1 + k] = wv + 4 * k < 10 ? (u >> 3) * 16384 + (u & 7) * 1024 + lane * 16 : OOB; }
    asm volatile("v_mov_b32 %0, -1" : "=v"(fo[4]));
    fo[5] = fo[6] = fo[7] = fo[4];
  }
  if (layer > 0) fan_wait<64, 60>(wv_, ws, fan_off(1, 1, 0), h);
  PH(2 + layer * 6)
#define PJ_OPS                                                                                                                      \
  : W_OPS, [rs] "s"(rs), [rdy] "s"((layer - 1) | 63), [ldx] "s"(lds0 + PJ_X + wv * 10240),                        \
    [ld1] "s"(lds0 + PJ_X + 40960 + (wv < 2 ? wv : 2) * 1024), [qf] "s"((int)OFF_QF), [kf] "s"((int)OFF_KF), [cnt] "s"(cnt_off(layer, CNT_IDFT, 0))
  if constexpr (VU) asm volatile(VU_ASM : "+{v[16:31]}"(ad) PJ_OPS : VU_CLOB, "vcc", "scc", "m0", "memory");
  else asm volatile(KQ_ASM : "+{v[16:31]}"(ad), "+&{s[56:59]}"(rf) PJ_OPS, "{v[152:159]}"(fo), [sel] "s"(pool.sel), [ysel] "s"(pool.ysel), [pool] "s"(pool.base),
                    [yb] "s"(pool.base + P_Y), [size] "s"((unsigned)OFF_END), [skip] "s"(h >= 20 && h < 30 ? 3ull << 62 : 2ull << 62)
                    : KQ_CLOB, "vcc", "scc", "m0", "memory");
  LDSBAR();
}

__device__ __forceinline__ unsigned dft_idx(int f, int d) { return (unsigned)((f == 1 ? 320 : f >> 1) * d + (((f & 1) && f != 1) ? 160 : 0)) % 640u; }
__device__ __forceinline__ float cos640(unsigned i) { return __builtin_amdgcn_cosf((float)i * (1.f / 640)); }
__device__ __forceinline__ void job_dft(int wv_, unsigned char* ws, int layer, int fg, int set) {
  const unsigned lds0 = (unsigned)(uintptr_t)(__attribute__((address_space(3))) unsigned char*)dyn_lds;
  const int lane = lane_id(), wv = wv_, g = lane >> 4, r = lane & 15;
  const v4u rs = make_rs(ws, (unsigned)OFF_END);
  v32u w[5];
#pragma unroll
  for (int t = 0; t < 2; ++t) {
    const int f = 16 * (fg * 8 + wv * 2 + t) + r;
    const unsigned step = dft_idx(f & ~(f != 1), 32);
#pragma unroll
    for (int q = 0; q < 4; ++q) {
      unsigned m0 = dft_idx(f, sigma(g, 2 * q)), m1 = dft_idx(f, sigma(g, 2 * q + 1));
#pragma unroll
      for (int kt = 0; kt < KT; ++kt) {
        const int i = t * KT + kt;
        w[i >> 3][(i & 7) * 4 + q] = pk(cos640(m0), cos640(m1));
        m0 += step; m0 = min(m0, m0 - 640u); m1 += step; m1 = min(m1, m1 - 640u);
      }
    }
  }
  v16u ad = (unsigned)lane;
  ad[0] = (int)OFF_XR + wv * 10240 + lane * 16;
  ad[1] = wv * 64 + lane < 96 ? (int)OFF_ST + wv * 1024 + lane * 16 : OOB;
  ad[3] = cnt_off(layer, CNT_OUT, 0);
  ad[5] = (int)OFF_X1T + (((fg * 8 + wv * 2) * 4 + g) * S_ + r) * 8;
  ad[6] = lds0 + lane * 16;
  ad[7] = lds0 + 40960 + r * 48;
  asm volatile(DF_ASM
               : "+{v[16:31]}"(ad)
               : "{a[0:31]}"(w[0]), "{a[32:63]}"(w[1]), "{a[64:95]}"(w[2]), "{a[96:127]}"(w[3]), "{a[128:159]}"(w[4]), [rs] "s"(rs), [set] "s"(set),
                 [ldx] "s"(lds0 + wv * 10240), [ld0] "s"(lds0), [ld1] "s"(lds0 + 40960 + (wv < 2 ? wv : 2) * 1024)
               : DF_CLOB, "vcc", "scc", "m0", "memory");
}

constexpr int NOUT = 44, NLATE = 108;
constexpr int L_HS = PJ_LDS, MAIN_LDS = L_HS + 1024;
static_assert(MAIN_LDS >= JOB_LDS && MAIN_LDS >= REC_LDS && MAIN_LDS <= 160 * 1024, "LDS");
struct Go { Pool pool; int rot; };
__device__ __forceinline__ void st32(rsrc_t rs, int off, unsigned v) { __builtin_amdgcn_raw_buffer_store_b32(v, rs, off, 0, AUX_SC1); }
__device__ __forceinline__ unsigned hello_load(rsrc_t rs, int b) { return __builtin_amdgcn_raw_buffer_load_b32(rs, HS_GO + b * 16, 0, AUX_SC1); }
__device__ __forceinline__ void hello(int wv, rsrc_t rs, int b, unsigned t) {
  if (wv == 0) {
    st32(rs, lane_id() == 0 ? HS_HI + b * 8 : OOB, t + 1u);
    LDS_U32(lds_base() + L_HS) = t + 1u;
  }
}
__device__ __forceinline__ int go_off(int b) {
  const int l = lane_id() & 3;
  return l == 0 ? HS_GO + b * 16 : (l == 3 ? HS_ASK + (b & 127) * 16 : HS_PAY + (b & 31) * 32 + l * 16 - 16);
}
__device__ __forceinline__ Go wait_go(int wv, unsigned char* ws, int b, v4u rep, v4u m) {
  const v4u rs = make_rs(ws, (unsigned)OFF_END);
  const int l = lane_id();
  const unsigned a = __builtin_amdgcn_readfirstlane(LDS_U32(lds_base() + L_HS));
  for (unsigned spins = 0; spins < (1u << 18); ++spins) {
    if (spins || b >= 128) {
      VM_LD128(m, go_off(b), 0, 0);
      VM_WAIT1(0, m);
    }
    const unsigned n = __builtin_amdgcn_readlane(m.y, 0);
    if (__builtin_amdgcn_readlane(m.x, 0) == a && __builtin_amdgcn_readlane(m.x, 1) == n && __builtin_amdgcn_readlane(m.x, 2) == n) break;
    const unsigned t = (wv & 2) ? ((wv & 1) ? m.w : m.z) : ((wv & 1) ? m.y : m.x);
    const bool again = b >= 128 && l == 3 && t != rep.x;
    rep.x = t;
    VM_ST128(rep, again ? HS_CAL - 128 * 64 + b * 64 + wv * 16 : OOB, 0);
    if (spins) __builtin_amdgcn_s_sleep(6);
  }
  VM_ST32(m.z, (wv == 0 && l == 0) ? HS_HI + b * 8 + 4 : OOB, 0);
  if (b >= 128) { PH(1) }
  const auto u64 = [](unsigned lo, int i, unsigned hi, int k) {
    return (unsigned)__builtin_amdgcn_readlane(lo, i) | (unsigned long long)(unsigned)__builtin_amdgcn_readlane(hi, k) << 32;
  };
  Go g;
  g.pool.sel = u64(m.y, 1, m.z, 1); g.pool.s3 = u64(m.w, 1, m.w, 2); g.pool.ysel = u64(m.y, 2, m.z, 2);
  g.pool.base = __builtin_amdgcn_readfirstlane(pool_base(ws));
  g.rot = __builtin_amdgcn_readlane(m.w, 0) & 7;
  return g;
}
#define LDSTAMP(n)
#define LDX
#define LDC
__device__ __forceinline__ v4u load_rows(v32u (&w)[10], int kind, const u16* A, const u16* B, const u16* x, int h, int wv, unsigned slot, unsigned char* ws, int b, bool first) {
  unsigned tr;
  const int l = lane_id(), i = l & 15, g = l >> 4;
  const v4u ra = make_rs(A, 640u * 8192u), rb = make_rs(B, 640u * 8192u), rs = make_rs(ws, (unsigned)OFF_END), rx = make_rs(x, S_ * D_ * 2);
  v8u ad, ex;
  const int wr = 4 * wv + g, rr = 4 * g + (i >> 2);
  ad[0] = wr * 8192 + h * 256 + i * 16;
  ad[1] = (h * 128 + wr) * 1280 + i * 16;
  ad[2] = slot + rr * 256 + ((2 * wv) ^ (rr & 7)) * 32 + (i & 3) * 8;
  ad[3] = ad[2] ^ 32u;
  ad[4] = slot + wr * 256 + ((i >> 1) ^ (wr & 7)) * 32 + (i & 1) * 16;
  ad[5] = slot + wr * 328 + ((i >> 2) * 2 + (i & 1)) * 8 + ((i >> 1) & 1) * 4;
  ad[6] = slot + (kind == 3 ? (16 * (g & 1) + 4 * (g >> 1) + ((i >> 2) & 1)) * 328 + (i >> 3) * 8 : rr * 328) + (i & 3) * 10560 + wv * 80;
  ad[7] = ad[4] + 65536u;
  const unsigned im = lds_base() + PJ_X + (wv >> 1) * PJ_BUF;
  const bool hi = first && wv == 0;
  ex[0] = go_off(b);
  ex[1] = hi ? HS_GO + b * 16 : OOB;
  ex[2] = hi && l == 0 ? HS_HI + b * 8 : OOB;
  ex[3] = lds_base() + L_HS + (hi ? 0 : 4);
  ex[4] = (wv * 16 + (l >> 3)) * 1280 + (l & 7) * 16;
  ex[5] = im + ((wv & 1) * KT + ((l >> 2) & 1)) * 1024 + ((l & 1) * 32 + (l >> 3)) * 16 + (l & 2) * 4;
  ex[6] = im + 40960 + ((wv & 1) * 16 + (l >> 3)) * 48 + min(l & 7, 5) * 8;
  ex[7] = (l & 7) ? 0u : ~0u;
  v4u m;
#define LD_OPS                                                                                                                                       \
  : "={a[0:31]}"(w[0]), "={a[32:63]}"(w[1]), "={a[64:95]}"(w[2]), "={a[96:127]}"(w[3]), "={a[128:159]}"(w[4]), "={a[160:191]}"(w[5]),                \
    "={a[192:223]}"(w[6]), "={a[224:255]}"(w[7]), "={v[192:223]}"(w[8]), "={v[224:255]}"(w[9]), "={v[28:31]}"(m) LDX                                 \
  : "{v[16:23]}"(ad), "{v[112:119]}"(ex), [ra] "s"(ra), [rb] "s"(rb), [rw] "s"(rs), [rx] "s"(rx), [p0] "s"(wv < 3 ? 3 * wv : 8)                      \
  : LDP_CLOB LDC, "scc", "memory"
  if (kind == 0) asm volatile(LDP_ASM LD_OPS);
  else if (kind == 1) asm volatile(LDK_ASM LD_OPS);
  else if (kind == 2) asm volatile(LDV_ASM LD_OPS);
  else asm volatile(LDY_ASM LD_OPS);
  return m;
}
__device__ __forceinline__ v4u prep_jobs(int wv, unsigned char* ws, rsrc_t rs, const u16* __restrict__ x, const u16* __restrict__ mem0, int j) {
  const int lane = lane_id(), g = lane >> 4, r = lane & 15, tid = wv * 64 + lane, pb = pool_base(ws);
  const int xc = (int)xcc_id(), u = (j >> 3) * 4 + (xc & 3), page = xc < 4 ? u >> 2 : 16 + u % NYP;
  const int cb = pb + page * PAGE + M_CAL + (xc < 4 ? wv * REG + xc * 512 : 3 * REG - u / NYP * WIN + wv * 512);
  stc(rs, lane < 32 ? cb + (lane >> 3) * 8192 + (lane & 7) * 16 : OOB, v4u{1u, 2u, 3u, 4u});
  const unsigned char* p = ws + __builtin_amdgcn_readfirstlane(cb);
  unsigned t0, t1, t2, t3, t4;
  asm volatile("s_load_dword %0, %4, 0x80\n\ts_load_dword %1, %4, 0x2080\n\ts_load_dword %2, %4, 0x4080\n\ts_load_dword %3, %4, 0x6080"
               : "={s40}"(t0), "={s41}"(t1), "={s42}"(t2), "={s43}"(t3) : "s"(p) : "memory");
  const unsigned tg = hello_load(rs, 128 + j);
  v2u c[5][2];
  unsigned t[4];
#pragma unroll
  for (int q = 0; q < 5; ++q) {
    const u16* xr = x + (size_t)(j * 16 + r) * D_ + 32 * (5 * wv + q) + 4 * g;
    c[q][0] = *(const v2u*)xr; c[q][1] = *(const v2u*)(xr + 16);
  }
#pragma unroll
  for (int k = 0; k < 4; ++k) t[k] = *(const unsigned*)(mem0 + (size_t)((tid >> 1) & 7) * D_ + colmap((min(j >> 3, 4) * 8 + ((tid >> 4) & 7)) * 2 + (tid & 1), 2 * k));
  __builtin_amdgcn_sched_barrier(0);
  const v4u ones = {0xFFFFFFFFu, 0xFFFFFFFFu, 0xFFFFFFFFu, 0xFFFFFFFFu};
  const int o = (j * 256 + tid) * 16;
  const bool cal = (o & 0x79800) == 0x79800;
  stc(rs, (int)OFF_HO + o, ones);
  stc(rs, o + REG < (int)HO_FIRST ? (int)OFF_HO + REG + o : OOB, ones);
  stc(rs, (cal || (o >= W_MEM && o < W_MISC)) ? OOB : pb + (NSEG - 1) * REG + o, ones);
  stc(rs, cal ? OOB : pb + o, ones);
  stc(rs, pb + P_Y + o, ones);
  stc(rs, o < 8 * 16384 ? pb + P_Y + REG + o : OOB, ones);
  hello(wv, rs, 128 + j, tg);
  asm volatile("" ::"v"(tg));
  __builtin_amdgcn_sched_barrier(0);
#define CAL_T(r, o) "s_load_dword " r ", %5, " o "\n\ts_waitcnt lgkmcnt(0)\n\ts_memtime s[46:47]\n\ts_waitcnt lgkmcnt(0)\n\ts_mov_b32 " r ", s46\n\t"
  asm volatile(CAL_T("%0", "0x80") CAL_T("%1", "0") CAL_T("%2", "0x2000") CAL_T("%3", "0x4000") CAL_T("%4", "0x6000")
               : "+{s40}"(t0), "+{s41}"(t1), "+{s42}"(t2), "+{s43}"(t3), "={s44}"(t4) : "s"(p) : "s46", "s47", "memory");
  PH(16)
  __builtin_amdgcn_sched_barrier(0);
  f32x2 st = {0.f, 0.f};
#pragma unroll
  for (int q = 0; q < 5; ++q) {
    const v4u f = v4u{c[q][0].x, c[q][0].y, c[q][1].x, c[q][1].y};
    stc(rs, (int)OFF_XR + ((j * KT + 5 * wv + q) * 64 + lane) * 16, f);
    st += frag_stat(f);
  }
  stc(rs, j < 40 ? pb + (NSEG - 1) * REG + W_MEM + o : OOB, v4u{nosent(t[0]), nosent(t[1]), nosent(t[2]), nosent(t[3])});
  st = rows_sum(st);
  __builtin_amdgcn_raw_buffer_store_b64(__builtin_bit_cast(v2u, g ? f32x2{0.f, 0.f} : st), rs,
                                        (g == 0 || (g == 1 && wv == 0)) ? (int)OFF_ST + ((j * 16 + r) * 6 + (g ? 4 : wv)) * 8 : OOB, 0, AUX_SC1);
  const unsigned ask = __builtin_amdgcn_raw_buffer_load_b32(rs, HS_ASK + j * 16 + wv * 4, 0, AUX_SC1);
  const unsigned b0[4] = {min(t1 - t0, 9999u), min(t2 - t1, 9999u), min(t3 - t2, 9999u), min(t4 - t3, 9999u)}, sh = ((xc ^ wv) & 1) * 16;
  const v4u rep = {ask, (unsigned)page, b0[0] << sh | b0[2] << (16 - sh), b0[1] << sh | b0[3] << (16 - sh)};
  asm volatile("s_waitcnt vmcnt(0)" ::: "memory");
  stc(rs, lane == 0 ? HS_CAL + j * 64 + wv * 16 : OOB, rep);
  LDSBAR();
  PH(18)
  return rep;
}
__device__ __forceinline__ int wave_isum(int v) {
  asm volatile("s_nop 1\n\tv_add_u32_dpp %0, %0, %0 quad_perm:[1,0,3,2] row_mask:0xf bank_mask:0xf\n\t"
               "s_nop 1\n\tv_add_u32_dpp %0, %0, %0 quad_perm:[2,3,0,1] row_mask:0xf bank_mask:0xf\n\t"
               "s_nop 1\n\tv_add_u32_dpp %0, %0, %0 row_half_mirror row_mask:0xf bank_mask:0xf\n\t"
               "s_nop 1\n\tv_add_u32_dpp %0, %0, %0 row_mirror row_mask:0xf bank_mask:0xf\n\ts_nop 0"
               : "+v"(v));
  return (__builtin_amdgcn_readlane(v, 0) + __builtin_amdgcn_readlane(v, 16)) + (__builtin_amdgcn_readlane(v, 32) + __builtin_amdgcn_readlane(v, 48));
}
__device__ __forceinline__ Go lead(int wv, unsigned char* ws, const u16* x, const u16* mem0, int rot) {
  const v4u rs = make_rs(ws, (unsigned)OFF_END);
  const int t = wv * 64 + lane_id();
  int* acc = (int*)(dyn_lds + L_HS + 128);
#define VM_LD64(dst, off) asm volatile("buffer_load_dwordx2 %0, %1, %2, 0 offen sc1" : "=v"(dst) : "v"(off), "s"(rs) : "memory")
  v4u c0, c1;
  v2u hi;
  unsigned old, n = 0;
  VM_LD128(c0, HS_CAL + t * 32, 0, 0);
  VM_LD128(c1, HS_CAL + t * 32, 0, 16);
  VM_LD64(hi, HS_HI + t * 8);
  VM_LD32(old, HS_PAY + (t & 63) * 16, 0);
  asm volatile("s_waitcnt vmcnt(0)" : "+v"(c0), "+v"(c1), "+v"(hi), "+v"(old)::"memory");
  const unsigned r0 = c0.x + 1u, r1 = c1.x + 1u, q = hi.y + 1u;
  __builtin_amdgcn_raw_buffer_store_b64(v2u{r0, r1}, ws_rsrc(ws), HS_ASK + t * 8, 0, AUX_SC1);
  while (__any(old == n)) ++n;
  if (t < 192) acc[t] = 9999;
  prep_jobs(wv, ws, ws_rsrc(ws), x, mem0, 127);
  for (unsigned spins = 0; spins < (1u << 18); ++spins) {
    VM_LD128(c0, HS_CAL + t * 32, 0, 0);
    VM_LD128(c1, HS_CAL + t * 32, 0, 16);
    VM_LD64(hi, HS_HI + t * 8);
    asm volatile("s_waitcnt vmcnt(0)" : "+v"(c0), "+v"(c1), "+v"(hi)::"memory");
    if (!__any(t < 254 && (c0.x != r0 || c1.x != r1))) break;
    __builtin_amdgcn_s_sleep(2);
  }
  if (t < 254) {
    int *a0 = acc + min(c0.y, 47u) * 4, *a1 = acc + min(c1.y, 47u) * 4;
    atomicMin(a0, c0.z & 0xFFFF); atomicMin(a0 + 1, c0.z >> 16); atomicMin(a0 + 2, c0.w & 0xFFFF); atomicMin(a0 + 3, c0.w >> 16);
    atomicMin(a1, c1.z & 0xFFFF); atomicMin(a1 + 1, c1.z >> 16); atomicMin(a1 + 2, c1.w & 0xFFFF); atomicMin(a1 + 3, c1.w >> 16);
  }
  __syncthreads();
  PH(19)
  const int s = lane_id(), fx = (s & 4) ? -1 : 1, fy = ((s * 43 >> 7) & 1) ? -1 : 1;
  const v4i u = ((const v4i*)acc)[s >> 2], v = ((const v4i*)acc)[16 + (s * 43 >> 7)];
  int ax = min(u.x, u.y) - min(u.z, u.w), yx = min(v.z, v.w) - min(v.x, v.y);
  const int n0 = ax > 0 ? u.z : u.x, n1 = ax > 0 ? u.w : u.y, lu = min(n0, n1), lv = min(min(v.x, v.y), min(v.z, v.w)), near = 110 - wave_imax(-min(lu, lv));
  const int sa = (lu < near && abs(ax) > 60) ? (ax > 0 ? fx : -fx) : 0, sy = (lv < near && abs(yx) > 60) ? (yx > 0 ? fy : -fy) : 0;
  const int g = wave_isum(sa + sy), m = wave_isum(abs(sa) + abs(sy));
  const bool with = sa * g > 0;
  const int bs = wave_imax(with ? -n1 : -9999) - wave_imax(with ? -n0 : -9999);
  int b = n0 - n1;
  if (abs(g) * 8 > m * 7) {
    if (sa * g >= 0) { ax = g * fx; b = bs; }
    if (sy == 0) yx = g * fy;
  }
  Go go;
  go.pool.sel = __ballot(ax > 0);
  go.pool.s3 = __ballot(b > 0) ^ 0xAAAAAAAAAAAAAAAAull;
  go.pool.ysel = __ballot(yx > 0);
  go.pool.base = __builtin_amdgcn_readfirstlane(pool_base(ws));
  go.rot = rot;
  PH(20)
  {
    const unsigned long long v = (s & 1) ? go.pool.ysel : go.pool.sel;
    VM_ST128((v4u{n, (unsigned)v, (unsigned)(v >> 32), (unsigned)(go.pool.s3 >> (s & 1) * 32)}), wv == 0 ? HS_PAY + s * 16 : OOB, 0);
  }
  for (unsigned spins = 0; spins < (1u << 18); ++spins) {
    VM_ST128((v4u{hi.x, n, q, (unsigned)rot}), HS_GO + t * 16, 0);
    const unsigned sent = hi.x;
    do {
      __builtin_amdgcn_s_sleep(8);
      VM_LD64(hi, HS_HI + t * 8);
      VM_WAIT1(0, hi);
    } while (!__any(hi.x != sent) && __any(t < 255 && hi.y != q) && ++spins < (1u << 18));
    if (!__any(t < 255 && hi.y != q)) break;
  }
  return go;
}
__device__ __forceinline__ void late_jobs(int wv_, unsigned char* ws, rsrc_t rs, const u16* __restrict__ wr, const u16* __restrict__ wi, int j) {
  const int lane = lane_id(), wv = wv_, g = lane >> 4, r = lane & 15;
  unsigned* t32 = (unsigned*)dyn_lds;
  for (int o = (int)HO_FIRST + (j * 256 + wv * 64 + lane) * 16; o < 2 * (int)HO_LAYER; o += NLATE * 4096)
    stc(rs, (int)OFF_HO + o, v4u{0xFFFFFFFFu, 0xFFFFFFFFu, 0xFFFFFFFFu, 0xFFFFFFFFu});
#pragma unroll 1
  for (int fi = j * 4 + wv; fi < 40 * NFK; fi += NLATE * 4) {
    v4u v;
#pragma unroll
    for (int q = 0; q < 4; ++q) {
      const int d = 16 * (fi / NFK) + r, f = 32 * (fi % NFK) + 8 * g + 2 * q;
      const float sc = f ? 2.f : 1.f;
      v[q] = pk(cos640(dft_idx(f, d)) * sc, cos640(dft_idx(f + 1, d)) * sc);
    }
    stc(rs, (int)OFF_INV + (fi * 64 + lane) * 16, v);
  }
#pragma unroll 1
  for (int b = j; b < 32 * 6; b += NLATE) {
    const int m0 = (b & 31) * 64, k0 = (b >> 5) * 64, n0 = (b & 24) * 2 + (b & 7) * 256;
#pragma unroll
    for (int q = 0; q < 16; ++q) {
      const size_t i = (size_t)(n0 + q + 64 * wv) * 321 + min(k0 + lane, 320);
      t32[(q * 4 + wv) * 65 + lane] = (unsigned)wr[i] | ((unsigned)wi[i] << 16);
    }
    __syncthreads();
#pragma unroll
    for (int q = 0; q < 16; ++q) {
      const int k = q * 4 + wv;
      st32(rs, k0 + k < 321 ? (int)OFF_WT + ((k0 + k) * S_ + m0 + lane) * 4 : OOB, t32[lane * 65 + k]);
    }
    __syncthreads();
  }
  dep_signal(wv_, ws, cnt_off(0, CNT_IDFT, 0));
}
__device__ __forceinline__ void late_wait(int wv_, unsigned char* ws) {
  if (TIDX == 0) {
    for (unsigned spins = 0; spins < (1u << 17); ++spins) {
      if (__hip_atomic_load((gu32*)(ws + cnt_off(0, CNT_IDFT, 0)), __ATOMIC_RELAXED, __HIP_MEMORY_SCOPE_AGENT) == NLATE - 1) break;
      __builtin_amdgcn_s_sleep(16);
    }
  }
  __syncthreads();
}
#define TROB (24 * 256 + 16)
#define TC2 30
#define TVIS 32
#define TDLT 170
#define TM1 88
#define TM2 60
__global__ void __launch_bounds__(256, 1) k_main(unsigned char* ws, const u16* x, const u16* mem0, const u16* Wq, const u16* Wk, const u16* Wv, const u16* Wo,
                                                const u16* wr, const u16* wi, long nlayer
                                                ) {
  v4u* smem = (v4u*)dyn_lds;
  const int wv_ = __builtin_amdgcn_readfirstlane(threadIdx.x >> 6);
  const RecTune tn = {TM1, TM2, 4, TROB, TC2, TVIS, TDLT, 1};
  unsigned long long* dbg = nullptr;
#define RSTAMP_(i)
  const rsrc_t rs = ws_rsrc(ws);
  PH(0)
  const int b = blockIdx.x, tt = b >> 3;
  int rot = (int)(xcc_id() - b) & 7;
  if (tt < 16) {
    v32u w[10];
    Go go;
    const unsigned lds0 = lds_base();
#pragma unroll 1
    for (int it = 0;; ++it) {
      const int p = (b + rot) & 7, h = (p & 3) * 8 + (tt & 7);
      const bool v = (tt & 8) != 0;
      const v4u m = load_rows(w, p < 4 ? (v ? 3 : 0) : (v ? 2 : 1), v ? Wv : Wq, v ? Wo : Wk, x, h, wv_, lds0 + (p < 4 ? 0 : PJ_X + 2 * PJ_BUF), ws, b, it == 0);
      PH(21 + (p >> 2))
      go = wait_go(wv_, ws, b, m, m);
      if (go.rot == rot || it) break;
      rot = go.rot;
    }
    Pool pool = go.pool;
    const int pl = (b + rot) & 7, role = pl < 4 ? (tt & 8) * 4 + pl * 8 + (tt & 7) : pl * 32 + tt;
    const int hd = (pl - 4) * 8 + (tt & 7);
#pragma unroll 1
    for (int layer = 0; layer < nlayer; ++layer) {
      int ro = role, xs = pl, wq = wv_, h2 = hd | (tt & 8) << 8;
      asm volatile("" : "+s"(ro), "+s"(xs), "+s"(wq), "+s"(h2), "+s"(ws), "+s"(pool.base));
      if (layer > 0) {
        const int jb = ro < 64 ? ro + 57 : (xs - 4) * 16 + (ro & 31);
        late_wait(wq, ws);
        if (ro < 64 || jb < 57) {
          job_col(wq, ws, ws_rsrc(ws), lds_base(), jb, fan_off(0, 1, 0));
          fan_signal<FSP>(wq, ws, fan_off(1, 0, 0), jb);
          RSTAMP_(5)
          PH(7)
        } else fan_wait<64, 20>(wq, ws, fan_off(0, 1, 0), jb);
      }
      if (ro < 32) { rec_p1(wq, w, ws, pool, layer, ro, xs, tn, dbg); PH(4 + layer * 6) }
      else if (ro < 64) rec_pv(wq, w, ws, pool, layer, ro - 32, xs, tn, dbg);
      else {
        if (h2 >> 8) job_proj<true>(wq, w, ws, pool, layer, h2 & 31);
        else job_proj<false>(wq, w, ws, pool, layer, h2 & 31);
        RSTAMP_(2)
      }
    }
    return;
  }
  const Go go = b == 255 ? lead(wv_, ws, x, mem0, rot) : wait_go(wv_, ws, b, prep_jobs(wv_, ws, rs, x, mem0, b - 128), v4u{0u, 0u, 0u, 0u});
  Pool pool = go.pool;
  const int pl = (b + go.rot) & 7;
  const int role = pl < 4 ? (tt < 26 ? 64 + pl * 10 + tt - 16 : 104 + pl * 6 + tt - 26) : pl * 32 + tt;
  if (pl < 4 || tt < 27) late_jobs(wv_, ws, rs, wr, wi, pl < 4 ? 44 + pl * 16 + tt - 16 : (pl - 4) * 11 + tt - 16);
  PH(23)
  if (role >= 104 && role < 128) return;
  if (role >= 64 && role < 104) {
#pragma unroll 1
    for (int layer = 0; layer < nlayer; ++layer) {
      if (layer > 0) {
        late_wait(wv_, ws);
        job_col(wv_, ws, rs, lds_base(), role + 57, fan_off(0, 1, 0));
        fan_signal<FSP>(wv_, ws, fan_off(1, 0, 0), role + 57);
        PH(7)
      }
      ((unsigned*)(dyn_lds + L_T))[lane_id() & 15] = 0u;
      __syncthreads();
      if (wv_ == 3) rec_fin(ws, pool, layer, role - 64, pl, tn, dbg);
      else if (wv_ == 0) rec_sched(ws, pool, layer, role - 64, pl, tn, dbg);
      __syncthreads();
    }
    return;
  }
#pragma unroll 1
  for (int layer = 0; layer < nlayer; ++layer) {
    if (layer > 0) late_wait(wv_, ws);
    if (layer > 0 && tt < 31) {
      const int i = tt - 16;
      job_idft<false>(wv_, ws, fan_off(1, 0, 0), rs, smem, (i / 5) * 4 + pl - 4, 12, i % 5, nullptr, dbg + 60000 + role * 8);
    }
    RSTAMP_(2)
    __syncthreads();
    if (tt >= 27) {
      job_dft(wv_, ws, layer, tt - 27, pl - 4);
      if (layer == 0) fan_signal<64>(wv_, ws, fan_off(0, 1, 0), (pl - 4) * 5 + tt - 27);
    } else {
#pragma unroll 1
      for (int j = (pl - 4) * 11 + tt - 16; j < 320; j += NOUT) {
        flags32_wait(wv_, ws, kqf_off(layer, j / 5, 32));
        job_out(wv_, ws, rs, smem, pool.ysel, layer, j / 5, j % 5);
        dep_signal(wv_, ws, cnt_off(layer, CNT_OUT, j / 5));
      }
    }
    RSTAMP_(3)
    PH((tt >= 27 ? 6 : 5) + layer * 6)
  }
}

extern "C" void kernel_launch(void* const* d_in, const int* in_sizes, int n_in, void* d_out, int out_size, void* d_ws, size_t ws_size,
                              hipStream_t stream) {
  const u16* x = (const u16*)d_in[0];
  const u16* mem0 = (const u16*)d_in[1];
  const u16* Wq = (const u16*)d_in[2];
  const u16* Wk = (const u16*)d_in[3];
  const u16* Wv = (const u16*)d_in[4];
  const u16* Wo = (const u16*)d_in[5];
  const u16* wr = (const u16*)d_in[8];
  const u16* wi = (const u16*)d_in[9];
  unsigned char* ws = (unsigned char*)d_ws;
  u16* out = (u16*)d_out;
  (void)hipFuncSetAttribute((const void*)k_main, hipFuncAttributeMaxDynamicSharedMemorySize, MAIN_LDS);
  (void)hipFuncSetAttribute((const void*)k_fin, hipFuncAttributeMaxDynamicSharedMemorySize, FIN_LDS);
  k_main<<<256, 256, MAIN_LDS, stream>>>(ws, x, mem0, Wq, Wk, Wv, Wo, wr, wi, 2);
  k_fin<<<NCOL + 320, 256, FIN_LDS, stream>>>(ws, out);
}
```

```cpp
#include <hip/hip_runtime.h>
#include <hip/hip_bf16.h>
#include <stdint.h>

typedef unsigned v4u __attribute__((ext_vector_type(4)));
typedef unsigned v2u __attribute__((ext_vector_type(2)));
typedef float f32x4 __attribute__((ext_vector_type(4)));
typedef float f32x2 __attribute__((ext_vector_type(2)));
typedef __bf16 bf16x8 __attribute__((ext_vector_type(8)));
typedef __bf16 bf16x2 __attribute__((ext_vector_type(2)));
typedef unsigned short u16;

constexpr int S_ = 2048, D_ = 640, NSEG = 64, KT = 20, RT = 128;
constexpr int NFT = 40;
constexpr int NFK = 20;
constexpr int NCOL = 161;
constexpr int WIN = 32768, REG = 16 * WIN;
constexpr int W_MEM = 10 * WIN, W_MISC = 15 * WIN;
__host__ __device__ constexpr int copy_off(int c) { return (c & 3) * 2048 + (c >> 2) * 16384; }
constexpr int M_STAMP = W_MISC + copy_off(4), M_TM = M_STAMP + 40 * 64, M_CAL = W_MISC + copy_off(3);
constexpr int PAGE = 4 * REG, NYP = 22, P_Y = NSEG * REG;
constexpr size_t SZ_POOL = (size_t)P_Y + (size_t)(NYP + 1) * PAGE;

constexpr size_t OFF_CNT = 0;
constexpr int CNT_IDFT = 0, CNT_OUT = 1;
constexpr size_t OFF_PROG = OFF_CNT + (size_t)3 * 32 * 128;
constexpr size_t OFF_KQF = OFF_PROG + (size_t)NSEG * 4096;
#define FNAP 6
#define FSP 16
#define FNG 32
constexpr int FAN_G = (161 * FSP + 127) / 128 * 128 > 3840 ? (161 * FSP + 127) / 128 * 128 : 3840;
constexpr size_t OFF_FAN = OFF_KQF + (size_t)NSEG * 4096;
constexpr size_t HO_LAYER = OFF_FAN + (size_t)2 * FNG * FAN_G;
constexpr size_t OFF_HO = SZ_POOL;
constexpr size_t HO_FIRST = OFF_FAN + (size_t)FNG * FAN_G;
constexpr size_t OFF_HS = OFF_HO + 2 * HO_LAYER;
constexpr int HS_GO = (int)OFF_HS, HS_PAY = HS_GO + 4096, HS_ASK = HS_PAY + 1024, HS_HI = HS_ASK + 2048, HS_CAL = HS_HI + 2048;
__host__ __device__ constexpr int prog_off(int layer, int rb, int slot) { return (int)(OFF_HO + layer * HO_LAYER + OFF_PROG) + (rb * 64 + slot) * 64; }
__host__ __device__ constexpr int kqf_off(int layer, int sg, int h) { return (int)(OFF_HO + layer * HO_LAYER + OFF_KQF) + (sg * 64 + h) * 64; }
__host__ __device__ constexpr int cnt_off(int layer, int kind, int rb) { return (int)(OFF_HO + layer * HO_LAYER + OFF_CNT) + (kind * 32 + rb) * 128; }
__host__ __device__ constexpr int fan_off(int layer, int set, int i) { return (int)(OFF_HO + layer * HO_LAYER + OFF_FAN) + ((set ^ layer ^ 1) * FNG + i) * FAN_G; }
constexpr size_t OFF_XR = (OFF_HS + 65536 + 4095) / 4096 * 4096;
constexpr size_t OFF_ST = OFF_XR + (size_t)RT * KT * 1024;
constexpr size_t OFF_QF = OFF_ST + (size_t)S_ * 48;
constexpr size_t OFF_KF = OFF_QF + (size_t)32 * RT * 4 * 1024;
constexpr size_t OFF_UF = OFF_KF + (size_t)32 * RT * 4 * 1024;
constexpr size_t OFF_PXF = OFF_UF + (size_t)NSEG * 80 * 16 * 1024;
constexpr size_t OFF_PXM = OFF_PXF + (size_t)NSEG * 32 * 2 * 1024;
constexpr size_t OFF_INV = OFF_PXM + (size_t)NSEG * 8 * 2 * 1024;
constexpr size_t OFF_X1T = OFF_INV + (size_t)20 * NFK * 1024;
#define ZPAD 32
#define XPAD 32
constexpr int XS = S_ + XPAD, ZS = S_ + ZPAD;
constexpr size_t OFF_ZT = OFF_X1T + (size_t)160 * XS * 8;
constexpr size_t OFF_WT = OFF_ZT + (size_t)161 * ZS * 8;
constexpr size_t OFF_DBG = OFF_WT + (size_t)321 * S_ * 4;
constexpr size_t OFF_END = OFF_DBG + (size_t)1048576;
static_assert(OFF_END <= ((size_t)256 << 20), "workspace too large");

constexpr int AUX_SC1 = 16;
#define PH(k)

__device__ __forceinline__ f32x4 mfma(v4u a, v4u b, f32x4 c) {
  return __builtin_amdgcn_mfma_f32_16x16x32_bf16(__builtin_bit_cast(bf16x8, a), __builtin_bit_cast(bf16x8, b), c, 0, 0, 0);
}
__device__ __forceinline__ unsigned pk(float a, float b) {
  f32x2 v = {a, b};
  return __builtin_bit_cast(unsigned, __builtin_convertvector(v, bf16x2));
}
__device__ __forceinline__ float lo_f(unsigned u) { return __builtin_bit_cast(float, u << 16); }
__device__ __forceinline__ float hi_f(unsigned u) { return __builtin_bit_cast(float, u & 0xFFFF0000u); }
__device__ __forceinline__ float bfr(float x) { return lo_f(pk(x, x)); }
__device__ __forceinline__ v4u pk8(f32x4 a, f32x4 b) { return v4u{pk(a.x, a.y), pk(a.z, a.w), pk(b.x, b.y), pk(b.z, b.w)}; }
__device__ __forceinline__ int sigma(int g, int jj) { return 16 * (jj >> 2) + 4 * g + (jj & 3); }
__device__ __forceinline__ int colmap(int c8, int jj) {
  const int p = c8 >> 3, ga = (c8 >> 1) & 3, half = c8 & 1;
  return 32 * (2 * p + (ga >> 1)) + 16 * ((jj >> 1) & 1) + 4 * (2 * (ga & 1) + (jj >> 2)) + 2 * half + (jj & 1);
}
__device__ __forceinline__ int pool_base(const void* ws) { return (int)((0u - (unsigned)(uintptr_t)ws) & (unsigned)(PAGE - 1)); }
__device__ __forceinline__ unsigned xcc_id() { return (unsigned)__builtin_amdgcn_s_getreg(20 | (3 << 11)) & 7u; }
__device__ __forceinline__ float shx(float v, int m) { return __shfl_xor(v, m); }
__device__ __forceinline__ unsigned nosent(unsigned x) { return x < 0xFFFFFFFEu ? x : 0xFFFFFFFEu; }

__device__ __forceinline__ f32x2 frag_stat(v4u c) {
  const float v[8] = {lo_f(c.x), hi_f(c.x), lo_f(c.y), hi_f(c.y), lo_f(c.z), hi_f(c.z), lo_f(c.w), hi_f(c.w)};
  float q = 0.f;
#pragma unroll
  for (int j = 0; j < 8; ++j) q = __builtin_fmaf(v[j], v[j], q);
  return f32x2{((v[0] + v[1]) + (v[2] + v[3])) + ((v[4] + v[5]) + (v[6] + v[7])), q};
}
__device__ __forceinline__ f32x2 rows_sum(f32x2 a) {
  a.x += shx(a.x, 16); a.y += shx(a.y, 16); a.x += shx(a.x, 32); a.y += shx(a.y, 32);
  return a;
}

typedef int v4i __attribute__((ext_vector_type(4)));
__device__ __forceinline__ int y_region(unsigned long long sel, int s) { return (s * 80 + (s * 43 >> 7) * 16 + (int)((sel >> s) & 1ull)) * 8192; }

__device__ __forceinline__ int lane_id() {
  int l;
  asm volatile("v_mbcnt_lo_u32_b32 %0, -1, 0\n\tv_mbcnt_hi_u32_b32 %0, -1, %0" : "=v"(l));
  return l;
}
#define TIDX (wv_ * 64 + lane_id())
typedef __amdgpu_buffer_rsrc_t rsrc_t;
__device__ __forceinline__ rsrc_t ws_rsrc(unsigned char* ws) { return __builtin_amdgcn_make_buffer_rsrc(ws, 0, (int)OFF_END, 0x00020000); }
__device__ __forceinline__ v4u ldc(rsrc_t rs, int off) { return __builtin_amdgcn_raw_buffer_load_b128(rs, off, 0, AUX_SC1); }
__device__ __forceinline__ void stc(rsrc_t rs, int off, v4u v) { __builtin_amdgcn_raw_buffer_store_b128(v, rs, off, 0, AUX_SC1); }

extern __shared__ __attribute__((aligned(16))) unsigned char dyn_lds[];
__device__ __forceinline__ unsigned lds_base() { return (unsigned)(uintptr_t)(__attribute__((address_space(3))) unsigned char*)dyn_lds; }
__device__ __forceinline__ unsigned rtc() { return (unsigned)__builtin_amdgcn_s_memrealtime(); }
__device__ __forceinline__ int wave_imax(int v) {
  asm volatile("s_nop 1\n\tv_max_i32_dpp %0, %0, %0 quad_perm:[1,0,3,2] row_mask:0xf bank_mask:0xf\n\t"
               "s_nop 1\n\tv_max_i32_dpp %0, %0, %0 quad_perm:[2,3,0,1] row_mask:0xf bank_mask:0xf\n\t"
               "s_nop 1\n\tv_max_i32_dpp %0, %0, %0 row_half_mirror row_mask:0xf bank_mask:0xf\n\t"
               "s_nop 1\n\tv_max_i32_dpp %0, %0, %0 row_mirror row_mask:0xf bank_mask:0xf\n\ts_nop 0"
               : "+v"(v));
  const int a = __builtin_amdgcn_readlane(v, 0), b = __builtin_amdgcn_readlane(v, 16), c = __builtin_amdgcn_readlane(v, 32),
            d = __builtin_amdgcn_readlane(v, 48);
  return max(max(a, b), max(c, d));
}

typedef __attribute__((address_space(1))) unsigned gu32;
__device__ __forceinline__ void prog_prewait(int wv_, unsigned char* ws, int off) {
  if (TIDX == 0) {
    gu32* p = (gu32*)(ws + off);
    for (unsigned spins = 0; spins < (1u << 16); ++spins) {
      if (__hip_atomic_load(p, __ATOMIC_RELAXED, __HIP_MEMORY_SCOPE_AGENT) != 0xFFFFFFFFu) break;
      __builtin_amdgcn_s_sleep(48);
    }
  }
}
__device__ __forceinline__ void prog_wait(int wv_, unsigned char* ws, int off) {
  if (wv_ == 0) {
    const int l = lane_id();
    gu32* p0 = (gu32*)(ws + off + l * 64);
    for (unsigned spins = 0; spins < (1u << 17); ++spins) {
      const unsigned a = __hip_atomic_load(p0, __ATOMIC_RELAXED, __HIP_MEMORY_SCOPE_AGENT);
      if (!__any(a == 0xFFFFFFFFu)) break;
      __builtin_amdgcn_s_sleep(8);
    }
  }
  __syncthreads();
}
__device__ __forceinline__ void flags32_wait(int wv_, unsigned char* ws, int off) {
  if (wv_ == 0) {
    const int l = lane_id();
    __attribute__((address_space(1))) unsigned long long* p0 = (__attribute__((address_space(1))) unsigned long long*)(ws + off + (l & 31) * 64 + (l >> 5) * 8);
    for (unsigned spins = 0; spins < (1u << 17); ++spins) {
      const unsigned long long v = __hip_atomic_load(p0, __ATOMIC_RELAXED, __HIP_MEMORY_SCOPE_AGENT);
      if (!__any(max((unsigned)v, (unsigned)(v >> 32)) == 0xFFFFFFFFu)) break;
      __builtin_amdgcn_s_sleep(16);
    }
  }
  __syncthreads();
}
__device__ __forceinline__ void dep_signal(int wv_, unsigned char* ws, int off, unsigned inc = 1u) {
  asm volatile("s_waitcnt vmcnt(0)" ::: "memory");
  __syncthreads();
  if (TIDX == 0) __hip_atomic_fetch_add((gu32*)(ws + off), inc, __ATOMIC_RELAXED, __HIP_MEMORY_SCOPE_AGENT);
}

template <int SP> __device__ __forceinline__ void fan_signal(int wv_, unsigned char* ws, int fan, int i) {
  asm volatile("s_waitcnt vmcnt(0)" ::: "memory");
  __syncthreads();
  const int l = lane_id();
  if (wv_ == 0 && l < FNG) __hip_atomic_store((gu32*)(ws + fan + l * FAN_G + i * SP), nosent(rtc()), __ATOMIC_RELAXED, __HIP_MEMORY_SCOPE_AGENT);
}
constexpr int L_WHEN = 90112;
template <int SP, int N> __device__ __forceinline__ void fan_wait(int wv_, unsigned char* ws, int fan, int salt) {
  if (wv_ * 64 < N) {
    gu32* p = (gu32*)(ws + fan + (((int)xcc_id() * (FNG / 8) + (salt & (FNG / 8 - 1))) * FAN_G + min(TIDX, N - 1) * SP));
    unsigned v;
    for (unsigned spins = 0; spins < (1u << 17); ++spins) {
      v = __hip_atomic_load(p, __ATOMIC_RELAXED, __HIP_MEMORY_SCOPE_AGENT);
      const unsigned long long m = __ballot(v == 0xFFFFFFFFu);
      if (m == 0) break;
      __builtin_amdgcn_s_sleep(N == NCOL ? 24 : FNAP);
      if (N == 20 && (m & 0x3FF)) __builtin_amdgcn_s_sleep(40);
    }
    if (N == 20) {
      const unsigned t = rtc();
      *(unsigned*)(dyn_lds + L_WHEN) = t + (unsigned)wave_imax((int)(v - t));
    }
  }
  __syncthreads();
}

__device__ __forceinline__ void stat_put(v4u* lds, int wv, int rt, int lane, v4u y) {
  const f32x2 st = rows_sum(frag_stat(y));
  if (lane < 16) ((f32x2*)(lds + 5120))[(wv * 2 + rt) * 16 + lane] = st;
}
__device__ __forceinline__ void stat_store(rsrc_t rs, v4u* lds, int wv, int lane, int sg, int cg) {
  __syncthreads();
  if (wv == 0) {
    const f32x2* sp = (const f32x2*)(lds + 5120) + (lane & 31);
    const f32x2 st = (sp[0] + sp[32]) + (sp[64] + sp[96]);
    __builtin_amdgcn_raw_buffer_store_b64(__builtin_bit_cast(v2u, st), rs, lane < 32 ? (int)OFF_ST + ((sg * 32 + lane) * 6 + cg) * 8 : 0x7FFFFFF0, 0, AUX_SC1);
  }
}

__device__ __forceinline__ void job_out(int wv_, unsigned char* ws, rsrc_t rs, v4u* lds, unsigned long long ysel, int layer, int s, int cg) {
  const int lane = lane_id(), wv = wv_, g = lane >> 4, r = lane & 15;
  const int kt = cg * 4 + wv;
  const int c16 = 2 * kt + (r >> 3), ci = (g * 16 + (r & 4) * 2 + (r & 3)) * 16;
  const int ubase = (int)OFF_UF + (s * 40 + c16) * 32768 + ci;
  const int ybase = pool_base(ws) + P_Y + y_region(ysel, s) + c16 * 16384 + ci;
  v4u z[40][2];
#pragma unroll
  for (int ks = 0; ks < 32; ++ks) { z[ks][0] = ldc(rs, ubase + ks * 1024); z[ks][1] = ldc(rs, ubase + ks * 1024 + 64); }
  const int xo = (int)OFF_XR + ((2 * s * KT + kt) * 64 + lane) * 16;
  const v4u x0 = ldc(rs, xo), x1 = ldc(rs, xo + KT * 1024);
  if (s > 1) prog_prewait(wv_, ws, prog_off(layer, s - 2, (s + cg * 7) & 31));
#define OSTAMP(i)
  OSTAMP(4)
  prog_wait(wv_, ws, prog_off(layer, s, 0));
  OSTAMP(0)
  {
    const unsigned l0 = (unsigned)(uintptr_t)(__attribute__((address_space(3))) v4u*)lds;
    const int vo = lane * 16;
#pragma unroll
    for (int q = 0; q < 5; ++q) {
      const unsigned d = l0 + (q < 4 ? wv * 16384 + q * 4096 : 65536 + wv * 4096);
      const int so = q < 4 ? (int)OFF_PXF + s * 65536 + wv * 16384 + q * 4096 : (int)OFF_PXM + s * 16384 + wv * 4096;
      asm volatile("s_mov_b32 m0, %0\n\ts_nop 0\n\tbuffer_load_dwordx4 %1, %2, %3 offen sc1 lds\n\t"
                   "buffer_load_dwordx4 %1, %2, %3 offen offset:1024 sc1 lds\n\tbuffer_load_dwordx4 %1, %2, %3 offen offset:2048 sc1 lds\n\t"
                   "buffer_load_dwordx4 %1, %2, %3 offen offset:3072 sc1 lds" ::"s"(d), "v"(vo), "s"(rs), "s"(so) : "m0", "memory");
    }
#pragma unroll
    for (int hq = 0; hq < 8; ++hq) { z[32 + hq][0] = ldc(rs, ybase + hq * 1024); z[32 + hq][1] = ldc(rs, ybase + hq * 1024 + 64); }
  }
  f32x4 acc[2][2] = {{{0, 0, 0, 0}, {0, 0, 0, 0}}, {{0, 0, 0, 0}, {0, 0, 0, 0}}};
  asm volatile("s_waitcnt vmcnt(16)\n\ts_barrier" ::: "memory");
  OSTAMP(1)
  v4u pf[2][8];
#pragma unroll
  for (int i = 0; i < 8; ++i) pf[0][i] = lds[i * 64 + lane];
#pragma unroll
  for (int b = 0; b < 10; ++b) {
    if (b < 9) {
#pragma unroll
      for (int i = 0; i < 8; ++i) pf[(b + 1) & 1][i] = lds[((b + 1) * 8 + i) * 64 + lane];
    }
    __builtin_amdgcn_sched_barrier(0);
#pragma unroll
    for (int i = 0; i < 8; ++i) {
      const v4u z0 = z[b * 4 + (i >> 1)][0], z1 = z[b * 4 + (i >> 1)][1];
      acc[0][i & 1] = mfma(b < 8 ? z0 : z0.xzyw, pf[b & 1][i], acc[0][i & 1]);
      acc[1][i & 1] = mfma(b < 8 ? z1 : z1.xzyw, pf[b & 1][i], acc[1][i & 1]);
    }
    __builtin_amdgcn_sched_barrier(0);
  }
#pragma unroll
  for (int qt = 0; qt < 2; ++qt) {
    const v4u x = qt ? x1 : x0;
    const auto s0 = __builtin_amdgcn_permlane16_swap(pk(acc[0][qt].x, acc[0][qt].y), pk(acc[1][qt].x, acc[1][qt].y), false, false);
    const auto s1 = __builtin_amdgcn_permlane16_swap(pk(acc[0][qt].z, acc[0][qt].w), pk(acc[1][qt].z, acc[1][qt].w), false, false);
    v4u y;
    y.x = pk(lo_f(x.x) + lo_f(s0[0]), hi_f(x.x) + hi_f(s0[0]));
    y.y = pk(lo_f(x.y) + lo_f(s0[1]), hi_f(x.y) + hi_f(s0[1]));
    y.z = pk(lo_f(x.z) + lo_f(s1[0]), hi_f(x.z) + hi_f(s1[0]));
    y.w = pk(lo_f(x.w) + lo_f(s1[1]), hi_f(x.w) + hi_f(s1[1]));
    stc(rs, xo + qt * KT * 1024, y);
    stat_put(lds, wv, qt, lane, y);
  }
  OSTAMP(2)
  stat_store(rs, lds, wv, lane, s, cg);
}

typedef unsigned v8u __attribute__((ext_vector_type(8)));
typedef unsigned v16u __attribute__((ext_vector_type(16)));
typedef unsigned v32u __attribute__((ext_vector_type(32)));
__device__ __forceinline__ float cosi(int i) { return __builtin_amdgcn_cosf((float)i * (1.f / 2048)); }
__device__ __forceinline__ float sini(int i) { return __builtin_amdgcn_sinf((float)i * (1.f / 2048)); }
__device__ __forceinline__ void cis8(v32u& d, int x0, int dx, int d4, unsigned m) {
#pragma unroll
  for (int q = 0; q < 4; ++q) {
    const int x = x0 + dx * 2 * (q & 1) + d4 * (q >> 1);
    d[q] = pk(cosi(x), cosi(x + dx)); d[4 + q] = d[q] ^ m;
    d[8 + q] = pk(sini(x), sini(x + dx)); d[12 + q] = d[8 + q] ^ m;
  }
}
#define LDS_V4(a) (*(__attribute__((address_space(3))) v4u*)(uintptr_t)(a))
#define LDS_U32(a) (*(__attribute__((address_space(3))) unsigned*)(uintptr_t)(a))
#define LDS_V2(a) (*(__attribute__((address_space(3))) v2u*)(uintptr_t)(a))
#define COL_ASM \
   "s_waitcnt lgkmcnt(0)\n\t" "s_barrier\n\t" "ds_read_b64_tr_b16 v[136:137], v16 offset:0\n\t" "ds_read_b64_tr_b16 v[138:139], v16 offset:1056\n\t" \
   "ds_read_b64_tr_b16 v[140:141], v16 offset:8448\n\t" "ds_read_b64_tr_b16 v[142:143], v16 offset:9504\n\t" \
   "ds_read_b64_tr_b16 v[144:145], v16 offset:8\n\t" "ds_read_b64_tr_b16 v[146:147], v16 offset:1064\n\t" \
   "ds_read_b64_tr_b16 v[148:149], v16 offset:8456\n\t" "ds_read_b64_tr_b16 v[150:151], v16 offset:9512\n\t" "s_waitcnt lgkmcnt(4)\n\t" \
   "v_mfma_f32_16x16x32_bf16 v[160:163], v[136:139], v[32:35], 0\n\t" "v_mfma_f32_16x16x32_bf16 v[164:167], v[136:139], v[40:43], 0\n\t" \
   "v_mfma_f32_16x16x32_bf16 v[160:163], v[140:143], v[36:39], v[160:163]\n\t" \
   "v_mfma_f32_16x16x32_bf16 v[164:167], v[140:143], v[44:47], v[164:167]\n\t" "ds_read_b64_tr_b16 v[152:153], v16 offset:16\n\t" \
   "ds_read_b64_tr_b16 v[154:155], v16 offset:1072\n\t" "ds_read_b64_tr_b16 v[156:157], v16 offset:8464\n\t" \
   "ds_read_b64_tr_b16 v[158:159], v16 offset:9520\n\t" "s_nop 3\n\t" "s_waitcnt lgkmcnt(4)\n\t" \
   "v_mfma_f32_16x16x32_bf16 v[168:171], v[144:147], v[32:35], 0\n\t" "v_add_f32 v160, v160, v165\n\t" "v_sub_f32 v161, v161, v164\n\t" \
   "v_mfma_f32_16x16x32_bf16 v[172:175], v[144:147], v[40:43], 0\n\t" "v_add_f32 v162, v162, v167\n\t" "v_sub_f32 v163, v163, v166\n\t" \
   "v_mfma_f32_16x16x32_bf16 v[168:171], v[148:151], v[36:39], v[168:171]\n\t" "v_mul_f32 v24, v161, v49\n\t" "v_mul_f32 v25, v161, v48\n\t" \
   "v_mfma_f32_16x16x32_bf16 v[172:175], v[148:151], v[44:47], v[172:175]\n\t" "v_mul_f32 v26, v163, v49\n\t" "v_mul_f32 v27, v163, v48\n\t" \
   "v_fma_f32 v177, v160, v49, v25\n\t" "v_fma_f32 v176, v160, v48, -v24\n\t" "v_fma_f32 v179, v162, v49, v27\n\t" \
   "v_fma_f32 v178, v162, v48, -v26\n\t" "ds_read_b64_tr_b16 v[136:137], v16 offset:24\n\t" "ds_read_b64_tr_b16 v[138:139], v16 offset:1080\n\t" \
   "ds_read_b64_tr_b16 v[140:141], v16 offset:8472\n\t" "ds_read_b64_tr_b16 v[142:143], v16 offset:9528\n\t" "s_waitcnt lgkmcnt(4)\n\t" \
   "v_mfma_f32_16x16x32_bf16 v[160:163], v[152:155], v[32:35], 0\n\t" "v_add_f32 v168, v168, v173\n\t" "v_sub_f32 v169, v169, v172\n\t" \
   "v_mfma_f32_16x16x32_bf16 v[164:167], v[152:155], v[40:43], 0\n\t" "v_add_f32 v170, v170, v175\n\t" "v_sub_f32 v171, v171, v174\n\t" \
   "v_mfma_f32_16x16x32_bf16 v[160:163], v[156:159], v[36:39], v[160:163]\n\t" "v_mul_f32 v24, v169, v51\n\t" "v_mul_f32 v25, v169, v50\n\t" \
   "v_mfma_f32_16x16x32_bf16 v[164:167], v[156:159], v[44:47], v[164:167]\n\t" "v_mul_f32 v26, v171, v51\n\t" "v_mul_f32 v27, v171, v50\n\t" \
   "v_fma_f32 v181, v168, v51, v25\n\t" "v_fma_f32 v180, v168, v50, -v24\n\t" "v_fma_f32 v183, v170, v51, v27\n\t" \
   "v_fma_f32 v182, v170, v50, -v26\n\t" "v_cvt_pk_bf16_f32 v12, v177, v181\n\t" "v_cvt_pk_bf16_f32 v8, v176, v180\n\t" \
   "v_cvt_pk_bf16_f32 v188, v179, v183\n\t" "v_cvt_pk_bf16_f32 v184, v178, v182\n\t" "ds_read_b64_tr_b16 v[144:145], v16 offset:128\n\t" \
   "ds_read_b64_tr_b16 v[146:147], v16 offset:1184\n\t" "ds_read_b64_tr_b16 v[148:149], v16 offset:8576\n\t" \
   "ds_read_b64_tr_b16 v[150:151], v16 offset:9632\n\t" "s_waitcnt lgkmcnt(4)\n\t" "v_mfma_f32_16x16x32_bf16 v[168:171], v[136:139], v[32:35], 0\n\t" \
   "v_add_f32 v160, v160, v165\n\t" "v_sub_f32 v161, v161, v164\n\t" "v_mfma_f32_16x16x32_bf16 v[172:175], v[136:139], v[40:43], 0\n\t" \
   "v_add_f32 v162, v162, v167\n\t" "v_sub_f32 v163, v163, v166\n\t" "v_mfma_f32_16x16x32_bf16 v[168:171], v[140:143], v[36:39], v[168:171]\n\t" \
   "v_mul_f32 v24, v161, v53\n\t" "v_mul_f32 v25, v161, v52\n\t" "v_mfma_f32_16x16x32_bf16 v[172:175], v[140:143], v[44:47], v[172:175]\n\t" \
   "v_mul_f32 v26, v163, v53\n\t" "v_mul_f32 v27, v163, v52\n\t" "v_fma_f32 v177, v160, v53, v25\n\t" "v_fma_f32 v176, v160, v52, -v24\n\t" \
   "v_fma_f32 v179, v162, v53, v27\n\t" "v_fma_f32 v178, v162, v52, -v26\n\t" "ds_read_b64_tr_b16 v[152:153], v16 offset:136\n\t" \
   "ds_read_b64_tr_b16 v[154:155], v16 offset:1192\n\t" "ds_read_b64_tr_b16 v[156:157], v16 offset:8584\n\t" \
   "ds_read_b64_tr_b16 v[158:159], v16 offset:9640\n\t" "s_waitcnt lgkmcnt(4)\n\t" "v_mfma_f32_16x16x32_bf16 v[160:163], v[144:147], v[32:35], 0\n\t" \
   "v_add_f32 v168, v168, v173\n\t" "v_sub_f32 v169, v169, v172\n\t" "v_mfma_f32_16x16x32_bf16 v[164:167], v[144:147], v[40:43], 0\n\t" \
   "v_add_f32 v170, v170, v175\n\t" "v_sub_f32 v171, v171, v174\n\t" "v_mfma_f32_16x16x32_bf16 v[160:163], v[148:151], v[36:39], v[160:163]\n\t" \
   "v_mul_f32 v24, v169, v55\n\t" "v_mul_f32 v25, v169, v54\n\t" "v_mfma_f32_16x16x32_bf16 v[164:167], v[148:151], v[44:47], v[164:167]\n\t" \
   "v_mul_f32 v26, v171, v55\n\t" "v_mul_f32 v27, v171, v54\n\t" "v_fma_f32 v181, v168, v55, v25\n\t" "v_fma_f32 v180, v168, v54, -v24\n\t" \
   "v_fma_f32 v183, v170, v55, v27\n\t" "v_fma_f32 v182, v170, v54, -v26\n\t" "v_cvt_pk_bf16_f32 v13, v177, v181\n\t" \
   "v_cvt_pk_bf16_f32 v9, v176, v180\n\t" "v_cvt_pk_bf16_f32 v189, v179, v183\n\t" "v_cvt_pk_bf16_f32 v185, v178, v182\n\t" \
   "ds_read_b64_tr_b16 v[136:137], v16 offset:144\n\t" "ds_read_b64_tr_b16 v[138:139], v16 offset:1200\n\t" \
   "ds_read_b64_tr_b16 v[140:141], v16 offset:8592\n\t" "ds_read_b64_tr_b16 v[142:143], v16 offset:9648\n\t" "s_waitcnt lgkmcnt(4)\n\t" \
   "v_mfma_f32_16x16x32_bf16 v[168:171], v[152:155], v[32:35], 0\n\t" "v_add_f32 v160, v160, v165\n\t" "v_sub_f32 v161, v161, v164\n\t" \
   "v_mfma_f32_16x16x32_bf16 v[172:175], v[152:155], v[40:43], 0\n\t" "v_add_f32 v162, v162, v167\n\t" "v_sub_f32 v163, v163, v166\n\t" \
   "v_mfma_f32_16x16x32_bf16 v[168:171], v[156:159], v[36:39], v[168:171]\n\t" "v_mul_f32 v24, v161, v57\n\t" "v_mul_f32 v25, v161, v56\n\t" \
   "v_mfma_f32_16x16x32_bf16 v[172:175], v[156:159], v[44:47], v[172:175]\n\t" "v_mul_f32 v26, v163, v57\n\t" "v_mul_f32 v27, v163, v56\n\t" \
   "v_fma_f32 v177, v160, v57, v25\n\t" "v_fma_f32 v176, v160, v56, -v24\n\t" "v_fma_f32 v179, v162, v57, v27\n\t" \
   "v_fma_f32 v178, v162, v56, -v26\n\t" "ds_read_b64_tr_b16 v[144:145], v16 offset:152\n\t" "ds_read_b64_tr_b16 v[146:147], v16 offset:1208\n\t" \
   "ds_read_b64_tr_b16 v[148:149], v16 offset:8600\n\t" "ds_read_b64_tr_b16 v[150:151], v16 offset:9656\n\t" "s_waitcnt lgkmcnt(4)\n\t" \
   "v_mfma_f32_16x16x32_bf16 v[160:163], v[136:139], v[32:35], 0\n\t" "v_add_f32 v168, v168, v173\n\t" "v_sub_f32 v169, v169, v172\n\t" \
   "v_mfma_f32_16x16x32_bf16 v[164:167], v[136:139], v[40:43], 0\n\t" "v_add_f32 v170, v170, v175\n\t" "v_sub_f32 v171, v171, v174\n\t" \
   "v_mfma_f32_16x16x32_bf16 v[160:163], v[140:143], v[36:39], v[160:163]\n\t" "v_mul_f32 v24, v169, v59\n\t" "v_mul_f32 v25, v169, v58\n\t" \
   "v_mfma_f32_16x16x32_bf16 v[164:167], v[140:143], v[44:47], v[164:167]\n\t" "v_mul_f32 v26, v171, v59\n\t" "v_mul_f32 v27, v171, v58\n\t" \
   "v_fma_f32 v181, v168, v59, v25\n\t" "v_fma_f32 v180, v168, v58, -v24\n\t" "v_fma_f32 v183, v170, v59, v27\n\t" \
   "v_fma_f32 v182, v170, v58, -v26\n\t" "v_cvt_pk_bf16_f32 v14, v177, v181\n\t" "v_cvt_pk_bf16_f32 v10, v176, v180\n\t" \
   "v_cvt_pk_bf16_f32 v190, v179, v183\n\t" "v_cvt_pk_bf16_f32 v186, v178, v182\n\t" "s_nop 1\n\t" "s_waitcnt lgkmcnt(0)\n\t" \
   "v_mfma_f32_16x16x32_bf16 v[168:171], v[144:147], v[32:35], 0\n\t" "v_add_f32 v160, v160, v165\n\t" "v_sub_f32 v161, v161, v164\n\t" \
   "v_mfma_f32_16x16x32_bf16 v[172:175], v[144:147], v[40:43], 0\n\t" "v_add_f32 v162, v162, v167\n\t" "v_sub_f32 v163, v163, v166\n\t" \
   "v_mfma_f32_16x16x32_bf16 v[168:171], v[148:151], v[36:39], v[168:171]\n\t" "v_mul_f32 v24, v161, v61\n\t" "v_mul_f32 v25, v161, v60\n\t" \
   "v_mfma_f32_16x16x32_bf16 v[172:175], v[148:151], v[44:47], v[172:175]\n\t" "v_mul_f32 v26, v163, v61\n\t" "v_mul_f32 v27, v163, v60\n\t" \
   "v_fma_f32 v177, v160, v61, v25\n\t" "v_fma_f32 v176, v160, v60, -v24\n\t" "v_fma_f32 v179, v162, v61, v27\n\t" \
   "v_fma_f32 v178, v162, v60, -v26\n\t" "s_nop 1\n\t" "v_add_f32 v168, v168, v173\n\t" "v_sub_f32 v169, v169, v172\n\t" "v_mul_f32 v24, v169, v63\n\t" \
   "v_mul_f32 v25, v169, v62\n\t" "v_fma_f32 v181, v168, v63, v25\n\t" "v_fma_f32 v180, v168, v62, -v24\n\t" "v_cvt_pk_bf16_f32 v15, v177, v181\n\t" \
   "v_cvt_pk_bf16_f32 v11, v176, v180\n\t" "s_nop 1\n\t" "v_mfma_f32_16x16x32_bf16 v[160:163], v[64:67], v[8:11], 0\n\t" \
   "v_add_f32 v170, v170, v175\n\t" "v_sub_f32 v171, v171, v174\n\t" "v_mfma_f32_16x16x32_bf16 v[164:167], v[64:67], v[12:15], 0\n\t" \
   "v_mul_f32 v24, v171, v63\n\t" "v_mfma_f32_16x16x32_bf16 v[160:163], v[72:75], v[12:15], v[160:163]\n\t" "v_mul_f32 v25, v171, v62\n\t" \
   "v_mfma_f32_16x16x32_bf16 v[164:167], v[128:131], v[8:11], v[164:167]\n\t" "v_fma_f32 v183, v170, v63, v25\n\t" \
   "v_fma_f32 v182, v170, v62, -v24\n\t" "v_cvt_pk_bf16_f32 v191, v179, v183\n\t" "v_cvt_pk_bf16_f32 v187, v178, v182\n\t" "s_nop 4\n\t" \
   "v_mfma_f32_16x16x32_bf16 v[168:171], v[68:71], v[8:11], 0\n\t" "v_mul_f32 v24, v164, v97\n\t" "v_mul_f32 v25, v164, v96\n\t" \
   "v_mul_f32 v26, v165, v99\n\t" "v_mfma_f32_16x16x32_bf16 v[172:175], v[68:71], v[12:15], 0\n\t" "v_mul_f32 v27, v165, v98\n\t" \
   "v_mul_f32 v28, v166, v101\n\t" "v_mul_f32 v29, v166, v100\n\t" "v_mfma_f32_16x16x32_bf16 v[168:171], v[76:79], v[12:15], v[168:171]\n\t" \
   "v_mul_f32 v30, v167, v103\n\t" "v_mul_f32 v31, v167, v102\n\t" "v_fma_f32 v164, v160, v97, v25\n\t" \
   "v_mfma_f32_16x16x32_bf16 v[172:175], v[132:135], v[8:11], v[172:175]\n\t" "v_fma_f32 v160, v160, v96, -v24\n\t" \
   "v_fma_f32 v165, v161, v99, v27\n\t" "v_fma_f32 v161, v161, v98, -v26\n\t" "v_fma_f32 v166, v162, v101, v29\n\t" \
   "v_fma_f32 v162, v162, v100, -v28\n\t" "v_fma_f32 v167, v163, v103, v31\n\t" "v_fma_f32 v163, v163, v102, -v30\n\t" \
   "v_cvt_pk_bf16_f32 v140, v164, v165\n\t" "v_cvt_pk_bf16_f32 v136, v160, v161\n\t" "v_cvt_pk_bf16_f32 v141, v166, v167\n\t" \
   "v_cvt_pk_bf16_f32 v137, v162, v163\n\t" "v_mfma_f32_16x16x32_bf16 v[160:163], v[64:67], v[184:187], 0\n\t" "v_mul_f32 v24, v172, v105\n\t" \
   "v_mul_f32 v25, v172, v104\n\t" "v_mul_f32 v26, v173, v107\n\t" "v_mfma_f32_16x16x32_bf16 v[164:167], v[64:67], v[188:191], 0\n\t" \
   "v_mul_f32 v27, v173, v106\n\t" "v_mul_f32 v28, v174, v109\n\t" "v_mul_f32 v29, v174, v108\n\t" \
   "v_mfma_f32_16x16x32_bf16 v[160:163], v[72:75], v[188:191], v[160:163]\n\t" "v_mul_f32 v30, v175, v111\n\t" "v_mul_f32 v31, v175, v110\n\t" \
   "v_fma_f32 v172, v168, v105, v25\n\t" "v_mfma_f32_16x16x32_bf16 v[164:167], v[128:131], v[184:187], v[164:167]\n\t" \
   "v_fma_f32 v168, v168, v104, -v24\n\t" "v_fma_f32 v173, v169, v107, v27\n\t" "v_fma_f32 v169, v169, v106, -v26\n\t" \
   "v_fma_f32 v174, v170, v109, v29\n\t" "v_fma_f32 v170, v170, v108, -v28\n\t" "v_fma_f32 v175, v171, v111, v31\n\t" \
   "v_fma_f32 v171, v171, v110, -v30\n\t" "v_cvt_pk_bf16_f32 v142, v172, v173\n\t" "v_cvt_pk_bf16_f32 v138, v168, v169\n\t" \
   "v_cvt_pk_bf16_f32 v143, v174, v175\n\t" "v_cvt_pk_bf16_f32 v139, v170, v171\n\t" "v_mfma_f32_16x16x32_bf16 v[168:171], v[68:71], v[184:187], 0\n\t" \
   "v_mul_f32 v24, v164, v113\n\t" "v_mul_f32 v25, v164, v112\n\t" "v_mul_f32 v26, v165, v115\n\t" \
   "v_mfma_f32_16x16x32_bf16 v[172:175], v[68:71], v[188:191], 0\n\t" "v_mul_f32 v27, v165, v114\n\t" "v_mul_f32 v28, v166, v117\n\t" \
   "v_mul_f32 v29, v166, v116\n\t" "v_mfma_f32_16x16x32_bf16 v[168:171], v[76:79], v[188:191], v[168:171]\n\t" "v_mul_f32 v30, v167, v119\n\t" \
   "v_mul_f32 v31, v167, v118\n\t" "v_fma_f32 v164, v160, v113, v25\n\t" "v_mfma_f32_16x16x32_bf16 v[172:175], v[132:135], v[184:187], v[172:175]\n\t" \
   "v_fma_f32 v160, v160, v112, -v24\n\t" "v_fma_f32 v165, v161, v115, v27\n\t" "v_fma_f32 v161, v161, v114, -v26\n\t" \
   "v_fma_f32 v166, v162, v117, v29\n\t" "v_fma_f32 v162, v162, v116, -v28\n\t" "v_fma_f32 v167, v163, v119, v31\n\t" \
   "v_fma_f32 v163, v163, v118, -v30\n\t" "v_cvt_pk_bf16_f32 v148, v164, v165\n\t" "v_cvt_pk_bf16_f32 v144, v160, v161\n\t" \
   "v_cvt_pk_bf16_f32 v149, v166, v167\n\t" "v_cvt_pk_bf16_f32 v145, v162, v163\n\t" "v_mfma_f32_16x16x32_bf16 v[160:163], v[136:139], v[64:67], 0\n\t" \
   "v_mul_f32 v24, v172, v121\n\t" "v_mul_f32 v25, v172, v120\n\t" "v_mul_f32 v26, v173, v123\n\t" \
   "v_mfma_f32_16x16x32_bf16 v[164:167], v[140:143], v[64:67], 0\n\t" "v_mul_f32 v27, v173, v122\n\t" "v_mul_f32 v28, v174, v125\n\t" \
   "v_mul_f32 v29, v174, v124\n\t" "v_mfma_f32_16x16x32_bf16 v[160:163], v[140:143], v[128:131], v[160:163]\n\t" "v_mul_f32 v30, v175, v127\n\t" \
   "v_mul_f32 v31, v175, v126\n\t" "v_fma_f32 v172, v168, v121, v25\n\t" "v_mfma_f32_16x16x32_bf16 v[164:167], v[136:139], v[72:75], v[164:167]\n\t" \
   "v_fma_f32 v168, v168, v120, -v24\n\t" "v_fma_f32 v173, v169, v123, v27\n\t" "v_fma_f32 v169, v169, v122, -v26\n\t" \
   "v_fma_f32 v174, v170, v125, v29\n\t" "v_fma_f32 v170, v170, v124, -v28\n\t" "v_fma_f32 v175, v171, v127, v31\n\t" \
   "v_fma_f32 v171, v171, v126, -v30\n\t" "v_cvt_pk_bf16_f32 v150, v172, v173\n\t" "v_cvt_pk_bf16_f32 v146, v168, v169\n\t" \
   "v_cvt_pk_bf16_f32 v151, v174, v175\n\t" "v_cvt_pk_bf16_f32 v147, v170, v171\n\t" "v_mfma_f32_16x16x32_bf16 v[168:171], v[136:139], v[68:71], 0\n\t" \
   "v_mul_f32 v24, v164, v81\n\t" "v_mul_f32 v25, v164, v80\n\t" "v_mul_f32 v26, v165, v83\n\t" \
   "v_mfma_f32_16x16x32_bf16 v[172:175], v[140:143], v[68:71], 0\n\t" "v_mul_f32 v27, v165, v82\n\t" "v_mul_f32 v28, v166, v85\n\t" \
   "v_mul_f32 v29, v166, v84\n\t" "v_mfma_f32_16x16x32_bf16 v[168:171], v[140:143], v[132:135], v[168:171]\n\t" "v_mul_f32 v30, v167, v87\n\t" \
   "v_mul_f32 v31, v167, v86\n\t" "v_fma_f32 v164, v160, v81, v25\n\t" "v_mfma_f32_16x16x32_bf16 v[172:175], v[136:139], v[76:79], v[172:175]\n\t" \
   "v_fma_f32 v160, v160, v80, -v24\n\t" "v_fma_f32 v165, v161, v83, v27\n\t" "v_fma_f32 v161, v161, v82, -v26\n\t" \
   "v_fma_f32 v166, v162, v85, v29\n\t" "v_fma_f32 v162, v162, v84, -v28\n\t" "v_fma_f32 v167, v163, v87, v31\n\t" \
   "v_fma_f32 v163, v163, v86, -v30\n\t" "v_cvt_pk_bf16_f32 v178, v164, v165\n\t" "v_cvt_pk_bf16_f32 v176, v160, v161\n\t" \
   "v_cvt_pk_bf16_f32 v179, v166, v167\n\t" "v_cvt_pk_bf16_f32 v177, v162, v163\n\t" "ds_write_b64 v17, v[178:179] offset:128\n\t" \
   "ds_write_b64 v17, v[176:177] offset:0\n\t" "v_mfma_f32_16x16x32_bf16 v[160:163], v[144:147], v[64:67], 0\n\t" "v_mul_f32 v24, v172, v89\n\t" \
   "v_mul_f32 v25, v172, v88\n\t" "v_mul_f32 v26, v173, v91\n\t" "v_mfma_f32_16x16x32_bf16 v[164:167], v[148:151], v[64:67], 0\n\t" \
   "v_mul_f32 v27, v173, v90\n\t" "v_mul_f32 v28, v174, v93\n\t" "v_mul_f32 v29, v174, v92\n\t" \
   "v_mfma_f32_16x16x32_bf16 v[160:163], v[148:151], v[128:131], v[160:163]\n\t" "v_mul_f32 v30, v175, v95\n\t" "v_mul_f32 v31, v175, v94\n\t" \
   "v_fma_f32 v172, v168, v89, v25\n\t" "v_mfma_f32_16x16x32_bf16 v[164:167], v[144:147], v[72:75], v[164:167]\n\t" \
   "v_fma_f32 v168, v168, v88, -v24\n\t" "v_fma_f32 v173, v169, v91, v27\n\t" "v_fma_f32 v169, v169, v90, -v26\n\t" \
   "v_fma_f32 v174, v170, v93, v29\n\t" "v_fma_f32 v170, v170, v92, -v28\n\t" "v_fma_f32 v175, v171, v95, v31\n\t" \
   "v_fma_f32 v171, v171, v94, -v30\n\t" "v_cvt_pk_bf16_f32 v182, v172, v173\n\t" "v_cvt_pk_bf16_f32 v180, v168, v169\n\t" \
   "v_cvt_pk_bf16_f32 v183, v174, v175\n\t" "v_cvt_pk_bf16_f32 v181, v170, v171\n\t" "ds_write_b64 v17, v[182:183] offset:8384\n\t" \
   "ds_write_b64 v17, v[180:181] offset:8256\n\t" "v_mfma_f32_16x16x32_bf16 v[168:171], v[144:147], v[68:71], 0\n\t" "v_mul_f32 v24, v164, v81\n\t" \
   "v_mul_f32 v25, v164, v80\n\t" "v_mul_f32 v26, v165, v83\n\t" "v_mfma_f32_16x16x32_bf16 v[172:175], v[148:151], v[68:71], 0\n\t" \
   "v_mul_f32 v27, v165, v82\n\t" "v_mul_f32 v28, v166, v85\n\t" "v_mul_f32 v29, v166, v84\n\t" \
   "v_mfma_f32_16x16x32_bf16 v[168:171], v[148:151], v[132:135], v[168:171]\n\t" "v_mul_f32 v30, v167, v87\n\t" "v_mul_f32 v31, v167, v86\n\t" \
   "v_fma_f32 v164, v160, v81, v25\n\t" "v_mfma_f32_16x16x32_bf16 v[172:175], v[144:147], v[76:79], v[172:175]\n\t" \
   "v_fma_f32 v160, v160, v80, -v24\n\t" "v_fma_f32 v165, v161, v83, v27\n\t" "v_fma_f32 v161, v161, v82, -v26\n\t" \
   "v_fma_f32 v166, v162, v85, v29\n\t" "v_fma_f32 v162, v162, v84, -v28\n\t" "v_fma_f32 v167, v163, v87, v31\n\t" \
   "v_fma_f32 v163, v163, v86, -v30\n\t" "v_cvt_pk_bf16_f32 v178, v164, v165\n\t" "v_cvt_pk_bf16_f32 v176, v160, v161\n\t" \
   "v_cvt_pk_bf16_f32 v179, v166, v167\n\t" "v_cvt_pk_bf16_f32 v177, v162, v163\n\t" "ds_write_b64 v18, v[178:179] offset:128\n\t" \
   "ds_write_b64 v18, v[176:177] offset:0\n\t" "v_mul_f32 v24, v172, v89\n\t" "v_mul_f32 v25, v172, v88\n\t" "v_mul_f32 v26, v173, v91\n\t" \
   "v_mul_f32 v27, v173, v90\n\t" "v_mul_f32 v28, v174, v93\n\t" "v_mul_f32 v29, v174, v92\n\t" "v_mul_f32 v30, v175, v95\n\t" \
   "v_mul_f32 v31, v175, v94\n\t" "v_fma_f32 v172, v168, v89, v25\n\t" "v_fma_f32 v168, v168, v88, -v24\n\t" "v_fma_f32 v173, v169, v91, v27\n\t" \
   "v_fma_f32 v169, v169, v90, -v26\n\t" "v_fma_f32 v174, v170, v93, v29\n\t" "v_fma_f32 v170, v170, v92, -v28\n\t" \
   "v_fma_f32 v175, v171, v95, v31\n\t" "v_fma_f32 v171, v171, v94, -v30\n\t" "v_cvt_pk_bf16_f32 v182, v172, v173\n\t" \
   "v_cvt_pk_bf16_f32 v180, v168, v169\n\t" "v_cvt_pk_bf16_f32 v183, v174, v175\n\t" "v_cvt_pk_bf16_f32 v181, v170, v171\n\t" \
   "ds_write_b64 v18, v[182:183] offset:8384\n\t" "ds_write_b64 v18, v[180:181] offset:8256\n\t" "s_waitcnt lgkmcnt(0)\n\t" "s_barrier\n\t" \
   "ds_read_b128 v[136:139], v19 offset:0\n\t" "ds_read_b128 v[140:143], v20 offset:0\n\t" "ds_read_b128 v[144:147], v19 offset:2064\n\t" \
   "ds_read_b128 v[148:151], v20 offset:2064\n\t" "s_waitcnt lgkmcnt(2)\n\t" "v_mfma_f32_16x16x32_bf16 v[160:163], v[136:139], v[32:35], 0\n\t" \
   "ds_read_b128 v[152:155], v19 offset:4128\n\t" "ds_read_b128 v[156:159], v20 offset:4128\n\t" \
   "v_mfma_f32_16x16x32_bf16 v[164:167], v[136:139], v[40:43], 0\n\t" "v_mfma_f32_16x16x32_bf16 v[160:163], v[140:143], v[36:39], v[160:163]\n\t" \
   "v_mfma_f32_16x16x32_bf16 v[164:167], v[140:143], v[44:47], v[164:167]\n\t" "s_waitcnt lgkmcnt(2)\n\t" \
   "v_mfma_f32_16x16x32_bf16 v[168:171], v[144:147], v[32:35], 0\n\t" "ds_read_b128 v[136:139], v19 offset:6192\n\t" \
   "ds_read_b128 v[140:143], v20 offset:6192\n\t" "v_mfma_f32_16x16x32_bf16 v[172:175], v[144:147], v[40:43], 0\n\t" "v_sub_f32 v160, v160, v165\n\t" \
   "v_add_f32 v161, v161, v164\n\t" "v_sub_f32 v162, v162, v167\n\t" "v_mfma_f32_16x16x32_bf16 v[168:171], v[148:151], v[36:39], v[168:171]\n\t" \
   "v_add_f32 v163, v163, v166\n\t" "v_cvt_pk_bf16_f32 v8, v160, v161\n\t" "v_cvt_pk_bf16_f32 v9, v162, v163\n\t" \
   "v_mfma_f32_16x16x32_bf16 v[172:175], v[148:151], v[44:47], v[172:175]\n\t" "s_waitcnt lgkmcnt(2)\n\t" \
   "v_mfma_f32_16x16x32_bf16 v[160:163], v[152:155], v[32:35], 0\n\t" "ds_read_b128 v[144:147], v19 offset:8256\n\t" \
   "ds_read_b128 v[148:151], v20 offset:8256\n\t" "v_mfma_f32_16x16x32_bf16 v[164:167], v[152:155], v[40:43], 0\n\t" "v_sub_f32 v168, v168, v173\n\t" \
   "v_add_f32 v169, v169, v172\n\t" "v_sub_f32 v170, v170, v175\n\t" "v_mfma_f32_16x16x32_bf16 v[160:163], v[156:159], v[36:39], v[160:163]\n\t" \
   "v_add_f32 v171, v171, v174\n\t" "v_cvt_pk_bf16_f32 v10, v168, v169\n\t" "v_cvt_pk_bf16_f32 v11, v170, v171\n\t" \
   "v_mfma_f32_16x16x32_bf16 v[164:167], v[156:159], v[44:47], v[164:167]\n\t" "buffer_store_dwordx4 v[8:11], v21, %[rs], 0 offen offset:0 sc1\n\t" \
   "s_waitcnt lgkmcnt(2)\n\t" "v_mfma_f32_16x16x32_bf16 v[168:171], v[136:139], v[32:35], 0\n\t" "ds_read_b128 v[152:155], v19 offset:10320\n\t" \
   "ds_read_b128 v[156:159], v20 offset:10320\n\t" "v_mfma_f32_16x16x32_bf16 v[172:175], v[136:139], v[40:43], 0\n\t" "v_sub_f32 v160, v160, v165\n\t" \
   "v_add_f32 v161, v161, v164\n\t" "v_sub_f32 v162, v162, v167\n\t" "v_mfma_f32_16x16x32_bf16 v[168:171], v[140:143], v[36:39], v[168:171]\n\t" \
   "v_add_f32 v163, v163, v166\n\t" "v_cvt_pk_bf16_f32 v12, v160, v161\n\t" "v_cvt_pk_bf16_f32 v13, v162, v163\n\t" \
   "v_mfma_f32_16x16x32_bf16 v[172:175], v[140:143], v[44:47], v[172:175]\n\t" "s_waitcnt lgkmcnt(2)\n\t" \
   "v_mfma_f32_16x16x32_bf16 v[160:163], v[144:147], v[32:35], 0\n\t" "ds_read_b128 v[136:139], v19 offset:12384\n\t" \
   "ds_read_b128 v[140:143], v20 offset:12384\n\t" "v_mfma_f32_16x16x32_bf16 v[164:167], v[144:147], v[40:43], 0\n\t" "v_sub_f32 v168, v168, v173\n\t" \
   "v_add_f32 v169, v169, v172\n\t" "v_sub_f32 v170, v170, v175\n\t" "v_mfma_f32_16x16x32_bf16 v[160:163], v[148:151], v[36:39], v[160:163]\n\t" \
   "v_add_f32 v171, v171, v174\n\t" "v_cvt_pk_bf16_f32 v14, v168, v169\n\t" "v_cvt_pk_bf16_f32 v15, v170, v171\n\t" \
   "v_mfma_f32_16x16x32_bf16 v[164:167], v[148:151], v[44:47], v[164:167]\n\t" "buffer_store_dwordx4 v[12:15], v21, %[rs], 0 offen offset:64 sc1\n\t" \
   "s_waitcnt lgkmcnt(2)\n\t" "v_mfma_f32_16x16x32_bf16 v[168:171], v[152:155], v[32:35], 0\n\t" "ds_read_b128 v[144:147], v19 offset:14448\n\t" \
   "ds_read_b128 v[148:151], v20 offset:14448\n\t" "v_mfma_f32_16x16x32_bf16 v[172:175], v[152:155], v[40:43], 0\n\t" "v_sub_f32 v160, v160, v165\n\t" \
   "v_add_f32 v161, v161, v164\n\t" "v_sub_f32 v162, v162, v167\n\t" "v_mfma_f32_16x16x32_bf16 v[168:171], v[156:159], v[36:39], v[168:171]\n\t" \
   "v_add_f32 v163, v163, v166\n\t" "v_cvt_pk_bf16_f32 v8, v160, v161\n\t" "v_cvt_pk_bf16_f32 v9, v162, v163\n\t" \
   "v_mfma_f32_16x16x32_bf16 v[172:175], v[156:159], v[44:47], v[172:175]\n\t" "s_waitcnt lgkmcnt(2)\n\t" \
   "v_mfma_f32_16x16x32_bf16 v[160:163], v[136:139], v[32:35], 0\n\t" "s_nop 0\n\t" "s_nop 0\n\t" \
   "v_mfma_f32_16x16x32_bf16 v[164:167], v[136:139], v[40:43], 0\n\t" "v_sub_f32 v168, v168, v173\n\t" "v_add_f32 v169, v169, v172\n\t" \
   "v_sub_f32 v170, v170, v175\n\t" "v_mfma_f32_16x16x32_bf16 v[160:163], v[140:143], v[36:39], v[160:163]\n\t" "v_add_f32 v171, v171, v174\n\t" \
   "v_cvt_pk_bf16_f32 v10, v168, v169\n\t" "v_cvt_pk_bf16_f32 v11, v170, v171\n\t" \
   "v_mfma_f32_16x16x32_bf16 v[164:167], v[140:143], v[44:47], v[164:167]\n\t" "buffer_store_dwordx4 v[8:11], v21, %[rs], 0 offen offset:128 sc1\n\t" \
   "s_waitcnt lgkmcnt(0)\n\t" "v_mfma_f32_16x16x32_bf16 v[168:171], v[144:147], v[32:35], 0\n\t" "s_nop 0\n\t" "s_nop 0\n\t" \
   "v_mfma_f32_16x16x32_bf16 v[172:175], v[144:147], v[40:43], 0\n\t" "v_sub_f32 v160, v160, v165\n\t" "v_add_f32 v161, v161, v164\n\t" \
   "v_sub_f32 v162, v162, v167\n\t" "v_mfma_f32_16x16x32_bf16 v[168:171], v[148:151], v[36:39], v[168:171]\n\t" "v_add_f32 v163, v163, v166\n\t" \
   "v_cvt_pk_bf16_f32 v12, v160, v161\n\t" "v_cvt_pk_bf16_f32 v13, v162, v163\n\t" \
   "v_mfma_f32_16x16x32_bf16 v[172:175], v[148:151], v[44:47], v[172:175]\n\t" "s_nop 7\n\t" "v_sub_f32 v168, v168, v173\n\t" \
   "v_add_f32 v169, v169, v172\n\t" "v_sub_f32 v170, v170, v175\n\t" "v_add_f32 v171, v171, v174\n\t" "v_cvt_pk_bf16_f32 v14, v168, v169\n\t" \
   "v_cvt_pk_bf16_f32 v15, v170, v171\n\t" "buffer_store_dwordx4 v[12:15], v21, %[rs], 0 offen offset:192 sc1\n\t" "s_nop 1"
#define COL_CLOB "v8", "v9", "v10", "v11", "v12", "v13", "v14", "v15", "v24", "v25", "v26", "v27", "v28", "v29", "v30", "v31", "v136", "v137", "v138", "v139", "v140", "v141", "v142", "v143", "v144", "v145", "v146", "v147", "v148", "v149", "v150", "v151", "v152", "v153", "v154", "v155", "v156", "v157", "v158", "v159", "v160", "v161", "v162", "v163", "v164", "v165", "v166", "v167", "v168", "v169", "v170", "v171", "v172", "v173", "v174", "v175", "v176", "v177", "v178", "v179", "v180", "v181", "v182", "v183", "v184", "v185", "v186", "v187", "v188", "v189", "v190", "v191"
#define IM_ASM \
   "s_mov_b32 s40, 0\n\t" "buffer_load_dwordx4 a[0:3], %[o0], %[rs], s40 offen offset:0 sc1\n\t" "s_mov_b32 s41, 20480\n\t" \
   "buffer_load_dwordx4 a[80:83], %[o0], %[rs], s41 offen offset:0 sc1\n\t" "s_mov_b32 s42, 40960\n\t" \
   "buffer_load_dwordx4 a[160:163], %[o1], %[rs], s42 offen offset:0 sc1\n\t" "buffer_load_dwordx4 a[4:7], %[o0], %[rs], s40 offen offset:1024 sc1\n\t" \
   "buffer_load_dwordx4 a[84:87], %[o0], %[rs], s41 offen offset:1024 sc1\n\t" \
   "buffer_load_dwordx4 a[164:167], %[o1], %[rs], s42 offen offset:1024 sc1\n\t" \
   "buffer_load_dwordx4 a[8:11], %[o0], %[rs], s40 offen offset:2048 sc1\n\t" \
   "buffer_load_dwordx4 a[88:91], %[o0], %[rs], s41 offen offset:2048 sc1\n\t" \
   "buffer_load_dwordx4 a[168:171], %[o1], %[rs], s42 offen offset:2048 sc1\n\t" \
   "buffer_load_dwordx4 a[12:15], %[o0], %[rs], s40 offen offset:3072 sc1\n\t" \
   "buffer_load_dwordx4 a[92:95], %[o0], %[rs], s41 offen offset:3072 sc1\n\t" \
   "buffer_load_dwordx4 a[172:175], %[o1], %[rs], s42 offen offset:3072 sc1\n\t" "s_mov_b32 s40, 4096\n\t" \
   "buffer_load_dwordx4 a[16:19], %[o0], %[rs], s40 offen offset:0 sc1\n\t" "s_mov_b32 s41, 24576\n\t" \
   "buffer_load_dwordx4 a[96:99], %[o0], %[rs], s41 offen offset:0 sc1\n\t" "s_mov_b32 s42, 45056\n\t" \
   "buffer_load_dwordx4 a[176:179], %[o1], %[rs], s42 offen offset:0 sc1\n\t" \
   "buffer_load_dwordx4 a[20:23], %[o0], %[rs], s40 offen offset:1024 sc1\n\t" \
   "buffer_load_dwordx4 a[100:103], %[o0], %[rs], s41 offen offset:1024 sc1\n\t" \
   "buffer_load_dwordx4 a[180:183], %[o1], %[rs], s42 offen offset:1024 sc1\n\t" \
   "buffer_load_dwordx4 a[24:27], %[o0], %[rs], s40 offen offset:2048 sc1\n\t" \
   "buffer_load_dwordx4 a[104:107], %[o0], %[rs], s41 offen offset:2048 sc1\n\t" \
   "buffer_load_dwordx4 a[184:187], %[o1], %[rs], s42 offen offset:2048 sc1\n\t" \
   "buffer_load_dwordx4 a[28:31], %[o0], %[rs], s40 offen offset:3072 sc1\n\t" \
   "buffer_load_dwordx4 a[108:111], %[o0], %[rs], s41 offen offset:3072 sc1\n\t" \
   "buffer_load_dwordx4 a[188:191], %[o1], %[rs], s42 offen offset:3072 sc1\n\t" "s_mov_b32 s40, 8192\n\t" \
   "buffer_load_dwordx4 a[32:35], %[o0], %[rs], s40 offen offset:0 sc1\n\t" "s_mov_b32 s41, 28672\n\t" \
   "buffer_load_dwordx4 a[112:115], %[o0], %[rs], s41 offen offset:0 sc1\n\t" "s_mov_b32 s42, 49152\n\t" \
   "buffer_load_dwordx4 a[192:195], %[o1], %[rs], s42 offen offset:0 sc1\n\t" \
   "buffer_load_dwordx4 a[36:39], %[o0], %[rs], s40 offen offset:1024 sc1\n\t" \
   "buffer_load_dwordx4 a[116:119], %[o0], %[rs], s41 offen offset:1024 sc1\n\t" \
   "buffer_load_dwordx4 a[196:199], %[o1], %[rs], s42 offen offset:1024 sc1\n\t" \
   "buffer_load_dwordx4 a[40:43], %[o0], %[rs], s40 offen offset:2048 sc1\n\t" \
   "buffer_load_dwordx4 a[120:123], %[o0], %[rs], s41 offen offset:2048 sc1\n\t" \
   "buffer_load_dwordx4 a[200:203], %[o1], %[rs], s42 offen offset:2048 sc1\n\t" \
   "buffer_load_dwordx4 a[44:47], %[o0], %[rs], s40 offen offset:3072 sc1\n\t" \
   "buffer_load_dwordx4 a[124:127], %[o0], %[rs], s41 offen offset:3072 sc1\n\t" \
   "buffer_load_dwordx4 a[204:207], %[o1], %[rs], s42 offen offset:3072 sc1\n\t" "s_mov_b32 s40, 12288\n\t" \
   "buffer_load_dwordx4 a[48:51], %[o0], %[rs], s40 offen offset:0 sc1\n\t" "s_mov_b32 s41, 32768\n\t" \
   "buffer_load_dwordx4 a[128:131], %[o0], %[rs], s41 offen offset:0 sc1\n\t" "s_mov_b32 s42, 53248\n\t" \
   "buffer_load_dwordx4 a[208:211], %[o1], %[rs], s42 offen offset:0 sc1\n\t" \
   "buffer_load_dwordx4 a[52:55], %[o0], %[rs], s40 offen offset:1024 sc1\n\t" \
   "buffer_load_dwordx4 a[132:135], %[o0], %[rs], s41 offen offset:1024 sc1\n\t" \
   "buffer_load_dwordx4 a[212:215], %[o1], %[rs], s42 offen offset:1024 sc1\n\t" \
   "buffer_load_dwordx4 a[56:59], %[o0], %[rs], s40 offen offset:2048 sc1\n\t" \
   "buffer_load_dwordx4 a[136:139], %[o0], %[rs], s41 offen offset:2048 sc1\n\t" \
   "buffer_load_dwordx4 a[216:219], %[o1], %[rs], s42 offen offset:2048 sc1\n\t" \
   "buffer_load_dwordx4 a[60:63], %[o0], %[rs], s40 offen offset:3072 sc1\n\t" \
   "buffer_load_dwordx4 a[140:143], %[o0], %[rs], s41 offen offset:3072 sc1\n\t" \
   "buffer_load_dwordx4 a[220:223], %[o1], %[rs], s42 offen offset:3072 sc1\n\t" "s_mov_b32 s40, 16384\n\t" \
   "buffer_load_dwordx4 a[64:67], %[o0], %[rs], s40 offen offset:0 sc1\n\t" "s_mov_b32 s41, 36864\n\t" \
   "buffer_load_dwordx4 a[144:147], %[o0], %[rs], s41 offen offset:0 sc1\n\t" "s_mov_b32 s42, 57344\n\t" \
   "buffer_load_dwordx4 a[224:227], %[o1], %[rs], s42 offen offset:0 sc1\n\t" \
   "buffer_load_dwordx4 a[68:71], %[o0], %[rs], s40 offen offset:1024 sc1\n\t" \
   "buffer_load_dwordx4 a[148:151], %[o0], %[rs], s41 offen offset:1024 sc1\n\t" \
   "buffer_load_dwordx4 a[228:231], %[o1], %[rs], s42 offen offset:1024 sc1\n\t" \
   "buffer_load_dwordx4 a[72:75], %[o0], %[rs], s40 offen offset:2048 sc1\n\t" \
   "buffer_load_dwordx4 a[152:155], %[o0], %[rs], s41 offen offset:2048 sc1\n\t" \
   "buffer_load_dwordx4 a[232:235], %[o1], %[rs], s42 offen offset:2048 sc1\n\t" \
   "buffer_load_dwordx4 a[76:79], %[o0], %[rs], s40 offen offset:3072 sc1\n\t" \
   "buffer_load_dwordx4 a[156:159], %[o0], %[rs], s41 offen offset:3072 sc1\n\t" \
   "buffer_load_dwordx4 a[236:239], %[o1], %[rs], s42 offen offset:3072 sc1\n\t" "s_waitcnt vmcnt(0)"
#define IM_CLOB "s40", "s41", "s42"
#define ID_ASM \
   "s_waitcnt lgkmcnt(0)\n\t" "s_barrier\n\t" "ds_read_b128 v[32:35], %[la] offset:0\n\t" "ds_read_b128 v[36:39], %[la] offset:10240\n\t" \
   "ds_read_b128 v[40:43], %[la] offset:1024\n\t" "ds_read_b128 v[44:47], %[la] offset:11264\n\t" "ds_read_b128 v[48:51], %[la] offset:2048\n\t" \
   "ds_read_b128 v[52:55], %[la] offset:12288\n\t" "ds_read_b128 v[56:59], %[la] offset:3072\n\t" "ds_read_b128 v[60:63], %[la] offset:13312\n\t" \
   "ds_read_b128 v[64:67], %[la] offset:4096\n\t" "ds_read_b128 v[68:71], %[la] offset:14336\n\t" "ds_read_b128 v[72:75], %[la] offset:5120\n\t" \
   "ds_read_b128 v[76:79], %[la] offset:15360\n\t" "ds_read_b128 v[80:83], %[la] offset:6144\n\t" "ds_read_b128 v[84:87], %[la] offset:16384\n\t" \
   "ds_read_b128 v[88:91], %[la] offset:7168\n\t" "ds_read_b128 v[92:95], %[la] offset:17408\n\t" "ds_read_b128 v[96:99], %[la] offset:8192\n\t" \
   "ds_read_b128 v[100:103], %[la] offset:18432\n\t" "ds_read_b128 v[104:107], %[la] offset:9216\n\t" "ds_read_b128 v[108:111], %[la] offset:19456\n\t" \
   "v_lshlrev_b32 v152, 16, v136\n\t" "v_and_b32 v153, 0xffff0000, v136\n\t" "v_lshlrev_b32 v154, 16, v137\n\t" "v_and_b32 v155, 0xffff0000, v137\n\t" \
   "v_lshlrev_b32 v156, 16, v140\n\t" "v_and_b32 v157, 0xffff0000, v140\n\t" "v_lshlrev_b32 v158, 16, v141\n\t" "v_and_b32 v159, 0xffff0000, v141\n\t" \
   "v_lshlrev_b32 v160, 16, v138\n\t" "v_and_b32 v161, 0xffff0000, v138\n\t" "v_lshlrev_b32 v162, 16, v139\n\t" "v_and_b32 v163, 0xffff0000, v139\n\t" \
   "s_waitcnt lgkmcnt(15)\n\t" "v_mfma_f32_16x16x32_bf16 v[112:115], a[0:3], v[32:35], 0\n\t" "v_lshlrev_b32 v164, 16, v142\n\t" \
   "v_and_b32 v165, 0xffff0000, v142\n\t" "v_mfma_f32_16x16x32_bf16 v[116:119], a[4:7], v[36:39], 0\n\t" "v_lshlrev_b32 v166, 16, v143\n\t" \
   "v_and_b32 v167, 0xffff0000, v143\n\t" "v_mfma_f32_16x16x32_bf16 v[112:115], a[8:11], v[40:43], v[112:115]\n\t" "v_lshlrev_b32 v168, 16, v144\n\t" \
   "v_and_b32 v169, 0xffff0000, v144\n\t" "v_mfma_f32_16x16x32_bf16 v[116:119], a[12:15], v[44:47], v[116:119]\n\t" "v_lshlrev_b32 v170, 16, v145\n\t" \
   "v_and_b32 v171, 0xffff0000, v145\n\t" "v_mfma_f32_16x16x32_bf16 v[112:115], a[16:19], v[48:51], v[112:115]\n\t" "v_lshlrev_b32 v172, 16, v146\n\t" \
   "v_and_b32 v173, 0xffff0000, v146\n\t" "s_waitcnt lgkmcnt(14)\n\t" "v_mfma_f32_16x16x32_bf16 v[116:119], a[20:23], v[52:55], v[116:119]\n\t" \
   "v_lshlrev_b32 v174, 16, v147\n\t" "v_and_b32 v175, 0xffff0000, v147\n\t" "s_waitcnt lgkmcnt(13)\n\t" \
   "v_mfma_f32_16x16x32_bf16 v[112:115], a[24:27], v[56:59], v[112:115]\n\t" "s_waitcnt lgkmcnt(12)\n\t" \
   "v_mfma_f32_16x16x32_bf16 v[116:119], a[28:31], v[60:63], v[116:119]\n\t" "s_waitcnt lgkmcnt(11)\n\t" \
   "v_mfma_f32_16x16x32_bf16 v[112:115], a[32:35], v[64:67], v[112:115]\n\t" "s_waitcnt lgkmcnt(10)\n\t" \
   "v_mfma_f32_16x16x32_bf16 v[116:119], a[36:39], v[68:71], v[116:119]\n\t" "s_waitcnt lgkmcnt(9)\n\t" \
   "v_mfma_f32_16x16x32_bf16 v[112:115], a[40:43], v[72:75], v[112:115]\n\t" "s_waitcnt lgkmcnt(8)\n\t" \
   "v_mfma_f32_16x16x32_bf16 v[116:119], a[44:47], v[76:79], v[116:119]\n\t" "s_waitcnt lgkmcnt(7)\n\t" \
   "v_mfma_f32_16x16x32_bf16 v[112:115], a[48:51], v[80:83], v[112:115]\n\t" "s_waitcnt lgkmcnt(6)\n\t" \
   "v_mfma_f32_16x16x32_bf16 v[116:119], a[52:55], v[84:87], v[116:119]\n\t" "s_waitcnt lgkmcnt(5)\n\t" \
   "v_mfma_f32_16x16x32_bf16 v[112:115], a[56:59], v[88:91], v[112:115]\n\t" "s_waitcnt lgkmcnt(4)\n\t" \
   "v_mfma_f32_16x16x32_bf16 v[116:119], a[60:63], v[92:95], v[116:119]\n\t" "s_waitcnt lgkmcnt(3)\n\t" \
   "v_mfma_f32_16x16x32_bf16 v[112:115], a[64:67], v[96:99], v[112:115]\n\t" "s_waitcnt lgkmcnt(2)\n\t" \
   "v_mfma_f32_16x16x32_bf16 v[116:119], a[68:71], v[100:103], v[116:119]\n\t" "s_waitcnt lgkmcnt(1)\n\t" \
   "v_mfma_f32_16x16x32_bf16 v[112:115], a[72:75], v[104:107], v[112:115]\n\t" "s_waitcnt lgkmcnt(0)\n\t" \
   "v_mfma_f32_16x16x32_bf16 v[116:119], a[76:79], v[108:111], v[116:119]\n\t" "v_mfma_f32_16x16x32_bf16 v[120:123], a[80:83], v[32:35], 0\n\t" \
   "v_mfma_f32_16x16x32_bf16 v[124:127], a[84:87], v[36:39], 0\n\t" "v_mfma_f32_16x16x32_bf16 v[120:123], a[88:91], v[40:43], v[120:123]\n\t" \
   "s_nop 1\n\t" "v_add_f32 v176, v112, v116\n\t" "v_sub_f32 v180, v112, v116\n\t" \
   "v_mfma_f32_16x16x32_bf16 v[124:127], a[92:95], v[44:47], v[124:127]\n\t" "v_add_f32 v177, v113, v117\n\t" "v_sub_f32 v181, v113, v117\n\t" \
   "v_mfma_f32_16x16x32_bf16 v[120:123], a[96:99], v[48:51], v[120:123]\n\t" "v_add_f32 v178, v114, v118\n\t" "v_sub_f32 v182, v114, v118\n\t" \
   "v_mfma_f32_16x16x32_bf16 v[124:127], a[100:103], v[52:55], v[124:127]\n\t" "v_add_f32 v179, v115, v119\n\t" "v_sub_f32 v183, v115, v119\n\t" \
   "v_mfma_f32_16x16x32_bf16 v[120:123], a[104:107], v[56:59], v[120:123]\n\t" "v_cvt_pk_bf16_f32 v184, v176, v177\n\t" \
   "v_cvt_pk_bf16_f32 v185, v178, v179\n\t" "v_mfma_f32_16x16x32_bf16 v[124:127], a[108:111], v[60:63], v[124:127]\n\t" \
   "v_cvt_pk_bf16_f32 v186, v180, v181\n\t" "v_cvt_pk_bf16_f32 v187, v182, v183\n\t" \
   "v_mfma_f32_16x16x32_bf16 v[120:123], a[112:115], v[64:67], v[120:123]\n\t" "v_lshlrev_b32 v176, 16, v184\n\t" \
   "v_and_b32 v177, 0xffff0000, v184\n\t" "v_mfma_f32_16x16x32_bf16 v[124:127], a[116:119], v[68:71], v[124:127]\n\t" \
   "v_lshlrev_b32 v178, 16, v185\n\t" "v_and_b32 v179, 0xffff0000, v185\n\t" \
   "v_mfma_f32_16x16x32_bf16 v[120:123], a[120:123], v[72:75], v[120:123]\n\t" "v_lshlrev_b32 v180, 16, v186\n\t" \
   "v_and_b32 v181, 0xffff0000, v186\n\t" "v_mfma_f32_16x16x32_bf16 v[124:127], a[124:127], v[76:79], v[124:127]\n\t" \
   "v_lshlrev_b32 v182, 16, v187\n\t" "v_and_b32 v183, 0xffff0000, v187\n\t" \
   "v_mfma_f32_16x16x32_bf16 v[120:123], a[128:131], v[80:83], v[120:123]\n\t" "v_add_f32 v176, v176, v152\n\t" "v_add_f32 v177, v177, v153\n\t" \
   "v_mfma_f32_16x16x32_bf16 v[124:127], a[132:135], v[84:87], v[124:127]\n\t" "v_add_f32 v178, v178, v154\n\t" "v_add_f32 v179, v179, v155\n\t" \
   "v_mfma_f32_16x16x32_bf16 v[120:123], a[136:139], v[88:91], v[120:123]\n\t" "v_add_f32 v180, v180, v156\n\t" "v_add_f32 v181, v181, v157\n\t" \
   "v_mfma_f32_16x16x32_bf16 v[124:127], a[140:143], v[92:95], v[124:127]\n\t" "v_add_f32 v182, v182, v158\n\t" "v_add_f32 v183, v183, v159\n\t" \
   "v_mfma_f32_16x16x32_bf16 v[120:123], a[144:147], v[96:99], v[120:123]\n\t" "v_cvt_pk_bf16_f32 v136, v176, v177\n\t" \
   "v_cvt_pk_bf16_f32 v137, v178, v179\n\t" "v_mfma_f32_16x16x32_bf16 v[124:127], a[148:151], v[100:103], v[124:127]\n\t" \
   "v_cvt_pk_bf16_f32 v140, v180, v181\n\t" "v_cvt_pk_bf16_f32 v141, v182, v183\n\t" \
   "v_mfma_f32_16x16x32_bf16 v[120:123], a[152:155], v[104:107], v[120:123]\n\t" \
   "v_mfma_f32_16x16x32_bf16 v[124:127], a[156:159], v[108:111], v[124:127]\n\t" "v_mfma_f32_16x16x32_bf16 v[128:131], a[160:163], v[32:35], 0\n\t" \
   "v_mfma_f32_16x16x32_bf16 v[132:135], a[164:167], v[36:39], 0\n\t" "v_mfma_f32_16x16x32_bf16 v[128:131], a[168:171], v[40:43], v[128:131]\n\t" \
   "s_nop 1\n\t" "v_add_f32 v176, v120, v124\n\t" "v_sub_f32 v180, v120, v124\n\t" \
   "v_mfma_f32_16x16x32_bf16 v[132:135], a[172:175], v[44:47], v[132:135]\n\t" "v_add_f32 v177, v121, v125\n\t" "v_sub_f32 v181, v121, v125\n\t" \
   "v_mfma_f32_16x16x32_bf16 v[128:131], a[176:179], v[48:51], v[128:131]\n\t" "v_add_f32 v178, v122, v126\n\t" "v_sub_f32 v182, v122, v126\n\t" \
   "v_mfma_f32_16x16x32_bf16 v[132:135], a[180:183], v[52:55], v[132:135]\n\t" "v_add_f32 v179, v123, v127\n\t" "v_sub_f32 v183, v123, v127\n\t" \
   "v_mfma_f32_16x16x32_bf16 v[128:131], a[184:187], v[56:59], v[128:131]\n\t" "v_cvt_pk_bf16_f32 v184, v176, v177\n\t" \
   "v_cvt_pk_bf16_f32 v185, v178, v179\n\t" "v_mfma_f32_16x16x32_bf16 v[132:135], a[188:191], v[60:63], v[132:135]\n\t" \
   "v_cvt_pk_bf16_f32 v186, v180, v181\n\t" "v_cvt_pk_bf16_f32 v187, v182, v183\n\t" \
   "v_mfma_f32_16x16x32_bf16 v[128:131], a[192:195], v[64:67], v[128:131]\n\t" "v_lshlrev_b32 v176, 16, v184\n\t" \
   "v_and_b32 v177, 0xffff0000, v184\n\t" "v_mfma_f32_16x16x32_bf16 v[132:135], a[196:199], v[68:71], v[132:135]\n\t" \
   "v_lshlrev_b32 v178, 16, v185\n\t" "v_and_b32 v179, 0xffff0000, v185\n\t" \
   "v_mfma_f32_16x16x32_bf16 v[128:131], a[200:203], v[72:75], v[128:131]\n\t" "v_lshlrev_b32 v180, 16, v186\n\t" \
   "v_and_b32 v181, 0xffff0000, v186\n\t" "v_mfma_f32_16x16x32_bf16 v[132:135], a[204:207], v[76:79], v[132:135]\n\t" \
   "v_lshlrev_b32 v182, 16, v187\n\t" "v_and_b32 v183, 0xffff0000, v187\n\t" \
   "v_mfma_f32_16x16x32_bf16 v[128:131], a[208:211], v[80:83], v[128:131]\n\t" "v_add_f32 v176, v176, v160\n\t" "v_add_f32 v177, v177, v161\n\t" \
   "v_mfma_f32_16x16x32_bf16 v[132:135], a[212:215], v[84:87], v[132:135]\n\t" "v_add_f32 v178, v178, v162\n\t" "v_add_f32 v179, v179, v163\n\t" \
   "v_mfma_f32_16x16x32_bf16 v[128:131], a[216:219], v[88:91], v[128:131]\n\t" "v_add_f32 v180, v180, v164\n\t" "v_add_f32 v181, v181, v165\n\t" \
   "v_mfma_f32_16x16x32_bf16 v[132:135], a[220:223], v[92:95], v[132:135]\n\t" "v_add_f32 v182, v182, v166\n\t" "v_add_f32 v183, v183, v167\n\t" \
   "v_mfma_f32_16x16x32_bf16 v[128:131], a[224:227], v[96:99], v[128:131]\n\t" "v_cvt_pk_bf16_f32 v138, v176, v177\n\t" \
   "v_cvt_pk_bf16_f32 v139, v178, v179\n\t" "v_mfma_f32_16x16x32_bf16 v[132:135], a[228:231], v[100:103], v[132:135]\n\t" \
   "v_cvt_pk_bf16_f32 v142, v180, v181\n\t" "v_cvt_pk_bf16_f32 v143, v182, v183\n\t" \
   "v_mfma_f32_16x16x32_bf16 v[128:131], a[232:235], v[104:107], v[128:131]\n\t" \
   "v_mfma_f32_16x16x32_bf16 v[132:135], a[236:239], v[108:111], v[132:135]\n\t" "s_nop 7\n\t" "v_add_f32 v176, v128, v132\n\t" \
   "v_sub_f32 v180, v128, v132\n\t" "v_add_f32 v177, v129, v133\n\t" "v_sub_f32 v181, v129, v133\n\t" "v_add_f32 v178, v130, v134\n\t" \
   "v_sub_f32 v182, v130, v134\n\t" "v_add_f32 v179, v131, v135\n\t" "v_sub_f32 v183, v131, v135\n\t" "v_cvt_pk_bf16_f32 v184, v176, v177\n\t" \
   "v_cvt_pk_bf16_f32 v185, v178, v179\n\t" "v_cvt_pk_bf16_f32 v186, v180, v181\n\t" "v_cvt_pk_bf16_f32 v187, v182, v183\n\t" \
   "v_lshlrev_b32 v176, 16, v184\n\t" "v_and_b32 v177, 0xffff0000, v184\n\t" "v_lshlrev_b32 v178, 16, v185\n\t" "v_and_b32 v179, 0xffff0000, v185\n\t" \
   "v_lshlrev_b32 v180, 16, v186\n\t" "v_and_b32 v181, 0xffff0000, v186\n\t" "v_lshlrev_b32 v182, 16, v187\n\t" "v_and_b32 v183, 0xffff0000, v187\n\t" \
   "v_add_f32 v176, v176, v168\n\t" "v_add_f32 v177, v177, v169\n\t" "v_add_f32 v178, v178, v170\n\t" "v_add_f32 v179, v179, v171\n\t" \
   "v_add_f32 v180, v180, v172\n\t" "v_add_f32 v181, v181, v173\n\t" "v_add_f32 v182, v182, v174\n\t" "v_add_f32 v183, v183, v175\n\t" \
   "v_cvt_pk_bf16_f32 v144, v176, v177\n\t" "v_cvt_pk_bf16_f32 v145, v178, v179\n\t" "v_cvt_pk_bf16_f32 v146, v180, v181\n\t" \
   "v_cvt_pk_bf16_f32 v147, v182, v183\n\t" "s_nop 1"
#define ID_CLOB "v32", "v33", "v34", "v35", "v36", "v37", "v38", "v39", "v40", "v41", "v42", "v43", "v44", "v45", "v46", "v47", "v48", "v49", "v50", "v51", "v52", "v53", "v54", "v55", "v56", "v57", "v58", "v59", "v60", "v61", "v62", "v63", "v64", "v65", "v66", "v67", "v68", "v69", "v70", "v71", "v72", "v73", "v74", "v75", "v76", "v77", "v78", "v79", "v80", "v81", "v82", "v83", "v84", "v85", "v86", "v87", "v88", "v89", "v90", "v91", "v92", "v93", "v94", "v95", "v96", "v97", "v98", "v99", "v100", "v101", "v102", "v103", "v104", "v105", "v106", "v107", "v108", "v109", "v110", "v111", "v112", "v113", "v114", "v115", "v116", "v117", "v118", "v119", "v120", "v121", "v122", "v123", "v124", "v125", "v126", "v127", "v128", "v129", "v130", "v131", "v132", "v133", "v134", "v135", "v152", "v153", "v154", "v155", "v156", "v157", "v158", "v159", "v160", "v161", "v162", "v163", "v164", "v165", "v166", "v167", "v168", "v169", "v170", "v171", "v172", "v173", "v174", "v175", "v176", "v177", "v178", "v179", "v180", "v181", "v182", "v183", "v184", "v185", "v186", "v187"
#define TZ 336
#define TZM 368
constexpr int COL_I2 = 64 * 264, COL_LDS = COL_I2 + 8 * 2064;
__device__ __forceinline__ void job_col(int wv, unsigned char* ws, rsrc_t rs, unsigned lds, int jb, int go, int clr, bool all) {
  const int lane = lane_id(), g = lane >> 4, r = lane & 15, j = wv * 64 + lane;
  const int xin = (int)OFF_X1T + (jb == 160 ? 0 : jb) * XS * 8 + j * 16;
  v4u x[4], wf[4];
#pragma unroll
  for (int q = 0; q < 4; ++q) stc(rs, (int)OFF_ZT + jb * ZS * 8 + q * 4096 + j * 16, v4u{0xFFFFFFFFu, 0xFFFFFFFFu, 0xFFFFFFFFu, 0xFFFFFFFFu});
#pragma unroll
  for (int q = 0; q < 4; ++q) {
    const int o = (int)OFF_WT + (jb == 160 ? 320 : 2 * jb + (q >> 1)) * S_ * 4 + ((wv * 2 + (q & 1)) * 64 + lane) * 16;
    wf[q] = ldc(rs, o);
  }
  v32u k0, k1, k2;
  v8u k3, ad = (unsigned)lane;
  {
    const int n = 16 * wv + r;
    cis8(k0, n * (512 * (g & 1) + 256 * (g >> 1)), n * 32, n * 128, (r & 1) ? 0x80008000u : 0u);
    cis8(k1, r * g * 256, r * 64, r * 1024, 0x80000000u);
#pragma unroll
    for (int q = 0; q < 8; ++q) {
      const int i = -n * sigma(g, q), k = (r + 16 * (q >> 2)) * (16 * wv + 4 * g + (q & 3));
      const float sc = 1.0f / ((float)S_ * (float)D_);
      k0[16 + 2 * q] = __builtin_bit_cast(unsigned, cosi(i)); k0[17 + 2 * q] = __builtin_bit_cast(unsigned, sini(i));
      k1[16 + 2 * q] = __builtin_bit_cast(unsigned, cosi(k) * sc); k1[17 + 2 * q] = __builtin_bit_cast(unsigned, sini(k) * sc);
      k3[q] = k1[8 + q] ^ 0x80008000u;
    }
    ad[0] = lds + 264 * (16 * (g & 1) + 8 * (g >> 1) + (r >> 2)) + 32 * (r & 3);
    ad[1] = lds + COL_I2 + (2 * (r >> 3) + (r & 1)) * 2064 + ((r >> 1) & 3) * 512 + (g & 1) * 8;
    ad[2] = ad[1] + 256 + (((4 * (wv >> 1) + 2 * (g >> 1) + (wv & 1)) ^ ((r & 6) + 1)) << 4);
    ad[1] += ((4 * (wv >> 1) + 2 * (g >> 1) + (wv & 1)) ^ (r & 6)) << 4;
    ad[3] = lds + COL_I2 + r * 128 + ((g ^ (r >> 1)) << 4);
    ad[4] = lds + COL_I2 + r * 128 + (((4 + g) ^ (r >> 1)) << 4);
    ad[5] = (int)OFF_ZT + (jb * ZS + 32 * (16 * wv + r) + 2 * g) * 8;
    asm volatile("" : "+{v[32:63]}"(k0), "+{v[64:95]}"(k1), "+{v[128:135]}"(k3), "+{v[16:23]}"(ad));
  }
  const unsigned sh = jb == 160 ? 16u : 0u, mk = jb == 0 ? 0xFFFFu : ~0u, d = lds + (j >> 4) * 264 + r * 16;
#pragma unroll
  for (int q = 0; q < 16; ++q) { k2[2 * q] = wf[q >> 2][q & 3] << 16; k2[2 * q + 1] = wf[q >> 2][q & 3] & 0xFFFF0000u; }
  asm volatile("" : "+{v[96:127]}"(k2));
  fan_signal<FSP>(wv, ws, clr, jb);
  if (all) fan_wait<FSP, NCOL>(wv, ws, clr, jb);
  fan_wait<64, 20>(wv, ws, go, jb);
  PH(27)
#pragma unroll
  for (int q = 0; q < 4; ++q) x[q] = ldc(rs, xin + q * 4096);
#define CSTAMP(i)
  CSTAMP(0)
#pragma unroll
  for (int q = 0; q < 4; ++q) {
    LDS_V2(d + q * 16 * 264) = v2u{(x[q].x >> sh) & mk, x[q].y};
    LDS_V2(d + q * 16 * 264 + 8) = v2u{(x[q].z >> sh) & mk, x[q].w};
  }
  CSTAMP(1)
  PH(28)
  asm volatile(COL_ASM ::"{v[32:63]}"(k0), "{v[64:95]}"(k1), "{v[96:127]}"(k2), "{v[128:135]}"(k3), "{v[16:23]}"(ad), [rs] "s"(rs) : COL_CLOB, "memory");
  CSTAMP(2)
  PH(29)
  CSTAMP(3)
}
constexpr int OOB = 0x7FFFFFF0;
struct XRows { v4u p[2]; v2u q[2]; };
__device__ __forceinline__ int xr_off(int rt, int hf, int wv, int lane, bool third) {
  const int o = (int)OFF_XR + ((rt * KT + hf * 5) * 64 + (lane & 48) + 2 * (lane & 7) + ((lane >> 3) & 1)) * 16;
  return third ? (wv < 2 ? o + 4096 + wv * 8 : OOB) : o + wv * 1024;
}
__device__ __forceinline__ void idft_x(XRows& x, rsrc_t rs, int rt, int hf, int wv, int lane) {
#pragma unroll
  for (int m = 0; m < 2; ++m) {
    x.p[m] = __builtin_amdgcn_raw_buffer_load_b128(rs, xr_off(rt, hf, wv, lane, false), m * 10240, AUX_SC1);
    x.q[m] = __builtin_amdgcn_raw_buffer_load_b64(rs, xr_off(rt, hf, wv, lane, true), m * 10240, AUX_SC1);
  }
}
__device__ __forceinline__ void idft_stage(rsrc_t rs, v4u* lds, int rt, int wv, int lane) {
  const int o = (int)OFF_ZT + ((lane >> 3) * ZS + rt * 16 + 2 * (lane & 7)) * 8;
  const bool more = lane >> 4 == wv;
  int off[9];
  v4u t[9];
#pragma unroll
  for (int i = 0; i < 9; ++i) {
    off[i] = i < 4 ? o + (wv * 32 + i * 8) * ZS * 8 : (more && (i < 8 || lane < 8) ? o + (96 + i * 8) * ZS * 8 : OOB);
    t[i] = ldc(rs, off[i]);
  }
  for (unsigned spins = 0; spins < (1u << 16); ++spins) {
    unsigned m = 0;
#pragma unroll
    for (int i = 0; i < 9; ++i) m = max(m, max(t[i].x, t[i].z));
    if (__builtin_expect(!__any(m == 0xFFFFFFFFu), 1)) break;
    __builtin_amdgcn_s_sleep(2);
#pragma unroll
    for (int i = 0; i < 9; ++i) {
      const bool again = max(t[i].x, t[i].z) == 0xFFFFFFFFu;
      const v4u u = ldc(rs, again ? off[i] : OOB);
      if (again) t[i] = u;
    }
    asm volatile("" ::: "memory");
  }
  if (more && lane < 8) {
    t[0].x = (t[0].x & 0xFFFFu) | (t[8].x << 16);
    t[0].z = (t[0].z & 0xFFFFu) | (t[8].z << 16);
  }
  v4u* dst = lds + (lane >> 3) * 16 + (lane & 7);
#pragma unroll
  for (int c = 0; c < 4; ++c) dst[wv * 128 + (c & 1) * 640 + (c >> 1) * 8] = v4u{t[0][c], t[1][c], t[2][c], t[3][c]};
  if (more) {
#pragma unroll
    for (int c = 0; c < 4; ++c) dst[512 + (c & 1) * 640 + (c >> 1) * 8] = v4u{t[4][c], t[5][c], t[6][c], t[7][c]};
  }
}
__device__ __forceinline__ void idft_mul(XRows& x, const v32u (&a)[8], unsigned la) {
  asm volatile(ID_ASM : "+{v[136:139]}"(x.p[0]), "+{v[140:143]}"(x.p[1]), "+{v[144:145]}"(x.q[0]), "+{v[146:147]}"(x.q[1])
               : "{a[0:31]}"(a[0]), "{a[32:63]}"(a[1]), "{a[64:95]}"(a[2]), "{a[96:127]}"(a[3]), "{a[128:159]}"(a[4]), "{a[160:191]}"(a[5]), "{a[192:223]}"(a[6]),
                 "{a[224:255]}"(a[7]), [la] "v"(la) : ID_CLOB, "memory");
}
__device__ __forceinline__ void idft_matrix(v32u (&a)[8], rsrc_t rs, int hf, int wv, int lane) {
  const int o = (int)OFF_INV + hf * 10 * NFK * 1024 + lane * 16;
  asm volatile(IM_ASM : "={a[0:31]}"(a[0]), "={a[32:63]}"(a[1]), "={a[64:95]}"(a[2]), "={a[96:127]}"(a[3]), "={a[128:159]}"(a[4]), "={a[160:191]}"(a[5]),
                 "={a[192:223]}"(a[6]), "={a[224:255]}"(a[7])
               : [o0] "v"(o + wv * 2 * NFK * 1024), [o1] "v"(wv < 2 ? o + (wv + 6) * NFK * 1024 : OOB), [rs] "s"(rs) : IM_CLOB, "memory");
}
__device__ __forceinline__ void idft_when(unsigned tz) {
  const unsigned t = __builtin_amdgcn_readfirstlane(*(unsigned*)(dyn_lds + L_WHEN)) + tz;
  while ((int)(t - rtc()) > 0 && (int)(t - rtc()) < 1000) __builtin_amdgcn_s_sleep(1);
}
constexpr int NFIRST = 40;
__device__ __forceinline__ void job_idft(int wv_, unsigned char* ws, rsrc_t rs, v4u* lds, int rt0, int hf, unsigned long long* dbg) {
  const int lane = lane_id(), wv = wv_;
  v32u a[8];
  idft_matrix(a, rs, hf, wv, lane);
  fan_wait<FSP, NCOL>(wv_, ws, fan_off(1, 0, 0), rt0 >> 2);
  fan_wait<64, 20>(wv_, ws, fan_off(0, 1, 0), rt0 >> 2);
#pragma unroll 1
  for (int rt = rt0; rt < RT; rt += 32) {
    XRows x;
    idft_x(x, rs, rt, hf, wv, lane);
    if (rt == rt0) { idft_when(TZM); PH(30) }
    idft_stage(rs, lds, rt, wv, lane);
    idft_mul(x, a, lds_base() + lane * 16);
#pragma unroll
    for (int m = 0; m < 2; ++m) {
      __builtin_amdgcn_raw_buffer_store_b128(x.p[m], rs, xr_off(rt, hf, wv, lane, false), m * 10240, AUX_SC1);
      __builtin_amdgcn_raw_buffer_store_b64(x.q[m], rs, xr_off(rt, hf, wv, lane, true), m * 10240, AUX_SC1);
    }
    const f32x2 st = rows_sum(frag_stat(x.p[0]) + frag_stat(x.p[1]) + frag_stat(v4u{x.q[0].x, x.q[0].y, x.q[1].x, x.q[1].y}));
    f32x2* sp = (f32x2*)(lds + 5120) + (lane & 15);
    if (lane < 16) sp[wv * 16] = st;
    asm volatile("s_waitcnt lgkmcnt(0)\n\ts_barrier" ::: "memory");
    if (wv == 0) {
      const f32x2 s0 = sp[0] + sp[16], s1 = sp[32] + sp[48];
      const f32x4 sv = lane < 16 ? f32x4{s0.x, s0.y, s1.x, s1.y} : f32x4{0, 0, 0, 0};
      stc(rs, lane < 16 + hf * 16 ? (int)OFF_ST + (rt * 16 + 2 * (lane & 7) + ((lane >> 3) & 1)) * 48 + (hf + (lane >> 4)) * 16 : OOB, __builtin_bit_cast(v4u, sv));
    }
    if (rt == rt0) { PH(31) }
    if (rt < NFIRST / 2) fan_signal<64>(wv_, ws, fan_off(1, 1, 0), rt * 2 + hf);
    else dep_signal(wv_, ws, cnt_off(1, CNT_IDFT, rt >> 2));
  }
}

constexpr int JOB_LDS = 4 * NFK * 1024;
constexpr int FAN_LAST = fan_off(0, 1, 0) + 20 * 64;
#define OAUX 0
__device__ __forceinline__ void job_last(int wv_, unsigned char* ws, u16* out, int jb, int rt, int hf) {
  const rsrc_t rs = ws_rsrc(ws), ro = __builtin_amdgcn_make_buffer_rsrc(out, 0, S_ * D_ * 2, 0x00020000);
  const int wv = wv_;
  v32u a[8];
  idft_matrix(a, rs, hf, wv, lane_id());
  PH(26)
  if (jb >= 0) {
    job_col(wv_, ws, rs, lds_base(), jb, FAN_LAST, fan_off(0, 0, 0), true);
    PH(13)
  } else {
    fan_wait<FSP, NCOL>(wv_, ws, fan_off(0, 0, 0), rt);
    fan_wait<64, 20>(wv_, ws, FAN_LAST, rt);
  }
  const int lane = lane_id();
  v4u* lds = (v4u*)dyn_lds + 3072;
  XRows x;
  idft_x(x, rs, rt, hf, wv, lane);
  idft_when(TZ);
  PH(24)
  idft_stage(rs, lds, rt, wv, lane);
  idft_mul(x, a, lds_base() + 49152 + lane * 16);
  const int o = ((rt * 16 + 2 * (lane & 7) + ((lane >> 3) & 1)) * D_ + hf * 160 + (lane >> 5) * 8) * 2;
#pragma unroll
  for (int m = 0; m < 3; ++m) {
    const v4u y = m < 2 ? x.p[m] : v4u{x.q[0].x, x.q[0].y, x.q[1].x, x.q[1].y};
    const auto s0 = __builtin_amdgcn_permlane16_swap(y.x, y.z, false, false), s1 = __builtin_amdgcn_permlane16_swap(y.y, y.w, false, false);
    __builtin_amdgcn_raw_buffer_store_b128(v4u{s0[0], s1[0], s0[1], s1[1]}, ro,
                                           m < 2 ? o + m * 640 + wv * 64 + (lane & 16) * 2 : (wv < 2 ? o + 256 + wv * 32 + (lane & 16) * 40 : OOB), 0, OAUX);
  }
  PH(15)
}

constexpr unsigned SPIN_CAP = 1u << 16;

#define VM_LD128(dst, off, so, IMM) asm volatile("buffer_load_dwordx4 %0, %1, %2, %3 offen offset:" #IMM " sc1" : "=v"(dst) : "v"(off), "s"(rs), "s"(so) : "memory")
#define VM_LD32(dst, off, so) asm volatile("buffer_load_dword %0, %1, %2, %3 offen sc1" : "=v"(dst) : "v"(off), "s"(rs), "s"(so) : "memory")
#define VM_ST32(val, off, so) asm volatile("buffer_store_dword %0, %1, %2, %3 offen sc1" ::"v"(val), "v"(off), "s"(rs), "s"(so) : "memory")
#define VM_ST128(val, off, so) asm volatile("buffer_store_dwordx4 %0, %1, %2, %3 offen sc1\n\ts_nop 1" ::"v"(val), "v"(off), "s"(rs), "s"(so) : "memory")
#define VM_WAIT(N) asm volatile("s_waitcnt vmcnt(" #N ")" ::: "memory")
#define VM_WAIT1(N, a) asm volatile("s_waitcnt vmcnt(" #N ")" : "+v"(a)::"memory")
__device__ __forceinline__ v4u make_rs(const void* p, unsigned bytes) {
  const unsigned long long a = (unsigned long long)p;
  v4u r;
  r.x = (unsigned)__builtin_amdgcn_readfirstlane((unsigned)a);
  r.y = (unsigned)__builtin_amdgcn_readfirstlane((unsigned)(a >> 32) & 0xFFFFu);
  r.z = bytes;
  r.w = 0x00020000u;
  return r;
}
struct Clk0 { unsigned r, c; };
__device__ __forceinline__ Clk0 clk_sample() {
  unsigned long long r, c;
  asm volatile("s_memrealtime %0\n\ts_memtime %1\n\ts_waitcnt lgkmcnt(0)" : "=s"(r), "=s"(c)::"memory");
  return Clk0{(unsigned)r, (unsigned)c};
}
__device__ __forceinline__ v4u clk_init(Clk0 a) {
  const Clk0 b = clk_sample();
  unsigned k = 1400u;
  if (b.r - a.r >= 100u) k = (unsigned)((float)(b.r - a.r) * 32768.f / (float)(b.c - a.c));
  k = (unsigned)__builtin_amdgcn_readfirstlane((int)min(max(k, 1200u), 3000u));
  asm volatile("" : "+s"(k));
  return v4u{b.r * 8u + 16u, b.c, k, 0u};
}
#define LDSBAR() asm volatile("s_waitcnt lgkmcnt(0)\n\ts_barrier" ::: "memory")
#define MEMPIN() asm volatile("" ::: "memory")

__device__ __forceinline__ int ladder(int x, int i) { return copy_off(((x + (i >> 1)) & 3) | ((i & 1) << 2)); }

struct RecTune { int m1, m2, dl1, rob, c2, vis, dlt, du; };

#define STAT(r)
__device__ __forceinline__ unsigned max4(v4u v) { return max(max(v.x, v.y), max(v.z, v.w)); }
template <int NAP = 6>
__device__ __forceinline__ unsigned flag_finish(const v4u rs, int off, unsigned cv) {
  for (unsigned spins = 0; __any(cv == 0xFFFFFFFFu) && spins < SPIN_CAP; ++spins) {
    __builtin_amdgcn_s_sleep(NAP);
    v4u t;
    VM_LD128(t, off, 0, 0);
    VM_WAIT1(0, t);
    cv = max4(t);
  }
  return cv;
}
struct Pool {
  unsigned long long sel, s3, ysel; int base;
  __device__ __forceinline__ int near(int s) const { return (int)((s3 >> s) & 1ull) * 16384; }
  __device__ __forceinline__ int region(int s) const { return base + s * REG + (int)((sel >> s) & 1ull) * 8192; }
};
constexpr int L_QT = 10240, L_KT = 12288, L_CL = 14336, L_KQ = 16384;
constexpr int L_T = 14336;
constexpr int L_M2 = L_KQ + 65536;
constexpr int REC_ST = L_M2 + 12288;
constexpr int REC_LDS = REC_ST + 4096 + 64;

#define RSTAMP(i)
#define YSTAMP(n)
#define RST_CLOB
#define P1TRACE
#define PVTRACE
#define FINTRACE
#define P1_ASM \
   "s_mov_b32 s64, -1\n\t" "s_mov_b32 s76, 0\n\t" "s_mov_b32 s77, 0\n\t" "s_mov_b32 s71, 63\n\t" "s_mov_b32 s47, 1300\n\t" "s_mov_b32 s49, 0\n\t" \
   "s_mov_b32 s51, 0\n\t" "s_mov_b32 s52, 0\n\t" "s_mov_b32 s70, 0\n\t" "s_mov_b32 s69, 0\n\t" "v_cmp_gt_i32 s[20:21], 0, v126\n\t" \
   "v_cmp_gt_i32 s[22:23], 1, v126\n\t" "v_cmp_gt_i32 s[24:25], 2, v126\n\t" "v_cmp_gt_i32 s[26:27], 3, v126\n\t" "v_cmp_gt_i32 s[28:29], 16, v126\n\t" \
   "v_cmp_gt_i32 s[30:31], 17, v126\n\t" "v_cmp_gt_i32 s[32:33], 18, v126\n\t" "v_cmp_gt_i32 s[34:35], 19, v126\n\t" "s_waitcnt vmcnt(0)\n\t" \
   "s_lshr_b64 s[72:73], %[sel], s71\n\t" "s_lshl_b32 s65, s71, 19\n\t" "s_and_b32 s72, s72, 1\n\t" "s_add_u32 s65, s65, %[pool]\n\t" \
   "s_lshl_b32 s72, s72, 13\n\t" "s_add_u32 s65, s65, s72\n\t" "s_lshr_b64 s[72:73], %[s3], s71\n\t" "s_and_b32 s54, s72, 1\n\t" \
   "s_lshl_b32 s54, s54, 14\n\t" "s_memtime s[62:63]\n\t" "s_memrealtime s[60:61]\n\t" "40:\n\t" "s_cmp_eq_u32 s64, 0\n\t" "s_cbranch_scc1 95f\n\t" \
   "96:\n\t" "s_add_i32 s72, s64, 2\n\t" "s_min_i32 s72, s72, 63\n\t" "s_lshl_b32 s59, s72, 12\n\t" "s_lshl_b32 s79, s72, 13\n\t" \
   "buffer_load_dword v108, v139, %[rs], s65 offen sc1\n\t" "s_cmp_gt_u32 %[wv], 1\n\t" "s_waitcnt vmcnt(4)\n\t" "s_cbranch_scc1 44f\n\t" \
   "buffer_load_dword v15, v140, %[rs], s59 offen sc1\n\t" "ds_write_b128 v127, v[16:19] offset:0\n\t" "ds_write_b128 v127, v[20:23] offset:1024\n\t" \
   "ds_write_b128 v127, v[24:27] offset:2048\n\t" "ds_write_b128 v127, v[28:31] offset:3072\n\t" "s_waitcnt lgkmcnt(0)\n\t" \
   "s_sub_u32 s74, s62, s57\n\t" "s_mul_i32 s74, s74, s58\n\t" "s_lshr_b32 s74, s74, 12\n\t" "s_add_u32 s74, s74, s56\n\t" "s_lshl_b32 s60, s60, 3\n\t" \
   "s_sub_u32 s75, s74, s60\n\t" "s_cmp_lt_u32 s75, 32\n\t" "s_cbranch_scc1 38f\n\t" "s_cmp_lt_i32 s75, 0\n\t" "s_cselect_b32 s61, 0, 31\n\t" \
   "s_add_u32 s74, s60, s61\n\t" "s_sub_i32 s75, s61, s75\n\t" "s_add_i32 s75, s75, 1\n\t" "s_ashr_i32 s75, s75, 1\n\t" "s_min_i32 s75, s75, 8\n\t" \
   "s_max_i32 s75, s75, -8\n\t" "s_mov_b32 s56, s74\n\t" "s_mov_b32 s57, s62\n\t" "s_add_u32 s58, s58, s75\n\t" "38:\n\t" "s_min_u32 s72, s70, 1\n\t" \
   "s_and_b32 s71, s74, -4\n\t" "s_or_b32 s71, s71, s72\n\t" "v_mov_b32 v111, s71\n\t" "buffer_store_dword v111, v131, %[rs], s65 offen sc1\n\t" \
   P1TRACE "s_barrier\n\t" "ds_read_b128 v[160:163], v122 offset:0\n\t" "ds_read_b128 v[164:167], v122 offset:1024\n\t" \
   "ds_read_b128 v[168:171], v122 offset:2048\n\t" "ds_read_b128 v[172:175], v122 offset:3072\n\t" "ds_read_b128 v[176:179], v122 offset:4096\n\t" \
   "ds_read_b128 v[180:183], v122 offset:5120\n\t" "ds_read_b128 v[184:187], v122 offset:6144\n\t" "ds_read_b128 v[188:191], v122 offset:7168\n\t" \
   "s_cmp_lt_i32 s64, 1\n\t" "s_cselect_b32 s72, 0x7ffffff0, 0\n\t" "s_max_i32 s71, s64, 1\n\t" "v_or_b32 v109, s72, v143\n\t" \
   "s_lshl_b32 s71, s71, 12\n\t" "v_mov_b32 v110, 1\n\t" "s_sub_u32 s71, s71, 4096\n\t" "buffer_store_dword v110, v109, %[rs], s71 offen sc1\n\t" \
   "s_add_u32 s48, s69, s47\n\t" "s_cmp_eq_u32 s70, 0\n\t" "s_cselect_b32 s48, s48, -1\n\t" "s_mov_b32 s66, s65\n\t" "s_add_i32 s64, s64, 1\n\t" \
   "s_and_b32 s71, s64, 63\n\t" "s_lshr_b64 s[72:73], %[sel], s71\n\t" "s_lshl_b32 s65, s71, 19\n\t" "s_and_b32 s72, s72, 1\n\t" \
   "s_add_u32 s65, s65, %[pool]\n\t" "s_lshl_b32 s72, s72, 13\n\t" "s_add_u32 s65, s65, s72\n\t" "s_xor_b32 s67, s54, %[lad0]\n\t" \
   "s_xor_b32 s68, s67, 0x4000\n\t" "s_add_u32 s67, s67, s66\n\t" "s_add_u32 s68, s68, s66\n\t" "s_lshr_b64 s[72:73], %[s3], s71\n\t" \
   "s_and_b32 s54, s72, 1\n\t" "s_lshl_b32 s54, s54, 14\n\t" "s_cmp_eq_u32 s64, 0\n\t" "s_cselect_b32 s70, 1, 0\n\t" "s_and_b32 s55, %[wv], 1\n\t" \
   "s_lshl_b32 s55, s55, 14\n\t" "s_xor_b32 s55, s55, s54\n\t" "s_xor_b32 s50, s55, 0x4000\n\t" "s_add_u32 s55, s55, s65\n\t" \
   "s_add_u32 s50, s50, s65\n\t" "v_mov_b32 v48, 0\n\t" "v_mov_b32 v49, 0\n\t" "v_mov_b32 v50, 0\n\t" "v_mov_b32 v51, 0\n\t" "v_mov_b32 v52, 0\n\t" \
   "v_mov_b32 v53, 0\n\t" "v_mov_b32 v54, 0\n\t" "v_mov_b32 v55, 0\n\t" "v_mov_b32 v56, 0\n\t" "v_mov_b32 v57, 0\n\t" "v_mov_b32 v58, 0\n\t" \
   "v_mov_b32 v59, 0\n\t" "v_mov_b32 v60, 0\n\t" "v_mov_b32 v61, 0\n\t" "v_mov_b32 v62, 0\n\t" "v_mov_b32 v63, 0\n\t" "s_waitcnt vmcnt(2)\n\t" \
   "v_cmp_eq_u32 vcc, -1, v15\n\t" "s_nop 0\n\t" "s_cbranch_vccnz 141f\n\t" "41:\n\t" "s_branch 48f\n\t" "44:\n\t" \
   "v_mfma_f32_16x16x32_bf16 v[32:35], v[160:163], v[16:19], 0\n\t" "s_max_i32 s51, s64, 0\n\t" "s_lshl_b32 s52, s51, 16\n\t" \
   "s_lshl_b32 s51, s51, 14\n\t" "v_mfma_f32_16x16x32_bf16 v[36:39], v[176:179], v[16:19], 0\n\t" "s_cmp_lt_i32 s64, 0\n\t" \
   "s_cselect_b32 s49, 0x7ffffff0, 0\n\t" "s_add_i32 s72, s64, 1\n\t" "v_mfma_f32_16x16x32_bf16 v[32:35], v[164:167], v[20:23], v[32:35]\n\t" \
   "s_min_i32 s72, s72, 63\n\t" "s_lshl_b32 s79, s72, 13\n\t" "v_mfma_f32_16x16x32_bf16 v[36:39], v[180:183], v[20:23], v[36:39]\n\t" \
   "v_mfma_f32_16x16x32_bf16 v[32:35], v[168:171], v[24:27], v[32:35]\n\t" "s_waitcnt lgkmcnt(0)\n\t" \
   "v_mfma_f32_16x16x32_bf16 v[36:39], v[184:187], v[24:27], v[36:39]\n\t" "s_sub_u32 s74, s62, s57\n\t" "s_mul_i32 s74, s74, s58\n\t" \
   "s_lshr_b32 s74, s74, 12\n\t" "v_mfma_f32_16x16x32_bf16 v[32:35], v[172:175], v[28:31], v[32:35]\n\t" "s_add_u32 s74, s74, s56\n\t" \
   "s_lshl_b32 s60, s60, 3\n\t" "s_sub_u32 s75, s74, s60\n\t" "v_mfma_f32_16x16x32_bf16 v[36:39], v[188:191], v[28:31], v[36:39]\n\t" \
   "s_cmp_lt_u32 s75, 32\n\t" "s_cbranch_scc1 39f\n\t" "s_cmp_lt_i32 s75, 0\n\t" "s_cselect_b32 s61, 0, 31\n\t" "s_add_u32 s74, s60, s61\n\t" \
   "s_sub_i32 s75, s61, s75\n\t" "s_add_i32 s75, s75, 1\n\t" "s_ashr_i32 s75, s75, 1\n\t" "s_min_i32 s75, s75, 8\n\t" "s_max_i32 s75, s75, -8\n\t" \
   "s_mov_b32 s56, s74\n\t" "s_mov_b32 s57, s62\n\t" "s_add_u32 s58, s58, s75\n\t" "39:\n\t" "s_barrier\n\t" \
   "v_mfma_f32_16x16x32_bf16 v[40:43], v[144:147], v[16:19], 0\n\t" "s_min_u32 s72, s70, 1\n\t" "s_and_b32 s71, s74, -4\n\t" \
   "s_or_b32 s71, s71, s72\n\t" "v_mov_b32 v111, s71\n\t" "v_mfma_f32_16x16x32_bf16 v[40:43], v[148:151], v[20:23], v[40:43]\n\t" \
   "buffer_store_dword v111, v131, %[rs], s65 offen sc1\n\t" P1TRACE "v_mov_b32 v48, 0\n\t" "v_mov_b32 v49, 0\n\t" \
   "v_mfma_f32_16x16x32_bf16 v[40:43], v[152:155], v[24:27], v[40:43]\n\t" "v_mov_b32 v50, 0\n\t" "v_mov_b32 v51, 0\n\t" "v_mov_b32 v52, 0\n\t" \
   "v_mov_b32 v53, 0\n\t" "v_mfma_f32_16x16x32_bf16 v[40:43], v[156:159], v[28:31], v[40:43]\n\t" "v_mov_b32 v54, 0\n\t" "v_mov_b32 v55, 0\n\t" \
   "v_cndmask_b32 v32, v32, v125, s[20:21]\n\t" "v_cndmask_b32 v33, v33, v125, s[22:23]\n\t" "v_cndmask_b32 v34, v34, v125, s[24:25]\n\t" \
   "v_cndmask_b32 v35, v35, v125, s[26:27]\n\t" "v_cndmask_b32 v36, v36, v125, s[28:29]\n\t" "v_cndmask_b32 v37, v37, v125, s[30:31]\n\t" \
   "v_cndmask_b32 v38, v38, v125, s[32:33]\n\t" "v_cndmask_b32 v39, v39, v125, s[34:35]\n\t" "v_exp_f32 v64, v32\n\t" "s_add_u32 s48, s69, s47\n\t" \
   "s_cmp_eq_u32 s70, 0\n\t" "s_cselect_b32 s48, s48, -1\n\t" "v_exp_f32 v65, v33\n\t" "s_mov_b32 s66, s65\n\t" "s_add_i32 s64, s64, 1\n\t" \
   "s_and_b32 s71, s64, 63\n\t" "v_exp_f32 v66, v34\n\t" "s_lshr_b64 s[72:73], %[sel], s71\n\t" "s_lshl_b32 s65, s71, 19\n\t" \
   "s_and_b32 s72, s72, 1\n\t" "v_exp_f32 v67, v35\n\t" "s_add_u32 s65, s65, %[pool]\n\t" "s_lshl_b32 s72, s72, 13\n\t" "s_add_u32 s65, s65, s72\n\t" \
   "v_exp_f32 v68, v36\n\t" "s_xor_b32 s67, s54, %[lad0]\n\t" "s_xor_b32 s68, s67, 0x4000\n\t" "s_add_u32 s67, s67, s66\n\t" "v_exp_f32 v69, v37\n\t" \
   "s_add_u32 s68, s68, s66\n\t" "s_lshr_b64 s[72:73], %[s3], s71\n\t" "s_and_b32 s54, s72, 1\n\t" "v_exp_f32 v70, v38\n\t" \
   "s_lshl_b32 s54, s54, 14\n\t" "s_cmp_eq_u32 s64, 0\n\t" "s_cselect_b32 s70, 1, 0\n\t" "v_exp_f32 v71, v39\n\t" "s_and_b32 s55, %[wv], 1\n\t" \
   "s_lshl_b32 s55, s55, 14\n\t" "s_xor_b32 s55, s55, s54\n\t" "v_exp_f32 v72, v40\n\t" "s_xor_b32 s50, s55, 0x4000\n\t" "s_add_u32 s55, s55, s65\n\t" \
   "s_add_u32 s50, s50, s65\n\t" "v_exp_f32 v73, v41\n\t" "v_mov_b32 v56, 0\n\t" "v_mov_b32 v57, 0\n\t" "v_mov_b32 v58, 0\n\t" "v_exp_f32 v74, v42\n\t" \
   "v_mov_b32 v59, 0\n\t" "v_mov_b32 v60, 0\n\t" "v_mov_b32 v61, 0\n\t" "v_exp_f32 v75, v43\n\t" "v_mov_b32 v62, 0\n\t" "v_mov_b32 v63, 0\n\t" \
   "v_add_f32 v80, v64, v65\n\t" "v_add_f32 v81, v66, v67\n\t" "v_add_f32 v82, v68, v69\n\t" "v_add_f32 v83, v70, v71\n\t" \
   "v_add_f32 v84, v72, v73\n\t" "v_add_f32 v85, v74, v75\n\t" "v_add_f32 v80, v80, v81\n\t" "v_add_f32 v82, v82, v83\n\t" \
   "v_add_f32 v84, v84, v85\n\t" "v_add_f32 v80, v80, v82\n\t" "v_fma_f32 v82, v80, 2.0, v84\n\t" "v_fma_f32 v80, v80, 2.0, v84\n\t" \
   "ds_read_b128 v[160:163], v122 offset:0\n\t" "ds_read_b128 v[164:167], v122 offset:1024\n\t" "v_permlane16_swap_b32 v80, v82\n\t" \
   "v_add_f32 v81, v80, v82\n\t" "v_add_f32 v80, v80, v82\n\t" "ds_read_b128 v[168:171], v122 offset:2048\n\t" \
   "ds_read_b128 v[172:175], v122 offset:3072\n\t" "v_permlane32_swap_b32 v80, v81\n\t" "v_add_f32 v80, v80, v81\n\t" "v_rcp_f32 v90, v80\n\t" \
   "v_add_u32 v81, 0xe0800000, v80\n\t" "v_add_f32 v90, v90, v90\n\t" "v_cmp_lt_u32 vcc, 0x40000000, v81\n\t" \
   "ds_read_b128 v[176:179], v122 offset:4096\n\t" "ds_read_b128 v[180:183], v122 offset:5120\n\t" "ds_read_b128 v[184:187], v122 offset:6144\n\t" \
   "ds_read_b128 v[188:191], v122 offset:7168\n\t" "s_cbranch_vccnz 74f\n\t" "75:\n\t" "v_pk_mul_f32 v[64:65], v[64:65], v[90:91] op_sel_hi:[1,0]\n\t" \
   "v_pk_mul_f32 v[66:67], v[66:67], v[90:91] op_sel_hi:[1,0]\n\t" "v_pk_mul_f32 v[68:69], v[68:69], v[90:91] op_sel_hi:[1,0]\n\t" \
   "v_pk_mul_f32 v[70:71], v[70:71], v[90:91] op_sel_hi:[1,0]\n\t" "v_pk_mul_f32 v[72:73], v[72:73], v[90:91] op_sel_hi:[1,0]\n\t" \
   "v_pk_mul_f32 v[74:75], v[74:75], v[90:91] op_sel_hi:[1,0]\n\t" "v_cvt_pk_bf16_f32 v44, v64, v65\n\t" "v_cvt_pk_bf16_f32 v46, v66, v67\n\t" \
   "v_cvt_pk_bf16_f32 v45, v68, v69\n\t" "v_cvt_pk_bf16_f32 v47, v70, v71\n\t" "v_cvt_pk_bf16_f32 v32, v72, v73\n\t" \
   "v_cvt_pk_bf16_f32 v33, v74, v75\n\t" "s_waitcnt vmcnt(1)\n\t" "48:\n\t" "v_readlane_b32 s69, v108, 0\n\t" "v_readlane_b32 s71, v108, 1\n\t" \
   "s_cmp_eq_u32 s64, 64\n\t" "s_cbranch_scc1 90f\n\t" "s_cmp_eq_u32 s69, -1\n\t" "s_cbranch_scc1 63f\n\t" "s_cmp_eq_u32 s71, -1\n\t" \
   "s_cselect_b32 s47, s47, s71\n\t" "50:\n\t" "s_add_u32 s73, s69, %[m1]\n\t" RSTAMP(5) "51:\n\t" "s_memrealtime s[60:61]\n\t" \
   "s_memtime s[62:63]\n\t" "s_waitcnt lgkmcnt(0)\n\t" "s_sub_u32 s74, s62, s57\n\t" "s_mul_i32 s74, s74, s58\n\t" "s_lshr_b32 s74, s74, 12\n\t" \
   "s_add_u32 s74, s74, s56\n\t" "s_lshl_b32 s60, s60, 3\n\t" "s_sub_u32 s75, s74, s60\n\t" "s_cmp_lt_u32 s75, 32\n\t" "s_cbranch_scc1 52f\n\t" \
   "s_cmp_lt_i32 s75, 0\n\t" "s_cselect_b32 s61, 0, 31\n\t" "s_add_u32 s74, s60, s61\n\t" "s_sub_i32 s75, s61, s75\n\t" "s_add_i32 s75, s75, 1\n\t" \
   "s_ashr_i32 s75, s75, 1\n\t" "s_min_i32 s75, s75, 8\n\t" "s_max_i32 s75, s75, -8\n\t" "s_mov_b32 s56, s74\n\t" "s_mov_b32 s57, s62\n\t" \
   "s_add_u32 s58, s58, s75\n\t" "52:\n\t" "s_sub_i32 s74, s73, s74\n\t" "s_cmp_le_i32 s74, 150\n\t" "s_cbranch_scc1 53f\n\t" \
   "s_cmp_lt_i32 s74, 260\n\t" "s_cbranch_scc1 51b\n\t" "s_sleep 2\n\t" "s_cmp_lt_i32 s74, 32000\n\t" "s_cbranch_scc1 51b\n\t" "s_mov_b32 s74, 0\n\t" \
   "53:\n\t" "s_sub_i32 s74, s74, 56\n\t" "s_max_i32 s74, s74, 0\n\t" "s_mul_i32 s74, s74, 23\n\t" "s_lshr_b32 s74, s74, 5\n\t" \
   "s_and_b32 s74, s74, -4\n\t" "s_getpc_b64 s[60:61]\n\t" "54:\n\t" "s_sub_u32 s74, 55f-54b, s74\n\t" "s_add_u32 s60, s60, s74\n\t" \
   "s_addc_u32 s61, s61, 0\n\t" "s_setpc_b64 s[60:61]\n\t" "s_nop 3\n\t" "s_nop 3\n\t" "s_nop 3\n\t" "s_nop 3\n\t" "s_nop 3\n\t" "s_nop 3\n\t" \
   "s_nop 3\n\t" "s_nop 3\n\t" "s_nop 3\n\t" "s_nop 3\n\t" "s_nop 3\n\t" "s_nop 3\n\t" "s_nop 3\n\t" "s_nop 3\n\t" "s_nop 3\n\t" "s_nop 3\n\t" \
   "s_nop 3\n\t" "s_nop 3\n\t" "s_nop 3\n\t" "s_nop 3\n\t" "s_nop 3\n\t" "s_nop 3\n\t" "s_nop 3\n\t" "s_nop 3\n\t" "55:\n\t" "20:\n\t" RSTAMP(4) \
   "buffer_load_dwordx4 v[64:67], v128, %[rs], s67 offen sc1\n\t" "buffer_load_dwordx4 v[68:71], v129, %[rs], s67 offen sc1\n\t" \
   "buffer_load_dwordx4 v[72:75], v130, %[rs], s67 offen sc1\n\t" "s_nop 7\n\t" "s_nop 7\n\t" "s_nop 7\n\t" "s_cmp_lg_u32 s70, 0\n\t" \
   "s_cbranch_scc1 27f\n\t" "buffer_load_dwordx4 v[16:19], v138, %[rs], s79 offen offset:0 sc1\n\t" \
   "buffer_load_dwordx4 v[20:23], v138, %[rs], s79 offen offset:1024 sc1\n\t" \
   "buffer_load_dwordx4 v[24:27], v138, %[rs], s79 offen offset:2048 sc1\n\t" \
   "buffer_load_dwordx4 v[28:31], v138, %[rs], s79 offen offset:3072 sc1\n\t" "v_or_b32 v108, s49, v141\n\t" "v_or_b32 v109, s49, v142\n\t" \
   "buffer_store_dwordx4 v[44:47], v108, %[rs], s52 offen sc1\n\t" "buffer_store_dwordx2 v[32:33], v109, %[rs], s51 offen sc1\n\t" "28:\n\t" RSTAMP(3) \
   "s_waitcnt vmcnt(8)\n\t" "ds_write_b128 v112, v[64:67]\n\t" "v_smfmac_f32_16x16x64_bf16 v[48:51], v[64:67], a[0:7], v134\n\t" \
   "s_waitcnt vmcnt(7)\n\t" "ds_write_b128 v113, v[68:71]\n\t" "s_waitcnt vmcnt(6)\n\t" "ds_write_b128 v114, v[72:75]\n\t" RSTAMP(7) "2:\n\t" \
   "v_smfmac_f32_16x16x64_bf16 v[52:55], v[64:67], a[64:71], v134\n\t" "v_max3_u32 v110, v64, v66, v68\n\t" "v_max3_u32 v111, v70, v72, v74\n\t" \
   "v_smfmac_f32_16x16x64_bf16 v[48:51], v[68:71], a[8:15], v134\n\t" "v_max_u32 v110, v110, v111\n\t" "v_cmp_eq_u32 vcc, -1, v110\n\t" \
   "v_smfmac_f32_16x16x64_bf16 v[52:55], v[68:71], a[72:79], v134\n\t" "s_cbranch_vccnz 8f\n\t" \
   "v_smfmac_f32_16x16x64_bf16 v[48:51], v[72:75], a[16:23], v134\n\t" "s_waitcnt lgkmcnt(0)\n\t" "s_barrier\n\t" RSTAMP(0) \
   "ds_read_b128 v[76:79], v115\n\t" "ds_read_b128 v[80:83], v116\n\t" "v_smfmac_f32_16x16x64_bf16 v[52:55], v[72:75], a[80:87], v134\n\t" \
   "ds_read_b128 v[84:87], v117\n\t" "ds_read_b128 v[88:91], v118\n\t" "v_smfmac_f32_16x16x64_bf16 v[56:59], v[64:67], a[128:135], v134\n\t" \
   "ds_read_b128 v[92:95], v119\n\t" "ds_read_b128 v[96:99], v120\n\t" "v_smfmac_f32_16x16x64_bf16 v[60:63], v[64:67], a[192:199], v134\n\t" \
   "ds_read_b128 v[100:103], v121\n\t" "v_smfmac_f32_16x16x64_bf16 v[56:59], v[68:71], a[136:143], v134\n\t" \
   "v_smfmac_f32_16x16x64_bf16 v[60:63], v[68:71], a[200:207], v134\n\t" "s_waitcnt lgkmcnt(6)\n\t" \
   "v_smfmac_f32_16x16x64_bf16 v[48:51], v[76:79], a[24:31], v134\n\t" "v_smfmac_f32_16x16x64_bf16 v[52:55], v[76:79], a[88:95], v134\n\t" \
   "s_waitcnt lgkmcnt(5)\n\t" "v_smfmac_f32_16x16x64_bf16 v[48:51], v[80:83], a[32:39], v134\n\t" \
   "v_smfmac_f32_16x16x64_bf16 v[52:55], v[80:83], a[96:103], v134\n\t" "s_waitcnt lgkmcnt(4)\n\t" \
   "v_smfmac_f32_16x16x64_bf16 v[48:51], v[84:87], a[40:47], v134\n\t" "v_smfmac_f32_16x16x64_bf16 v[52:55], v[84:87], a[104:111], v134\n\t" \
   "s_waitcnt lgkmcnt(3)\n\t" "v_smfmac_f32_16x16x64_bf16 v[48:51], v[88:91], a[48:55], v134\n\t" \
   "v_smfmac_f32_16x16x64_bf16 v[52:55], v[88:91], a[112:119], v134\n\t" "s_waitcnt lgkmcnt(2)\n\t" \
   "v_smfmac_f32_16x16x64_bf16 v[48:51], v[92:95], a[56:63], v134\n\t" "v_smfmac_f32_16x16x64_bf16 v[52:55], v[92:95], a[120:127], v134\n\t" \
   "s_waitcnt lgkmcnt(0)\n\t" "v_smfmac_f32_16x16x64_bf16 v[48:51], v[96:99], v[192:199], v134\n\t" \
   "v_smfmac_f32_16x16x64_bf16 v[48:51], v[100:103], v[200:207], v134\n\t" "v_smfmac_f32_16x16x64_bf16 v[52:55], v[96:99], v[208:215], v134\n\t" \
   "v_smfmac_f32_16x16x64_bf16 v[52:55], v[100:103], v[216:223], v134\n\t" "v_smfmac_f32_16x16x64_bf16 v[56:59], v[72:75], a[144:151], v134\n\t" \
   "v_add_f32 v48, v48, v49\n\t" "v_add_f32 v49, v50, v51\n\t" "v_smfmac_f32_16x16x64_bf16 v[60:63], v[72:75], a[208:215], v134\n\t" \
   "v_mul_f32 v48, 0x3e0293ee, v48\n\t" "v_mul_f32 v49, 0x3e0293ee, v49\n\t" "v_smfmac_f32_16x16x64_bf16 v[56:59], v[76:79], a[152:159], v134\n\t" \
   "v_add_f32 v52, v52, v53\n\t" "v_add_f32 v53, v54, v55\n\t" "v_smfmac_f32_16x16x64_bf16 v[56:59], v[80:83], a[160:167], v134\n\t" \
   "v_mul_f32 v52, 0x3e0293ee, v52\n\t" "v_mul_f32 v53, 0x3e0293ee, v53\n\t" "v_smfmac_f32_16x16x64_bf16 v[56:59], v[84:87], a[168:175], v134\n\t" \
   "v_cvt_pk_bf16_f32 v48, v48, v49\n\t" "ds_write_b32 v135, v48 offset:10240\n\t" \
   "v_smfmac_f32_16x16x64_bf16 v[56:59], v[88:91], a[176:183], v134\n\t" "v_cvt_pk_bf16_f32 v52, v52, v53\n\t" \
   "ds_write_b32 v135, v52 offset:10496\n\t" "v_smfmac_f32_16x16x64_bf16 v[56:59], v[92:95], a[184:191], v134\n\t" \
   "v_smfmac_f32_16x16x64_bf16 v[56:59], v[96:99], v[224:231], v134\n\t" "v_smfmac_f32_16x16x64_bf16 v[56:59], v[100:103], v[232:239], v134\n\t" \
   "s_waitcnt lgkmcnt(0)\n\t" "s_barrier\n\t" "ds_read_b64_tr_b16 v[64:65], v136 offset:10240\n\t" "ds_read_b64_tr_b16 v[66:67], v136 offset:10496\n\t" \
   "v_smfmac_f32_16x16x64_bf16 v[60:63], v[76:79], a[216:223], v134\n\t" "ds_read_b64_tr_b16 v[68:69], v136 offset:10752\n\t" \
   "v_smfmac_f32_16x16x64_bf16 v[60:63], v[80:83], a[224:231], v134\n\t" "ds_read_b64_tr_b16 v[70:71], v136 offset:11008\n\t" \
   "v_smfmac_f32_16x16x64_bf16 v[60:63], v[84:87], a[232:239], v134\n\t" "ds_read_b64_tr_b16 v[72:73], v136 offset:11264\n\t" \
   "v_smfmac_f32_16x16x64_bf16 v[60:63], v[88:91], a[240:247], v134\n\t" "ds_read_b64_tr_b16 v[74:75], v136 offset:11520\n\t" \
   "v_add_f32 v56, v56, v57\n\t" "v_add_f32 v57, v58, v59\n\t" "v_smfmac_f32_16x16x64_bf16 v[60:63], v[92:95], a[248:255], v134\n\t" \
   "ds_read_b64_tr_b16 v[104:105], v136 offset:11776\n\t" "ds_read_b64_tr_b16 v[106:107], v136 offset:12032\n\t" \
   "v_smfmac_f32_16x16x64_bf16 v[60:63], v[96:99], v[240:247], v134\n\t" "v_cvt_pk_bf16_f32 v56, v56, v57\n\t" \
   "ds_write_b32 v135, v56 offset:12288\n\t" "v_smfmac_f32_16x16x64_bf16 v[60:63], v[100:103], v[248:255], v134\n\t" RSTAMP(6) \
   "s_waitcnt lgkmcnt(7)\n\t" "v_mfma_f32_16x16x32_bf16 v[48:51], v[160:163], v[64:67], 0\n\t" \
   "v_mfma_f32_16x16x32_bf16 v[52:55], v[176:179], v[64:67], 0\n\t" "s_waitcnt lgkmcnt(3)\n\t" "s_nop 0\n\t" "v_add_f32 v60, v60, v61\n\t" \
   "v_add_f32 v61, v62, v63\n\t" "v_cvt_pk_bf16_f32 v60, v60, v61\n\t" "ds_write_b32 v135, v60 offset:12544\n\t" \
   "v_mfma_f32_16x16x32_bf16 v[48:51], v[164:167], v[68:71], v[48:51]\n\t" "v_mfma_f32_16x16x32_bf16 v[52:55], v[180:183], v[68:71], v[52:55]\n\t" \
   "v_mfma_f32_16x16x32_bf16 v[48:51], v[168:171], v[72:75], v[48:51]\n\t" "v_mfma_f32_16x16x32_bf16 v[52:55], v[184:187], v[72:75], v[52:55]\n\t" \
   "s_waitcnt lgkmcnt(2)\n\t" "v_mfma_f32_16x16x32_bf16 v[48:51], v[172:175], v[104:107], v[48:51]\n\t" \
   "v_mfma_f32_16x16x32_bf16 v[52:55], v[188:191], v[104:107], v[52:55]\n\t" "s_waitcnt lgkmcnt(0)\n\t" "s_barrier\n\t" \
   "ds_read_b64_tr_b16 v[144:145], v136 offset:12288\n\t" "ds_read_b64_tr_b16 v[146:147], v136 offset:12544\n\t" \
   "ds_read_b64_tr_b16 v[148:149], v136 offset:12800\n\t" "ds_read_b64_tr_b16 v[150:151], v136 offset:13056\n\t" \
   "ds_read_b64_tr_b16 v[152:153], v136 offset:13312\n\t" "ds_read_b64_tr_b16 v[154:155], v136 offset:13568\n\t" \
   "ds_read_b64_tr_b16 v[156:157], v136 offset:13824\n\t" "ds_read_b64_tr_b16 v[158:159], v136 offset:14080\n\t" "s_cmp_eq_u32 s64, 0\n\t" \
   "s_cbranch_scc1 92f\n\t" "93:\n\t" RSTAMP(1) "v_cndmask_b32 v48, v48, v52, %[hi8]\n\t" "v_cndmask_b32 v49, v49, v53, %[hi8]\n\t" \
   "v_cndmask_b32 v50, v50, v54, %[hi8]\n\t" "v_cndmask_b32 v51, v51, v55, %[hi8]\n\t" "v_exp_f32 v80, v48\n\t" "v_exp_f32 v81, v49\n\t" \
   "s_waitcnt lgkmcnt(6)\n\t" "v_mfma_f32_16x16x32_bf16 v[76:79], v[144:147], v[64:67], 0\n\t" "v_exp_f32 v82, v50\n\t" "v_exp_f32 v83, v51\n\t" \
   "s_waitcnt lgkmcnt(4)\n\t" "v_mfma_f32_16x16x32_bf16 v[76:79], v[148:151], v[68:71], v[76:79]\n\t" "v_add_f32 v96, v80, v81\n\t" \
   "v_add_f32 v103, v82, v83\n\t" "s_waitcnt lgkmcnt(2)\n\t" "v_mfma_f32_16x16x32_bf16 v[76:79], v[152:155], v[72:75], v[76:79]\n\t" \
   "v_add_f32 v94, v96, v103\n\t" "v_add_f32 v96, v96, v103\n\t" "s_waitcnt lgkmcnt(0)\n\t" \
   "v_mfma_f32_16x16x32_bf16 v[76:79], v[156:159], v[104:107], v[76:79]\n\t" "v_permlane32_swap_b32 v96, v94\n\t" "v_add_f32 v96, v96, v94\n\t" \
   "s_nop 1\n\t" "v_add_f32_dpp v96, v96, v96 row_ror:8 row_mask:0xf bank_mask:0xf\n\t" "s_nop 2\n\t" "v_exp_f32 v88, v76\n\t" "v_exp_f32 v89, v77\n\t" \
   "v_add_f32 v103, v96, v88\n\t" "v_exp_f32 v90, v78\n\t" "v_add_f32 v103, v103, v89\n\t" "v_exp_f32 v91, v79\n\t" "v_add_f32 v103, v103, v90\n\t" \
   "v_add_f32 v97, v103, v91\n\t" "v_add_f32 v103, v103, v91\n\t" "s_nop 1\n\t" "v_permlane16_swap_b32 v103, v97\n\t" "v_add_f32 v103, v103, v97\n\t" \
   "v_rcp_f32 v100, v103\n\t" "v_add_u32 v94, 0xe0800000, v103\n\t" "v_pk_mul_f32 v[80:81], v[80:81], v[100:101] op_sel_hi:[1,0]\n\t" \
   "v_cmp_lt_u32 vcc, 0x40000000, v94\n\t" "v_pk_mul_f32 v[82:83], v[82:83], v[100:101] op_sel_hi:[1,0]\n\t" "s_cbranch_vccnz 70f\n\t" \
   "v_cvt_pk_bf16_f32 v48, v80, v81\n\t" "v_cvt_pk_bf16_f32 v49, v82, v83\n\t" "buffer_store_dwordx2 v[48:49], v132, %[rs], s55 offen sc1\n\t" \
   "v_pk_mul_f32 v[88:89], v[88:89], v[100:101] op_sel_hi:[1,0]\n\t" "v_pk_mul_f32 v[90:91], v[90:91], v[100:101] op_sel_hi:[1,0]\n\t" \
   "v_cvt_pk_bf16_f32 v52, v88, v89\n\t" "v_cvt_pk_bf16_f32 v53, v90, v91\n\t" "71:\n\t" \
   "buffer_store_dwordx2 v[52:53], v133, %[rs], s65 offen sc1\n\t" "s_memtime s[62:63]\n\t" "s_memrealtime s[60:61]\n\t" \
   "buffer_store_dwordx2 v[48:49], v132, %[rs], s50 offen sc1\n\t" RSTAMP(2) "s_branch 40b\n\t" "70:\n\t" "v_max3_f32 v94, v48, v49, v50\n\t" \
   "v_max3_f32 v102, v76, v77, v78\n\t" "v_max3_f32 v94, v94, v51, v79\n\t" "v_max_f32 v92, v94, v102\n\t" "s_nop 1\n\t" \
   "v_max_f32_dpp v102, v92, v92 row_ror:8 row_mask:0xf bank_mask:0xf\n\t" "v_max_f32_dpp v94, v92, v92 row_ror:8 row_mask:0xf bank_mask:0xf\n\t" \
   "s_nop 1\n\t" "v_permlane32_swap_b32 v102, v94\n\t" "v_max_f32 v95, v102, v94\n\t" "v_max_f32 v102, v102, v94\n\t" "s_nop 1\n\t" \
   "v_permlane16_swap_b32 v102, v95\n\t" "v_max_f32 v102, v102, v95\n\t" "v_sub_f32 v80, v48, v102\n\t" "v_sub_f32 v88, v76, v102\n\t" \
   "v_sub_f32 v81, v49, v102\n\t" "v_sub_f32 v89, v77, v102\n\t" "v_sub_f32 v82, v50, v102\n\t" "v_sub_f32 v90, v78, v102\n\t" \
   "v_sub_f32 v83, v51, v102\n\t" "v_sub_f32 v91, v79, v102\n\t" "v_exp_f32 v80, v80\n\t" "v_exp_f32 v88, v88\n\t" "v_exp_f32 v81, v81\n\t" \
   "v_exp_f32 v89, v89\n\t" "v_exp_f32 v82, v82\n\t" "v_exp_f32 v90, v90\n\t" "v_exp_f32 v83, v83\n\t" "v_exp_f32 v91, v91\n\t" "s_nop 0\n\t" \
   "v_add_f32 v96, v80, v81\n\t" "v_add_f32 v103, v82, v83\n\t" "v_add_f32 v94, v96, v103\n\t" "v_add_f32 v96, v96, v103\n\t" "s_nop 1\n\t" \
   "v_permlane32_swap_b32 v96, v94\n\t" "v_add_f32 v96, v96, v94\n\t" "s_nop 1\n\t" \
   "v_add_f32_dpp v96, v96, v96 row_ror:8 row_mask:0xf bank_mask:0xf\n\t" "v_add_f32 v103, v96, v88\n\t" "v_add_f32 v103, v103, v89\n\t" \
   "v_add_f32 v103, v103, v90\n\t" "v_add_f32 v97, v103, v91\n\t" "v_add_f32 v103, v103, v91\n\t" "s_nop 1\n\t" "v_permlane16_swap_b32 v103, v97\n\t" \
   "v_add_f32 v103, v103, v97\n\t" "v_rcp_f32 v100, v103\n\t" "s_nop 0\n\t" "v_pk_mul_f32 v[80:81], v[80:81], v[100:101] op_sel_hi:[1,0]\n\t" \
   "v_pk_mul_f32 v[82:83], v[82:83], v[100:101] op_sel_hi:[1,0]\n\t" "v_pk_mul_f32 v[88:89], v[88:89], v[100:101] op_sel_hi:[1,0]\n\t" \
   "v_pk_mul_f32 v[90:91], v[90:91], v[100:101] op_sel_hi:[1,0]\n\t" "v_cvt_pk_bf16_f32 v48, v80, v81\n\t" "v_cvt_pk_bf16_f32 v49, v82, v83\n\t" \
   "v_cvt_pk_bf16_f32 v52, v88, v89\n\t" "v_cvt_pk_bf16_f32 v53, v90, v91\n\t" "v_min_u32 v48, -2, v48\n\t" "v_min_u32 v52, -2, v52\n\t" \
   "buffer_store_dwordx2 v[48:49], v132, %[rs], s55 offen sc1\n\t" "s_branch 71b\n\t" "74:\n\t" "v_max3_f32 v80, v32, v33, v34\n\t" \
   "v_max3_f32 v81, v35, v36, v37\n\t" "v_max3_f32 v82, v38, v39, v40\n\t" "v_max3_f32 v83, v41, v42, v43\n\t" "v_max3_f32 v80, v80, v81, v82\n\t" \
   "v_max_f32 v81, v80, v83\n\t" "v_max_f32 v80, v80, v83\n\t" "s_nop 1\n\t" "v_permlane16_swap_b32 v80, v81\n\t" "v_max_f32 v82, v80, v81\n\t" \
   "v_max_f32 v80, v80, v81\n\t" "s_nop 1\n\t" "v_permlane32_swap_b32 v80, v82\n\t" "v_max_f32 v80, v80, v82\n\t" "v_sub_f32 v64, v32, v80\n\t" \
   "v_sub_f32 v65, v33, v80\n\t" "v_sub_f32 v66, v34, v80\n\t" "v_sub_f32 v67, v35, v80\n\t" "v_sub_f32 v68, v36, v80\n\t" \
   "v_sub_f32 v69, v37, v80\n\t" "v_sub_f32 v70, v38, v80\n\t" "v_sub_f32 v71, v39, v80\n\t" "v_sub_f32 v72, v40, v80\n\t" \
   "v_sub_f32 v73, v41, v80\n\t" "v_sub_f32 v74, v42, v80\n\t" "v_sub_f32 v75, v43, v80\n\t" "v_exp_f32 v64, v64\n\t" "v_exp_f32 v65, v65\n\t" \
   "v_exp_f32 v66, v66\n\t" "v_exp_f32 v67, v67\n\t" "v_exp_f32 v68, v68\n\t" "v_exp_f32 v69, v69\n\t" "v_exp_f32 v70, v70\n\t" \
   "v_exp_f32 v71, v71\n\t" "v_exp_f32 v72, v72\n\t" "v_exp_f32 v73, v73\n\t" "v_exp_f32 v74, v74\n\t" "v_exp_f32 v75, v75\n\t" "s_nop 0\n\t" \
   "v_add_f32 v80, v64, v65\n\t" "v_add_f32 v81, v66, v67\n\t" "v_add_f32 v82, v68, v69\n\t" "v_add_f32 v83, v70, v71\n\t" \
   "v_add_f32 v84, v72, v73\n\t" "v_add_f32 v85, v74, v75\n\t" "v_add_f32 v80, v80, v81\n\t" "v_add_f32 v82, v82, v83\n\t" \
   "v_add_f32 v84, v84, v85\n\t" "v_add_f32 v80, v80, v82\n\t" "v_fma_f32 v82, v80, 2.0, v84\n\t" "v_fma_f32 v80, v80, 2.0, v84\n\t" "s_nop 0\n\t" \
   "s_nop 0\n\t" "v_permlane16_swap_b32 v80, v82\n\t" "v_add_f32 v81, v80, v82\n\t" "v_add_f32 v80, v80, v82\n\t" "s_nop 0\n\t" "s_nop 0\n\t" \
   "v_permlane32_swap_b32 v80, v81\n\t" "v_add_f32 v80, v80, v81\n\t" "v_rcp_f32 v90, v80\n\t" "s_nop 0\n\t" "v_add_f32 v90, v90, v90\n\t" \
   "s_branch 75b\n\t" "63:\n\t" "s_cmp_lg_u32 s64, 0\n\t" "s_cbranch_scc1 64f\n\t" "s_sub_u32 s69, s56, 0x10000\n\t" "s_and_b32 s69, s69, -2\n\t" \
   "s_branch 50b\n\t" "64:\n\t" "s_mov_b32 s69, s48\n\t" "s_cmp_lt_u32 s64, 3\n\t" "s_cselect_b32 s69, -1, s69\n\t" "s_cmp_lg_u32 s69, -1\n\t" \
   "s_cbranch_scc1 50b\n\t" "s_add_u32 s76, s76, 0x1000000\n\t" "s_mov_b32 s73, 0\n\t" "65:\n\t" "s_add_u32 s73, s73, 1\n\t" \
   "s_min_u32 s72, s73, 7\n\t" "s_mul_i32 s72, s72, 5\n\t" "s_add_u32 s72, s72, %[xs]\n\t" "s_and_b32 s72, s72, 7\n\t" "s_lshl_b32 s71, %[xs], 7\n\t" \
   "v_subrev_u32 v109, s71, v139\n\t" "s_lshl_b32 s72, s72, 7\n\t" "s_add_u32 s72, s72, s66\n\t" "s_cmp_lt_u32 s73, 7\n\t" "s_cbranch_scc1 66f\n\t" \
   "s_sleep 4\n\t" "66:\n\t" "buffer_load_dword v108, v109, %[rs], s72 offen sc1\n\t" "s_waitcnt vmcnt(0)\n\t" "v_readfirstlane_b32 s69, v108\n\t" \
   "s_cmp_gt_u32 s73, 0x10000\n\t" "s_cbranch_scc1 50b\n\t" "s_cmp_eq_u32 s69, -1\n\t" "s_cbranch_scc1 65b\n\t" "s_branch 50b\n\t" "8:\n\t" \
   "s_nop 7\n\t" "s_waitcnt vmcnt(6) lgkmcnt(0)\n\t" "s_add_u32 s70, s70, 1\n\t" "s_cmp_gt_u32 s70, 2\n\t" "s_cbranch_scc1 81f\n\t" \
   "s_lshl_b32 s71, s70, 3\n\t" "s_lshl_b32 s71, 1, s71\n\t" "s_add_u32 s76, s76, s71\n\t" "81:\n\t" "v_mov_b32 v48, 0\n\t" "v_mov_b32 v49, 0\n\t" \
   "v_mov_b32 v50, 0\n\t" "v_mov_b32 v51, 0\n\t" "v_mov_b32 v52, 0\n\t" "v_mov_b32 v53, 0\n\t" "v_mov_b32 v54, 0\n\t" "v_mov_b32 v55, 0\n\t" \
   "v_mov_b32 v56, 0\n\t" "v_mov_b32 v57, 0\n\t" "v_mov_b32 v58, 0\n\t" "v_mov_b32 v59, 0\n\t" "v_mov_b32 v60, 0\n\t" "v_mov_b32 v61, 0\n\t" \
   "v_mov_b32 v62, 0\n\t" "v_mov_b32 v63, 0\n\t" "s_min_u32 s71, s70, 7\n\t" "s_lshr_b32 s67, s71, 1\n\t" "s_add_u32 s67, s67, %[xs]\n\t" \
   "s_and_b32 s67, s67, 3\n\t" "s_lshl_b32 s67, s67, 11\n\t" "s_and_b32 s72, s71, 1\n\t" "s_lshl_b32 s72, s72, 14\n\t" "s_add_u32 s67, s67, s72\n\t" \
   "s_add_u32 s67, s67, s66\n\t" "s_cmp_eq_u32 s70, 1\n\t" "s_cselect_b32 s67, s68, s67\n\t" "s_cmp_lt_u32 s70, 7\n\t" "s_cbranch_scc1 20b\n\t" \
   "s_sleep 3\n\t" "s_cmp_lt_u32 s70, 0x20000\n\t" "s_cbranch_scc1 20b\n\t" "s_mov_b32 s67, %[esc]\n\t" "s_mov_b32 s68, %[esc]\n\t" "s_branch 20b\n\t" \
   "27:\n\t" "v_mov_b32 v98, 0x7ffffff0\n\t" "buffer_store_dword v98, v98, %[rs], 0 offen\n\t" "buffer_store_dword v98, v98, %[rs], 0 offen\n\t" \
   "buffer_store_dword v98, v98, %[rs], 0 offen\n\t" "buffer_store_dword v98, v98, %[rs], 0 offen\n\t" \
   "buffer_store_dword v98, v98, %[rs], 0 offen\n\t" "buffer_store_dword v98, v98, %[rs], 0 offen\n\t" "s_branch 28b\n\t" "141:\n\t" \
   "s_mov_b32 s74, 0\n\t" "s_cmp_eq_u32 s64, 0\n\t" "s_cbranch_scc1 41b\n\t" "42:\n\t" "buffer_load_dword v15, v140, %[rs], s59 offen sc1\n\t" \
   "s_add_u32 s74, s74, 1\n\t" "s_waitcnt vmcnt(0)\n\t" "v_cmp_eq_u32 vcc, -1, v15\n\t" "s_cmp_gt_u32 s74, 0x10000\n\t" "s_cbranch_scc1 41b\n\t" \
   "s_cbranch_vccz 41b\n\t" "s_sleep 2\n\t" "s_branch 42b\n\t" "95:\n\t" "s_movk_i32 s59, 0x1000\n\t" "s_branch 197f\n\t" "97:\n\t" \
   "buffer_load_dwordx4 v[16:19], v138, %[rs], s79 offen offset:0 sc1\n\t" "buffer_load_dwordx4 v[20:23], v138, %[rs], s79 offen offset:1024 sc1\n\t" \
   "buffer_load_dwordx4 v[24:27], v138, %[rs], s79 offen offset:2048 sc1\n\t" \
   "buffer_load_dwordx4 v[28:31], v138, %[rs], s79 offen offset:3072 sc1\n\t" "v_or_b32 v108, s49, v141\n\t" "v_or_b32 v109, s49, v142\n\t" \
   "buffer_store_dwordx4 v[44:47], v108, %[rs], s52 offen sc1\n\t" "buffer_store_dwordx2 v[32:33], v109, %[rs], s51 offen sc1\n\t" \
   "s_waitcnt vmcnt(0)\n\t" "s_branch 96b\n\t" "197:\n\t" "s_mov_b32 s74, 0\n\t" "98:\n\t" "buffer_load_dword v15, v140, %[rs], s59 offen sc1\n\t" \
   "s_add_u32 s74, s74, 1\n\t" "s_waitcnt vmcnt(0)\n\t" "v_cmp_eq_u32 vcc, -1, v15\n\t" "s_cmp_gt_u32 s74, 0x10000\n\t" "s_cbranch_scc1 97b\n\t" \
   "s_cbranch_vccz 97b\n\t" "s_sleep 2\n\t" "s_branch 98b\n\t" "92:\n\t" "s_mov_b32 s59, 0\n\t" "s_branch 145f\n\t" "45:\n\t" \
   "v_mbcnt_lo_u32_b32 v98, -1, 0\n\t" "v_mbcnt_hi_u32_b32 v98, -1, v98\n\t" "v_lshlrev_b32 v98, 4, v98\n\t" "s_add_u32 s72, %[kf0], 4096\n\t" \
   "buffer_load_dwordx4 v[160:163], v98, %[rs], %[kf0] offen offset:0 sc1\n\t" \
   "buffer_load_dwordx4 v[164:167], v98, %[rs], %[kf0] offen offset:1024 sc1\n\t" \
   "buffer_load_dwordx4 v[168:171], v98, %[rs], %[kf0] offen offset:2048 sc1\n\t" \
   "buffer_load_dwordx4 v[172:175], v98, %[rs], %[kf0] offen offset:3072 sc1\n\t" \
   "buffer_load_dwordx4 v[176:179], v98, %[rs], s72 offen offset:0 sc1\n\t" "buffer_load_dwordx4 v[180:183], v98, %[rs], s72 offen offset:1024 sc1\n\t" \
   "buffer_load_dwordx4 v[184:187], v98, %[rs], s72 offen offset:2048 sc1\n\t" \
   "buffer_load_dwordx4 v[188:191], v98, %[rs], s72 offen offset:3072 sc1\n\t" "s_waitcnt vmcnt(0)\n\t" \
   "v_mfma_f32_16x16x32_bf16 v[48:51], v[160:163], v[64:67], 0\n\t" "v_mfma_f32_16x16x32_bf16 v[52:55], v[176:179], v[64:67], 0\n\t" \
   "v_mfma_f32_16x16x32_bf16 v[48:51], v[164:167], v[68:71], v[48:51]\n\t" "v_mfma_f32_16x16x32_bf16 v[52:55], v[180:183], v[68:71], v[52:55]\n\t" \
   "v_mfma_f32_16x16x32_bf16 v[48:51], v[168:171], v[72:75], v[48:51]\n\t" "v_mfma_f32_16x16x32_bf16 v[52:55], v[184:187], v[72:75], v[52:55]\n\t" \
   "v_mfma_f32_16x16x32_bf16 v[48:51], v[172:175], v[104:107], v[48:51]\n\t" "v_mfma_f32_16x16x32_bf16 v[52:55], v[188:191], v[104:107], v[52:55]\n\t" \
   "s_nop 7\n\t" "s_branch 93b\n\t" "145:\n\t" "s_mov_b32 s74, 0\n\t" "46:\n\t" "buffer_load_dword v15, v140, %[rs], s59 offen sc1\n\t" \
   "s_add_u32 s74, s74, 1\n\t" "s_waitcnt vmcnt(0)\n\t" "v_cmp_eq_u32 vcc, -1, v15\n\t" "s_cmp_gt_u32 s74, 0x10000\n\t" "s_cbranch_scc1 45b\n\t" \
   "s_cbranch_vccz 45b\n\t" "s_sleep 2\n\t" "s_branch 46b\n\t" "90:\n\t" "v_or_b32 v108, s49, v141\n\t" "v_or_b32 v109, s49, v142\n\t" \
   "buffer_store_dwordx4 v[44:47], v108, %[rs], s52 offen sc1\n\t" "buffer_store_dwordx2 v[32:33], v109, %[rs], s51 offen sc1\n\t" \
   "s_waitcnt vmcnt(0) lgkmcnt(0)"
#define P1_CLOB "v15", "v16", "v17", "v18", "v19", "v20", "v21", "v22", "v23", "v24", "v25", "v26", "v27", "v28", "v29", "v30", "v31", "v32", "v33", "v34", "v35", "v36", "v37", "v38", "v39", "v40", "v41", "v42", "v43", "v44", "v45", "v46", "v47", "v48", "v49", "v50", "v51", "v52", "v53", "v54", "v55", "v56", "v57", "v58", "v59", "v60", "v61", "v62", "v63", "v64", "v65", "v66", "v67", "v68", "v69", "v70", "v71", "v72", "v73", "v74", "v75", "v76", "v77", "v78", "v79", "v80", "v81", "v82", "v83", "v84", "v85", "v86", "v87", "v88", "v89", "v90", "v91", "v92", "v93", "v94", "v95", "v96", "v97", "v98", "v99", "v100", "v101", "v102", "v103", "v104", "v105", "v106", "v107", "v108", "v109", "v110", "v111", "v144", "v145", "v146", "v147", "v148", "v149", "v150", "v151", "v152", "v153", "v154", "v155", "v156", "v157", "v158", "v159", "v160", "v161", "v162", "v163", "v164", "v165", "v166", "v167", "v168", "v169", "v170", "v171", "v172", "v173", "v174", "v175", "v176", "v177", "v178", "v179", "v180", "v181", "v182", "v183", "v184", "v185", "v186", "v187", "v188", "v189", "v190", "v191", "s60", "s61", "s62", "s63", "s64", "s65", "s66", "s67", "s68", "s69", "s70", "s71", "s72", "s73", "s74", "s75", "s47", "s48", "s49", "s50", "s51", "s52", "s54", "s55", "s79", "s20", "s21", "s22", "s23", "s24", "s25", "s26", "s27", "s28", "s29", "s30", "s31", "s32", "s33", "s34", "s35"
#define PV_ASM \
   "s_mov_b32 s64, -1\n\t" "s_mov_b32 s76, 0\n\t" "s_mov_b32 s77, 0\n\t" "s_mov_b32 s71, 63\n\t" "s_mov_b32 s47, 1300\n\t" "s_mov_b32 s70, 0\n\t" \
   "s_mov_b32 s69, 0\n\t" "s_lshr_b64 s[72:73], %[sel], s71\n\t" "s_lshl_b32 s65, s71, 19\n\t" "s_and_b32 s72, s72, 1\n\t" \
   "s_add_u32 s65, s65, %[pool]\n\t" "s_lshl_b32 s72, s72, 13\n\t" "s_add_u32 s65, s65, s72\n\t" "s_lshr_b64 s[72:73], %[s3], s71\n\t" \
   "s_and_b32 s54, s72, 1\n\t" "s_lshl_b32 s54, s54, 14\n\t" "40:\n\t" "s_cmp_lt_i32 s64, 0\n\t" "s_cselect_b32 s72, 0x7ffffff0, 0\n\t" \
   "s_max_i32 s71, s64, 0\n\t" "v_or_b32 v109, s72, v143\n\t" "s_lshl_b32 s71, s71, 12\n\t" "v_mov_b32 v110, 1\n\t" "s_cmp_eq_u32 s64, 63\n\t" \
   "s_cbranch_scc1 70f\n\t" "71:\n\t" "s_waitcnt vmcnt(0)\n\t" RSTAMP(3) "s_barrier\n\t" "buffer_load_dword v108, v139, %[rs], s65 offen sc1\n\t" \
   "buffer_store_dword v110, v109, %[rs], s71 offen sc1\n\t" PVTRACE "s_add_u32 s48, s69, s47\n\t" "s_cmp_eq_u32 s70, 0\n\t" \
   "s_cselect_b32 s48, s48, -1\n\t" "s_mov_b32 s66, s65\n\t" "s_add_i32 s64, s64, 1\n\t" "s_and_b32 s71, s64, 63\n\t" \
   "s_lshr_b64 s[72:73], %[sel], s71\n\t" "s_lshl_b32 s65, s71, 19\n\t" "s_and_b32 s72, s72, 1\n\t" "s_add_u32 s65, s65, %[pool]\n\t" \
   "s_lshl_b32 s72, s72, 13\n\t" "s_add_u32 s65, s65, s72\n\t" "s_xor_b32 s67, s54, %[lad0]\n\t" "s_xor_b32 s68, s67, 0x4000\n\t" \
   "s_add_u32 s67, s67, s66\n\t" "s_add_u32 s68, s68, s66\n\t" "s_lshr_b64 s[72:73], %[s3], s71\n\t" "s_and_b32 s54, s72, 1\n\t" \
   "s_lshl_b32 s54, s54, 14\n\t" "s_mov_b32 s70, 0\n\t" "s_lshr_b64 s[72:73], %[ysel], s71\n\t" "s_mul_i32 s73, s71, 43\n\t" \
   "s_mul_i32 s49, s71, 80\n\t" "s_and_b32 s72, s72, 1\n\t" "s_lshr_b32 s73, s73, 7\n\t" "s_add_u32 s49, s49, s72\n\t" \
   "s_lshl4_add_u32 s49, s73, s49\n\t" "s_lshl_b32 s49, s49, 13\n\t" "s_add_u32 s49, s49, %[yb]\n\t" "s_add_u32 s50, s49, 32768\n\t" \
   "s_add_u32 s51, s49, 65536\n\t" "s_add_u32 s52, s49, 98304\n\t" "s_add_u32 s53, s49, 131072\n\t" "v_mov_b32 v48, 0\n\t" "v_mov_b32 v49, 0\n\t" \
   "v_mov_b32 v50, 0\n\t" "v_mov_b32 v51, 0\n\t" "v_mov_b32 v52, 0\n\t" "v_mov_b32 v53, 0\n\t" "v_mov_b32 v54, 0\n\t" "v_mov_b32 v55, 0\n\t" \
   "v_mov_b32 v144, 0\n\t" "v_mov_b32 v145, 0\n\t" "v_mov_b32 v146, 0\n\t" "v_mov_b32 v147, 0\n\t" "v_mov_b32 v148, 0\n\t" "v_mov_b32 v149, 0\n\t" \
   "v_mov_b32 v150, 0\n\t" "v_mov_b32 v151, 0\n\t" "v_mov_b32 v152, 0\n\t" "v_mov_b32 v153, 0\n\t" "v_mov_b32 v154, 0\n\t" "v_mov_b32 v155, 0\n\t" \
   "v_mov_b32 v156, 0\n\t" "v_mov_b32 v157, 0\n\t" "v_mov_b32 v158, 0\n\t" "v_mov_b32 v159, 0\n\t" "v_mov_b32 v160, 0\n\t" "v_mov_b32 v161, 0\n\t" \
   "v_mov_b32 v162, 0\n\t" "v_mov_b32 v163, 0\n\t" "s_cmp_eq_u32 s64, 64\n\t" "s_cbranch_scc1 90f\n\t" "s_waitcnt vmcnt(1)\n\t" \
   "v_readlane_b32 s69, v108, 0\n\t" "v_readlane_b32 s71, v108, 1\n\t" "s_cmp_eq_u32 s69, -1\n\t" "s_cbranch_scc1 63f\n\t" "s_cmp_eq_u32 s71, -1\n\t" \
   "s_cselect_b32 s47, s47, s71\n\t" "50:\n\t" "s_add_u32 s73, s69, %[m1c]\n\t" RSTAMP(5) "51:\n\t" "s_memrealtime s[60:61]\n\t" \
   "s_memtime s[62:63]\n\t" "s_waitcnt lgkmcnt(0)\n\t" "s_sub_u32 s74, s62, s57\n\t" "s_mul_i32 s74, s74, s58\n\t" "s_lshr_b32 s74, s74, 12\n\t" \
   "s_add_u32 s74, s74, s56\n\t" "s_lshl_b32 s60, s60, 3\n\t" "s_sub_u32 s75, s74, s60\n\t" "s_cmp_lt_u32 s75, 32\n\t" "s_cbranch_scc1 52f\n\t" \
   "s_cmp_lt_i32 s75, 0\n\t" "s_cselect_b32 s61, 0, 31\n\t" "s_add_u32 s74, s60, s61\n\t" "s_sub_i32 s75, s61, s75\n\t" "s_add_i32 s75, s75, 1\n\t" \
   "s_ashr_i32 s75, s75, 1\n\t" "s_min_i32 s75, s75, 8\n\t" "s_max_i32 s75, s75, -8\n\t" "s_mov_b32 s56, s74\n\t" "s_mov_b32 s57, s62\n\t" \
   "s_add_u32 s58, s58, s75\n\t" "52:\n\t" "s_sub_i32 s74, s73, s74\n\t" "s_cmp_le_i32 s74, 150\n\t" "s_cbranch_scc1 53f\n\t" \
   "s_cmp_lt_i32 s74, 260\n\t" "s_cbranch_scc1 51b\n\t" "s_sleep 2\n\t" "s_cmp_lt_i32 s74, 32000\n\t" "s_cbranch_scc1 51b\n\t" "s_mov_b32 s74, 0\n\t" \
   "53:\n\t" "s_sub_i32 s74, s74, 56\n\t" "s_max_i32 s74, s74, 0\n\t" "s_mul_i32 s74, s74, 23\n\t" "s_lshr_b32 s74, s74, 5\n\t" \
   "s_and_b32 s74, s74, -4\n\t" "s_getpc_b64 s[60:61]\n\t" "54:\n\t" "s_sub_u32 s74, 55f-54b, s74\n\t" "s_add_u32 s60, s60, s74\n\t" \
   "s_addc_u32 s61, s61, 0\n\t" "s_setpc_b64 s[60:61]\n\t" "s_nop 3\n\t" "s_nop 3\n\t" "s_nop 3\n\t" "s_nop 3\n\t" "s_nop 3\n\t" "s_nop 3\n\t" \
   "s_nop 3\n\t" "s_nop 3\n\t" "s_nop 3\n\t" "s_nop 3\n\t" "s_nop 3\n\t" "s_nop 3\n\t" "s_nop 3\n\t" "s_nop 3\n\t" "s_nop 3\n\t" "s_nop 3\n\t" \
   "s_nop 3\n\t" "s_nop 3\n\t" "s_nop 3\n\t" "s_nop 3\n\t" "s_nop 3\n\t" "s_nop 3\n\t" "s_nop 3\n\t" "s_nop 3\n\t" "55:\n\t" "20:\n\t" RSTAMP(4) \
   "buffer_load_dwordx4 v[64:67], v128, %[rs], s67 offen sc1\n\t" "buffer_load_dwordx4 v[68:71], v129, %[rs], s67 offen sc1\n\t" \
   "buffer_load_dwordx4 v[72:75], v130, %[rs], s67 offen sc1\n\t" "s_waitcnt vmcnt(2)\n\t" "ds_write_b128 v112, v[64:67]\n\t" \
   "v_smfmac_f32_16x16x64_bf16 v[48:51], v[64:67], a[0:7], v134\n\t" "s_waitcnt vmcnt(1)\n\t" "ds_write_b128 v113, v[68:71]\n\t" \
   "s_waitcnt vmcnt(0)\n\t" "ds_write_b128 v114, v[72:75]\n\t" RSTAMP(7) "v_smfmac_f32_16x16x64_bf16 v[52:55], v[64:67], a[64:71], v134\n\t" \
   "v_max3_u32 v110, v64, v66, v68\n\t" "v_max3_u32 v111, v70, v72, v74\n\t" "v_smfmac_f32_16x16x64_bf16 v[48:51], v[68:71], a[8:15], v134\n\t" \
   "v_max_u32 v110, v110, v111\n\t" "v_cmp_eq_u32 vcc, -1, v110\n\t" "v_smfmac_f32_16x16x64_bf16 v[52:55], v[68:71], a[72:79], v134\n\t" \
   "s_cbranch_vccnz 8f\n\t" "s_waitcnt lgkmcnt(0)\n\t" "s_barrier\n\t" RSTAMP(0) "ds_read_b128 v[76:79], v115\n\t" "ds_read_b128 v[80:83], v116\n\t" \
   "ds_read_b128 v[84:87], v117\n\t" "v_smfmac_f32_16x16x64_bf16 v[48:51], v[72:75], a[16:23], v134\n\t" "ds_read_b128 v[88:91], v118\n\t" \
   "ds_read_b128 v[92:95], v119\n\t" "v_smfmac_f32_16x16x64_bf16 v[52:55], v[72:75], a[80:87], v134\n\t" "ds_read_b128 v[96:99], v120\n\t" \
   "ds_read_b128 v[100:103], v121\n\t" "s_waitcnt lgkmcnt(6)\n\t" "v_smfmac_f32_16x16x64_bf16 v[48:51], v[76:79], a[24:31], v134\n\t" \
   "v_smfmac_f32_16x16x64_bf16 v[52:55], v[76:79], a[88:95], v134\n\t" "s_waitcnt lgkmcnt(5)\n\t" \
   "v_smfmac_f32_16x16x64_bf16 v[48:51], v[80:83], a[32:39], v134\n\t" "v_smfmac_f32_16x16x64_bf16 v[52:55], v[80:83], a[96:103], v134\n\t" \
   "s_waitcnt lgkmcnt(4)\n\t" "v_smfmac_f32_16x16x64_bf16 v[48:51], v[84:87], a[40:47], v134\n\t" \
   "v_smfmac_f32_16x16x64_bf16 v[52:55], v[84:87], a[104:111], v134\n\t" "s_waitcnt lgkmcnt(3)\n\t" \
   "v_smfmac_f32_16x16x64_bf16 v[48:51], v[88:91], a[48:55], v134\n\t" "v_smfmac_f32_16x16x64_bf16 v[52:55], v[88:91], a[112:119], v134\n\t" \
   "s_waitcnt lgkmcnt(0)\n\t" "v_smfmac_f32_16x16x64_bf16 v[48:51], v[92:95], a[56:63], v134\n\t" \
   "v_smfmac_f32_16x16x64_bf16 v[48:51], v[96:99], v[192:199], v134\n\t" "v_smfmac_f32_16x16x64_bf16 v[48:51], v[100:103], v[200:207], v134\n\t" \
   "v_smfmac_f32_16x16x64_bf16 v[52:55], v[92:95], a[120:127], v134\n\t" "v_smfmac_f32_16x16x64_bf16 v[52:55], v[96:99], v[208:215], v134\n\t" \
   "v_smfmac_f32_16x16x64_bf16 v[52:55], v[100:103], v[216:223], v134\n\t" "v_add_f32 v48, v48, v49\n\t" "v_add_f32 v49, v50, v51\n\t" \
   "v_cvt_pk_bf16_f32 v48, v48, v49\n\t" "ds_write_b32 v135, v48 offset:10240\n\t" "s_nop 3\n\t" "v_add_f32 v52, v52, v53\n\t" \
   "v_add_f32 v53, v54, v55\n\t" "v_cvt_pk_bf16_f32 v52, v52, v53\n\t" "ds_write_b32 v135, v52 offset:10496\n\t" "s_waitcnt lgkmcnt(0)\n\t" \
   "s_barrier\n\t" RSTAMP(6) "ds_read_b64_tr_b16 v[16:17], v136 offset:10240\n\t" "ds_read_b64_tr_b16 v[18:19], v136 offset:10496\n\t" \
   "ds_read_b64_tr_b16 v[20:21], v136 offset:10752\n\t" "ds_read_b64_tr_b16 v[22:23], v136 offset:11008\n\t" \
   "ds_read_b64_tr_b16 v[24:25], v136 offset:11264\n\t" "ds_read_b64_tr_b16 v[26:27], v136 offset:11520\n\t" \
   "ds_read_b64_tr_b16 v[28:29], v136 offset:11776\n\t" "ds_read_b64_tr_b16 v[30:31], v136 offset:12032\n\t" "s_waitcnt lgkmcnt(6)\n\t" \
   "v_smfmac_f32_16x16x64_bf16 v[144:147], v[16:19], a[128:135], v131\n\t" "v_smfmac_f32_16x16x64_bf16 v[148:151], v[16:19], a[160:167], v131\n\t" \
   "v_smfmac_f32_16x16x64_bf16 v[152:155], v[16:19], a[192:199], v131\n\t" "v_smfmac_f32_16x16x64_bf16 v[156:159], v[16:19], a[224:231], v131\n\t" \
   "v_smfmac_f32_16x16x64_bf16 v[160:163], v[16:19], v[224:231], v131\n\t" "s_waitcnt lgkmcnt(0)\n\t" \
   "v_smfmac_f32_16x16x64_bf16 v[144:147], v[20:23], a[136:143], v131\n\t" "v_smfmac_f32_16x16x64_bf16 v[144:147], v[24:27], a[144:151], v131\n\t" \
   "v_smfmac_f32_16x16x64_bf16 v[144:147], v[28:31], a[152:159], v131\n\t" "v_smfmac_f32_16x16x64_bf16 v[148:151], v[20:23], a[168:175], v131\n\t" \
   "v_smfmac_f32_16x16x64_bf16 v[148:151], v[24:27], a[176:183], v131\n\t" "v_smfmac_f32_16x16x64_bf16 v[148:151], v[28:31], a[184:191], v131\n\t" \
   "v_smfmac_f32_16x16x64_bf16 v[152:155], v[20:23], a[200:207], v131\n\t" "v_cvt_pk_bf16_f32 v164, v144, v145\n\t" \
   "v_cvt_pk_bf16_f32 v165, v146, v147\n\t" "v_smfmac_f32_16x16x64_bf16 v[152:155], v[24:27], a[208:215], v131\n\t" "v_min_u32 v164, -2, v164\n\t" \
   "buffer_store_dwordx2 v[164:165], v132, %[rs], s49 offen sc1\n\t" "v_smfmac_f32_16x16x64_bf16 v[152:155], v[28:31], a[216:223], v131\n\t" \
   "v_smfmac_f32_16x16x64_bf16 v[156:159], v[20:23], a[232:239], v131\n\t" "v_cvt_pk_bf16_f32 v166, v148, v149\n\t" \
   "v_cvt_pk_bf16_f32 v167, v150, v151\n\t" "v_smfmac_f32_16x16x64_bf16 v[156:159], v[24:27], a[240:247], v131\n\t" "v_min_u32 v166, -2, v166\n\t" \
   "buffer_store_dwordx2 v[166:167], v132, %[rs], s50 offen sc1\n\t" "v_smfmac_f32_16x16x64_bf16 v[156:159], v[28:31], a[248:255], v131\n\t" \
   "v_smfmac_f32_16x16x64_bf16 v[160:163], v[20:23], v[232:239], v131\n\t" "v_cvt_pk_bf16_f32 v168, v152, v153\n\t" \
   "v_cvt_pk_bf16_f32 v169, v154, v155\n\t" "v_smfmac_f32_16x16x64_bf16 v[160:163], v[24:27], v[240:247], v131\n\t" "v_min_u32 v168, -2, v168\n\t" \
   "buffer_store_dwordx2 v[168:169], v132, %[rs], s51 offen sc1\n\t" "v_smfmac_f32_16x16x64_bf16 v[160:163], v[28:31], v[248:255], v131\n\t" \
   "v_cvt_pk_bf16_f32 v170, v156, v157\n\t" "v_cvt_pk_bf16_f32 v171, v158, v159\n\t" "v_min_u32 v170, -2, v170\n\t" \
   "buffer_store_dwordx2 v[170:171], v132, %[rs], s52 offen sc1\n\t" "s_nop 3\n\t" "v_cvt_pk_bf16_f32 v172, v160, v161\n\t" \
   "v_cvt_pk_bf16_f32 v173, v162, v163\n\t" "v_min_u32 v172, -2, v172\n\t" "buffer_store_dwordx2 v[172:173], v132, %[rs], s53 offen sc1\n\t" RSTAMP(2) \
   "s_branch 40b\n\t" "70:\n\t" "s_mov_b32 s46, 0\n\t" "s_lshr_b64 s[72:73], %[sel], s46\n\t" "s_lshl_b32 s45, s46, 19\n\t" "s_and_b32 s72, s72, 1\n\t" \
   "s_add_u32 s45, s45, %[pool]\n\t" "s_lshl_b32 s72, s72, 13\n\t" "s_add_u32 s45, s45, s72\n\t" "v_mov_b32 v76, -1\n\t" "v_mov_b32 v77, -1\n\t" \
   "v_mov_b32 v78, -1\n\t" "v_mov_b32 v79, -1\n\t" "buffer_store_dwordx4 v[76:79], v133, %[rs], s45 offen sc1\n\t" \
   "buffer_store_dwordx4 v[76:79], v133, %[rs], s45 offen offset:1024 sc1\n\t" "s_branch 71b\n\t" "63:\n\t" "s_cmp_lg_u32 s64, 0\n\t" \
   "s_cbranch_scc1 64f\n\t" "s_sub_u32 s69, s56, 0x10000\n\t" "s_and_b32 s69, s69, -2\n\t" "s_branch 50b\n\t" "64:\n\t" "s_mov_b32 s69, s48\n\t" \
   "s_cmp_lt_u32 s64, 3\n\t" "s_cselect_b32 s69, -1, s69\n\t" "s_cmp_lg_u32 s69, -1\n\t" "s_cbranch_scc1 50b\n\t" "s_add_u32 s76, s76, 0x1000000\n\t" \
   "s_mov_b32 s73, 0\n\t" "65:\n\t" "s_add_u32 s73, s73, 1\n\t" "s_sleep 6\n\t" "s_and_b32 s72, s73, 3\n\t" "s_add_u32 s72, s72, 8\n\t" \
   "s_sub_u32 s72, s72, %[xs]\n\t" "s_lshl_b32 s72, s72, 7\n\t" "s_add_u32 s72, s72, s66\n\t" "buffer_load_dword v108, v139, %[rs], s72 offen sc1\n\t" \
   "s_waitcnt vmcnt(0)\n\t" "v_readfirstlane_b32 s69, v108\n\t" "s_cmp_gt_u32 s73, 0x10000\n\t" "s_cbranch_scc1 50b\n\t" "s_cmp_eq_u32 s69, -1\n\t" \
   "s_cbranch_scc1 65b\n\t" "s_branch 50b\n\t" "8:\n\t" "s_nop 7\n\t" "s_waitcnt lgkmcnt(0)\n\t" "s_add_u32 s70, s70, 1\n\t" "s_cmp_gt_u32 s70, 2\n\t" \
   "s_cbranch_scc1 81f\n\t" "s_lshl_b32 s71, s70, 3\n\t" "s_lshl_b32 s71, 1, s71\n\t" "s_add_u32 s76, s76, s71\n\t" "81:\n\t" "v_mov_b32 v48, 0\n\t" \
   "v_mov_b32 v49, 0\n\t" "v_mov_b32 v50, 0\n\t" "v_mov_b32 v51, 0\n\t" "v_mov_b32 v52, 0\n\t" "v_mov_b32 v53, 0\n\t" "v_mov_b32 v54, 0\n\t" \
   "v_mov_b32 v55, 0\n\t" "s_add_u32 s71, s70, 1\n\t" "s_min_u32 s71, s71, 7\n\t" "s_lshr_b32 s67, s71, 1\n\t" "s_add_u32 s67, s67, %[xs]\n\t" \
   "s_and_b32 s67, s67, 3\n\t" "s_lshl_b32 s67, s67, 11\n\t" "s_and_b32 s72, s71, 1\n\t" "s_lshl_b32 s72, s72, 14\n\t" "s_add_u32 s67, s67, s72\n\t" \
   "s_add_u32 s67, s67, s66\n\t" "s_cmp_lt_u32 s70, 6\n\t" "s_cbranch_scc1 20b\n\t" "s_sleep 3\n\t" "s_cmp_lt_u32 s70, 0x20000\n\t" \
   "s_cbranch_scc1 20b\n\t" "s_mov_b32 s67, %[esc]\n\t" "s_branch 20b\n\t" "90:\n\t" "s_waitcnt vmcnt(0) lgkmcnt(0)"
#define PV_CLOB "v16", "v17", "v18", "v19", "v20", "v21", "v22", "v23", "v24", "v25", "v26", "v27", "v28", "v29", "v30", "v31", "v48", "v49", "v50", "v51", "v52", "v53", "v54", "v55", "v56", "v57", "v58", "v59", "v60", "v61", "v62", "v63", "v64", "v65", "v66", "v67", "v68", "v69", "v70", "v71", "v72", "v73", "v74", "v75", "v76", "v77", "v78", "v79", "v80", "v81", "v82", "v83", "v84", "v85", "v86", "v87", "v88", "v89", "v90", "v91", "v92", "v93", "v94", "v95", "v96", "v97", "v98", "v99", "v100", "v101", "v102", "v103", "v104", "v105", "v106", "v107", "v108", "v109", "v110", "v111", "v144", "v145", "v146", "v147", "v148", "v149", "v150", "v151", "v152", "v153", "v154", "v155", "v156", "v157", "v158", "v159", "v160", "v161", "v162", "v163", "v164", "v165", "v166", "v167", "v168", "v169", "v170", "v171", "v172", "v173", "v174", "v175", "s60", "s61", "s62", "s63", "s64", "s65", "s66", "s67", "s68", "s69", "s70", "s71", "s72", "s73", "s74", "s75", "s45", "s46", "s47", "s48", "s49", "s50", "s51", "s52", "s53", "s54"
#define WAIT_ASM \
   "11:\n\t" "s_memrealtime s[60:61]\n\t" "s_memtime s[62:63]\n\t" "s_waitcnt lgkmcnt(0)\n\t" "s_sub_u32 s74, s62, s57\n\t" \
   "s_mul_i32 s74, s74, s58\n\t" "s_lshr_b32 s74, s74, 12\n\t" "s_add_u32 s74, s74, s56\n\t" "s_lshl_b32 s60, s60, 3\n\t" "s_sub_u32 s75, s74, s60\n\t" \
   "s_cmp_lt_u32 s75, 32\n\t" "s_cbranch_scc1 12f\n\t" "s_cmp_lt_i32 s75, 0\n\t" "s_cselect_b32 s61, 0, 31\n\t" "s_add_u32 s74, s60, s61\n\t" \
   "s_sub_i32 s75, s61, s75\n\t" "s_add_i32 s75, s75, 1\n\t" "s_ashr_i32 s75, s75, 1\n\t" "s_min_i32 s75, s75, 8\n\t" "s_max_i32 s75, s75, -8\n\t" \
   "s_mov_b32 s56, s74\n\t" "s_mov_b32 s57, s62\n\t" "s_add_u32 s58, s58, s75\n\t" "12:\n\t" "s_sub_i32 s74, %[ta], s74\n\t" \
   "s_cmp_le_i32 s74, 150\n\t" "s_cbranch_scc1 13f\n\t" "s_cmp_lt_i32 s74, 260\n\t" "s_cbranch_scc1 11b\n\t" "s_sleep 2\n\t" \
   "s_cmp_lt_i32 s74, 32000\n\t" "s_cbranch_scc1 11b\n\t" "s_mov_b32 s74, 0\n\t" "13:\n\t" "s_sub_i32 s74, s74, 56\n\t" "s_max_i32 s74, s74, 0\n\t" \
   "s_mul_i32 s74, s74, 23\n\t" "s_lshr_b32 s74, s74, 5\n\t" "s_and_b32 s74, s74, -4\n\t" "s_getpc_b64 s[60:61]\n\t" "14:\n\t" \
   "s_sub_u32 s74, 15f-14b, s74\n\t" "s_add_u32 s60, s60, s74\n\t" "s_addc_u32 s61, s61, 0\n\t" "s_setpc_b64 s[60:61]\n\t" "s_nop 3\n\t" "s_nop 3\n\t" \
   "s_nop 3\n\t" "s_nop 3\n\t" "s_nop 3\n\t" "s_nop 3\n\t" "s_nop 3\n\t" "s_nop 3\n\t" "s_nop 3\n\t" "s_nop 3\n\t" "s_nop 3\n\t" "s_nop 3\n\t" \
   "s_nop 3\n\t" "s_nop 3\n\t" "s_nop 3\n\t" "s_nop 3\n\t" "s_nop 3\n\t" "s_nop 3\n\t" "s_nop 3\n\t" "s_nop 3\n\t" "s_nop 3\n\t" "s_nop 3\n\t" \
   "s_nop 3\n\t" "s_nop 3\n\t" "15:\n\t" ""
#define WAIT_CLOB "s60", "s61", "s62", "s63", "s74", "s75"
#define FIN_ASM \
   "s_mov_b32 s40, 0\n\t" "s_mov_b32 s76, 0\n\t" "s_mov_b32 s77, 0\n\t" "s_sub_u32 s45, s56, 0x10000\n\t" "v_mov_b32 v13, -2\n\t" \
   "v_mov_b32 v11, -1\n\t" "30:\n\t" "s_lshr_b64 s[72:73], %[sel], s40\n\t" "s_lshl_b32 s41, s40, 19\n\t" "s_and_b32 s72, s72, 1\n\t" \
   "s_add_u32 s41, s41, %[pool]\n\t" "s_lshl_b32 s72, s72, 13\n\t" "s_add_u32 s41, s41, s72\n\t" "s_lshr_b64 s[72:73], %[s3], s40\n\t" \
   "s_and_b32 s47, s72, 1\n\t" "s_lshl_b32 s47, s47, 14\n\t" "s_mul_i32 s49, s40, 1310720\n\t" "s_add_u32 s49, s49, %[su]\n\t" \
   "s_lshr_b64 s[72:73], %[ysel], s40\n\t" "s_mul_i32 s73, s40, 43\n\t" "s_mul_i32 s50, s40, 80\n\t" "s_and_b32 s72, s72, 1\n\t" \
   "s_lshr_b32 s73, s73, 7\n\t" "s_add_u32 s50, s50, s72\n\t" "s_lshl4_add_u32 s50, s73, s50\n\t" "s_lshl_b32 s50, s50, 13\n\t" \
   "s_add_u32 s50, s50, %[yb]\n\t" "s_add_u32 s51, s50, 4096\n\t" "s_lshl_b32 s52, s40, 12\n\t" "s_add_u32 s52, s52, %[uf]\n\t" \
   "s_add_u32 s69, s45, %[tu]\n\t" "v_max3_u32 v15, v8, v9, v10\n\t" "v_max_u32 v15, v15, v11\n\t" "v_cmp_eq_u32 vcc, -1, v15\n\t" \
   "s_add_u32 s40, s40, 1\n\t" "s_cbranch_vccnz 95f\n\t" "96:\n\t" "11:\n\t" "s_memrealtime s[60:61]\n\t" "s_memtime s[62:63]\n\t" \
   "s_waitcnt lgkmcnt(0)\n\t" "s_sub_u32 s74, s62, s57\n\t" "s_mul_i32 s74, s74, s58\n\t" "s_lshr_b32 s74, s74, 12\n\t" "s_add_u32 s74, s74, s56\n\t" \
   "s_lshl_b32 s60, s60, 3\n\t" "s_sub_u32 s75, s74, s60\n\t" "s_cmp_lt_u32 s75, 32\n\t" "s_cbranch_scc1 12f\n\t" "s_cmp_lt_i32 s75, 0\n\t" \
   "s_cselect_b32 s61, 0, 31\n\t" "s_add_u32 s74, s60, s61\n\t" "s_sub_i32 s75, s61, s75\n\t" "s_add_i32 s75, s75, 1\n\t" "s_ashr_i32 s75, s75, 1\n\t" \
   "s_min_i32 s75, s75, 8\n\t" "s_max_i32 s75, s75, -8\n\t" "s_mov_b32 s56, s74\n\t" "s_mov_b32 s57, s62\n\t" "s_add_u32 s58, s58, s75\n\t" "12:\n\t" \
   "s_sub_i32 s74, s69, s74\n\t" "s_cmp_le_i32 s74, 150\n\t" "s_cbranch_scc1 13f\n\t" "s_cmp_lt_i32 s74, 260\n\t" "s_cbranch_scc1 11b\n\t" \
   "s_sleep 2\n\t" "s_cmp_lt_i32 s74, 32000\n\t" "s_cbranch_scc1 11b\n\t" "s_mov_b32 s74, 0\n\t" "13:\n\t" "s_sub_i32 s74, s74, 56\n\t" \
   "s_max_i32 s74, s74, 0\n\t" "s_mul_i32 s74, s74, 23\n\t" "s_lshr_b32 s74, s74, 5\n\t" "s_and_b32 s74, s74, -4\n\t" "s_getpc_b64 s[60:61]\n\t" \
   "14:\n\t" "s_sub_u32 s74, 15f-14b, s74\n\t" "s_add_u32 s60, s60, s74\n\t" "s_addc_u32 s61, s61, 0\n\t" "s_setpc_b64 s[60:61]\n\t" "s_nop 3\n\t" \
   "s_nop 3\n\t" "s_nop 3\n\t" "s_nop 3\n\t" "s_nop 3\n\t" "s_nop 3\n\t" "s_nop 3\n\t" "s_nop 3\n\t" "s_nop 3\n\t" "s_nop 3\n\t" "s_nop 3\n\t" \
   "s_nop 3\n\t" "s_nop 3\n\t" "s_nop 3\n\t" "s_nop 3\n\t" "s_nop 3\n\t" "s_nop 3\n\t" "s_nop 3\n\t" "s_nop 3\n\t" "s_nop 3\n\t" "s_nop 3\n\t" \
   "s_nop 3\n\t" "s_nop 3\n\t" "s_nop 3\n\t" "15:\n\t" RSTAMP(4) "buffer_load_dwordx4 a[0:3], v16, %[rs], s49 offen offset:0 sc1\n\t" "s_nop 0\n\t" \
   "buffer_load_dwordx4 a[4:7], v16, %[rs], s49 offen offset:1024 sc1\n\t" "s_nop 0\n\t" \
   "buffer_load_dwordx4 a[8:11], v16, %[rs], s49 offen offset:2048 sc1\n\t" "s_nop 0\n\t" \
   "buffer_load_dwordx4 a[12:15], v16, %[rs], s49 offen offset:3072 sc1\n\t" "s_nop 0\n\t" "s_add_u32 s49, s49, 4096\n\t" \
   "buffer_load_dwordx4 a[16:19], v16, %[rs], s49 offen offset:0 sc1\n\t" "s_nop 0\n\t" \
   "buffer_load_dwordx4 a[20:23], v16, %[rs], s49 offen offset:1024 sc1\n\t" "s_nop 0\n\t" \
   "buffer_load_dwordx4 a[24:27], v16, %[rs], s49 offen offset:2048 sc1\n\t" "s_nop 0\n\t" \
   "buffer_load_dwordx4 a[28:31], v16, %[rs], s49 offen offset:3072 sc1\n\t" "s_nop 0\n\t" "s_add_u32 s49, s49, 4096\n\t" \
   "buffer_load_dwordx4 a[32:35], v16, %[rs], s49 offen offset:0 sc1\n\t" "s_nop 0\n\t" \
   "buffer_load_dwordx4 a[36:39], v16, %[rs], s49 offen offset:1024 sc1\n\t" "s_nop 0\n\t" \
   "buffer_load_dwordx4 a[40:43], v16, %[rs], s49 offen offset:2048 sc1\n\t" "s_nop 0\n\t" \
   "buffer_load_dwordx4 a[44:47], v16, %[rs], s49 offen offset:3072 sc1\n\t" "s_nop 0\n\t" "s_add_u32 s49, s49, 4096\n\t" \
   "buffer_load_dwordx4 a[48:51], v16, %[rs], s49 offen offset:0 sc1\n\t" "s_nop 0\n\t" \
   "buffer_load_dwordx4 a[52:55], v16, %[rs], s49 offen offset:1024 sc1\n\t" "s_nop 0\n\t" \
   "buffer_load_dwordx4 a[56:59], v16, %[rs], s49 offen offset:2048 sc1\n\t" "s_nop 0\n\t" \
   "buffer_load_dwordx4 a[60:63], v16, %[rs], s49 offen offset:3072 sc1\n\t" "s_nop 0\n\t" "s_add_u32 s49, s49, 4096\n\t" \
   "buffer_load_dwordx4 a[64:67], v16, %[rs], s49 offen offset:0 sc1\n\t" "s_nop 0\n\t" \
   "buffer_load_dwordx4 a[68:71], v16, %[rs], s49 offen offset:1024 sc1\n\t" "s_nop 0\n\t" \
   "buffer_load_dwordx4 a[72:75], v16, %[rs], s49 offen offset:2048 sc1\n\t" "s_nop 0\n\t" \
   "buffer_load_dwordx4 a[76:79], v16, %[rs], s49 offen offset:3072 sc1\n\t" "s_nop 0\n\t" "s_add_u32 s49, s49, 4096\n\t" \
   "buffer_load_dwordx4 a[80:83], v16, %[rs], s49 offen offset:0 sc1\n\t" "s_nop 0\n\t" \
   "buffer_load_dwordx4 a[84:87], v16, %[rs], s49 offen offset:1024 sc1\n\t" "s_nop 0\n\t" \
   "buffer_load_dwordx4 a[88:91], v16, %[rs], s49 offen offset:2048 sc1\n\t" "s_nop 0\n\t" \
   "buffer_load_dwordx4 a[92:95], v16, %[rs], s49 offen offset:3072 sc1\n\t" "s_nop 0\n\t" "s_add_u32 s49, s49, 4096\n\t" \
   "buffer_load_dwordx4 a[96:99], v16, %[rs], s49 offen offset:0 sc1\n\t" "s_nop 0\n\t" \
   "buffer_load_dwordx4 a[100:103], v16, %[rs], s49 offen offset:1024 sc1\n\t" "s_nop 0\n\t" \
   "buffer_load_dwordx4 a[104:107], v16, %[rs], s49 offen offset:2048 sc1\n\t" "s_nop 0\n\t" \
   "buffer_load_dwordx4 a[108:111], v16, %[rs], s49 offen offset:3072 sc1\n\t" "s_nop 0\n\t" "s_add_u32 s49, s49, 4096\n\t" \
   "buffer_load_dwordx4 a[112:115], v16, %[rs], s49 offen offset:0 sc1\n\t" "s_nop 0\n\t" \
   "buffer_load_dwordx4 a[116:119], v16, %[rs], s49 offen offset:1024 sc1\n\t" "s_nop 0\n\t" \
   "buffer_load_dwordx4 a[120:123], v16, %[rs], s49 offen offset:2048 sc1\n\t" "s_nop 0\n\t" \
   "buffer_load_dwordx4 a[124:127], v16, %[rs], s49 offen offset:3072 sc1\n\t" "s_nop 0\n\t" "s_add_u32 s52, s52, 4096\n\t" \
   "buffer_load_dwordx4 v[8:11], v19, %[rs], s52 offen sc1\n\t" RSTAMP(0) "s_mov_b32 s46, 0\n\t" "s_xor_b32 s64, s47, %[lad0]\n\t" \
   "s_and_b32 s65, s64, 0x4000\n\t" "s_xor_b32 s64, s64, s65\n\t" "v_or_b32 v224, s65, v16\n\t" "v_or_b32 v225, s65, v17\n\t" \
   "v_xor_b32 v226, 0x4000, v224\n\t" "v_xor_b32 v227, 0x4000, v225\n\t" "s_add_u32 s64, s64, s41\n\t" "v_xor_b32 v14, s47, v20\n\t" \
   "v_xor_b32 v15, 0x4000, v14\n\t" "s_add_u32 s65, s64, 32768\n\t" "s_add_u32 s66, s64, 65536\n\t" "s_add_u32 s67, s64, 98304\n\t" \
   "s_add_u32 s68, s64, 131072\n\t" "s_add_u32 s69, s64, 163840\n\t" "s_add_u32 s70, s64, 196608\n\t" "s_add_u32 s71, s64, 229376\n\t" \
   "s_add_u32 s72, s64, 262144\n\t" "s_add_u32 s73, s64, 294912\n\t" "s_add_u32 s53, s41, 4096\n\t" "v_mov_b32 v28, 0\n\t" "v_mov_b32 v29, 0\n\t" \
   "v_mov_b32 v30, 0\n\t" "v_mov_b32 v31, 0\n\t" "19:\n\t" "ds_read_b128 v[24:27], v23\n\t" "s_waitcnt lgkmcnt(0)\n\t" \
   "v_readfirstlane_b32 s74, v26\n\t" "v_readfirstlane_b32 s44, v24\n\t" "v_readfirstlane_b32 s45, v25\n\t" "s_sub_u32 s48, s44, 110\n\t" \
   "s_cmp_lt_u32 s74, s40\n\t" "s_cbranch_scc1 18f\n\t" RSTAMP(7) "21:\n\t" "s_memrealtime s[60:61]\n\t" "s_memtime s[62:63]\n\t" \
   "s_waitcnt lgkmcnt(0)\n\t" "s_sub_u32 s74, s62, s57\n\t" "s_mul_i32 s74, s74, s58\n\t" "s_lshr_b32 s74, s74, 12\n\t" "s_add_u32 s74, s74, s56\n\t" \
   "s_lshl_b32 s60, s60, 3\n\t" "s_sub_u32 s75, s74, s60\n\t" "s_cmp_lt_u32 s75, 32\n\t" "s_cbranch_scc1 22f\n\t" "s_cmp_lt_i32 s75, 0\n\t" \
   "s_cselect_b32 s61, 0, 31\n\t" "s_add_u32 s74, s60, s61\n\t" "s_sub_i32 s75, s61, s75\n\t" "s_add_i32 s75, s75, 1\n\t" "s_ashr_i32 s75, s75, 1\n\t" \
   "s_min_i32 s75, s75, 8\n\t" "s_max_i32 s75, s75, -8\n\t" "s_mov_b32 s56, s74\n\t" "s_mov_b32 s57, s62\n\t" "s_add_u32 s58, s58, s75\n\t" "22:\n\t" \
   "s_sub_i32 s74, s48, s74\n\t" "s_cmp_le_i32 s74, 150\n\t" "s_cbranch_scc1 23f\n\t" "s_cmp_lt_i32 s74, 260\n\t" "s_cbranch_scc1 21b\n\t" \
   "s_sleep 2\n\t" "s_cmp_lt_i32 s74, 32000\n\t" "s_cbranch_scc1 21b\n\t" "s_mov_b32 s74, 0\n\t" "23:\n\t" "s_sub_i32 s74, s74, 56\n\t" \
   "s_max_i32 s74, s74, 0\n\t" "s_mul_i32 s74, s74, 23\n\t" "s_lshr_b32 s74, s74, 5\n\t" "s_and_b32 s74, s74, -4\n\t" "s_getpc_b64 s[60:61]\n\t" \
   "24:\n\t" "s_sub_u32 s74, 25f-24b, s74\n\t" "s_add_u32 s60, s60, s74\n\t" "s_addc_u32 s61, s61, 0\n\t" "s_setpc_b64 s[60:61]\n\t" "s_nop 3\n\t" \
   "s_nop 3\n\t" "s_nop 3\n\t" "s_nop 3\n\t" "s_nop 3\n\t" "s_nop 3\n\t" "s_nop 3\n\t" "s_nop 3\n\t" "s_nop 3\n\t" "s_nop 3\n\t" "s_nop 3\n\t" \
   "s_nop 3\n\t" "s_nop 3\n\t" "s_nop 3\n\t" "s_nop 3\n\t" "s_nop 3\n\t" "s_nop 3\n\t" "s_nop 3\n\t" "s_nop 3\n\t" "s_nop 3\n\t" "s_nop 3\n\t" \
   "s_nop 3\n\t" "s_nop 3\n\t" "s_nop 3\n\t" "25:\n\t" "buffer_load_dwordx4 v[32:35], v16, %[rs], s50 offen offset:0 sc1\n\t" \
   "buffer_load_dwordx4 v[36:39], v16, %[rs], s50 offen offset:1024 sc1\n\t" "buffer_load_dwordx4 v[40:43], v16, %[rs], s50 offen offset:2048 sc1\n\t" \
   "buffer_load_dwordx4 v[44:47], v16, %[rs], s50 offen offset:3072 sc1\n\t" "buffer_load_dwordx4 v[48:51], v16, %[rs], s51 offen offset:0 sc1\n\t" \
   "buffer_load_dwordx4 v[52:55], v16, %[rs], s51 offen offset:1024 sc1\n\t" "buffer_load_dwordx4 v[56:59], v16, %[rs], s51 offen offset:2048 sc1\n\t" \
   "buffer_load_dwordx4 v[60:63], v16, %[rs], s51 offen offset:3072 sc1\n\t" "s_nop 3\n\t" "s_nop 3\n\t" "s_nop 3\n\t" "s_nop 3\n\t" "s_nop 3\n\t" \
   "s_nop 3\n\t" "s_nop 3\n\t" "s_nop 3\n\t" "s_nop 3\n\t" "s_nop 3\n\t" "s_nop 3\n\t" "s_nop 3\n\t" "s_nop 3\n\t" "s_nop 3\n\t" "s_nop 3\n\t" \
   "s_nop 3\n\t" RSTAMP(6) "buffer_load_dwordx4 v[64:67], v224, %[rs], s64 offen offset:0 sc1\n\t" \
   "buffer_load_dwordx4 v[68:71], v224, %[rs], s64 offen offset:1024 sc1\n\t" "buffer_load_dwordx4 v[72:75], v224, %[rs], s65 offen offset:0 sc1\n\t" \
   "buffer_load_dwordx4 v[76:79], v224, %[rs], s65 offen offset:1024 sc1\n\t" "buffer_load_dwordx4 v[80:83], v224, %[rs], s66 offen offset:0 sc1\n\t" \
   "buffer_load_dwordx4 v[84:87], v224, %[rs], s66 offen offset:1024 sc1\n\t" "buffer_load_dwordx4 v[88:91], v224, %[rs], s67 offen offset:0 sc1\n\t" \
   "buffer_load_dwordx4 v[92:95], v224, %[rs], s67 offen offset:1024 sc1\n\t" "buffer_load_dwordx4 v[96:99], v224, %[rs], s68 offen offset:0 sc1\n\t" \
   "buffer_load_dwordx4 v[100:103], v224, %[rs], s68 offen offset:1024 sc1\n\t" \
   "buffer_load_dwordx4 v[104:107], v224, %[rs], s69 offen offset:0 sc1\n\t" \
   "buffer_load_dwordx4 v[108:111], v224, %[rs], s69 offen offset:1024 sc1\n\t" \
   "buffer_load_dwordx4 v[112:115], v224, %[rs], s70 offen offset:0 sc1\n\t" \
   "buffer_load_dwordx4 v[116:119], v224, %[rs], s70 offen offset:1024 sc1\n\t" \
   "buffer_load_dwordx4 v[120:123], v224, %[rs], s71 offen offset:0 sc1\n\t" \
   "buffer_load_dwordx4 v[124:127], v224, %[rs], s71 offen offset:1024 sc1\n\t" \
   "buffer_load_dwordx2 v[128:129], v225, %[rs], s72 offen offset:0 sc1\n\t" \
   "buffer_load_dwordx2 v[130:131], v225, %[rs], s72 offen offset:128 sc1\n\t" \
   "buffer_load_dwordx2 v[132:133], v225, %[rs], s72 offen offset:1024 sc1\n\t" \
   "buffer_load_dwordx2 v[134:135], v225, %[rs], s72 offen offset:1152 sc1\n\t" \
   "buffer_load_dwordx2 v[136:137], v225, %[rs], s73 offen offset:0 sc1\n\t" \
   "buffer_load_dwordx2 v[138:139], v225, %[rs], s73 offen offset:128 sc1\n\t" \
   "buffer_load_dwordx2 v[140:141], v225, %[rs], s73 offen offset:1024 sc1\n\t" \
   "buffer_load_dwordx2 v[142:143], v225, %[rs], s73 offen offset:1152 sc1\n\t" YSTAMP(24) "s_waitcnt vmcnt(23)\n\t" RSTAMP(1) \
   "v_smfmac_f32_16x16x64_bf16 v[28:31], v[64:67], a[0:7], v22\n\t" "s_waitcnt vmcnt(22)\n\t" \
   "v_smfmac_f32_16x16x64_bf16 v[28:31], v[68:71], a[8:15], v22\n\t" "s_waitcnt vmcnt(21)\n\t" \
   "v_smfmac_f32_16x16x64_bf16 v[28:31], v[72:75], a[16:23], v22\n\t" "s_waitcnt vmcnt(20)\n\t" \
   "v_smfmac_f32_16x16x64_bf16 v[28:31], v[76:79], a[24:31], v22\n\t" "s_waitcnt vmcnt(19)\n\t" \
   "v_smfmac_f32_16x16x64_bf16 v[28:31], v[80:83], a[32:39], v22\n\t" "s_waitcnt vmcnt(18)\n\t" \
   "v_smfmac_f32_16x16x64_bf16 v[28:31], v[84:87], a[40:47], v22\n\t" "s_waitcnt vmcnt(17)\n\t" \
   "v_smfmac_f32_16x16x64_bf16 v[28:31], v[88:91], a[48:55], v22\n\t" "s_waitcnt vmcnt(16)\n\t" \
   "v_smfmac_f32_16x16x64_bf16 v[28:31], v[92:95], a[56:63], v22\n\t" "s_waitcnt vmcnt(15)\n\t" \
   "v_smfmac_f32_16x16x64_bf16 v[28:31], v[96:99], a[64:71], v22\n\t" "s_waitcnt vmcnt(14)\n\t" \
   "v_smfmac_f32_16x16x64_bf16 v[28:31], v[100:103], a[72:79], v22\n\t" "s_waitcnt vmcnt(13)\n\t" \
   "v_smfmac_f32_16x16x64_bf16 v[28:31], v[104:107], a[80:87], v22\n\t" "s_waitcnt vmcnt(12)\n\t" \
   "v_smfmac_f32_16x16x64_bf16 v[28:31], v[108:111], a[88:95], v22\n\t" "s_waitcnt vmcnt(11)\n\t" \
   "v_smfmac_f32_16x16x64_bf16 v[28:31], v[112:115], a[96:103], v22\n\t" "s_waitcnt vmcnt(10)\n\t" \
   "v_smfmac_f32_16x16x64_bf16 v[28:31], v[116:119], a[104:111], v22\n\t" "s_waitcnt vmcnt(9)\n\t" \
   "v_smfmac_f32_16x16x64_bf16 v[28:31], v[120:123], a[112:119], v22\n\t" "s_waitcnt vmcnt(8)\n\t" \
   "v_smfmac_f32_16x16x64_bf16 v[28:31], v[124:127], a[120:127], v22\n\t" "s_waitcnt vmcnt(6)\n\t" \
   "v_smfmac_f32_16x16x64_bf16 v[28:31], v[128:131], v[32:39], v22\n\t" "s_waitcnt vmcnt(4)\n\t" \
   "v_smfmac_f32_16x16x64_bf16 v[28:31], v[132:135], v[40:47], v22\n\t" "s_waitcnt vmcnt(2)\n\t" \
   "v_smfmac_f32_16x16x64_bf16 v[28:31], v[136:139], v[48:55], v22\n\t" "s_waitcnt vmcnt(0)\n\t" \
   "v_smfmac_f32_16x16x64_bf16 v[28:31], v[140:143], v[56:63], v22\n\t" "s_nop 7\n\t" "v_add_f32 v28, v28, v29\n\t" "v_add_f32 v30, v30, v31\n\t" \
   "v_cvt_pk_bf16_f32 v24, v28, v30\n\t" "v_cmp_u_f32 vcc, v28, v30\n\t" "s_nop 0\n\t" \
   "v_mov_b32_dpp v25, v24 quad_perm:[1,0,3,2] row_mask:0xf bank_mask:0xf\n\t" "s_cbranch_vccnz 8f\n\t" "2:\n\t" RSTAMP(2) \
   "v_perm_b32 v26, v25, v24, v18\n\t" "s_nop 1\n\t" "v_min_u32_dpp v24, v26, v13 quad_perm:[0,1,0,1] row_mask:0xf bank_mask:0xf\n\t" \
   "v_mov_b32_dpp v25, v26 quad_perm:[2,3,2,3] row_mask:0xf bank_mask:0xf\n\t" "buffer_store_dwordx2 v[24:25], v14, %[rs], s41 offen sc1\n\t" \
   "buffer_store_dwordx2 v[24:25], v14, %[rs], s53 offen sc1\n\t" "s_memtime s[62:63]\n\t" "s_memrealtime s[60:61]\n\t" \
   "buffer_store_dwordx2 v[24:25], v15, %[rs], s41 offen sc1\n\t" "buffer_store_dwordx2 v[24:25], v15, %[rs], s53 offen sc1\n\t" RSTAMP(3) \
   "s_waitcnt lgkmcnt(0)\n\t" "s_sub_u32 s74, s62, s57\n\t" "s_mul_i32 s74, s74, s58\n\t" "s_lshr_b32 s74, s74, 12\n\t" "s_add_u32 s74, s74, s56\n\t" \
   "s_lshl_b32 s60, s60, 3\n\t" "s_sub_u32 s75, s74, s60\n\t" "s_cmp_lt_u32 s75, 32\n\t" "s_cbranch_scc1 38f\n\t" "s_cmp_lt_i32 s75, 0\n\t" \
   "s_cselect_b32 s61, 0, 31\n\t" "s_add_u32 s74, s60, s61\n\t" "s_sub_i32 s75, s61, s75\n\t" "s_add_i32 s75, s75, 1\n\t" "s_ashr_i32 s75, s75, 1\n\t" \
   "s_min_i32 s75, s75, 8\n\t" "s_max_i32 s75, s75, -8\n\t" "s_mov_b32 s56, s74\n\t" "s_mov_b32 s57, s62\n\t" "s_add_u32 s58, s58, s75\n\t" "38:\n\t" \
   "s_min_u32 s75, s46, 1\n\t" "s_and_b32 s74, s74, -4\n\t" "s_or_b32 s74, s74, s75\n\t" "v_mov_b32 v26, s74\n\t" \
   "buffer_store_dword v26, v21, %[rs], s41 offen sc1\n\t" FINTRACE "v_xor_b32 v23, 32, v23\n\t" "s_cmp_lt_u32 s40, 63\n\t" "s_cbranch_scc1 30b\n\t" \
   "s_branch 90f\n\t" "18:\n\t" "s_sleep 2\n\t" "s_branch 19b\n\t" "8:\n\t" "s_mov_b32 s74, 0\n\t" "87:\n\t" "v_max3_u32 v12, v32, v34, v36\n\t" \
   "v_max3_u32 v12, v12, v38, v40\n\t" "v_max3_u32 v12, v12, v42, v44\n\t" "v_max3_u32 v12, v12, v46, v48\n\t" "v_max3_u32 v12, v12, v50, v52\n\t" \
   "v_max3_u32 v12, v12, v54, v56\n\t" "v_max3_u32 v12, v12, v58, v60\n\t" "v_max_u32 v12, v12, v62\n\t" "v_cmp_eq_u32 vcc, -1, v12\n\t" \
   "s_cmp_gt_u32 s74, 0x10000\n\t" "s_cbranch_scc1 86f\n\t" "s_cbranch_vccz 86f\n\t" "s_cmp_eq_u32 s74, 0\n\t" "s_cselect_b32 s75, 0x100, 0\n\t" \
   "s_cbranch_scc1 88f\n\t" "s_sleep 3\n\t" "88:\n\t" "buffer_load_dwordx4 v[32:35], v16, %[rs], s50 offen offset:0 sc1\n\t" \
   "buffer_load_dwordx4 v[36:39], v16, %[rs], s50 offen offset:1024 sc1\n\t" "buffer_load_dwordx4 v[40:43], v16, %[rs], s50 offen offset:2048 sc1\n\t" \
   "buffer_load_dwordx4 v[44:47], v16, %[rs], s50 offen offset:3072 sc1\n\t" "buffer_load_dwordx4 v[48:51], v16, %[rs], s51 offen offset:0 sc1\n\t" \
   "buffer_load_dwordx4 v[52:55], v16, %[rs], s51 offen offset:1024 sc1\n\t" "buffer_load_dwordx4 v[56:59], v16, %[rs], s51 offen offset:2048 sc1\n\t" \
   "buffer_load_dwordx4 v[60:63], v16, %[rs], s51 offen offset:3072 sc1\n\t" "s_add_u32 s76, s76, s75\n\t" "s_add_u32 s74, s74, 1\n\t" \
   "s_waitcnt vmcnt(0)\n\t" "s_branch 87b\n\t" "86:\n\t" "v_mov_b32 v27, 0\n\t" "v_mov_b32 v28, 0\n\t" "v_mov_b32 v29, 0\n\t" "v_mov_b32 v30, 0\n\t" \
   "v_mov_b32 v31, 0\n\t" "v_max3_u32 v27, v27, v64, v66\n\t" "v_max3_u32 v27, v27, v68, v70\n\t" "v_max3_u32 v27, v27, v72, v74\n\t" \
   "v_max3_u32 v27, v27, v76, v78\n\t" "v_max3_u32 v27, v27, v80, v82\n\t" "v_max3_u32 v27, v27, v84, v86\n\t" "v_max3_u32 v27, v27, v88, v90\n\t" \
   "v_max3_u32 v27, v27, v92, v94\n\t" "v_max3_u32 v27, v27, v96, v98\n\t" "v_max3_u32 v27, v27, v100, v102\n\t" "v_max3_u32 v27, v27, v104, v106\n\t" \
   "v_max3_u32 v27, v27, v108, v110\n\t" "v_max3_u32 v27, v27, v112, v114\n\t" "v_max3_u32 v27, v27, v116, v118\n\t" \
   "v_max3_u32 v27, v27, v120, v122\n\t" "v_max3_u32 v27, v27, v124, v126\n\t" "v_max3_u32 v27, v27, v128, v130\n\t" \
   "v_max3_u32 v27, v27, v132, v134\n\t" "v_max3_u32 v27, v27, v136, v138\n\t" "v_max3_u32 v27, v27, v140, v142\n\t" "v_cmp_eq_u32 vcc, -1, v27\n\t" \
   "s_nop 0\n\t" "s_cbranch_vccnz 85f\n\t" "v_smfmac_f32_16x16x64_bf16 v[28:31], v[64:67], a[0:7], v22\n\t" \
   "v_smfmac_f32_16x16x64_bf16 v[28:31], v[68:71], a[8:15], v22\n\t" "v_smfmac_f32_16x16x64_bf16 v[28:31], v[72:75], a[16:23], v22\n\t" \
   "v_smfmac_f32_16x16x64_bf16 v[28:31], v[76:79], a[24:31], v22\n\t" "v_smfmac_f32_16x16x64_bf16 v[28:31], v[80:83], a[32:39], v22\n\t" \
   "v_smfmac_f32_16x16x64_bf16 v[28:31], v[84:87], a[40:47], v22\n\t" "v_smfmac_f32_16x16x64_bf16 v[28:31], v[88:91], a[48:55], v22\n\t" \
   "v_smfmac_f32_16x16x64_bf16 v[28:31], v[92:95], a[56:63], v22\n\t" "v_smfmac_f32_16x16x64_bf16 v[28:31], v[96:99], a[64:71], v22\n\t" \
   "v_smfmac_f32_16x16x64_bf16 v[28:31], v[100:103], a[72:79], v22\n\t" "v_smfmac_f32_16x16x64_bf16 v[28:31], v[104:107], a[80:87], v22\n\t" \
   "v_smfmac_f32_16x16x64_bf16 v[28:31], v[108:111], a[88:95], v22\n\t" "v_smfmac_f32_16x16x64_bf16 v[28:31], v[112:115], a[96:103], v22\n\t" \
   "v_smfmac_f32_16x16x64_bf16 v[28:31], v[116:119], a[104:111], v22\n\t" "v_smfmac_f32_16x16x64_bf16 v[28:31], v[120:123], a[112:119], v22\n\t" \
   "v_smfmac_f32_16x16x64_bf16 v[28:31], v[124:127], a[120:127], v22\n\t" "v_smfmac_f32_16x16x64_bf16 v[28:31], v[128:131], v[32:39], v22\n\t" \
   "v_smfmac_f32_16x16x64_bf16 v[28:31], v[132:135], v[40:47], v22\n\t" "v_smfmac_f32_16x16x64_bf16 v[28:31], v[136:139], v[48:55], v22\n\t" \
   "v_smfmac_f32_16x16x64_bf16 v[28:31], v[140:143], v[56:63], v22\n\t" "84:\n\t" "v_cmp_eq_u32 vcc, -1, v27\n\t" "s_cmp_gt_u32 s46, 0x10000\n\t" \
   "s_cbranch_scc1 83f\n\t" "s_cbranch_vccz 83f\n\t" "85:\n\t" "s_add_u32 s46, s46, 1\n\t" "s_cmp_gt_u32 s46, 2\n\t" "s_cbranch_scc1 81f\n\t" \
   "s_lshl_b32 s74, s46, 3\n\t" "s_lshl_b32 s74, 1, s74\n\t" "s_add_u32 s77, s77, s74\n\t" "81:\n\t" "s_min_u32 s74, s46, 7\n\t" \
   "s_lshr_b32 s64, s74, 1\n\t" "s_add_u32 s64, s64, %[xs]\n\t" "s_and_b32 s64, s64, 3\n\t" "s_lshl_b32 s64, s64, 11\n\t" "s_and_b32 s75, s74, 1\n\t" \
   "s_lshl_b32 s75, s75, 14\n\t" "s_xor_b32 s75, s75, s47\n\t" "s_xor_b32 s75, s75, %[lad0]\n\t" "s_and_b32 s75, s75, 0x4000\n\t" \
   "s_add_u32 s64, s64, s75\n\t" "s_add_u32 s64, s64, s41\n\t" "s_add_u32 s65, s64, 32768\n\t" "s_add_u32 s66, s64, 65536\n\t" \
   "s_add_u32 s67, s64, 98304\n\t" "s_add_u32 s68, s64, 131072\n\t" "s_add_u32 s69, s64, 163840\n\t" "s_add_u32 s70, s64, 196608\n\t" \
   "s_add_u32 s71, s64, 229376\n\t" "s_add_u32 s72, s64, 262144\n\t" "s_add_u32 s73, s64, 294912\n\t" "v_mov_b32 v28, 0\n\t" "v_mov_b32 v29, 0\n\t" \
   "v_mov_b32 v30, 0\n\t" "v_mov_b32 v31, 0\n\t" "v_mov_b32 v27, 0\n\t" "s_cmp_lt_u32 s46, 7\n\t" "s_cbranch_scc1 82f\n\t" "s_sleep 3\n\t" "82:\n\t" \
   RSTAMP(6) "buffer_load_dwordx4 v[64:67], v16, %[rs], s64 offen offset:0 sc1\n\t" \
   "buffer_load_dwordx4 v[68:71], v16, %[rs], s64 offen offset:1024 sc1\n\t" "buffer_load_dwordx4 v[72:75], v16, %[rs], s65 offen offset:0 sc1\n\t" \
   "buffer_load_dwordx4 v[76:79], v16, %[rs], s65 offen offset:1024 sc1\n\t" "buffer_load_dwordx4 v[80:83], v16, %[rs], s66 offen offset:0 sc1\n\t" \
   "buffer_load_dwordx4 v[84:87], v16, %[rs], s66 offen offset:1024 sc1\n\t" "buffer_load_dwordx4 v[88:91], v16, %[rs], s67 offen offset:0 sc1\n\t" \
   "buffer_load_dwordx4 v[92:95], v16, %[rs], s67 offen offset:1024 sc1\n\t" "buffer_load_dwordx4 v[96:99], v16, %[rs], s68 offen offset:0 sc1\n\t" \
   "buffer_load_dwordx4 v[100:103], v16, %[rs], s68 offen offset:1024 sc1\n\t" "buffer_load_dwordx4 v[104:107], v16, %[rs], s69 offen offset:0 sc1\n\t" \
   "buffer_load_dwordx4 v[108:111], v16, %[rs], s69 offen offset:1024 sc1\n\t" "buffer_load_dwordx4 v[112:115], v16, %[rs], s70 offen offset:0 sc1\n\t" \
   "buffer_load_dwordx4 v[116:119], v16, %[rs], s70 offen offset:1024 sc1\n\t" "buffer_load_dwordx4 v[120:123], v16, %[rs], s71 offen offset:0 sc1\n\t" \
   "buffer_load_dwordx4 v[124:127], v16, %[rs], s71 offen offset:1024 sc1\n\t" "buffer_load_dwordx2 v[128:129], v17, %[rs], s72 offen offset:0 sc1\n\t" \
   "buffer_load_dwordx2 v[130:131], v17, %[rs], s72 offen offset:128 sc1\n\t" \
   "buffer_load_dwordx2 v[132:133], v17, %[rs], s72 offen offset:1024 sc1\n\t" \
   "buffer_load_dwordx2 v[134:135], v17, %[rs], s72 offen offset:1152 sc1\n\t" "buffer_load_dwordx2 v[136:137], v17, %[rs], s73 offen offset:0 sc1\n\t" \
   "buffer_load_dwordx2 v[138:139], v17, %[rs], s73 offen offset:128 sc1\n\t" \
   "buffer_load_dwordx2 v[140:141], v17, %[rs], s73 offen offset:1024 sc1\n\t" \
   "buffer_load_dwordx2 v[142:143], v17, %[rs], s73 offen offset:1152 sc1\n\t" "s_waitcnt vmcnt(23)\n\t" \
   "v_smfmac_f32_16x16x64_bf16 v[28:31], v[64:67], a[0:7], v22\n\t" "v_max3_u32 v27, v27, v64, v66\n\t" "s_waitcnt vmcnt(22)\n\t" \
   "v_smfmac_f32_16x16x64_bf16 v[28:31], v[68:71], a[8:15], v22\n\t" "v_max3_u32 v27, v27, v68, v70\n\t" "s_waitcnt vmcnt(21)\n\t" \
   "v_smfmac_f32_16x16x64_bf16 v[28:31], v[72:75], a[16:23], v22\n\t" "v_max3_u32 v27, v27, v72, v74\n\t" "s_waitcnt vmcnt(20)\n\t" \
   "v_smfmac_f32_16x16x64_bf16 v[28:31], v[76:79], a[24:31], v22\n\t" "v_max3_u32 v27, v27, v76, v78\n\t" "s_waitcnt vmcnt(19)\n\t" \
   "v_smfmac_f32_16x16x64_bf16 v[28:31], v[80:83], a[32:39], v22\n\t" "v_max3_u32 v27, v27, v80, v82\n\t" "s_waitcnt vmcnt(18)\n\t" \
   "v_smfmac_f32_16x16x64_bf16 v[28:31], v[84:87], a[40:47], v22\n\t" "v_max3_u32 v27, v27, v84, v86\n\t" "s_waitcnt vmcnt(17)\n\t" \
   "v_smfmac_f32_16x16x64_bf16 v[28:31], v[88:91], a[48:55], v22\n\t" "v_max3_u32 v27, v27, v88, v90\n\t" "s_waitcnt vmcnt(16)\n\t" \
   "v_smfmac_f32_16x16x64_bf16 v[28:31], v[92:95], a[56:63], v22\n\t" "v_max3_u32 v27, v27, v92, v94\n\t" "s_waitcnt vmcnt(15)\n\t" \
   "v_smfmac_f32_16x16x64_bf16 v[28:31], v[96:99], a[64:71], v22\n\t" "v_max3_u32 v27, v27, v96, v98\n\t" "s_waitcnt vmcnt(14)\n\t" \
   "v_smfmac_f32_16x16x64_bf16 v[28:31], v[100:103], a[72:79], v22\n\t" "v_max3_u32 v27, v27, v100, v102\n\t" "s_waitcnt vmcnt(13)\n\t" \
   "v_smfmac_f32_16x16x64_bf16 v[28:31], v[104:107], a[80:87], v22\n\t" "v_max3_u32 v27, v27, v104, v106\n\t" "s_waitcnt vmcnt(12)\n\t" \
   "v_smfmac_f32_16x16x64_bf16 v[28:31], v[108:111], a[88:95], v22\n\t" "v_max3_u32 v27, v27, v108, v110\n\t" "s_waitcnt vmcnt(11)\n\t" \
   "v_smfmac_f32_16x16x64_bf16 v[28:31], v[112:115], a[96:103], v22\n\t" "v_max3_u32 v27, v27, v112, v114\n\t" "s_waitcnt vmcnt(10)\n\t" \
   "v_smfmac_f32_16x16x64_bf16 v[28:31], v[116:119], a[104:111], v22\n\t" "v_max3_u32 v27, v27, v116, v118\n\t" "s_waitcnt vmcnt(9)\n\t" \
   "v_smfmac_f32_16x16x64_bf16 v[28:31], v[120:123], a[112:119], v22\n\t" "v_max3_u32 v27, v27, v120, v122\n\t" "s_waitcnt vmcnt(8)\n\t" \
   "v_smfmac_f32_16x16x64_bf16 v[28:31], v[124:127], a[120:127], v22\n\t" "v_max3_u32 v27, v27, v124, v126\n\t" "s_waitcnt vmcnt(6)\n\t" \
   "v_smfmac_f32_16x16x64_bf16 v[28:31], v[128:131], v[32:39], v22\n\t" "v_max3_u32 v27, v27, v128, v130\n\t" "s_waitcnt vmcnt(4)\n\t" \
   "v_smfmac_f32_16x16x64_bf16 v[28:31], v[132:135], v[40:47], v22\n\t" "v_max3_u32 v27, v27, v132, v134\n\t" "s_waitcnt vmcnt(2)\n\t" \
   "v_smfmac_f32_16x16x64_bf16 v[28:31], v[136:139], v[48:55], v22\n\t" "v_max3_u32 v27, v27, v136, v138\n\t" "s_waitcnt vmcnt(0)\n\t" \
   "v_smfmac_f32_16x16x64_bf16 v[28:31], v[140:143], v[56:63], v22\n\t" "v_max3_u32 v27, v27, v140, v142\n\t" "s_branch 84b\n\t" "83:\n\t" \
   "s_or_b32 s46, s46, 0x10000000\n\t" "s_nop 4\n\t" "v_add_f32 v28, v28, v29\n\t" "v_add_f32 v30, v30, v31\n\t" "v_cvt_pk_bf16_f32 v24, v28, v30\n\t" \
   "s_nop 1\n\t" "v_mov_b32_dpp v25, v24 quad_perm:[1,0,3,2] row_mask:0xf bank_mask:0xf\n\t" "s_branch 2b\n\t" "95:\n\t" "s_mov_b32 s74, 0\n\t" \
   "97:\n\t" "buffer_load_dwordx4 v[8:11], v19, %[rs], s52 offen sc1\n\t" "s_add_u32 s74, s74, 1\n\t" "s_waitcnt vmcnt(0)\n\t" \
   "v_max3_u32 v15, v8, v9, v10\n\t" "v_max_u32 v15, v15, v11\n\t" "v_cmp_eq_u32 vcc, -1, v15\n\t" "s_cmp_gt_u32 s74, 0x10000\n\t" \
   "s_cbranch_scc1 96b\n\t" "s_cbranch_vccz 96b\n\t" "s_sleep 2\n\t" "s_branch 97b\n\t" "90:\n\t" "s_waitcnt vmcnt(0) lgkmcnt(0)"
#define FIN_CLOB "v8", "v9", "v10", "v11", "v12", "v13", "v14", "v15", "v24", "v25", "v26", "v27", "v28", "v29", "v30", "v31", "v32", "v33", "v34", "v35", "v36", "v37", "v38", "v39", "v40", "v41", "v42", "v43", "v44", "v45", "v46", "v47", "v48", "v49", "v50", "v51", "v52", "v53", "v54", "v55", "v56", "v57", "v58", "v59", "v60", "v61", "v62", "v63", "v64", "v65", "v66", "v67", "v68", "v69", "v70", "v71", "v72", "v73", "v74", "v75", "v76", "v77", "v78", "v79", "v80", "v81", "v82", "v83", "v84", "v85", "v86", "v87", "v88", "v89", "v90", "v91", "v92", "v93", "v94", "v95", "v96", "v97", "v98", "v99", "v100", "v101", "v102", "v103", "v104", "v105", "v106", "v107", "v108", "v109", "v110", "v111", "v112", "v113", "v114", "v115", "v116", "v117", "v118", "v119", "v120", "v121", "v122", "v123", "v124", "v125", "v126", "v127", "v128", "v129", "v130", "v131", "v132", "v133", "v134", "v135", "v136", "v137", "v138", "v139", "v140", "v141", "v142", "v143", "v144", "v145", "v146", "v147", "v148", "v149", "v150", "v151", "v152", "v153", "v154", "v155", "v156", "v157", "v158", "v159", "v160", "v161", "v162", "v163", "v164", "v165", "v166", "v167", "v168", "v169", "v170", "v171", "v172", "v173", "v174", "v175", "v176", "v177", "v178", "v179", "v180", "v181", "v182", "v183", "v184", "v185", "v186", "v187", "v188", "v189", "v190", "v191", "v192", "v193", "v194", "v195", "v196", "v197", "v198", "v199", "v200", "v201", "v202", "v203", "v204", "v205", "v206", "v207", "v208", "v209", "v210", "v211", "v212", "v213", "v214", "v215", "v216", "v217", "v218", "v219", "v220", "v221", "v222", "v223", "v224", "v225", "v226", "v227", "s40", "s41", "s42", "s43", "s44", "s45", "s46", "s47", "s48", "s49", "s50", "s51", "s52", "s53", "s60", "s61", "s62", "s63", "s64", "s65", "s66", "s67", "s68", "s69", "s70", "s71", "s72", "s73", "s74", "s75", "a0", "a1", "a2", "a3", "a4", "a5", "a6", "a7", "a8", "a9", "a10", "a11", "a12", "a13", "a14", "a15", "a16", "a17", "a18", "a19", "a20", "a21", "a22", "a23", "a24", "a25", "a26", "a27", "a28", "a29", "a30", "a31", "a32", "a33", "a34", "a35", "a36", "a37", "a38", "a39", "a40", "a41", "a42", "a43", "a44", "a45", "a46", "a47", "a48", "a49", "a50", "a51", "a52", "a53", "a54", "a55", "a56", "a57", "a58", "a59", "a60", "a61", "a62", "a63", "a64", "a65", "a66", "a67", "a68", "a69", "a70", "a71", "a72", "a73", "a74", "a75", "a76", "a77", "a78", "a79", "a80", "a81", "a82", "a83", "a84", "a85", "a86", "a87", "a88", "a89", "a90", "a91", "a92", "a93", "a94", "a95", "a96", "a97", "a98", "a99", "a100", "a101", "a102", "a103", "a104", "a105", "a106", "a107", "a108", "a109", "a110", "a111", "a112", "a113", "a114", "a115", "a116", "a117", "a118", "a119", "a120", "a121", "a122", "a123", "a124", "a125", "a126", "a127"
__device__ __forceinline__ void put4(v32u& d, int i, v4u v) { d[4 * i] = v.x; d[4 * i + 1] = v.y; d[4 * i + 2] = v.z; d[4 * i + 3] = v.w; }
#define LDP_ASM \
   "buffer_load_dword v26, v113, %[rw], 0 offen sc1\n\t" "s_mov_b32 s41, 0\n\t" "buffer_load_dwordx4 a[0:3], v16, %[ra], s41 offen\n\t" \
   "s_mov_b32 s41, 131072\n\t" "buffer_load_dwordx4 a[64:67], v16, %[ra], s41 offen\n\t" "s_mov_b32 s41, 524288\n\t" \
   "buffer_load_dwordx4 a[8:11], v16, %[ra], s41 offen\n\t" "s_mov_b32 s41, 655360\n\t" "buffer_load_dwordx4 a[72:75], v16, %[ra], s41 offen\n\t" \
   "s_mov_b32 s41, 1048576\n\t" "buffer_load_dwordx4 a[16:19], v16, %[ra], s41 offen\n\t" "s_mov_b32 s41, 1179648\n\t" \
   "buffer_load_dwordx4 a[80:83], v16, %[ra], s41 offen\n\t" "s_mov_b32 s41, 1572864\n\t" "buffer_load_dwordx4 a[24:27], v16, %[ra], s41 offen\n\t" \
   "s_mov_b32 s41, 1703936\n\t" "buffer_load_dwordx4 a[88:91], v16, %[ra], s41 offen\n\t" "s_mov_b32 s41, 2097152\n\t" \
   "buffer_load_dwordx4 a[32:35], v16, %[ra], s41 offen\n\t" "s_mov_b32 s41, 2228224\n\t" "buffer_load_dwordx4 a[96:99], v16, %[ra], s41 offen\n\t" \
   "s_mov_b32 s41, 2621440\n\t" "buffer_load_dwordx4 a[40:43], v16, %[ra], s41 offen\n\t" "s_mov_b32 s41, 2752512\n\t" \
   "buffer_load_dwordx4 a[104:107], v16, %[ra], s41 offen\n\t" "s_mov_b32 s41, 3145728\n\t" "buffer_load_dwordx4 a[48:51], v16, %[ra], s41 offen\n\t" \
   "s_mov_b32 s41, 3276800\n\t" "buffer_load_dwordx4 a[112:115], v16, %[ra], s41 offen\n\t" "s_mov_b32 s41, 3670016\n\t" \
   "buffer_load_dwordx4 a[56:59], v16, %[ra], s41 offen\n\t" "s_mov_b32 s41, 3801088\n\t" "buffer_load_dwordx4 a[120:123], v16, %[ra], s41 offen\n\t" \
   "s_waitcnt vmcnt(16)\n\t" "v_add_u32 v26, 1, v26\n\t" "buffer_store_dword v26, v114, %[rw], 0 offen sc1\n\t" "ds_write_b32 v115, v26\n\t" LDSTAMP(0) \
   "s_waitcnt vmcnt(15)\n\t" LDSTAMP(1) "ds_write_b128 v20, a[0:3] offset:0\n\t" "s_mov_b32 s41, 4194304\n\t" \
   "buffer_load_dwordx4 v[192:195], v16, %[ra], s41 offen\n\t" "s_waitcnt vmcnt(15)\n\t" "ds_write_b128 v20, a[64:67] offset:4096\n\t" \
   "s_mov_b32 s41, 4325376\n\t" "buffer_load_dwordx4 v[208:211], v16, %[ra], s41 offen\n\t" "s_waitcnt vmcnt(15)\n\t" \
   "ds_write_b128 v20, a[8:11] offset:8192\n\t" "s_mov_b32 s41, 4718592\n\t" "buffer_load_dwordx4 v[200:203], v16, %[ra], s41 offen\n\t" \
   "s_waitcnt vmcnt(15)\n\t" "ds_write_b128 v20, a[72:75] offset:12288\n\t" "s_mov_b32 s41, 4849664\n\t" \
   "buffer_load_dwordx4 v[216:219], v16, %[ra], s41 offen\n\t" "s_waitcnt vmcnt(15)\n\t" "ds_write_b128 v20, a[16:19] offset:16384\n\t" \
   "s_mov_b32 s41, 0\n\t" "buffer_load_dwordx4 a[128:131], v16, %[rb], s41 offen\n\t" "s_waitcnt vmcnt(15)\n\t" \
   "ds_write_b128 v20, a[80:83] offset:20480\n\t" "s_mov_b32 s41, 131072\n\t" "buffer_load_dwordx4 a[192:195], v16, %[rb], s41 offen\n\t" \
   "s_waitcnt vmcnt(15)\n\t" "ds_write_b128 v20, a[24:27] offset:24576\n\t" "s_mov_b32 s41, 524288\n\t" \
   "buffer_load_dwordx4 a[136:139], v16, %[rb], s41 offen\n\t" "s_waitcnt vmcnt(15)\n\t" "ds_write_b128 v20, a[88:91] offset:28672\n\t" \
   "s_mov_b32 s41, 655360\n\t" "buffer_load_dwordx4 a[200:203], v16, %[rb], s41 offen\n\t" "s_waitcnt vmcnt(15)\n\t" \
   "ds_write_b128 v20, a[32:35] offset:32768\n\t" "s_mov_b32 s41, 1048576\n\t" "buffer_load_dwordx4 a[144:147], v16, %[rb], s41 offen\n\t" \
   "s_waitcnt vmcnt(15)\n\t" "ds_write_b128 v20, a[96:99] offset:36864\n\t" "s_mov_b32 s41, 1179648\n\t" \
   "buffer_load_dwordx4 a[208:211], v16, %[rb], s41 offen\n\t" "s_waitcnt vmcnt(15)\n\t" "ds_write_b128 v20, a[40:43] offset:40960\n\t" \
   "s_mov_b32 s41, 1572864\n\t" "buffer_load_dwordx4 a[152:155], v16, %[rb], s41 offen\n\t" "s_waitcnt vmcnt(15)\n\t" \
   "ds_write_b128 v20, a[104:107] offset:45056\n\t" "s_mov_b32 s41, 1703936\n\t" "buffer_load_dwordx4 a[216:219], v16, %[rb], s41 offen\n\t" \
   "s_waitcnt vmcnt(15)\n\t" "ds_write_b128 v20, a[48:51] offset:49152\n\t" "s_mov_b32 s41, 2097152\n\t" \
   "buffer_load_dwordx4 a[160:163], v16, %[rb], s41 offen\n\t" "s_waitcnt vmcnt(15)\n\t" "ds_write_b128 v20, a[112:115] offset:53248\n\t" \
   "s_mov_b32 s41, 2228224\n\t" "buffer_load_dwordx4 a[224:227], v16, %[rb], s41 offen\n\t" "s_waitcnt vmcnt(15)\n\t" \
   "ds_write_b128 v20, a[56:59] offset:57344\n\t" "s_mov_b32 s41, 2621440\n\t" "buffer_load_dwordx4 a[168:171], v16, %[rb], s41 offen\n\t" \
   "s_waitcnt vmcnt(15)\n\t" "ds_write_b128 v20, a[120:123] offset:61440\n\t" "s_mov_b32 s41, 2752512\n\t" \
   "buffer_load_dwordx4 a[232:235], v16, %[rb], s41 offen\n\t" "s_waitcnt vmcnt(15)\n\t" "ds_write_b128 v23, v[192:195] offset:0\n\t" \
   "s_mov_b32 s41, 3145728\n\t" "buffer_load_dwordx4 a[176:179], v16, %[rb], s41 offen\n\t" "s_waitcnt vmcnt(15)\n\t" \
   "ds_write_b128 v23, v[208:211] offset:4096\n\t" "s_mov_b32 s41, 3276800\n\t" "buffer_load_dwordx4 a[240:243], v16, %[rb], s41 offen\n\t" \
   "s_waitcnt vmcnt(15)\n\t" "ds_write_b128 v23, v[200:203] offset:8192\n\t" "s_mov_b32 s41, 3670016\n\t" \
   "buffer_load_dwordx4 a[184:187], v16, %[rb], s41 offen\n\t" "s_waitcnt vmcnt(15)\n\t" "ds_write_b128 v23, v[216:219] offset:12288\n\t" \
   "s_mov_b32 s41, 3801088\n\t" "buffer_load_dwordx4 a[248:251], v16, %[rb], s41 offen\n\t" "s_waitcnt lgkmcnt(0)\n\t" "s_barrier\n\t" LDSTAMP(2) \
   "s_lshl_b32 s40, %[p0], 13\n\t" "v_add_u32 v24, s40, v18\n\t" "v_add_u32 v25, s40, v19\n\t" "s_add_u32 s40, s40, 8192\n\t" \
   "s_cmp_eq_u32 s40, 81920\n\t" "s_cselect_b32 s40, 0, s40\n\t" "ds_read_b64_tr_b16 a[0:1], v24 offset:0\n\t" \
   "ds_read_b64_tr_b16 a[2:3], v24 offset:4096\n\t" "ds_read_b64_tr_b16 a[64:65], v25 offset:0\n\t" "ds_read_b64_tr_b16 a[66:67], v25 offset:4096\n\t" \
   "v_add_u32 v24, s40, v18\n\t" "v_add_u32 v25, s40, v19\n\t" "s_add_u32 s40, s40, 8192\n\t" "s_cmp_eq_u32 s40, 81920\n\t" \
   "s_cselect_b32 s40, 0, s40\n\t" "ds_read_b64_tr_b16 a[8:9], v24 offset:0\n\t" "ds_read_b64_tr_b16 a[10:11], v24 offset:4096\n\t" \
   "ds_read_b64_tr_b16 a[72:73], v25 offset:0\n\t" "ds_read_b64_tr_b16 a[74:75], v25 offset:4096\n\t" "v_add_u32 v24, s40, v18\n\t" \
   "v_add_u32 v25, s40, v19\n\t" "s_add_u32 s40, s40, 8192\n\t" "s_cmp_eq_u32 s40, 81920\n\t" "s_cselect_b32 s40, 0, s40\n\t" \
   "ds_read_b64_tr_b16 a[16:17], v24 offset:0\n\t" "ds_read_b64_tr_b16 a[18:19], v24 offset:4096\n\t" "ds_read_b64_tr_b16 a[80:81], v25 offset:0\n\t" \
   "ds_read_b64_tr_b16 a[82:83], v25 offset:4096\n\t" "v_add_u32 v24, s40, v18\n\t" "v_add_u32 v25, s40, v19\n\t" "s_add_u32 s40, s40, 8192\n\t" \
   "s_cmp_eq_u32 s40, 81920\n\t" "s_cselect_b32 s40, 0, s40\n\t" "ds_read_b64_tr_b16 a[24:25], v24 offset:0\n\t" \
   "ds_read_b64_tr_b16 a[26:27], v24 offset:4096\n\t" "ds_read_b64_tr_b16 a[88:89], v25 offset:0\n\t" \
   "ds_read_b64_tr_b16 a[90:91], v25 offset:4096\n\t" "v_add_u32 v24, s40, v18\n\t" "v_add_u32 v25, s40, v19\n\t" "s_add_u32 s40, s40, 8192\n\t" \
   "s_cmp_eq_u32 s40, 81920\n\t" "s_cselect_b32 s40, 0, s40\n\t" "ds_read_b64_tr_b16 a[32:33], v24 offset:0\n\t" \
   "ds_read_b64_tr_b16 a[34:35], v24 offset:4096\n\t" "ds_read_b64_tr_b16 a[96:97], v25 offset:0\n\t" \
   "ds_read_b64_tr_b16 a[98:99], v25 offset:4096\n\t" "v_add_u32 v24, s40, v18\n\t" "v_add_u32 v25, s40, v19\n\t" "s_add_u32 s40, s40, 8192\n\t" \
   "s_cmp_eq_u32 s40, 81920\n\t" "s_cselect_b32 s40, 0, s40\n\t" "ds_read_b64_tr_b16 a[40:41], v24 offset:0\n\t" \
   "ds_read_b64_tr_b16 a[42:43], v24 offset:4096\n\t" "ds_read_b64_tr_b16 a[104:105], v25 offset:0\n\t" \
   "ds_read_b64_tr_b16 a[106:107], v25 offset:4096\n\t" "v_add_u32 v24, s40, v18\n\t" "v_add_u32 v25, s40, v19\n\t" "s_add_u32 s40, s40, 8192\n\t" \
   "s_cmp_eq_u32 s40, 81920\n\t" "s_cselect_b32 s40, 0, s40\n\t" "ds_read_b64_tr_b16 a[48:49], v24 offset:0\n\t" \
   "ds_read_b64_tr_b16 a[50:51], v24 offset:4096\n\t" "ds_read_b64_tr_b16 a[112:113], v25 offset:0\n\t" \
   "ds_read_b64_tr_b16 a[114:115], v25 offset:4096\n\t" "v_add_u32 v24, s40, v18\n\t" "v_add_u32 v25, s40, v19\n\t" "s_add_u32 s40, s40, 8192\n\t" \
   "s_cmp_eq_u32 s40, 81920\n\t" "s_cselect_b32 s40, 0, s40\n\t" "ds_read_b64_tr_b16 a[56:57], v24 offset:0\n\t" \
   "ds_read_b64_tr_b16 a[58:59], v24 offset:4096\n\t" "ds_read_b64_tr_b16 a[120:121], v25 offset:0\n\t" \
   "ds_read_b64_tr_b16 a[122:123], v25 offset:4096\n\t" "v_add_u32 v24, s40, v18\n\t" "v_add_u32 v25, s40, v19\n\t" "s_add_u32 s40, s40, 8192\n\t" \
   "s_cmp_eq_u32 s40, 81920\n\t" "s_cselect_b32 s40, 0, s40\n\t" "ds_read_b64_tr_b16 v[192:193], v24 offset:0\n\t" \
   "ds_read_b64_tr_b16 v[194:195], v24 offset:4096\n\t" "ds_read_b64_tr_b16 v[208:209], v25 offset:0\n\t" \
   "ds_read_b64_tr_b16 v[210:211], v25 offset:4096\n\t" "v_add_u32 v24, s40, v18\n\t" "v_add_u32 v25, s40, v19\n\t" "s_add_u32 s40, s40, 8192\n\t" \
   "s_cmp_eq_u32 s40, 81920\n\t" "s_cselect_b32 s40, 0, s40\n\t" "ds_read_b64_tr_b16 v[200:201], v24 offset:0\n\t" \
   "ds_read_b64_tr_b16 v[202:203], v24 offset:4096\n\t" "ds_read_b64_tr_b16 v[216:217], v25 offset:0\n\t" \
   "ds_read_b64_tr_b16 v[218:219], v25 offset:4096\n\t" "s_waitcnt vmcnt(15)\n\t" "s_waitcnt lgkmcnt(0)\n\t" "s_barrier\n\t" \
   "ds_write_b128 v20, a[128:131] offset:0\n\t" "s_mov_b32 s41, 4194304\n\t" "buffer_load_dwordx4 v[224:227], v16, %[rb], s41 offen\n\t" \
   "s_waitcnt vmcnt(15)\n\t" "ds_write_b128 v20, a[192:195] offset:4096\n\t" "s_mov_b32 s41, 4325376\n\t" \
   "buffer_load_dwordx4 v[240:243], v16, %[rb], s41 offen\n\t" "s_waitcnt vmcnt(15)\n\t" "ds_write_b128 v20, a[136:139] offset:8192\n\t" \
   "s_mov_b32 s41, 4718592\n\t" "buffer_load_dwordx4 v[232:235], v16, %[rb], s41 offen\n\t" "s_waitcnt vmcnt(15)\n\t" \
   "ds_write_b128 v20, a[200:203] offset:12288\n\t" "s_mov_b32 s41, 4849664\n\t" "buffer_load_dwordx4 v[248:251], v16, %[rb], s41 offen\n\t" \
   "s_waitcnt vmcnt(15)\n\t" "ds_write_b128 v20, a[144:147] offset:16384\n\t" "s_mov_b32 s41, 262144\n\t" \
   "buffer_load_dwordx4 a[4:7], v16, %[ra], s41 offen\n\t" "s_waitcnt vmcnt(15)\n\t" "ds_write_b128 v20, a[208:211] offset:20480\n\t" \
   "s_mov_b32 s41, 393216\n\t" "buffer_load_dwordx4 a[68:71], v16, %[ra], s41 offen\n\t" "s_waitcnt vmcnt(15)\n\t" \
   "ds_write_b128 v20, a[152:155] offset:24576\n\t" "s_mov_b32 s41, 786432\n\t" "buffer_load_dwordx4 a[12:15], v16, %[ra], s41 offen\n\t" \
   "s_waitcnt vmcnt(15)\n\t" "ds_write_b128 v20, a[216:219] offset:28672\n\t" "s_mov_b32 s41, 917504\n\t" \
   "buffer_load_dwordx4 a[76:79], v16, %[ra], s41 offen\n\t" "s_waitcnt vmcnt(15)\n\t" "ds_write_b128 v20, a[160:163] offset:32768\n\t" \
   "s_mov_b32 s41, 1310720\n\t" "buffer_load_dwordx4 a[20:23], v16, %[ra], s41 offen\n\t" "s_waitcnt vmcnt(15)\n\t" \
   "ds_write_b128 v20, a[224:227] offset:36864\n\t" "s_mov_b32 s41, 1441792\n\t" "buffer_load_dwordx4 a[84:87], v16, %[ra], s41 offen\n\t" \
   "s_waitcnt vmcnt(15)\n\t" "ds_write_b128 v20, a[168:171] offset:40960\n\t" "s_mov_b32 s41, 1835008\n\t" \
   "buffer_load_dwordx4 a[28:31], v16, %[ra], s41 offen\n\t" "s_waitcnt vmcnt(15)\n\t" "ds_write_b128 v20, a[232:235] offset:45056\n\t" \
   "s_mov_b32 s41, 1966080\n\t" "buffer_load_dwordx4 a[92:95], v16, %[ra], s41 offen\n\t" "s_waitcnt vmcnt(15)\n\t" \
   "ds_write_b128 v20, a[176:179] offset:49152\n\t" "s_mov_b32 s41, 2359296\n\t" "buffer_load_dwordx4 a[36:39], v16, %[ra], s41 offen\n\t" \
   "s_waitcnt vmcnt(15)\n\t" "ds_write_b128 v20, a[240:243] offset:53248\n\t" "s_mov_b32 s41, 2490368\n\t" \
   "buffer_load_dwordx4 a[100:103], v16, %[ra], s41 offen\n\t" "s_waitcnt vmcnt(15)\n\t" "ds_write_b128 v20, a[184:187] offset:57344\n\t" \
   "s_mov_b32 s41, 2883584\n\t" "buffer_load_dwordx4 a[44:47], v16, %[ra], s41 offen\n\t" "s_waitcnt vmcnt(15)\n\t" \
   "ds_write_b128 v20, a[248:251] offset:61440\n\t" "s_mov_b32 s41, 3014656\n\t" "buffer_load_dwordx4 a[108:111], v16, %[ra], s41 offen\n\t" \
   "s_waitcnt vmcnt(15)\n\t" "ds_write_b128 v23, v[224:227] offset:0\n\t" "s_mov_b32 s41, 3407872\n\t" \
   "buffer_load_dwordx4 a[52:55], v16, %[ra], s41 offen\n\t" "s_waitcnt vmcnt(15)\n\t" "ds_write_b128 v23, v[240:243] offset:4096\n\t" \
   "s_mov_b32 s41, 3538944\n\t" "buffer_load_dwordx4 a[116:119], v16, %[ra], s41 offen\n\t" "s_waitcnt vmcnt(15)\n\t" \
   "ds_write_b128 v23, v[232:235] offset:8192\n\t" "s_mov_b32 s41, 3932160\n\t" "buffer_load_dwordx4 a[60:63], v16, %[ra], s41 offen\n\t" \
   "s_waitcnt vmcnt(15)\n\t" "ds_write_b128 v23, v[248:251] offset:12288\n\t" "s_mov_b32 s41, 4063232\n\t" \
   "buffer_load_dwordx4 a[124:127], v16, %[ra], s41 offen\n\t" "s_waitcnt lgkmcnt(0)\n\t" "s_barrier\n\t" LDSTAMP(3) "s_lshl_b32 s40, %[p0], 13\n\t" \
   "v_add_u32 v24, s40, v18\n\t" "v_add_u32 v25, s40, v19\n\t" "s_add_u32 s40, s40, 8192\n\t" "s_cmp_eq_u32 s40, 81920\n\t" \
   "s_cselect_b32 s40, 0, s40\n\t" "ds_read_b64_tr_b16 a[128:129], v24 offset:0\n\t" "ds_read_b64_tr_b16 a[130:131], v24 offset:4096\n\t" \
   "ds_read_b64_tr_b16 a[192:193], v25 offset:0\n\t" "ds_read_b64_tr_b16 a[194:195], v25 offset:4096\n\t" "v_add_u32 v24, s40, v18\n\t" \
   "v_add_u32 v25, s40, v19\n\t" "s_add_u32 s40, s40, 8192\n\t" "s_cmp_eq_u32 s40, 81920\n\t" "s_cselect_b32 s40, 0, s40\n\t" \
   "ds_read_b64_tr_b16 a[136:137], v24 offset:0\n\t" "ds_read_b64_tr_b16 a[138:139], v24 offset:4096\n\t" \
   "ds_read_b64_tr_b16 a[200:201], v25 offset:0\n\t" "ds_read_b64_tr_b16 a[202:203], v25 offset:4096\n\t" "v_add_u32 v24, s40, v18\n\t" \
   "v_add_u32 v25, s40, v19\n\t" "s_add_u32 s40, s40, 8192\n\t" "s_cmp_eq_u32 s40, 81920\n\t" "s_cselect_b32 s40, 0, s40\n\t" \
   "ds_read_b64_tr_b16 a[144:145], v24 offset:0\n\t" "ds_read_b64_tr_b16 a[146:147], v24 offset:4096\n\t" \
   "ds_read_b64_tr_b16 a[208:209], v25 offset:0\n\t" "ds_read_b64_tr_b16 a[210:211], v25 offset:4096\n\t" "v_add_u32 v24, s40, v18\n\t" \
   "v_add_u32 v25, s40, v19\n\t" "s_add_u32 s40, s40, 8192\n\t" "s_cmp_eq_u32 s40, 81920\n\t" "s_cselect_b32 s40, 0, s40\n\t" \
   "ds_read_b64_tr_b16 a[152:153], v24 offset:0\n\t" "ds_read_b64_tr_b16 a[154:155], v24 offset:4096\n\t" \
   "ds_read_b64_tr_b16 a[216:217], v25 offset:0\n\t" "ds_read_b64_tr_b16 a[218:219], v25 offset:4096\n\t" "v_add_u32 v24, s40, v18\n\t" \
   "v_add_u32 v25, s40, v19\n\t" "s_add_u32 s40, s40, 8192\n\t" "s_cmp_eq_u32 s40, 81920\n\t" "s_cselect_b32 s40, 0, s40\n\t" \
   "ds_read_b64_tr_b16 a[160:161], v24 offset:0\n\t" "ds_read_b64_tr_b16 a[162:163], v24 offset:4096\n\t" \
   "ds_read_b64_tr_b16 a[224:225], v25 offset:0\n\t" "ds_read_b64_tr_b16 a[226:227], v25 offset:4096\n\t" "v_add_u32 v24, s40, v18\n\t" \
   "v_add_u32 v25, s40, v19\n\t" "s_add_u32 s40, s40, 8192\n\t" "s_cmp_eq_u32 s40, 81920\n\t" "s_cselect_b32 s40, 0, s40\n\t" \
   "ds_read_b64_tr_b16 a[168:169], v24 offset:0\n\t" "ds_read_b64_tr_b16 a[170:171], v24 offset:4096\n\t" \
   "ds_read_b64_tr_b16 a[232:233], v25 offset:0\n\t" "ds_read_b64_tr_b16 a[234:235], v25 offset:4096\n\t" "v_add_u32 v24, s40, v18\n\t" \
   "v_add_u32 v25, s40, v19\n\t" "s_add_u32 s40, s40, 8192\n\t" "s_cmp_eq_u32 s40, 81920\n\t" "s_cselect_b32 s40, 0, s40\n\t" \
   "ds_read_b64_tr_b16 a[176:177], v24 offset:0\n\t" "ds_read_b64_tr_b16 a[178:179], v24 offset:4096\n\t" \
   "ds_read_b64_tr_b16 a[240:241], v25 offset:0\n\t" "ds_read_b64_tr_b16 a[242:243], v25 offset:4096\n\t" "v_add_u32 v24, s40, v18\n\t" \
   "v_add_u32 v25, s40, v19\n\t" "s_add_u32 s40, s40, 8192\n\t" "s_cmp_eq_u32 s40, 81920\n\t" "s_cselect_b32 s40, 0, s40\n\t" \
   "ds_read_b64_tr_b16 a[184:185], v24 offset:0\n\t" "ds_read_b64_tr_b16 a[186:187], v24 offset:4096\n\t" \
   "ds_read_b64_tr_b16 a[248:249], v25 offset:0\n\t" "ds_read_b64_tr_b16 a[250:251], v25 offset:4096\n\t" "v_add_u32 v24, s40, v18\n\t" \
   "v_add_u32 v25, s40, v19\n\t" "s_add_u32 s40, s40, 8192\n\t" "s_cmp_eq_u32 s40, 81920\n\t" "s_cselect_b32 s40, 0, s40\n\t" \
   "ds_read_b64_tr_b16 v[224:225], v24 offset:0\n\t" "ds_read_b64_tr_b16 v[226:227], v24 offset:4096\n\t" \
   "ds_read_b64_tr_b16 v[240:241], v25 offset:0\n\t" "ds_read_b64_tr_b16 v[242:243], v25 offset:4096\n\t" "v_add_u32 v24, s40, v18\n\t" \
   "v_add_u32 v25, s40, v19\n\t" "s_add_u32 s40, s40, 8192\n\t" "s_cmp_eq_u32 s40, 81920\n\t" "s_cselect_b32 s40, 0, s40\n\t" \
   "ds_read_b64_tr_b16 v[232:233], v24 offset:0\n\t" "ds_read_b64_tr_b16 v[234:235], v24 offset:4096\n\t" \
   "ds_read_b64_tr_b16 v[248:249], v25 offset:0\n\t" "ds_read_b64_tr_b16 v[250:251], v25 offset:4096\n\t" "s_waitcnt vmcnt(15)\n\t" \
   "s_waitcnt lgkmcnt(0)\n\t" "s_barrier\n\t" "ds_write_b128 v20, a[4:7] offset:0\n\t" "s_mov_b32 s41, 4456448\n\t" \
   "buffer_load_dwordx4 v[196:199], v16, %[ra], s41 offen\n\t" "s_waitcnt vmcnt(15)\n\t" "ds_write_b128 v20, a[68:71] offset:4096\n\t" \
   "s_mov_b32 s41, 4587520\n\t" "buffer_load_dwordx4 v[212:215], v16, %[ra], s41 offen\n\t" "s_waitcnt vmcnt(15)\n\t" \
   "ds_write_b128 v20, a[12:15] offset:8192\n\t" "s_mov_b32 s41, 4980736\n\t" "buffer_load_dwordx4 v[204:207], v16, %[ra], s41 offen\n\t" \
   "s_waitcnt vmcnt(15)\n\t" "ds_write_b128 v20, a[76:79] offset:12288\n\t" "s_mov_b32 s41, 5111808\n\t" \
   "buffer_load_dwordx4 v[220:223], v16, %[ra], s41 offen\n\t" "s_waitcnt vmcnt(15)\n\t" "ds_write_b128 v20, a[20:23] offset:16384\n\t" \
   "s_mov_b32 s41, 262144\n\t" "buffer_load_dwordx4 a[132:135], v16, %[rb], s41 offen\n\t" "s_waitcnt vmcnt(15)\n\t" \
   "ds_write_b128 v20, a[84:87] offset:20480\n\t" "s_mov_b32 s41, 393216\n\t" "buffer_load_dwordx4 a[196:199], v16, %[rb], s41 offen\n\t" \
   "s_waitcnt vmcnt(15)\n\t" "ds_write_b128 v20, a[28:31] offset:24576\n\t" "s_mov_b32 s41, 786432\n\t" \
   "buffer_load_dwordx4 a[140:143], v16, %[rb], s41 offen\n\t" "s_waitcnt vmcnt(15)\n\t" "ds_write_b128 v20, a[92:95] offset:28672\n\t" \
   "s_mov_b32 s41, 917504\n\t" "buffer_load_dwordx4 a[204:207], v16, %[rb], s41 offen\n\t" "s_waitcnt vmcnt(15)\n\t" \
   "ds_write_b128 v20, a[36:39] offset:32768\n\t" "s_mov_b32 s41, 1310720\n\t" "buffer_load_dwordx4 a[148:151], v16, %[rb], s41 offen\n\t" \
   "s_waitcnt vmcnt(15)\n\t" "ds_write_b128 v20, a[100:103] offset:36864\n\t" "s_mov_b32 s41, 1441792\n\t" \
   "buffer_load_dwordx4 a[212:215], v16, %[rb], s41 offen\n\t" "s_waitcnt vmcnt(15)\n\t" "ds_write_b128 v20, a[44:47] offset:40960\n\t" \
   "s_mov_b32 s41, 1835008\n\t" "buffer_load_dwordx4 a[156:159], v16, %[rb], s41 offen\n\t" "s_waitcnt vmcnt(15)\n\t" \
   "ds_write_b128 v20, a[108:111] offset:45056\n\t" "s_mov_b32 s41, 1966080\n\t" "buffer_load_dwordx4 a[220:223], v16, %[rb], s41 offen\n\t" \
   "s_waitcnt vmcnt(15)\n\t" "ds_write_b128 v20, a[52:55] offset:49152\n\t" "s_mov_b32 s41, 2359296\n\t" \
   "buffer_load_dwordx4 a[164:167], v16, %[rb], s41 offen\n\t" "s_waitcnt vmcnt(15)\n\t" "ds_write_b128 v20, a[116:119] offset:53248\n\t" \
   "s_mov_b32 s41, 2490368\n\t" "buffer_load_dwordx4 a[228:231], v16, %[rb], s41 offen\n\t" "s_waitcnt vmcnt(15)\n\t" \
   "ds_write_b128 v20, a[60:63] offset:57344\n\t" "s_mov_b32 s41, 2883584\n\t" "buffer_load_dwordx4 a[172:175], v16, %[rb], s41 offen\n\t" \
   "s_waitcnt vmcnt(15)\n\t" "ds_write_b128 v20, a[124:127] offset:61440\n\t" "s_mov_b32 s41, 3014656\n\t" \
   "buffer_load_dwordx4 a[236:239], v16, %[rb], s41 offen\n\t" "s_waitcnt vmcnt(15)\n\t" "ds_write_b128 v23, v[196:199] offset:0\n\t" \
   "s_mov_b32 s41, 3407872\n\t" "buffer_load_dwordx4 a[180:183], v16, %[rb], s41 offen\n\t" "s_waitcnt vmcnt(15)\n\t" \
   "ds_write_b128 v23, v[212:215] offset:4096\n\t" "s_mov_b32 s41, 3538944\n\t" "buffer_load_dwordx4 a[244:247], v16, %[rb], s41 offen\n\t" \
   "s_waitcnt vmcnt(15)\n\t" "ds_write_b128 v23, v[204:207] offset:8192\n\t" "s_mov_b32 s41, 3932160\n\t" \
   "buffer_load_dwordx4 a[188:191], v16, %[rb], s41 offen\n\t" "s_waitcnt vmcnt(15)\n\t" "ds_write_b128 v23, v[220:223] offset:12288\n\t" \
   "s_mov_b32 s41, 4063232\n\t" "buffer_load_dwordx4 a[252:255], v16, %[rb], s41 offen\n\t" "s_waitcnt lgkmcnt(0)\n\t" "s_barrier\n\t" LDSTAMP(4) \
   "s_lshl_b32 s40, %[p0], 13\n\t" "v_add_u32 v24, s40, v18\n\t" "v_add_u32 v25, s40, v19\n\t" "s_add_u32 s40, s40, 8192\n\t" \
   "s_cmp_eq_u32 s40, 81920\n\t" "s_cselect_b32 s40, 0, s40\n\t" "ds_read_b64_tr_b16 a[4:5], v24 offset:0\n\t" \
   "ds_read_b64_tr_b16 a[6:7], v24 offset:4096\n\t" "ds_read_b64_tr_b16 a[68:69], v25 offset:0\n\t" "ds_read_b64_tr_b16 a[70:71], v25 offset:4096\n\t" \
   "v_add_u32 v24, s40, v18\n\t" "v_add_u32 v25, s40, v19\n\t" "s_add_u32 s40, s40, 8192\n\t" "s_cmp_eq_u32 s40, 81920\n\t" \
   "s_cselect_b32 s40, 0, s40\n\t" "ds_read_b64_tr_b16 a[12:13], v24 offset:0\n\t" "ds_read_b64_tr_b16 a[14:15], v24 offset:4096\n\t" \
   "ds_read_b64_tr_b16 a[76:77], v25 offset:0\n\t" "ds_read_b64_tr_b16 a[78:79], v25 offset:4096\n\t" "v_add_u32 v24, s40, v18\n\t" \
   "v_add_u32 v25, s40, v19\n\t" "s_add_u32 s40, s40, 8192\n\t" "s_cmp_eq_u32 s40, 81920\n\t" "s_cselect_b32 s40, 0, s40\n\t" \
   "ds_read_b64_tr_b16 a[20:21], v24 offset:0\n\t" "ds_read_b64_tr_b16 a[22:23], v24 offset:4096\n\t" "ds_read_b64_tr_b16 a[84:85], v25 offset:0\n\t" \
   "ds_read_b64_tr_b16 a[86:87], v25 offset:4096\n\t" "v_add_u32 v24, s40, v18\n\t" "v_add_u32 v25, s40, v19\n\t" "s_add_u32 s40, s40, 8192\n\t" \
   "s_cmp_eq_u32 s40, 81920\n\t" "s_cselect_b32 s40, 0, s40\n\t" "ds_read_b64_tr_b16 a[28:29], v24 offset:0\n\t" \
   "ds_read_b64_tr_b16 a[30:31], v24 offset:4096\n\t" "ds_read_b64_tr_b16 a[92:93], v25 offset:0\n\t" \
   "ds_read_b64_tr_b16 a[94:95], v25 offset:4096\n\t" "v_add_u32 v24, s40, v18\n\t" "v_add_u32 v25, s40, v19\n\t" "s_add_u32 s40, s40, 8192\n\t" \
   "s_cmp_eq_u32 s40, 81920\n\t" "s_cselect_b32 s40, 0, s40\n\t" "ds_read_b64_tr_b16 a[36:37], v24 offset:0\n\t" \
   "ds_read_b64_tr_b16 a[38:39], v24 offset:4096\n\t" "ds_read_b64_tr_b16 a[100:101], v25 offset:0\n\t" \
   "ds_read_b64_tr_b16 a[102:103], v25 offset:4096\n\t" "v_add_u32 v24, s40, v18\n\t" "v_add_u32 v25, s40, v19\n\t" "s_add_u32 s40, s40, 8192\n\t" \
   "s_cmp_eq_u32 s40, 81920\n\t" "s_cselect_b32 s40, 0, s40\n\t" "ds_read_b64_tr_b16 a[44:45], v24 offset:0\n\t" \
   "ds_read_b64_tr_b16 a[46:47], v24 offset:4096\n\t" "ds_read_b64_tr_b16 a[108:109], v25 offset:0\n\t" \
   "ds_read_b64_tr_b16 a[110:111], v25 offset:4096\n\t" "v_add_u32 v24, s40, v18\n\t" "v_add_u32 v25, s40, v19\n\t" "s_add_u32 s40, s40, 8192\n\t" \
   "s_cmp_eq_u32 s40, 81920\n\t" "s_cselect_b32 s40, 0, s40\n\t" "ds_read_b64_tr_b16 a[52:53], v24 offset:0\n\t" \
   "ds_read_b64_tr_b16 a[54:55], v24 offset:4096\n\t" "ds_read_b64_tr_b16 a[116:117], v25 offset:0\n\t" \
   "ds_read_b64_tr_b16 a[118:119], v25 offset:4096\n\t" "v_add_u32 v24, s40, v18\n\t" "v_add_u32 v25, s40, v19\n\t" "s_add_u32 s40, s40, 8192\n\t" \
   "s_cmp_eq_u32 s40, 81920\n\t" "s_cselect_b32 s40, 0, s40\n\t" "ds_read_b64_tr_b16 a[60:61], v24 offset:0\n\t" \
   "ds_read_b64_tr_b16 a[62:63], v24 offset:4096\n\t" "ds_read_b64_tr_b16 a[124:125], v25 offset:0\n\t" \
   "ds_read_b64_tr_b16 a[126:127], v25 offset:4096\n\t" "v_add_u32 v24, s40, v18\n\t" "v_add_u32 v25, s40, v19\n\t" "s_add_u32 s40, s40, 8192\n\t" \
   "s_cmp_eq_u32 s40, 81920\n\t" "s_cselect_b32 s40, 0, s40\n\t" "ds_read_b64_tr_b16 v[196:197], v24 offset:0\n\t" \
   "ds_read_b64_tr_b16 v[198:199], v24 offset:4096\n\t" "ds_read_b64_tr_b16 v[212:213], v25 offset:0\n\t" \
   "ds_read_b64_tr_b16 v[214:215], v25 offset:4096\n\t" "v_add_u32 v24, s40, v18\n\t" "v_add_u32 v25, s40, v19\n\t" "s_add_u32 s40, s40, 8192\n\t" \
   "s_cmp_eq_u32 s40, 81920\n\t" "s_cselect_b32 s40, 0, s40\n\t" "ds_read_b64_tr_b16 v[204:205], v24 offset:0\n\t" \
   "ds_read_b64_tr_b16 v[206:207], v24 offset:4096\n\t" "ds_read_b64_tr_b16 v[220:221], v25 offset:0\n\t" \
   "ds_read_b64_tr_b16 v[222:223], v25 offset:4096\n\t" "s_waitcnt vmcnt(15)\n\t" "s_waitcnt lgkmcnt(0)\n\t" "s_barrier\n\t" \
   "ds_write_b128 v20, a[132:135] offset:0\n\t" "s_mov_b32 s41, 4456448\n\t" "buffer_load_dwordx4 v[228:231], v16, %[rb], s41 offen\n\t" \
   "s_waitcnt vmcnt(15)\n\t" "ds_write_b128 v20, a[196:199] offset:4096\n\t" "s_mov_b32 s41, 4587520\n\t" \
   "buffer_load_dwordx4 v[244:247], v16, %[rb], s41 offen\n\t" "s_waitcnt vmcnt(15)\n\t" "ds_write_b128 v20, a[140:143] offset:8192\n\t" \
   "s_mov_b32 s41, 4980736\n\t" "buffer_load_dwordx4 v[236:239], v16, %[rb], s41 offen\n\t" "s_waitcnt vmcnt(15)\n\t" \
   "ds_write_b128 v20, a[204:207] offset:12288\n\t" "s_mov_b32 s41, 5111808\n\t" "buffer_load_dwordx4 v[252:255], v16, %[rb], s41 offen\n\t" \
   "s_waitcnt vmcnt(15)\n\t" "ds_write_b128 v20, a[148:151] offset:16384\n\t" "s_waitcnt vmcnt(14)\n\t" \
   "ds_write_b128 v20, a[212:215] offset:20480\n\t" "s_waitcnt vmcnt(13)\n\t" "ds_write_b128 v20, a[156:159] offset:24576\n\t" \
   "s_waitcnt vmcnt(12)\n\t" "ds_write_b128 v20, a[220:223] offset:28672\n\t" "s_waitcnt vmcnt(11)\n\t" \
   "ds_write_b128 v20, a[164:167] offset:32768\n\t" "s_waitcnt vmcnt(10)\n\t" "ds_write_b128 v20, a[228:231] offset:36864\n\t" \
   "buffer_load_dwordx4 v[28:31], v112, %[rw], 0 offen sc1\n\t" "s_waitcnt vmcnt(10)\n\t" "ds_write_b128 v20, a[172:175] offset:40960\n\t" \
   "s_waitcnt vmcnt(9)\n\t" "ds_write_b128 v20, a[236:239] offset:45056\n\t" "s_waitcnt vmcnt(8)\n\t" "ds_write_b128 v20, a[180:183] offset:49152\n\t" \
   "s_waitcnt vmcnt(7)\n\t" "ds_write_b128 v20, a[244:247] offset:53248\n\t" "s_waitcnt vmcnt(6)\n\t" "ds_write_b128 v20, a[188:191] offset:57344\n\t" \
   "s_waitcnt vmcnt(5)\n\t" "ds_write_b128 v20, a[252:255] offset:61440\n\t" "s_waitcnt vmcnt(4)\n\t" "ds_write_b128 v23, v[228:231] offset:0\n\t" \
   "s_waitcnt vmcnt(3)\n\t" "ds_write_b128 v23, v[244:247] offset:4096\n\t" "s_waitcnt vmcnt(2)\n\t" "ds_write_b128 v23, v[236:239] offset:8192\n\t" \
   "s_waitcnt vmcnt(1)\n\t" "ds_write_b128 v23, v[252:255] offset:12288\n\t" "s_waitcnt lgkmcnt(0)\n\t" "s_barrier\n\t" LDSTAMP(5) \
   "s_lshl_b32 s40, %[p0], 13\n\t" "v_add_u32 v24, s40, v18\n\t" "v_add_u32 v25, s40, v19\n\t" "s_add_u32 s40, s40, 8192\n\t" \
   "s_cmp_eq_u32 s40, 81920\n\t" "s_cselect_b32 s40, 0, s40\n\t" "ds_read_b64_tr_b16 a[132:133], v24 offset:0\n\t" \
   "ds_read_b64_tr_b16 a[134:135], v24 offset:4096\n\t" "ds_read_b64_tr_b16 a[196:197], v25 offset:0\n\t" \
   "ds_read_b64_tr_b16 a[198:199], v25 offset:4096\n\t" "v_add_u32 v24, s40, v18\n\t" "v_add_u32 v25, s40, v19\n\t" "s_add_u32 s40, s40, 8192\n\t" \
   "s_cmp_eq_u32 s40, 81920\n\t" "s_cselect_b32 s40, 0, s40\n\t" "ds_read_b64_tr_b16 a[140:141], v24 offset:0\n\t" \
   "ds_read_b64_tr_b16 a[142:143], v24 offset:4096\n\t" "ds_read_b64_tr_b16 a[204:205], v25 offset:0\n\t" \
   "ds_read_b64_tr_b16 a[206:207], v25 offset:4096\n\t" "v_add_u32 v24, s40, v18\n\t" "v_add_u32 v25, s40, v19\n\t" "s_add_u32 s40, s40, 8192\n\t" \
   "s_cmp_eq_u32 s40, 81920\n\t" "s_cselect_b32 s40, 0, s40\n\t" "ds_read_b64_tr_b16 a[148:149], v24 offset:0\n\t" \
   "ds_read_b64_tr_b16 a[150:151], v24 offset:4096\n\t" "ds_read_b64_tr_b16 a[212:213], v25 offset:0\n\t" \
   "ds_read_b64_tr_b16 a[214:215], v25 offset:4096\n\t" "v_add_u32 v24, s40, v18\n\t" "v_add_u32 v25, s40, v19\n\t" "s_add_u32 s40, s40, 8192\n\t" \
   "s_cmp_eq_u32 s40, 81920\n\t" "s_cselect_b32 s40, 0, s40\n\t" "ds_read_b64_tr_b16 a[156:157], v24 offset:0\n\t" \
   "ds_read_b64_tr_b16 a[158:159], v24 offset:4096\n\t" "ds_read_b64_tr_b16 a[220:221], v25 offset:0\n\t" \
   "ds_read_b64_tr_b16 a[222:223], v25 offset:4096\n\t" "v_add_u32 v24, s40, v18\n\t" "v_add_u32 v25, s40, v19\n\t" "s_add_u32 s40, s40, 8192\n\t" \
   "s_cmp_eq_u32 s40, 81920\n\t" "s_cselect_b32 s40, 0, s40\n\t" "ds_read_b64_tr_b16 a[164:165], v24 offset:0\n\t" \
   "ds_read_b64_tr_b16 a[166:167], v24 offset:4096\n\t" "ds_read_b64_tr_b16 a[228:229], v25 offset:0\n\t" \
   "ds_read_b64_tr_b16 a[230:231], v25 offset:4096\n\t" "v_add_u32 v24, s40, v18\n\t" "v_add_u32 v25, s40, v19\n\t" "s_add_u32 s40, s40, 8192\n\t" \
   "s_cmp_eq_u32 s40, 81920\n\t" "s_cselect_b32 s40, 0, s40\n\t" "ds_read_b64_tr_b16 a[172:173], v24 offset:0\n\t" \
   "ds_read_b64_tr_b16 a[174:175], v24 offset:4096\n\t" "ds_read_b64_tr_b16 a[236:237], v25 offset:0\n\t" \
   "ds_read_b64_tr_b16 a[238:239], v25 offset:4096\n\t" "v_add_u32 v24, s40, v18\n\t" "v_add_u32 v25, s40, v19\n\t" "s_add_u32 s40, s40, 8192\n\t" \
   "s_cmp_eq_u32 s40, 81920\n\t" "s_cselect_b32 s40, 0, s40\n\t" "ds_read_b64_tr_b16 a[180:181], v24 offset:0\n\t" \
   "ds_read_b64_tr_b16 a[182:183], v24 offset:4096\n\t" "ds_read_b64_tr_b16 a[244:245], v25 offset:0\n\t" \
   "ds_read_b64_tr_b16 a[246:247], v25 offset:4096\n\t" "v_add_u32 v24, s40, v18\n\t" "v_add_u32 v25, s40, v19\n\t" "s_add_u32 s40, s40, 8192\n\t" \
   "s_cmp_eq_u32 s40, 81920\n\t" "s_cselect_b32 s40, 0, s40\n\t" "ds_read_b64_tr_b16 a[188:189], v24 offset:0\n\t" \
   "ds_read_b64_tr_b16 a[190:191], v24 offset:4096\n\t" "ds_read_b64_tr_b16 a[252:253], v25 offset:0\n\t" \
   "ds_read_b64_tr_b16 a[254:255], v25 offset:4096\n\t" "v_add_u32 v24, s40, v18\n\t" "v_add_u32 v25, s40, v19\n\t" "s_add_u32 s40, s40, 8192\n\t" \
   "s_cmp_eq_u32 s40, 81920\n\t" "s_cselect_b32 s40, 0, s40\n\t" "ds_read_b64_tr_b16 v[228:229], v24 offset:0\n\t" \
   "ds_read_b64_tr_b16 v[230:231], v24 offset:4096\n\t" "ds_read_b64_tr_b16 v[244:245], v25 offset:0\n\t" \
   "ds_read_b64_tr_b16 v[246:247], v25 offset:4096\n\t" "v_add_u32 v24, s40, v18\n\t" "v_add_u32 v25, s40, v19\n\t" "s_add_u32 s40, s40, 8192\n\t" \
   "s_cmp_eq_u32 s40, 81920\n\t" "s_cselect_b32 s40, 0, s40\n\t" "ds_read_b64_tr_b16 v[236:237], v24 offset:0\n\t" \
   "ds_read_b64_tr_b16 v[238:239], v24 offset:4096\n\t" "ds_read_b64_tr_b16 v[252:253], v25 offset:0\n\t" \
   "ds_read_b64_tr_b16 v[254:255], v25 offset:4096\n\t" "s_waitcnt vmcnt(0) lgkmcnt(0)\n\t" "s_barrier"
#define LDP_CLOB "v24", "v25", "v26", "v32", "v33", "v34", "v35", "v36", "v37", "v38", "v39", "v40", "v41", "v42", "v43", "v44", "v45", "v46", "v47", "v48", "v49", "v50", "v51", "v52", "v53", "v54", "v55", "v56", "v57", "v58", "v59", "v60", "v61", "v62", "v63", "v64", "v65", "v66", "v67", "v68", "v69", "v70", "v71", "v72", "v73", "v74", "v75", "v76", "v77", "v78", "v79", "v80", "v81", "v82", "v83", "v84", "v85", "v86", "v87", "v88", "v89", "v90", "v91", "v92", "v93", "v94", "v95", "v96", "v97", "v98", "v99", "v100", "v101", "v102", "v103", "v104", "v105", "v106", "v107", "v108", "v109", "v110", "v111", "v120", "v121", "v122", "v123", "s40", "s41"
#define LDK_ASM \
   "buffer_load_dword v26, v113, %[rw], 0 offen sc1\n\t" "s_mov_b32 s41, 0\n\t" "buffer_load_dwordx4 v[32:35], v116, %[rx], s41 offen\n\t" \
   "s_mov_b32 s41, 10240\n\t" "buffer_load_dwordx4 v[36:39], v116, %[rx], s41 offen\n\t" "s_mov_b32 s41, 128\n\t" \
   "buffer_load_dwordx4 v[40:43], v116, %[rx], s41 offen\n\t" "s_mov_b32 s41, 10368\n\t" "buffer_load_dwordx4 v[44:47], v116, %[rx], s41 offen\n\t" \
   "s_mov_b32 s41, 256\n\t" "buffer_load_dwordx4 v[48:51], v116, %[rx], s41 offen\n\t" "s_mov_b32 s41, 10496\n\t" \
   "buffer_load_dwordx4 v[52:55], v116, %[rx], s41 offen\n\t" "s_mov_b32 s41, 384\n\t" "buffer_load_dwordx4 v[56:59], v116, %[rx], s41 offen\n\t" \
   "s_mov_b32 s41, 10624\n\t" "buffer_load_dwordx4 v[60:63], v116, %[rx], s41 offen\n\t" "s_mov_b32 s41, 512\n\t" \
   "buffer_load_dwordx4 v[64:67], v116, %[rx], s41 offen\n\t" "s_mov_b32 s41, 10752\n\t" "buffer_load_dwordx4 v[68:71], v116, %[rx], s41 offen\n\t" \
   "s_mov_b32 s41, 640\n\t" "buffer_load_dwordx4 v[72:75], v116, %[rx], s41 offen\n\t" "s_mov_b32 s41, 10880\n\t" \
   "buffer_load_dwordx4 v[76:79], v116, %[rx], s41 offen\n\t" "s_mov_b32 s41, 768\n\t" "buffer_load_dwordx4 v[80:83], v116, %[rx], s41 offen\n\t" \
   "s_mov_b32 s41, 11008\n\t" "buffer_load_dwordx4 v[84:87], v116, %[rx], s41 offen\n\t" "s_mov_b32 s41, 896\n\t" \
   "buffer_load_dwordx4 v[88:91], v116, %[rx], s41 offen\n\t" "s_mov_b32 s41, 11136\n\t" "buffer_load_dwordx4 v[92:95], v116, %[rx], s41 offen\n\t" \
   "s_mov_b32 s40, 0x3f803f80\n\t" "v_mov_b32 v120, 0\n\t" "v_mov_b32 v121, 0\n\t" "v_mov_b32 v122, 0\n\t" "v_mov_b32 v123, 0\n\t" \
   "s_waitcnt vmcnt(16)\n\t" "v_add_u32 v26, 1, v26\n\t" "buffer_store_dword v26, v114, %[rw], 0 offen sc1\n\t" "ds_write_b32 v115, v26\n\t" LDSTAMP(0) \
   "s_waitcnt vmcnt(15)\n\t" LDSTAMP(1) "ds_write_b64 v117, v[32:33] offset:0\n\t" "ds_write_b64 v117, v[34:35] offset:256\n\t" \
   "s_mov_b32 s41, 1024\n\t" "buffer_load_dwordx4 v[96:99], v116, %[rx], s41 offen\n\t" "v_dot2c_f32_bf16 v120, s40, v32\n\t" \
   "v_dot2c_f32_bf16 v121, v32, v32\n\t" "v_dot2c_f32_bf16 v120, s40, v33\n\t" "v_dot2c_f32_bf16 v121, v33, v33\n\t" \
   "v_dot2c_f32_bf16 v120, s40, v34\n\t" "v_dot2c_f32_bf16 v121, v34, v34\n\t" "v_dot2c_f32_bf16 v120, s40, v35\n\t" \
   "v_dot2c_f32_bf16 v121, v35, v35\n\t" "s_waitcnt vmcnt(15)\n\t" "ds_write_b64 v117, v[36:37] offset:128\n\t" \
   "ds_write_b64 v117, v[38:39] offset:384\n\t" "s_mov_b32 s41, 11264\n\t" "buffer_load_dwordx4 v[100:103], v116, %[rx], s41 offen\n\t" \
   "v_dot2c_f32_bf16 v122, s40, v36\n\t" "v_dot2c_f32_bf16 v123, v36, v36\n\t" "v_dot2c_f32_bf16 v122, s40, v37\n\t" \
   "v_dot2c_f32_bf16 v123, v37, v37\n\t" "v_dot2c_f32_bf16 v122, s40, v38\n\t" "v_dot2c_f32_bf16 v123, v38, v38\n\t" \
   "v_dot2c_f32_bf16 v122, s40, v39\n\t" "v_dot2c_f32_bf16 v123, v39, v39\n\t" "s_waitcnt vmcnt(15)\n\t" "ds_write_b64 v117, v[40:41] offset:2048\n\t" \
   "ds_write_b64 v117, v[42:43] offset:2304\n\t" "s_mov_b32 s41, 1152\n\t" "buffer_load_dwordx4 v[104:107], v116, %[rx], s41 offen\n\t" \
   "v_dot2c_f32_bf16 v120, s40, v40\n\t" "v_dot2c_f32_bf16 v121, v40, v40\n\t" "v_dot2c_f32_bf16 v120, s40, v41\n\t" \
   "v_dot2c_f32_bf16 v121, v41, v41\n\t" "v_dot2c_f32_bf16 v120, s40, v42\n\t" "v_dot2c_f32_bf16 v121, v42, v42\n\t" \
   "v_dot2c_f32_bf16 v120, s40, v43\n\t" "v_dot2c_f32_bf16 v121, v43, v43\n\t" "s_waitcnt vmcnt(15)\n\t" "ds_write_b64 v117, v[44:45] offset:2176\n\t" \
   "ds_write_b64 v117, v[46:47] offset:2432\n\t" "s_mov_b32 s41, 11392\n\t" "buffer_load_dwordx4 v[108:111], v116, %[rx], s41 offen\n\t" \
   "v_dot2c_f32_bf16 v122, s40, v44\n\t" "v_dot2c_f32_bf16 v123, v44, v44\n\t" "v_dot2c_f32_bf16 v122, s40, v45\n\t" \
   "v_dot2c_f32_bf16 v123, v45, v45\n\t" "v_dot2c_f32_bf16 v122, s40, v46\n\t" "v_dot2c_f32_bf16 v123, v46, v46\n\t" \
   "v_dot2c_f32_bf16 v122, s40, v47\n\t" "v_dot2c_f32_bf16 v123, v47, v47\n\t" "s_waitcnt vmcnt(15)\n\t" "ds_write_b64 v117, v[48:49] offset:4096\n\t" \
   "ds_write_b64 v117, v[50:51] offset:4352\n\t" "s_mov_b32 s41, 0\n\t" "buffer_load_dwordx4 a[0:3], v16, %[ra], s41 offen\n\t" \
   "v_dot2c_f32_bf16 v120, s40, v48\n\t" "v_dot2c_f32_bf16 v121, v48, v48\n\t" "v_dot2c_f32_bf16 v120, s40, v49\n\t" \
   "v_dot2c_f32_bf16 v121, v49, v49\n\t" "v_dot2c_f32_bf16 v120, s40, v50\n\t" "v_dot2c_f32_bf16 v121, v50, v50\n\t" \
   "v_dot2c_f32_bf16 v120, s40, v51\n\t" "v_dot2c_f32_bf16 v121, v51, v51\n\t" "s_waitcnt vmcnt(15)\n\t" "ds_write_b64 v117, v[52:53] offset:4224\n\t" \
   "ds_write_b64 v117, v[54:55] offset:4480\n\t" "s_mov_b32 s41, 131072\n\t" "buffer_load_dwordx4 a[4:7], v16, %[ra], s41 offen\n\t" \
   "v_dot2c_f32_bf16 v122, s40, v52\n\t" "v_dot2c_f32_bf16 v123, v52, v52\n\t" "v_dot2c_f32_bf16 v122, s40, v53\n\t" \
   "v_dot2c_f32_bf16 v123, v53, v53\n\t" "v_dot2c_f32_bf16 v122, s40, v54\n\t" "v_dot2c_f32_bf16 v123, v54, v54\n\t" \
   "v_dot2c_f32_bf16 v122, s40, v55\n\t" "v_dot2c_f32_bf16 v123, v55, v55\n\t" "s_waitcnt vmcnt(15)\n\t" "ds_write_b64 v117, v[56:57] offset:6144\n\t" \
   "ds_write_b64 v117, v[58:59] offset:6400\n\t" "s_mov_b32 s41, 262144\n\t" "buffer_load_dwordx4 a[16:19], v16, %[ra], s41 offen\n\t" \
   "v_dot2c_f32_bf16 v120, s40, v56\n\t" "v_dot2c_f32_bf16 v121, v56, v56\n\t" "v_dot2c_f32_bf16 v120, s40, v57\n\t" \
   "v_dot2c_f32_bf16 v121, v57, v57\n\t" "v_dot2c_f32_bf16 v120, s40, v58\n\t" "v_dot2c_f32_bf16 v121, v58, v58\n\t" \
   "v_dot2c_f32_bf16 v120, s40, v59\n\t" "v_dot2c_f32_bf16 v121, v59, v59\n\t" "s_waitcnt vmcnt(15)\n\t" "ds_write_b64 v117, v[60:61] offset:6272\n\t" \
   "ds_write_b64 v117, v[62:63] offset:6528\n\t" "s_mov_b32 s41, 393216\n\t" "buffer_load_dwordx4 a[20:23], v16, %[ra], s41 offen\n\t" \
   "v_dot2c_f32_bf16 v122, s40, v60\n\t" "v_dot2c_f32_bf16 v123, v60, v60\n\t" "v_dot2c_f32_bf16 v122, s40, v61\n\t" \
   "v_dot2c_f32_bf16 v123, v61, v61\n\t" "v_dot2c_f32_bf16 v122, s40, v62\n\t" "v_dot2c_f32_bf16 v123, v62, v62\n\t" \
   "v_dot2c_f32_bf16 v122, s40, v63\n\t" "v_dot2c_f32_bf16 v123, v63, v63\n\t" "s_waitcnt vmcnt(15)\n\t" "ds_write_b64 v117, v[64:65] offset:8192\n\t" \
   "ds_write_b64 v117, v[66:67] offset:8448\n\t" "s_mov_b32 s41, 524288\n\t" "buffer_load_dwordx4 a[32:35], v16, %[ra], s41 offen\n\t" \
   "v_dot2c_f32_bf16 v120, s40, v64\n\t" "v_dot2c_f32_bf16 v121, v64, v64\n\t" "v_dot2c_f32_bf16 v120, s40, v65\n\t" \
   "v_dot2c_f32_bf16 v121, v65, v65\n\t" "v_dot2c_f32_bf16 v120, s40, v66\n\t" "v_dot2c_f32_bf16 v121, v66, v66\n\t" \
   "v_dot2c_f32_bf16 v120, s40, v67\n\t" "v_dot2c_f32_bf16 v121, v67, v67\n\t" "s_waitcnt vmcnt(15)\n\t" "ds_write_b64 v117, v[68:69] offset:8320\n\t" \
   "ds_write_b64 v117, v[70:71] offset:8576\n\t" "s_mov_b32 s41, 655360\n\t" "buffer_load_dwordx4 a[36:39], v16, %[ra], s41 offen\n\t" \
   "v_dot2c_f32_bf16 v122, s40, v68\n\t" "v_dot2c_f32_bf16 v123, v68, v68\n\t" "v_dot2c_f32_bf16 v122, s40, v69\n\t" \
   "v_dot2c_f32_bf16 v123, v69, v69\n\t" "v_dot2c_f32_bf16 v122, s40, v70\n\t" "v_dot2c_f32_bf16 v123, v70, v70\n\t" \
   "v_dot2c_f32_bf16 v122, s40, v71\n\t" "v_dot2c_f32_bf16 v123, v71, v71\n\t" "s_waitcnt vmcnt(15)\n\t" "ds_write_b64 v117, v[72:73] offset:10240\n\t" \
   "ds_write_b64 v117, v[74:75] offset:10496\n\t" "s_mov_b32 s41, 786432\n\t" "buffer_load_dwordx4 a[48:51], v16, %[ra], s41 offen\n\t" \
   "v_dot2c_f32_bf16 v120, s40, v72\n\t" "v_dot2c_f32_bf16 v121, v72, v72\n\t" "v_dot2c_f32_bf16 v120, s40, v73\n\t" \
   "v_dot2c_f32_bf16 v121, v73, v73\n\t" "v_dot2c_f32_bf16 v120, s40, v74\n\t" "v_dot2c_f32_bf16 v121, v74, v74\n\t" \
   "v_dot2c_f32_bf16 v120, s40, v75\n\t" "v_dot2c_f32_bf16 v121, v75, v75\n\t" "s_waitcnt vmcnt(15)\n\t" "ds_write_b64 v117, v[76:77] offset:10368\n\t" \
   "ds_write_b64 v117, v[78:79] offset:10624\n\t" "s_mov_b32 s41, 917504\n\t" "buffer_load_dwordx4 a[52:55], v16, %[ra], s41 offen\n\t" \
   "v_dot2c_f32_bf16 v122, s40, v76\n\t" "v_dot2c_f32_bf16 v123, v76, v76\n\t" "v_dot2c_f32_bf16 v122, s40, v77\n\t" \
   "v_dot2c_f32_bf16 v123, v77, v77\n\t" "v_dot2c_f32_bf16 v122, s40, v78\n\t" "v_dot2c_f32_bf16 v123, v78, v78\n\t" \
   "v_dot2c_f32_bf16 v122, s40, v79\n\t" "v_dot2c_f32_bf16 v123, v79, v79\n\t" "s_waitcnt vmcnt(15)\n\t" "ds_write_b64 v117, v[80:81] offset:12288\n\t" \
   "ds_write_b64 v117, v[82:83] offset:12544\n\t" "s_mov_b32 s41, 1048576\n\t" "buffer_load_dwordx4 a[64:67], v16, %[ra], s41 offen\n\t" \
   "v_dot2c_f32_bf16 v120, s40, v80\n\t" "v_dot2c_f32_bf16 v121, v80, v80\n\t" "v_dot2c_f32_bf16 v120, s40, v81\n\t" \
   "v_dot2c_f32_bf16 v121, v81, v81\n\t" "v_dot2c_f32_bf16 v120, s40, v82\n\t" "v_dot2c_f32_bf16 v121, v82, v82\n\t" \
   "v_dot2c_f32_bf16 v120, s40, v83\n\t" "v_dot2c_f32_bf16 v121, v83, v83\n\t" "s_waitcnt vmcnt(15)\n\t" "ds_write_b64 v117, v[84:85] offset:12416\n\t" \
   "ds_write_b64 v117, v[86:87] offset:12672\n\t" "s_mov_b32 s41, 1179648\n\t" "buffer_load_dwordx4 a[68:71], v16, %[ra], s41 offen\n\t" \
   "v_dot2c_f32_bf16 v122, s40, v84\n\t" "v_dot2c_f32_bf16 v123, v84, v84\n\t" "v_dot2c_f32_bf16 v122, s40, v85\n\t" \
   "v_dot2c_f32_bf16 v123, v85, v85\n\t" "v_dot2c_f32_bf16 v122, s40, v86\n\t" "v_dot2c_f32_bf16 v123, v86, v86\n\t" \
   "v_dot2c_f32_bf16 v122, s40, v87\n\t" "v_dot2c_f32_bf16 v123, v87, v87\n\t" "s_waitcnt vmcnt(15)\n\t" "ds_write_b64 v117, v[88:89] offset:14336\n\t" \
   "ds_write_b64 v117, v[90:91] offset:14592\n\t" "s_mov_b32 s41, 0\n\t" "buffer_load_dwordx4 a[8:11], v16, %[rb], s41 offen\n\t" \
   "v_dot2c_f32_bf16 v120, s40, v88\n\t" "v_dot2c_f32_bf16 v121, v88, v88\n\t" "v_dot2c_f32_bf16 v120, s40, v89\n\t" \
   "v_dot2c_f32_bf16 v121, v89, v89\n\t" "v_dot2c_f32_bf16 v120, s40, v90\n\t" "v_dot2c_f32_bf16 v121, v90, v90\n\t" \
   "v_dot2c_f32_bf16 v120, s40, v91\n\t" "v_dot2c_f32_bf16 v121, v91, v91\n\t" "s_waitcnt vmcnt(15)\n\t" "ds_write_b64 v117, v[92:93] offset:14464\n\t" \
   "ds_write_b64 v117, v[94:95] offset:14720\n\t" "s_mov_b32 s41, 131072\n\t" "buffer_load_dwordx4 a[12:15], v16, %[rb], s41 offen\n\t" \
   "v_dot2c_f32_bf16 v122, s40, v92\n\t" "v_dot2c_f32_bf16 v123, v92, v92\n\t" "v_dot2c_f32_bf16 v122, s40, v93\n\t" \
   "v_dot2c_f32_bf16 v123, v93, v93\n\t" "v_dot2c_f32_bf16 v122, s40, v94\n\t" "v_dot2c_f32_bf16 v123, v94, v94\n\t" \
   "v_dot2c_f32_bf16 v122, s40, v95\n\t" "v_dot2c_f32_bf16 v123, v95, v95\n\t" "s_waitcnt vmcnt(15)\n\t" "ds_write_b64 v117, v[96:97] offset:16384\n\t" \
   "ds_write_b64 v117, v[98:99] offset:16640\n\t" "s_mov_b32 s41, 262144\n\t" "buffer_load_dwordx4 a[24:27], v16, %[rb], s41 offen\n\t" \
   "v_dot2c_f32_bf16 v120, s40, v96\n\t" "v_dot2c_f32_bf16 v121, v96, v96\n\t" "v_dot2c_f32_bf16 v120, s40, v97\n\t" \
   "v_dot2c_f32_bf16 v121, v97, v97\n\t" "v_dot2c_f32_bf16 v120, s40, v98\n\t" "v_dot2c_f32_bf16 v121, v98, v98\n\t" \
   "v_dot2c_f32_bf16 v120, s40, v99\n\t" "v_dot2c_f32_bf16 v121, v99, v99\n\t" "s_waitcnt vmcnt(15)\n\t" \
   "ds_write_b64 v117, v[100:101] offset:16512\n\t" "ds_write_b64 v117, v[102:103] offset:16768\n\t" "s_mov_b32 s41, 393216\n\t" \
   "buffer_load_dwordx4 a[28:31], v16, %[rb], s41 offen\n\t" "v_dot2c_f32_bf16 v122, s40, v100\n\t" "v_dot2c_f32_bf16 v123, v100, v100\n\t" \
   "v_dot2c_f32_bf16 v122, s40, v101\n\t" "v_dot2c_f32_bf16 v123, v101, v101\n\t" "v_dot2c_f32_bf16 v122, s40, v102\n\t" \
   "v_dot2c_f32_bf16 v123, v102, v102\n\t" "v_dot2c_f32_bf16 v122, s40, v103\n\t" "v_dot2c_f32_bf16 v123, v103, v103\n\t" "s_waitcnt vmcnt(15)\n\t" \
   "ds_write_b64 v117, v[104:105] offset:18432\n\t" "ds_write_b64 v117, v[106:107] offset:18688\n\t" "s_mov_b32 s41, 524288\n\t" \
   "buffer_load_dwordx4 a[40:43], v16, %[rb], s41 offen\n\t" "v_dot2c_f32_bf16 v120, s40, v104\n\t" "v_dot2c_f32_bf16 v121, v104, v104\n\t" \
   "v_dot2c_f32_bf16 v120, s40, v105\n\t" "v_dot2c_f32_bf16 v121, v105, v105\n\t" "v_dot2c_f32_bf16 v120, s40, v106\n\t" \
   "v_dot2c_f32_bf16 v121, v106, v106\n\t" "v_dot2c_f32_bf16 v120, s40, v107\n\t" "v_dot2c_f32_bf16 v121, v107, v107\n\t" "s_waitcnt vmcnt(15)\n\t" \
   "ds_write_b64 v117, v[108:109] offset:18560\n\t" "ds_write_b64 v117, v[110:111] offset:18816\n\t" "s_mov_b32 s41, 655360\n\t" \
   "buffer_load_dwordx4 a[44:47], v16, %[rb], s41 offen\n\t" "v_dot2c_f32_bf16 v122, s40, v108\n\t" "v_dot2c_f32_bf16 v123, v108, v108\n\t" \
   "v_dot2c_f32_bf16 v122, s40, v109\n\t" "v_dot2c_f32_bf16 v123, v109, v109\n\t" "v_dot2c_f32_bf16 v122, s40, v110\n\t" \
   "v_dot2c_f32_bf16 v123, v110, v110\n\t" "v_dot2c_f32_bf16 v122, s40, v111\n\t" "v_dot2c_f32_bf16 v123, v111, v111\n\t" "s_nop 3\n\t" \
   "v_add_f32_dpp v120, v120, v120 quad_perm:[1,0,3,2] row_mask:0xf bank_mask:0xf\n\t" \
   "v_add_f32_dpp v121, v121, v121 quad_perm:[1,0,3,2] row_mask:0xf bank_mask:0xf\n\t" \
   "v_add_f32_dpp v122, v122, v122 quad_perm:[1,0,3,2] row_mask:0xf bank_mask:0xf\n\t" \
   "v_add_f32_dpp v123, v123, v123 quad_perm:[1,0,3,2] row_mask:0xf bank_mask:0xf\n\t" \
   "v_add_f32_dpp v120, v120, v120 quad_perm:[2,3,0,1] row_mask:0xf bank_mask:0xf\n\t" \
   "v_add_f32_dpp v121, v121, v121 quad_perm:[2,3,0,1] row_mask:0xf bank_mask:0xf\n\t" \
   "v_add_f32_dpp v122, v122, v122 quad_perm:[2,3,0,1] row_mask:0xf bank_mask:0xf\n\t" \
   "v_add_f32_dpp v123, v123, v123 quad_perm:[2,3,0,1] row_mask:0xf bank_mask:0xf\n\t" \
   "v_add_f32_dpp v120, v120, v120 row_half_mirror row_mask:0xf bank_mask:0xf\n\t" \
   "v_add_f32_dpp v121, v121, v121 row_half_mirror row_mask:0xf bank_mask:0xf\n\t" \
   "v_add_f32_dpp v122, v122, v122 row_half_mirror row_mask:0xf bank_mask:0xf\n\t" \
   "v_add_f32_dpp v123, v123, v123 row_half_mirror row_mask:0xf bank_mask:0xf\n\t" "v_and_b32 v120, v119, v120\n\t" "v_and_b32 v121, v119, v121\n\t" \
   "v_and_b32 v122, v119, v122\n\t" "v_and_b32 v123, v119, v123\n\t" "ds_write_b64 v118, v[120:121]\n\t" "ds_write_b64 v118, v[122:123] offset:384\n\t" \
   "s_waitcnt vmcnt(15)\n\t" "ds_write_b128 v20, a[0:3] offset:0\n\t" "s_mov_b32 s41, 786432\n\t" \
   "buffer_load_dwordx4 a[56:59], v16, %[rb], s41 offen\n\t" "s_waitcnt vmcnt(15)\n\t" "ds_write_b128 v20, a[4:7] offset:4096\n\t" \
   "s_mov_b32 s41, 917504\n\t" "buffer_load_dwordx4 a[60:63], v16, %[rb], s41 offen\n\t" "s_waitcnt vmcnt(15)\n\t" \
   "ds_write_b128 v20, a[16:19] offset:8192\n\t" "s_mov_b32 s41, 1048576\n\t" "buffer_load_dwordx4 a[72:75], v16, %[rb], s41 offen\n\t" \
   "s_waitcnt vmcnt(15)\n\t" "ds_write_b128 v20, a[20:23] offset:12288\n\t" "s_mov_b32 s41, 1179648\n\t" \
   "buffer_load_dwordx4 a[76:79], v16, %[rb], s41 offen\n\t" "s_waitcnt vmcnt(15)\n\t" "ds_write_b128 v20, a[32:35] offset:16384\n\t" \
   "s_mov_b32 s41, 1310720\n\t" "buffer_load_dwordx4 a[80:83], v16, %[ra], s41 offen\n\t" "s_waitcnt vmcnt(15)\n\t" \
   "ds_write_b128 v20, a[36:39] offset:20480\n\t" "s_mov_b32 s41, 1441792\n\t" "buffer_load_dwordx4 a[84:87], v16, %[ra], s41 offen\n\t" \
   "s_waitcnt vmcnt(15)\n\t" "ds_write_b128 v20, a[48:51] offset:24576\n\t" "s_mov_b32 s41, 1572864\n\t" \
   "buffer_load_dwordx4 a[96:99], v16, %[ra], s41 offen\n\t" "s_waitcnt vmcnt(15)\n\t" "ds_write_b128 v20, a[52:55] offset:28672\n\t" \
   "s_mov_b32 s41, 1703936\n\t" "buffer_load_dwordx4 a[100:103], v16, %[ra], s41 offen\n\t" "s_waitcnt vmcnt(15)\n\t" \
   "ds_write_b128 v20, a[64:67] offset:32768\n\t" "s_mov_b32 s41, 1835008\n\t" "buffer_load_dwordx4 a[112:115], v16, %[ra], s41 offen\n\t" \
   "s_waitcnt vmcnt(15)\n\t" "ds_write_b128 v20, a[68:71] offset:36864\n\t" "s_mov_b32 s41, 1966080\n\t" \
   "buffer_load_dwordx4 a[116:119], v16, %[ra], s41 offen\n\t" "s_waitcnt lgkmcnt(0)\n\t" "s_barrier\n\t" LDSTAMP(2) \
   "ds_read_b64_tr_b16 a[0:1], v18 offset:0\n\t" "ds_read_b64_tr_b16 a[2:3], v18 offset:4096\n\t" "ds_read_b64_tr_b16 a[4:5], v19 offset:0\n\t" \
   "ds_read_b64_tr_b16 a[6:7], v19 offset:4096\n\t" "ds_read_b64_tr_b16 a[16:17], v18 offset:8192\n\t" \
   "ds_read_b64_tr_b16 a[18:19], v18 offset:12288\n\t" "ds_read_b64_tr_b16 a[20:21], v19 offset:8192\n\t" \
   "ds_read_b64_tr_b16 a[22:23], v19 offset:12288\n\t" "ds_read_b64_tr_b16 a[32:33], v18 offset:16384\n\t" \
   "ds_read_b64_tr_b16 a[34:35], v18 offset:20480\n\t" "ds_read_b64_tr_b16 a[36:37], v19 offset:16384\n\t" \
   "ds_read_b64_tr_b16 a[38:39], v19 offset:20480\n\t" "ds_read_b64_tr_b16 a[48:49], v18 offset:24576\n\t" \
   "ds_read_b64_tr_b16 a[50:51], v18 offset:28672\n\t" "ds_read_b64_tr_b16 a[52:53], v19 offset:24576\n\t" \
   "ds_read_b64_tr_b16 a[54:55], v19 offset:28672\n\t" "ds_read_b64_tr_b16 a[64:65], v18 offset:32768\n\t" \
   "ds_read_b64_tr_b16 a[66:67], v18 offset:36864\n\t" "ds_read_b64_tr_b16 a[68:69], v19 offset:32768\n\t" \
   "ds_read_b64_tr_b16 a[70:71], v19 offset:36864\n\t" "s_waitcnt vmcnt(15)\n\t" "s_waitcnt lgkmcnt(0)\n\t" "s_barrier\n\t" \
   "ds_write_b128 v20, a[8:11] offset:0\n\t" "s_mov_b32 s41, 2097152\n\t" "buffer_load_dwordx4 a[128:131], v16, %[ra], s41 offen\n\t" \
   "s_waitcnt vmcnt(15)\n\t" "ds_write_b128 v20, a[12:15] offset:4096\n\t" "s_mov_b32 s41, 2228224\n\t" \
   "buffer_load_dwordx4 a[132:135], v16, %[ra], s41 offen\n\t" "s_waitcnt vmcnt(15)\n\t" "ds_write_b128 v20, a[24:27] offset:8192\n\t" \
   "s_mov_b32 s41, 2359296\n\t" "buffer_load_dwordx4 a[144:147], v16, %[ra], s41 offen\n\t" "s_waitcnt vmcnt(15)\n\t" \
   "ds_write_b128 v20, a[28:31] offset:12288\n\t" "s_mov_b32 s41, 2490368\n\t" "buffer_load_dwordx4 a[148:151], v16, %[ra], s41 offen\n\t" \
   "s_waitcnt vmcnt(15)\n\t" "ds_write_b128 v20, a[40:43] offset:16384\n\t" "s_mov_b32 s41, 1310720\n\t" \
   "buffer_load_dwordx4 a[88:91], v16, %[rb], s41 offen\n\t" "s_waitcnt vmcnt(15)\n\t" "ds_write_b128 v20, a[44:47] offset:20480\n\t" \
   "s_mov_b32 s41, 1441792\n\t" "buffer_load_dwordx4 a[92:95], v16, %[rb], s41 offen\n\t" "s_waitcnt vmcnt(15)\n\t" \
   "ds_write_b128 v20, a[56:59] offset:24576\n\t" "s_mov_b32 s41, 1572864\n\t" "buffer_load_dwordx4 a[104:107], v16, %[rb], s41 offen\n\t" \
   "s_waitcnt vmcnt(15)\n\t" "ds_write_b128 v20, a[60:63] offset:28672\n\t" "s_mov_b32 s41, 1703936\n\t" \
   "buffer_load_dwordx4 a[108:111], v16, %[rb], s41 offen\n\t" "s_waitcnt vmcnt(15)\n\t" "ds_write_b128 v20, a[72:75] offset:32768\n\t" \
   "s_mov_b32 s41, 1835008\n\t" "buffer_load_dwordx4 a[120:123], v16, %[rb], s41 offen\n\t" "s_waitcnt vmcnt(15)\n\t" \
   "ds_write_b128 v20, a[76:79] offset:36864\n\t" "s_mov_b32 s41, 1966080\n\t" "buffer_load_dwordx4 a[124:127], v16, %[rb], s41 offen\n\t" \
   "s_waitcnt lgkmcnt(0)\n\t" "s_barrier\n\t" LDSTAMP(3) "ds_read_b64_tr_b16 a[8:9], v18 offset:0\n\t" \
   "ds_read_b64_tr_b16 a[10:11], v18 offset:4096\n\t" "ds_read_b64_tr_b16 a[12:13], v19 offset:0\n\t" \
   "ds_read_b64_tr_b16 a[14:15], v19 offset:4096\n\t" "ds_read_b64_tr_b16 a[24:25], v18 offset:8192\n\t" \
   "ds_read_b64_tr_b16 a[26:27], v18 offset:12288\n\t" "ds_read_b64_tr_b16 a[28:29], v19 offset:8192\n\t" \
   "ds_read_b64_tr_b16 a[30:31], v19 offset:12288\n\t" "ds_read_b64_tr_b16 a[40:41], v18 offset:16384\n\t" \
   "ds_read_b64_tr_b16 a[42:43], v18 offset:20480\n\t" "ds_read_b64_tr_b16 a[44:45], v19 offset:16384\n\t" \
   "ds_read_b64_tr_b16 a[46:47], v19 offset:20480\n\t" "ds_read_b64_tr_b16 a[56:57], v18 offset:24576\n\t" \
   "ds_read_b64_tr_b16 a[58:59], v18 offset:28672\n\t" "ds_read_b64_tr_b16 a[60:61], v19 offset:24576\n\t" \
   "ds_read_b64_tr_b16 a[62:63], v19 offset:28672\n\t" "ds_read_b64_tr_b16 a[72:73], v18 offset:32768\n\t" \
   "ds_read_b64_tr_b16 a[74:75], v18 offset:36864\n\t" "ds_read_b64_tr_b16 a[76:77], v19 offset:32768\n\t" \
   "ds_read_b64_tr_b16 a[78:79], v19 offset:36864\n\t" "s_waitcnt vmcnt(15)\n\t" "s_waitcnt lgkmcnt(0)\n\t" "s_barrier\n\t" \
   "ds_write_b128 v20, a[80:83] offset:0\n\t" "s_mov_b32 s41, 2097152\n\t" "buffer_load_dwordx4 a[136:139], v16, %[rb], s41 offen\n\t" \
   "s_waitcnt vmcnt(15)\n\t" "ds_write_b128 v20, a[84:87] offset:4096\n\t" "s_mov_b32 s41, 2228224\n\t" \
   "buffer_load_dwordx4 a[140:143], v16, %[rb], s41 offen\n\t" "s_waitcnt vmcnt(15)\n\t" "ds_write_b128 v20, a[96:99] offset:8192\n\t" \
   "s_mov_b32 s41, 2359296\n\t" "buffer_load_dwordx4 a[152:155], v16, %[rb], s41 offen\n\t" "s_waitcnt vmcnt(15)\n\t" \
   "ds_write_b128 v20, a[100:103] offset:12288\n\t" "s_mov_b32 s41, 2490368\n\t" "buffer_load_dwordx4 a[156:159], v16, %[rb], s41 offen\n\t" \
   "s_waitcnt vmcnt(15)\n\t" "ds_write_b128 v20, a[112:115] offset:16384\n\t" "s_mov_b32 s41, 2621440\n\t" \
   "buffer_load_dwordx4 a[160:163], v16, %[ra], s41 offen\n\t" "s_waitcnt vmcnt(15)\n\t" "ds_write_b128 v20, a[116:119] offset:20480\n\t" \
   "s_mov_b32 s41, 2752512\n\t" "buffer_load_dwordx4 a[164:167], v16, %[ra], s41 offen\n\t" "s_waitcnt vmcnt(15)\n\t" \
   "ds_write_b128 v20, a[128:131] offset:24576\n\t" "s_mov_b32 s41, 2883584\n\t" "buffer_load_dwordx4 a[176:179], v16, %[ra], s41 offen\n\t" \
   "s_waitcnt vmcnt(15)\n\t" "ds_write_b128 v20, a[132:135] offset:28672\n\t" "s_mov_b32 s41, 3014656\n\t" \
   "buffer_load_dwordx4 a[180:183], v16, %[ra], s41 offen\n\t" "s_waitcnt vmcnt(15)\n\t" "ds_write_b128 v20, a[144:147] offset:32768\n\t" \
   "s_mov_b32 s41, 3145728\n\t" "buffer_load_dwordx4 a[192:195], v16, %[ra], s41 offen\n\t" "s_waitcnt vmcnt(15)\n\t" \
   "ds_write_b128 v20, a[148:151] offset:36864\n\t" "s_mov_b32 s41, 3276800\n\t" "buffer_load_dwordx4 a[196:199], v16, %[ra], s41 offen\n\t" \
   "s_waitcnt lgkmcnt(0)\n\t" "s_barrier\n\t" LDSTAMP(4) "ds_read_b64_tr_b16 a[80:81], v18 offset:0\n\t" \
   "ds_read_b64_tr_b16 a[82:83], v18 offset:4096\n\t" "ds_read_b64_tr_b16 a[84:85], v19 offset:0\n\t" \
   "ds_read_b64_tr_b16 a[86:87], v19 offset:4096\n\t" "ds_read_b64_tr_b16 a[96:97], v18 offset:8192\n\t" \
   "ds_read_b64_tr_b16 a[98:99], v18 offset:12288\n\t" "ds_read_b64_tr_b16 a[100:101], v19 offset:8192\n\t" \
   "ds_read_b64_tr_b16 a[102:103], v19 offset:12288\n\t" "ds_read_b64_tr_b16 a[112:113], v18 offset:16384\n\t" \
   "ds_read_b64_tr_b16 a[114:115], v18 offset:20480\n\t" "ds_read_b64_tr_b16 a[116:117], v19 offset:16384\n\t" \
   "ds_read_b64_tr_b16 a[118:119], v19 offset:20480\n\t" "ds_read_b64_tr_b16 a[128:129], v18 offset:24576\n\t" \
   "ds_read_b64_tr_b16 a[130:131], v18 offset:28672\n\t" "ds_read_b64_tr_b16 a[132:133], v19 offset:24576\n\t" \
   "ds_read_b64_tr_b16 a[134:135], v19 offset:28672\n\t" "ds_read_b64_tr_b16 a[144:145], v18 offset:32768\n\t" \
   "ds_read_b64_tr_b16 a[146:147], v18 offset:36864\n\t" "ds_read_b64_tr_b16 a[148:149], v19 offset:32768\n\t" \
   "ds_read_b64_tr_b16 a[150:151], v19 offset:36864\n\t" "s_waitcnt vmcnt(15)\n\t" "s_waitcnt lgkmcnt(0)\n\t" "s_barrier\n\t" \
   "ds_write_b128 v20, a[88:91] offset:0\n\t" "s_mov_b32 s41, 3407872\n\t" "buffer_load_dwordx4 a[208:211], v16, %[ra], s41 offen\n\t" \
   "s_waitcnt vmcnt(15)\n\t" "ds_write_b128 v20, a[92:95] offset:4096\n\t" "s_mov_b32 s41, 3538944\n\t" \
   "buffer_load_dwordx4 a[212:215], v16, %[ra], s41 offen\n\t" "s_waitcnt vmcnt(15)\n\t" "ds_write_b128 v20, a[104:107] offset:8192\n\t" \
   "s_mov_b32 s41, 3670016\n\t" "buffer_load_dwordx4 a[224:227], v16, %[ra], s41 offen\n\t" "s_waitcnt vmcnt(15)\n\t" \
   "ds_write_b128 v20, a[108:111] offset:12288\n\t" "s_mov_b32 s41, 3801088\n\t" "buffer_load_dwordx4 a[228:231], v16, %[ra], s41 offen\n\t" \
   "s_waitcnt vmcnt(15)\n\t" "ds_write_b128 v20, a[120:123] offset:16384\n\t" "s_mov_b32 s41, 2621440\n\t" \
   "buffer_load_dwordx4 a[168:171], v16, %[rb], s41 offen\n\t" "s_waitcnt vmcnt(15)\n\t" "ds_write_b128 v20, a[124:127] offset:20480\n\t" \
   "s_mov_b32 s41, 2752512\n\t" "buffer_load_dwordx4 a[172:175], v16, %[rb], s41 offen\n\t" "s_waitcnt vmcnt(15)\n\t" \
   "ds_write_b128 v20, a[136:139] offset:24576\n\t" "s_mov_b32 s41, 2883584\n\t" "buffer_load_dwordx4 a[184:187], v16, %[rb], s41 offen\n\t" \
   "s_waitcnt vmcnt(15)\n\t" "ds_write_b128 v20, a[140:143] offset:28672\n\t" "s_mov_b32 s41, 3014656\n\t" \
   "buffer_load_dwordx4 a[188:191], v16, %[rb], s41 offen\n\t" "s_waitcnt vmcnt(15)\n\t" "ds_write_b128 v20, a[152:155] offset:32768\n\t" \
   "s_mov_b32 s41, 3145728\n\t" "buffer_load_dwordx4 a[200:203], v16, %[rb], s41 offen\n\t" "s_waitcnt vmcnt(15)\n\t" \
   "ds_write_b128 v20, a[156:159] offset:36864\n\t" "s_mov_b32 s41, 3276800\n\t" "buffer_load_dwordx4 a[204:207], v16, %[rb], s41 offen\n\t" \
   "s_waitcnt lgkmcnt(0)\n\t" "s_barrier\n\t" LDSTAMP(5) "ds_read_b64_tr_b16 a[88:89], v18 offset:0\n\t" \
   "ds_read_b64_tr_b16 a[90:91], v18 offset:4096\n\t" "ds_read_b64_tr_b16 a[92:93], v19 offset:0\n\t" \
   "ds_read_b64_tr_b16 a[94:95], v19 offset:4096\n\t" "ds_read_b64_tr_b16 a[104:105], v18 offset:8192\n\t" \
   "ds_read_b64_tr_b16 a[106:107], v18 offset:12288\n\t" "ds_read_b64_tr_b16 a[108:109], v19 offset:8192\n\t" \
   "ds_read_b64_tr_b16 a[110:111], v19 offset:12288\n\t" "ds_read_b64_tr_b16 a[120:121], v18 offset:16384\n\t" \
   "ds_read_b64_tr_b16 a[122:123], v18 offset:20480\n\t" "ds_read_b64_tr_b16 a[124:125], v19 offset:16384\n\t" \
   "ds_read_b64_tr_b16 a[126:127], v19 offset:20480\n\t" "ds_read_b64_tr_b16 a[136:137], v18 offset:24576\n\t" \
   "ds_read_b64_tr_b16 a[138:139], v18 offset:28672\n\t" "ds_read_b64_tr_b16 a[140:141], v19 offset:24576\n\t" \
   "ds_read_b64_tr_b16 a[142:143], v19 offset:28672\n\t" "ds_read_b64_tr_b16 a[152:153], v18 offset:32768\n\t" \
   "ds_read_b64_tr_b16 a[154:155], v18 offset:36864\n\t" "ds_read_b64_tr_b16 a[156:157], v19 offset:32768\n\t" \
   "ds_read_b64_tr_b16 a[158:159], v19 offset:36864\n\t" "s_waitcnt vmcnt(15)\n\t" "s_waitcnt lgkmcnt(0)\n\t" "s_barrier\n\t" \
   "ds_write_b128 v20, a[160:163] offset:0\n\t" "s_mov_b32 s41, 3407872\n\t" "buffer_load_dwordx4 a[216:219], v16, %[rb], s41 offen\n\t" \
   "s_waitcnt vmcnt(15)\n\t" "ds_write_b128 v20, a[164:167] offset:4096\n\t" "s_mov_b32 s41, 3538944\n\t" \
   "buffer_load_dwordx4 a[220:223], v16, %[rb], s41 offen\n\t" "s_waitcnt vmcnt(15)\n\t" "ds_write_b128 v20, a[176:179] offset:8192\n\t" \
   "s_mov_b32 s41, 3670016\n\t" "buffer_load_dwordx4 a[232:235], v16, %[rb], s41 offen\n\t" "s_waitcnt vmcnt(15)\n\t" \
   "ds_write_b128 v20, a[180:183] offset:12288\n\t" "s_mov_b32 s41, 3801088\n\t" "buffer_load_dwordx4 a[236:239], v16, %[rb], s41 offen\n\t" \
   "s_waitcnt vmcnt(15)\n\t" "ds_write_b128 v20, a[192:195] offset:16384\n\t" "s_mov_b32 s41, 3932160\n\t" \
   "buffer_load_dwordx4 a[240:243], v16, %[ra], s41 offen\n\t" "s_waitcnt vmcnt(15)\n\t" "ds_write_b128 v20, a[196:199] offset:20480\n\t" \
   "s_mov_b32 s41, 4063232\n\t" "buffer_load_dwordx4 a[244:247], v16, %[ra], s41 offen\n\t" "s_waitcnt vmcnt(15)\n\t" \
   "ds_write_b128 v20, a[208:211] offset:24576\n\t" "s_mov_b32 s41, 4194304\n\t" "buffer_load_dwordx4 v[192:195], v16, %[ra], s41 offen\n\t" \
   "s_waitcnt vmcnt(15)\n\t" "ds_write_b128 v20, a[212:215] offset:28672\n\t" "s_mov_b32 s41, 4325376\n\t" \
   "buffer_load_dwordx4 v[196:199], v16, %[ra], s41 offen\n\t" "s_waitcnt vmcnt(15)\n\t" "ds_write_b128 v20, a[224:227] offset:32768\n\t" \
   "s_mov_b32 s41, 4456448\n\t" "buffer_load_dwordx4 v[208:211], v16, %[ra], s41 offen\n\t" "s_waitcnt vmcnt(15)\n\t" \
   "ds_write_b128 v20, a[228:231] offset:36864\n\t" "s_mov_b32 s41, 4587520\n\t" "buffer_load_dwordx4 v[212:215], v16, %[ra], s41 offen\n\t" \
   "s_waitcnt lgkmcnt(0)\n\t" "s_barrier\n\t" LDSTAMP(6) "ds_read_b64_tr_b16 a[160:161], v18 offset:0\n\t" \
   "ds_read_b64_tr_b16 a[162:163], v18 offset:4096\n\t" "ds_read_b64_tr_b16 a[164:165], v19 offset:0\n\t" \
   "ds_read_b64_tr_b16 a[166:167], v19 offset:4096\n\t" "ds_read_b64_tr_b16 a[176:177], v18 offset:8192\n\t" \
   "ds_read_b64_tr_b16 a[178:179], v18 offset:12288\n\t" "ds_read_b64_tr_b16 a[180:181], v19 offset:8192\n\t" \
   "ds_read_b64_tr_b16 a[182:183], v19 offset:12288\n\t" "ds_read_b64_tr_b16 a[192:193], v18 offset:16384\n\t" \
   "ds_read_b64_tr_b16 a[194:195], v18 offset:20480\n\t" "ds_read_b64_tr_b16 a[196:197], v19 offset:16384\n\t" \
   "ds_read_b64_tr_b16 a[198:199], v19 offset:20480\n\t" "ds_read_b64_tr_b16 a[208:209], v18 offset:24576\n\t" \
   "ds_read_b64_tr_b16 a[210:211], v18 offset:28672\n\t" "ds_read_b64_tr_b16 a[212:213], v19 offset:24576\n\t" \
   "ds_read_b64_tr_b16 a[214:215], v19 offset:28672\n\t" "ds_read_b64_tr_b16 a[224:225], v18 offset:32768\n\t" \
   "ds_read_b64_tr_b16 a[226:227], v18 offset:36864\n\t" "ds_read_b64_tr_b16 a[228:229], v19 offset:32768\n\t" \
   "ds_read_b64_tr_b16 a[230:231], v19 offset:36864\n\t" "s_waitcnt vmcnt(15)\n\t" "s_waitcnt lgkmcnt(0)\n\t" "s_barrier\n\t" \
   "ds_write_b128 v20, a[168:171] offset:0\n\t" "s_mov_b32 s41, 4718592\n\t" "buffer_load_dwordx4 v[224:227], v16, %[ra], s41 offen\n\t" \
   "s_waitcnt vmcnt(15)\n\t" "ds_write_b128 v20, a[172:175] offset:4096\n\t" "s_mov_b32 s41, 4849664\n\t" \
   "buffer_load_dwordx4 v[228:231], v16, %[ra], s41 offen\n\t" "s_waitcnt vmcnt(15)\n\t" "ds_write_b128 v20, a[184:187] offset:8192\n\t" \
   "s_mov_b32 s41, 4980736\n\t" "buffer_load_dwordx4 v[240:243], v16, %[ra], s41 offen\n\t" "s_waitcnt vmcnt(15)\n\t" \
   "ds_write_b128 v20, a[188:191] offset:12288\n\t" "s_mov_b32 s41, 5111808\n\t" "buffer_load_dwordx4 v[244:247], v16, %[ra], s41 offen\n\t" \
   "s_waitcnt vmcnt(15)\n\t" "ds_write_b128 v20, a[200:203] offset:16384\n\t" "s_mov_b32 s41, 3932160\n\t" \
   "buffer_load_dwordx4 a[248:251], v16, %[rb], s41 offen\n\t" "s_waitcnt vmcnt(15)\n\t" "ds_write_b128 v20, a[204:207] offset:20480\n\t" \
   "s_mov_b32 s41, 4063232\n\t" "buffer_load_dwordx4 a[252:255], v16, %[rb], s41 offen\n\t" "s_waitcnt vmcnt(15)\n\t" \
   "ds_write_b128 v20, a[216:219] offset:24576\n\t" "s_mov_b32 s41, 4194304\n\t" "buffer_load_dwordx4 v[200:203], v16, %[rb], s41 offen\n\t" \
   "s_waitcnt vmcnt(15)\n\t" "ds_write_b128 v20, a[220:223] offset:28672\n\t" "s_mov_b32 s41, 4325376\n\t" \
   "buffer_load_dwordx4 v[204:207], v16, %[rb], s41 offen\n\t" "s_waitcnt vmcnt(15)\n\t" "ds_write_b128 v20, a[232:235] offset:32768\n\t" \
   "s_mov_b32 s41, 4456448\n\t" "buffer_load_dwordx4 v[216:219], v16, %[rb], s41 offen\n\t" "s_waitcnt vmcnt(15)\n\t" \
   "ds_write_b128 v20, a[236:239] offset:36864\n\t" "s_mov_b32 s41, 4587520\n\t" "buffer_load_dwordx4 v[220:223], v16, %[rb], s41 offen\n\t" \
   "s_waitcnt lgkmcnt(0)\n\t" "s_barrier\n\t" LDSTAMP(7) "ds_read_b64_tr_b16 a[168:169], v18 offset:0\n\t" \
   "ds_read_b64_tr_b16 a[170:171], v18 offset:4096\n\t" "ds_read_b64_tr_b16 a[172:173], v19 offset:0\n\t" \
   "ds_read_b64_tr_b16 a[174:175], v19 offset:4096\n\t" "ds_read_b64_tr_b16 a[184:185], v18 offset:8192\n\t" \
   "ds_read_b64_tr_b16 a[186:187], v18 offset:12288\n\t" "ds_read_b64_tr_b16 a[188:189], v19 offset:8192\n\t" \
   "ds_read_b64_tr_b16 a[190:191], v19 offset:12288\n\t" "ds_read_b64_tr_b16 a[200:201], v18 offset:16384\n\t" \
   "ds_read_b64_tr_b16 a[202:203], v18 offset:20480\n\t" "ds_read_b64_tr_b16 a[204:205], v19 offset:16384\n\t" \
   "ds_read_b64_tr_b16 a[206:207], v19 offset:20480\n\t" "ds_read_b64_tr_b16 a[216:217], v18 offset:24576\n\t" \
   "ds_read_b64_tr_b16 a[218:219], v18 offset:28672\n\t" "ds_read_b64_tr_b16 a[220:221], v19 offset:24576\n\t" \
   "ds_read_b64_tr_b16 a[222:223], v19 offset:28672\n\t" "ds_read_b64_tr_b16 a[232:233], v18 offset:32768\n\t" \
   "ds_read_b64_tr_b16 a[234:235], v18 offset:36864\n\t" "ds_read_b64_tr_b16 a[236:237], v19 offset:32768\n\t" \
   "ds_read_b64_tr_b16 a[238:239], v19 offset:36864\n\t" "s_waitcnt vmcnt(15)\n\t" "s_waitcnt lgkmcnt(0)\n\t" "s_barrier\n\t" \
   "ds_write_b128 v20, a[240:243] offset:0\n\t" "s_mov_b32 s41, 4718592\n\t" "buffer_load_dwordx4 v[232:235], v16, %[rb], s41 offen\n\t" \
   "s_waitcnt vmcnt(15)\n\t" "ds_write_b128 v20, a[244:247] offset:4096\n\t" "s_mov_b32 s41, 4849664\n\t" \
   "buffer_load_dwordx4 v[236:239], v16, %[rb], s41 offen\n\t" "s_waitcnt vmcnt(15)\n\t" "ds_write_b128 v20, v[192:195] offset:8192\n\t" \
   "s_mov_b32 s41, 4980736\n\t" "buffer_load_dwordx4 v[248:251], v16, %[rb], s41 offen\n\t" "s_waitcnt vmcnt(15)\n\t" \
   "ds_write_b128 v20, v[196:199] offset:12288\n\t" "s_mov_b32 s41, 5111808\n\t" "buffer_load_dwordx4 v[252:255], v16, %[rb], s41 offen\n\t" \
   "s_waitcnt vmcnt(15)\n\t" "ds_write_b128 v20, v[208:211] offset:16384\n\t" "s_waitcnt vmcnt(14)\n\t" \
   "ds_write_b128 v20, v[212:215] offset:20480\n\t" "s_waitcnt vmcnt(13)\n\t" "ds_write_b128 v20, v[224:227] offset:24576\n\t" \
   "s_waitcnt vmcnt(12)\n\t" "ds_write_b128 v20, v[228:231] offset:28672\n\t" "s_waitcnt vmcnt(11)\n\t" \
   "ds_write_b128 v20, v[240:243] offset:32768\n\t" "s_waitcnt vmcnt(10)\n\t" "ds_write_b128 v20, v[244:247] offset:36864\n\t" \
   "buffer_load_dwordx4 v[28:31], v112, %[rw], 0 offen sc1\n\t" "s_waitcnt lgkmcnt(0)\n\t" "s_barrier\n\t" LDSTAMP(8) \
   "ds_read_b64_tr_b16 a[240:241], v18 offset:0\n\t" "ds_read_b64_tr_b16 a[242:243], v18 offset:4096\n\t" \
   "ds_read_b64_tr_b16 a[244:245], v19 offset:0\n\t" "ds_read_b64_tr_b16 a[246:247], v19 offset:4096\n\t" \
   "ds_read_b64_tr_b16 v[192:193], v18 offset:8192\n\t" "ds_read_b64_tr_b16 v[194:195], v18 offset:12288\n\t" \
   "ds_read_b64_tr_b16 v[196:197], v19 offset:8192\n\t" "ds_read_b64_tr_b16 v[198:199], v19 offset:12288\n\t" \
   "ds_read_b64_tr_b16 v[208:209], v18 offset:16384\n\t" "ds_read_b64_tr_b16 v[210:211], v18 offset:20480\n\t" \
   "ds_read_b64_tr_b16 v[212:213], v19 offset:16384\n\t" "ds_read_b64_tr_b16 v[214:215], v19 offset:20480\n\t" \
   "ds_read_b64_tr_b16 v[224:225], v18 offset:24576\n\t" "ds_read_b64_tr_b16 v[226:227], v18 offset:28672\n\t" \
   "ds_read_b64_tr_b16 v[228:229], v19 offset:24576\n\t" "ds_read_b64_tr_b16 v[230:231], v19 offset:28672\n\t" \
   "ds_read_b64_tr_b16 v[240:241], v18 offset:32768\n\t" "ds_read_b64_tr_b16 v[242:243], v18 offset:36864\n\t" \
   "ds_read_b64_tr_b16 v[244:245], v19 offset:32768\n\t" "ds_read_b64_tr_b16 v[246:247], v19 offset:36864\n\t" "s_waitcnt vmcnt(10)\n\t" \
   "s_waitcnt lgkmcnt(0)\n\t" "s_barrier\n\t" "ds_write_b128 v20, a[248:251] offset:0\n\t" "s_waitcnt vmcnt(9)\n\t" \
   "ds_write_b128 v20, a[252:255] offset:4096\n\t" "s_waitcnt vmcnt(8)\n\t" "ds_write_b128 v20, v[200:203] offset:8192\n\t" "s_waitcnt vmcnt(7)\n\t" \
   "ds_write_b128 v20, v[204:207] offset:12288\n\t" "s_waitcnt vmcnt(6)\n\t" "ds_write_b128 v20, v[216:219] offset:16384\n\t" "s_waitcnt vmcnt(5)\n\t" \
   "ds_write_b128 v20, v[220:223] offset:20480\n\t" "s_waitcnt vmcnt(4)\n\t" "ds_write_b128 v20, v[232:235] offset:24576\n\t" "s_waitcnt vmcnt(3)\n\t" \
   "ds_write_b128 v20, v[236:239] offset:28672\n\t" "s_waitcnt vmcnt(2)\n\t" "ds_write_b128 v20, v[248:251] offset:32768\n\t" "s_waitcnt vmcnt(1)\n\t" \
   "ds_write_b128 v20, v[252:255] offset:36864\n\t" "s_waitcnt lgkmcnt(0)\n\t" "s_barrier\n\t" LDSTAMP(9) \
   "ds_read_b64_tr_b16 a[248:249], v18 offset:0\n\t" "ds_read_b64_tr_b16 a[250:251], v18 offset:4096\n\t" \
   "ds_read_b64_tr_b16 a[252:253], v19 offset:0\n\t" "ds_read_b64_tr_b16 a[254:255], v19 offset:4096\n\t" \
   "ds_read_b64_tr_b16 v[200:201], v18 offset:8192\n\t" "ds_read_b64_tr_b16 v[202:203], v18 offset:12288\n\t" \
   "ds_read_b64_tr_b16 v[204:205], v19 offset:8192\n\t" "ds_read_b64_tr_b16 v[206:207], v19 offset:12288\n\t" \
   "ds_read_b64_tr_b16 v[216:217], v18 offset:16384\n\t" "ds_read_b64_tr_b16 v[218:219], v18 offset:20480\n\t" \
   "ds_read_b64_tr_b16 v[220:221], v19 offset:16384\n\t" "ds_read_b64_tr_b16 v[222:223], v19 offset:20480\n\t" \
   "ds_read_b64_tr_b16 v[232:233], v18 offset:24576\n\t" "ds_read_b64_tr_b16 v[234:235], v18 offset:28672\n\t" \
   "ds_read_b64_tr_b16 v[236:237], v19 offset:24576\n\t" "ds_read_b64_tr_b16 v[238:239], v19 offset:28672\n\t" \
   "ds_read_b64_tr_b16 v[248:249], v18 offset:32768\n\t" "ds_read_b64_tr_b16 v[250:251], v18 offset:36864\n\t" \
   "ds_read_b64_tr_b16 v[252:253], v19 offset:32768\n\t" "ds_read_b64_tr_b16 v[254:255], v19 offset:36864\n\t" "s_waitcnt vmcnt(0) lgkmcnt(0)\n\t" \
   "s_barrier"
#define LDK_CLOB "v24", "v25", "v26", "v32", "v33", "v34", "v35", "v36", "v37", "v38", "v39", "v40", "v41", "v42", "v43", "v44", "v45", "v46", "v47", "v48", "v49", "v50", "v51", "v52", "v53", "v54", "v55", "v56", "v57", "v58", "v59", "v60", "v61", "v62", "v63", "v64", "v65", "v66", "v67", "v68", "v69", "v70", "v71", "v72", "v73", "v74", "v75", "v76", "v77", "v78", "v79", "v80", "v81", "v82", "v83", "v84", "v85", "v86", "v87", "v88", "v89", "v90", "v91", "v92", "v93", "v94", "v95", "v96", "v97", "v98", "v99", "v100", "v101", "v102", "v103", "v104", "v105", "v106", "v107", "v108", "v109", "v110", "v111", "v120", "v121", "v122", "v123", "s40", "s41"
#define LDV_ASM \
   "buffer_load_dword v26, v113, %[rw], 0 offen sc1\n\t" "s_mov_b32 s41, 0\n\t" "buffer_load_dwordx4 v[32:35], v116, %[rx], s41 offen\n\t" \
   "s_mov_b32 s41, 10240\n\t" "buffer_load_dwordx4 v[36:39], v116, %[rx], s41 offen\n\t" "s_mov_b32 s41, 128\n\t" \
   "buffer_load_dwordx4 v[40:43], v116, %[rx], s41 offen\n\t" "s_mov_b32 s41, 10368\n\t" "buffer_load_dwordx4 v[44:47], v116, %[rx], s41 offen\n\t" \
   "s_mov_b32 s41, 256\n\t" "buffer_load_dwordx4 v[48:51], v116, %[rx], s41 offen\n\t" "s_mov_b32 s41, 10496\n\t" \
   "buffer_load_dwordx4 v[52:55], v116, %[rx], s41 offen\n\t" "s_mov_b32 s41, 384\n\t" "buffer_load_dwordx4 v[56:59], v116, %[rx], s41 offen\n\t" \
   "s_mov_b32 s41, 10624\n\t" "buffer_load_dwordx4 v[60:63], v116, %[rx], s41 offen\n\t" "s_mov_b32 s41, 512\n\t" \
   "buffer_load_dwordx4 v[64:67], v116, %[rx], s41 offen\n\t" "s_mov_b32 s41, 10752\n\t" "buffer_load_dwordx4 v[68:71], v116, %[rx], s41 offen\n\t" \
   "s_mov_b32 s41, 640\n\t" "buffer_load_dwordx4 v[72:75], v116, %[rx], s41 offen\n\t" "s_mov_b32 s41, 10880\n\t" \
   "buffer_load_dwordx4 v[76:79], v116, %[rx], s41 offen\n\t" "s_mov_b32 s41, 768\n\t" "buffer_load_dwordx4 v[80:83], v116, %[rx], s41 offen\n\t" \
   "s_mov_b32 s41, 11008\n\t" "buffer_load_dwordx4 v[84:87], v116, %[rx], s41 offen\n\t" "s_mov_b32 s41, 896\n\t" \
   "buffer_load_dwordx4 v[88:91], v116, %[rx], s41 offen\n\t" "s_mov_b32 s41, 11136\n\t" "buffer_load_dwordx4 v[92:95], v116, %[rx], s41 offen\n\t" \
   "s_mov_b32 s40, 0x3f803f80\n\t" "v_mov_b32 v120, 0\n\t" "v_mov_b32 v121, 0\n\t" "v_mov_b32 v122, 0\n\t" "v_mov_b32 v123, 0\n\t" \
   "s_waitcnt vmcnt(16)\n\t" "v_add_u32 v26, 1, v26\n\t" "buffer_store_dword v26, v114, %[rw], 0 offen sc1\n\t" "ds_write_b32 v115, v26\n\t" LDSTAMP(0) \
   "s_waitcnt vmcnt(15)\n\t" LDSTAMP(1) "ds_write_b64 v117, v[32:33] offset:0\n\t" "ds_write_b64 v117, v[34:35] offset:256\n\t" \
   "s_mov_b32 s41, 1024\n\t" "buffer_load_dwordx4 v[96:99], v116, %[rx], s41 offen\n\t" "v_dot2c_f32_bf16 v120, s40, v32\n\t" \
   "v_dot2c_f32_bf16 v121, v32, v32\n\t" "v_dot2c_f32_bf16 v120, s40, v33\n\t" "v_dot2c_f32_bf16 v121, v33, v33\n\t" \
   "v_dot2c_f32_bf16 v120, s40, v34\n\t" "v_dot2c_f32_bf16 v121, v34, v34\n\t" "v_dot2c_f32_bf16 v120, s40, v35\n\t" \
   "v_dot2c_f32_bf16 v121, v35, v35\n\t" "s_waitcnt vmcnt(15)\n\t" "ds_write_b64 v117, v[36:37] offset:128\n\t" \
   "ds_write_b64 v117, v[38:39] offset:384\n\t" "s_mov_b32 s41, 11264\n\t" "buffer_load_dwordx4 v[100:103], v116, %[rx], s41 offen\n\t" \
   "v_dot2c_f32_bf16 v122, s40, v36\n\t" "v_dot2c_f32_bf16 v123, v36, v36\n\t" "v_dot2c_f32_bf16 v122, s40, v37\n\t" \
   "v_dot2c_f32_bf16 v123, v37, v37\n\t" "v_dot2c_f32_bf16 v122, s40, v38\n\t" "v_dot2c_f32_bf16 v123, v38, v38\n\t" \
   "v_dot2c_f32_bf16 v122, s40, v39\n\t" "v_dot2c_f32_bf16 v123, v39, v39\n\t" "s_waitcnt vmcnt(15)\n\t" "ds_write_b64 v117, v[40:41] offset:2048\n\t" \
   "ds_write_b64 v117, v[42:43] offset:2304\n\t" "s_mov_b32 s41, 1152\n\t" "buffer_load_dwordx4 v[104:107], v116, %[rx], s41 offen\n\t" \
   "v_dot2c_f32_bf16 v120, s40, v40\n\t" "v_dot2c_f32_bf16 v121, v40, v40\n\t" "v_dot2c_f32_bf16 v120, s40, v41\n\t" \
   "v_dot2c_f32_bf16 v121, v41, v41\n\t" "v_dot2c_f32_bf16 v120, s40, v42\n\t" "v_dot2c_f32_bf16 v121, v42, v42\n\t" \
   "v_dot2c_f32_bf16 v120, s40, v43\n\t" "v_dot2c_f32_bf16 v121, v43, v43\n\t" "s_waitcnt vmcnt(15)\n\t" "ds_write_b64 v117, v[44:45] offset:2176\n\t" \
   "ds_write_b64 v117, v[46:47] offset:2432\n\t" "s_mov_b32 s41, 11392\n\t" "buffer_load_dwordx4 v[108:111], v116, %[rx], s41 offen\n\t" \
   "v_dot2c_f32_bf16 v122, s40, v44\n\t" "v_dot2c_f32_bf16 v123, v44, v44\n\t" "v_dot2c_f32_bf16 v122, s40, v45\n\t" \
   "v_dot2c_f32_bf16 v123, v45, v45\n\t" "v_dot2c_f32_bf16 v122, s40, v46\n\t" "v_dot2c_f32_bf16 v123, v46, v46\n\t" \
   "v_dot2c_f32_bf16 v122, s40, v47\n\t" "v_dot2c_f32_bf16 v123, v47, v47\n\t" "s_waitcnt vmcnt(15)\n\t" "ds_write_b64 v117, v[48:49] offset:4096\n\t" \
   "ds_write_b64 v117, v[50:51] offset:4352\n\t" "s_mov_b32 s41, 0\n\t" "buffer_load_dwordx4 a[0:3], v16, %[ra], s41 offen\n\t" \
   "v_dot2c_f32_bf16 v120, s40, v48\n\t" "v_dot2c_f32_bf16 v121, v48, v48\n\t" "v_dot2c_f32_bf16 v120, s40, v49\n\t" \
   "v_dot2c_f32_bf16 v121, v49, v49\n\t" "v_dot2c_f32_bf16 v120, s40, v50\n\t" "v_dot2c_f32_bf16 v121, v50, v50\n\t" \
   "v_dot2c_f32_bf16 v120, s40, v51\n\t" "v_dot2c_f32_bf16 v121, v51, v51\n\t" "s_waitcnt vmcnt(15)\n\t" "ds_write_b64 v117, v[52:53] offset:4224\n\t" \
   "ds_write_b64 v117, v[54:55] offset:4480\n\t" "s_mov_b32 s41, 131072\n\t" "buffer_load_dwordx4 a[4:7], v16, %[ra], s41 offen\n\t" \
   "v_dot2c_f32_bf16 v122, s40, v52\n\t" "v_dot2c_f32_bf16 v123, v52, v52\n\t" "v_dot2c_f32_bf16 v122, s40, v53\n\t" \
   "v_dot2c_f32_bf16 v123, v53, v53\n\t" "v_dot2c_f32_bf16 v122, s40, v54\n\t" "v_dot2c_f32_bf16 v123, v54, v54\n\t" \
   "v_dot2c_f32_bf16 v122, s40, v55\n\t" "v_dot2c_f32_bf16 v123, v55, v55\n\t" "s_waitcnt vmcnt(15)\n\t" "ds_write_b64 v117, v[56:57] offset:6144\n\t" \
   "ds_write_b64 v117, v[58:59] offset:6400\n\t" "s_mov_b32 s41, 262144\n\t" "buffer_load_dwordx4 a[8:11], v16, %[ra], s41 offen\n\t" \
   "v_dot2c_f32_bf16 v120, s40, v56\n\t" "v_dot2c_f32_bf16 v121, v56, v56\n\t" "v_dot2c_f32_bf16 v120, s40, v57\n\t" \
   "v_dot2c_f32_bf16 v121, v57, v57\n\t" "v_dot2c_f32_bf16 v120, s40, v58\n\t" "v_dot2c_f32_bf16 v121, v58, v58\n\t" \
   "v_dot2c_f32_bf16 v120, s40, v59\n\t" "v_dot2c_f32_bf16 v121, v59, v59\n\t" "s_waitcnt vmcnt(15)\n\t" "ds_write_b64 v117, v[60:61] offset:6272\n\t" \
   "ds_write_b64 v117, v[62:63] offset:6528\n\t" "s_mov_b32 s41, 393216\n\t" "buffer_load_dwordx4 a[12:15], v16, %[ra], s41 offen\n\t" \
   "v_dot2c_f32_bf16 v122, s40, v60\n\t" "v_dot2c_f32_bf16 v123, v60, v60\n\t" "v_dot2c_f32_bf16 v122, s40, v61\n\t" \
   "v_dot2c_f32_bf16 v123, v61, v61\n\t" "v_dot2c_f32_bf16 v122, s40, v62\n\t" "v_dot2c_f32_bf16 v123, v62, v62\n\t" \
   "v_dot2c_f32_bf16 v122, s40, v63\n\t" "v_dot2c_f32_bf16 v123, v63, v63\n\t" "s_waitcnt vmcnt(15)\n\t" "ds_write_b64 v117, v[64:65] offset:8192\n\t" \
   "ds_write_b64 v117, v[66:67] offset:8448\n\t" "s_mov_b32 s41, 524288\n\t" "buffer_load_dwordx4 a[16:19], v16, %[ra], s41 offen\n\t" \
   "v_dot2c_f32_bf16 v120, s40, v64\n\t" "v_dot2c_f32_bf16 v121, v64, v64\n\t" "v_dot2c_f32_bf16 v120, s40, v65\n\t" \
   "v_dot2c_f32_bf16 v121, v65, v65\n\t" "v_dot2c_f32_bf16 v120, s40, v66\n\t" "v_dot2c_f32_bf16 v121, v66, v66\n\t" \
   "v_dot2c_f32_bf16 v120, s40, v67\n\t" "v_dot2c_f32_bf16 v121, v67, v67\n\t" "s_waitcnt vmcnt(15)\n\t" "ds_write_b64 v117, v[68:69] offset:8320\n\t" \
   "ds_write_b64 v117, v[70:71] offset:8576\n\t" "s_mov_b32 s41, 655360\n\t" "buffer_load_dwordx4 a[20:23], v16, %[ra], s41 offen\n\t" \
   "v_dot2c_f32_bf16 v122, s40, v68\n\t" "v_dot2c_f32_bf16 v123, v68, v68\n\t" "v_dot2c_f32_bf16 v122, s40, v69\n\t" \
   "v_dot2c_f32_bf16 v123, v69, v69\n\t" "v_dot2c_f32_bf16 v122, s40, v70\n\t" "v_dot2c_f32_bf16 v123, v70, v70\n\t" \
   "v_dot2c_f32_bf16 v122, s40, v71\n\t" "v_dot2c_f32_bf16 v123, v71, v71\n\t" "s_waitcnt vmcnt(15)\n\t" "ds_write_b64 v117, v[72:73] offset:10240\n\t" \
   "ds_write_b64 v117, v[74:75] offset:10496\n\t" "s_mov_b32 s41, 786432\n\t" "buffer_load_dwordx4 a[24:27], v16, %[ra], s41 offen\n\t" \
   "v_dot2c_f32_bf16 v120, s40, v72\n\t" "v_dot2c_f32_bf16 v121, v72, v72\n\t" "v_dot2c_f32_bf16 v120, s40, v73\n\t" \
   "v_dot2c_f32_bf16 v121, v73, v73\n\t" "v_dot2c_f32_bf16 v120, s40, v74\n\t" "v_dot2c_f32_bf16 v121, v74, v74\n\t" \
   "v_dot2c_f32_bf16 v120, s40, v75\n\t" "v_dot2c_f32_bf16 v121, v75, v75\n\t" "s_waitcnt vmcnt(15)\n\t" "ds_write_b64 v117, v[76:77] offset:10368\n\t" \
   "ds_write_b64 v117, v[78:79] offset:10624\n\t" "s_mov_b32 s41, 917504\n\t" "buffer_load_dwordx4 a[28:31], v16, %[ra], s41 offen\n\t" \
   "v_dot2c_f32_bf16 v122, s40, v76\n\t" "v_dot2c_f32_bf16 v123, v76, v76\n\t" "v_dot2c_f32_bf16 v122, s40, v77\n\t" \
   "v_dot2c_f32_bf16 v123, v77, v77\n\t" "v_dot2c_f32_bf16 v122, s40, v78\n\t" "v_dot2c_f32_bf16 v123, v78, v78\n\t" \
   "v_dot2c_f32_bf16 v122, s40, v79\n\t" "v_dot2c_f32_bf16 v123, v79, v79\n\t" "s_waitcnt vmcnt(15)\n\t" "ds_write_b64 v117, v[80:81] offset:12288\n\t" \
   "ds_write_b64 v117, v[82:83] offset:12544\n\t" "s_mov_b32 s41, 1048576\n\t" "buffer_load_dwordx4 a[32:35], v16, %[ra], s41 offen\n\t" \
   "v_dot2c_f32_bf16 v120, s40, v80\n\t" "v_dot2c_f32_bf16 v121, v80, v80\n\t" "v_dot2c_f32_bf16 v120, s40, v81\n\t" \
   "v_dot2c_f32_bf16 v121, v81, v81\n\t" "v_dot2c_f32_bf16 v120, s40, v82\n\t" "v_dot2c_f32_bf16 v121, v82, v82\n\t" \
   "v_dot2c_f32_bf16 v120, s40, v83\n\t" "v_dot2c_f32_bf16 v121, v83, v83\n\t" "s_waitcnt vmcnt(15)\n\t" "ds_write_b64 v117, v[84:85] offset:12416\n\t" \
   "ds_write_b64 v117, v[86:87] offset:12672\n\t" "s_mov_b32 s41, 1179648\n\t" "buffer_load_dwordx4 a[36:39], v16, %[ra], s41 offen\n\t" \
   "v_dot2c_f32_bf16 v122, s40, v84\n\t" "v_dot2c_f32_bf16 v123, v84, v84\n\t" "v_dot2c_f32_bf16 v122, s40, v85\n\t" \
   "v_dot2c_f32_bf16 v123, v85, v85\n\t" "v_dot2c_f32_bf16 v122, s40, v86\n\t" "v_dot2c_f32_bf16 v123, v86, v86\n\t" \
   "v_dot2c_f32_bf16 v122, s40, v87\n\t" "v_dot2c_f32_bf16 v123, v87, v87\n\t" "s_waitcnt vmcnt(15)\n\t" "ds_write_b64 v117, v[88:89] offset:14336\n\t" \
   "ds_write_b64 v117, v[90:91] offset:14592\n\t" "s_mov_b32 s41, 1310720\n\t" "buffer_load_dwordx4 a[40:43], v16, %[ra], s41 offen\n\t" \
   "v_dot2c_f32_bf16 v120, s40, v88\n\t" "v_dot2c_f32_bf16 v121, v88, v88\n\t" "v_dot2c_f32_bf16 v120, s40, v89\n\t" \
   "v_dot2c_f32_bf16 v121, v89, v89\n\t" "v_dot2c_f32_bf16 v120, s40, v90\n\t" "v_dot2c_f32_bf16 v121, v90, v90\n\t" \
   "v_dot2c_f32_bf16 v120, s40, v91\n\t" "v_dot2c_f32_bf16 v121, v91, v91\n\t" "s_waitcnt vmcnt(15)\n\t" "ds_write_b64 v117, v[92:93] offset:14464\n\t" \
   "ds_write_b64 v117, v[94:95] offset:14720\n\t" "s_mov_b32 s41, 1441792\n\t" "buffer_load_dwordx4 a[44:47], v16, %[ra], s41 offen\n\t" \
   "v_dot2c_f32_bf16 v122, s40, v92\n\t" "v_dot2c_f32_bf16 v123, v92, v92\n\t" "v_dot2c_f32_bf16 v122, s40, v93\n\t" \
   "v_dot2c_f32_bf16 v123, v93, v93\n\t" "v_dot2c_f32_bf16 v122, s40, v94\n\t" "v_dot2c_f32_bf16 v123, v94, v94\n\t" \
   "v_dot2c_f32_bf16 v122, s40, v95\n\t" "v_dot2c_f32_bf16 v123, v95, v95\n\t" "s_waitcnt vmcnt(15)\n\t" "ds_write_b64 v117, v[96:97] offset:16384\n\t" \
   "ds_write_b64 v117, v[98:99] offset:16640\n\t" "s_mov_b32 s41, 1572864\n\t" "buffer_load_dwordx4 a[48:51], v16, %[ra], s41 offen\n\t" \
   "v_dot2c_f32_bf16 v120, s40, v96\n\t" "v_dot2c_f32_bf16 v121, v96, v96\n\t" "v_dot2c_f32_bf16 v120, s40, v97\n\t" \
   "v_dot2c_f32_bf16 v121, v97, v97\n\t" "v_dot2c_f32_bf16 v120, s40, v98\n\t" "v_dot2c_f32_bf16 v121, v98, v98\n\t" \
   "v_dot2c_f32_bf16 v120, s40, v99\n\t" "v_dot2c_f32_bf16 v121, v99, v99\n\t" "s_waitcnt vmcnt(15)\n\t" \
   "ds_write_b64 v117, v[100:101] offset:16512\n\t" "ds_write_b64 v117, v[102:103] offset:16768\n\t" "s_mov_b32 s41, 1703936\n\t" \
   "buffer_load_dwordx4 a[52:55], v16, %[ra], s41 offen\n\t" "v_dot2c_f32_bf16 v122, s40, v100\n\t" "v_dot2c_f32_bf16 v123, v100, v100\n\t" \
   "v_dot2c_f32_bf16 v122, s40, v101\n\t" "v_dot2c_f32_bf16 v123, v101, v101\n\t" "v_dot2c_f32_bf16 v122, s40, v102\n\t" \
   "v_dot2c_f32_bf16 v123, v102, v102\n\t" "v_dot2c_f32_bf16 v122, s40, v103\n\t" "v_dot2c_f32_bf16 v123, v103, v103\n\t" "s_waitcnt vmcnt(15)\n\t" \
   "ds_write_b64 v117, v[104:105] offset:18432\n\t" "ds_write_b64 v117, v[106:107] offset:18688\n\t" "s_mov_b32 s41, 1835008\n\t" \
   "buffer_load_dwordx4 a[56:59], v16, %[ra], s41 offen\n\t" "v_dot2c_f32_bf16 v120, s40, v104\n\t" "v_dot2c_f32_bf16 v121, v104, v104\n\t" \
   "v_dot2c_f32_bf16 v120, s40, v105\n\t" "v_dot2c_f32_bf16 v121, v105, v105\n\t" "v_dot2c_f32_bf16 v120, s40, v106\n\t" \
   "v_dot2c_f32_bf16 v121, v106, v106\n\t" "v_dot2c_f32_bf16 v120, s40, v107\n\t" "v_dot2c_f32_bf16 v121, v107, v107\n\t" "s_waitcnt vmcnt(15)\n\t" \
   "ds_write_b64 v117, v[108:109] offset:18560\n\t" "ds_write_b64 v117, v[110:111] offset:18816\n\t" "s_mov_b32 s41, 1966080\n\t" \
   "buffer_load_dwordx4 a[60:63], v16, %[ra], s41 offen\n\t" "v_dot2c_f32_bf16 v122, s40, v108\n\t" "v_dot2c_f32_bf16 v123, v108, v108\n\t" \
   "v_dot2c_f32_bf16 v122, s40, v109\n\t" "v_dot2c_f32_bf16 v123, v109, v109\n\t" "v_dot2c_f32_bf16 v122, s40, v110\n\t" \
   "v_dot2c_f32_bf16 v123, v110, v110\n\t" "v_dot2c_f32_bf16 v122, s40, v111\n\t" "v_dot2c_f32_bf16 v123, v111, v111\n\t" "s_nop 3\n\t" \
   "v_add_f32_dpp v120, v120, v120 quad_perm:[1,0,3,2] row_mask:0xf bank_mask:0xf\n\t" \
   "v_add_f32_dpp v121, v121, v121 quad_perm:[1,0,3,2] row_mask:0xf bank_mask:0xf\n\t" \
   "v_add_f32_dpp v122, v122, v122 quad_perm:[1,0,3,2] row_mask:0xf bank_mask:0xf\n\t" \
   "v_add_f32_dpp v123, v123, v123 quad_perm:[1,0,3,2] row_mask:0xf bank_mask:0xf\n\t" \
   "v_add_f32_dpp v120, v120, v120 quad_perm:[2,3,0,1] row_mask:0xf bank_mask:0xf\n\t" \
   "v_add_f32_dpp v121, v121, v121 quad_perm:[2,3,0,1] row_mask:0xf bank_mask:0xf\n\t" \
   "v_add_f32_dpp v122, v122, v122 quad_perm:[2,3,0,1] row_mask:0xf bank_mask:0xf\n\t" \
   "v_add_f32_dpp v123, v123, v123 quad_perm:[2,3,0,1] row_mask:0xf bank_mask:0xf\n\t" \
   "v_add_f32_dpp v120, v120, v120 row_half_mirror row_mask:0xf bank_mask:0xf\n\t" \
   "v_add_f32_dpp v121, v121, v121 row_half_mirror row_mask:0xf bank_mask:0xf\n\t" \
   "v_add_f32_dpp v122, v122, v122 row_half_mirror row_mask:0xf bank_mask:0xf\n\t" \
   "v_add_f32_dpp v123, v123, v123 row_half_mirror row_mask:0xf bank_mask:0xf\n\t" "v_and_b32 v120, v119, v120\n\t" "v_and_b32 v121, v119, v121\n\t" \
   "v_and_b32 v122, v119, v122\n\t" "v_and_b32 v123, v119, v123\n\t" "ds_write_b64 v118, v[120:121]\n\t" "ds_write_b64 v118, v[122:123] offset:384\n\t" \
   "s_waitcnt vmcnt(15)\n\t" "ds_write_b128 v20, a[0:3] offset:0\n\t" "s_mov_b32 s41, 2097152\n\t" \
   "buffer_load_dwordx4 a[64:67], v16, %[ra], s41 offen\n\t" "s_waitcnt vmcnt(15)\n\t" "ds_write_b128 v20, a[4:7] offset:4096\n\t" \
   "s_mov_b32 s41, 2228224\n\t" "buffer_load_dwordx4 a[68:71], v16, %[ra], s41 offen\n\t" "s_waitcnt vmcnt(15)\n\t" \
   "ds_write_b128 v20, a[8:11] offset:8192\n\t" "s_mov_b32 s41, 2359296\n\t" "buffer_load_dwordx4 a[72:75], v16, %[ra], s41 offen\n\t" \
   "s_waitcnt vmcnt(15)\n\t" "ds_write_b128 v20, a[12:15] offset:12288\n\t" "s_mov_b32 s41, 2490368\n\t" \
   "buffer_load_dwordx4 a[76:79], v16, %[ra], s41 offen\n\t" "s_waitcnt vmcnt(15)\n\t" "ds_write_b128 v20, a[16:19] offset:16384\n\t" \
   "s_mov_b32 s41, 2621440\n\t" "buffer_load_dwordx4 a[80:83], v16, %[ra], s41 offen\n\t" "s_waitcnt vmcnt(15)\n\t" \
   "ds_write_b128 v20, a[20:23] offset:20480\n\t" "s_mov_b32 s41, 2752512\n\t" "buffer_load_dwordx4 a[84:87], v16, %[ra], s41 offen\n\t" \
   "s_waitcnt vmcnt(15)\n\t" "ds_write_b128 v20, a[24:27] offset:24576\n\t" "s_mov_b32 s41, 2883584\n\t" \
   "buffer_load_dwordx4 a[88:91], v16, %[ra], s41 offen\n\t" "s_waitcnt vmcnt(15)\n\t" "ds_write_b128 v20, a[28:31] offset:28672\n\t" \
   "s_mov_b32 s41, 3014656\n\t" "buffer_load_dwordx4 a[92:95], v16, %[ra], s41 offen\n\t" "s_waitcnt vmcnt(15)\n\t" \
   "ds_write_b128 v20, a[32:35] offset:32768\n\t" "s_mov_b32 s41, 3145728\n\t" "buffer_load_dwordx4 a[96:99], v16, %[ra], s41 offen\n\t" \
   "s_waitcnt vmcnt(15)\n\t" "ds_write_b128 v20, a[36:39] offset:36864\n\t" "s_mov_b32 s41, 3276800\n\t" \
   "buffer_load_dwordx4 a[100:103], v16, %[ra], s41 offen\n\t" "s_waitcnt lgkmcnt(0)\n\t" "s_barrier\n\t" LDSTAMP(2) \
   "ds_read_b64_tr_b16 a[0:1], v18 offset:0\n\t" "ds_read_b64_tr_b16 a[2:3], v18 offset:4096\n\t" "ds_read_b64_tr_b16 a[4:5], v19 offset:0\n\t" \
   "ds_read_b64_tr_b16 a[6:7], v19 offset:4096\n\t" "ds_read_b64_tr_b16 a[8:9], v18 offset:8192\n\t" \
   "ds_read_b64_tr_b16 a[10:11], v18 offset:12288\n\t" "ds_read_b64_tr_b16 a[12:13], v19 offset:8192\n\t" \
   "ds_read_b64_tr_b16 a[14:15], v19 offset:12288\n\t" "ds_read_b64_tr_b16 a[16:17], v18 offset:16384\n\t" \
   "ds_read_b64_tr_b16 a[18:19], v18 offset:20480\n\t" "ds_read_b64_tr_b16 a[20:21], v19 offset:16384\n\t" \
   "ds_read_b64_tr_b16 a[22:23], v19 offset:20480\n\t" "ds_read_b64_tr_b16 a[24:25], v18 offset:24576\n\t" \
   "ds_read_b64_tr_b16 a[26:27], v18 offset:28672\n\t" "ds_read_b64_tr_b16 a[28:29], v19 offset:24576\n\t" \
   "ds_read_b64_tr_b16 a[30:31], v19 offset:28672\n\t" "ds_read_b64_tr_b16 a[32:33], v18 offset:32768\n\t" \
   "ds_read_b64_tr_b16 a[34:35], v18 offset:36864\n\t" "ds_read_b64_tr_b16 a[36:37], v19 offset:32768\n\t" \
   "ds_read_b64_tr_b16 a[38:39], v19 offset:36864\n\t" "s_waitcnt vmcnt(15)\n\t" "s_waitcnt lgkmcnt(0)\n\t" "s_barrier\n\t" \
   "ds_write_b128 v20, a[40:43] offset:0\n\t" "s_mov_b32 s41, 3407872\n\t" "buffer_load_dwordx4 a[104:107], v16, %[ra], s41 offen\n\t" \
   "s_waitcnt vmcnt(15)\n\t" "ds_write_b128 v20, a[44:47] offset:4096\n\t" "s_mov_b32 s41, 3538944\n\t" \
   "buffer_load_dwordx4 a[108:111], v16, %[ra], s41 offen\n\t" "s_waitcnt vmcnt(15)\n\t" "ds_write_b128 v20, a[48:51] offset:8192\n\t" \
   "s_mov_b32 s41, 3670016\n\t" "buffer_load_dwordx4 a[112:115], v16, %[ra], s41 offen\n\t" "s_waitcnt vmcnt(15)\n\t" \
   "ds_write_b128 v20, a[52:55] offset:12288\n\t" "s_mov_b32 s41, 3801088\n\t" "buffer_load_dwordx4 a[116:119], v16, %[ra], s41 offen\n\t" \
   "s_waitcnt vmcnt(15)\n\t" "ds_write_b128 v20, a[56:59] offset:16384\n\t" "s_mov_b32 s41, 3932160\n\t" \
   "buffer_load_dwordx4 a[120:123], v16, %[ra], s41 offen\n\t" "s_waitcnt vmcnt(15)\n\t" "ds_write_b128 v20, a[60:63] offset:20480\n\t" \
   "s_mov_b32 s41, 4063232\n\t" "buffer_load_dwordx4 a[124:127], v16, %[ra], s41 offen\n\t" "s_waitcnt vmcnt(15)\n\t" \
   "ds_write_b128 v20, a[64:67] offset:24576\n\t" "s_mov_b32 s41, 4194304\n\t" "buffer_load_dwordx4 a[128:131], v16, %[ra], s41 offen\n\t" \
   "s_waitcnt vmcnt(15)\n\t" "ds_write_b128 v20, a[68:71] offset:28672\n\t" "s_mov_b32 s41, 4325376\n\t" \
   "buffer_load_dwordx4 a[132:135], v16, %[ra], s41 offen\n\t" "s_waitcnt vmcnt(15)\n\t" "ds_write_b128 v20, a[72:75] offset:32768\n\t" \
   "s_mov_b32 s41, 4456448\n\t" "buffer_load_dwordx4 a[136:139], v16, %[ra], s41 offen\n\t" "s_waitcnt vmcnt(15)\n\t" \
   "ds_write_b128 v20, a[76:79] offset:36864\n\t" "s_mov_b32 s41, 4587520\n\t" "buffer_load_dwordx4 a[140:143], v16, %[ra], s41 offen\n\t" \
   "s_waitcnt lgkmcnt(0)\n\t" "s_barrier\n\t" LDSTAMP(3) "ds_read_b64_tr_b16 a[40:41], v18 offset:0\n\t" \
   "ds_read_b64_tr_b16 a[42:43], v18 offset:4096\n\t" "ds_read_b64_tr_b16 a[44:45], v19 offset:0\n\t" \
   "ds_read_b64_tr_b16 a[46:47], v19 offset:4096\n\t" "ds_read_b64_tr_b16 a[48:49], v18 offset:8192\n\t" \
   "ds_read_b64_tr_b16 a[50:51], v18 offset:12288\n\t" "ds_read_b64_tr_b16 a[52:53], v19 offset:8192\n\t" \
   "ds_read_b64_tr_b16 a[54:55], v19 offset:12288\n\t" "ds_read_b64_tr_b16 a[56:57], v18 offset:16384\n\t" \
   "ds_read_b64_tr_b16 a[58:59], v18 offset:20480\n\t" "ds_read_b64_tr_b16 a[60:61], v19 offset:16384\n\t" \
   "ds_read_b64_tr_b16 a[62:63], v19 offset:20480\n\t" "ds_read_b64_tr_b16 a[64:65], v18 offset:24576\n\t" \
   "ds_read_b64_tr_b16 a[66:67], v18 offset:28672\n\t" "ds_read_b64_tr_b16 a[68:69], v19 offset:24576\n\t" \
   "ds_read_b64_tr_b16 a[70:71], v19 offset:28672\n\t" "ds_read_b64_tr_b16 a[72:73], v18 offset:32768\n\t" \
   "ds_read_b64_tr_b16 a[74:75], v18 offset:36864\n\t" "ds_read_b64_tr_b16 a[76:77], v19 offset:32768\n\t" \
   "ds_read_b64_tr_b16 a[78:79], v19 offset:36864\n\t" "s_waitcnt vmcnt(15)\n\t" "s_waitcnt lgkmcnt(0)\n\t" "s_barrier\n\t" \
   "ds_write_b128 v20, a[80:83] offset:0\n\t" "s_mov_b32 s41, 4718592\n\t" "buffer_load_dwordx4 a[144:147], v16, %[ra], s41 offen\n\t" \
   "s_waitcnt vmcnt(15)\n\t" "ds_write_b128 v20, a[84:87] offset:4096\n\t" "s_mov_b32 s41, 4849664\n\t" \
   "buffer_load_dwordx4 a[148:151], v16, %[ra], s41 offen\n\t" "s_waitcnt vmcnt(15)\n\t" "ds_write_b128 v20, a[88:91] offset:8192\n\t" \
   "s_mov_b32 s41, 4980736\n\t" "buffer_load_dwordx4 a[152:155], v16, %[ra], s41 offen\n\t" "s_waitcnt vmcnt(15)\n\t" \
   "ds_write_b128 v20, a[92:95] offset:12288\n\t" "s_mov_b32 s41, 5111808\n\t" "buffer_load_dwordx4 a[156:159], v16, %[ra], s41 offen\n\t" \
   "s_waitcnt vmcnt(15)\n\t" "ds_write_b128 v20, a[96:99] offset:16384\n\t" "s_mov_b32 s41, 0\n\t" \
   "buffer_load_dwordx4 a[160:163], v17, %[rb], s41 offen\n\t" "s_waitcnt vmcnt(15)\n\t" "ds_write_b128 v20, a[100:103] offset:20480\n\t" \
   "s_mov_b32 s41, 256\n\t" "buffer_load_dwordx4 a[176:179], v17, %[rb], s41 offen\n\t" "s_waitcnt vmcnt(15)\n\t" \
   "ds_write_b128 v20, a[104:107] offset:24576\n\t" "s_mov_b32 s41, 512\n\t" "buffer_load_dwordx4 a[192:195], v17, %[rb], s41 offen\n\t" \
   "s_waitcnt vmcnt(15)\n\t" "ds_write_b128 v20, a[108:111] offset:28672\n\t" "s_mov_b32 s41, 768\n\t" \
   "buffer_load_dwordx4 a[208:211], v17, %[rb], s41 offen\n\t" "s_waitcnt vmcnt(15)\n\t" "ds_write_b128 v20, a[112:115] offset:32768\n\t" \
   "s_mov_b32 s41, 1024\n\t" "buffer_load_dwordx4 a[224:227], v17, %[rb], s41 offen\n\t" "s_waitcnt vmcnt(15)\n\t" \
   "ds_write_b128 v20, a[116:119] offset:36864\n\t" "s_mov_b32 s41, 20480\n\t" "buffer_load_dwordx4 a[240:243], v17, %[rb], s41 offen\n\t" \
   "s_waitcnt lgkmcnt(0)\n\t" "s_barrier\n\t" LDSTAMP(4) "ds_read_b64_tr_b16 a[80:81], v18 offset:0\n\t" \
   "ds_read_b64_tr_b16 a[82:83], v18 offset:4096\n\t" "ds_read_b64_tr_b16 a[84:85], v19 offset:0\n\t" \
   "ds_read_b64_tr_b16 a[86:87], v19 offset:4096\n\t" "ds_read_b64_tr_b16 a[88:89], v18 offset:8192\n\t" \
   "ds_read_b64_tr_b16 a[90:91], v18 offset:12288\n\t" "ds_read_b64_tr_b16 a[92:93], v19 offset:8192\n\t" \
   "ds_read_b64_tr_b16 a[94:95], v19 offset:12288\n\t" "ds_read_b64_tr_b16 a[96:97], v18 offset:16384\n\t" \
   "ds_read_b64_tr_b16 a[98:99], v18 offset:20480\n\t" "ds_read_b64_tr_b16 a[100:101], v19 offset:16384\n\t" \
   "ds_read_b64_tr_b16 a[102:103], v19 offset:20480\n\t" "ds_read_b64_tr_b16 a[104:105], v18 offset:24576\n\t" \
   "ds_read_b64_tr_b16 a[106:107], v18 offset:28672\n\t" "ds_read_b64_tr_b16 a[108:109], v19 offset:24576\n\t" \
   "ds_read_b64_tr_b16 a[110:111], v19 offset:28672\n\t" "ds_read_b64_tr_b16 a[112:113], v18 offset:32768\n\t" \
   "ds_read_b64_tr_b16 a[114:115], v18 offset:36864\n\t" "ds_read_b64_tr_b16 a[116:117], v19 offset:32768\n\t" \
   "ds_read_b64_tr_b16 a[118:119], v19 offset:36864\n\t" "s_waitcnt vmcnt(15)\n\t" "s_waitcnt lgkmcnt(0)\n\t" "s_barrier\n\t" \
   "ds_write_b128 v20, a[120:123] offset:0\n\t" "s_mov_b32 s41, 20736\n\t" "buffer_load_dwordx4 v[192:195], v17, %[rb], s41 offen\n\t" \
   "s_waitcnt vmcnt(15)\n\t" "ds_write_b128 v20, a[124:127] offset:4096\n\t" "s_mov_b32 s41, 20992\n\t" \
   "buffer_load_dwordx4 v[208:211], v17, %[rb], s41 offen\n\t" "s_waitcnt vmcnt(15)\n\t" "ds_write_b128 v20, a[128:131] offset:8192\n\t" \
   "s_mov_b32 s41, 21248\n\t" "buffer_load_dwordx4 v[224:227], v17, %[rb], s41 offen\n\t" "s_waitcnt vmcnt(15)\n\t" \
   "ds_write_b128 v20, a[132:135] offset:12288\n\t" "s_mov_b32 s41, 21504\n\t" "buffer_load_dwordx4 v[240:243], v17, %[rb], s41 offen\n\t" \
   "s_waitcnt vmcnt(15)\n\t" "ds_write_b128 v20, a[136:139] offset:16384\n\t" "s_mov_b32 s41, 40960\n\t" \
   "buffer_load_dwordx4 a[164:167], v17, %[rb], s41 offen\n\t" "s_waitcnt vmcnt(15)\n\t" "ds_write_b128 v20, a[140:143] offset:20480\n\t" \
   "s_mov_b32 s41, 41216\n\t" "buffer_load_dwordx4 a[180:183], v17, %[rb], s41 offen\n\t" "s_waitcnt vmcnt(15)\n\t" \
   "ds_write_b128 v20, a[144:147] offset:24576\n\t" "s_mov_b32 s41, 41472\n\t" "buffer_load_dwordx4 a[196:199], v17, %[rb], s41 offen\n\t" \
   "s_waitcnt vmcnt(15)\n\t" "ds_write_b128 v20, a[148:151] offset:28672\n\t" "s_mov_b32 s41, 41728\n\t" \
   "buffer_load_dwordx4 a[212:215], v17, %[rb], s41 offen\n\t" "s_waitcnt vmcnt(15)\n\t" "ds_write_b128 v20, a[152:155] offset:32768\n\t" \
   "s_mov_b32 s41, 41984\n\t" "buffer_load_dwordx4 a[228:231], v17, %[rb], s41 offen\n\t" "s_waitcnt vmcnt(15)\n\t" \
   "ds_write_b128 v20, a[156:159] offset:36864\n\t" "s_mov_b32 s41, 61440\n\t" "buffer_load_dwordx4 a[244:247], v17, %[rb], s41 offen\n\t" \
   "s_waitcnt lgkmcnt(0)\n\t" "s_barrier\n\t" LDSTAMP(5) "ds_read_b64_tr_b16 a[120:121], v18 offset:0\n\t" \
   "ds_read_b64_tr_b16 a[122:123], v18 offset:4096\n\t" "ds_read_b64_tr_b16 a[124:125], v19 offset:0\n\t" \
   "ds_read_b64_tr_b16 a[126:127], v19 offset:4096\n\t" "ds_read_b64_tr_b16 a[128:129], v18 offset:8192\n\t" \
   "ds_read_b64_tr_b16 a[130:131], v18 offset:12288\n\t" "ds_read_b64_tr_b16 a[132:133], v19 offset:8192\n\t" \
   "ds_read_b64_tr_b16 a[134:135], v19 offset:12288\n\t" "ds_read_b64_tr_b16 a[136:137], v18 offset:16384\n\t" \
   "ds_read_b64_tr_b16 a[138:139], v18 offset:20480\n\t" "ds_read_b64_tr_b16 a[140:141], v19 offset:16384\n\t" \
   "ds_read_b64_tr_b16 a[142:143], v19 offset:20480\n\t" "ds_read_b64_tr_b16 a[144:145], v18 offset:24576\n\t" \
   "ds_read_b64_tr_b16 a[146:147], v18 offset:28672\n\t" "ds_read_b64_tr_b16 a[148:149], v19 offset:24576\n\t" \
   "ds_read_b64_tr_b16 a[150:151], v19 offset:28672\n\t" "ds_read_b64_tr_b16 a[152:153], v18 offset:32768\n\t" \
   "ds_read_b64_tr_b16 a[154:155], v18 offset:36864\n\t" "ds_read_b64_tr_b16 a[156:157], v19 offset:32768\n\t" \
   "ds_read_b64_tr_b16 a[158:159], v19 offset:36864\n\t" "s_waitcnt vmcnt(15)\n\t" "s_waitcnt lgkmcnt(0)\n\t" "s_barrier\n\t" \
   "ds_write_b32 v21, a160 offset:0\n\t" "ds_write_b32 v21, a161 offset:21120\n\t" "ds_write_b32 v21, a162 offset:10560\n\t" \
   "ds_write_b32 v21, a163 offset:31680\n\t" "s_mov_b32 s41, 61696\n\t" "buffer_load_dwordx4 v[196:199], v17, %[rb], s41 offen\n\t" \
   "s_waitcnt vmcnt(15)\n\t" "ds_write_b32 v21, a176 offset:64\n\t" "ds_write_b32 v21, a177 offset:21184\n\t" "ds_write_b32 v21, a178 offset:10624\n\t" \
   "ds_write_b32 v21, a179 offset:31744\n\t" "s_mov_b32 s41, 61952\n\t" "buffer_load_dwordx4 v[212:215], v17, %[rb], s41 offen\n\t" \
   "s_waitcnt vmcnt(15)\n\t" "ds_write_b32 v21, a192 offset:128\n\t" "ds_write_b32 v21, a193 offset:21248\n\t" \
   "ds_write_b32 v21, a194 offset:10688\n\t" "ds_write_b32 v21, a195 offset:31808\n\t" "s_mov_b32 s41, 62208\n\t" \
   "buffer_load_dwordx4 v[228:231], v17, %[rb], s41 offen\n\t" "s_waitcnt vmcnt(15)\n\t" "ds_write_b32 v21, a208 offset:192\n\t" \
   "ds_write_b32 v21, a209 offset:21312\n\t" "ds_write_b32 v21, a210 offset:10752\n\t" "ds_write_b32 v21, a211 offset:31872\n\t" \
   "s_mov_b32 s41, 62464\n\t" "buffer_load_dwordx4 v[244:247], v17, %[rb], s41 offen\n\t" "s_waitcnt vmcnt(15)\n\t" \
   "ds_write_b32 v21, a224 offset:256\n\t" "ds_write_b32 v21, a225 offset:21376\n\t" "ds_write_b32 v21, a226 offset:10816\n\t" \
   "ds_write_b32 v21, a227 offset:31936\n\t" "s_mov_b32 s41, 81920\n\t" "buffer_load_dwordx4 a[168:171], v17, %[rb], s41 offen\n\t" \
   "s_waitcnt vmcnt(15)\n\t" "ds_write_b32 v21, a240 offset:5248\n\t" "ds_write_b32 v21, a241 offset:26368\n\t" \
   "ds_write_b32 v21, a242 offset:15808\n\t" "ds_write_b32 v21, a243 offset:36928\n\t" "s_mov_b32 s41, 82176\n\t" \
   "buffer_load_dwordx4 a[184:187], v17, %[rb], s41 offen\n\t" "s_waitcnt vmcnt(15)\n\t" "ds_write_b32 v21, v192 offset:5312\n\t" \
   "ds_write_b32 v21, v193 offset:26432\n\t" "ds_write_b32 v21, v194 offset:15872\n\t" "ds_write_b32 v21, v195 offset:36992\n\t" \
   "s_mov_b32 s41, 82432\n\t" "buffer_load_dwordx4 a[200:203], v17, %[rb], s41 offen\n\t" "s_waitcnt vmcnt(15)\n\t" \
   "ds_write_b32 v21, v208 offset:5376\n\t" "ds_write_b32 v21, v209 offset:26496\n\t" "ds_write_b32 v21, v210 offset:15936\n\t" \
   "ds_write_b32 v21, v211 offset:37056\n\t" "s_mov_b32 s41, 82688\n\t" "buffer_load_dwordx4 a[216:219], v17, %[rb], s41 offen\n\t" \
   "s_waitcnt vmcnt(15)\n\t" "ds_write_b32 v21, v224 offset:5440\n\t" "ds_write_b32 v21, v225 offset:26560\n\t" \
   "ds_write_b32 v21, v226 offset:16000\n\t" "ds_write_b32 v21, v227 offset:37120\n\t" "s_mov_b32 s41, 82944\n\t" \
   "buffer_load_dwordx4 a[232:235], v17, %[rb], s41 offen\n\t" "s_waitcnt vmcnt(15)\n\t" "ds_write_b32 v21, v240 offset:5504\n\t" \
   "ds_write_b32 v21, v241 offset:26624\n\t" "ds_write_b32 v21, v242 offset:16064\n\t" "ds_write_b32 v21, v243 offset:37184\n\t" \
   "s_mov_b32 s41, 102400\n\t" "buffer_load_dwordx4 a[248:251], v17, %[rb], s41 offen\n\t" "s_waitcnt lgkmcnt(0)\n\t" "s_barrier\n\t" LDSTAMP(6) \
   "ds_read_b64_tr_b16 a[160:161], v22 offset:0\n\t" "ds_read_b64_tr_b16 a[162:163], v22 offset:2624\n\t" \
   "ds_read_b64_tr_b16 a[176:177], v22 offset:5248\n\t" "ds_read_b64_tr_b16 a[178:179], v22 offset:7872\n\t" \
   "ds_read_b64_tr_b16 a[192:193], v22 offset:16\n\t" "ds_read_b64_tr_b16 a[194:195], v22 offset:2640\n\t" \
   "ds_read_b64_tr_b16 a[208:209], v22 offset:5264\n\t" "ds_read_b64_tr_b16 a[210:211], v22 offset:7888\n\t" \
   "ds_read_b64_tr_b16 a[224:225], v22 offset:32\n\t" "ds_read_b64_tr_b16 a[226:227], v22 offset:2656\n\t" \
   "ds_read_b64_tr_b16 a[240:241], v22 offset:5280\n\t" "ds_read_b64_tr_b16 a[242:243], v22 offset:7904\n\t" \
   "ds_read_b64_tr_b16 v[192:193], v22 offset:48\n\t" "ds_read_b64_tr_b16 v[194:195], v22 offset:2672\n\t" \
   "ds_read_b64_tr_b16 v[208:209], v22 offset:5296\n\t" "ds_read_b64_tr_b16 v[210:211], v22 offset:7920\n\t" \
   "ds_read_b64_tr_b16 v[224:225], v22 offset:64\n\t" "ds_read_b64_tr_b16 v[226:227], v22 offset:2688\n\t" \
   "ds_read_b64_tr_b16 v[240:241], v22 offset:5312\n\t" "ds_read_b64_tr_b16 v[242:243], v22 offset:7936\n\t" "s_waitcnt vmcnt(15)\n\t" \
   "s_waitcnt lgkmcnt(0)\n\t" "s_barrier\n\t" "ds_write_b32 v21, a164 offset:0\n\t" "ds_write_b32 v21, a165 offset:21120\n\t" \
   "ds_write_b32 v21, a166 offset:10560\n\t" "ds_write_b32 v21, a167 offset:31680\n\t" "s_mov_b32 s41, 102656\n\t" \
   "buffer_load_dwordx4 v[200:203], v17, %[rb], s41 offen\n\t" "s_waitcnt vmcnt(15)\n\t" "ds_write_b32 v21, a180 offset:64\n\t" \
   "ds_write_b32 v21, a181 offset:21184\n\t" "ds_write_b32 v21, a182 offset:10624\n\t" "ds_write_b32 v21, a183 offset:31744\n\t" \
   "s_mov_b32 s41, 102912\n\t" "buffer_load_dwordx4 v[216:219], v17, %[rb], s41 offen\n\t" "s_waitcnt vmcnt(15)\n\t" \
   "ds_write_b32 v21, a196 offset:128\n\t" "ds_write_b32 v21, a197 offset:21248\n\t" "ds_write_b32 v21, a198 offset:10688\n\t" \
   "ds_write_b32 v21, a199 offset:31808\n\t" "s_mov_b32 s41, 103168\n\t" "buffer_load_dwordx4 v[232:235], v17, %[rb], s41 offen\n\t" \
   "s_waitcnt vmcnt(15)\n\t" "ds_write_b32 v21, a212 offset:192\n\t" "ds_write_b32 v21, a213 offset:21312\n\t" \
   "ds_write_b32 v21, a214 offset:10752\n\t" "ds_write_b32 v21, a215 offset:31872\n\t" "s_mov_b32 s41, 103424\n\t" \
   "buffer_load_dwordx4 v[248:251], v17, %[rb], s41 offen\n\t" "s_waitcnt vmcnt(15)\n\t" "ds_write_b32 v21, a228 offset:256\n\t" \
   "ds_write_b32 v21, a229 offset:21376\n\t" "ds_write_b32 v21, a230 offset:10816\n\t" "ds_write_b32 v21, a231 offset:31936\n\t" \
   "s_mov_b32 s41, 122880\n\t" "buffer_load_dwordx4 a[172:175], v17, %[rb], s41 offen\n\t" "s_waitcnt vmcnt(15)\n\t" \
   "ds_write_b32 v21, a244 offset:5248\n\t" "ds_write_b32 v21, a245 offset:26368\n\t" "ds_write_b32 v21, a246 offset:15808\n\t" \
   "ds_write_b32 v21, a247 offset:36928\n\t" "s_mov_b32 s41, 123136\n\t" "buffer_load_dwordx4 a[188:191], v17, %[rb], s41 offen\n\t" \
   "s_waitcnt vmcnt(15)\n\t" "ds_write_b32 v21, v196 offset:5312\n\t" "ds_write_b32 v21, v197 offset:26432\n\t" \
   "ds_write_b32 v21, v198 offset:15872\n\t" "ds_write_b32 v21, v199 offset:36992\n\t" "s_mov_b32 s41, 123392\n\t" \
   "buffer_load_dwordx4 a[204:207], v17, %[rb], s41 offen\n\t" "s_waitcnt vmcnt(15)\n\t" "ds_write_b32 v21, v212 offset:5376\n\t" \
   "ds_write_b32 v21, v213 offset:26496\n\t" "ds_write_b32 v21, v214 offset:15936\n\t" "ds_write_b32 v21, v215 offset:37056\n\t" \
   "s_mov_b32 s41, 123648\n\t" "buffer_load_dwordx4 a[220:223], v17, %[rb], s41 offen\n\t" "s_waitcnt vmcnt(15)\n\t" \
   "ds_write_b32 v21, v228 offset:5440\n\t" "ds_write_b32 v21, v229 offset:26560\n\t" "ds_write_b32 v21, v230 offset:16000\n\t" \
   "ds_write_b32 v21, v231 offset:37120\n\t" "s_mov_b32 s41, 123904\n\t" "buffer_load_dwordx4 a[236:239], v17, %[rb], s41 offen\n\t" \
   "s_waitcnt vmcnt(15)\n\t" "ds_write_b32 v21, v244 offset:5504\n\t" "ds_write_b32 v21, v245 offset:26624\n\t" \
   "ds_write_b32 v21, v246 offset:16064\n\t" "ds_write_b32 v21, v247 offset:37184\n\t" "s_mov_b32 s41, 143360\n\t" \
   "buffer_load_dwordx4 a[252:255], v17, %[rb], s41 offen\n\t" "s_waitcnt lgkmcnt(0)\n\t" "s_barrier\n\t" LDSTAMP(7) \
   "ds_read_b64_tr_b16 a[164:165], v22 offset:0\n\t" "ds_read_b64_tr_b16 a[166:167], v22 offset:2624\n\t" \
   "ds_read_b64_tr_b16 a[180:181], v22 offset:5248\n\t" "ds_read_b64_tr_b16 a[182:183], v22 offset:7872\n\t" \
   "ds_read_b64_tr_b16 a[196:197], v22 offset:16\n\t" "ds_read_b64_tr_b16 a[198:199], v22 offset:2640\n\t" \
   "ds_read_b64_tr_b16 a[212:213], v22 offset:5264\n\t" "ds_read_b64_tr_b16 a[214:215], v22 offset:7888\n\t" \
   "ds_read_b64_tr_b16 a[228:229], v22 offset:32\n\t" "ds_read_b64_tr_b16 a[230:231], v22 offset:2656\n\t" \
   "ds_read_b64_tr_b16 a[244:245], v22 offset:5280\n\t" "ds_read_b64_tr_b16 a[246:247], v22 offset:7904\n\t" \
   "ds_read_b64_tr_b16 v[196:197], v22 offset:48\n\t" "ds_read_b64_tr_b16 v[198:199], v22 offset:2672\n\t" \
   "ds_read_b64_tr_b16 v[212:213], v22 offset:5296\n\t" "ds_read_b64_tr_b16 v[214:215], v22 offset:7920\n\t" \
   "ds_read_b64_tr_b16 v[228:229], v22 offset:64\n\t" "ds_read_b64_tr_b16 v[230:231], v22 offset:2688\n\t" \
   "ds_read_b64_tr_b16 v[244:245], v22 offset:5312\n\t" "ds_read_b64_tr_b16 v[246:247], v22 offset:7936\n\t" "s_waitcnt vmcnt(15)\n\t" \
   "s_waitcnt lgkmcnt(0)\n\t" "s_barrier\n\t" "ds_write_b32 v21, a168 offset:0\n\t" "ds_write_b32 v21, a169 offset:21120\n\t" \
   "ds_write_b32 v21, a170 offset:10560\n\t" "ds_write_b32 v21, a171 offset:31680\n\t" "s_mov_b32 s41, 143616\n\t" \
   "buffer_load_dwordx4 v[204:207], v17, %[rb], s41 offen\n\t" "s_waitcnt vmcnt(15)\n\t" "ds_write_b32 v21, a184 offset:64\n\t" \
   "ds_write_b32 v21, a185 offset:21184\n\t" "ds_write_b32 v21, a186 offset:10624\n\t" "ds_write_b32 v21, a187 offset:31744\n\t" \
   "s_mov_b32 s41, 143872\n\t" "buffer_load_dwordx4 v[220:223], v17, %[rb], s41 offen\n\t" "s_waitcnt vmcnt(15)\n\t" \
   "ds_write_b32 v21, a200 offset:128\n\t" "ds_write_b32 v21, a201 offset:21248\n\t" "ds_write_b32 v21, a202 offset:10688\n\t" \
   "ds_write_b32 v21, a203 offset:31808\n\t" "s_mov_b32 s41, 144128\n\t" "buffer_load_dwordx4 v[236:239], v17, %[rb], s41 offen\n\t" \
   "s_waitcnt vmcnt(15)\n\t" "ds_write_b32 v21, a216 offset:192\n\t" "ds_write_b32 v21, a217 offset:21312\n\t" \
   "ds_write_b32 v21, a218 offset:10752\n\t" "ds_write_b32 v21, a219 offset:31872\n\t" "s_mov_b32 s41, 144384\n\t" \
   "buffer_load_dwordx4 v[252:255], v17, %[rb], s41 offen\n\t" "s_waitcnt vmcnt(15)\n\t" "ds_write_b32 v21, a232 offset:256\n\t" \
   "ds_write_b32 v21, a233 offset:21376\n\t" "ds_write_b32 v21, a234 offset:10816\n\t" "ds_write_b32 v21, a235 offset:31936\n\t" \
   "s_waitcnt vmcnt(14)\n\t" "ds_write_b32 v21, a248 offset:5248\n\t" "ds_write_b32 v21, a249 offset:26368\n\t" \
   "ds_write_b32 v21, a250 offset:15808\n\t" "ds_write_b32 v21, a251 offset:36928\n\t" "s_waitcnt vmcnt(13)\n\t" \
   "ds_write_b32 v21, v200 offset:5312\n\t" "ds_write_b32 v21, v201 offset:26432\n\t" "ds_write_b32 v21, v202 offset:15872\n\t" \
   "ds_write_b32 v21, v203 offset:36992\n\t" "s_waitcnt vmcnt(12)\n\t" "ds_write_b32 v21, v216 offset:5376\n\t" \
   "ds_write_b32 v21, v217 offset:26496\n\t" "ds_write_b32 v21, v218 offset:15936\n\t" "ds_write_b32 v21, v219 offset:37056\n\t" \
   "s_waitcnt vmcnt(11)\n\t" "ds_write_b32 v21, v232 offset:5440\n\t" "ds_write_b32 v21, v233 offset:26560\n\t" \
   "ds_write_b32 v21, v234 offset:16000\n\t" "ds_write_b32 v21, v235 offset:37120\n\t" "s_waitcnt vmcnt(10)\n\t" \
   "ds_write_b32 v21, v248 offset:5504\n\t" "ds_write_b32 v21, v249 offset:26624\n\t" "ds_write_b32 v21, v250 offset:16064\n\t" \
   "ds_write_b32 v21, v251 offset:37184\n\t" "buffer_load_dwordx4 v[28:31], v112, %[rw], 0 offen sc1\n\t" "s_waitcnt lgkmcnt(0)\n\t" "s_barrier\n\t" \
   LDSTAMP(8) "ds_read_b64_tr_b16 a[168:169], v22 offset:0\n\t" "ds_read_b64_tr_b16 a[170:171], v22 offset:2624\n\t" \
   "ds_read_b64_tr_b16 a[184:185], v22 offset:5248\n\t" "ds_read_b64_tr_b16 a[186:187], v22 offset:7872\n\t" \
   "ds_read_b64_tr_b16 a[200:201], v22 offset:16\n\t" "ds_read_b64_tr_b16 a[202:203], v22 offset:2640\n\t" \
   "ds_read_b64_tr_b16 a[216:217], v22 offset:5264\n\t" "ds_read_b64_tr_b16 a[218:219], v22 offset:7888\n\t" \
   "ds_read_b64_tr_b16 a[232:233], v22 offset:32\n\t" "ds_read_b64_tr_b16 a[234:235], v22 offset:2656\n\t" \
   "ds_read_b64_tr_b16 a[248:249], v22 offset:5280\n\t" "ds_read_b64_tr_b16 a[250:251], v22 offset:7904\n\t" \
   "ds_read_b64_tr_b16 v[200:201], v22 offset:48\n\t" "ds_read_b64_tr_b16 v[202:203], v22 offset:2672\n\t" \
   "ds_read_b64_tr_b16 v[216:217], v22 offset:5296\n\t" "ds_read_b64_tr_b16 v[218:219], v22 offset:7920\n\t" \
   "ds_read_b64_tr_b16 v[232:233], v22 offset:64\n\t" "ds_read_b64_tr_b16 v[234:235], v22 offset:2688\n\t" \
   "ds_read_b64_tr_b16 v[248:249], v22 offset:5312\n\t" "ds_read_b64_tr_b16 v[250:251], v22 offset:7936\n\t" "s_waitcnt vmcnt(10)\n\t" \
   "s_waitcnt lgkmcnt(0)\n\t" "s_barrier\n\t" "ds_write_b32 v21, a172 offset:0\n\t" "ds_write_b32 v21, a173 offset:21120\n\t" \
   "ds_write_b32 v21, a174 offset:10560\n\t" "ds_write_b32 v21, a175 offset:31680\n\t" "s_waitcnt vmcnt(9)\n\t" "ds_write_b32 v21, a188 offset:64\n\t" \
   "ds_write_b32 v21, a189 offset:21184\n\t" "ds_write_b32 v21, a190 offset:10624\n\t" "ds_write_b32 v21, a191 offset:31744\n\t" \
   "s_waitcnt vmcnt(8)\n\t" "ds_write_b32 v21, a204 offset:128\n\t" "ds_write_b32 v21, a205 offset:21248\n\t" "ds_write_b32 v21, a206 offset:10688\n\t" \
   "ds_write_b32 v21, a207 offset:31808\n\t" "s_waitcnt vmcnt(7)\n\t" "ds_write_b32 v21, a220 offset:192\n\t" "ds_write_b32 v21, a221 offset:21312\n\t" \
   "ds_write_b32 v21, a222 offset:10752\n\t" "ds_write_b32 v21, a223 offset:31872\n\t" "s_waitcnt vmcnt(6)\n\t" "ds_write_b32 v21, a236 offset:256\n\t" \
   "ds_write_b32 v21, a237 offset:21376\n\t" "ds_write_b32 v21, a238 offset:10816\n\t" "ds_write_b32 v21, a239 offset:31936\n\t" \
   "s_waitcnt vmcnt(5)\n\t" "ds_write_b32 v21, a252 offset:5248\n\t" "ds_write_b32 v21, a253 offset:26368\n\t" \
   "ds_write_b32 v21, a254 offset:15808\n\t" "ds_write_b32 v21, a255 offset:36928\n\t" "s_waitcnt vmcnt(4)\n\t" \
   "ds_write_b32 v21, v204 offset:5312\n\t" "ds_write_b32 v21, v205 offset:26432\n\t" "ds_write_b32 v21, v206 offset:15872\n\t" \
   "ds_write_b32 v21, v207 offset:36992\n\t" "s_waitcnt vmcnt(3)\n\t" "ds_write_b32 v21, v220 offset:5376\n\t" \
   "ds_write_b32 v21, v221 offset:26496\n\t" "ds_write_b32 v21, v222 offset:15936\n\t" "ds_write_b32 v21, v223 offset:37056\n\t" \
   "s_waitcnt vmcnt(2)\n\t" "ds_write_b32 v21, v236 offset:5440\n\t" "ds_write_b32 v21, v237 offset:26560\n\t" \
   "ds_write_b32 v21, v238 offset:16000\n\t" "ds_write_b32 v21, v239 offset:37120\n\t" "s_waitcnt vmcnt(1)\n\t" \
   "ds_write_b32 v21, v252 offset:5504\n\t" "ds_write_b32 v21, v253 offset:26624\n\t" "ds_write_b32 v21, v254 offset:16064\n\t" \
   "ds_write_b32 v21, v255 offset:37184\n\t" "s_waitcnt lgkmcnt(0)\n\t" "s_barrier\n\t" LDSTAMP(9) "ds_read_b64_tr_b16 a[172:173], v22 offset:0\n\t" \
   "ds_read_b64_tr_b16 a[174:175], v22 offset:2624\n\t" "ds_read_b64_tr_b16 a[188:189], v22 offset:5248\n\t" \
   "ds_read_b64_tr_b16 a[190:191], v22 offset:7872\n\t" "ds_read_b64_tr_b16 a[204:205], v22 offset:16\n\t" \
   "ds_read_b64_tr_b16 a[206:207], v22 offset:2640\n\t" "ds_read_b64_tr_b16 a[220:221], v22 offset:5264\n\t" \
   "ds_read_b64_tr_b16 a[222:223], v22 offset:7888\n\t" "ds_read_b64_tr_b16 a[236:237], v22 offset:32\n\t" \
   "ds_read_b64_tr_b16 a[238:239], v22 offset:2656\n\t" "ds_read_b64_tr_b16 a[252:253], v22 offset:5280\n\t" \
   "ds_read_b64_tr_b16 a[254:255], v22 offset:7904\n\t" "ds_read_b64_tr_b16 v[204:205], v22 offset:48\n\t" \
   "ds_read_b64_tr_b16 v[206:207], v22 offset:2672\n\t" "ds_read_b64_tr_b16 v[220:221], v22 offset:5296\n\t" \
   "ds_read_b64_tr_b16 v[222:223], v22 offset:7920\n\t" "ds_read_b64_tr_b16 v[236:237], v22 offset:64\n\t" \
   "ds_read_b64_tr_b16 v[238:239], v22 offset:2688\n\t" "ds_read_b64_tr_b16 v[252:253], v22 offset:5312\n\t" \
   "ds_read_b64_tr_b16 v[254:255], v22 offset:7936\n\t" "s_waitcnt vmcnt(0) lgkmcnt(0)\n\t" "s_barrier"
#define LDV_CLOB "v24", "v25", "v26", "v32", "v33", "v34", "v35", "v36", "v37", "v38", "v39", "v40", "v41", "v42", "v43", "v44", "v45", "v46", "v47", "v48", "v49", "v50", "v51", "v52", "v53", "v54", "v55", "v56", "v57", "v58", "v59", "v60", "v61", "v62", "v63", "v64", "v65", "v66", "v67", "v68", "v69", "v70", "v71", "v72", "v73", "v74", "v75", "v76", "v77", "v78", "v79", "v80", "v81", "v82", "v83", "v84", "v85", "v86", "v87", "v88", "v89", "v90", "v91", "v92", "v93", "v94", "v95", "v96", "v97", "v98", "v99", "v100", "v101", "v102", "v103", "v104", "v105", "v106", "v107", "v108", "v109", "v110", "v111", "v120", "v121", "v122", "v123", "s40", "s41"
#define LDY_ASM \
   "buffer_load_dword v26, v113, %[rw], 0 offen sc1\n\t" "s_mov_b32 s41, 0\n\t" "buffer_load_dwordx4 a[0:3], v16, %[ra], s41 offen\n\t" \
   "s_mov_b32 s41, 131072\n\t" "buffer_load_dwordx4 a[64:67], v16, %[ra], s41 offen\n\t" "s_mov_b32 s41, 524288\n\t" \
   "buffer_load_dwordx4 a[8:11], v16, %[ra], s41 offen\n\t" "s_mov_b32 s41, 655360\n\t" "buffer_load_dwordx4 a[72:75], v16, %[ra], s41 offen\n\t" \
   "s_mov_b32 s41, 1048576\n\t" "buffer_load_dwordx4 a[16:19], v16, %[ra], s41 offen\n\t" "s_mov_b32 s41, 1179648\n\t" \
   "buffer_load_dwordx4 a[80:83], v16, %[ra], s41 offen\n\t" "s_mov_b32 s41, 1572864\n\t" "buffer_load_dwordx4 a[24:27], v16, %[ra], s41 offen\n\t" \
   "s_mov_b32 s41, 1703936\n\t" "buffer_load_dwordx4 a[88:91], v16, %[ra], s41 offen\n\t" "s_mov_b32 s41, 2097152\n\t" \
   "buffer_load_dwordx4 a[32:35], v16, %[ra], s41 offen\n\t" "s_mov_b32 s41, 2228224\n\t" "buffer_load_dwordx4 a[96:99], v16, %[ra], s41 offen\n\t" \
   "s_mov_b32 s41, 2621440\n\t" "buffer_load_dwordx4 a[40:43], v16, %[ra], s41 offen\n\t" "s_mov_b32 s41, 2752512\n\t" \
   "buffer_load_dwordx4 a[104:107], v16, %[ra], s41 offen\n\t" "s_mov_b32 s41, 3145728\n\t" "buffer_load_dwordx4 a[48:51], v16, %[ra], s41 offen\n\t" \
   "s_mov_b32 s41, 3276800\n\t" "buffer_load_dwordx4 a[112:115], v16, %[ra], s41 offen\n\t" "s_mov_b32 s41, 3670016\n\t" \
   "buffer_load_dwordx4 a[56:59], v16, %[ra], s41 offen\n\t" "s_mov_b32 s41, 3801088\n\t" "buffer_load_dwordx4 a[120:123], v16, %[ra], s41 offen\n\t" \
   "s_waitcnt vmcnt(16)\n\t" "v_add_u32 v26, 1, v26\n\t" "buffer_store_dword v26, v114, %[rw], 0 offen sc1\n\t" "ds_write_b32 v115, v26\n\t" LDSTAMP(0) \
   "s_waitcnt vmcnt(15)\n\t" LDSTAMP(1) "ds_write_b128 v20, a[0:3] offset:0\n\t" "s_mov_b32 s41, 4194304\n\t" \
   "buffer_load_dwordx4 v[192:195], v16, %[ra], s41 offen\n\t" "s_waitcnt vmcnt(15)\n\t" "ds_write_b128 v20, a[64:67] offset:4096\n\t" \
   "s_mov_b32 s41, 4325376\n\t" "buffer_load_dwordx4 v[208:211], v16, %[ra], s41 offen\n\t" "s_waitcnt vmcnt(15)\n\t" \
   "ds_write_b128 v20, a[8:11] offset:8192\n\t" "s_mov_b32 s41, 4718592\n\t" "buffer_load_dwordx4 v[200:203], v16, %[ra], s41 offen\n\t" \
   "s_waitcnt vmcnt(15)\n\t" "ds_write_b128 v20, a[72:75] offset:12288\n\t" "s_mov_b32 s41, 4849664\n\t" \
   "buffer_load_dwordx4 v[216:219], v16, %[ra], s41 offen\n\t" "s_waitcnt vmcnt(15)\n\t" "ds_write_b128 v20, a[16:19] offset:16384\n\t" \
   "s_mov_b32 s41, 262144\n\t" "buffer_load_dwordx4 a[4:7], v16, %[ra], s41 offen\n\t" "s_waitcnt vmcnt(15)\n\t" \
   "ds_write_b128 v20, a[80:83] offset:20480\n\t" "s_mov_b32 s41, 393216\n\t" "buffer_load_dwordx4 a[68:71], v16, %[ra], s41 offen\n\t" \
   "s_waitcnt vmcnt(15)\n\t" "ds_write_b128 v20, a[24:27] offset:24576\n\t" "s_mov_b32 s41, 786432\n\t" \
   "buffer_load_dwordx4 a[12:15], v16, %[ra], s41 offen\n\t" "s_waitcnt vmcnt(15)\n\t" "ds_write_b128 v20, a[88:91] offset:28672\n\t" \
   "s_mov_b32 s41, 917504\n\t" "buffer_load_dwordx4 a[76:79], v16, %[ra], s41 offen\n\t" "s_waitcnt vmcnt(15)\n\t" \
   "ds_write_b128 v20, a[32:35] offset:32768\n\t" "s_mov_b32 s41, 1310720\n\t" "buffer_load_dwordx4 a[20:23], v16, %[ra], s41 offen\n\t" \
   "s_waitcnt vmcnt(15)\n\t" "ds_write_b128 v20, a[96:99] offset:36864\n\t" "s_mov_b32 s41, 1441792\n\t" \
   "buffer_load_dwordx4 a[84:87], v16, %[ra], s41 offen\n\t" "s_waitcnt vmcnt(15)\n\t" "ds_write_b128 v20, a[40:43] offset:40960\n\t" \
   "s_mov_b32 s41, 1835008\n\t" "buffer_load_dwordx4 a[28:31], v16, %[ra], s41 offen\n\t" "s_waitcnt vmcnt(15)\n\t" \
   "ds_write_b128 v20, a[104:107] offset:45056\n\t" "s_mov_b32 s41, 1966080\n\t" "buffer_load_dwordx4 a[92:95], v16, %[ra], s41 offen\n\t" \
   "s_waitcnt vmcnt(15)\n\t" "ds_write_b128 v20, a[48:51] offset:49152\n\t" "s_mov_b32 s41, 2359296\n\t" \
   "buffer_load_dwordx4 a[36:39], v16, %[ra], s41 offen\n\t" "s_waitcnt vmcnt(15)\n\t" "ds_write_b128 v20, a[112:115] offset:53248\n\t" \
   "s_mov_b32 s41, 2490368\n\t" "buffer_load_dwordx4 a[100:103], v16, %[ra], s41 offen\n\t" "s_waitcnt vmcnt(15)\n\t" \
   "ds_write_b128 v20, a[56:59] offset:57344\n\t" "s_mov_b32 s41, 2883584\n\t" "buffer_load_dwordx4 a[44:47], v16, %[ra], s41 offen\n\t" \
   "s_waitcnt vmcnt(15)\n\t" "ds_write_b128 v20, a[120:123] offset:61440\n\t" "s_mov_b32 s41, 3014656\n\t" \
   "buffer_load_dwordx4 a[108:111], v16, %[ra], s41 offen\n\t" "s_waitcnt vmcnt(15)\n\t" "ds_write_b128 v23, v[192:195] offset:0\n\t" \
   "s_mov_b32 s41, 3407872\n\t" "buffer_load_dwordx4 a[52:55], v16, %[ra], s41 offen\n\t" "s_waitcnt vmcnt(15)\n\t" \
   "ds_write_b128 v23, v[208:211] offset:4096\n\t" "s_mov_b32 s41, 3538944\n\t" "buffer_load_dwordx4 a[116:119], v16, %[ra], s41 offen\n\t" \
   "s_waitcnt vmcnt(15)\n\t" "ds_write_b128 v23, v[200:203] offset:8192\n\t" "s_mov_b32 s41, 3932160\n\t" \
   "buffer_load_dwordx4 a[60:63], v16, %[ra], s41 offen\n\t" "s_waitcnt vmcnt(15)\n\t" "ds_write_b128 v23, v[216:219] offset:12288\n\t" \
   "s_mov_b32 s41, 4063232\n\t" "buffer_load_dwordx4 a[124:127], v16, %[ra], s41 offen\n\t" "s_waitcnt lgkmcnt(0)\n\t" "s_barrier\n\t" LDSTAMP(2) \
   "s_lshl_b32 s40, %[p0], 13\n\t" "v_add_u32 v24, s40, v18\n\t" "v_add_u32 v25, s40, v19\n\t" "s_add_u32 s40, s40, 8192\n\t" \
   "s_cmp_eq_u32 s40, 81920\n\t" "s_cselect_b32 s40, 0, s40\n\t" "ds_read_b64_tr_b16 a[0:1], v24 offset:0\n\t" \
   "ds_read_b64_tr_b16 a[2:3], v24 offset:4096\n\t" "ds_read_b64_tr_b16 a[64:65], v25 offset:0\n\t" "ds_read_b64_tr_b16 a[66:67], v25 offset:4096\n\t" \
   "v_add_u32 v24, s40, v18\n\t" "v_add_u32 v25, s40, v19\n\t" "s_add_u32 s40, s40, 8192\n\t" "s_cmp_eq_u32 s40, 81920\n\t" \
   "s_cselect_b32 s40, 0, s40\n\t" "ds_read_b64_tr_b16 a[8:9], v24 offset:0\n\t" "ds_read_b64_tr_b16 a[10:11], v24 offset:4096\n\t" \
   "ds_read_b64_tr_b16 a[72:73], v25 offset:0\n\t" "ds_read_b64_tr_b16 a[74:75], v25 offset:4096\n\t" "v_add_u32 v24, s40, v18\n\t" \
   "v_add_u32 v25, s40, v19\n\t" "s_add_u32 s40, s40, 8192\n\t" "s_cmp_eq_u32 s40, 81920\n\t" "s_cselect_b32 s40, 0, s40\n\t" \
   "ds_read_b64_tr_b16 a[16:17], v24 offset:0\n\t" "ds_read_b64_tr_b16 a[18:19], v24 offset:4096\n\t" "ds_read_b64_tr_b16 a[80:81], v25 offset:0\n\t" \
   "ds_read_b64_tr_b16 a[82:83], v25 offset:4096\n\t" "v_add_u32 v24, s40, v18\n\t" "v_add_u32 v25, s40, v19\n\t" "s_add_u32 s40, s40, 8192\n\t" \
   "s_cmp_eq_u32 s40, 81920\n\t" "s_cselect_b32 s40, 0, s40\n\t" "ds_read_b64_tr_b16 a[24:25], v24 offset:0\n\t" \
   "ds_read_b64_tr_b16 a[26:27], v24 offset:4096\n\t" "ds_read_b64_tr_b16 a[88:89], v25 offset:0\n\t" \
   "ds_read_b64_tr_b16 a[90:91], v25 offset:4096\n\t" "v_add_u32 v24, s40, v18\n\t" "v_add_u32 v25, s40, v19\n\t" "s_add_u32 s40, s40, 8192\n\t" \
   "s_cmp_eq_u32 s40, 81920\n\t" "s_cselect_b32 s40, 0, s40\n\t" "ds_read_b64_tr_b16 a[32:33], v24 offset:0\n\t" \
   "ds_read_b64_tr_b16 a[34:35], v24 offset:4096\n\t" "ds_read_b64_tr_b16 a[96:97], v25 offset:0\n\t" \
   "ds_read_b64_tr_b16 a[98:99], v25 offset:4096\n\t" "v_add_u32 v24, s40, v18\n\t" "v_add_u32 v25, s40, v19\n\t" "s_add_u32 s40, s40, 8192\n\t" \
   "s_cmp_eq_u32 s40, 81920\n\t" "s_cselect_b32 s40, 0, s40\n\t" "ds_read_b64_tr_b16 a[40:41], v24 offset:0\n\t" \
   "ds_read_b64_tr_b16 a[42:43], v24 offset:4096\n\t" "ds_read_b64_tr_b16 a[104:105], v25 offset:0\n\t" \
   "ds_read_b64_tr_b16 a[106:107], v25 offset:4096\n\t" "v_add_u32 v24, s40, v18\n\t" "v_add_u32 v25, s40, v19\n\t" "s_add_u32 s40, s40, 8192\n\t" \
   "s_cmp_eq_u32 s40, 81920\n\t" "s_cselect_b32 s40, 0, s40\n\t" "ds_read_b64_tr_b16 a[48:49], v24 offset:0\n\t" \
   "ds_read_b64_tr_b16 a[50:51], v24 offset:4096\n\t" "ds_read_b64_tr_b16 a[112:113], v25 offset:0\n\t" \
   "ds_read_b64_tr_b16 a[114:115], v25 offset:4096\n\t" "v_add_u32 v24, s40, v18\n\t" "v_add_u32 v25, s40, v19\n\t" "s_add_u32 s40, s40, 8192\n\t" \
   "s_cmp_eq_u32 s40, 81920\n\t" "s_cselect_b32 s40, 0, s40\n\t" "ds_read_b64_tr_b16 a[56:57], v24 offset:0\n\t" \
   "ds_read_b64_tr_b16 a[58:59], v24 offset:4096\n\t" "ds_read_b64_tr_b16 a[120:121], v25 offset:0\n\t" \
   "ds_read_b64_tr_b16 a[122:123], v25 offset:4096\n\t" "v_add_u32 v24, s40, v18\n\t" "v_add_u32 v25, s40, v19\n\t" "s_add_u32 s40, s40, 8192\n\t" \
   "s_cmp_eq_u32 s40, 81920\n\t" "s_cselect_b32 s40, 0, s40\n\t" "ds_read_b64_tr_b16 v[192:193], v24 offset:0\n\t" \
   "ds_read_b64_tr_b16 v[194:195], v24 offset:4096\n\t" "ds_read_b64_tr_b16 v[208:209], v25 offset:0\n\t" \
   "ds_read_b64_tr_b16 v[210:211], v25 offset:4096\n\t" "v_add_u32 v24, s40, v18\n\t" "v_add_u32 v25, s40, v19\n\t" "s_add_u32 s40, s40, 8192\n\t" \
   "s_cmp_eq_u32 s40, 81920\n\t" "s_cselect_b32 s40, 0, s40\n\t" "ds_read_b64_tr_b16 v[200:201], v24 offset:0\n\t" \
   "ds_read_b64_tr_b16 v[202:203], v24 offset:4096\n\t" "ds_read_b64_tr_b16 v[216:217], v25 offset:0\n\t" \
   "ds_read_b64_tr_b16 v[218:219], v25 offset:4096\n\t" "s_waitcnt vmcnt(15)\n\t" "s_waitcnt lgkmcnt(0)\n\t" "s_barrier\n\t" \
   "ds_write_b128 v20, a[4:7] offset:0\n\t" "s_mov_b32 s41, 4456448\n\t" "buffer_load_dwordx4 v[196:199], v16, %[ra], s41 offen\n\t" \
   "s_waitcnt vmcnt(15)\n\t" "ds_write_b128 v20, a[68:71] offset:4096\n\t" "s_mov_b32 s41, 4587520\n\t" \
   "buffer_load_dwordx4 v[212:215], v16, %[ra], s41 offen\n\t" "s_waitcnt vmcnt(15)\n\t" "ds_write_b128 v20, a[12:15] offset:8192\n\t" \
   "s_mov_b32 s41, 4980736\n\t" "buffer_load_dwordx4 v[204:207], v16, %[ra], s41 offen\n\t" "s_waitcnt vmcnt(15)\n\t" \
   "ds_write_b128 v20, a[76:79] offset:12288\n\t" "s_mov_b32 s41, 5111808\n\t" "buffer_load_dwordx4 v[220:223], v16, %[ra], s41 offen\n\t" \
   "s_waitcnt vmcnt(15)\n\t" "ds_write_b128 v20, a[20:23] offset:16384\n\t" "s_mov_b32 s41, 0\n\t" \
   "buffer_load_dwordx4 a[128:131], v17, %[rb], s41 offen\n\t" "s_waitcnt vmcnt(15)\n\t" "ds_write_b128 v20, a[84:87] offset:20480\n\t" \
   "s_mov_b32 s41, 256\n\t" "buffer_load_dwordx4 a[132:135], v17, %[rb], s41 offen\n\t" "s_waitcnt vmcnt(15)\n\t" \
   "ds_write_b128 v20, a[28:31] offset:24576\n\t" "s_mov_b32 s41, 512\n\t" "buffer_load_dwordx4 a[160:163], v17, %[rb], s41 offen\n\t" \
   "s_waitcnt vmcnt(15)\n\t" "ds_write_b128 v20, a[92:95] offset:28672\n\t" "s_mov_b32 s41, 768\n\t" \
   "buffer_load_dwordx4 a[164:167], v17, %[rb], s41 offen\n\t" "s_waitcnt vmcnt(15)\n\t" "ds_write_b128 v20, a[36:39] offset:32768\n\t" \
   "s_mov_b32 s41, 1024\n\t" "buffer_load_dwordx4 a[192:195], v17, %[rb], s41 offen\n\t" "s_waitcnt vmcnt(15)\n\t" \
   "ds_write_b128 v20, a[100:103] offset:36864\n\t" "s_mov_b32 s41, 20480\n\t" "buffer_load_dwordx4 a[196:199], v17, %[rb], s41 offen\n\t" \
   "s_waitcnt vmcnt(15)\n\t" "ds_write_b128 v20, a[44:47] offset:40960\n\t" "s_mov_b32 s41, 20736\n\t" \
   "buffer_load_dwordx4 a[224:227], v17, %[rb], s41 offen\n\t" "s_waitcnt vmcnt(15)\n\t" "ds_write_b128 v20, a[108:111] offset:45056\n\t" \
   "s_mov_b32 s41, 20992\n\t" "buffer_load_dwordx4 a[228:231], v17, %[rb], s41 offen\n\t" "s_waitcnt vmcnt(15)\n\t" \
   "ds_write_b128 v20, a[52:55] offset:49152\n\t" "s_mov_b32 s41, 21248\n\t" "buffer_load_dwordx4 v[224:227], v17, %[rb], s41 offen\n\t" \
   "s_waitcnt vmcnt(15)\n\t" "ds_write_b128 v20, a[116:119] offset:53248\n\t" "s_mov_b32 s41, 21504\n\t" \
   "buffer_load_dwordx4 v[228:231], v17, %[rb], s41 offen\n\t" "s_waitcnt vmcnt(15)\n\t" "ds_write_b128 v20, a[60:63] offset:57344\n\t" \
   "s_mov_b32 s41, 40960\n\t" "buffer_load_dwordx4 a[136:139], v17, %[rb], s41 offen\n\t" "s_waitcnt vmcnt(15)\n\t" \
   "ds_write_b128 v20, a[124:127] offset:61440\n\t" "s_mov_b32 s41, 41216\n\t" "buffer_load_dwordx4 a[140:143], v17, %[rb], s41 offen\n\t" \
   "s_waitcnt vmcnt(15)\n\t" "ds_write_b128 v23, v[196:199] offset:0\n\t" "s_mov_b32 s41, 41472\n\t" \
   "buffer_load_dwordx4 a[168:171], v17, %[rb], s41 offen\n\t" "s_waitcnt vmcnt(15)\n\t" "ds_write_b128 v23, v[212:215] offset:4096\n\t" \
   "s_mov_b32 s41, 41728\n\t" "buffer_load_dwordx4 a[172:175], v17, %[rb], s41 offen\n\t" "s_waitcnt vmcnt(15)\n\t" \
   "ds_write_b128 v23, v[204:207] offset:8192\n\t" "s_mov_b32 s41, 41984\n\t" "buffer_load_dwordx4 a[200:203], v17, %[rb], s41 offen\n\t" \
   "s_waitcnt vmcnt(15)\n\t" "ds_write_b128 v23, v[220:223] offset:12288\n\t" "s_mov_b32 s41, 61440\n\t" \
   "buffer_load_dwordx4 a[204:207], v17, %[rb], s41 offen\n\t" "s_waitcnt lgkmcnt(0)\n\t" "s_barrier\n\t" LDSTAMP(3) "s_lshl_b32 s40, %[p0], 13\n\t" \
   "v_add_u32 v24, s40, v18\n\t" "v_add_u32 v25, s40, v19\n\t" "s_add_u32 s40, s40, 8192\n\t" "s_cmp_eq_u32 s40, 81920\n\t" \
   "s_cselect_b32 s40, 0, s40\n\t" "ds_read_b64_tr_b16 a[4:5], v24 offset:0\n\t" "ds_read_b64_tr_b16 a[6:7], v24 offset:4096\n\t" \
   "ds_read_b64_tr_b16 a[68:69], v25 offset:0\n\t" "ds_read_b64_tr_b16 a[70:71], v25 offset:4096\n\t" "v_add_u32 v24, s40, v18\n\t" \
   "v_add_u32 v25, s40, v19\n\t" "s_add_u32 s40, s40, 8192\n\t" "s_cmp_eq_u32 s40, 81920\n\t" "s_cselect_b32 s40, 0, s40\n\t" \
   "ds_read_b64_tr_b16 a[12:13], v24 offset:0\n\t" "ds_read_b64_tr_b16 a[14:15], v24 offset:4096\n\t" "ds_read_b64_tr_b16 a[76:77], v25 offset:0\n\t" \
   "ds_read_b64_tr_b16 a[78:79], v25 offset:4096\n\t" "v_add_u32 v24, s40, v18\n\t" "v_add_u32 v25, s40, v19\n\t" "s_add_u32 s40, s40, 8192\n\t" \
   "s_cmp_eq_u32 s40, 81920\n\t" "s_cselect_b32 s40, 0, s40\n\t" "ds_read_b64_tr_b16 a[20:21], v24 offset:0\n\t" \
   "ds_read_b64_tr_b16 a[22:23], v24 offset:4096\n\t" "ds_read_b64_tr_b16 a[84:85], v25 offset:0\n\t" \
   "ds_read_b64_tr_b16 a[86:87], v25 offset:4096\n\t" "v_add_u32 v24, s40, v18\n\t" "v_add_u32 v25, s40, v19\n\t" "s_add_u32 s40, s40, 8192\n\t" \
   "s_cmp_eq_u32 s40, 81920\n\t" "s_cselect_b32 s40, 0, s40\n\t" "ds_read_b64_tr_b16 a[28:29], v24 offset:0\n\t" \
   "ds_read_b64_tr_b16 a[30:31], v24 offset:4096\n\t" "ds_read_b64_tr_b16 a[92:93], v25 offset:0\n\t" \
   "ds_read_b64_tr_b16 a[94:95], v25 offset:4096\n\t" "v_add_u32 v24, s40, v18\n\t" "v_add_u32 v25, s40, v19\n\t" "s_add_u32 s40, s40, 8192\n\t" \
   "s_cmp_eq_u32 s40, 81920\n\t" "s_cselect_b32 s40, 0, s40\n\t" "ds_read_b64_tr_b16 a[36:37], v24 offset:0\n\t" \
   "ds_read_b64_tr_b16 a[38:39], v24 offset:4096\n\t" "ds_read_b64_tr_b16 a[100:101], v25 offset:0\n\t" \
   "ds_read_b64_tr_b16 a[102:103], v25 offset:4096\n\t" "v_add_u32 v24, s40, v18\n\t" "v_add_u32 v25, s40, v19\n\t" "s_add_u32 s40, s40, 8192\n\t" \
   "s_cmp_eq_u32 s40, 81920\n\t" "s_cselect_b32 s40, 0, s40\n\t" "ds_read_b64_tr_b16 a[44:45], v24 offset:0\n\t" \
   "ds_read_b64_tr_b16 a[46:47], v24 offset:4096\n\t" "ds_read_b64_tr_b16 a[108:109], v25 offset:0\n\t" \
   "ds_read_b64_tr_b16 a[110:111], v25 offset:4096\n\t" "v_add_u32 v24, s40, v18\n\t" "v_add_u32 v25, s40, v19\n\t" "s_add_u32 s40, s40, 8192\n\t" \
   "s_cmp_eq_u32 s40, 81920\n\t" "s_cselect_b32 s40, 0, s40\n\t" "ds_read_b64_tr_b16 a[52:53], v24 offset:0\n\t" \
   "ds_read_b64_tr_b16 a[54:55], v24 offset:4096\n\t" "ds_read_b64_tr_b16 a[116:117], v25 offset:0\n\t" \
   "ds_read_b64_tr_b16 a[118:119], v25 offset:4096\n\t" "v_add_u32 v24, s40, v18\n\t" "v_add_u32 v25, s40, v19\n\t" "s_add_u32 s40, s40, 8192\n\t" \
   "s_cmp_eq_u32 s40, 81920\n\t" "s_cselect_b32 s40, 0, s40\n\t" "ds_read_b64_tr_b16 a[60:61], v24 offset:0\n\t" \
   "ds_read_b64_tr_b16 a[62:63], v24 offset:4096\n\t" "ds_read_b64_tr_b16 a[124:125], v25 offset:0\n\t" \
   "ds_read_b64_tr_b16 a[126:127], v25 offset:4096\n\t" "v_add_u32 v24, s40, v18\n\t" "v_add_u32 v25, s40, v19\n\t" "s_add_u32 s40, s40, 8192\n\t" \
   "s_cmp_eq_u32 s40, 81920\n\t" "s_cselect_b32 s40, 0, s40\n\t" "ds_read_b64_tr_b16 v[196:197], v24 offset:0\n\t" \
   "ds_read_b64_tr_b16 v[198:199], v24 offset:4096\n\t" "ds_read_b64_tr_b16 v[212:213], v25 offset:0\n\t" \
   "ds_read_b64_tr_b16 v[214:215], v25 offset:4096\n\t" "v_add_u32 v24, s40, v18\n\t" "v_add_u32 v25, s40, v19\n\t" "s_add_u32 s40, s40, 8192\n\t" \
   "s_cmp_eq_u32 s40, 81920\n\t" "s_cselect_b32 s40, 0, s40\n\t" "ds_read_b64_tr_b16 v[204:205], v24 offset:0\n\t" \
   "ds_read_b64_tr_b16 v[206:207], v24 offset:4096\n\t" "ds_read_b64_tr_b16 v[220:221], v25 offset:0\n\t" \
   "ds_read_b64_tr_b16 v[222:223], v25 offset:4096\n\t" "s_waitcnt vmcnt(15)\n\t" "s_waitcnt lgkmcnt(0)\n\t" "s_barrier\n\t" \
   "ds_write_b32 v21, a128 offset:0\n\t" "ds_write_b32 v21, a129 offset:21120\n\t" "ds_write_b32 v21, a130 offset:10560\n\t" \
   "ds_write_b32 v21, a131 offset:31680\n\t" "s_mov_b32 s41, 61696\n\t" "buffer_load_dwordx4 a[232:235], v17, %[rb], s41 offen\n\t" \
   "s_waitcnt vmcnt(15)\n\t" "ds_write_b32 v21, a132 offset:64\n\t" "ds_write_b32 v21, a133 offset:21184\n\t" "ds_write_b32 v21, a134 offset:10624\n\t" \
   "ds_write_b32 v21, a135 offset:31744\n\t" "s_mov_b32 s41, 61952\n\t" "buffer_load_dwordx4 a[236:239], v17, %[rb], s41 offen\n\t" \
   "s_waitcnt vmcnt(15)\n\t" "ds_write_b32 v21, a160 offset:128\n\t" "ds_write_b32 v21, a161 offset:21248\n\t" \
   "ds_write_b32 v21, a162 offset:10688\n\t" "ds_write_b32 v21, a163 offset:31808\n\t" "s_mov_b32 s41, 62208\n\t" \
   "buffer_load_dwordx4 v[232:235], v17, %[rb], s41 offen\n\t" "s_waitcnt vmcnt(15)\n\t" "ds_write_b32 v21, a164 offset:192\n\t" \
   "ds_write_b32 v21, a165 offset:21312\n\t" "ds_write_b32 v21, a166 offset:10752\n\t" "ds_write_b32 v21, a167 offset:31872\n\t" \
   "s_mov_b32 s41, 62464\n\t" "buffer_load_dwordx4 v[236:239], v17, %[rb], s41 offen\n\t" "s_waitcnt vmcnt(15)\n\t" \
   "ds_write_b32 v21, a192 offset:256\n\t" "ds_write_b32 v21, a193 offset:21376\n\t" "ds_write_b32 v21, a194 offset:10816\n\t" \
   "ds_write_b32 v21, a195 offset:31936\n\t" "s_mov_b32 s41, 81920\n\t" "buffer_load_dwordx4 a[144:147], v17, %[rb], s41 offen\n\t" \
   "s_waitcnt vmcnt(15)\n\t" "ds_write_b32 v21, a196 offset:5248\n\t" "ds_write_b32 v21, a197 offset:26368\n\t" \
   "ds_write_b32 v21, a198 offset:15808\n\t" "ds_write_b32 v21, a199 offset:36928\n\t" "s_mov_b32 s41, 82176\n\t" \
   "buffer_load_dwordx4 a[148:151], v17, %[rb], s41 offen\n\t" "s_waitcnt vmcnt(15)\n\t" "ds_write_b32 v21, a224 offset:5312\n\t" \
   "ds_write_b32 v21, a225 offset:26432\n\t" "ds_write_b32 v21, a226 offset:15872\n\t" "ds_write_b32 v21, a227 offset:36992\n\t" \
   "s_mov_b32 s41, 82432\n\t" "buffer_load_dwordx4 a[176:179], v17, %[rb], s41 offen\n\t" "s_waitcnt vmcnt(15)\n\t" \
   "ds_write_b32 v21, a228 offset:5376\n\t" "ds_write_b32 v21, a229 offset:26496\n\t" "ds_write_b32 v21, a230 offset:15936\n\t" \
   "ds_write_b32 v21, a231 offset:37056\n\t" "s_mov_b32 s41, 82688\n\t" "buffer_load_dwordx4 a[180:183], v17, %[rb], s41 offen\n\t" \
   "s_waitcnt vmcnt(15)\n\t" "ds_write_b32 v21, v224 offset:5440\n\t" "ds_write_b32 v21, v225 offset:26560\n\t" \
   "ds_write_b32 v21, v226 offset:16000\n\t" "ds_write_b32 v21, v227 offset:37120\n\t" "s_mov_b32 s41, 82944\n\t" \
   "buffer_load_dwordx4 a[208:211], v17, %[rb], s41 offen\n\t" "s_waitcnt vmcnt(15)\n\t" "ds_write_b32 v21, v228 offset:5504\n\t" \
   "ds_write_b32 v21, v229 offset:26624\n\t" "ds_write_b32 v21, v230 offset:16064\n\t" "ds_write_b32 v21, v231 offset:37184\n\t" \
   "s_mov_b32 s41, 102400\n\t" "buffer_load_dwordx4 a[212:215], v17, %[rb], s41 offen\n\t" "s_waitcnt lgkmcnt(0)\n\t" "s_barrier\n\t" LDSTAMP(4) \
   "ds_read_b64_tr_b16 a[128:129], v22 offset:0\n\t" "ds_read_b64_tr_b16 a[130:131], v22 offset:656\n\t" \
   "ds_read_b64_tr_b16 a[132:133], v22 offset:2624\n\t" "ds_read_b64_tr_b16 a[134:135], v22 offset:3280\n\t" \
   "ds_read_b64_tr_b16 a[160:161], v22 offset:16\n\t" "ds_read_b64_tr_b16 a[162:163], v22 offset:672\n\t" \
   "ds_read_b64_tr_b16 a[164:165], v22 offset:2640\n\t" "ds_read_b64_tr_b16 a[166:167], v22 offset:3296\n\t" \
   "ds_read_b64_tr_b16 a[192:193], v22 offset:32\n\t" "ds_read_b64_tr_b16 a[194:195], v22 offset:688\n\t" \
   "ds_read_b64_tr_b16 a[196:197], v22 offset:2656\n\t" "ds_read_b64_tr_b16 a[198:199], v22 offset:3312\n\t" \
   "ds_read_b64_tr_b16 a[224:225], v22 offset:48\n\t" "ds_read_b64_tr_b16 a[226:227], v22 offset:704\n\t" \
   "ds_read_b64_tr_b16 a[228:229], v22 offset:2672\n\t" "ds_read_b64_tr_b16 a[230:231], v22 offset:3328\n\t" \
   "ds_read_b64_tr_b16 v[224:225], v22 offset:64\n\t" "ds_read_b64_tr_b16 v[226:227], v22 offset:720\n\t" \
   "ds_read_b64_tr_b16 v[228:229], v22 offset:2688\n\t" "ds_read_b64_tr_b16 v[230:231], v22 offset:3344\n\t" "s_waitcnt vmcnt(15)\n\t" \
   "s_waitcnt lgkmcnt(0)\n\t" "s_barrier\n\t" "ds_write_b32 v21, a136 offset:0\n\t" "ds_write_b32 v21, a137 offset:21120\n\t" \
   "ds_write_b32 v21, a138 offset:10560\n\t" "ds_write_b32 v21, a139 offset:31680\n\t" "s_mov_b32 s41, 102656\n\t" \
   "buffer_load_dwordx4 a[240:243], v17, %[rb], s41 offen\n\t" "s_waitcnt vmcnt(15)\n\t" "ds_write_b32 v21, a140 offset:64\n\t" \
   "ds_write_b32 v21, a141 offset:21184\n\t" "ds_write_b32 v21, a142 offset:10624\n\t" "ds_write_b32 v21, a143 offset:31744\n\t" \
   "s_mov_b32 s41, 102912\n\t" "buffer_load_dwordx4 a[244:247], v17, %[rb], s41 offen\n\t" "s_waitcnt vmcnt(15)\n\t" \
   "ds_write_b32 v21, a168 offset:128\n\t" "ds_write_b32 v21, a169 offset:21248\n\t" "ds_write_b32 v21, a170 offset:10688\n\t" \
   "ds_write_b32 v21, a171 offset:31808\n\t" "s_mov_b32 s41, 103168\n\t" "buffer_load_dwordx4 v[240:243], v17, %[rb], s41 offen\n\t" \
   "s_waitcnt vmcnt(15)\n\t" "ds_write_b32 v21, a172 offset:192\n\t" "ds_write_b32 v21, a173 offset:21312\n\t" \
   "ds_write_b32 v21, a174 offset:10752\n\t" "ds_write_b32 v21, a175 offset:31872\n\t" "s_mov_b32 s41, 103424\n\t" \
   "buffer_load_dwordx4 v[244:247], v17, %[rb], s41 offen\n\t" "s_waitcnt vmcnt(15)\n\t" "ds_write_b32 v21, a200 offset:256\n\t" \
   "ds_write_b32 v21, a201 offset:21376\n\t" "ds_write_b32 v21, a202 offset:10816\n\t" "ds_write_b32 v21, a203 offset:31936\n\t" \
   "s_mov_b32 s41, 122880\n\t" "buffer_load_dwordx4 a[152:155], v17, %[rb], s41 offen\n\t" "s_waitcnt vmcnt(15)\n\t" \
   "ds_write_b32 v21, a204 offset:5248\n\t" "ds_write_b32 v21, a205 offset:26368\n\t" "ds_write_b32 v21, a206 offset:15808\n\t" \
   "ds_write_b32 v21, a207 offset:36928\n\t" "s_mov_b32 s41, 123136\n\t" "buffer_load_dwordx4 a[156:159], v17, %[rb], s41 offen\n\t" \
   "s_waitcnt vmcnt(15)\n\t" "ds_write_b32 v21, a232 offset:5312\n\t" "ds_write_b32 v21, a233 offset:26432\n\t" \
   "ds_write_b32 v21, a234 offset:15872\n\t" "ds_write_b32 v21, a235 offset:36992\n\t" "s_mov_b32 s41, 123392\n\t" \
   "buffer_load_dwordx4 a[184:187], v17, %[rb], s41 offen\n\t" "s_waitcnt vmcnt(15)\n\t" "ds_write_b32 v21, a236 offset:5376\n\t" \
   "ds_write_b32 v21, a237 offset:26496\n\t" "ds_write_b32 v21, a238 offset:15936\n\t" "ds_write_b32 v21, a239 offset:37056\n\t" \
   "s_mov_b32 s41, 123648\n\t" "buffer_load_dwordx4 a[188:191], v17, %[rb], s41 offen\n\t" "s_waitcnt vmcnt(15)\n\t" \
   "ds_write_b32 v21, v232 offset:5440\n\t" "ds_write_b32 v21, v233 offset:26560\n\t" "ds_write_b32 v21, v234 offset:16000\n\t" \
   "ds_write_b32 v21, v235 offset:37120\n\t" "s_mov_b32 s41, 123904\n\t" "buffer_load_dwordx4 a[216:219], v17, %[rb], s41 offen\n\t" \
   "s_waitcnt vmcnt(15)\n\t" "ds_write_b32 v21, v236 offset:5504\n\t" "ds_write_b32 v21, v237 offset:26624\n\t" \
   "ds_write_b32 v21, v238 offset:16064\n\t" "ds_write_b32 v21, v239 offset:37184\n\t" "s_mov_b32 s41, 143360\n\t" \
   "buffer_load_dwordx4 a[220:223], v17, %[rb], s41 offen\n\t" "s_waitcnt lgkmcnt(0)\n\t" "s_barrier\n\t" LDSTAMP(5) \
   "ds_read_b64_tr_b16 a[136:137], v22 offset:0\n\t" "ds_read_b64_tr_b16 a[138:139], v22 offset:656\n\t" \
   "ds_read_b64_tr_b16 a[140:141], v22 offset:2624\n\t" "ds_read_b64_tr_b16 a[142:143], v22 offset:3280\n\t" \
   "ds_read_b64_tr_b16 a[168:169], v22 offset:16\n\t" "ds_read_b64_tr_b16 a[170:171], v22 offset:672\n\t" \
   "ds_read_b64_tr_b16 a[172:173], v22 offset:2640\n\t" "ds_read_b64_tr_b16 a[174:175], v22 offset:3296\n\t" \
   "ds_read_b64_tr_b16 a[200:201], v22 offset:32\n\t" "ds_read_b64_tr_b16 a[202:203], v22 offset:688\n\t" \
   "ds_read_b64_tr_b16 a[204:205], v22 offset:2656\n\t" "ds_read_b64_tr_b16 a[206:207], v22 offset:3312\n\t" \
   "ds_read_b64_tr_b16 a[232:233], v22 offset:48\n\t" "ds_read_b64_tr_b16 a[234:235], v22 offset:704\n\t" \
   "ds_read_b64_tr_b16 a[236:237], v22 offset:2672\n\t" "ds_read_b64_tr_b16 a[238:239], v22 offset:3328\n\t" \
   "ds_read_b64_tr_b16 v[232:233], v22 offset:64\n\t" "ds_read_b64_tr_b16 v[234:235], v22 offset:720\n\t" \
   "ds_read_b64_tr_b16 v[236:237], v22 offset:2688\n\t" "ds_read_b64_tr_b16 v[238:239], v22 offset:3344\n\t" "s_waitcnt vmcnt(15)\n\t" \
   "s_waitcnt lgkmcnt(0)\n\t" "s_barrier\n\t" "ds_write_b32 v21, a144 offset:0\n\t" "ds_write_b32 v21, a145 offset:21120\n\t" \
   "ds_write_b32 v21, a146 offset:10560\n\t" "ds_write_b32 v21, a147 offset:31680\n\t" "s_mov_b32 s41, 143616\n\t" \
   "buffer_load_dwordx4 a[248:251], v17, %[rb], s41 offen\n\t" "s_waitcnt vmcnt(15)\n\t" "ds_write_b32 v21, a148 offset:64\n\t" \
   "ds_write_b32 v21, a149 offset:21184\n\t" "ds_write_b32 v21, a150 offset:10624\n\t" "ds_write_b32 v21, a151 offset:31744\n\t" \
   "s_mov_b32 s41, 143872\n\t" "buffer_load_dwordx4 a[252:255], v17, %[rb], s41 offen\n\t" "s_waitcnt vmcnt(15)\n\t" \
   "ds_write_b32 v21, a176 offset:128\n\t" "ds_write_b32 v21, a177 offset:21248\n\t" "ds_write_b32 v21, a178 offset:10688\n\t" \
   "ds_write_b32 v21, a179 offset:31808\n\t" "s_mov_b32 s41, 144128\n\t" "buffer_load_dwordx4 v[248:251], v17, %[rb], s41 offen\n\t" \
   "s_waitcnt vmcnt(15)\n\t" "ds_write_b32 v21, a180 offset:192\n\t" "ds_write_b32 v21, a181 offset:21312\n\t" \
   "ds_write_b32 v21, a182 offset:10752\n\t" "ds_write_b32 v21, a183 offset:31872\n\t" "s_mov_b32 s41, 144384\n\t" \
   "buffer_load_dwordx4 v[252:255], v17, %[rb], s41 offen\n\t" "s_waitcnt vmcnt(15)\n\t" "ds_write_b32 v21, a208 offset:256\n\t" \
   "ds_write_b32 v21, a209 offset:21376\n\t" "ds_write_b32 v21, a210 offset:10816\n\t" "ds_write_b32 v21, a211 offset:31936\n\t" \
   "s_waitcnt vmcnt(14)\n\t" "ds_write_b32 v21, a212 offset:5248\n\t" "ds_write_b32 v21, a213 offset:26368\n\t" \
   "ds_write_b32 v21, a214 offset:15808\n\t" "ds_write_b32 v21, a215 offset:36928\n\t" "s_waitcnt vmcnt(13)\n\t" \
   "ds_write_b32 v21, a240 offset:5312\n\t" "ds_write_b32 v21, a241 offset:26432\n\t" "ds_write_b32 v21, a242 offset:15872\n\t" \
   "ds_write_b32 v21, a243 offset:36992\n\t" "s_waitcnt vmcnt(12)\n\t" "ds_write_b32 v21, a244 offset:5376\n\t" \
   "ds_write_b32 v21, a245 offset:26496\n\t" "ds_write_b32 v21, a246 offset:15936\n\t" "ds_write_b32 v21, a247 offset:37056\n\t" \
   "s_waitcnt vmcnt(11)\n\t" "ds_write_b32 v21, v240 offset:5440\n\t" "ds_write_b32 v21, v241 offset:26560\n\t" \
   "ds_write_b32 v21, v242 offset:16000\n\t" "ds_write_b32 v21, v243 offset:37120\n\t" "s_waitcnt vmcnt(10)\n\t" \
   "ds_write_b32 v21, v244 offset:5504\n\t" "ds_write_b32 v21, v245 offset:26624\n\t" "ds_write_b32 v21, v246 offset:16064\n\t" \
   "ds_write_b32 v21, v247 offset:37184\n\t" "buffer_load_dwordx4 v[28:31], v112, %[rw], 0 offen sc1\n\t" "s_waitcnt lgkmcnt(0)\n\t" "s_barrier\n\t" \
   LDSTAMP(6) "ds_read_b64_tr_b16 a[144:145], v22 offset:0\n\t" "ds_read_b64_tr_b16 a[146:147], v22 offset:656\n\t" \
   "ds_read_b64_tr_b16 a[148:149], v22 offset:2624\n\t" "ds_read_b64_tr_b16 a[150:151], v22 offset:3280\n\t" \
   "ds_read_b64_tr_b16 a[176:177], v22 offset:16\n\t" "ds_read_b64_tr_b16 a[178:179], v22 offset:672\n\t" \
   "ds_read_b64_tr_b16 a[180:181], v22 offset:2640\n\t" "ds_read_b64_tr_b16 a[182:183], v22 offset:3296\n\t" \
   "ds_read_b64_tr_b16 a[208:209], v22 offset:32\n\t" "ds_read_b64_tr_b16 a[210:211], v22 offset:688\n\t" \
   "ds_read_b64_tr_b16 a[212:213], v22 offset:2656\n\t" "ds_read_b64_tr_b16 a[214:215], v22 offset:3312\n\t" \
   "ds_read_b64_tr_b16 a[240:241], v22 offset:48\n\t" "ds_read_b64_tr_b16 a[242:243], v22 offset:704\n\t" \
   "ds_read_b64_tr_b16 a[244:245], v22 offset:2672\n\t" "ds_read_b64_tr_b16 a[246:247], v22 offset:3328\n\t" \
   "ds_read_b64_tr_b16 v[240:241], v22 offset:64\n\t" "ds_read_b64_tr_b16 v[242:243], v22 offset:720\n\t" \
   "ds_read_b64_tr_b16 v[244:245], v22 offset:2688\n\t" "ds_read_b64_tr_b16 v[246:247], v22 offset:3344\n\t" "s_waitcnt vmcnt(10)\n\t" \
   "s_waitcnt lgkmcnt(0)\n\t" "s_barrier\n\t" "ds_write_b32 v21, a152 offset:0\n\t" "ds_write_b32 v21, a153 offset:21120\n\t" \
   "ds_write_b32 v21, a154 offset:10560\n\t" "ds_write_b32 v21, a155 offset:31680\n\t" "s_waitcnt vmcnt(9)\n\t" "ds_write_b32 v21, a156 offset:64\n\t" \
   "ds_write_b32 v21, a157 offset:21184\n\t" "ds_write_b32 v21, a158 offset:10624\n\t" "ds_write_b32 v21, a159 offset:31744\n\t" \
   "s_waitcnt vmcnt(8)\n\t" "ds_write_b32 v21, a184 offset:128\n\t" "ds_write_b32 v21, a185 offset:21248\n\t" "ds_write_b32 v21, a186 offset:10688\n\t" \
   "ds_write_b32 v21, a187 offset:31808\n\t" "s_waitcnt vmcnt(7)\n\t" "ds_write_b32 v21, a188 offset:192\n\t" "ds_write_b32 v21, a189 offset:21312\n\t" \
   "ds_write_b32 v21, a190 offset:10752\n\t" "ds_write_b32 v21, a191 offset:31872\n\t" "s_waitcnt vmcnt(6)\n\t" "ds_write_b32 v21, a216 offset:256\n\t" \
   "ds_write_b32 v21, a217 offset:21376\n\t" "ds_write_b32 v21, a218 offset:10816\n\t" "ds_write_b32 v21, a219 offset:31936\n\t" \
   "s_waitcnt vmcnt(5)\n\t" "ds_write_b32 v21, a220 offset:5248\n\t" "ds_write_b32 v21, a221 offset:26368\n\t" \
   "ds_write_b32 v21, a222 offset:15808\n\t" "ds_write_b32 v21, a223 offset:36928\n\t" "s_waitcnt vmcnt(4)\n\t" \
   "ds_write_b32 v21, a248 offset:5312\n\t" "ds_write_b32 v21, a249 offset:26432\n\t" "ds_write_b32 v21, a250 offset:15872\n\t" \
   "ds_write_b32 v21, a251 offset:36992\n\t" "s_waitcnt vmcnt(3)\n\t" "ds_write_b32 v21, a252 offset:5376\n\t" \
   "ds_write_b32 v21, a253 offset:26496\n\t" "ds_write_b32 v21, a254 offset:15936\n\t" "ds_write_b32 v21, a255 offset:37056\n\t" \
   "s_waitcnt vmcnt(2)\n\t" "ds_write_b32 v21, v248 offset:5440\n\t" "ds_write_b32 v21, v249 offset:26560\n\t" \
   "ds_write_b32 v21, v250 offset:16000\n\t" "ds_write_b32 v21, v251 offset:37120\n\t" "s_waitcnt vmcnt(1)\n\t" \
   "ds_write_b32 v21, v252 offset:5504\n\t" "ds_write_b32 v21, v253 offset:26624\n\t" "ds_write_b32 v21, v254 offset:16064\n\t" \
   "ds_write_b32 v21, v255 offset:37184\n\t" "s_waitcnt lgkmcnt(0)\n\t" "s_barrier\n\t" LDSTAMP(7) "ds_read_b64_tr_b16 a[152:153], v22 offset:0\n\t" \
   "ds_read_b64_tr_b16 a[154:155], v22 offset:656\n\t" "ds_read_b64_tr_b16 a[156:157], v22 offset:2624\n\t" \
   "ds_read_b64_tr_b16 a[158:159], v22 offset:3280\n\t" "ds_read_b64_tr_b16 a[184:185], v22 offset:16\n\t" \
   "ds_read_b64_tr_b16 a[186:187], v22 offset:672\n\t" "ds_read_b64_tr_b16 a[188:189], v22 offset:2640\n\t" \
   "ds_read_b64_tr_b16 a[190:191], v22 offset:3296\n\t" "ds_read_b64_tr_b16 a[216:217], v22 offset:32\n\t" \
   "ds_read_b64_tr_b16 a[218:219], v22 offset:688\n\t" "ds_read_b64_tr_b16 a[220:221], v22 offset:2656\n\t" \
   "ds_read_b64_tr_b16 a[222:223], v22 offset:3312\n\t" "ds_read_b64_tr_b16 a[248:249], v22 offset:48\n\t" \
   "ds_read_b64_tr_b16 a[250:251], v22 offset:704\n\t" "ds_read_b64_tr_b16 a[252:253], v22 offset:2672\n\t" \
   "ds_read_b64_tr_b16 a[254:255], v22 offset:3328\n\t" "ds_read_b64_tr_b16 v[248:249], v22 offset:64\n\t" \
   "ds_read_b64_tr_b16 v[250:251], v22 offset:720\n\t" "ds_read_b64_tr_b16 v[252:253], v22 offset:2688\n\t" \
   "ds_read_b64_tr_b16 v[254:255], v22 offset:3344\n\t" "s_waitcnt vmcnt(0) lgkmcnt(0)\n\t" "s_barrier"
#define LDY_CLOB "v24", "v25", "v26", "v32", "v33", "v34", "v35", "v36", "v37", "v38", "v39", "v40", "v41", "v42", "v43", "v44", "v45", "v46", "v47", "v48", "v49", "v50", "v51", "v52", "v53", "v54", "v55", "v56", "v57", "v58", "v59", "v60", "v61", "v62", "v63", "v64", "v65", "v66", "v67", "v68", "v69", "v70", "v71", "v72", "v73", "v74", "v75", "v76", "v77", "v78", "v79", "v80", "v81", "v82", "v83", "v84", "v85", "v86", "v87", "v88", "v89", "v90", "v91", "v92", "v93", "v94", "v95", "v96", "v97", "v98", "v99", "v100", "v101", "v102", "v103", "v104", "v105", "v106", "v107", "v108", "v109", "v110", "v111", "v120", "v121", "v122", "v123", "s40", "s41"
#define W_OPS "{a[0:31]}"(w[0]), "{a[32:63]}"(w[1]), "{a[64:95]}"(w[2]), "{a[96:127]}"(w[3]), "{a[128:159]}"(w[4]), "{a[160:191]}"(w[5]), \
              "{a[192:223]}"(w[6]), "{a[224:255]}"(w[7]), "{v[192:223]}"(w[8]), "{v[224:255]}"(w[9])

__device__ __forceinline__ int mem_lane(int lane) { return lane * 16; }
__device__ __forceinline__ int pair_of(int g) { return (g & 1) * 2 + (g >> 1); }

__device__ __forceinline__ void rec_p1(int wv_, const v32u (&w)[10], unsigned char* ws, const Pool& pool, int layer, int h, int xs, RecTune tn, unsigned long long* stamps) {
  const Clk0 c0 = clk_sample();
  const unsigned lds0 = (unsigned)(uintptr_t)(__attribute__((address_space(3))) unsigned char*)dyn_lds;
  const int lane = lane_id(), wv = wv_, g = lane >> 4, r = lane & 15;
  const v4u rs = make_rs(ws, (unsigned)OFF_END);
  const int p0 = wv < 3 ? 3 * wv : 8;
  v16u ad, la = (unsigned)lane;
  const int kqo = (int)(wv < 2 ? OFF_KF : OFF_QF) + ((h * RT + (wv & 1)) * 4 * 64 + lane) * 16;
  const int progo = (wv == 0 && lane == 0) ? prog_off(layer, 0, h) : OOB;
  {
    const int p2 = wv < 3 ? p0 + 2 : 0;
    ad[0] = W_MEM + (p0 >> 1) * WIN + (p0 & 1) * 1024 + mem_lane(lane);
    ad[1] = W_MEM + ((p0 + 1) >> 1) * WIN + ((p0 + 1) & 1) * 1024 + mem_lane(lane);
    ad[2] = W_MEM + (p2 >> 1) * WIN + (p2 & 1) * 1024 + mem_lane(lane);
    ad[3] = lane == 0 ? W_MISC + h * 64 + wv * 4 : OOB;
    ad[4] = copy_off(wv) + (h >> 2) * WIN + (h & 3) * 512 + (g >> 1) * 256 + (2 * (r & 7) + (r >> 3)) * 16 + (g & 1) * 8;
    ad[5] = g < 2 ? copy_off(wv + 4 * (r >> 3)) + (8 + (h >> 4)) * WIN + ((h >> 3) & 1) * 1024 + (((h >> 1) & 3) * 16 + (h & 1) * 8 + (r & 7)) * 16 + g * 8 : OOB;
    ad[6] = (r & 1) ? 0xEEEEu : 0x4444u;
    ad[7] = lds0 + (32 * wv + r) * 16 + g * 4;
    ad[8] = lds0 + (4 * g + (r >> 2)) * 16 + (r & 1) * 8;
    ad[9] = lds0 + L_M2 + wv * 3072 + lane * 16;
    ad[10] = kqo;
    ad[11] = M_TM + xs * 128 + (lane & 1) * 12;
    ad[12] = kqf_off(layer, 0, h) + (lane & 3) * 4;
    ad[13] = wv > 1 ? (int)OFF_PXF + ((h * 2 + (wv & 1)) * 64 + lane) * 16 : OOB;
    ad[14] = (g < 2 && wv > 1) ? (int)OFF_PXM + (((h >> 2) * 2 + (wv & 1)) * 64 + (h & 3) * 16 + r) * 16 + g * 8 : OOB;
    ad[15] = progo;
#pragma unroll
    for (int i = 0; i < 10; ++i) { int p = p0 + i; p = p >= 10 ? p - 10 : p; la[i] = lds0 + p * 1024 + lane * 16; }
    la[10] = lds0 + L_KQ + lane * 16;
    la[12] = lds0 + REC_ST + wv * 8;
    asm volatile("v_mov_b32 %0, 0xceabfb8f" : "=v"(la[13]));
    la[14] = 16 * (wv & 1) + r - 4 * g;
    la[15] = lds0 + L_KQ + (wv & 1) * 4096 + lane * 16;
  }
  __builtin_amdgcn_s_waitcnt(0x0F70);
  if (layer) __builtin_amdgcn_s_sleep(64);
  v4u clk = clk_init(c0);
  unsigned long long stat;
  asm volatile(P1_ASM
               : "=&{s[76:77]}"(stat), "+{s[56:59]}"(clk)
               : W_OPS, "{v[128:143]}"(ad), "{v[112:127]}"(la), [rs] "s"(rs), [sel] "s"(pool.sel), [pool] "s"(pool.base), [xs] "s"(xs),
                 [lad0] "s"(ladder(xs, xs & 1)), [s3] "s"(pool.s3), [m1] "s"(tn.m1), [l2s] "s"(lds0 + L_M2 + wv * 3072),
                 [hi8] "s"(0xFF00FF00FF00FF00ull), [wv] "s"(wv), [kf0] "s"((int)OFF_KF + h * RT * 4096),
                 [esc] "s"(pool.base + (NSEG - 1) * REG)
               : P1_CLOB RST_CLOB, "vcc", "scc", "m0", "memory");
  LDSBAR();
  {
    const unsigned one = 1u;
    const int so = (NSEG - 1) * 4096;
    VM_ST32(one, progo, so);
    VM_WAIT(0);
  }
}

__device__ __forceinline__ void rec_pv(int wv_, const v32u (&w)[10], unsigned char* ws, const Pool& pool, int layer, int h, int xs, RecTune tn, unsigned long long* stamps) {
  const Clk0 c0 = clk_sample();
  const unsigned lds0 = (unsigned)(uintptr_t)(__attribute__((address_space(3))) unsigned char*)dyn_lds;
  const int lane = lane_id(), wv = wv_, g = lane >> 4, r = lane & 15;
  const v4u rs = make_rs(ws, (unsigned)OFF_END);
  const int p0 = wv < 3 ? 3 * wv : 8, p2 = wv < 3 ? p0 + 2 : 0;
  v16u ad = (unsigned)lane, la = (unsigned)lane;
  ad[0] = W_MEM + (p0 >> 1) * WIN + (p0 & 1) * 1024 + mem_lane(lane);
  ad[1] = W_MEM + ((p0 + 1) >> 1) * WIN + ((p0 + 1) & 1) * 1024 + mem_lane(lane);
  ad[2] = W_MEM + (p2 >> 1) * WIN + (p2 & 1) * 1024 + mem_lane(lane);
  ad[3] = (r & 8) ? 0xEEEEu : 0x4444u;
  ad[4] = (wv * 10 + (g >> 1)) * 16384 + h * 256 + r * 16 + (g & 1) * 8;
  ad[5] = h * 16384 + wv * 2048 + lane * 16;
  ad[6] = (r & 1) ? 0xEEEEu : 0x4444u;
  ad[7] = lds0 + (32 * wv + r) * 16 + pair_of(g) * 4;
  ad[8] = lds0 + (4 * g + (r >> 2)) * 16 + (r & 1) * 8;
  ad[11] = M_TM + (4 + xs) * 128 + (lane & 1) * 12;
  ad[15] = (wv == 0 && lane == 0) ? prog_off(layer, 0, 32 + h) : OOB;
#pragma unroll
  for (int i = 0; i < 10; ++i) { int p = p0 + i; p = p >= 10 ? p - 10 : p; la[i] = lds0 + p * 1024 + lane * 16; }
  la[12] = lds0 + REC_ST + wv * 8;
  __builtin_amdgcn_s_waitcnt(0x0F70);
  if (layer) __builtin_amdgcn_s_sleep(64);
  v4u clk = clk_init(c0);
  unsigned long long stat;
  asm volatile(PV_ASM
               : "=&{s[76:77]}"(stat), "+{s[56:59]}"(clk)
               : W_OPS, "{v[128:143]}"(ad), "{v[112:127]}"(la), [rs] "s"(rs), [sel] "s"(pool.sel), [pool] "s"(pool.base), [xs] "s"(xs),
                 [lad0] "s"(ladder(xs, xs & 1)), [s3] "s"(pool.s3), [m1c] "s"(tn.m1 + tn.c2), [esc] "s"(pool.base + (NSEG - 1) * REG),
                 [ysel] "s"(pool.ysel), [yb] "s"(pool.base + P_Y)
               : PV_CLOB RST_CLOB, "vcc", "scc", "memory");
}

#define TSTART 5000
#define TD1 900u
#define TR2 400u
#define TEMA 1
#define TEMB 2
#define TBUP 48
#define TBDN 6
#define TLATE 520
#define TSEC 50
__device__ __forceinline__ unsigned track_dur(unsigned e, int m, int rob, int s) {
  const int lo = (int)e - (s < 8 ? 64 : (rob & 255)), hi = (int)e + (rob >> 8);
  if (s >= 8) m = (int)e + ((m - (int)e + (m > (int)e ? (1 << TEMA) - 1 : 0)) >> (m > (int)e ? TEMA : TEMB));
  return (unsigned)min(max(m, lo), hi);
}
#define TTS 200
#define TTU 240
__device__ __forceinline__ void rec_sched(unsigned char* ws, const Pool& pool, int layer, int c16, int xs, RecTune tn, unsigned long long* stamps) {
  const Clk0 c0 = clk_sample();
  const unsigned lds0 = (unsigned)(uintptr_t)(__attribute__((address_space(3))) unsigned char*)dyn_lds;
  const int lane = lane_id();
  const v4u rs = make_rs(ws, (unsigned)OFF_END);
  unsigned long long stat = 0;
  const int sm = M_STAMP + min(lane, 39) * 64, sp = W_MISC + (lane & 31) * 64;
  const int yo = M_TM + xs * 128, oc = (c16 == 0 && lane < 16) ? M_TM + lane * 128 : OOB;
  const unsigned cv = flag_finish<2>(rs, kqf_off(layer, 0, 32 + (lane & 31)), 0xFFFFFFFFu);
  v4u clk = clk_init(c0);
  PH(3 + layer * 6)
  unsigned r2a = TR2, d1a = TD1, dd = TD1 + tn.m2, tp = 0, tm = 0, anchor = clk.x - dd;
  unsigned rr = (unsigned)min(max(wave_imax((int)(cv * 8u - clk.x)) + TSTART, 300), 4000);
  int bonus = 0;
  bool clean = false;
#pragma unroll 1
  for (int s = 0; s < NSEG - 1; ++s) {
    const int rgm = pool.region((s + NSEG - 1) & (NSEG - 1)), rg = pool.region(s);
    const unsigned slot = lds0 + L_T + (s & 1) * 32, ao = anchor;
    unsigned b2 = 1u;
    v4u b1 = {1u, 1u, 1u, 1u};
    if (s > 0) {
      asm volatile(WAIT_ASM : "+{s[56:59]}"(clk) : [ta] "s"(tm + TTS) : WAIT_CLOB, "scc", "memory");
      VM_LD32(b2, sm, rgm);
      VM_LD128(b1, sp, rgm, 0);
      asm volatile("s_waitcnt vmcnt(0)" : "+v"(b2), "+v"(b1)::"memory");
      anchor = tm;
      if (__builtin_expect(__any(b2 == 0xFFFFFFFFu || (int)(b2 - (tm + tn.m1 - tn.vis)) > 0), 0)) {
        STAT(3);
        for (unsigned spins = 0; __any(b2 == 0xFFFFFFFFu) && spins < SPIN_CAP; ++spins) {
          __builtin_amdgcn_s_sleep(2);
          VM_LD32(b2, sm, rgm);
          VM_WAIT1(0, b2);
        }
        const int d = wave_imax((int)(b2 - tm)) + tn.vis - tn.m1;
        anchor = tm + (d <= 0 ? 0 : (d <= tn.dlt ? tn.dlt + TSEC : d + TLATE));
      }
    }
    const unsigned tpo = tp, tmo = tm;
    tp = anchor + dd;
    tm = nosent(tp + rr);
    LDS_V4(slot) = v4u{tp, tm, (unsigned)(s + 1), anchor};
    if (anchor != tmo && s > 2) bonus = TBUP; else bonus = max(bonus - TBDN, 0);
    dd = d1a + tn.m2 + bonus;
    rr = r2a + bonus;
    {
      const v4u wq = v4u{tm, dd, rr, dd + rr};
      VM_ST128(wq, oc, rg);
    }
    if (s > 1 && !__any((b2 & 1u) != 0u)) r2a = track_dur(r2a, wave_imax((int)(b2 - tpo)), tn.rob, s);
    if (s > 1 && clean && !__any(((b1.x | b1.y | b1.z | b1.w) & 1u) != 0u))
      d1a = track_dur(d1a, wave_imax(max(max((int)(b1.x - ao), (int)(b1.y - ao)), max((int)(b1.z - ao), (int)(b1.w - ao)))), tn.rob, s);
    clean = anchor == tmo;
    while ((int)(tp + 40u - rtc() * 8u) > 0 && (int)(tp - rtc() * 8u) < 32000) __builtin_amdgcn_s_sleep(1);
    v4u lw;
    VM_LD128(lw, yo, rg, 0);
    VM_WAIT1(0, lw);
    const unsigned l0 = (unsigned)__builtin_amdgcn_readfirstlane(lw.x), l1 = (unsigned)__builtin_amdgcn_readfirstlane(lw.y),
                   l2 = (unsigned)__builtin_amdgcn_readfirstlane(lw.z);
    if (max(max(l0, l1), l2) != 0xFFFFFFFFu) { tm = l0; dd = l1; rr = l2; }
  }
}

__device__ __forceinline__ void rec_fin(unsigned char* ws, const Pool& pool, int layer, int c16, int xs, RecTune tn, unsigned long long* stamps) {
  const Clk0 c0 = clk_sample();
  const unsigned lds0 = (unsigned)(uintptr_t)(__attribute__((address_space(3))) unsigned char*)dyn_lds;
  const int lane = lane_id(), g = lane >> 4, r = lane & 15;
  const v4u rs = make_rs(ws, (unsigned)OFF_END);
  v8u ad;
  ad[0] = lane * 16;
  ad[1] = g * 256 + r * 8;
  ad[2] = (r & 1) ? 0x03020706u : 0x05040100u;
  ad[3] = (lane & 31) * 64;
  ad[4] = W_MEM + copy_off(((r >> 1) & 1) * 5) + (c16 >> 3) * WIN + (c16 & 7) * 256 + (2 * (2 * g + (r & 1)) + (r >> 3)) * 16 + ((r >> 2) & 1) * 8;
  ad[5] = lane == 0 ? M_STAMP + c16 * 64 : OOB;
  ad[6] = (r & 1) ? 0xEEEEu : 0x4444u;
  ad[7] = lds0 + L_T;
  __builtin_amdgcn_s_sleep(64);
  v4u clk = clk_init(c0);
  unsigned long long stat;
  asm volatile(FIN_ASM
               : "=&{s[76:77]}"(stat), "+{s[56:59]}"(clk), "+{v[16:23]}"(ad)
               : [rs] "s"(rs), [sel] "s"(pool.sel), [pool] "s"(pool.base), [xs] "s"(xs), [lad0] "s"(ladder(xs, xs & 1)), [s3] "s"(pool.s3),
                 [su] "s"((int)OFF_UF + c16 * 32768), [yb] "s"(pool.base + P_Y + c16 * 16384), [ysel] "s"(pool.ysel), [tu] "s"(TTU),
                 [stl] "s"(lds0 + REC_ST - 32), [uf] "s"(kqf_off(layer, 0, 32))
               : FIN_CLOB RST_CLOB, "vcc", "scc", "memory");
}

constexpr int PJ_BUF = 43 * 1024, PJ_X = 16384, PJ_LDS = PJ_X + 3 * PJ_BUF;
#define KQ_ASM \
   "s_mov_b32 s44, %[rdy]\n\t" "s_mov_b32 s45, 0\n\t" "s_mov_b32 s46, 0\n\t" "s_cmp_eq_u32 s44, -1\n\t" "s_cbranch_scc1 68f\n\t" \
   "s_mul_i32 s52, s45, 1536\n\t" "s_mul_i32 s51, s45, 40960\n\t" "s_add_u32 m0, s46, %[ldx]\n\t" "s_mov_b32 s53, s46\n\t" \
   "buffer_load_dwordx4 v16, %[rs], s51 offen offset:0 sc1 lds\n\t" "buffer_load_dwordx4 v16, %[rs], s51 offen offset:1024 sc1 lds\n\t" \
   "buffer_load_dwordx4 v16, %[rs], s51 offen offset:2048 sc1 lds\n\t" "buffer_load_dwordx4 v16, %[rs], s51 offen offset:3072 sc1 lds\n\t" \
   "s_add_u32 m0, m0, 4096\n\t" "s_add_u32 s51, s51, 4096\n\t" "buffer_load_dwordx4 v16, %[rs], s51 offen offset:0 sc1 lds\n\t" \
   "buffer_load_dwordx4 v16, %[rs], s51 offen offset:1024 sc1 lds\n\t" "buffer_load_dwordx4 v16, %[rs], s51 offen offset:2048 sc1 lds\n\t" \
   "buffer_load_dwordx4 v16, %[rs], s51 offen offset:3072 sc1 lds\n\t" "s_add_u32 m0, m0, 4096\n\t" "s_add_u32 s51, s51, 4096\n\t" \
   "buffer_load_dwordx4 v16, %[rs], s51 offen offset:0 sc1 lds\n\t" "buffer_load_dwordx4 v16, %[rs], s51 offen offset:1024 sc1 lds\n\t" \
   "s_add_u32 m0, s53, %[ld1]\n\t" "s_nop 0\n\t" "buffer_load_dwordx4 v17, %[rs], s52 offen sc1 lds\n\t" "s_mov_b32 s45, 1\n\t" \
   "s_mov_b32 s46, 44032\n\t" "s_mul_i32 s52, s45, 1536\n\t" "s_mul_i32 s51, s45, 40960\n\t" "s_add_u32 m0, s46, %[ldx]\n\t" "s_mov_b32 s53, s46\n\t" \
   "buffer_load_dwordx4 v16, %[rs], s51 offen offset:0 sc1 lds\n\t" "buffer_load_dwordx4 v16, %[rs], s51 offen offset:1024 sc1 lds\n\t" \
   "buffer_load_dwordx4 v16, %[rs], s51 offen offset:2048 sc1 lds\n\t" "buffer_load_dwordx4 v16, %[rs], s51 offen offset:3072 sc1 lds\n\t" \
   "s_add_u32 m0, m0, 4096\n\t" "s_add_u32 s51, s51, 4096\n\t" "buffer_load_dwordx4 v16, %[rs], s51 offen offset:0 sc1 lds\n\t" \
   "buffer_load_dwordx4 v16, %[rs], s51 offen offset:1024 sc1 lds\n\t" "buffer_load_dwordx4 v16, %[rs], s51 offen offset:2048 sc1 lds\n\t" \
   "buffer_load_dwordx4 v16, %[rs], s51 offen offset:3072 sc1 lds\n\t" "s_add_u32 m0, m0, 4096\n\t" "s_add_u32 s51, s51, 4096\n\t" \
   "buffer_load_dwordx4 v16, %[rs], s51 offen offset:0 sc1 lds\n\t" "buffer_load_dwordx4 v16, %[rs], s51 offen offset:1024 sc1 lds\n\t" \
   "s_add_u32 m0, s53, %[ld1]\n\t" "s_nop 0\n\t" "buffer_load_dwordx4 v17, %[rs], s52 offen sc1 lds\n\t" "68:\n\t" "v_mov_b32 v160, 0x3f803f80\n\t" \
   "v_mov_b32 v161, 0x3f803f80\n\t" "v_mov_b32 v162, 0x3f803f80\n\t" "v_mov_b32 v163, 0x3f803f80\n\t" "v_mov_b32 v31, 1\n\t" "v_mov_b32 v138, 0\n\t" \
   "v_mov_b32 v13, %[flw]\n\t" "v_and_b32 v12, -16, v13\n\t" "ds_write_b32 v13, v138\n\t" \
   "v_mfma_f32_32x32x16_bf16 v[32:47], a[0:3], v[160:163], 0\n\t" "v_mfma_f32_32x32x16_bf16 v[48:63], a[8:11], v[160:163], 0\n\t" \
   "v_mfma_f32_32x32x16_bf16 v[32:47], a[4:7], v[160:163], v[32:47]\n\t" "v_mfma_f32_32x32x16_bf16 v[48:63], a[12:15], v[160:163], v[48:63]\n\t" \
   "v_mfma_f32_32x32x16_bf16 v[32:47], a[16:19], v[160:163], v[32:47]\n\t" "v_mfma_f32_32x32x16_bf16 v[48:63], a[24:27], v[160:163], v[48:63]\n\t" \
   "v_mfma_f32_32x32x16_bf16 v[32:47], a[20:23], v[160:163], v[32:47]\n\t" "v_mfma_f32_32x32x16_bf16 v[48:63], a[28:31], v[160:163], v[48:63]\n\t" \
   "v_mfma_f32_32x32x16_bf16 v[32:47], a[32:35], v[160:163], v[32:47]\n\t" "v_mfma_f32_32x32x16_bf16 v[48:63], a[40:43], v[160:163], v[48:63]\n\t" \
   "v_mfma_f32_32x32x16_bf16 v[32:47], a[36:39], v[160:163], v[32:47]\n\t" "v_mfma_f32_32x32x16_bf16 v[48:63], a[44:47], v[160:163], v[48:63]\n\t" \
   "v_mfma_f32_32x32x16_bf16 v[32:47], a[48:51], v[160:163], v[32:47]\n\t" "v_mfma_f32_32x32x16_bf16 v[48:63], a[56:59], v[160:163], v[48:63]\n\t" \
   "v_mfma_f32_32x32x16_bf16 v[32:47], a[52:55], v[160:163], v[32:47]\n\t" "v_mfma_f32_32x32x16_bf16 v[48:63], a[60:63], v[160:163], v[48:63]\n\t" \
   "v_mfma_f32_32x32x16_bf16 v[32:47], a[64:67], v[160:163], v[32:47]\n\t" "v_mfma_f32_32x32x16_bf16 v[48:63], a[72:75], v[160:163], v[48:63]\n\t" \
   "v_mfma_f32_32x32x16_bf16 v[32:47], a[68:71], v[160:163], v[32:47]\n\t" "v_mfma_f32_32x32x16_bf16 v[48:63], a[76:79], v[160:163], v[48:63]\n\t" \
   "v_mfma_f32_32x32x16_bf16 v[32:47], a[80:83], v[160:163], v[32:47]\n\t" "v_mfma_f32_32x32x16_bf16 v[48:63], a[88:91], v[160:163], v[48:63]\n\t" \
   "v_mfma_f32_32x32x16_bf16 v[32:47], a[84:87], v[160:163], v[32:47]\n\t" "v_mfma_f32_32x32x16_bf16 v[48:63], a[92:95], v[160:163], v[48:63]\n\t" \
   "v_mfma_f32_32x32x16_bf16 v[32:47], a[96:99], v[160:163], v[32:47]\n\t" "v_mfma_f32_32x32x16_bf16 v[48:63], a[104:107], v[160:163], v[48:63]\n\t" \
   "v_mfma_f32_32x32x16_bf16 v[32:47], a[100:103], v[160:163], v[32:47]\n\t" "v_mfma_f32_32x32x16_bf16 v[48:63], a[108:111], v[160:163], v[48:63]\n\t" \
   "v_mfma_f32_32x32x16_bf16 v[32:47], a[112:115], v[160:163], v[32:47]\n\t" "v_mfma_f32_32x32x16_bf16 v[48:63], a[120:123], v[160:163], v[48:63]\n\t" \
   "v_mfma_f32_32x32x16_bf16 v[32:47], a[116:119], v[160:163], v[32:47]\n\t" "v_mfma_f32_32x32x16_bf16 v[48:63], a[124:127], v[160:163], v[48:63]\n\t" \
   "v_mfma_f32_32x32x16_bf16 v[32:47], a[128:131], v[160:163], v[32:47]\n\t" "v_mfma_f32_32x32x16_bf16 v[48:63], a[136:139], v[160:163], v[48:63]\n\t" \
   "v_mfma_f32_32x32x16_bf16 v[32:47], a[132:135], v[160:163], v[32:47]\n\t" "v_mfma_f32_32x32x16_bf16 v[48:63], a[140:143], v[160:163], v[48:63]\n\t" \
   "v_mfma_f32_32x32x16_bf16 v[32:47], a[144:147], v[160:163], v[32:47]\n\t" "v_mfma_f32_32x32x16_bf16 v[48:63], a[152:155], v[160:163], v[48:63]\n\t" \
   "v_mfma_f32_32x32x16_bf16 v[32:47], a[148:151], v[160:163], v[32:47]\n\t" "v_mfma_f32_32x32x16_bf16 v[48:63], a[156:159], v[160:163], v[48:63]\n\t" \
   "v_mfma_f32_32x32x16_bf16 v[32:47], a[160:163], v[160:163], v[32:47]\n\t" "v_mfma_f32_32x32x16_bf16 v[48:63], a[168:171], v[160:163], v[48:63]\n\t" \
   "v_mfma_f32_32x32x16_bf16 v[32:47], a[164:167], v[160:163], v[32:47]\n\t" "v_mfma_f32_32x32x16_bf16 v[48:63], a[172:175], v[160:163], v[48:63]\n\t" \
   "v_mfma_f32_32x32x16_bf16 v[32:47], a[176:179], v[160:163], v[32:47]\n\t" "v_mfma_f32_32x32x16_bf16 v[48:63], a[184:187], v[160:163], v[48:63]\n\t" \
   "v_mfma_f32_32x32x16_bf16 v[32:47], a[180:183], v[160:163], v[32:47]\n\t" "v_mfma_f32_32x32x16_bf16 v[48:63], a[188:191], v[160:163], v[48:63]\n\t" \
   "v_mfma_f32_32x32x16_bf16 v[32:47], a[192:195], v[160:163], v[32:47]\n\t" "v_mfma_f32_32x32x16_bf16 v[48:63], a[200:203], v[160:163], v[48:63]\n\t" \
   "v_mfma_f32_32x32x16_bf16 v[32:47], a[196:199], v[160:163], v[32:47]\n\t" "v_mfma_f32_32x32x16_bf16 v[48:63], a[204:207], v[160:163], v[48:63]\n\t" \
   "v_mfma_f32_32x32x16_bf16 v[32:47], a[208:211], v[160:163], v[32:47]\n\t" "v_mfma_f32_32x32x16_bf16 v[48:63], a[216:219], v[160:163], v[48:63]\n\t" \
   "v_mfma_f32_32x32x16_bf16 v[32:47], a[212:215], v[160:163], v[32:47]\n\t" "v_mfma_f32_32x32x16_bf16 v[48:63], a[220:223], v[160:163], v[48:63]\n\t" \
   "v_mfma_f32_32x32x16_bf16 v[32:47], a[224:227], v[160:163], v[32:47]\n\t" "v_mfma_f32_32x32x16_bf16 v[48:63], a[232:235], v[160:163], v[48:63]\n\t" \
   "v_mfma_f32_32x32x16_bf16 v[32:47], a[228:231], v[160:163], v[32:47]\n\t" "v_mfma_f32_32x32x16_bf16 v[48:63], a[236:239], v[160:163], v[48:63]\n\t" \
   "v_mfma_f32_32x32x16_bf16 v[32:47], a[240:243], v[160:163], v[32:47]\n\t" "v_mfma_f32_32x32x16_bf16 v[48:63], a[248:251], v[160:163], v[48:63]\n\t" \
   "v_mfma_f32_32x32x16_bf16 v[32:47], a[244:247], v[160:163], v[32:47]\n\t" "v_mfma_f32_32x32x16_bf16 v[48:63], a[252:255], v[160:163], v[48:63]\n\t" \
   "v_mfma_f32_32x32x16_bf16 v[32:47], v[192:195], v[160:163], v[32:47]\n\t" "v_mfma_f32_32x32x16_bf16 v[48:63], v[200:203], v[160:163], v[48:63]\n\t" \
   "v_mfma_f32_32x32x16_bf16 v[32:47], v[196:199], v[160:163], v[32:47]\n\t" "v_mfma_f32_32x32x16_bf16 v[48:63], v[204:207], v[160:163], v[48:63]\n\t" \
   "v_mfma_f32_32x32x16_bf16 v[32:47], v[208:211], v[160:163], v[32:47]\n\t" "v_mfma_f32_32x32x16_bf16 v[48:63], v[216:219], v[160:163], v[48:63]\n\t" \
   "v_mfma_f32_32x32x16_bf16 v[32:47], v[212:215], v[160:163], v[32:47]\n\t" "v_mfma_f32_32x32x16_bf16 v[48:63], v[220:223], v[160:163], v[48:63]\n\t" \
   "v_mfma_f32_32x32x16_bf16 v[32:47], v[224:227], v[160:163], v[32:47]\n\t" "v_mfma_f32_32x32x16_bf16 v[48:63], v[232:235], v[160:163], v[48:63]\n\t" \
   "v_mfma_f32_32x32x16_bf16 v[32:47], v[228:231], v[160:163], v[32:47]\n\t" "v_mfma_f32_32x32x16_bf16 v[48:63], v[236:239], v[160:163], v[48:63]\n\t" \
   "v_mfma_f32_32x32x16_bf16 v[32:47], v[240:243], v[160:163], v[32:47]\n\t" "v_mfma_f32_32x32x16_bf16 v[48:63], v[248:251], v[160:163], v[48:63]\n\t" \
   "v_mfma_f32_32x32x16_bf16 v[32:47], v[244:247], v[160:163], v[32:47]\n\t" "v_mfma_f32_32x32x16_bf16 v[48:63], v[252:255], v[160:163], v[48:63]\n\t" \
   "s_mov_b32 s40, 0\n\t" "s_mov_b32 s41, 0\n\t" "s_mov_b32 s42, 0\n\t" "s_mov_b32 s43, 44032\n\t" "v_mov_b32 v28, v22\n\t" "v_mov_b32 v29, v23\n\t" \
   "s_mov_b32 s45, 2\n\t" "s_mov_b32 s46, 88064\n\t" "s_waitcnt vmcnt(11) lgkmcnt(0)\n\t" "s_barrier\n\t" "s_lshr_b32 s48, s45, 1\n\t" \
   "s_bitcmp1_b32 s44, s48\n\t" "s_cbranch_scc0 172f\n\t" "72:\n\t" "ds_read_b128 v[160:163], v28 offset:0\n\t" \
   "ds_read_b128 v[164:167], v28 offset:512\n\t" "ds_read_b128 v[168:171], v28 offset:1024\n\t" "ds_read_b128 v[172:175], v28 offset:1536\n\t" "1:\n\t" \
   "v_mov_b32 v26, v28\n\t" "v_mov_b32 v27, v29\n\t" "s_waitcnt lgkmcnt(2)\n\t" "v_mfma_f32_32x32x16_bf16 v[64:79], a[0:3], v[160:163], 0\n\t" \
   "ds_read_b128 v[176:179], v26 offset:2048\n\t" "v_mfma_f32_32x32x16_bf16 v[80:95], a[8:11], v[160:163], 0\n\t" "v_mul_f32 v11, 0x3e0293ee, v10\n\t" \
   "s_lshl_b32 s47, s41, 13\n\t" "s_sub_i32 s47, s47, 8192\n\t" "s_max_i32 s47, s47, 0\n\t" "s_cmp_eq_u32 s40, 0\n\t" \
   "s_cselect_b32 s48, 0x7ffffff0, 0\n\t" "v_mfma_f32_32x32x16_bf16 v[64:79], a[4:7], v[164:167], v[64:79]\n\t" \
   "ds_read_b128 v[180:183], v26 offset:2560\n\t" "v_or_b32 v30, s48, v21\n\t" "v_fmac_f32 v96, v32, v8\n\t" "v_fmac_f32 v97, v33, v8\n\t" \
   "v_fmac_f32 v98, v34, v8\n\t" "v_mfma_f32_32x32x16_bf16 v[80:95], a[12:15], v[164:167], v[80:95]\n\t" "v_fmac_f32 v99, v35, v8\n\t" \
   "v_fmac_f32 v100, v36, v8\n\t" "v_fmac_f32 v101, v37, v8\n\t" "v_fmac_f32 v102, v38, v8\n\t" "v_fmac_f32 v103, v39, v8\n\t" \
   "s_waitcnt lgkmcnt(2)\n\t" "v_mfma_f32_32x32x16_bf16 v[64:79], a[16:19], v[168:171], v[64:79]\n\t" "ds_read_b128 v[184:187], v26 offset:3072\n\t" \
   "v_fmac_f32 v104, v40, v8\n\t" "v_fmac_f32 v105, v41, v8\n\t" "v_fmac_f32 v106, v42, v8\n\t" "v_fmac_f32 v107, v43, v8\n\t" \
   "v_mfma_f32_32x32x16_bf16 v[80:95], a[24:27], v[168:171], v[80:95]\n\t" "v_fmac_f32 v108, v44, v8\n\t" "v_fmac_f32 v109, v45, v8\n\t" \
   "v_fmac_f32 v110, v46, v8\n\t" "v_fmac_f32 v111, v47, v8\n\t" "v_mul_f32 v96, v96, v11\n\t" "v_mul_f32 v97, v97, v11\n\t" \
   "v_mfma_f32_32x32x16_bf16 v[64:79], a[20:23], v[172:175], v[64:79]\n\t" "ds_read_b128 v[188:191], v26 offset:3584\n\t" "v_mul_f32 v98, v98, v11\n\t" \
   "v_mul_f32 v99, v99, v11\n\t" "v_mul_f32 v100, v100, v11\n\t" "v_mul_f32 v101, v101, v11\n\t" \
   "v_mfma_f32_32x32x16_bf16 v[80:95], a[28:31], v[172:175], v[80:95]\n\t" "v_mul_f32 v102, v102, v11\n\t" "v_mul_f32 v103, v103, v11\n\t" \
   "v_mul_f32 v104, v104, v11\n\t" "v_mul_f32 v105, v105, v11\n\t" "v_mul_f32 v106, v106, v11\n\t" "s_waitcnt lgkmcnt(2)\n\t" \
   "v_mfma_f32_32x32x16_bf16 v[64:79], a[32:35], v[176:179], v[64:79]\n\t" "ds_read_b128 v[160:163], v26 offset:4096\n\t" \
   "v_mul_f32 v107, v107, v11\n\t" "v_mul_f32 v108, v108, v11\n\t" "v_mul_f32 v109, v109, v11\n\t" "v_mul_f32 v110, v110, v11\n\t" \
   "v_mfma_f32_32x32x16_bf16 v[80:95], a[40:43], v[176:179], v[80:95]\n\t" "v_mul_f32 v111, v111, v11\n\t" "s_add_u32 s48, s47, %[qf]\n\t" \
   "v_cvt_pk_bf16_f32 v96, v96, v97\n\t" "v_cvt_pk_bf16_f32 v97, v98, v99\n\t" "v_cvt_pk_bf16_f32 v98, v104, v105\n\t" \
   "v_cvt_pk_bf16_f32 v99, v106, v107\n\t" "v_mfma_f32_32x32x16_bf16 v[64:79], a[36:39], v[180:183], v[64:79]\n\t" \
   "ds_read_b128 v[164:167], v26 offset:4608\n\t" "v_mfma_f32_32x32x16_bf16 v[80:95], a[44:47], v[180:183], v[80:95]\n\t" \
   "buffer_store_dwordx4 v[96:99], v30, %[rs], s48 offen offset:0 sc1\n\t" "s_waitcnt lgkmcnt(2)\n\t" \
   "v_mfma_f32_32x32x16_bf16 v[64:79], a[48:51], v[184:187], v[64:79]\n\t" "ds_read_b128 v[168:171], v26 offset:5120\n\t" \
   "v_cvt_pk_bf16_f32 v100, v100, v101\n\t" "v_cvt_pk_bf16_f32 v101, v102, v103\n\t" "v_cvt_pk_bf16_f32 v102, v108, v109\n\t" \
   "v_cvt_pk_bf16_f32 v103, v110, v111\n\t" "v_mfma_f32_32x32x16_bf16 v[80:95], a[56:59], v[184:187], v[80:95]\n\t" \
   "buffer_store_dwordx4 v[100:103], v30, %[rs], s48 offen offset:512 sc1\n\t" "v_fmac_f32 v112, v48, v8\n\t" \
   "v_mfma_f32_32x32x16_bf16 v[64:79], a[52:55], v[188:191], v[64:79]\n\t" "ds_read_b128 v[172:175], v26 offset:5632\n\t" \
   "v_fmac_f32 v113, v49, v8\n\t" "v_fmac_f32 v114, v50, v8\n\t" "v_fmac_f32 v115, v51, v8\n\t" "v_fmac_f32 v116, v52, v8\n\t" \
   "v_mfma_f32_32x32x16_bf16 v[80:95], a[60:63], v[188:191], v[80:95]\n\t" "v_fmac_f32 v117, v53, v8\n\t" "v_fmac_f32 v118, v54, v8\n\t" \
   "v_fmac_f32 v119, v55, v8\n\t" "v_fmac_f32 v120, v56, v8\n\t" "v_fmac_f32 v121, v57, v8\n\t" "s_waitcnt lgkmcnt(2)\n\t" \
   "v_mfma_f32_32x32x16_bf16 v[64:79], a[64:67], v[160:163], v[64:79]\n\t" "ds_read_b128 v[176:179], v26 offset:6144\n\t" \
   "v_fmac_f32 v122, v58, v8\n\t" "v_fmac_f32 v123, v59, v8\n\t" "v_fmac_f32 v124, v60, v8\n\t" "v_fmac_f32 v125, v61, v8\n\t" \
   "v_mfma_f32_32x32x16_bf16 v[80:95], a[72:75], v[160:163], v[80:95]\n\t" "v_fmac_f32 v126, v62, v8\n\t" "v_fmac_f32 v127, v63, v8\n\t" \
   "v_mul_f32 v112, v112, v10\n\t" "v_mul_f32 v113, v113, v10\n\t" "v_mul_f32 v114, v114, v10\n\t" "v_mul_f32 v115, v115, v10\n\t" \
   "v_mfma_f32_32x32x16_bf16 v[64:79], a[68:71], v[164:167], v[64:79]\n\t" "ds_read_b128 v[180:183], v26 offset:6656\n\t" \
   "v_mul_f32 v116, v116, v10\n\t" "v_mul_f32 v117, v117, v10\n\t" "v_mul_f32 v118, v118, v10\n\t" "v_mul_f32 v119, v119, v10\n\t" \
   "v_mfma_f32_32x32x16_bf16 v[80:95], a[76:79], v[164:167], v[80:95]\n\t" "v_mul_f32 v120, v120, v10\n\t" "v_mul_f32 v121, v121, v10\n\t" \
   "v_mul_f32 v122, v122, v10\n\t" "v_mul_f32 v123, v123, v10\n\t" "v_mul_f32 v124, v124, v10\n\t" "s_waitcnt lgkmcnt(2)\n\t" \
   "v_mfma_f32_32x32x16_bf16 v[64:79], a[80:83], v[168:171], v[64:79]\n\t" "ds_read_b128 v[184:187], v26 offset:7168\n\t" \
   "v_mul_f32 v125, v125, v10\n\t" "v_mul_f32 v126, v126, v10\n\t" "v_mul_f32 v127, v127, v10\n\t" "s_add_u32 s48, s47, %[kf]\n\t" \
   "v_mfma_f32_32x32x16_bf16 v[80:95], a[88:91], v[168:171], v[80:95]\n\t" "v_cvt_pk_bf16_f32 v112, v112, v113\n\t" \
   "v_cvt_pk_bf16_f32 v113, v114, v115\n\t" "v_cvt_pk_bf16_f32 v114, v120, v121\n\t" "v_cvt_pk_bf16_f32 v115, v122, v123\n\t" \
   "ds_read_b128 v[128:131], v27 offset:0\n\t" "v_mfma_f32_32x32x16_bf16 v[64:79], a[84:87], v[172:175], v[64:79]\n\t" \
   "ds_read_b128 v[188:191], v26 offset:7680\n\t" "s_add_u32 s54, s41, 1\n\t" "s_lshr_b64 s[60:61], %[sel], s54\n\t" "s_lshl_b32 s55, s54, 19\n\t" \
   "s_and_b32 s60, s60, 1\n\t" "v_mfma_f32_32x32x16_bf16 v[80:95], a[92:95], v[172:175], v[80:95]\n\t" \
   "buffer_store_dwordx4 v[112:115], v30, %[rs], s48 offen offset:0 sc1\n\t" "s_waitcnt lgkmcnt(3)\n\t" \
   "v_mfma_f32_32x32x16_bf16 v[64:79], a[96:99], v[176:179], v[64:79]\n\t" "ds_read_b128 v[160:163], v26 offset:8192\n\t" \
   "v_cvt_pk_bf16_f32 v116, v116, v117\n\t" "v_cvt_pk_bf16_f32 v117, v118, v119\n\t" "v_cvt_pk_bf16_f32 v118, v124, v125\n\t" \
   "v_cvt_pk_bf16_f32 v119, v126, v127\n\t" "v_mfma_f32_32x32x16_bf16 v[80:95], a[104:107], v[176:179], v[80:95]\n\t" \
   "buffer_store_dwordx4 v[116:119], v30, %[rs], s48 offen offset:512 sc1\n\t" "s_add_u32 s55, s55, %[pool]\n\t" \
   "v_mfma_f32_32x32x16_bf16 v[64:79], a[100:103], v[180:183], v[64:79]\n\t" "ds_read_b128 v[164:167], v26 offset:8704\n\t" \
   "s_lshl_b32 s60, s60, 13\n\t" "s_add_u32 s55, s55, s60\n\t" "ds_read_b128 v[132:135], v27 offset:16\n\t" "v_add_u32 v28, s43, v22\n\t" \
   "v_mfma_f32_32x32x16_bf16 v[80:95], a[108:111], v[180:183], v[80:95]\n\t" "s_bitcmp1_b64 %[skip], s41\n\t" "s_cselect_b32 s58, 0, %[size]\n\t" \
   "ds_read_b64 v[136:137], v27 offset:32\n\t" "v_add_u32 v29, s43, v23\n\t" "s_waitcnt lgkmcnt(4)\n\t" \
   "v_mfma_f32_32x32x16_bf16 v[64:79], a[112:115], v[184:187], v[64:79]\n\t" "ds_read_b128 v[168:171], v26 offset:9216\n\t" \
   "v_mfma_f32_32x32x16_bf16 v[80:95], a[120:123], v[184:187], v[80:95]\n\t" \
   "buffer_store_dwordx4 v[156:159], v152, s[56:59], s55 offen offset:0 sc1\n\t" \
   "v_mfma_f32_32x32x16_bf16 v[64:79], a[116:119], v[188:191], v[64:79]\n\t" "ds_read_b128 v[172:175], v26 offset:9728\n\t" \
   "ds_read_b128 v[140:143], v12\n\t" "s_waitcnt lgkmcnt(3)\n\t" "v_add_f32 v128, v128, v130\n\t" "v_add_f32 v129, v129, v131\n\t" \
   "v_mfma_f32_32x32x16_bf16 v[80:95], a[124:127], v[188:191], v[80:95]\n\t" \
   "buffer_store_dwordx4 v[156:159], v152, s[56:59], s55 offen offset:1024 sc1\n\t" \
   "v_mfma_f32_32x32x16_bf16 v[64:79], a[128:131], v[160:163], v[64:79]\n\t" "ds_read_b128 v[176:179], v26 offset:10240\n\t" "s_waitcnt lgkmcnt(1)\n\t" \
   "v_min3_u32 v140, v140, v141, v142\n\t" "v_min_u32 v140, v140, v143\n\t" "v_cmp_ge_u32 vcc, v140, v138\n\t" \
   "v_mfma_f32_32x32x16_bf16 v[80:95], a[136:139], v[160:163], v[80:95]\n\t" "s_cbranch_vccz 150f\n\t" "50:\n\t" "s_mul_i32 s52, s45, 1536\n\t" \
   "s_mul_i32 s51, s45, 40960\n\t" "s_add_u32 m0, s46, %[ldx]\n\t" "s_mov_b32 s53, s46\n\t" \
   "v_mfma_f32_32x32x16_bf16 v[64:79], a[132:135], v[164:167], v[64:79]\n\t" "ds_read_b128 v[180:183], v26 offset:10752\n\t" \
   "buffer_load_dwordx4 v16, %[rs], s51 offen offset:0 sc1 lds\n\t" "s_lshr_b64 s[60:61], %[ysel], s54\n\t" \
   "v_mfma_f32_32x32x16_bf16 v[80:95], a[140:143], v[164:167], v[80:95]\n\t" "s_mul_i32 s61, s54, 43\n\t" "s_mul_i32 s55, s54, 80\n\t" \
   "v_add_f32 v132, v132, v134\n\t" "s_and_b32 s60, s60, 1\n\t" "v_add_f32 v133, v133, v135\n\t" \
   "v_mfma_f32_32x32x16_bf16 v[64:79], a[144:147], v[168:171], v[64:79]\n\t" "ds_read_b128 v[184:187], v26 offset:11264\n\t" \
   "v_add_f32 v128, v128, v136\n\t" "s_lshr_b32 s61, s61, 7\n\t" "v_add_f32 v129, v129, v137\n\t" "v_add_f32 v128, v128, v132\n\t" \
   "v_mfma_f32_32x32x16_bf16 v[80:95], a[152:155], v[168:171], v[80:95]\n\t" "buffer_load_dwordx4 v16, %[rs], s51 offen offset:1024 sc1 lds\n\t" \
   "s_add_u32 s55, s55, s60\n\t" "v_add_f32 v129, v129, v133\n\t" "v_mul_f32 v128, 0xbacccccd, v128\n\t" \
   "v_mfma_f32_32x32x16_bf16 v[64:79], a[148:151], v[172:175], v[64:79]\n\t" "ds_read_b128 v[188:191], v26 offset:11776\n\t" \
   "s_lshl4_add_u32 s55, s61, s55\n\t" "v_mul_f32 v129, 0x3acccccd, v129\n\t" "v_fma_f32 v129, -v128, v128, v129\n\t" "v_max_f32 v129, 0, v129\n\t" \
   "v_mfma_f32_32x32x16_bf16 v[80:95], a[156:159], v[172:175], v[80:95]\n\t" "s_lshl_b32 s55, s55, 13\n\t" "v_cvt_pk_bf16_f32 v129, v129, v128\n\t" \
   "v_and_b32 v8, 0xffff0000, v129\n\t" "v_lshlrev_b32 v129, 16, v129\n\t" "s_add_u32 s55, s55, %[yb]\n\t" "s_waitcnt lgkmcnt(2)\n\t" \
   "v_mfma_f32_32x32x16_bf16 v[64:79], a[160:163], v[176:179], v[64:79]\n\t" "ds_read_b128 v[160:163], v26 offset:12288\n\t" \
   "buffer_load_dwordx4 v16, %[rs], s51 offen offset:2048 sc1 lds\n\t" "v_add_f32 v129, 0x3727c5ac, v129\n\t" \
   "v_mfma_f32_32x32x16_bf16 v[80:95], a[168:171], v[176:179], v[80:95]\n\t" "v_cvt_pk_bf16_f32 v129, v129, v129\n\t" "s_cmp_eq_u32 s41, 63\n\t" \
   "s_cselect_b32 s58, 0, %[size]\n\t" "v_lshlrev_b32 v129, 16, v129\n\t" "v_sqrt_f32 v129, v129\n\t" \
   "v_mfma_f32_32x32x16_bf16 v[64:79], a[164:167], v[180:183], v[64:79]\n\t" "ds_read_b128 v[164:167], v26 offset:12800\n\t" "s_nop 0\n\t" \
   "v_cvt_pk_bf16_f32 v129, v129, v129\n\t" "v_lshlrev_b32 v129, 16, v129\n\t" \
   "v_mfma_f32_32x32x16_bf16 v[80:95], a[172:175], v[180:183], v[80:95]\n\t" "buffer_load_dwordx4 v16, %[rs], s51 offen offset:3072 sc1 lds\n\t" \
   "s_add_u32 m0, m0, 4096\n\t" "s_add_u32 s51, s51, 4096\n\t" "s_waitcnt lgkmcnt(2)\n\t" \
   "v_mfma_f32_32x32x16_bf16 v[64:79], a[176:179], v[184:187], v[64:79]\n\t" "ds_read_b128 v[168:171], v26 offset:13312\n\t" "s_waitcnt vmcnt(10)\n\t" \
   "v_add_u32 v138, 1, v138\n\t" "ds_write_b32 v13, v138\n\t" "v_mfma_f32_32x32x16_bf16 v[80:95], a[184:187], v[184:187], v[80:95]\n\t" \
   "v_rcp_f32 v10, v129\n\t" "v_mfma_f32_32x32x16_bf16 v[64:79], a[180:183], v[188:191], v[64:79]\n\t" "ds_read_b128 v[172:175], v26 offset:13824\n\t" \
   "buffer_load_dwordx4 v16, %[rs], s51 offen offset:0 sc1 lds\n\t" "v_mfma_f32_32x32x16_bf16 v[80:95], a[188:191], v[188:191], v[80:95]\n\t" \
   "s_waitcnt lgkmcnt(3)\n\t" "v_mfma_f32_32x32x16_bf16 v[64:79], a[192:195], v[160:163], v[64:79]\n\t" "ds_read_b128 v[176:179], v26 offset:14336\n\t" \
   "v_mfma_f32_32x32x16_bf16 v[80:95], a[200:203], v[160:163], v[80:95]\n\t" "buffer_store_dwordx4 v[156:159], v153, s[56:59], s55 offen sc1\n\t" \
   "v_mfma_f32_32x32x16_bf16 v[64:79], a[196:199], v[164:167], v[64:79]\n\t" "ds_read_b128 v[180:183], v26 offset:14848\n\t" \
   "buffer_load_dwordx4 v16, %[rs], s51 offen offset:1024 sc1 lds\n\t" "v_mfma_f32_32x32x16_bf16 v[80:95], a[204:207], v[164:167], v[80:95]\n\t" \
   "s_waitcnt lgkmcnt(2)\n\t" "v_mfma_f32_32x32x16_bf16 v[64:79], a[208:211], v[168:171], v[64:79]\n\t" "ds_read_b128 v[184:187], v26 offset:15360\n\t" \
   "buffer_load_dwordx4 v16, %[rs], s51 offen offset:2048 sc1 lds\n\t" "v_mfma_f32_32x32x16_bf16 v[80:95], a[216:219], v[168:171], v[80:95]\n\t" \
   "buffer_store_dwordx4 v[156:159], v154, s[56:59], s55 offen sc1\n\t" "v_mfma_f32_32x32x16_bf16 v[64:79], a[212:215], v[172:175], v[64:79]\n\t" \
   "ds_read_b128 v[188:191], v26 offset:15872\n\t" "v_mfma_f32_32x32x16_bf16 v[80:95], a[220:223], v[172:175], v[80:95]\n\t" \
   "buffer_load_dwordx4 v16, %[rs], s51 offen offset:3072 sc1 lds\n\t" "s_add_u32 m0, m0, 4096\n\t" "s_add_u32 s51, s51, 4096\n\t" \
   "s_waitcnt lgkmcnt(2)\n\t" "v_mfma_f32_32x32x16_bf16 v[64:79], a[224:227], v[176:179], v[64:79]\n\t" "ds_read_b128 v[160:163], v26 offset:16384\n\t" \
   "v_mfma_f32_32x32x16_bf16 v[80:95], a[232:235], v[176:179], v[80:95]\n\t" "buffer_store_dwordx4 v[156:159], v155, s[56:59], s55 offen sc1\n\t" \
   "v_mfma_f32_32x32x16_bf16 v[64:79], a[228:231], v[180:183], v[64:79]\n\t" "ds_read_b128 v[164:167], v26 offset:16896\n\t" \
   "buffer_load_dwordx4 v16, %[rs], s51 offen offset:0 sc1 lds\n\t" "v_mfma_f32_32x32x16_bf16 v[80:95], a[236:239], v[180:183], v[80:95]\n\t" \
   "s_waitcnt lgkmcnt(2)\n\t" "v_mfma_f32_32x32x16_bf16 v[64:79], a[240:243], v[184:187], v[64:79]\n\t" "ds_read_b128 v[168:171], v26 offset:17408\n\t" \
   "s_cmp_lt_i32 s40, 1\n\t" "s_cselect_b32 s49, 0x7ffffff0, 0\n\t" "v_or_b32 v9, s49, v20\n\t" "s_sub_i32 s49, s41, 1\n\t" \
   "v_mfma_f32_32x32x16_bf16 v[80:95], a[248:251], v[184:187], v[80:95]\n\t" "s_max_i32 s49, s49, 0\n\t" "s_lshl_b32 s49, s49, 12\n\t" \
   "s_waitcnt vmcnt(14)\n\t" "buffer_store_dword v31, v9, %[rs], s49 offen sc1\n\t" \
   "v_mfma_f32_32x32x16_bf16 v[64:79], a[244:247], v[188:191], v[64:79]\n\t" "ds_read_b128 v[172:175], v26 offset:17920\n\t" \
   "v_mfma_f32_32x32x16_bf16 v[80:95], a[252:255], v[188:191], v[80:95]\n\t" "buffer_load_dwordx4 v16, %[rs], s51 offen offset:1024 sc1 lds\n\t" \
   "s_add_u32 m0, s53, %[ld1]\n\t" "s_nop 0\n\t" "s_waitcnt lgkmcnt(2)\n\t" "v_mfma_f32_32x32x16_bf16 v[64:79], v[192:195], v[160:163], v[64:79]\n\t" \
   "ds_read_b128 v[176:179], v26 offset:18432\n\t" "buffer_load_dwordx4 v17, %[rs], s52 offen sc1 lds\n\t" "ds_read_b128 v[140:143], v12\n\t" \
   "v_mfma_f32_32x32x16_bf16 v[80:95], v[200:203], v[160:163], v[80:95]\n\t" "v_mfma_f32_32x32x16_bf16 v[64:79], v[196:199], v[164:167], v[64:79]\n\t" \
   "ds_read_b128 v[180:183], v26 offset:18944\n\t" "v_mfma_f32_32x32x16_bf16 v[80:95], v[204:207], v[164:167], v[80:95]\n\t" "s_waitcnt lgkmcnt(3)\n\t" \
   "v_mfma_f32_32x32x16_bf16 v[64:79], v[208:211], v[168:171], v[64:79]\n\t" "ds_read_b128 v[184:187], v26 offset:19456\n\t" "s_waitcnt lgkmcnt(2)\n\t" \
   "v_min3_u32 v140, v140, v141, v142\n\t" "v_min_u32 v140, v140, v143\n\t" "v_cmp_ge_u32 vcc, v140, v138\n\t" \
   "v_mfma_f32_32x32x16_bf16 v[80:95], v[216:219], v[168:171], v[80:95]\n\t" "s_cbranch_vccz 152f\n\t" "52:\n\t" \
   "v_mfma_f32_32x32x16_bf16 v[64:79], v[212:215], v[172:175], v[64:79]\n\t" "ds_read_b128 v[188:191], v26 offset:19968\n\t" \
   "v_mfma_f32_32x32x16_bf16 v[80:95], v[220:223], v[172:175], v[80:95]\n\t" "v_add_u32 v138, 1, v138\n\t" "ds_write_b32 v13, v138\n\t" \
   "s_waitcnt lgkmcnt(3)\n\t" "v_mfma_f32_32x32x16_bf16 v[64:79], v[224:227], v[176:179], v[64:79]\n\t" "ds_read_b128 v[160:163], v28 offset:0\n\t" \
   "s_add_u32 s45, s40, 3\n\t" "s_min_u32 s45, s45, 63\n\t" "s_mov_b32 s46, s42\n\t" \
   "v_mfma_f32_32x32x16_bf16 v[80:95], v[232:235], v[176:179], v[80:95]\n\t" "s_lshr_b32 s48, s45, 1\n\t" "s_bitcmp1_b32 s44, s48\n\t" \
   "s_cbranch_scc0 174f\n\t" "74:\n\t" "s_mov_b32 s42, s43\n\t" "s_add_u32 s43, s43, 44032\n\t" \
   "v_mfma_f32_32x32x16_bf16 v[64:79], v[228:231], v[180:183], v[64:79]\n\t" "ds_read_b128 v[164:167], v28 offset:512\n\t" \
   "s_cmp_eq_u32 s43, 132096\n\t" "s_cselect_b32 s43, 0, s43\n\t" "s_add_u32 s41, s41, 1\n\t" "s_add_u32 s40, s40, 1\n\t" \
   "v_mfma_f32_32x32x16_bf16 v[80:95], v[236:239], v[180:183], v[80:95]\n\t" "s_waitcnt lgkmcnt(3)\n\t" \
   "v_mfma_f32_32x32x16_bf16 v[64:79], v[240:243], v[184:187], v[64:79]\n\t" "ds_read_b128 v[168:171], v28 offset:1024\n\t" \
   "v_mfma_f32_32x32x16_bf16 v[80:95], v[248:251], v[184:187], v[80:95]\n\t" "v_mfma_f32_32x32x16_bf16 v[64:79], v[244:247], v[188:191], v[64:79]\n\t" \
   "ds_read_b128 v[172:175], v28 offset:1536\n\t" "v_mfma_f32_32x32x16_bf16 v[80:95], v[252:255], v[188:191], v[80:95]\n\t" "v_mov_b32 v26, v28\n\t" \
   "v_mov_b32 v27, v29\n\t" "s_waitcnt lgkmcnt(2)\n\t" "v_mfma_f32_32x32x16_bf16 v[96:111], a[0:3], v[160:163], 0\n\t" \
   "ds_read_b128 v[176:179], v26 offset:2048\n\t" "v_mfma_f32_32x32x16_bf16 v[112:127], a[8:11], v[160:163], 0\n\t" \
   "v_mul_f32 v11, 0x3e0293ee, v10\n\t" "s_lshl_b32 s47, s41, 13\n\t" "s_sub_i32 s47, s47, 8192\n\t" "v_fmac_f32 v64, v32, v8\n\t" \
   "v_fmac_f32 v65, v33, v8\n\t" "v_fmac_f32 v66, v34, v8\n\t" "v_mfma_f32_32x32x16_bf16 v[96:111], a[4:7], v[164:167], v[96:111]\n\t" \
   "ds_read_b128 v[180:183], v26 offset:2560\n\t" "v_fmac_f32 v67, v35, v8\n\t" "v_fmac_f32 v68, v36, v8\n\t" "v_fmac_f32 v69, v37, v8\n\t" \
   "v_fmac_f32 v70, v38, v8\n\t" "v_mfma_f32_32x32x16_bf16 v[112:127], a[12:15], v[164:167], v[112:127]\n\t" "v_fmac_f32 v71, v39, v8\n\t" \
   "v_fmac_f32 v72, v40, v8\n\t" "v_fmac_f32 v73, v41, v8\n\t" "v_fmac_f32 v74, v42, v8\n\t" "v_fmac_f32 v75, v43, v8\n\t" "s_waitcnt lgkmcnt(2)\n\t" \
   "v_mfma_f32_32x32x16_bf16 v[96:111], a[16:19], v[168:171], v[96:111]\n\t" "ds_read_b128 v[184:187], v26 offset:3072\n\t" \
   "v_fmac_f32 v76, v44, v8\n\t" "v_fmac_f32 v77, v45, v8\n\t" "v_fmac_f32 v78, v46, v8\n\t" "v_fmac_f32 v79, v47, v8\n\t" \
   "v_mfma_f32_32x32x16_bf16 v[112:127], a[24:27], v[168:171], v[112:127]\n\t" "v_mul_f32 v64, v64, v11\n\t" "v_mul_f32 v65, v65, v11\n\t" \
   "v_mul_f32 v66, v66, v11\n\t" "v_mul_f32 v67, v67, v11\n\t" "v_mul_f32 v68, v68, v11\n\t" "v_mul_f32 v69, v69, v11\n\t" \
   "v_mfma_f32_32x32x16_bf16 v[96:111], a[20:23], v[172:175], v[96:111]\n\t" "ds_read_b128 v[188:191], v26 offset:3584\n\t" \
   "v_mul_f32 v70, v70, v11\n\t" "v_mul_f32 v71, v71, v11\n\t" "v_mul_f32 v72, v72, v11\n\t" "v_mul_f32 v73, v73, v11\n\t" \
   "v_mfma_f32_32x32x16_bf16 v[112:127], a[28:31], v[172:175], v[112:127]\n\t" "v_mul_f32 v74, v74, v11\n\t" "v_mul_f32 v75, v75, v11\n\t" \
   "v_mul_f32 v76, v76, v11\n\t" "v_mul_f32 v77, v77, v11\n\t" "v_mul_f32 v78, v78, v11\n\t" "s_waitcnt lgkmcnt(2)\n\t" \
   "v_mfma_f32_32x32x16_bf16 v[96:111], a[32:35], v[176:179], v[96:111]\n\t" "ds_read_b128 v[160:163], v26 offset:4096\n\t" \
   "v_mul_f32 v79, v79, v11\n\t" "s_add_u32 s48, s47, %[qf]\n\t" "v_cvt_pk_bf16_f32 v64, v64, v65\n\t" "v_cvt_pk_bf16_f32 v65, v66, v67\n\t" \
   "v_mfma_f32_32x32x16_bf16 v[112:127], a[40:43], v[176:179], v[112:127]\n\t" "v_cvt_pk_bf16_f32 v66, v72, v73\n\t" \
   "v_cvt_pk_bf16_f32 v67, v74, v75\n\t" "v_mfma_f32_32x32x16_bf16 v[96:111], a[36:39], v[180:183], v[96:111]\n\t" \
   "ds_read_b128 v[164:167], v26 offset:4608\n\t" "v_mfma_f32_32x32x16_bf16 v[112:127], a[44:47], v[180:183], v[112:127]\n\t" \
   "buffer_store_dwordx4 v[64:67], v21, %[rs], s48 offen offset:0 sc1\n\t" "s_waitcnt lgkmcnt(2)\n\t" \
   "v_mfma_f32_32x32x16_bf16 v[96:111], a[48:51], v[184:187], v[96:111]\n\t" "ds_read_b128 v[168:171], v26 offset:5120\n\t" \
   "v_cvt_pk_bf16_f32 v68, v68, v69\n\t" "v_cvt_pk_bf16_f32 v69, v70, v71\n\t" "v_cvt_pk_bf16_f32 v70, v76, v77\n\t" \
   "v_cvt_pk_bf16_f32 v71, v78, v79\n\t" "v_mfma_f32_32x32x16_bf16 v[112:127], a[56:59], v[184:187], v[112:127]\n\t" \
   "buffer_store_dwordx4 v[68:71], v21, %[rs], s48 offen offset:512 sc1\n\t" "v_fmac_f32 v80, v48, v8\n\t" \
   "v_mfma_f32_32x32x16_bf16 v[96:111], a[52:55], v[188:191], v[96:111]\n\t" "ds_read_b128 v[172:175], v26 offset:5632\n\t" \
   "v_fmac_f32 v81, v49, v8\n\t" "v_fmac_f32 v82, v50, v8\n\t" "v_fmac_f32 v83, v51, v8\n\t" "v_fmac_f32 v84, v52, v8\n\t" \
   "v_mfma_f32_32x32x16_bf16 v[112:127], a[60:63], v[188:191], v[112:127]\n\t" "v_fmac_f32 v85, v53, v8\n\t" "v_fmac_f32 v86, v54, v8\n\t" \
   "v_fmac_f32 v87, v55, v8\n\t" "v_fmac_f32 v88, v56, v8\n\t" "v_fmac_f32 v89, v57, v8\n\t" "s_waitcnt lgkmcnt(2)\n\t" \
   "v_mfma_f32_32x32x16_bf16 v[96:111], a[64:67], v[160:163], v[96:111]\n\t" "ds_read_b128 v[176:179], v26 offset:6144\n\t" \
   "v_fmac_f32 v90, v58, v8\n\t" "v_fmac_f32 v91, v59, v8\n\t" "v_fmac_f32 v92, v60, v8\n\t" "v_fmac_f32 v93, v61, v8\n\t" \
   "v_mfma_f32_32x32x16_bf16 v[112:127], a[72:75], v[160:163], v[112:127]\n\t" "v_fmac_f32 v94, v62, v8\n\t" "v_fmac_f32 v95, v63, v8\n\t" \
   "v_mul_f32 v80, v80, v10\n\t" "v_mul_f32 v81, v81, v10\n\t" "v_mul_f32 v82, v82, v10\n\t" "v_mul_f32 v83, v83, v10\n\t" \
   "v_mfma_f32_32x32x16_bf16 v[96:111], a[68:71], v[164:167], v[96:111]\n\t" "ds_read_b128 v[180:183], v26 offset:6656\n\t" \
   "v_mul_f32 v84, v84, v10\n\t" "v_mul_f32 v85, v85, v10\n\t" "v_mul_f32 v86, v86, v10\n\t" "v_mul_f32 v87, v87, v10\n\t" \
   "v_mfma_f32_32x32x16_bf16 v[112:127], a[76:79], v[164:167], v[112:127]\n\t" "v_mul_f32 v88, v88, v10\n\t" "v_mul_f32 v89, v89, v10\n\t" \
   "v_mul_f32 v90, v90, v10\n\t" "v_mul_f32 v91, v91, v10\n\t" "v_mul_f32 v92, v92, v10\n\t" "s_waitcnt lgkmcnt(2)\n\t" \
   "v_mfma_f32_32x32x16_bf16 v[96:111], a[80:83], v[168:171], v[96:111]\n\t" "ds_read_b128 v[184:187], v26 offset:7168\n\t" \
   "v_mul_f32 v93, v93, v10\n\t" "v_mul_f32 v94, v94, v10\n\t" "v_mul_f32 v95, v95, v10\n\t" "s_add_u32 s48, s47, %[kf]\n\t" \
   "v_mfma_f32_32x32x16_bf16 v[112:127], a[88:91], v[168:171], v[112:127]\n\t" "v_cvt_pk_bf16_f32 v80, v80, v81\n\t" \
   "v_cvt_pk_bf16_f32 v81, v82, v83\n\t" "v_cvt_pk_bf16_f32 v82, v88, v89\n\t" "v_cvt_pk_bf16_f32 v83, v90, v91\n\t" \
   "ds_read_b128 v[128:131], v27 offset:0\n\t" "v_mfma_f32_32x32x16_bf16 v[96:111], a[84:87], v[172:175], v[96:111]\n\t" \
   "ds_read_b128 v[188:191], v26 offset:7680\n\t" "s_add_u32 s54, s41, 1\n\t" "s_lshr_b64 s[60:61], %[sel], s54\n\t" "s_lshl_b32 s55, s54, 19\n\t" \
   "s_and_b32 s60, s60, 1\n\t" "v_mfma_f32_32x32x16_bf16 v[112:127], a[92:95], v[172:175], v[112:127]\n\t" \
   "buffer_store_dwordx4 v[80:83], v21, %[rs], s48 offen offset:0 sc1\n\t" "s_waitcnt lgkmcnt(3)\n\t" \
   "v_mfma_f32_32x32x16_bf16 v[96:111], a[96:99], v[176:179], v[96:111]\n\t" "ds_read_b128 v[160:163], v26 offset:8192\n\t" \
   "v_cvt_pk_bf16_f32 v84, v84, v85\n\t" "v_cvt_pk_bf16_f32 v85, v86, v87\n\t" "v_cvt_pk_bf16_f32 v86, v92, v93\n\t" \
   "v_cvt_pk_bf16_f32 v87, v94, v95\n\t" "v_mfma_f32_32x32x16_bf16 v[112:127], a[104:107], v[176:179], v[112:127]\n\t" \
   "buffer_store_dwordx4 v[84:87], v21, %[rs], s48 offen offset:512 sc1\n\t" "s_add_u32 s55, s55, %[pool]\n\t" \
   "v_mfma_f32_32x32x16_bf16 v[96:111], a[100:103], v[180:183], v[96:111]\n\t" "ds_read_b128 v[164:167], v26 offset:8704\n\t" \
   "s_lshl_b32 s60, s60, 13\n\t" "s_add_u32 s55, s55, s60\n\t" "ds_read_b128 v[132:135], v27 offset:16\n\t" "v_add_u32 v28, s43, v22\n\t" \
   "v_mfma_f32_32x32x16_bf16 v[112:127], a[108:111], v[180:183], v[112:127]\n\t" "s_bitcmp1_b64 %[skip], s41\n\t" "s_cselect_b32 s58, 0, %[size]\n\t" \
   "ds_read_b64 v[136:137], v27 offset:32\n\t" "v_add_u32 v29, s43, v23\n\t" "s_waitcnt lgkmcnt(4)\n\t" \
   "v_mfma_f32_32x32x16_bf16 v[96:111], a[112:115], v[184:187], v[96:111]\n\t" "ds_read_b128 v[168:171], v26 offset:9216\n\t" \
   "v_mfma_f32_32x32x16_bf16 v[112:127], a[120:123], v[184:187], v[112:127]\n\t" \
   "buffer_store_dwordx4 v[156:159], v152, s[56:59], s55 offen offset:0 sc1\n\t" \
   "v_mfma_f32_32x32x16_bf16 v[96:111], a[116:119], v[188:191], v[96:111]\n\t" "ds_read_b128 v[172:175], v26 offset:9728\n\t" \
   "ds_read_b128 v[140:143], v12\n\t" "s_waitcnt lgkmcnt(3)\n\t" "v_add_f32 v128, v128, v130\n\t" "v_add_f32 v129, v129, v131\n\t" \
   "v_mfma_f32_32x32x16_bf16 v[112:127], a[124:127], v[188:191], v[112:127]\n\t" \
   "buffer_store_dwordx4 v[156:159], v152, s[56:59], s55 offen offset:1024 sc1\n\t" \
   "v_mfma_f32_32x32x16_bf16 v[96:111], a[128:131], v[160:163], v[96:111]\n\t" "ds_read_b128 v[176:179], v26 offset:10240\n\t" \
   "s_waitcnt lgkmcnt(1)\n\t" "v_min3_u32 v140, v140, v141, v142\n\t" "v_min_u32 v140, v140, v143\n\t" "v_cmp_ge_u32 vcc, v140, v138\n\t" \
   "v_mfma_f32_32x32x16_bf16 v[112:127], a[136:139], v[160:163], v[112:127]\n\t" "s_cbranch_vccz 151f\n\t" "51:\n\t" "s_mul_i32 s52, s45, 1536\n\t" \
   "s_mul_i32 s51, s45, 40960\n\t" "s_add_u32 m0, s46, %[ldx]\n\t" "s_mov_b32 s53, s46\n\t" \
   "v_mfma_f32_32x32x16_bf16 v[96:111], a[132:135], v[164:167], v[96:111]\n\t" "ds_read_b128 v[180:183], v26 offset:10752\n\t" \
   "buffer_load_dwordx4 v16, %[rs], s51 offen offset:0 sc1 lds\n\t" "s_lshr_b64 s[60:61], %[ysel], s54\n\t" \
   "v_mfma_f32_32x32x16_bf16 v[112:127], a[140:143], v[164:167], v[112:127]\n\t" "s_mul_i32 s61, s54, 43\n\t" "s_mul_i32 s55, s54, 80\n\t" \
   "v_add_f32 v132, v132, v134\n\t" "s_and_b32 s60, s60, 1\n\t" "v_add_f32 v133, v133, v135\n\t" \
   "v_mfma_f32_32x32x16_bf16 v[96:111], a[144:147], v[168:171], v[96:111]\n\t" "ds_read_b128 v[184:187], v26 offset:11264\n\t" \
   "v_add_f32 v128, v128, v136\n\t" "s_lshr_b32 s61, s61, 7\n\t" "v_add_f32 v129, v129, v137\n\t" "v_add_f32 v128, v128, v132\n\t" \
   "v_mfma_f32_32x32x16_bf16 v[112:127], a[152:155], v[168:171], v[112:127]\n\t" "buffer_load_dwordx4 v16, %[rs], s51 offen offset:1024 sc1 lds\n\t" \
   "s_add_u32 s55, s55, s60\n\t" "v_add_f32 v129, v129, v133\n\t" "v_mul_f32 v128, 0xbacccccd, v128\n\t" \
   "v_mfma_f32_32x32x16_bf16 v[96:111], a[148:151], v[172:175], v[96:111]\n\t" "ds_read_b128 v[188:191], v26 offset:11776\n\t" \
   "s_lshl4_add_u32 s55, s61, s55\n\t" "v_mul_f32 v129, 0x3acccccd, v129\n\t" "v_fma_f32 v129, -v128, v128, v129\n\t" "v_max_f32 v129, 0, v129\n\t" \
   "v_mfma_f32_32x32x16_bf16 v[112:127], a[156:159], v[172:175], v[112:127]\n\t" "s_lshl_b32 s55, s55, 13\n\t" "v_cvt_pk_bf16_f32 v129, v129, v128\n\t" \
   "v_and_b32 v8, 0xffff0000, v129\n\t" "v_lshlrev_b32 v129, 16, v129\n\t" "s_add_u32 s55, s55, %[yb]\n\t" "s_waitcnt lgkmcnt(2)\n\t" \
   "v_mfma_f32_32x32x16_bf16 v[96:111], a[160:163], v[176:179], v[96:111]\n\t" "ds_read_b128 v[160:163], v26 offset:12288\n\t" \
   "buffer_load_dwordx4 v16, %[rs], s51 offen offset:2048 sc1 lds\n\t" "v_add_f32 v129, 0x3727c5ac, v129\n\t" \
   "v_mfma_f32_32x32x16_bf16 v[112:127], a[168:171], v[176:179], v[112:127]\n\t" "v_cvt_pk_bf16_f32 v129, v129, v129\n\t" "s_cmp_eq_u32 s41, 63\n\t" \
   "s_cselect_b32 s58, 0, %[size]\n\t" "v_lshlrev_b32 v129, 16, v129\n\t" "v_sqrt_f32 v129, v129\n\t" \
   "v_mfma_f32_32x32x16_bf16 v[96:111], a[164:167], v[180:183], v[96:111]\n\t" "ds_read_b128 v[164:167], v26 offset:12800\n\t" "s_nop 0\n\t" \
   "v_cvt_pk_bf16_f32 v129, v129, v129\n\t" "v_lshlrev_b32 v129, 16, v129\n\t" \
   "v_mfma_f32_32x32x16_bf16 v[112:127], a[172:175], v[180:183], v[112:127]\n\t" "buffer_load_dwordx4 v16, %[rs], s51 offen offset:3072 sc1 lds\n\t" \
   "s_add_u32 m0, m0, 4096\n\t" "s_add_u32 s51, s51, 4096\n\t" "s_waitcnt lgkmcnt(2)\n\t" \
   "v_mfma_f32_32x32x16_bf16 v[96:111], a[176:179], v[184:187], v[96:111]\n\t" "ds_read_b128 v[168:171], v26 offset:13312\n\t" \
   "s_waitcnt vmcnt(10)\n\t" "v_add_u32 v138, 1, v138\n\t" "ds_write_b32 v13, v138\n\t" \
   "v_mfma_f32_32x32x16_bf16 v[112:127], a[184:187], v[184:187], v[112:127]\n\t" "v_rcp_f32 v10, v129\n\t" \
   "v_mfma_f32_32x32x16_bf16 v[96:111], a[180:183], v[188:191], v[96:111]\n\t" "ds_read_b128 v[172:175], v26 offset:13824\n\t" \
   "buffer_load_dwordx4 v16, %[rs], s51 offen offset:0 sc1 lds\n\t" "v_mfma_f32_32x32x16_bf16 v[112:127], a[188:191], v[188:191], v[112:127]\n\t" \
   "s_waitcnt lgkmcnt(3)\n\t" "v_mfma_f32_32x32x16_bf16 v[96:111], a[192:195], v[160:163], v[96:111]\n\t" \
   "ds_read_b128 v[176:179], v26 offset:14336\n\t" "v_mfma_f32_32x32x16_bf16 v[112:127], a[200:203], v[160:163], v[112:127]\n\t" \
   "buffer_store_dwordx4 v[156:159], v153, s[56:59], s55 offen sc1\n\t" "v_mfma_f32_32x32x16_bf16 v[96:111], a[196:199], v[164:167], v[96:111]\n\t" \
   "ds_read_b128 v[180:183], v26 offset:14848\n\t" "buffer_load_dwordx4 v16, %[rs], s51 offen offset:1024 sc1 lds\n\t" \
   "v_mfma_f32_32x32x16_bf16 v[112:127], a[204:207], v[164:167], v[112:127]\n\t" "s_waitcnt lgkmcnt(2)\n\t" \
   "v_mfma_f32_32x32x16_bf16 v[96:111], a[208:211], v[168:171], v[96:111]\n\t" "ds_read_b128 v[184:187], v26 offset:15360\n\t" \
   "buffer_load_dwordx4 v16, %[rs], s51 offen offset:2048 sc1 lds\n\t" "v_mfma_f32_32x32x16_bf16 v[112:127], a[216:219], v[168:171], v[112:127]\n\t" \
   "buffer_store_dwordx4 v[156:159], v154, s[56:59], s55 offen sc1\n\t" "v_mfma_f32_32x32x16_bf16 v[96:111], a[212:215], v[172:175], v[96:111]\n\t" \
   "ds_read_b128 v[188:191], v26 offset:15872\n\t" "v_mfma_f32_32x32x16_bf16 v[112:127], a[220:223], v[172:175], v[112:127]\n\t" \
   "buffer_load_dwordx4 v16, %[rs], s51 offen offset:3072 sc1 lds\n\t" "s_add_u32 m0, m0, 4096\n\t" "s_add_u32 s51, s51, 4096\n\t" \
   "s_waitcnt lgkmcnt(2)\n\t" "v_mfma_f32_32x32x16_bf16 v[96:111], a[224:227], v[176:179], v[96:111]\n\t" \
   "ds_read_b128 v[160:163], v26 offset:16384\n\t" "v_mfma_f32_32x32x16_bf16 v[112:127], a[232:235], v[176:179], v[112:127]\n\t" \
   "buffer_store_dwordx4 v[156:159], v155, s[56:59], s55 offen sc1\n\t" "v_mfma_f32_32x32x16_bf16 v[96:111], a[228:231], v[180:183], v[96:111]\n\t" \
   "ds_read_b128 v[164:167], v26 offset:16896\n\t" "buffer_load_dwordx4 v16, %[rs], s51 offen offset:0 sc1 lds\n\t" \
   "v_mfma_f32_32x32x16_bf16 v[112:127], a[236:239], v[180:183], v[112:127]\n\t" "s_waitcnt lgkmcnt(2)\n\t" \
   "v_mfma_f32_32x32x16_bf16 v[96:111], a[240:243], v[184:187], v[96:111]\n\t" "ds_read_b128 v[168:171], v26 offset:17408\n\t" \
   "s_cmp_lt_i32 s40, 1\n\t" "s_cselect_b32 s49, 0x7ffffff0, 0\n\t" "v_or_b32 v9, s49, v20\n\t" "s_sub_i32 s49, s41, 1\n\t" \
   "v_mfma_f32_32x32x16_bf16 v[112:127], a[248:251], v[184:187], v[112:127]\n\t" "s_max_i32 s49, s49, 0\n\t" "s_lshl_b32 s49, s49, 12\n\t" \
   "s_waitcnt vmcnt(14)\n\t" "buffer_store_dword v31, v9, %[rs], s49 offen sc1\n\t" \
   "v_mfma_f32_32x32x16_bf16 v[96:111], a[244:247], v[188:191], v[96:111]\n\t" "ds_read_b128 v[172:175], v26 offset:17920\n\t" \
   "v_mfma_f32_32x32x16_bf16 v[112:127], a[252:255], v[188:191], v[112:127]\n\t" "buffer_load_dwordx4 v16, %[rs], s51 offen offset:1024 sc1 lds\n\t" \
   "s_add_u32 m0, s53, %[ld1]\n\t" "s_nop 0\n\t" "s_waitcnt lgkmcnt(2)\n\t" "v_mfma_f32_32x32x16_bf16 v[96:111], v[192:195], v[160:163], v[96:111]\n\t" \
   "ds_read_b128 v[176:179], v26 offset:18432\n\t" "buffer_load_dwordx4 v17, %[rs], s52 offen sc1 lds\n\t" "ds_read_b128 v[140:143], v12\n\t" \
   "v_mfma_f32_32x32x16_bf16 v[112:127], v[200:203], v[160:163], v[112:127]\n\t" \
   "v_mfma_f32_32x32x16_bf16 v[96:111], v[196:199], v[164:167], v[96:111]\n\t" "ds_read_b128 v[180:183], v26 offset:18944\n\t" \
   "v_mfma_f32_32x32x16_bf16 v[112:127], v[204:207], v[164:167], v[112:127]\n\t" "s_waitcnt lgkmcnt(3)\n\t" \
   "v_mfma_f32_32x32x16_bf16 v[96:111], v[208:211], v[168:171], v[96:111]\n\t" "ds_read_b128 v[184:187], v26 offset:19456\n\t" \
   "s_waitcnt lgkmcnt(2)\n\t" "v_min3_u32 v140, v140, v141, v142\n\t" "v_min_u32 v140, v140, v143\n\t" "v_cmp_ge_u32 vcc, v140, v138\n\t" \
   "v_mfma_f32_32x32x16_bf16 v[112:127], v[216:219], v[168:171], v[112:127]\n\t" "s_cbranch_vccz 153f\n\t" "53:\n\t" \
   "v_mfma_f32_32x32x16_bf16 v[96:111], v[212:215], v[172:175], v[96:111]\n\t" "ds_read_b128 v[188:191], v26 offset:19968\n\t" \
   "v_mfma_f32_32x32x16_bf16 v[112:127], v[220:223], v[172:175], v[112:127]\n\t" "v_add_u32 v138, 1, v138\n\t" "ds_write_b32 v13, v138\n\t" \
   "s_waitcnt lgkmcnt(3)\n\t" "v_mfma_f32_32x32x16_bf16 v[96:111], v[224:227], v[176:179], v[96:111]\n\t" "ds_read_b128 v[160:163], v28 offset:0\n\t" \
   "s_add_u32 s45, s40, 3\n\t" "s_min_u32 s45, s45, 63\n\t" "s_mov_b32 s46, s42\n\t" \
   "v_mfma_f32_32x32x16_bf16 v[112:127], v[232:235], v[176:179], v[112:127]\n\t" "s_lshr_b32 s48, s45, 1\n\t" "s_bitcmp1_b32 s44, s48\n\t" \
   "s_cbranch_scc0 178f\n\t" "78:\n\t" "s_mov_b32 s42, s43\n\t" "s_add_u32 s43, s43, 44032\n\t" \
   "v_mfma_f32_32x32x16_bf16 v[96:111], v[228:231], v[180:183], v[96:111]\n\t" "ds_read_b128 v[164:167], v28 offset:512\n\t" \
   "s_cmp_eq_u32 s43, 132096\n\t" "s_cselect_b32 s43, 0, s43\n\t" "s_add_u32 s41, s41, 1\n\t" "s_add_u32 s40, s40, 1\n\t" \
   "v_mfma_f32_32x32x16_bf16 v[112:127], v[236:239], v[180:183], v[112:127]\n\t" "s_waitcnt lgkmcnt(3)\n\t" \
   "v_mfma_f32_32x32x16_bf16 v[96:111], v[240:243], v[184:187], v[96:111]\n\t" "ds_read_b128 v[168:171], v28 offset:1024\n\t" \
   "v_mfma_f32_32x32x16_bf16 v[112:127], v[248:251], v[184:187], v[112:127]\n\t" \
   "v_mfma_f32_32x32x16_bf16 v[96:111], v[244:247], v[188:191], v[96:111]\n\t" "ds_read_b128 v[172:175], v28 offset:1536\n\t" \
   "v_mfma_f32_32x32x16_bf16 v[112:127], v[252:255], v[188:191], v[112:127]\n\t" "s_cmp_lt_u32 s40, 64\n\t" "s_cbranch_scc1 1b\n\t" "s_nop 7\n\t" \
   "v_mul_f32 v11, 0x3e0293ee, v10\n\t" "s_lshl_b32 s47, s41, 13\n\t" "s_sub_i32 s47, s47, 8192\n\t" "v_fmac_f32 v96, v32, v8\n\t" \
   "v_fmac_f32 v97, v33, v8\n\t" "v_fmac_f32 v98, v34, v8\n\t" "v_fmac_f32 v99, v35, v8\n\t" "v_fmac_f32 v100, v36, v8\n\t" \
   "v_fmac_f32 v101, v37, v8\n\t" "v_fmac_f32 v102, v38, v8\n\t" "v_fmac_f32 v103, v39, v8\n\t" "v_fmac_f32 v104, v40, v8\n\t" \
   "v_fmac_f32 v105, v41, v8\n\t" "v_fmac_f32 v106, v42, v8\n\t" "v_fmac_f32 v107, v43, v8\n\t" "v_fmac_f32 v108, v44, v8\n\t" \
   "v_fmac_f32 v109, v45, v8\n\t" "v_fmac_f32 v110, v46, v8\n\t" "v_fmac_f32 v111, v47, v8\n\t" "v_mul_f32 v96, v96, v11\n\t" \
   "v_mul_f32 v97, v97, v11\n\t" "v_mul_f32 v98, v98, v11\n\t" "v_mul_f32 v99, v99, v11\n\t" "v_mul_f32 v100, v100, v11\n\t" \
   "v_mul_f32 v101, v101, v11\n\t" "v_mul_f32 v102, v102, v11\n\t" "v_mul_f32 v103, v103, v11\n\t" "v_mul_f32 v104, v104, v11\n\t" \
   "v_mul_f32 v105, v105, v11\n\t" "v_mul_f32 v106, v106, v11\n\t" "v_mul_f32 v107, v107, v11\n\t" "v_mul_f32 v108, v108, v11\n\t" \
   "v_mul_f32 v109, v109, v11\n\t" "v_mul_f32 v110, v110, v11\n\t" "v_mul_f32 v111, v111, v11\n\t" "s_add_u32 s48, s47, %[qf]\n\t" \
   "v_cvt_pk_bf16_f32 v96, v96, v97\n\t" "v_cvt_pk_bf16_f32 v97, v98, v99\n\t" "v_cvt_pk_bf16_f32 v98, v104, v105\n\t" \
   "v_cvt_pk_bf16_f32 v99, v106, v107\n\t" "buffer_store_dwordx4 v[96:99], v21, %[rs], s48 offen offset:0 sc1\n\t" \
   "v_cvt_pk_bf16_f32 v100, v100, v101\n\t" "v_cvt_pk_bf16_f32 v101, v102, v103\n\t" "v_cvt_pk_bf16_f32 v102, v108, v109\n\t" \
   "v_cvt_pk_bf16_f32 v103, v110, v111\n\t" "buffer_store_dwordx4 v[100:103], v21, %[rs], s48 offen offset:512 sc1\n\t" "v_fmac_f32 v112, v48, v8\n\t" \
   "v_fmac_f32 v113, v49, v8\n\t" "v_fmac_f32 v114, v50, v8\n\t" "v_fmac_f32 v115, v51, v8\n\t" "v_fmac_f32 v116, v52, v8\n\t" \
   "v_fmac_f32 v117, v53, v8\n\t" "v_fmac_f32 v118, v54, v8\n\t" "v_fmac_f32 v119, v55, v8\n\t" "v_fmac_f32 v120, v56, v8\n\t" \
   "v_fmac_f32 v121, v57, v8\n\t" "v_fmac_f32 v122, v58, v8\n\t" "v_fmac_f32 v123, v59, v8\n\t" "v_fmac_f32 v124, v60, v8\n\t" \
   "v_fmac_f32 v125, v61, v8\n\t" "v_fmac_f32 v126, v62, v8\n\t" "v_fmac_f32 v127, v63, v8\n\t" "v_mul_f32 v112, v112, v10\n\t" \
   "v_mul_f32 v113, v113, v10\n\t" "v_mul_f32 v114, v114, v10\n\t" "v_mul_f32 v115, v115, v10\n\t" "v_mul_f32 v116, v116, v10\n\t" \
   "v_mul_f32 v117, v117, v10\n\t" "v_mul_f32 v118, v118, v10\n\t" "v_mul_f32 v119, v119, v10\n\t" "v_mul_f32 v120, v120, v10\n\t" \
   "v_mul_f32 v121, v121, v10\n\t" "v_mul_f32 v122, v122, v10\n\t" "v_mul_f32 v123, v123, v10\n\t" "v_mul_f32 v124, v124, v10\n\t" \
   "v_mul_f32 v125, v125, v10\n\t" "v_mul_f32 v126, v126, v10\n\t" "v_mul_f32 v127, v127, v10\n\t" "s_add_u32 s48, s47, %[kf]\n\t" \
   "v_cvt_pk_bf16_f32 v112, v112, v113\n\t" "v_cvt_pk_bf16_f32 v113, v114, v115\n\t" "v_cvt_pk_bf16_f32 v114, v120, v121\n\t" \
   "v_cvt_pk_bf16_f32 v115, v122, v123\n\t" "buffer_store_dwordx4 v[112:115], v21, %[rs], s48 offen offset:0 sc1\n\t" \
   "v_cvt_pk_bf16_f32 v116, v116, v117\n\t" "v_cvt_pk_bf16_f32 v117, v118, v119\n\t" "v_cvt_pk_bf16_f32 v118, v124, v125\n\t" \
   "v_cvt_pk_bf16_f32 v119, v126, v127\n\t" "buffer_store_dwordx4 v[116:119], v21, %[rs], s48 offen offset:512 sc1\n\t" \
   "s_waitcnt vmcnt(0) lgkmcnt(0)\n\t" "s_cmp_lt_i32 s40, 1\n\t" "s_cselect_b32 s47, 0x7ffffff0, 0\n\t" "v_or_b32 v30, s47, v20\n\t" \
   "s_sub_i32 s47, s41, 1\n\t" "s_max_i32 s47, s47, 0\n\t" "s_lshl_b32 s47, s47, 12\n\t" "buffer_store_dword v31, v30, %[rs], s47 offen sc1\n\t" \
   "s_branch 9f\n\t" "172:\n\t" "s_mov_b32 s47, 0\n\t" "buffer_load_dword v30, v19, %[rs], 0 offen sc1\n\t" "s_waitcnt vmcnt(0)\n\t" \
   "v_add_u32 v30, 1, v30\n\t" "v_cmp_lt_u32 vcc, 7, v30\n\t" "s_mov_b32 s44, vcc_lo\n\t" "s_bitcmp1_b32 s44, s48\n\t" "s_cbranch_scc1 72b\n\t" \
   "s_bitset1_b32 s44, s48\n\t" "s_lshl_b32 s48, s48, 7\n\t" "s_add_u32 s48, s48, %[cnt]\n\t" "73:\n\t" "s_sleep 10\n\t" \
   "buffer_load_dword v30, off, %[rs], s48 sc1\n\t" "s_add_u32 s47, s47, 1\n\t" "s_waitcnt vmcnt(0)\n\t" "v_add_u32 v30, 1, v30\n\t" \
   "v_cmp_lt_u32 vcc, 7, v30\n\t" "s_cmp_gt_u32 s47, 0x20000\n\t" "s_cbranch_scc1 72b\n\t" "s_cbranch_vccz 73b\n\t" "s_branch 72b\n\t" "174:\n\t" \
   "s_mov_b32 s47, 0\n\t" "buffer_load_dword v30, v19, %[rs], 0 offen sc1\n\t" "s_waitcnt vmcnt(0)\n\t" "v_add_u32 v30, 1, v30\n\t" \
   "v_cmp_lt_u32 vcc, 7, v30\n\t" "s_mov_b32 s44, vcc_lo\n\t" "s_bitcmp1_b32 s44, s48\n\t" "s_cbranch_scc1 74b\n\t" "s_bitset1_b32 s44, s48\n\t" \
   "s_lshl_b32 s48, s48, 7\n\t" "s_add_u32 s48, s48, %[cnt]\n\t" "75:\n\t" "s_sleep 10\n\t" "buffer_load_dword v30, off, %[rs], s48 sc1\n\t" \
   "s_add_u32 s47, s47, 1\n\t" "s_waitcnt vmcnt(0)\n\t" "v_add_u32 v30, 1, v30\n\t" "v_cmp_lt_u32 vcc, 7, v30\n\t" "s_cmp_gt_u32 s47, 0x20000\n\t" \
   "s_cbranch_scc1 74b\n\t" "s_cbranch_vccz 75b\n\t" "s_branch 74b\n\t" "178:\n\t" "s_mov_b32 s47, 0\n\t" \
   "buffer_load_dword v30, v19, %[rs], 0 offen sc1\n\t" "s_waitcnt vmcnt(0)\n\t" "v_add_u32 v30, 1, v30\n\t" "v_cmp_lt_u32 vcc, 7, v30\n\t" \
   "s_mov_b32 s44, vcc_lo\n\t" "s_bitcmp1_b32 s44, s48\n\t" "s_cbranch_scc1 78b\n\t" "s_bitset1_b32 s44, s48\n\t" "s_lshl_b32 s48, s48, 7\n\t" \
   "s_add_u32 s48, s48, %[cnt]\n\t" "79:\n\t" "s_sleep 10\n\t" "buffer_load_dword v30, off, %[rs], s48 sc1\n\t" "s_add_u32 s47, s47, 1\n\t" \
   "s_waitcnt vmcnt(0)\n\t" "v_add_u32 v30, 1, v30\n\t" "v_cmp_lt_u32 vcc, 7, v30\n\t" "s_cmp_gt_u32 s47, 0x20000\n\t" "s_cbranch_scc1 78b\n\t" \
   "s_cbranch_vccz 79b\n\t" "s_branch 78b\n\t" "150:\n\t" "ds_read_b128 v[140:143], v12\n\t" "s_waitcnt lgkmcnt(0)\n\t" \
   "v_min3_u32 v140, v140, v141, v142\n\t" "v_min_u32 v140, v140, v143\n\t" "v_cmp_ge_u32 vcc, v140, v138\n\t" "s_cbranch_vccz 150b\n\t" \
   "s_branch 50b\n\t" "152:\n\t" "ds_read_b128 v[140:143], v12\n\t" "s_waitcnt lgkmcnt(0)\n\t" "v_min3_u32 v140, v140, v141, v142\n\t" \
   "v_min_u32 v140, v140, v143\n\t" "v_cmp_ge_u32 vcc, v140, v138\n\t" "s_cbranch_vccz 152b\n\t" "s_branch 52b\n\t" "151:\n\t" \
   "ds_read_b128 v[140:143], v12\n\t" "s_waitcnt lgkmcnt(0)\n\t" "v_min3_u32 v140, v140, v141, v142\n\t" "v_min_u32 v140, v140, v143\n\t" \
   "v_cmp_ge_u32 vcc, v140, v138\n\t" "s_cbranch_vccz 151b\n\t" "s_branch 51b\n\t" "153:\n\t" "ds_read_b128 v[140:143], v12\n\t" \
   "s_waitcnt lgkmcnt(0)\n\t" "v_min3_u32 v140, v140, v141, v142\n\t" "v_min_u32 v140, v140, v143\n\t" "v_cmp_ge_u32 vcc, v140, v138\n\t" \
   "s_cbranch_vccz 153b\n\t" "s_branch 53b\n\t" "9:\n\t" "s_waitcnt vmcnt(0)"
#define KQ_CLOB "v8", "v9", "v10", "v11", "v12", "v13", "v14", "v15", "v32", "v33", "v34", "v35", "v36", "v37", "v38", "v39", "v40", "v41", "v42", "v43", "v44", "v45", "v46", "v47", "v48", "v49", "v50", "v51", "v52", "v53", "v54", "v55", "v56", "v57", "v58", "v59", "v60", "v61", "v62", "v63", "v64", "v65", "v66", "v67", "v68", "v69", "v70", "v71", "v72", "v73", "v74", "v75", "v76", "v77", "v78", "v79", "v80", "v81", "v82", "v83", "v84", "v85", "v86", "v87", "v88", "v89", "v90", "v91", "v92", "v93", "v94", "v95", "v96", "v97", "v98", "v99", "v100", "v101", "v102", "v103", "v104", "v105", "v106", "v107", "v108", "v109", "v110", "v111", "v112", "v113", "v114", "v115", "v116", "v117", "v118", "v119", "v120", "v121", "v122", "v123", "v124", "v125", "v126", "v127", "v128", "v129", "v130", "v131", "v132", "v133", "v134", "v135", "v136", "v137", "v138", "v139", "v140", "v141", "v142", "v143", "v144", "v145", "v146", "v147", "v148", "v149", "v150", "v151", "v160", "v161", "v162", "v163", "v164", "v165", "v166", "v167", "v168", "v169", "v170", "v171", "v172", "v173", "v174", "v175", "v176", "v177", "v178", "v179", "v180", "v181", "v182", "v183", "v184", "v185", "v186", "v187", "v188", "v189", "v190", "v191", "s40", "s41", "s42", "s43", "s44", "s45", "s46", "s47", "s48", "s49", "s50", "s51", "s52", "s53", "s54", "s55", "s60", "s61"
#define VU_ASM \
   "s_mov_b32 s44, %[rdy]\n\t" "s_mov_b32 s45, 0\n\t" "s_mov_b32 s46, 0\n\t" "s_cmp_eq_u32 s44, -1\n\t" "s_cbranch_scc1 68f\n\t" \
   "s_mul_i32 s52, s45, 1536\n\t" "s_mul_i32 s51, s45, 40960\n\t" "s_add_u32 m0, s46, %[ldx]\n\t" "s_mov_b32 s53, s46\n\t" \
   "buffer_load_dwordx4 v16, %[rs], s51 offen offset:0 sc1 lds\n\t" "buffer_load_dwordx4 v16, %[rs], s51 offen offset:1024 sc1 lds\n\t" \
   "buffer_load_dwordx4 v16, %[rs], s51 offen offset:2048 sc1 lds\n\t" "buffer_load_dwordx4 v16, %[rs], s51 offen offset:3072 sc1 lds\n\t" \
   "s_add_u32 m0, m0, 4096\n\t" "s_add_u32 s51, s51, 4096\n\t" "buffer_load_dwordx4 v16, %[rs], s51 offen offset:0 sc1 lds\n\t" \
   "buffer_load_dwordx4 v16, %[rs], s51 offen offset:1024 sc1 lds\n\t" "buffer_load_dwordx4 v16, %[rs], s51 offen offset:2048 sc1 lds\n\t" \
   "buffer_load_dwordx4 v16, %[rs], s51 offen offset:3072 sc1 lds\n\t" "s_add_u32 m0, m0, 4096\n\t" "s_add_u32 s51, s51, 4096\n\t" \
   "buffer_load_dwordx4 v16, %[rs], s51 offen offset:0 sc1 lds\n\t" "buffer_load_dwordx4 v16, %[rs], s51 offen offset:1024 sc1 lds\n\t" \
   "s_add_u32 m0, s53, %[ld1]\n\t" "s_nop 0\n\t" "buffer_load_dwordx4 v17, %[rs], s52 offen sc1 lds\n\t" "s_mov_b32 s45, 1\n\t" \
   "s_mov_b32 s46, 44032\n\t" "s_mul_i32 s52, s45, 1536\n\t" "s_mul_i32 s51, s45, 40960\n\t" "s_add_u32 m0, s46, %[ldx]\n\t" "s_mov_b32 s53, s46\n\t" \
   "buffer_load_dwordx4 v16, %[rs], s51 offen offset:0 sc1 lds\n\t" "buffer_load_dwordx4 v16, %[rs], s51 offen offset:1024 sc1 lds\n\t" \
   "buffer_load_dwordx4 v16, %[rs], s51 offen offset:2048 sc1 lds\n\t" "buffer_load_dwordx4 v16, %[rs], s51 offen offset:3072 sc1 lds\n\t" \
   "s_add_u32 m0, m0, 4096\n\t" "s_add_u32 s51, s51, 4096\n\t" "buffer_load_dwordx4 v16, %[rs], s51 offen offset:0 sc1 lds\n\t" \
   "buffer_load_dwordx4 v16, %[rs], s51 offen offset:1024 sc1 lds\n\t" "buffer_load_dwordx4 v16, %[rs], s51 offen offset:2048 sc1 lds\n\t" \
   "buffer_load_dwordx4 v16, %[rs], s51 offen offset:3072 sc1 lds\n\t" "s_add_u32 m0, m0, 4096\n\t" "s_add_u32 s51, s51, 4096\n\t" \
   "buffer_load_dwordx4 v16, %[rs], s51 offen offset:0 sc1 lds\n\t" "buffer_load_dwordx4 v16, %[rs], s51 offen offset:1024 sc1 lds\n\t" \
   "s_add_u32 m0, s53, %[ld1]\n\t" "s_nop 0\n\t" "buffer_load_dwordx4 v17, %[rs], s52 offen sc1 lds\n\t" "68:\n\t" "v_mov_b32 v160, 0x3f803f80\n\t" \
   "v_mov_b32 v161, 0x3f803f80\n\t" "v_mov_b32 v162, 0x3f803f80\n\t" "v_mov_b32 v163, 0x3f803f80\n\t" "v_mov_b32 v31, 1\n\t" "v_mov_b32 v138, 0\n\t" \
   "v_mov_b32 v13, %[flw]\n\t" "v_and_b32 v12, -16, v13\n\t" "ds_write_b32 v13, v138\n\t" \
   "v_mfma_f32_32x32x16_bf16 v[32:47], a[0:3], v[160:163], 0\n\t" "v_mfma_f32_32x32x16_bf16 v[32:47], a[4:7], v[160:163], v[32:47]\n\t" \
   "v_mfma_f32_32x32x16_bf16 v[32:47], a[8:11], v[160:163], v[32:47]\n\t" "v_mfma_f32_32x32x16_bf16 v[32:47], a[12:15], v[160:163], v[32:47]\n\t" \
   "v_mfma_f32_32x32x16_bf16 v[32:47], a[16:19], v[160:163], v[32:47]\n\t" "v_mfma_f32_32x32x16_bf16 v[32:47], a[20:23], v[160:163], v[32:47]\n\t" \
   "v_mfma_f32_32x32x16_bf16 v[32:47], a[24:27], v[160:163], v[32:47]\n\t" "v_mfma_f32_32x32x16_bf16 v[32:47], a[28:31], v[160:163], v[32:47]\n\t" \
   "v_mfma_f32_32x32x16_bf16 v[32:47], a[32:35], v[160:163], v[32:47]\n\t" "v_mfma_f32_32x32x16_bf16 v[32:47], a[36:39], v[160:163], v[32:47]\n\t" \
   "v_mfma_f32_32x32x16_bf16 v[32:47], a[40:43], v[160:163], v[32:47]\n\t" "v_mfma_f32_32x32x16_bf16 v[32:47], a[44:47], v[160:163], v[32:47]\n\t" \
   "v_mfma_f32_32x32x16_bf16 v[32:47], a[48:51], v[160:163], v[32:47]\n\t" "v_mfma_f32_32x32x16_bf16 v[32:47], a[52:55], v[160:163], v[32:47]\n\t" \
   "v_mfma_f32_32x32x16_bf16 v[32:47], a[56:59], v[160:163], v[32:47]\n\t" "v_mfma_f32_32x32x16_bf16 v[32:47], a[60:63], v[160:163], v[32:47]\n\t" \
   "v_mfma_f32_32x32x16_bf16 v[32:47], a[64:67], v[160:163], v[32:47]\n\t" "v_mfma_f32_32x32x16_bf16 v[32:47], a[68:71], v[160:163], v[32:47]\n\t" \
   "v_mfma_f32_32x32x16_bf16 v[32:47], a[72:75], v[160:163], v[32:47]\n\t" "v_mfma_f32_32x32x16_bf16 v[32:47], a[76:79], v[160:163], v[32:47]\n\t" \
   "v_mfma_f32_32x32x16_bf16 v[32:47], a[80:83], v[160:163], v[32:47]\n\t" "v_mfma_f32_32x32x16_bf16 v[32:47], a[84:87], v[160:163], v[32:47]\n\t" \
   "v_mfma_f32_32x32x16_bf16 v[32:47], a[88:91], v[160:163], v[32:47]\n\t" "v_mfma_f32_32x32x16_bf16 v[32:47], a[92:95], v[160:163], v[32:47]\n\t" \
   "v_mfma_f32_32x32x16_bf16 v[32:47], a[96:99], v[160:163], v[32:47]\n\t" "v_mfma_f32_32x32x16_bf16 v[32:47], a[100:103], v[160:163], v[32:47]\n\t" \
   "v_mfma_f32_32x32x16_bf16 v[32:47], a[104:107], v[160:163], v[32:47]\n\t" "v_mfma_f32_32x32x16_bf16 v[32:47], a[108:111], v[160:163], v[32:47]\n\t" \
   "v_mfma_f32_32x32x16_bf16 v[32:47], a[112:115], v[160:163], v[32:47]\n\t" "v_mfma_f32_32x32x16_bf16 v[32:47], a[116:119], v[160:163], v[32:47]\n\t" \
   "v_mfma_f32_32x32x16_bf16 v[32:47], a[120:123], v[160:163], v[32:47]\n\t" "v_mfma_f32_32x32x16_bf16 v[32:47], a[124:127], v[160:163], v[32:47]\n\t" \
   "v_mfma_f32_32x32x16_bf16 v[32:47], a[128:131], v[160:163], v[32:47]\n\t" "v_mfma_f32_32x32x16_bf16 v[32:47], a[132:135], v[160:163], v[32:47]\n\t" \
   "v_mfma_f32_32x32x16_bf16 v[32:47], a[136:139], v[160:163], v[32:47]\n\t" "v_mfma_f32_32x32x16_bf16 v[32:47], a[140:143], v[160:163], v[32:47]\n\t" \
   "v_mfma_f32_32x32x16_bf16 v[32:47], a[144:147], v[160:163], v[32:47]\n\t" "v_mfma_f32_32x32x16_bf16 v[32:47], a[148:151], v[160:163], v[32:47]\n\t" \
   "v_mfma_f32_32x32x16_bf16 v[32:47], a[152:155], v[160:163], v[32:47]\n\t" "v_mfma_f32_32x32x16_bf16 v[32:47], a[156:159], v[160:163], v[32:47]\n\t" \
   "s_mov_b32 s40, 0\n\t" "s_mov_b32 s41, 0\n\t" "s_mov_b32 s42, 0\n\t" "s_mov_b32 s43, 44032\n\t" "v_mov_b32 v28, v22\n\t" "v_mov_b32 v29, v23\n\t" \
   "s_mov_b32 s45, 2\n\t" "s_mov_b32 s46, 88064\n\t" "s_waitcnt vmcnt(11) lgkmcnt(0)\n\t" "s_barrier\n\t" "s_lshr_b32 s48, s45, 1\n\t" \
   "s_bitcmp1_b32 s44, s48\n\t" "s_cbranch_scc0 172f\n\t" "72:\n\t" "ds_read_b128 v[160:163], v28 offset:0\n\t" \
   "ds_read_b128 v[164:167], v28 offset:512\n\t" "ds_read_b128 v[168:171], v28 offset:1024\n\t" "ds_read_b128 v[172:175], v28 offset:1536\n\t" \
   "ds_read_b128 v[176:179], v28 offset:2048\n\t" "ds_read_b128 v[180:183], v28 offset:2560\n\t" "ds_read_b128 v[184:187], v28 offset:3072\n\t" \
   "v_mov_b32 v14, v24\n\t" "v_mov_b32 v15, v25\n\t" "v_mov_b32 v18, 0x7ffffff0\n\t" "s_mov_b32 s49, 0\n\t" "v_mov_b32 v26, v28\n\t" \
   "v_mov_b32 v27, v29\n\t" "s_waitcnt lgkmcnt(5)\n\t" "v_mfma_f32_32x32x16_bf16 v[48:63], a[0:3], v[160:163], 0\n\t" \
   "ds_read_b128 v[188:191], v26 offset:3584\n\t" "s_mul_i32 s52, s45, 1536\n\t" "s_mul_i32 s51, s45, 40960\n\t" "s_add_u32 m0, s46, %[ldx]\n\t" \
   "s_mov_b32 s53, s46\n\t" "v_mfma_f32_32x32x16_bf16 v[48:63], a[4:7], v[164:167], v[48:63]\n\t" "ds_read_b128 v[160:163], v26 offset:4096\n\t" \
   "ds_read_b128 v[128:131], v27 offset:0\n\t" "ds_read_b128 v[132:135], v27 offset:16\n\t" "s_waitcnt lgkmcnt(7)\n\t" \
   "v_mfma_f32_32x32x16_bf16 v[48:63], a[8:11], v[168:171], v[48:63]\n\t" "ds_read_b128 v[164:167], v26 offset:4608\n\t" \
   "ds_read_b64 v[136:137], v27 offset:32\n\t" "v_add_u32 v28, s43, v22\n\t" "v_add_u32 v29, s43, v23\n\t" \
   "v_mfma_f32_32x32x16_bf16 v[48:63], a[12:15], v[172:175], v[48:63]\n\t" "ds_read_b128 v[168:171], v26 offset:5120\n\t" \
   "buffer_load_dwordx4 v16, %[rs], s51 offen offset:0 sc1 lds\n\t" "s_waitcnt lgkmcnt(8)\n\t" \
   "v_mfma_f32_32x32x16_bf16 v[48:63], a[16:19], v[176:179], v[48:63]\n\t" "ds_read_b128 v[172:175], v26 offset:5632\n\t" \
   "v_mfma_f32_32x32x16_bf16 v[48:63], a[20:23], v[180:183], v[48:63]\n\t" "ds_read_b128 v[176:179], v26 offset:6144\n\t" "s_waitcnt lgkmcnt(3)\n\t" \
   "v_add_f32 v128, v128, v130\n\t" "v_add_f32 v129, v129, v131\n\t" "v_add_f32 v132, v132, v134\n\t" \
   "v_mfma_f32_32x32x16_bf16 v[48:63], a[24:27], v[184:187], v[48:63]\n\t" "ds_read_b128 v[180:183], v26 offset:6656\n\t" \
   "buffer_load_dwordx4 v16, %[rs], s51 offen offset:1024 sc1 lds\n\t" "v_add_f32 v133, v133, v135\n\t" \
   "v_mfma_f32_32x32x16_bf16 v[48:63], a[28:31], v[188:191], v[48:63]\n\t" "ds_read_b128 v[184:187], v26 offset:7168\n\t" \
   "v_add_f32 v128, v128, v136\n\t" "v_add_f32 v129, v129, v137\n\t" "v_add_f32 v128, v128, v132\n\t" \
   "v_mfma_f32_32x32x16_bf16 v[48:63], a[32:35], v[160:163], v[48:63]\n\t" "ds_read_b128 v[188:191], v26 offset:7680\n\t" \
   "v_add_f32 v129, v129, v133\n\t" "v_mul_f32 v128, 0xbacccccd, v128\n\t" "v_mul_f32 v129, 0x3acccccd, v129\n\t" \
   "v_fma_f32 v129, -v128, v128, v129\n\t" "v_mfma_f32_32x32x16_bf16 v[48:63], a[36:39], v[164:167], v[48:63]\n\t" \
   "ds_read_b128 v[160:163], v26 offset:8192\n\t" "buffer_load_dwordx4 v16, %[rs], s51 offen offset:2048 sc1 lds\n\t" "s_waitcnt lgkmcnt(5)\n\t" \
   "v_mfma_f32_32x32x16_bf16 v[48:63], a[40:43], v[168:171], v[48:63]\n\t" "ds_read_b128 v[164:167], v26 offset:8704\n\t" "v_max_f32 v129, 0, v129\n\t" \
   "v_cvt_pk_bf16_f32 v129, v129, v128\n\t" "v_and_b32 v8, 0xffff0000, v129\n\t" "v_lshlrev_b32 v129, 16, v129\n\t" \
   "v_mfma_f32_32x32x16_bf16 v[48:63], a[44:47], v[172:175], v[48:63]\n\t" "ds_read_b128 v[168:171], v26 offset:9216\n\t" \
   "v_add_f32 v129, 0x3727c5ac, v129\n\t" "v_cvt_pk_bf16_f32 v129, v129, v129\n\t" "v_lshlrev_b32 v129, 16, v129\n\t" "s_waitcnt lgkmcnt(5)\n\t" \
   "v_mfma_f32_32x32x16_bf16 v[48:63], a[48:51], v[176:179], v[48:63]\n\t" "ds_read_b128 v[172:175], v26 offset:9728\n\t" "v_sqrt_f32 v129, v129\n\t" \
   "s_nop 0\n\t" "v_cvt_pk_bf16_f32 v129, v129, v129\n\t" "v_mfma_f32_32x32x16_bf16 v[48:63], a[52:55], v[180:183], v[48:63]\n\t" \
   "ds_read_b128 v[176:179], v26 offset:10240\n\t" "v_lshlrev_b32 v129, 16, v129\n\t" "v_rcp_f32 v10, v129\n\t" "s_waitcnt lgkmcnt(5)\n\t" \
   "v_mfma_f32_32x32x16_bf16 v[48:63], a[56:59], v[184:187], v[48:63]\n\t" "ds_read_b128 v[180:183], v26 offset:10752\n\t" \
   "buffer_load_dwordx4 v16, %[rs], s51 offen offset:3072 sc1 lds\n\t" "s_add_u32 m0, m0, 4096\n\t" "s_add_u32 s51, s51, 4096\n\t" \
   "v_mfma_f32_32x32x16_bf16 v[48:63], a[60:63], v[188:191], v[48:63]\n\t" "ds_read_b128 v[184:187], v26 offset:11264\n\t" \
   "buffer_load_dwordx4 v16, %[rs], s51 offen offset:0 sc1 lds\n\t" "s_waitcnt lgkmcnt(5)\n\t" \
   "v_mfma_f32_32x32x16_bf16 v[48:63], a[64:67], v[160:163], v[48:63]\n\t" "ds_read_b128 v[188:191], v26 offset:11776\n\t" \
   "v_mfma_f32_32x32x16_bf16 v[48:63], a[68:71], v[164:167], v[48:63]\n\t" "ds_read_b128 v[160:163], v26 offset:12288\n\t" "s_waitcnt lgkmcnt(5)\n\t" \
   "v_mfma_f32_32x32x16_bf16 v[48:63], a[72:75], v[168:171], v[48:63]\n\t" "ds_read_b128 v[164:167], v26 offset:12800\n\t" \
   "buffer_load_dwordx4 v16, %[rs], s51 offen offset:1024 sc1 lds\n\t" "v_mfma_f32_32x32x16_bf16 v[48:63], a[76:79], v[172:175], v[48:63]\n\t" \
   "ds_read_b128 v[168:171], v26 offset:13312\n\t" "s_waitcnt lgkmcnt(5)\n\t" "v_mfma_f32_32x32x16_bf16 v[48:63], a[80:83], v[176:179], v[48:63]\n\t" \
   "ds_read_b128 v[172:175], v26 offset:13824\n\t" "v_mfma_f32_32x32x16_bf16 v[48:63], a[84:87], v[180:183], v[48:63]\n\t" \
   "ds_read_b128 v[176:179], v26 offset:14336\n\t" "buffer_load_dwordx4 v16, %[rs], s51 offen offset:2048 sc1 lds\n\t" "s_waitcnt lgkmcnt(5)\n\t" \
   "v_mfma_f32_32x32x16_bf16 v[48:63], a[88:91], v[184:187], v[48:63]\n\t" "ds_read_b128 v[180:183], v26 offset:14848\n\t" \
   "v_mfma_f32_32x32x16_bf16 v[48:63], a[92:95], v[188:191], v[48:63]\n\t" "ds_read_b128 v[184:187], v26 offset:15360\n\t" "s_waitcnt lgkmcnt(5)\n\t" \
   "v_mfma_f32_32x32x16_bf16 v[48:63], a[96:99], v[160:163], v[48:63]\n\t" "ds_read_b128 v[188:191], v26 offset:15872\n\t" \
   "v_mfma_f32_32x32x16_bf16 v[48:63], a[100:103], v[164:167], v[48:63]\n\t" "ds_read_b128 v[160:163], v26 offset:16384\n\t" "s_waitcnt lgkmcnt(5)\n\t" \
   "v_mfma_f32_32x32x16_bf16 v[48:63], a[104:107], v[168:171], v[48:63]\n\t" "ds_read_b128 v[164:167], v26 offset:16896\n\t" \
   "buffer_load_dwordx4 v16, %[rs], s51 offen offset:3072 sc1 lds\n\t" "s_add_u32 m0, m0, 4096\n\t" "s_add_u32 s51, s51, 4096\n\t" \
   "v_mfma_f32_32x32x16_bf16 v[48:63], a[108:111], v[172:175], v[48:63]\n\t" "ds_read_b128 v[168:171], v26 offset:17408\n\t" \
   "buffer_load_dwordx4 v16, %[rs], s51 offen offset:0 sc1 lds\n\t" "s_waitcnt lgkmcnt(5)\n\t" \
   "v_mfma_f32_32x32x16_bf16 v[48:63], a[112:115], v[176:179], v[48:63]\n\t" "ds_read_b128 v[172:175], v26 offset:17920\n\t" \
   "v_mfma_f32_32x32x16_bf16 v[48:63], a[116:119], v[180:183], v[48:63]\n\t" "ds_read_b128 v[176:179], v26 offset:18432\n\t" "s_waitcnt lgkmcnt(5)\n\t" \
   "v_mfma_f32_32x32x16_bf16 v[48:63], a[120:123], v[184:187], v[48:63]\n\t" "ds_read_b128 v[180:183], v26 offset:18944\n\t" \
   "v_mfma_f32_32x32x16_bf16 v[48:63], a[124:127], v[188:191], v[48:63]\n\t" "ds_read_b128 v[184:187], v26 offset:19456\n\t" "s_waitcnt lgkmcnt(5)\n\t" \
   "v_mfma_f32_32x32x16_bf16 v[48:63], a[128:131], v[160:163], v[48:63]\n\t" "ds_read_b128 v[188:191], v26 offset:19968\n\t" \
   "buffer_load_dwordx4 v16, %[rs], s51 offen offset:1024 sc1 lds\n\t" "s_add_u32 m0, s53, %[ld1]\n\t" "s_nop 0\n\t" \
   "v_mfma_f32_32x32x16_bf16 v[48:63], a[132:135], v[164:167], v[48:63]\n\t" "buffer_load_dwordx4 v17, %[rs], s52 offen sc1 lds\n\t" \
   "s_waitcnt lgkmcnt(4)\n\t" "v_mfma_f32_32x32x16_bf16 v[48:63], a[136:139], v[168:171], v[48:63]\n\t" \
   "v_mfma_f32_32x32x16_bf16 v[48:63], a[140:143], v[172:175], v[48:63]\n\t" "s_waitcnt lgkmcnt(2)\n\t" \
   "v_mfma_f32_32x32x16_bf16 v[48:63], a[144:147], v[176:179], v[48:63]\n\t" "v_mfma_f32_32x32x16_bf16 v[48:63], a[148:151], v[180:183], v[48:63]\n\t" \
   "s_waitcnt lgkmcnt(0)\n\t" "v_mfma_f32_32x32x16_bf16 v[48:63], a[152:155], v[184:187], v[48:63]\n\t" \
   "v_mfma_f32_32x32x16_bf16 v[48:63], a[156:159], v[188:191], v[48:63]\n\t" "s_memrealtime s[54:55]\n\t" "s_nop 7\n\t" "s_nop 2\n\t" \
   "v_fmac_f32 v48, v32, v8\n\t" "v_fmac_f32 v49, v33, v8\n\t" "v_fmac_f32 v50, v34, v8\n\t" "v_fmac_f32 v51, v35, v8\n\t" \
   "v_fmac_f32 v52, v36, v8\n\t" "v_fmac_f32 v53, v37, v8\n\t" "v_fmac_f32 v54, v38, v8\n\t" "v_fmac_f32 v55, v39, v8\n\t" \
   "v_fmac_f32 v56, v40, v8\n\t" "v_fmac_f32 v57, v41, v8\n\t" "v_fmac_f32 v58, v42, v8\n\t" "v_fmac_f32 v59, v43, v8\n\t" \
   "v_fmac_f32 v60, v44, v8\n\t" "v_fmac_f32 v61, v45, v8\n\t" "v_fmac_f32 v62, v46, v8\n\t" "v_fmac_f32 v63, v47, v8\n\t" \
   "v_mul_f32 v48, v48, v10\n\t" "v_mul_f32 v49, v49, v10\n\t" "v_mul_f32 v50, v50, v10\n\t" "v_mul_f32 v51, v51, v10\n\t" \
   "v_mul_f32 v52, v52, v10\n\t" "v_mul_f32 v53, v53, v10\n\t" "v_mul_f32 v54, v54, v10\n\t" "v_mul_f32 v55, v55, v10\n\t" \
   "v_mul_f32 v56, v56, v10\n\t" "v_mul_f32 v57, v57, v10\n\t" "v_mul_f32 v58, v58, v10\n\t" "v_mul_f32 v59, v59, v10\n\t" \
   "v_mul_f32 v60, v60, v10\n\t" "v_mul_f32 v61, v61, v10\n\t" "v_mul_f32 v62, v62, v10\n\t" "v_mul_f32 v63, v63, v10\n\t" \
   "v_cvt_pk_bf16_f32 v48, v48, v49\n\t" "v_cvt_pk_bf16_f32 v49, v50, v51\n\t" "v_cvt_pk_bf16_f32 v50, v52, v53\n\t" \
   "v_cvt_pk_bf16_f32 v51, v54, v55\n\t" "ds_write_b128 v14, v[48:51] offset:0\n\t" "v_cvt_pk_bf16_f32 v56, v56, v57\n\t" \
   "v_cvt_pk_bf16_f32 v57, v58, v59\n\t" "v_cvt_pk_bf16_f32 v58, v60, v61\n\t" "v_cvt_pk_bf16_f32 v59, v62, v63\n\t" \
   "ds_write_b128 v14, v[56:59] offset:1024\n\t" "s_waitcnt vmcnt(11) lgkmcnt(0)\n\t" "s_barrier\n\t" "ds_read_b128 v[160:163], v28 offset:0\n\t" \
   "ds_read_b128 v[164:167], v28 offset:512\n\t" "ds_read_b128 v[168:171], v28 offset:1024\n\t" "ds_read_b128 v[172:175], v28 offset:1536\n\t" \
   "ds_read_b128 v[176:179], v28 offset:2048\n\t" "ds_read_b128 v[180:183], v28 offset:2560\n\t" "ds_read_b128 v[184:187], v28 offset:3072\n\t" \
   "s_add_u32 s45, s40, 3\n\t" "s_min_u32 s45, s45, 63\n\t" "s_mov_b32 s46, s42\n\t" "s_lshr_b32 s48, s45, 1\n\t" "s_bitcmp1_b32 s44, s48\n\t" \
   "s_cbranch_scc0 176f\n\t" "76:\n\t" "s_mov_b32 s42, s43\n\t" "s_add_u32 s43, s43, 44032\n\t" "s_cmp_eq_u32 s43, 132096\n\t" \
   "s_cselect_b32 s43, 0, s43\n\t" "s_add_u32 s41, s41, 1\n\t" "s_add_u32 s40, s40, 1\n\t" "v_xor_b32 v14, 0x2000, v14\n\t" \
   "v_xor_b32 v15, 0x2000, v15\n\t" "v_mov_b32 v31, s54\n\t" "v_min_u32 v31, -2, v31\n\t" "1:\n\t" "v_mov_b32 v26, v28\n\t" "v_mov_b32 v27, v29\n\t" \
   "s_waitcnt lgkmcnt(5)\n\t" "v_mfma_f32_32x32x16_bf16 v[48:63], a[0:3], v[160:163], 0\n\t" "ds_read_b128 v[188:191], v26 offset:3584\n\t" \
   "ds_read_b128 v[156:159], v12\n\t" "v_mfma_f32_32x32x16_bf16 v[48:63], a[4:7], v[164:167], v[48:63]\n\t" \
   "ds_read_b128 v[160:163], v26 offset:4096\n\t" "s_add_u32 s50, s49, 262144\n\t" "v_cvt_pk_bf16_f32 v140, v140, v141\n\t" \
   "v_cvt_pk_bf16_f32 v141, v148, v149\n\t" "s_waitcnt lgkmcnt(6)\n\t" "v_mfma_f32_32x32x16_bf16 v[48:63], a[8:11], v[168:171], v[48:63]\n\t" \
   "ds_read_b128 v[164:167], v26 offset:4608\n\t" "s_waitcnt lgkmcnt(2)\n\t" "v_min3_u32 v156, v156, v157, v158\n\t" "v_min_u32 v156, v156, v159\n\t" \
   "v_cmp_ge_u32 vcc, v156, v138\n\t" "v_mfma_f32_32x32x16_bf16 v[48:63], a[12:15], v[172:175], v[48:63]\n\t" \
   "ds_read_b128 v[168:171], v26 offset:5120\n\t" "s_cbranch_vccz 150f\n\t" "50:\n\t" "ds_read_b128 v[96:99], v15 offset:0\n\t" \
   "v_mfma_f32_32x32x16_bf16 v[48:63], a[16:19], v[176:179], v[48:63]\n\t" "ds_read_b128 v[172:175], v26 offset:5632\n\t" \
   "ds_read_b128 v[100:103], v15 offset:1024\n\t" "ds_read_b128 v[104:107], v15 offset:2048\n\t" "ds_read_b128 v[108:111], v15 offset:3072\n\t" \
   "v_mfma_f32_32x32x16_bf16 v[48:63], a[20:23], v[180:183], v[48:63]\n\t" "ds_read_b128 v[176:179], v26 offset:6144\n\t" \
   "ds_read_b128 v[112:115], v15 offset:4096\n\t" "ds_read_b128 v[116:119], v15 offset:5120\n\t" \
   "v_mfma_f32_32x32x16_bf16 v[48:63], a[24:27], v[184:187], v[48:63]\n\t" "ds_read_b128 v[180:183], v26 offset:6656\n\t" \
   "ds_read_b128 v[120:123], v15 offset:6144\n\t" "ds_read_b128 v[124:127], v15 offset:7168\n\t" "s_mul_i32 s52, s45, 1536\n\t" \
   "s_mul_i32 s51, s45, 40960\n\t" "s_add_u32 m0, s46, %[ldx]\n\t" "s_mov_b32 s53, s46\n\t" \
   "v_mfma_f32_32x32x16_bf16 v[48:63], a[28:31], v[188:191], v[48:63]\n\t" "ds_read_b128 v[184:187], v26 offset:7168\n\t" \
   "v_cvt_pk_bf16_f32 v142, v142, v143\n\t" "v_cvt_pk_bf16_f32 v143, v150, v151\n\t" "s_waitcnt lgkmcnt(9)\n\t" \
   "v_mfma_f32_32x32x16_bf16 v[64:79], v[96:99], a[160:163], 0\n\t" "buffer_store_dwordx4 v[140:143], v18, %[rs], s50 offen offset:0 sc1\n\t" \
   "v_mfma_f32_32x32x16_bf16 v[48:63], a[32:35], v[160:163], v[48:63]\n\t" "ds_read_b128 v[188:191], v26 offset:7680\n\t" \
   "v_cvt_pk_bf16_f32 v144, v144, v145\n\t" "v_cvt_pk_bf16_f32 v145, v152, v153\n\t" "v_cvt_pk_bf16_f32 v146, v146, v147\n\t" \
   "v_cvt_pk_bf16_f32 v147, v154, v155\n\t" "v_mfma_f32_32x32x16_bf16 v[48:63], a[36:39], v[164:167], v[48:63]\n\t" \
   "ds_read_b128 v[160:163], v26 offset:8192\n\t" "v_add_u32 v28, s43, v22\n\t" "v_add_u32 v29, s43, v23\n\t" \
   "v_mfma_f32_32x32x16_bf16 v[64:79], v[100:103], a[176:179], v[64:79]\n\t" \
   "buffer_store_dwordx4 v[144:147], v18, %[rs], s50 offen offset:512 sc1\n\t" "s_sub_i32 s49, s41, 1\n\t" "s_mul_i32 s49, s49, 1310720\n\t" \
   "v_mfma_f32_32x32x16_bf16 v[48:63], a[40:43], v[168:171], v[48:63]\n\t" "ds_read_b128 v[164:167], v26 offset:8704\n\t" \
   "ds_read_b128 v[128:131], v27 offset:0\n\t" "buffer_load_dwordx4 v16, %[rs], s51 offen offset:0 sc1 lds\n\t" \
   "v_mfma_f32_32x32x16_bf16 v[48:63], a[44:47], v[172:175], v[48:63]\n\t" "ds_read_b128 v[168:171], v26 offset:9216\n\t" \
   "ds_read_b128 v[132:135], v27 offset:16\n\t" "ds_read_b64 v[136:137], v27 offset:32\n\t" "s_waitcnt lgkmcnt(14)\n\t" \
   "v_mfma_f32_32x32x16_bf16 v[64:79], v[104:107], a[164:167], v[64:79]\n\t" "s_waitcnt lgkmcnt(10)\n\t" \
   "v_mfma_f32_32x32x16_bf16 v[48:63], a[48:51], v[176:179], v[48:63]\n\t" "ds_read_b128 v[172:175], v26 offset:9728\n\t" \
   "v_mfma_f32_32x32x16_bf16 v[48:63], a[52:55], v[180:183], v[48:63]\n\t" "ds_read_b128 v[176:179], v26 offset:10240\n\t" \
   "buffer_load_dwordx4 v16, %[rs], s51 offen offset:1024 sc1 lds\n\t" "v_mfma_f32_32x32x16_bf16 v[64:79], v[108:111], a[180:183], v[64:79]\n\t" \
   "s_waitcnt lgkmcnt(2)\n\t" "v_add_f32 v128, v128, v130\n\t" "v_add_f32 v129, v129, v131\n\t" "v_add_f32 v132, v132, v134\n\t" \
   "v_add_f32 v133, v133, v135\n\t" "v_mfma_f32_32x32x16_bf16 v[48:63], a[56:59], v[184:187], v[48:63]\n\t" \
   "ds_read_b128 v[180:183], v26 offset:10752\n\t" "v_add_f32 v128, v128, v136\n\t" "v_add_f32 v129, v129, v137\n\t" "v_add_f32 v128, v128, v132\n\t" \
   "v_add_f32 v129, v129, v133\n\t" "v_mfma_f32_32x32x16_bf16 v[48:63], a[60:63], v[188:191], v[48:63]\n\t" \
   "ds_read_b128 v[184:187], v26 offset:11264\n\t" "v_mul_f32 v128, 0xbacccccd, v128\n\t" "v_mul_f32 v129, 0x3acccccd, v129\n\t" \
   "v_fma_f32 v129, -v128, v128, v129\n\t" "v_max_f32 v129, 0, v129\n\t" "v_mfma_f32_32x32x16_bf16 v[64:79], v[112:115], a[168:171], v[64:79]\n\t" \
   "buffer_load_dwordx4 v16, %[rs], s51 offen offset:2048 sc1 lds\n\t" "v_cvt_pk_bf16_f32 v129, v129, v128\n\t" "v_and_b32 v8, 0xffff0000, v129\n\t" \
   "v_mfma_f32_32x32x16_bf16 v[48:63], a[64:67], v[160:163], v[48:63]\n\t" "ds_read_b128 v[188:191], v26 offset:11776\n\t" \
   "v_lshlrev_b32 v129, 16, v129\n\t" "v_add_f32 v129, 0x3727c5ac, v129\n\t" "v_cvt_pk_bf16_f32 v129, v129, v129\n\t" \
   "v_lshlrev_b32 v129, 16, v129\n\t" "v_mfma_f32_32x32x16_bf16 v[48:63], a[68:71], v[164:167], v[48:63]\n\t" \
   "ds_read_b128 v[160:163], v26 offset:12288\n\t" "v_sqrt_f32 v129, v129\n\t" "s_nop 0\n\t" "v_cvt_pk_bf16_f32 v129, v129, v129\n\t" \
   "v_mfma_f32_32x32x16_bf16 v[64:79], v[116:119], a[184:187], v[64:79]\n\t" "v_lshlrev_b32 v129, 16, v129\n\t" "v_rcp_f32 v10, v129\n\t" \
   "s_waitcnt lgkmcnt(5)\n\t" "v_mfma_f32_32x32x16_bf16 v[48:63], a[72:75], v[168:171], v[48:63]\n\t" "ds_read_b128 v[164:167], v26 offset:12800\n\t" \
   "v_mfma_f32_32x32x16_bf16 v[48:63], a[76:79], v[172:175], v[48:63]\n\t" "ds_read_b128 v[168:171], v26 offset:13312\n\t" \
   "v_mfma_f32_32x32x16_bf16 v[64:79], v[120:123], a[172:175], v[64:79]\n\t" "buffer_load_dwordx4 v16, %[rs], s51 offen offset:3072 sc1 lds\n\t" \
   "s_add_u32 m0, m0, 4096\n\t" "s_add_u32 s51, s51, 4096\n\t" "s_waitcnt lgkmcnt(5)\n\t" \
   "v_mfma_f32_32x32x16_bf16 v[48:63], a[80:83], v[176:179], v[48:63]\n\t" "ds_read_b128 v[172:175], v26 offset:13824\n\t" \
   "v_mfma_f32_32x32x16_bf16 v[48:63], a[84:87], v[180:183], v[48:63]\n\t" "ds_read_b128 v[176:179], v26 offset:14336\n\t" \
   "buffer_load_dwordx4 v16, %[rs], s51 offen offset:0 sc1 lds\n\t" "v_mfma_f32_32x32x16_bf16 v[64:79], v[124:127], a[188:191], v[64:79]\n\t" \
   "s_waitcnt lgkmcnt(5)\n\t" "v_mfma_f32_32x32x16_bf16 v[48:63], a[88:91], v[184:187], v[48:63]\n\t" "ds_read_b128 v[180:183], v26 offset:14848\n\t" \
   "v_mfma_f32_32x32x16_bf16 v[48:63], a[92:95], v[188:191], v[48:63]\n\t" "ds_read_b128 v[184:187], v26 offset:15360\n\t" "s_add_u32 s50, s49, 0\n\t" \
   "v_cvt_pk_bf16_f32 v64, v64, v65\n\t" "v_cvt_pk_bf16_f32 v65, v72, v73\n\t" "v_cvt_pk_bf16_f32 v66, v66, v67\n\t" \
   "v_mfma_f32_32x32x16_bf16 v[80:95], v[96:99], a[192:195], 0\n\t" "v_cvt_pk_bf16_f32 v67, v74, v75\n\t" \
   "buffer_load_dwordx4 v16, %[rs], s51 offen offset:1024 sc1 lds\n\t" "s_waitcnt lgkmcnt(5)\n\t" \
   "v_mfma_f32_32x32x16_bf16 v[48:63], a[96:99], v[160:163], v[48:63]\n\t" "ds_read_b128 v[188:191], v26 offset:15872\n\t" \
   "v_mfma_f32_32x32x16_bf16 v[48:63], a[100:103], v[164:167], v[48:63]\n\t" "ds_read_b128 v[160:163], v26 offset:16384\n\t" \
   "v_mfma_f32_32x32x16_bf16 v[80:95], v[100:103], a[208:211], v[80:95]\n\t" "buffer_store_dwordx4 v[64:67], v21, %[rs], s50 offen offset:0 sc1\n\t" \
   "s_waitcnt lgkmcnt(5)\n\t" "v_mfma_f32_32x32x16_bf16 v[48:63], a[104:107], v[168:171], v[48:63]\n\t" "ds_read_b128 v[164:167], v26 offset:16896\n\t" \
   "v_cvt_pk_bf16_f32 v68, v68, v69\n\t" "v_cvt_pk_bf16_f32 v69, v76, v77\n\t" "v_cvt_pk_bf16_f32 v70, v70, v71\n\t" \
   "v_cvt_pk_bf16_f32 v71, v78, v79\n\t" "v_mfma_f32_32x32x16_bf16 v[48:63], a[108:111], v[172:175], v[48:63]\n\t" \
   "ds_read_b128 v[168:171], v26 offset:17408\n\t" "s_waitcnt vmcnt(9)\n\t" "v_add_u32 v138, 1, v138\n\t" "ds_write_b32 v13, v138\n\t" \
   "v_mfma_f32_32x32x16_bf16 v[80:95], v[104:107], a[196:199], v[80:95]\n\t" "buffer_store_dwordx4 v[68:71], v21, %[rs], s50 offen offset:512 sc1\n\t" \
   "s_waitcnt lgkmcnt(6)\n\t" "v_mfma_f32_32x32x16_bf16 v[48:63], a[112:115], v[176:179], v[48:63]\n\t" "ds_read_b128 v[172:175], v26 offset:17920\n\t" \
   "buffer_load_dwordx4 v16, %[rs], s51 offen offset:2048 sc1 lds\n\t" "v_mfma_f32_32x32x16_bf16 v[48:63], a[116:119], v[180:183], v[48:63]\n\t" \
   "ds_read_b128 v[176:179], v26 offset:18432\n\t" "v_mfma_f32_32x32x16_bf16 v[80:95], v[108:111], a[212:215], v[80:95]\n\t" \
   "buffer_load_dwordx4 v16, %[rs], s51 offen offset:3072 sc1 lds\n\t" "s_add_u32 m0, m0, 4096\n\t" "s_add_u32 s51, s51, 4096\n\t" \
   "s_waitcnt lgkmcnt(6)\n\t" "v_mfma_f32_32x32x16_bf16 v[48:63], a[120:123], v[184:187], v[48:63]\n\t" "ds_read_b128 v[180:183], v26 offset:18944\n\t" \
   "v_mfma_f32_32x32x16_bf16 v[48:63], a[124:127], v[188:191], v[48:63]\n\t" "ds_read_b128 v[184:187], v26 offset:19456\n\t" \
   "v_mfma_f32_32x32x16_bf16 v[80:95], v[112:115], a[200:203], v[80:95]\n\t" "buffer_load_dwordx4 v16, %[rs], s51 offen offset:0 sc1 lds\n\t" \
   "s_waitcnt lgkmcnt(6)\n\t" "v_mfma_f32_32x32x16_bf16 v[48:63], a[128:131], v[160:163], v[48:63]\n\t" "ds_read_b128 v[188:191], v26 offset:19968\n\t" \
   "v_mfma_f32_32x32x16_bf16 v[48:63], a[132:135], v[164:167], v[48:63]\n\t" "v_mfma_f32_32x32x16_bf16 v[80:95], v[116:119], a[216:219], v[80:95]\n\t" \
   "s_waitcnt lgkmcnt(4)\n\t" "v_mfma_f32_32x32x16_bf16 v[48:63], a[136:139], v[168:171], v[48:63]\n\t" "s_cmp_lt_i32 s40, 2\n\t" \
   "s_cselect_b32 s47, 0x7ffffff0, 0\n\t" "v_or_b32 v30, s47, v20\n\t" "s_sub_i32 s47, s41, 2\n\t" "s_max_i32 s47, s47, 0\n\t" \
   "s_lshl_b32 s47, s47, 12\n\t" "v_mfma_f32_32x32x16_bf16 v[48:63], a[140:143], v[172:175], v[48:63]\n\t" "s_waitcnt vmcnt(11)\n\t" \
   "buffer_load_dwordx4 v16, %[rs], s51 offen offset:1024 sc1 lds\n\t" "s_add_u32 m0, s53, %[ld1]\n\t" "s_nop 0\n\t" \
   "v_mfma_f32_32x32x16_bf16 v[80:95], v[120:123], a[204:207], v[80:95]\n\t" "buffer_store_dword v31, v30, %[rs], s47 offen sc1\n\t" \
   "s_waitcnt lgkmcnt(2)\n\t" "v_mfma_f32_32x32x16_bf16 v[48:63], a[144:147], v[176:179], v[48:63]\n\t" "ds_read_b128 v[156:159], v12\n\t" \
   "v_mfma_f32_32x32x16_bf16 v[48:63], a[148:151], v[180:183], v[48:63]\n\t" "buffer_load_dwordx4 v17, %[rs], s52 offen sc1 lds\n\t" \
   "v_mfma_f32_32x32x16_bf16 v[80:95], v[124:127], a[220:223], v[80:95]\n\t" "s_waitcnt lgkmcnt(1)\n\t" \
   "v_mfma_f32_32x32x16_bf16 v[48:63], a[152:155], v[184:187], v[48:63]\n\t" "s_waitcnt lgkmcnt(0)\n\t" "v_min3_u32 v156, v156, v157, v158\n\t" \
   "v_min_u32 v156, v156, v159\n\t" "v_cmp_ge_u32 vcc, v156, v138\n\t" "s_cbranch_vccz 152f\n\t" "52:\n\t" \
   "v_mfma_f32_32x32x16_bf16 v[48:63], a[156:159], v[188:191], v[48:63]\n\t" "s_add_u32 s50, s49, 65536\n\t" "v_cvt_pk_bf16_f32 v80, v80, v81\n\t" \
   "v_cvt_pk_bf16_f32 v81, v88, v89\n\t" "v_cvt_pk_bf16_f32 v82, v82, v83\n\t" "v_cvt_pk_bf16_f32 v83, v90, v91\n\t" \
   "v_mfma_f32_32x32x16_bf16 v[64:79], v[96:99], a[224:227], 0\n\t" "buffer_store_dwordx4 v[80:83], v21, %[rs], s50 offen offset:0 sc1\n\t" \
   "v_cvt_pk_bf16_f32 v84, v84, v85\n\t" "v_mfma_f32_32x32x16_bf16 v[64:79], v[100:103], a[240:243], v[64:79]\n\t" "v_fmac_f32 v48, v32, v8\n\t" \
   "v_fmac_f32 v49, v33, v8\n\t" "v_fmac_f32 v50, v34, v8\n\t" "v_fmac_f32 v51, v35, v8\n\t" "v_fmac_f32 v52, v36, v8\n\t" \
   "v_fmac_f32 v53, v37, v8\n\t" "v_mfma_f32_32x32x16_bf16 v[64:79], v[104:107], a[228:231], v[64:79]\n\t" "v_fmac_f32 v54, v38, v8\n\t" \
   "v_fmac_f32 v55, v39, v8\n\t" "v_fmac_f32 v56, v40, v8\n\t" "v_fmac_f32 v57, v41, v8\n\t" "v_fmac_f32 v58, v42, v8\n\t" \
   "v_fmac_f32 v59, v43, v8\n\t" "v_mfma_f32_32x32x16_bf16 v[64:79], v[108:111], a[244:247], v[64:79]\n\t" "v_fmac_f32 v60, v44, v8\n\t" \
   "v_fmac_f32 v61, v45, v8\n\t" "v_fmac_f32 v62, v46, v8\n\t" "v_fmac_f32 v63, v47, v8\n\t" "v_mul_f32 v48, v48, v10\n\t" \
   "v_mul_f32 v49, v49, v10\n\t" "v_mfma_f32_32x32x16_bf16 v[64:79], v[112:115], a[232:235], v[64:79]\n\t" "v_mul_f32 v50, v50, v10\n\t" \
   "v_mul_f32 v51, v51, v10\n\t" "v_mul_f32 v52, v52, v10\n\t" "v_mul_f32 v53, v53, v10\n\t" "v_mul_f32 v54, v54, v10\n\t" \
   "v_mul_f32 v55, v55, v10\n\t" "v_mfma_f32_32x32x16_bf16 v[64:79], v[116:119], a[248:251], v[64:79]\n\t" "v_mul_f32 v56, v56, v10\n\t" \
   "v_mul_f32 v57, v57, v10\n\t" "v_mul_f32 v58, v58, v10\n\t" "v_mul_f32 v59, v59, v10\n\t" "v_mul_f32 v60, v60, v10\n\t" \
   "v_mul_f32 v61, v61, v10\n\t" "v_mfma_f32_32x32x16_bf16 v[64:79], v[120:123], a[236:239], v[64:79]\n\t" "v_cvt_pk_bf16_f32 v85, v92, v93\n\t" \
   "v_cvt_pk_bf16_f32 v86, v86, v87\n\t" "v_cvt_pk_bf16_f32 v87, v94, v95\n\t" \
   "buffer_store_dwordx4 v[84:87], v21, %[rs], s50 offen offset:512 sc1\n\t" "v_mfma_f32_32x32x16_bf16 v[64:79], v[124:127], a[252:255], v[64:79]\n\t" \
   "v_mul_f32 v62, v62, v10\n\t" "v_mul_f32 v63, v63, v10\n\t" "v_cvt_pk_bf16_f32 v48, v48, v49\n\t" "v_cvt_pk_bf16_f32 v49, v50, v51\n\t" \
   "v_cvt_pk_bf16_f32 v50, v52, v53\n\t" "v_cvt_pk_bf16_f32 v51, v54, v55\n\t" "v_mfma_f32_32x32x16_bf16 v[80:95], v[96:99], v[192:195], 0\n\t" \
   "ds_write_b128 v14, v[48:51] offset:0\n\t" "v_cvt_pk_bf16_f32 v56, v56, v57\n\t" "v_cvt_pk_bf16_f32 v57, v58, v59\n\t" \
   "v_mfma_f32_32x32x16_bf16 v[80:95], v[100:103], v[208:211], v[80:95]\n\t" "v_cvt_pk_bf16_f32 v58, v60, v61\n\t" \
   "v_cvt_pk_bf16_f32 v59, v62, v63\n\t" "ds_write_b128 v14, v[56:59] offset:1024\n\t" "v_add_u32 v138, 1, v138\n\t" "ds_write_b32 v13, v138\n\t" \
   "v_mfma_f32_32x32x16_bf16 v[80:95], v[104:107], v[196:199], v[80:95]\n\t" "s_add_u32 s50, s49, 131072\n\t" "v_cvt_pk_bf16_f32 v64, v64, v65\n\t" \
   "v_cvt_pk_bf16_f32 v65, v72, v73\n\t" "v_cvt_pk_bf16_f32 v66, v66, v67\n\t" "v_cvt_pk_bf16_f32 v67, v74, v75\n\t" "s_add_u32 s45, s40, 3\n\t" \
   "v_mfma_f32_32x32x16_bf16 v[80:95], v[108:111], v[212:215], v[80:95]\n\t" "buffer_store_dwordx4 v[64:67], v21, %[rs], s50 offen offset:0 sc1\n\t" \
   "v_cvt_pk_bf16_f32 v68, v68, v69\n\t" "v_mfma_f32_32x32x16_bf16 v[80:95], v[112:115], v[200:203], v[80:95]\n\t" \
   "v_cvt_pk_bf16_f32 v69, v76, v77\n\t" "v_cvt_pk_bf16_f32 v70, v70, v71\n\t" "v_cvt_pk_bf16_f32 v71, v78, v79\n\t" "s_min_u32 s45, s45, 63\n\t" \
   "s_mov_b32 s46, s42\n\t" "v_mfma_f32_32x32x16_bf16 v[80:95], v[116:119], v[216:219], v[80:95]\n\t" \
   "buffer_store_dwordx4 v[68:71], v21, %[rs], s50 offen offset:512 sc1\n\t" "v_mfma_f32_32x32x16_bf16 v[80:95], v[120:123], v[204:207], v[80:95]\n\t" \
   "s_lshr_b32 s48, s45, 1\n\t" "s_bitcmp1_b32 s44, s48\n\t" "s_cbranch_scc0 174f\n\t" "74:\n\t" "ds_read_b128 v[160:163], v28 offset:0\n\t" \
   "ds_read_b128 v[164:167], v28 offset:512\n\t" "v_mfma_f32_32x32x16_bf16 v[80:95], v[124:127], v[220:223], v[80:95]\n\t" "s_mov_b32 s42, s43\n\t" \
   "ds_read_b128 v[168:171], v28 offset:1024\n\t" "s_add_u32 s43, s43, 44032\n\t" "s_cmp_eq_u32 s43, 132096\n\t" "s_cselect_b32 s43, 0, s43\n\t" \
   "v_mfma_f32_32x32x16_bf16 v[140:155], v[96:99], v[224:227], 0\n\t" "ds_read_b128 v[172:175], v28 offset:1536\n\t" \
   "ds_read_b128 v[176:179], v28 offset:2048\n\t" "s_add_u32 s41, s41, 1\n\t" "s_add_u32 s40, s40, 1\n\t" "v_xor_b32 v14, 0x2000, v14\n\t" \
   "v_mfma_f32_32x32x16_bf16 v[140:155], v[100:103], v[240:243], v[140:155]\n\t" "s_add_u32 s50, s49, 196608\n\t" "v_cvt_pk_bf16_f32 v80, v80, v81\n\t" \
   "v_cvt_pk_bf16_f32 v81, v88, v89\n\t" "v_cvt_pk_bf16_f32 v82, v82, v83\n\t" "v_cvt_pk_bf16_f32 v83, v90, v91\n\t" "v_xor_b32 v15, 0x2000, v15\n\t" \
   "v_mfma_f32_32x32x16_bf16 v[140:155], v[104:107], v[228:231], v[140:155]\n\t" \
   "buffer_store_dwordx4 v[80:83], v21, %[rs], s50 offen offset:0 sc1\n\t" "v_cvt_pk_bf16_f32 v84, v84, v85\n\t" \
   "v_mfma_f32_32x32x16_bf16 v[140:155], v[108:111], v[244:247], v[140:155]\n\t" "v_cvt_pk_bf16_f32 v85, v92, v93\n\t" \
   "v_cvt_pk_bf16_f32 v86, v86, v87\n\t" "v_cvt_pk_bf16_f32 v87, v94, v95\n\t" "ds_read_b128 v[180:183], v28 offset:2560\n\t" \
   "ds_read_b128 v[184:187], v28 offset:3072\n\t" "v_mfma_f32_32x32x16_bf16 v[140:155], v[112:115], v[232:235], v[140:155]\n\t" \
   "buffer_store_dwordx4 v[84:87], v21, %[rs], s50 offen offset:512 sc1\n\t" "v_mov_b32 v18, v21\n\t" \
   "v_mfma_f32_32x32x16_bf16 v[140:155], v[116:119], v[248:251], v[140:155]\n\t" \
   "v_mfma_f32_32x32x16_bf16 v[140:155], v[120:123], v[236:239], v[140:155]\n\t" \
   "v_mfma_f32_32x32x16_bf16 v[140:155], v[124:127], v[252:255], v[140:155]\n\t" "s_cmp_lt_u32 s40, 64\n\t" "s_cbranch_scc1 1b\n\t" "s_nop 7\n\t" \
   "s_nop 2\n\t" "s_waitcnt lgkmcnt(0)\n\t" "s_barrier\n\t" "s_add_u32 s50, s49, 262144\n\t" "v_cvt_pk_bf16_f32 v140, v140, v141\n\t" \
   "v_cvt_pk_bf16_f32 v141, v148, v149\n\t" "v_cvt_pk_bf16_f32 v142, v142, v143\n\t" "v_cvt_pk_bf16_f32 v143, v150, v151\n\t" \
   "buffer_store_dwordx4 v[140:143], v21, %[rs], s50 offen offset:0 sc1\n\t" "v_cvt_pk_bf16_f32 v144, v144, v145\n\t" \
   "v_cvt_pk_bf16_f32 v145, v152, v153\n\t" "v_cvt_pk_bf16_f32 v146, v146, v147\n\t" "v_cvt_pk_bf16_f32 v147, v154, v155\n\t" \
   "buffer_store_dwordx4 v[144:147], v21, %[rs], s50 offen offset:512 sc1\n\t" "s_sub_i32 s49, s41, 1\n\t" "s_mul_i32 s49, s49, 1310720\n\t" \
   "ds_read_b128 v[96:99], v15 offset:0\n\t" "ds_read_b128 v[100:103], v15 offset:1024\n\t" "ds_read_b128 v[104:107], v15 offset:2048\n\t" \
   "ds_read_b128 v[108:111], v15 offset:3072\n\t" "ds_read_b128 v[112:115], v15 offset:4096\n\t" "ds_read_b128 v[116:119], v15 offset:5120\n\t" \
   "ds_read_b128 v[120:123], v15 offset:6144\n\t" "ds_read_b128 v[124:127], v15 offset:7168\n\t" "s_waitcnt lgkmcnt(6)\n\t" \
   "v_mfma_f32_32x32x16_bf16 v[64:79], v[96:99], a[160:163], 0\n\t" "v_mfma_f32_32x32x16_bf16 v[64:79], v[100:103], a[176:179], v[64:79]\n\t" \
   "s_waitcnt lgkmcnt(4)\n\t" "v_mfma_f32_32x32x16_bf16 v[64:79], v[104:107], a[164:167], v[64:79]\n\t" \
   "v_mfma_f32_32x32x16_bf16 v[64:79], v[108:111], a[180:183], v[64:79]\n\t" "s_waitcnt lgkmcnt(2)\n\t" \
   "v_mfma_f32_32x32x16_bf16 v[64:79], v[112:115], a[168:171], v[64:79]\n\t" "v_mfma_f32_32x32x16_bf16 v[64:79], v[116:119], a[184:187], v[64:79]\n\t" \
   "s_waitcnt lgkmcnt(0)\n\t" "v_mfma_f32_32x32x16_bf16 v[64:79], v[120:123], a[172:175], v[64:79]\n\t" \
   "v_mfma_f32_32x32x16_bf16 v[64:79], v[124:127], a[188:191], v[64:79]\n\t" "v_mfma_f32_32x32x16_bf16 v[80:95], v[96:99], a[192:195], 0\n\t" \
   "v_mfma_f32_32x32x16_bf16 v[80:95], v[100:103], a[208:211], v[80:95]\n\t" "s_add_u32 s50, s49, 0\n\t" "v_cvt_pk_bf16_f32 v64, v64, v65\n\t" \
   "v_cvt_pk_bf16_f32 v65, v72, v73\n\t" "v_cvt_pk_bf16_f32 v66, v66, v67\n\t" "v_cvt_pk_bf16_f32 v67, v74, v75\n\t" \
   "v_mfma_f32_32x32x16_bf16 v[80:95], v[104:107], a[196:199], v[80:95]\n\t" "buffer_store_dwordx4 v[64:67], v21, %[rs], s50 offen offset:0 sc1\n\t" \
   "v_cvt_pk_bf16_f32 v68, v68, v69\n\t" "v_mfma_f32_32x32x16_bf16 v[80:95], v[108:111], a[212:215], v[80:95]\n\t" \
   "v_cvt_pk_bf16_f32 v69, v76, v77\n\t" "v_cvt_pk_bf16_f32 v70, v70, v71\n\t" "v_cvt_pk_bf16_f32 v71, v78, v79\n\t" \
   "v_mfma_f32_32x32x16_bf16 v[80:95], v[112:115], a[200:203], v[80:95]\n\t" "buffer_store_dwordx4 v[68:71], v21, %[rs], s50 offen offset:512 sc1\n\t" \
   "v_mfma_f32_32x32x16_bf16 v[80:95], v[116:119], a[216:219], v[80:95]\n\t" "v_mfma_f32_32x32x16_bf16 v[80:95], v[120:123], a[204:207], v[80:95]\n\t" \
   "v_mfma_f32_32x32x16_bf16 v[80:95], v[124:127], a[220:223], v[80:95]\n\t" "v_mfma_f32_32x32x16_bf16 v[64:79], v[96:99], a[224:227], 0\n\t" \
   "v_mfma_f32_32x32x16_bf16 v[64:79], v[100:103], a[240:243], v[64:79]\n\t" "s_add_u32 s50, s49, 65536\n\t" "v_cvt_pk_bf16_f32 v80, v80, v81\n\t" \
   "v_cvt_pk_bf16_f32 v81, v88, v89\n\t" "v_cvt_pk_bf16_f32 v82, v82, v83\n\t" "v_cvt_pk_bf16_f32 v83, v90, v91\n\t" \
   "v_mfma_f32_32x32x16_bf16 v[64:79], v[104:107], a[228:231], v[64:79]\n\t" "buffer_store_dwordx4 v[80:83], v21, %[rs], s50 offen offset:0 sc1\n\t" \
   "v_cvt_pk_bf16_f32 v84, v84, v85\n\t" "v_mfma_f32_32x32x16_bf16 v[64:79], v[108:111], a[244:247], v[64:79]\n\t" \
   "v_cvt_pk_bf16_f32 v85, v92, v93\n\t" "v_cvt_pk_bf16_f32 v86, v86, v87\n\t" "v_cvt_pk_bf16_f32 v87, v94, v95\n\t" \
   "v_mfma_f32_32x32x16_bf16 v[64:79], v[112:115], a[232:235], v[64:79]\n\t" "buffer_store_dwordx4 v[84:87], v21, %[rs], s50 offen offset:512 sc1\n\t" \
   "v_mfma_f32_32x32x16_bf16 v[64:79], v[116:119], a[248:251], v[64:79]\n\t" "v_mfma_f32_32x32x16_bf16 v[64:79], v[120:123], a[236:239], v[64:79]\n\t" \
   "v_mfma_f32_32x32x16_bf16 v[64:79], v[124:127], a[252:255], v[64:79]\n\t" "v_mfma_f32_32x32x16_bf16 v[80:95], v[96:99], v[192:195], 0\n\t" \
   "v_mfma_f32_32x32x16_bf16 v[80:95], v[100:103], v[208:211], v[80:95]\n\t" "s_add_u32 s50, s49, 131072\n\t" "v_cvt_pk_bf16_f32 v64, v64, v65\n\t" \
   "v_cvt_pk_bf16_f32 v65, v72, v73\n\t" "v_cvt_pk_bf16_f32 v66, v66, v67\n\t" "v_cvt_pk_bf16_f32 v67, v74, v75\n\t" \
   "v_mfma_f32_32x32x16_bf16 v[80:95], v[104:107], v[196:199], v[80:95]\n\t" "buffer_store_dwordx4 v[64:67], v21, %[rs], s50 offen offset:0 sc1\n\t" \
   "v_cvt_pk_bf16_f32 v68, v68, v69\n\t" "v_mfma_f32_32x32x16_bf16 v[80:95], v[108:111], v[212:215], v[80:95]\n\t" \
   "v_cvt_pk_bf16_f32 v69, v76, v77\n\t" "v_cvt_pk_bf16_f32 v70, v70, v71\n\t" "v_cvt_pk_bf16_f32 v71, v78, v79\n\t" \
   "v_mfma_f32_32x32x16_bf16 v[80:95], v[112:115], v[200:203], v[80:95]\n\t" "buffer_store_dwordx4 v[68:71], v21, %[rs], s50 offen offset:512 sc1\n\t" \
   "v_mfma_f32_32x32x16_bf16 v[80:95], v[116:119], v[216:219], v[80:95]\n\t" "v_mfma_f32_32x32x16_bf16 v[80:95], v[120:123], v[204:207], v[80:95]\n\t" \
   "v_mfma_f32_32x32x16_bf16 v[80:95], v[124:127], v[220:223], v[80:95]\n\t" "v_mfma_f32_32x32x16_bf16 v[140:155], v[96:99], v[224:227], 0\n\t" \
   "v_mfma_f32_32x32x16_bf16 v[140:155], v[100:103], v[240:243], v[140:155]\n\t" "s_add_u32 s50, s49, 196608\n\t" "v_cvt_pk_bf16_f32 v80, v80, v81\n\t" \
   "v_cvt_pk_bf16_f32 v81, v88, v89\n\t" "v_cvt_pk_bf16_f32 v82, v82, v83\n\t" "v_cvt_pk_bf16_f32 v83, v90, v91\n\t" \
   "v_mfma_f32_32x32x16_bf16 v[140:155], v[104:107], v[228:231], v[140:155]\n\t" \
   "buffer_store_dwordx4 v[80:83], v21, %[rs], s50 offen offset:0 sc1\n\t" "v_cvt_pk_bf16_f32 v84, v84, v85\n\t" \
   "v_mfma_f32_32x32x16_bf16 v[140:155], v[108:111], v[244:247], v[140:155]\n\t" "v_cvt_pk_bf16_f32 v85, v92, v93\n\t" \
   "v_cvt_pk_bf16_f32 v86, v86, v87\n\t" "v_cvt_pk_bf16_f32 v87, v94, v95\n\t" \
   "v_mfma_f32_32x32x16_bf16 v[140:155], v[112:115], v[232:235], v[140:155]\n\t" \
   "buffer_store_dwordx4 v[84:87], v21, %[rs], s50 offen offset:512 sc1\n\t" \
   "v_mfma_f32_32x32x16_bf16 v[140:155], v[116:119], v[248:251], v[140:155]\n\t" \
   "v_mfma_f32_32x32x16_bf16 v[140:155], v[120:123], v[236:239], v[140:155]\n\t" \
   "v_mfma_f32_32x32x16_bf16 v[140:155], v[124:127], v[252:255], v[140:155]\n\t" "s_nop 7\n\t" "s_nop 2\n\t" "s_add_u32 s50, s49, 262144\n\t" \
   "v_cvt_pk_bf16_f32 v140, v140, v141\n\t" "v_cvt_pk_bf16_f32 v141, v148, v149\n\t" "v_cvt_pk_bf16_f32 v142, v142, v143\n\t" \
   "v_cvt_pk_bf16_f32 v143, v150, v151\n\t" "buffer_store_dwordx4 v[140:143], v21, %[rs], s50 offen offset:0 sc1\n\t" \
   "v_cvt_pk_bf16_f32 v144, v144, v145\n\t" "v_cvt_pk_bf16_f32 v145, v152, v153\n\t" "v_cvt_pk_bf16_f32 v146, v146, v147\n\t" \
   "v_cvt_pk_bf16_f32 v147, v154, v155\n\t" "buffer_store_dwordx4 v[144:147], v21, %[rs], s50 offen offset:512 sc1\n\t" "s_waitcnt vmcnt(0)\n\t" \
   "s_cmp_lt_i32 s40, 2\n\t" "s_cselect_b32 s47, 0x7ffffff0, 0\n\t" "v_or_b32 v30, s47, v20\n\t" "s_sub_i32 s47, s41, 2\n\t" \
   "s_max_i32 s47, s47, 0\n\t" "s_lshl_b32 s47, s47, 12\n\t" "buffer_store_dword v31, v30, %[rs], s47 offen sc1\n\t" "s_cmp_lt_i32 s40, 1\n\t" \
   "s_cselect_b32 s47, 0x7ffffff0, 0\n\t" "v_or_b32 v30, s47, v20\n\t" "s_sub_i32 s47, s41, 1\n\t" "s_max_i32 s47, s47, 0\n\t" \
   "s_lshl_b32 s47, s47, 12\n\t" "buffer_store_dword v31, v30, %[rs], s47 offen sc1\n\t" "s_branch 9f\n\t" "172:\n\t" "s_mov_b32 s47, 0\n\t" \
   "buffer_load_dword v30, v19, %[rs], 0 offen sc1\n\t" "s_waitcnt vmcnt(0)\n\t" "v_add_u32 v30, 1, v30\n\t" "v_cmp_lt_u32 vcc, 7, v30\n\t" \
   "s_mov_b32 s44, vcc_lo\n\t" "s_bitcmp1_b32 s44, s48\n\t" "s_cbranch_scc1 72b\n\t" "s_bitset1_b32 s44, s48\n\t" "s_lshl_b32 s48, s48, 7\n\t" \
   "s_add_u32 s48, s48, %[cnt]\n\t" "73:\n\t" "s_sleep 10\n\t" "buffer_load_dword v30, off, %[rs], s48 sc1\n\t" "s_add_u32 s47, s47, 1\n\t" \
   "s_waitcnt vmcnt(0)\n\t" "v_add_u32 v30, 1, v30\n\t" "v_cmp_lt_u32 vcc, 7, v30\n\t" "s_cmp_gt_u32 s47, 0x20000\n\t" "s_cbranch_scc1 72b\n\t" \
   "s_cbranch_vccz 73b\n\t" "s_branch 72b\n\t" "176:\n\t" "s_mov_b32 s47, 0\n\t" "buffer_load_dword v30, v19, %[rs], 0 offen sc1\n\t" \
   "s_waitcnt vmcnt(0)\n\t" "v_add_u32 v30, 1, v30\n\t" "v_cmp_lt_u32 vcc, 7, v30\n\t" "s_mov_b32 s44, vcc_lo\n\t" "s_bitcmp1_b32 s44, s48\n\t" \
   "s_cbranch_scc1 76b\n\t" "s_bitset1_b32 s44, s48\n\t" "s_lshl_b32 s48, s48, 7\n\t" "s_add_u32 s48, s48, %[cnt]\n\t" "77:\n\t" "s_sleep 10\n\t" \
   "buffer_load_dword v30, off, %[rs], s48 sc1\n\t" "s_add_u32 s47, s47, 1\n\t" "s_waitcnt vmcnt(0)\n\t" "v_add_u32 v30, 1, v30\n\t" \
   "v_cmp_lt_u32 vcc, 7, v30\n\t" "s_cmp_gt_u32 s47, 0x20000\n\t" "s_cbranch_scc1 76b\n\t" "s_cbranch_vccz 77b\n\t" "s_branch 76b\n\t" "174:\n\t" \
   "s_mov_b32 s47, 0\n\t" "buffer_load_dword v30, v19, %[rs], 0 offen sc1\n\t" "s_waitcnt vmcnt(0)\n\t" "v_add_u32 v30, 1, v30\n\t" \
   "v_cmp_lt_u32 vcc, 7, v30\n\t" "s_mov_b32 s44, vcc_lo\n\t" "s_bitcmp1_b32 s44, s48\n\t" "s_cbranch_scc1 74b\n\t" "s_bitset1_b32 s44, s48\n\t" \
   "s_lshl_b32 s48, s48, 7\n\t" "s_add_u32 s48, s48, %[cnt]\n\t" "75:\n\t" "s_sleep 10\n\t" "buffer_load_dword v30, off, %[rs], s48 sc1\n\t" \
   "s_add_u32 s47, s47, 1\n\t" "s_waitcnt vmcnt(0)\n\t" "v_add_u32 v30, 1, v30\n\t" "v_cmp_lt_u32 vcc, 7, v30\n\t" "s_cmp_gt_u32 s47, 0x20000\n\t" \
   "s_cbranch_scc1 74b\n\t" "s_cbranch_vccz 75b\n\t" "s_branch 74b\n\t" "150:\n\t" "ds_read_b128 v[156:159], v12\n\t" "s_waitcnt lgkmcnt(0)\n\t" \
   "v_min3_u32 v156, v156, v157, v158\n\t" "v_min_u32 v156, v156, v159\n\t" "v_cmp_ge_u32 vcc, v156, v138\n\t" "s_cbranch_vccz 150b\n\t" \
   "s_branch 50b\n\t" "152:\n\t" "ds_read_b128 v[156:159], v12\n\t" "s_waitcnt lgkmcnt(0)\n\t" "v_min3_u32 v156, v156, v157, v158\n\t" \
   "v_min_u32 v156, v156, v159\n\t" "v_cmp_ge_u32 vcc, v156, v138\n\t" "s_cbranch_vccz 152b\n\t" "s_branch 52b\n\t" "9:\n\t" "s_waitcnt vmcnt(0)"
#define VU_CLOB "v8", "v9", "v10", "v11", "v12", "v13", "v14", "v15", "v32", "v33", "v34", "v35", "v36", "v37", "v38", "v39", "v40", "v41", "v42", "v43", "v44", "v45", "v46", "v47", "v48", "v49", "v50", "v51", "v52", "v53", "v54", "v55", "v56", "v57", "v58", "v59", "v60", "v61", "v62", "v63", "v64", "v65", "v66", "v67", "v68", "v69", "v70", "v71", "v72", "v73", "v74", "v75", "v76", "v77", "v78", "v79", "v80", "v81", "v82", "v83", "v84", "v85", "v86", "v87", "v88", "v89", "v90", "v91", "v92", "v93", "v94", "v95", "v96", "v97", "v98", "v99", "v100", "v101", "v102", "v103", "v104", "v105", "v106", "v107", "v108", "v109", "v110", "v111", "v112", "v113", "v114", "v115", "v116", "v117", "v118", "v119", "v120", "v121", "v122", "v123", "v124", "v125", "v126", "v127", "v128", "v129", "v130", "v131", "v132", "v133", "v134", "v135", "v136", "v137", "v138", "v139", "v140", "v141", "v142", "v143", "v144", "v145", "v146", "v147", "v148", "v149", "v150", "v151", "v152", "v153", "v154", "v155", "v156", "v157", "v158", "v159", "v160", "v161", "v162", "v163", "v164", "v165", "v166", "v167", "v168", "v169", "v170", "v171", "v172", "v173", "v174", "v175", "v176", "v177", "v178", "v179", "v180", "v181", "v182", "v183", "v184", "v185", "v186", "v187", "v188", "v189", "v190", "v191", "s40", "s41", "s42", "s43", "s44", "s45", "s46", "s47", "s48", "s49", "s50", "s51", "s52", "s53", "s54", "s55"
#define DF_ASM \
   "v_mov_b32 v80, 0x3f803f80\n\t" "v_mov_b32 v81, 0x3f803f80\n\t" "v_mov_b32 v82, 0x3f803f80\n\t" "v_mov_b32 v83, 0x3f803f80\n\t" \
   "s_mov_b32 s45, %[set]\n\t" "s_mov_b32 s46, 0\n\t" "v_mfma_f32_16x16x32_bf16 v[32:35], a[0:3], v[80:83], 0\n\t" \
   "v_mfma_f32_16x16x32_bf16 v[32:35], a[4:7], v[80:83], v[32:35]\n\t" "v_mfma_f32_16x16x32_bf16 v[32:35], a[8:11], v[80:83], v[32:35]\n\t" \
   "v_mfma_f32_16x16x32_bf16 v[32:35], a[12:15], v[80:83], v[32:35]\n\t" "v_mfma_f32_16x16x32_bf16 v[32:35], a[16:19], v[80:83], v[32:35]\n\t" \
   "v_mfma_f32_16x16x32_bf16 v[32:35], a[20:23], v[80:83], v[32:35]\n\t" "v_mfma_f32_16x16x32_bf16 v[32:35], a[24:27], v[80:83], v[32:35]\n\t" \
   "v_mfma_f32_16x16x32_bf16 v[32:35], a[28:31], v[80:83], v[32:35]\n\t" "v_mfma_f32_16x16x32_bf16 v[32:35], a[32:35], v[80:83], v[32:35]\n\t" \
   "v_mfma_f32_16x16x32_bf16 v[32:35], a[36:39], v[80:83], v[32:35]\n\t" "v_mfma_f32_16x16x32_bf16 v[32:35], a[40:43], v[80:83], v[32:35]\n\t" \
   "v_mfma_f32_16x16x32_bf16 v[32:35], a[44:47], v[80:83], v[32:35]\n\t" "v_mfma_f32_16x16x32_bf16 v[32:35], a[48:51], v[80:83], v[32:35]\n\t" \
   "v_mfma_f32_16x16x32_bf16 v[32:35], a[52:55], v[80:83], v[32:35]\n\t" "v_mfma_f32_16x16x32_bf16 v[32:35], a[56:59], v[80:83], v[32:35]\n\t" \
   "v_mfma_f32_16x16x32_bf16 v[32:35], a[60:63], v[80:83], v[32:35]\n\t" "v_mfma_f32_16x16x32_bf16 v[32:35], a[64:67], v[80:83], v[32:35]\n\t" \
   "v_mfma_f32_16x16x32_bf16 v[32:35], a[68:71], v[80:83], v[32:35]\n\t" "v_mfma_f32_16x16x32_bf16 v[32:35], a[72:75], v[80:83], v[32:35]\n\t" \
   "v_mfma_f32_16x16x32_bf16 v[32:35], a[76:79], v[80:83], v[32:35]\n\t" "v_mfma_f32_16x16x32_bf16 v[36:39], a[80:83], v[80:83], 0\n\t" \
   "v_mfma_f32_16x16x32_bf16 v[36:39], a[84:87], v[80:83], v[36:39]\n\t" "v_mfma_f32_16x16x32_bf16 v[36:39], a[88:91], v[80:83], v[36:39]\n\t" \
   "v_mfma_f32_16x16x32_bf16 v[36:39], a[92:95], v[80:83], v[36:39]\n\t" "v_mfma_f32_16x16x32_bf16 v[36:39], a[96:99], v[80:83], v[36:39]\n\t" \
   "v_mfma_f32_16x16x32_bf16 v[36:39], a[100:103], v[80:83], v[36:39]\n\t" "v_mfma_f32_16x16x32_bf16 v[36:39], a[104:107], v[80:83], v[36:39]\n\t" \
   "v_mfma_f32_16x16x32_bf16 v[36:39], a[108:111], v[80:83], v[36:39]\n\t" "v_mfma_f32_16x16x32_bf16 v[36:39], a[112:115], v[80:83], v[36:39]\n\t" \
   "v_mfma_f32_16x16x32_bf16 v[36:39], a[116:119], v[80:83], v[36:39]\n\t" "v_mfma_f32_16x16x32_bf16 v[36:39], a[120:123], v[80:83], v[36:39]\n\t" \
   "v_mfma_f32_16x16x32_bf16 v[36:39], a[124:127], v[80:83], v[36:39]\n\t" "v_mfma_f32_16x16x32_bf16 v[36:39], a[128:131], v[80:83], v[36:39]\n\t" \
   "v_mfma_f32_16x16x32_bf16 v[36:39], a[132:135], v[80:83], v[36:39]\n\t" "v_mfma_f32_16x16x32_bf16 v[36:39], a[136:139], v[80:83], v[36:39]\n\t" \
   "v_mfma_f32_16x16x32_bf16 v[36:39], a[140:143], v[80:83], v[36:39]\n\t" "v_mfma_f32_16x16x32_bf16 v[36:39], a[144:147], v[80:83], v[36:39]\n\t" \
   "v_mfma_f32_16x16x32_bf16 v[36:39], a[148:151], v[80:83], v[36:39]\n\t" "v_mfma_f32_16x16x32_bf16 v[36:39], a[152:155], v[80:83], v[36:39]\n\t" \
   "v_mfma_f32_16x16x32_bf16 v[36:39], a[156:159], v[80:83], v[36:39]\n\t" "1:\n\t" "s_lshl_b32 s48, s45, 7\n\t" "s_mov_b32 s47, 0\n\t" \
   "s_cmp_lg_u32 %[ldx], %[ld0]\n\t" "s_cbranch_scc1 3f\n\t" "2:\n\t" "buffer_load_dword v30, v19, %[rs], s48 offen sc1\n\t" \
   "s_add_u32 s47, s47, 1\n\t" "s_waitcnt vmcnt(0)\n\t" "v_readfirstlane_b32 s44, v30\n\t" "s_add_u32 s44, s44, 1\n\t" "s_cmp_ge_u32 s44, 5\n\t" \
   "s_cbranch_scc1 3f\n\t" "s_cmp_gt_u32 s47, 0x20000\n\t" "s_cbranch_scc1 3f\n\t" "s_sleep 3\n\t" "s_cmp_lg_u32 s44, 0\n\t" "s_cbranch_scc1 2b\n\t" \
   "s_sleep 8\n\t" "s_branch 2b\n\t" "3:\n\t" "s_barrier\n\t" "s_mul_i32 s52, s45, 1536\n\t" "s_mul_i32 s51, s45, 40960\n\t" \
   "s_add_u32 m0, s46, %[ldx]\n\t" "s_mov_b32 s53, s46\n\t" "buffer_load_dwordx4 v16, %[rs], s51 offen offset:0 sc1 lds\n\t" \
   "buffer_load_dwordx4 v16, %[rs], s51 offen offset:1024 sc1 lds\n\t" "buffer_load_dwordx4 v16, %[rs], s51 offen offset:2048 sc1 lds\n\t" \
   "buffer_load_dwordx4 v16, %[rs], s51 offen offset:3072 sc1 lds\n\t" "s_add_u32 m0, m0, 4096\n\t" "s_add_u32 s51, s51, 4096\n\t" \
   "buffer_load_dwordx4 v16, %[rs], s51 offen offset:0 sc1 lds\n\t" "buffer_load_dwordx4 v16, %[rs], s51 offen offset:1024 sc1 lds\n\t" \
   "buffer_load_dwordx4 v16, %[rs], s51 offen offset:2048 sc1 lds\n\t" "buffer_load_dwordx4 v16, %[rs], s51 offen offset:3072 sc1 lds\n\t" \
   "s_add_u32 m0, m0, 4096\n\t" "s_add_u32 s51, s51, 4096\n\t" "buffer_load_dwordx4 v16, %[rs], s51 offen offset:0 sc1 lds\n\t" \
   "buffer_load_dwordx4 v16, %[rs], s51 offen offset:1024 sc1 lds\n\t" "s_add_u32 m0, s53, %[ld1]\n\t" "s_nop 0\n\t" \
   "buffer_load_dwordx4 v17, %[rs], s52 offen sc1 lds\n\t" "v_add_u32 v26, s46, v22\n\t" "v_add_u32 v27, s46, v23\n\t" "s_waitcnt vmcnt(0)\n\t" \
   "s_barrier\n\t" "ds_read_b128 v[80:83], v26 offset:0\n\t" "ds_read_b128 v[84:87], v26 offset:1024\n\t" "ds_read_b128 v[88:91], v26 offset:2048\n\t" \
   "ds_read_b128 v[92:95], v26 offset:3072\n\t" "ds_read_b128 v[96:99], v26 offset:4096\n\t" "ds_read_b128 v[100:103], v26 offset:5120\n\t" \
   "ds_read_b128 v[104:107], v26 offset:6144\n\t" "s_waitcnt lgkmcnt(5)\n\t" "v_mfma_f32_16x16x32_bf16 v[52:55], a[80:83], v[80:83], 0\n\t" \
   "ds_read_b128 v[112:115], v27 offset:0\n\t" "v_mfma_f32_16x16x32_bf16 v[48:51], a[0:3], v[80:83], 0\n\t" \
   "ds_read_b128 v[108:111], v26 offset:7168\n\t" "ds_read_b128 v[116:119], v27 offset:16\n\t" \
   "v_mfma_f32_16x16x32_bf16 v[52:55], a[84:87], v[84:87], v[52:55]\n\t" "ds_read_b64 v[120:121], v27 offset:32\n\t" \
   "v_mfma_f32_16x16x32_bf16 v[48:51], a[4:7], v[84:87], v[48:51]\n\t" "ds_read_b128 v[80:83], v26 offset:8192\n\t" "s_waitcnt lgkmcnt(8)\n\t" \
   "v_mfma_f32_16x16x32_bf16 v[52:55], a[88:91], v[88:91], v[52:55]\n\t" "ds_read_b128 v[122:125], v27 offset:768\n\t" \
   "v_mfma_f32_16x16x32_bf16 v[48:51], a[8:11], v[88:91], v[48:51]\n\t" "ds_read_b128 v[84:87], v26 offset:9216\n\t" \
   "ds_read_b128 v[126:129], v27 offset:784\n\t" "v_mfma_f32_16x16x32_bf16 v[52:55], a[92:95], v[92:95], v[52:55]\n\t" \
   "v_mfma_f32_16x16x32_bf16 v[48:51], a[12:15], v[92:95], v[48:51]\n\t" "ds_read_b128 v[88:91], v26 offset:10240\n\t" \
   "ds_read_b64 v[130:131], v27 offset:800\n\t" "s_waitcnt lgkmcnt(11)\n\t" "v_mfma_f32_16x16x32_bf16 v[52:55], a[96:99], v[96:99], v[52:55]\n\t" \
   "s_lshl_b32 s48, s45, 8\n\t" "v_mfma_f32_16x16x32_bf16 v[48:51], a[16:19], v[96:99], v[48:51]\n\t" "ds_read_b128 v[92:95], v26 offset:11264\n\t" \
   "s_add_u32 s47, s48, %[xs4]\n\t" "v_mfma_f32_16x16x32_bf16 v[52:55], a[100:103], v[100:103], v[52:55]\n\t" \
   "v_mfma_f32_16x16x32_bf16 v[48:51], a[20:23], v[100:103], v[48:51]\n\t" "ds_read_b128 v[96:99], v26 offset:12288\n\t" "s_waitcnt lgkmcnt(8)\n\t" \
   "v_add_f32 v112, v112, v114\n\t" "v_mfma_f32_16x16x32_bf16 v[52:55], a[104:107], v[104:107], v[52:55]\n\t" "v_add_f32 v113, v113, v115\n\t" \
   "v_mfma_f32_16x16x32_bf16 v[48:51], a[24:27], v[104:107], v[48:51]\n\t" "ds_read_b128 v[100:103], v26 offset:13312\n\t" \
   "v_mfma_f32_16x16x32_bf16 v[52:55], a[108:111], v[108:111], v[52:55]\n\t" "v_add_f32 v116, v116, v118\n\t" \
   "v_mfma_f32_16x16x32_bf16 v[48:51], a[28:31], v[108:111], v[48:51]\n\t" "ds_read_b128 v[104:107], v26 offset:14336\n\t" \
   "v_add_f32 v117, v117, v119\n\t" "s_waitcnt lgkmcnt(7)\n\t" "v_mfma_f32_16x16x32_bf16 v[52:55], a[112:115], v[80:83], v[52:55]\n\t" \
   "v_mfma_f32_16x16x32_bf16 v[48:51], a[32:35], v[80:83], v[48:51]\n\t" "ds_read_b128 v[108:111], v26 offset:15360\n\t" \
   "v_add_f32 v112, v112, v120\n\t" "v_mfma_f32_16x16x32_bf16 v[52:55], a[116:119], v[84:87], v[52:55]\n\t" "v_add_f32 v113, v113, v121\n\t" \
   "v_mfma_f32_16x16x32_bf16 v[48:51], a[36:39], v[84:87], v[48:51]\n\t" "ds_read_b128 v[80:83], v26 offset:16384\n\t" "v_add_f32 v112, v112, v116\n\t" \
   "s_waitcnt lgkmcnt(5)\n\t" "v_mfma_f32_16x16x32_bf16 v[52:55], a[120:123], v[88:91], v[52:55]\n\t" \
   "v_mfma_f32_16x16x32_bf16 v[48:51], a[40:43], v[88:91], v[48:51]\n\t" "ds_read_b128 v[84:87], v26 offset:17408\n\t" "v_add_f32 v113, v113, v117\n\t" \
   "v_mfma_f32_16x16x32_bf16 v[52:55], a[124:127], v[92:95], v[52:55]\n\t" "v_mul_f32 v112, 0xbacccccd, v112\n\t" \
   "v_mfma_f32_16x16x32_bf16 v[48:51], a[44:47], v[92:95], v[48:51]\n\t" "ds_read_b128 v[88:91], v26 offset:18432\n\t" "s_waitcnt lgkmcnt(5)\n\t" \
   "v_mfma_f32_16x16x32_bf16 v[52:55], a[128:131], v[96:99], v[52:55]\n\t" "v_mul_f32 v113, 0x3acccccd, v113\n\t" \
   "v_mfma_f32_16x16x32_bf16 v[48:51], a[48:51], v[96:99], v[48:51]\n\t" "ds_read_b128 v[92:95], v26 offset:19456\n\t" \
   "v_fma_f32 v113, -v112, v112, v113\n\t" "v_mfma_f32_16x16x32_bf16 v[52:55], a[132:135], v[100:103], v[52:55]\n\t" "v_max_f32 v113, 0, v113\n\t" \
   "v_mfma_f32_16x16x32_bf16 v[48:51], a[52:55], v[100:103], v[48:51]\n\t" "ds_read_b128 v[96:99], v26 offset:20480\n\t" "s_waitcnt lgkmcnt(5)\n\t" \
   "v_mfma_f32_16x16x32_bf16 v[52:55], a[136:139], v[104:107], v[52:55]\n\t" "v_cvt_pk_bf16_f32 v113, v113, v112\n\t" \
   "v_mfma_f32_16x16x32_bf16 v[48:51], a[56:59], v[104:107], v[48:51]\n\t" "ds_read_b128 v[100:103], v26 offset:21504\n\t" \
   "v_and_b32 v8, 0xffff0000, v113\n\t" "v_mfma_f32_16x16x32_bf16 v[52:55], a[140:143], v[108:111], v[52:55]\n\t" \
   "v_mfma_f32_16x16x32_bf16 v[48:51], a[60:63], v[108:111], v[48:51]\n\t" "ds_read_b128 v[104:107], v26 offset:22528\n\t" \
   "v_lshlrev_b32 v113, 16, v113\n\t" "s_waitcnt lgkmcnt(5)\n\t" "v_mfma_f32_16x16x32_bf16 v[52:55], a[144:147], v[80:83], v[52:55]\n\t" \
   "v_add_f32 v113, 0x3727c5ac, v113\n\t" "v_mfma_f32_16x16x32_bf16 v[48:51], a[64:67], v[80:83], v[48:51]\n\t" \
   "ds_read_b128 v[108:111], v26 offset:23552\n\t" "v_mfma_f32_16x16x32_bf16 v[52:55], a[148:151], v[84:87], v[52:55]\n\t" \
   "v_cvt_pk_bf16_f32 v113, v113, v113\n\t" "v_mfma_f32_16x16x32_bf16 v[48:51], a[68:71], v[84:87], v[48:51]\n\t" \
   "ds_read_b128 v[80:83], v26 offset:24576\n\t" "v_lshlrev_b32 v113, 16, v113\n\t" "s_waitcnt lgkmcnt(5)\n\t" \
   "v_mfma_f32_16x16x32_bf16 v[52:55], a[152:155], v[88:91], v[52:55]\n\t" "v_sqrt_f32 v113, v113\n\t" \
   "v_mfma_f32_16x16x32_bf16 v[48:51], a[72:75], v[88:91], v[48:51]\n\t" "ds_read_b128 v[84:87], v26 offset:25600\n\t" \
   "v_mfma_f32_16x16x32_bf16 v[52:55], a[156:159], v[92:95], v[52:55]\n\t" "s_nop 0\n\t" \
   "v_mfma_f32_16x16x32_bf16 v[48:51], a[76:79], v[92:95], v[48:51]\n\t" "ds_read_b128 v[88:91], v26 offset:26624\n\t" \
   "v_cvt_pk_bf16_f32 v113, v113, v113\n\t" "s_waitcnt lgkmcnt(5)\n\t" "v_mfma_f32_16x16x32_bf16 v[60:63], a[80:83], v[96:99], 0\n\t" \
   "v_mfma_f32_16x16x32_bf16 v[56:59], a[0:3], v[96:99], 0\n\t" "ds_read_b128 v[92:95], v26 offset:27648\n\t" "v_lshlrev_b32 v113, 16, v113\n\t" \
   "v_mfma_f32_16x16x32_bf16 v[60:63], a[84:87], v[100:103], v[60:63]\n\t" "v_rcp_f32 v10, v113\n\t" \
   "v_mfma_f32_16x16x32_bf16 v[56:59], a[4:7], v[100:103], v[56:59]\n\t" "ds_read_b128 v[96:99], v26 offset:28672\n\t" "s_waitcnt lgkmcnt(5)\n\t" \
   "v_mfma_f32_16x16x32_bf16 v[60:63], a[88:91], v[104:107], v[60:63]\n\t" "v_fmac_f32 v48, v32, v8\n\t" "v_fmac_f32 v49, v33, v8\n\t" \
   "v_mfma_f32_16x16x32_bf16 v[56:59], a[8:11], v[104:107], v[56:59]\n\t" "ds_read_b128 v[100:103], v26 offset:29696\n\t" "v_fmac_f32 v50, v34, v8\n\t" \
   "v_mfma_f32_16x16x32_bf16 v[60:63], a[92:95], v[108:111], v[60:63]\n\t" "v_fmac_f32 v51, v35, v8\n\t" \
   "v_mfma_f32_16x16x32_bf16 v[56:59], a[12:15], v[108:111], v[56:59]\n\t" "ds_read_b128 v[104:107], v26 offset:30720\n\t" \
   "v_fmac_f32 v52, v36, v8\n\t" "s_waitcnt lgkmcnt(5)\n\t" "v_mfma_f32_16x16x32_bf16 v[60:63], a[96:99], v[80:83], v[60:63]\n\t" \
   "v_fmac_f32 v53, v37, v8\n\t" "v_fmac_f32 v54, v38, v8\n\t" "v_mfma_f32_16x16x32_bf16 v[56:59], a[16:19], v[80:83], v[56:59]\n\t" \
   "ds_read_b128 v[108:111], v26 offset:31744\n\t" "v_fmac_f32 v55, v39, v8\n\t" \
   "v_mfma_f32_16x16x32_bf16 v[60:63], a[100:103], v[84:87], v[60:63]\n\t" "v_mul_f32 v48, v48, v10\n\t" \
   "v_mfma_f32_16x16x32_bf16 v[56:59], a[20:23], v[84:87], v[56:59]\n\t" "ds_read_b128 v[80:83], v26 offset:32768\n\t" "v_mul_f32 v49, v49, v10\n\t" \
   "s_waitcnt lgkmcnt(5)\n\t" "v_mfma_f32_16x16x32_bf16 v[60:63], a[104:107], v[88:91], v[60:63]\n\t" "v_mul_f32 v50, v50, v10\n\t" \
   "v_mul_f32 v51, v51, v10\n\t" "v_mfma_f32_16x16x32_bf16 v[56:59], a[24:27], v[88:91], v[56:59]\n\t" "ds_read_b128 v[84:87], v26 offset:33792\n\t" \
   "v_mul_f32 v52, v52, v10\n\t" "v_mfma_f32_16x16x32_bf16 v[60:63], a[108:111], v[92:95], v[60:63]\n\t" "v_mul_f32 v53, v53, v10\n\t" \
   "v_mfma_f32_16x16x32_bf16 v[56:59], a[28:31], v[92:95], v[56:59]\n\t" "ds_read_b128 v[88:91], v26 offset:34816\n\t" "v_mul_f32 v54, v54, v10\n\t" \
   "s_waitcnt lgkmcnt(5)\n\t" "v_mfma_f32_16x16x32_bf16 v[60:63], a[112:115], v[96:99], v[60:63]\n\t" "v_mul_f32 v55, v55, v10\n\t" \
   "v_cvt_pk_bf16_f32 v64, v48, v49\n\t" "v_mfma_f32_16x16x32_bf16 v[56:59], a[32:35], v[96:99], v[56:59]\n\t" \
   "ds_read_b128 v[92:95], v26 offset:35840\n\t" "v_cvt_pk_bf16_f32 v65, v50, v51\n\t" \
   "v_mfma_f32_16x16x32_bf16 v[60:63], a[116:119], v[100:103], v[60:63]\n\t" "v_cvt_pk_bf16_f32 v66, v52, v53\n\t" \
   "v_mfma_f32_16x16x32_bf16 v[56:59], a[36:39], v[100:103], v[56:59]\n\t" "ds_read_b128 v[96:99], v26 offset:36864\n\t" \
   "v_cvt_pk_bf16_f32 v67, v54, v55\n\t" "s_waitcnt lgkmcnt(5)\n\t" "v_mfma_f32_16x16x32_bf16 v[60:63], a[120:123], v[104:107], v[60:63]\n\t" \
   "buffer_store_dwordx2 v[64:65], v21, %[rs], s48 offen offset:0 sc1\n\t" "buffer_store_dwordx2 v[66:67], v21, %[rs], s47 offen offset:0 sc1\n\t" \
   "v_mfma_f32_16x16x32_bf16 v[56:59], a[40:43], v[104:107], v[56:59]\n\t" "ds_read_b128 v[100:103], v26 offset:37888\n\t" \
   "v_add_f32 v122, v122, v124\n\t" "v_mfma_f32_16x16x32_bf16 v[60:63], a[124:127], v[108:111], v[60:63]\n\t" "v_add_f32 v123, v123, v125\n\t" \
   "v_mfma_f32_16x16x32_bf16 v[56:59], a[44:47], v[108:111], v[56:59]\n\t" "ds_read_b128 v[104:107], v26 offset:38912\n\t" \
   "v_add_f32 v126, v126, v128\n\t" "s_waitcnt lgkmcnt(5)\n\t" "v_mfma_f32_16x16x32_bf16 v[60:63], a[128:131], v[80:83], v[60:63]\n\t" \
   "v_add_f32 v127, v127, v129\n\t" "v_add_f32 v122, v122, v130\n\t" "v_mfma_f32_16x16x32_bf16 v[56:59], a[48:51], v[80:83], v[56:59]\n\t" \
   "ds_read_b128 v[108:111], v26 offset:39936\n\t" "v_add_f32 v123, v123, v131\n\t" \
   "v_mfma_f32_16x16x32_bf16 v[60:63], a[132:135], v[84:87], v[60:63]\n\t" "v_add_f32 v122, v122, v126\n\t" \
   "v_mfma_f32_16x16x32_bf16 v[56:59], a[52:55], v[84:87], v[56:59]\n\t" "v_add_f32 v123, v123, v127\n\t" "s_waitcnt lgkmcnt(4)\n\t" \
   "v_mfma_f32_16x16x32_bf16 v[60:63], a[136:139], v[88:91], v[60:63]\n\t" "v_mul_f32 v122, 0xbacccccd, v122\n\t" \
   "v_mul_f32 v123, 0x3acccccd, v123\n\t" "v_mfma_f32_16x16x32_bf16 v[56:59], a[56:59], v[88:91], v[56:59]\n\t" "v_fma_f32 v123, -v122, v122, v123\n\t" \
   "v_mfma_f32_16x16x32_bf16 v[60:63], a[140:143], v[92:95], v[60:63]\n\t" "v_max_f32 v123, 0, v123\n\t" \
   "v_mfma_f32_16x16x32_bf16 v[56:59], a[60:63], v[92:95], v[56:59]\n\t" "v_cvt_pk_bf16_f32 v123, v123, v122\n\t" "s_waitcnt lgkmcnt(2)\n\t" \
   "v_mfma_f32_16x16x32_bf16 v[60:63], a[144:147], v[96:99], v[60:63]\n\t" "v_and_b32 v12, 0xffff0000, v123\n\t" "v_lshlrev_b32 v123, 16, v123\n\t" \
   "v_mfma_f32_16x16x32_bf16 v[56:59], a[64:67], v[96:99], v[56:59]\n\t" "v_add_f32 v123, 0x3727c5ac, v123\n\t" \
   "v_mfma_f32_16x16x32_bf16 v[60:63], a[148:151], v[100:103], v[60:63]\n\t" "v_cvt_pk_bf16_f32 v123, v123, v123\n\t" \
   "v_mfma_f32_16x16x32_bf16 v[56:59], a[68:71], v[100:103], v[56:59]\n\t" "v_lshlrev_b32 v123, 16, v123\n\t" "s_waitcnt lgkmcnt(0)\n\t" \
   "v_mfma_f32_16x16x32_bf16 v[60:63], a[152:155], v[104:107], v[60:63]\n\t" "v_sqrt_f32 v123, v123\n\t" "s_nop 0\n\t" \
   "v_mfma_f32_16x16x32_bf16 v[56:59], a[72:75], v[104:107], v[56:59]\n\t" "v_cvt_pk_bf16_f32 v123, v123, v123\n\t" \
   "v_mfma_f32_16x16x32_bf16 v[60:63], a[156:159], v[108:111], v[60:63]\n\t" "v_lshlrev_b32 v123, 16, v123\n\t" \
   "v_mfma_f32_16x16x32_bf16 v[56:59], a[76:79], v[108:111], v[56:59]\n\t" "v_rcp_f32 v132, v123\n\t" "s_xor_b32 s46, s46, 44032\n\t" \
   "s_add_u32 s45, s45, 4\n\t" "s_nop 5\n\t" "v_fmac_f32 v56, v32, v12\n\t" "v_fmac_f32 v57, v33, v12\n\t" "v_fmac_f32 v58, v34, v12\n\t" \
   "v_fmac_f32 v59, v35, v12\n\t" "v_fmac_f32 v60, v36, v12\n\t" "v_fmac_f32 v61, v37, v12\n\t" "v_fmac_f32 v62, v38, v12\n\t" \
   "v_fmac_f32 v63, v39, v12\n\t" "v_mul_f32 v56, v56, v132\n\t" "v_mul_f32 v57, v57, v132\n\t" "v_mul_f32 v58, v58, v132\n\t" \
   "v_mul_f32 v59, v59, v132\n\t" "v_mul_f32 v60, v60, v132\n\t" "v_mul_f32 v61, v61, v132\n\t" "v_mul_f32 v62, v62, v132\n\t" \
   "v_mul_f32 v63, v63, v132\n\t" "v_cvt_pk_bf16_f32 v68, v56, v57\n\t" "v_cvt_pk_bf16_f32 v69, v58, v59\n\t" "v_cvt_pk_bf16_f32 v70, v60, v61\n\t" \
   "v_cvt_pk_bf16_f32 v71, v62, v63\n\t" "buffer_store_dwordx2 v[68:69], v21, %[rs], s48 offen offset:128 sc1\n\t" \
   "buffer_store_dwordx2 v[70:71], v21, %[rs], s47 offen offset:128 sc1\n\t" "s_cmp_lt_u32 s45, 64\n\t" "s_cbranch_scc1 1b\n\t" \
   "s_waitcnt vmcnt(0) lgkmcnt(0)"
#define DF_CLOB "v8", "v9", "v10", "v11", "v12", "v13", "v14", "v15", "v32", "v33", "v34", "v35", "v36", "v37", "v38", "v39", "v40", "v41", "v42", "v43", "v44", "v45", "v46", "v47", "v48", "v49", "v50", "v51", "v52", "v53", "v54", "v55", "v56", "v57", "v58", "v59", "v60", "v61", "v62", "v63", "v64", "v65", "v66", "v67", "v68", "v69", "v70", "v71", "v72", "v73", "v74", "v75", "v76", "v77", "v78", "v79", "v80", "v81", "v82", "v83", "v84", "v85", "v86", "v87", "v88", "v89", "v90", "v91", "v92", "v93", "v94", "v95", "v96", "v97", "v98", "v99", "v100", "v101", "v102", "v103", "v104", "v105", "v106", "v107", "v108", "v109", "v110", "v111", "v112", "v113", "v114", "v115", "v116", "v117", "v118", "v119", "v120", "v121", "v122", "v123", "v124", "v125", "v126", "v127", "v128", "v129", "v130", "v131", "v132", "v133", "v134", "v135", "s40", "s41", "s42", "s43", "s44", "s45", "s46", "s47", "s48", "s49", "s50", "s51", "s52", "s53"
template <bool VU>
__device__ __forceinline__ void job_proj(int wv_, const v32u (&w)[10], unsigned char* ws, const Pool& pool, int layer, int h) {
  const unsigned lds0 = (unsigned)(uintptr_t)(__attribute__((address_space(3))) unsigned char*)dyn_lds;
  const int lane = lane_id(), wv = wv_, g = lane >> 4, r = lane & 15;
  const v4u rs = make_rs(ws, (unsigned)OFF_END);
  v16u ad = (unsigned)lane;
  ad[0] = (int)OFF_XR + wv * 10240 + lane * 16;
  ad[1] = wv * 64 + lane < 96 ? (int)OFF_ST + wv * 1024 + lane * 16 : OOB;
  ad[3] = cnt_off(layer, CNT_IDFT, lane & 31);
  ad[4] = lane == 0 ? kqf_off(layer, 0, (VU ? 32 : 0) + h) + wv * 4 : OOB;
  const int q = (g >> 1) * 256 + r * 16;
  ad[5] = (VU ? (int)OFF_UF + (wv * 320 + h) * 1024 + (g & 1) * 32768 : (h * RT * 4 + wv) * 1024 + (g & 1) * 4096) + q;
  ad[6] = lds0 + PJ_X + (g & 1) * 20480 + q;
  ad[7] = lds0 + PJ_X + 40960 + (lane & 31) * 48;
  ad[8] = lds0 + wv * 2048 + lane * 16;
  ad[9] = lds0 + 8192 + lane * 16;
  v4u rf = rs;
  v8u fo;
  if constexpr (!VU) {
    fo[0] = h * 16384 + wv * 2048 + lane * 16;
#pragma unroll
    for (int k = 0; k < 3; ++k) { const int u = h * 10 + wv + 4 * k; fo[1 + k] = wv + 4 * k < 10 ? (u >> 3) * 16384 + (u & 7) * 1024 + lane * 16 : OOB; }
    asm volatile("v_mov_b32 %0, -1" : "=v"(fo[4]));
    fo[5] = fo[6] = fo[7] = fo[4];
  }
  if (layer > 0) fan_wait<64, NFIRST>(wv_, ws, fan_off(1, 1, 0), h);
  PH(2 + layer * 6)
#define PJ_DBG
#define PJ_OPS                                                                                                                      \
  : W_OPS, [rs] "s"(rs), [rdy] "s"((layer - 1) | ((1 << NFIRST / 8) - 1)), [ldx] "s"(lds0 + PJ_X + wv * 10240),                        \
    [ld1] "s"(lds0 + PJ_X + 40960 + (wv < 2 ? wv : 2) * 1024), [qf] "s"((int)OFF_QF), [kf] "s"((int)OFF_KF), [cnt] "s"(cnt_off(layer, CNT_IDFT, 0)), \
    [flw] "s"(lds0 + PJ_LDS + 512 + wv * 4) PJ_DBG
  if constexpr (VU) asm volatile(VU_ASM : "+{v[16:31]}"(ad) PJ_OPS : VU_CLOB, "vcc", "scc", "m0", "memory");
  else asm volatile(KQ_ASM : "+{v[16:31]}"(ad), "+&{s[56:59]}"(rf) PJ_OPS, "{v[152:159]}"(fo), [sel] "s"(pool.sel), [ysel] "s"(pool.ysel), [pool] "s"(pool.base),
                    [yb] "s"(pool.base + P_Y), [size] "s"((unsigned)OFF_END), [skip] "s"(h >= 20 && h < 30 ? 3ull << 62 : 2ull << 62)
                    : KQ_CLOB, "vcc", "scc", "m0", "memory");
  LDSBAR();
}

__device__ __forceinline__ unsigned dft_idx(int f, int d) { return (unsigned)((f == 1 ? 320 : f >> 1) * d + (((f & 1) && f != 1) ? 160 : 0)) % 640u; }
__device__ __forceinline__ float cos640(unsigned i) { return __builtin_amdgcn_cosf((float)i * (1.f / 640)); }
__device__ __forceinline__ void job_dft(int wv_, unsigned char* ws, int layer, int fg, int set) {
  const unsigned lds0 = (unsigned)(uintptr_t)(__attribute__((address_space(3))) unsigned char*)dyn_lds;
  const int lane = lane_id(), wv = wv_, g = lane >> 4, r = lane & 15;
  const v4u rs = make_rs(ws, (unsigned)OFF_END);
  v32u w[5];
#pragma unroll
  for (int t = 0; t < 2; ++t) {
    const int f = 16 * (fg * 8 + wv * 2 + t) + r;
    const unsigned step = dft_idx(f & ~(f != 1), 32);
#pragma unroll
    for (int q = 0; q < 4; ++q) {
      unsigned m0 = dft_idx(f, sigma(g, 2 * q)), m1 = dft_idx(f, sigma(g, 2 * q + 1));
#pragma unroll
      for (int kt = 0; kt < KT; ++kt) {
        const int i = t * KT + kt;
        w[i >> 3][(i & 7) * 4 + q] = pk(cos640(m0), cos640(m1));
        m0 += step; m0 = min(m0, m0 - 640u); m1 += step; m1 = min(m1, m1 - 640u);
      }
    }
  }
  v16u ad = (unsigned)lane;
  ad[0] = (int)OFF_XR + wv * 10240 + lane * 16;
  ad[1] = wv * 64 + lane < 96 ? (int)OFF_ST + wv * 1024 + lane * 16 : OOB;
  ad[3] = cnt_off(layer, CNT_OUT, 0);
  ad[5] = (int)OFF_X1T + (((fg * 8 + wv * 2) * 4 + g) * XS + r) * 8;
  ad[6] = lds0 + lane * 16;
  ad[7] = lds0 + 40960 + r * 48;
  asm volatile(DF_ASM
               : "+{v[16:31]}"(ad)
               : "{a[0:31]}"(w[0]), "{a[32:63]}"(w[1]), "{a[64:95]}"(w[2]), "{a[96:127]}"(w[3]), "{a[128:159]}"(w[4]), [rs] "s"(rs), [set] "s"(set), [xs4] "s"(4 * XS * 8),
                 [ldx] "s"(lds0 + wv * 10240), [ld0] "s"(lds0), [ld1] "s"(lds0 + 40960 + (wv < 2 ? wv : 2) * 1024)
               : DF_CLOB, "vcc", "scc", "m0", "memory");
}

constexpr int NOUT = 44, NLATE = 108;
constexpr int L_HS = PJ_LDS, MAIN_LDS = L_HS + 1024;
static_assert(MAIN_LDS >= JOB_LDS && MAIN_LDS >= REC_LDS && MAIN_LDS <= 160 * 1024, "LDS");
struct Go { Pool pool; int rot; };
__device__ __forceinline__ void st32(rsrc_t rs, int off, unsigned v) { __builtin_amdgcn_raw_buffer_store_b32(v, rs, off, 0, AUX_SC1); }
__device__ __forceinline__ unsigned hello_load(rsrc_t rs, int b) { return __builtin_amdgcn_raw_buffer_load_b32(rs, HS_GO + b * 16, 0, AUX_SC1); }
__device__ __forceinline__ void hello(int wv, rsrc_t rs, int b, unsigned t) {
  if (wv == 0) {
    st32(rs, lane_id() == 0 ? HS_HI + b * 8 : OOB, t + 1u);
    LDS_U32(lds_base() + L_HS) = t + 1u;
  }
}
__device__ __forceinline__ int go_off(int b) {
  const int l = lane_id() & 3;
  return l == 0 ? HS_GO + b * 16 : (l == 3 ? HS_ASK + (b & 127) * 16 : HS_PAY + (b & 31) * 32 + l * 16 - 16);
}
__device__ __forceinline__ Go wait_go(int wv, unsigned char* ws, int b, v4u rep, v4u m) {
  const v4u rs = make_rs(ws, (unsigned)OFF_END);
  const int l = lane_id();
  const unsigned a = __builtin_amdgcn_readfirstlane(LDS_U32(lds_base() + L_HS));
  for (unsigned spins = 0; spins < (1u << 18); ++spins) {
    if (spins || b >= 128) {
      VM_LD128(m, go_off(b), 0, 0);
      VM_WAIT1(0, m);
    }
    const unsigned n = __builtin_amdgcn_readlane(m.y, 0);
    if (__builtin_amdgcn_readlane(m.x, 0) == a && __builtin_amdgcn_readlane(m.x, 1) == n && __builtin_amdgcn_readlane(m.x, 2) == n) break;
    const unsigned t = (wv & 2) ? ((wv & 1) ? m.w : m.z) : ((wv & 1) ? m.y : m.x);
    const bool again = b >= 128 && l == 3 && t != rep.x;
    rep.x = t;
    VM_ST128(rep, again ? HS_CAL - 128 * 64 + b * 64 + wv * 16 : OOB, 0);
    if (spins) __builtin_amdgcn_s_sleep(6);
  }
  VM_ST32(m.z, (wv == 0 && l == 0) ? HS_HI + b * 8 + 4 : OOB, 0);
  if (b >= 128) { PH(1) }
  const auto u64 = [](unsigned lo, int i, unsigned hi, int k) {
    return (unsigned)__builtin_amdgcn_readlane(lo, i) | (unsigned long long)(unsigned)__builtin_amdgcn_readlane(hi, k) << 32;
  };
  Go g;
  g.pool.sel = u64(m.y, 1, m.z, 1); g.pool.s3 = u64(m.w, 1, m.w, 2); g.pool.ysel = u64(m.y, 2, m.z, 2);
  g.pool.base = __builtin_amdgcn_readfirstlane(pool_base(ws));
  g.rot = __builtin_amdgcn_readlane(m.w, 0) & 7;
  return g;
}
#define LDSTAMP(n)
#define LDX
#define LDC
__device__ __forceinline__ v4u load_rows(v32u (&w)[10], int kind, const u16* A, const u16* B, const u16* x, int h, int wv, unsigned slot, unsigned char* ws, int b, bool first) {
  unsigned tr;
  const int l = lane_id(), i = l & 15, g = l >> 4;
  const v4u ra = make_rs(A, 640u * 8192u), rb = make_rs(B, 640u * 8192u), rs = make_rs(ws, (unsigned)OFF_END), rx = make_rs(x, S_ * D_ * 2);
  v8u ad, ex;
  const bool pj = kind == 1 || kind == 2;
  const int wr = 4 * wv + g, rr = 4 * (pj ? g >> 1 : g) + (i >> 2), gt = pj ? g & 1 : 0;
  ad[0] = wr * 8192 + h * 256 + i * 16;
  ad[1] = (h * 128 + wr) * 1280 + i * 16;
  ad[2] = slot + rr * 256 + ((2 * wv + gt) ^ (rr & 7)) * 32 + (i & 3) * 8;
  ad[3] = pj ? ad[2] + 2048u : ad[2] ^ 32u;
  ad[4] = slot + wr * 256 + ((i >> 1) ^ (wr & 7)) * 32 + (i & 1) * 16;
  ad[5] = slot + wr * 328 + ((i >> 2) * 2 + (i & 1)) * 8 + ((i >> 1) & 1) * 4;
  ad[6] = slot + (kind == 3 ? (16 * (g & 1) + 4 * (g >> 1) + ((i >> 2) & 1)) * 328 + (i >> 3) * 8 : rr * 328 + gt * 8) + (i & 3) * 10560 + wv * 80;
  ad[7] = ad[4] + 65536u;
  const unsigned im = lds_base() + PJ_X + (wv >> 1) * PJ_BUF;
  const bool hi = first && wv == 0;
  ex[0] = go_off(b);
  ex[1] = hi ? HS_GO + b * 16 : OOB;
  ex[2] = hi && l == 0 ? HS_HI + b * 8 : OOB;
  ex[3] = lds_base() + L_HS + (hi ? 0 : 4);
  ex[4] = (wv * 16 + (l >> 3)) * 1280 + (l & 7) * 16;
  ex[5] = im + ((wv & 1) * KT + ((l >> 2) & 1)) * 1024 + ((l & 1) * 32 + (l >> 3)) * 16 + (l & 2) * 4;
  ex[6] = im + 40960 + ((wv & 1) * 16 + (l >> 3)) * 48 + min(l & 7, 5) * 8;
  ex[7] = (l & 7) ? 0u : ~0u;
  v4u m;
#define LD_OPS                                                                                                                                       \
  : "={a[0:31]}"(w[0]), "={a[32:63]}"(w[1]), "={a[64:95]}"(w[2]), "={a[96:127]}"(w[3]), "={a[128:159]}"(w[4]), "={a[160:191]}"(w[5]),                \
    "={a[192:223]}"(w[6]), "={a[224:255]}"(w[7]), "={v[192:223]}"(w[8]), "={v[224:255]}"(w[9]), "={v[28:31]}"(m) LDX                                 \
  : "{v[16:23]}"(ad), "{v[112:119]}"(ex), [ra] "s"(ra), [rb] "s"(rb), [rw] "s"(rs), [rx] "s"(rx), [p0] "s"(wv < 3 ? 3 * wv : 8)                      \
  : LDP_CLOB LDC, "scc", "memory"
  if (kind == 0) asm volatile(LDP_ASM LD_OPS);
  else if (kind == 1) asm volatile(LDK_ASM LD_OPS);
  else if (kind == 2) asm volatile(LDV_ASM LD_OPS);
  else asm volatile(LDY_ASM LD_OPS);
  return m;
}
__device__ __forceinline__ v4u prep_jobs(int wv, unsigned char* ws, rsrc_t rs, const u16* __restrict__ x, const u16* __restrict__ mem0, int j) {
  const int lane = lane_id(), g = lane >> 4, r = lane & 15, tid = wv * 64 + lane, pb = pool_base(ws);
  const int xc = (int)xcc_id(), u = (j >> 3) * 4 + (xc & 3), page = xc < 4 ? u >> 2 : 16 + u % NYP;
  const int cb = pb + page * PAGE + M_CAL + (xc < 4 ? wv * REG + xc * 512 : 3 * REG - u / NYP * WIN + wv * 512);
  stc(rs, lane < 32 ? cb + (lane >> 3) * 8192 + (lane & 7) * 16 : OOB, v4u{1u, 2u, 3u, 4u});
  const unsigned char* p = ws + __builtin_amdgcn_readfirstlane(cb);
  unsigned t0, t1, t2, t3, t4;
  asm volatile("s_load_dword %0, %4, 0x80\n\ts_load_dword %1, %4, 0x2080\n\ts_load_dword %2, %4, 0x4080\n\ts_load_dword %3, %4, 0x6080"
               : "={s40}"(t0), "={s41}"(t1), "={s42}"(t2), "={s43}"(t3) : "s"(p) : "memory");
  const unsigned tg = hello_load(rs, 128 + j);
  v2u c[5][2];
  unsigned t[4];
#pragma unroll
  for (int q = 0; q < 5; ++q) {
    const u16* xr = x + (size_t)(j * 16 + r) * D_ + 32 * (5 * wv + q) + 4 * g;
    c[q][0] = *(const v2u*)xr; c[q][1] = *(const v2u*)(xr + 16);
  }
#pragma unroll
  for (int k = 0; k < 4; ++k) t[k] = *(const unsigned*)(mem0 + (size_t)((tid >> 1) & 7) * D_ + colmap((min(j >> 3, 4) * 8 + ((tid >> 4) & 7)) * 2 + (tid & 1), 2 * k));
  __builtin_amdgcn_sched_barrier(0);
  const v4u ones = {0xFFFFFFFFu, 0xFFFFFFFFu, 0xFFFFFFFFu, 0xFFFFFFFFu};
  const int o = (j * 256 + tid) * 16;
  const bool cal = (o & 0x79800) == 0x79800;
  stc(rs, (int)OFF_HO + o, ones);
  stc(rs, o + REG < (int)HO_FIRST ? (int)OFF_HO + REG + o : OOB, ones);
  stc(rs, (cal || (o >= W_MEM && o < W_MISC)) ? OOB : pb + (NSEG - 1) * REG + o, ones);
  stc(rs, cal ? OOB : pb + o, ones);
  stc(rs, pb + P_Y + o, ones);
  stc(rs, o < 8 * 16384 ? pb + P_Y + REG + o : OOB, ones);
  hello(wv, rs, 128 + j, tg);
  asm volatile("" ::"v"(tg));
  __builtin_amdgcn_sched_barrier(0);
#define CAL_T(r, o) "s_load_dword " r ", %5, " o "\n\ts_waitcnt lgkmcnt(0)\n\ts_memtime s[46:47]\n\ts_waitcnt lgkmcnt(0)\n\ts_mov_b32 " r ", s46\n\t"
  asm volatile(CAL_T("%0", "0x80") CAL_T("%1", "0") CAL_T("%2", "0x2000") CAL_T("%3", "0x4000") CAL_T("%4", "0x6000")
               : "+{s40}"(t0), "+{s41}"(t1), "+{s42}"(t2), "+{s43}"(t3), "={s44}"(t4) : "s"(p) : "s46", "s47", "memory");
  PH(16)
  __builtin_amdgcn_sched_barrier(0);
  f32x2 st = {0.f, 0.f};
#pragma unroll
  for (int q = 0; q < 5; ++q) {
    const v4u f = v4u{c[q][0].x, c[q][0].y, c[q][1].x, c[q][1].y};
    stc(rs, (int)OFF_XR + ((j * KT + 5 * wv + q) * 64 + lane) * 16, f);
    st += frag_stat(f);
  }
  stc(rs, j < 40 ? pb + (NSEG - 1) * REG + W_MEM + o : OOB, v4u{nosent(t[0]), nosent(t[1]), nosent(t[2]), nosent(t[3])});
  st = rows_sum(st);
  __builtin_amdgcn_raw_buffer_store_b64(__builtin_bit_cast(v2u, g ? f32x2{0.f, 0.f} : st), rs,
                                        (g == 0 || (g == 1 && wv == 0)) ? (int)OFF_ST + ((j * 16 + r) * 6 + (g ? 4 : wv)) * 8 : OOB, 0, AUX_SC1);
  const unsigned ask = __builtin_amdgcn_raw_buffer_load_b32(rs, HS_ASK + j * 16 + wv * 4, 0, AUX_SC1);
  const unsigned b0[4] = {min(t1 - t0, 9999u), min(t2 - t1, 9999u), min(t3 - t2, 9999u), min(t4 - t3, 9999u)}, sh = ((xc ^ wv) & 1) * 16;
  const v4u rep = {ask, (unsigned)page, b0[0] << sh | b0[2] << (16 - sh), b0[1] << sh | b0[3] << (16 - sh)};
  asm volatile("s_waitcnt vmcnt(0)" ::: "memory");
  stc(rs, lane == 0 ? HS_CAL + j * 64 + wv * 16 : OOB, rep);
  LDSBAR();
  PH(18)
  return rep;
}
__device__ __forceinline__ int wave_isum(int v) {
  asm volatile("s_nop 1\n\tv_add_u32_dpp %0, %0, %0 quad_perm:[1,0,3,2] row_mask:0xf bank_mask:0xf\n\t"
               "s_nop 1\n\tv_add_u32_dpp %0, %0, %0 quad_perm:[2,3,0,1] row_mask:0xf bank_mask:0xf\n\t"
               "s_nop 1\n\tv_add_u32_dpp %0, %0, %0 row_half_mirror row_mask:0xf bank_mask:0xf\n\t"
               "s_nop 1\n\tv_add_u32_dpp %0, %0, %0 row_mirror row_mask:0xf bank_mask:0xf\n\ts_nop 0"
               : "+v"(v));
  return (__builtin_amdgcn_readlane(v, 0) + __builtin_amdgcn_readlane(v, 16)) + (__builtin_amdgcn_readlane(v, 32) + __builtin_amdgcn_readlane(v, 48));
}
__device__ __forceinline__ Go lead(int wv, unsigned char* ws, const u16* x, const u16* mem0, int rot) {
  const v4u rs = make_rs(ws, (unsigned)OFF_END);
  const int t = wv * 64 + lane_id();
  int* acc = (int*)(dyn_lds + L_HS + 128);
#define VM_LD64(dst, off) asm volatile("buffer_load_dwordx2 %0, %1, %2, 0 offen sc1" : "=v"(dst) : "v"(off), "s"(rs) : "memory")
  v4u c0, c1;
  v2u hi;
  unsigned old, n = 0;
  VM_LD128(c0, HS_CAL + t * 32, 0, 0);
  VM_LD128(c1, HS_CAL + t * 32, 0, 16);
  VM_LD64(hi, HS_HI + t * 8);
  VM_LD32(old, HS_PAY + (t & 63) * 16, 0);
  asm volatile("s_waitcnt vmcnt(0)" : "+v"(c0), "+v"(c1), "+v"(hi), "+v"(old)::"memory");
  const unsigned r0 = c0.x + 1u, r1 = c1.x + 1u, q = hi.y + 1u;
  __builtin_amdgcn_raw_buffer_store_b64(v2u{r0, r1}, ws_rsrc(ws), HS_ASK + t * 8, 0, AUX_SC1);
  while (__any(old == n)) ++n;
  if (t < 192) acc[t] = 9999;
  prep_jobs(wv, ws, ws_rsrc(ws), x, mem0, 127);
  for (unsigned spins = 0; spins < (1u << 18); ++spins) {
    VM_LD128(c0, HS_CAL + t * 32, 0, 0);
    VM_LD128(c1, HS_CAL + t * 32, 0, 16);
    VM_LD64(hi, HS_HI + t * 8);
    asm volatile("s_waitcnt vmcnt(0)" : "+v"(c0), "+v"(c1), "+v"(hi)::"memory");
    if (!__any(t < 254 && (c0.x != r0 || c1.x != r1))) break;
    __builtin_amdgcn_s_sleep(2);
  }
  if (t < 254) {
    int *a0 = acc + min(c0.y, 47u) * 4, *a1 = acc + min(c1.y, 47u) * 4;
    atomicMin(a0, c0.z & 0xFFFF); atomicMin(a0 + 1, c0.z >> 16); atomicMin(a0 + 2, c0.w & 0xFFFF); atomicMin(a0 + 3, c0.w >> 16);
    atomicMin(a1, c1.z & 0xFFFF); atomicMin(a1 + 1, c1.z >> 16); atomicMin(a1 + 2, c1.w & 0xFFFF); atomicMin(a1 + 3, c1.w >> 16);
  }
  __syncthreads();
  PH(19)
  const int s = lane_id(), fx = (s & 4) ? -1 : 1, fy = ((s * 43 >> 7) & 1) ? -1 : 1;
  const v4i u = ((const v4i*)acc)[s >> 2], v = ((const v4i*)acc)[16 + (s * 43 >> 7)];
  int ax = min(u.x, u.y) - min(u.z, u.w), yx = min(v.z, v.w) - min(v.x, v.y);
  const int n0 = ax > 0 ? u.z : u.x, n1 = ax > 0 ? u.w : u.y, lu = min(n0, n1), lv = min(min(v.x, v.y), min(v.z, v.w)), near = 110 - wave_imax(-min(lu, lv));
  const int sa = (lu < near && abs(ax) > 60) ? (ax > 0 ? fx : -fx) : 0, sy = (lv < near && abs(yx) > 60) ? (yx > 0 ? fy : -fy) : 0;
  const int g = wave_isum(sa + sy), m = wave_isum(abs(sa) + abs(sy));
  const bool with = sa * g > 0;
  const int bs = wave_imax(with ? -n1 : -9999) - wave_imax(with ? -n0 : -9999);
  int b = n0 - n1;
  if (abs(g) * 8 > m * 7) {
    if (sa * g >= 0) { ax = g * fx; b = bs; }
    if (sy == 0) yx = g * fy;
  }
  Go go;
  go.pool.sel = __ballot(ax > 0);
  go.pool.s3 = __ballot(b > 0) ^ 0xAAAAAAAAAAAAAAAAull;
  go.pool.ysel = __ballot(yx > 0);
  go.pool.base = __builtin_amdgcn_readfirstlane(pool_base(ws));
  go.rot = rot;
  PH(20)
  {
    const unsigned long long v = (s & 1) ? go.pool.ysel : go.pool.sel;
    VM_ST128((v4u{n, (unsigned)v, (unsigned)(v >> 32), (unsigned)(go.pool.s3 >> (s & 1) * 32)}), wv == 0 ? HS_PAY + s * 16 : OOB, 0);
  }
  for (unsigned spins = 0; spins < (1u << 18); ++spins) {
    VM_ST128((v4u{hi.x, n, q, (unsigned)rot}), HS_GO + t * 16, 0);
    const unsigned sent = hi.x;
    do {
      __builtin_amdgcn_s_sleep(8);
      VM_LD64(hi, HS_HI + t * 8);
      VM_WAIT1(0, hi);
    } while (!__any(hi.x != sent) && __any(t < 255 && hi.y != q) && ++spins < (1u << 18));
    if (!__any(t < 255 && hi.y != q)) break;
  }
  return go;
}
__device__ __forceinline__ void late_jobs(int wv_, unsigned char* ws, rsrc_t rs, const u16* __restrict__ wr, const u16* __restrict__ wi, int j) {
  const int lane = lane_id(), wv = wv_, g = lane >> 4, r = lane & 15;
  unsigned* t32 = (unsigned*)dyn_lds;
  for (int o = (int)HO_FIRST + (j * 256 + wv * 64 + lane) * 16; o < 2 * (int)HO_LAYER; o += NLATE * 4096)
    stc(rs, (int)OFF_HO + o, v4u{0xFFFFFFFFu, 0xFFFFFFFFu, 0xFFFFFFFFu, 0xFFFFFFFFu});
#pragma unroll 1
  for (int fi = j * 4 + wv; fi < 20 * NFK; fi += NLATE * 4) {
    v4u v;
#pragma unroll
    for (int q = 0; q < 4; ++q) {
      const int e = fi % NFK, d = 16 * (fi / NFK) + r, f = 4 * (32 * (e >> 2) + 8 * q + 2 * (e & 2) + g) + 2 * (e & 1);
      const float sc = f ? 2.f : 1.f;
      v[q] = pk(cos640(dft_idx(f, d)) * sc, cos640(dft_idx(f + 1, d)) * sc);
    }
    stc(rs, (int)OFF_INV + (fi * 64 + lane) * 16, v);
  }
#pragma unroll 1
  for (int b = j; b < 32 * 6; b += NLATE) {
    const int m0 = (b & 31) * 64, k0 = (b >> 5) * 64, n0 = (b & 24) * 2 + (b & 7) * 256;
#pragma unroll
    for (int q = 0; q < 16; ++q) {
      const size_t i = (size_t)(n0 + q + 64 * wv) * 321 + min(k0 + lane, 320);
      t32[(q * 4 + wv) * 65 + lane] = (unsigned)wr[i] | ((unsigned)wi[i] << 16);
    }
    __syncthreads();
#pragma unroll
    for (int q = 0; q < 16; ++q) {
      const int k = q * 4 + wv;
      st32(rs, k0 + k < 321 ? (int)OFF_WT + ((k0 + k) * S_ + m0 + lane) * 4 : OOB, t32[lane * 65 + k]);
    }
    __syncthreads();
  }
  dep_signal(wv_, ws, cnt_off(0, CNT_IDFT, 0));
}
__device__ __forceinline__ void late_wait(int wv_, unsigned char* ws) {
  if (TIDX == 0) {
    for (unsigned spins = 0; spins < (1u << 17); ++spins) {
      if (__hip_atomic_load((gu32*)(ws + cnt_off(0, CNT_IDFT, 0)), __ATOMIC_RELAXED, __HIP_MEMORY_SCOPE_AGENT) == NLATE - 1) break;
      __builtin_amdgcn_s_sleep(16);
    }
  }
  __syncthreads();
}
#define TROB (24 * 256 + 16)
#define TC2 30
#define TVIS 32
#define TDLT 170
#define TM1 88
#define SPARE_US 200
#define SPARE_NAP 127
#define TM2 60
__global__ void __launch_bounds__(256, 1) k_main(unsigned char* ws, const u16* x, const u16* mem0, const u16* Wq, const u16* Wk, const u16* Wv, const u16* Wo,
                                                const u16* wr, const u16* wi, u16* out, long nlayer
                                                ) {
  v4u* smem = (v4u*)dyn_lds;
  const int wv_ = __builtin_amdgcn_readfirstlane(threadIdx.x >> 6);
  const RecTune tn = {TM1, TM2, 4, TROB, TC2, TVIS, TDLT, 1};
  unsigned long long* dbg = nullptr;
#define RSTAMP_(i)
  const rsrc_t rs = ws_rsrc(ws);
  PH(0)
  const int b = blockIdx.x, tt = b >> 3;
  const unsigned t00 = rtc();
  int rot = (int)(xcc_id() - b) & 7, lpl, ljb = -1;
  if (tt < 16) {
    v32u w[10];
    Go go;
    const unsigned lds0 = lds_base();
#pragma unroll 1
    for (int it = 0;; ++it) {
      const int p = (b + rot) & 7, h = (p & 3) * 8 + (tt & 7);
      const bool v = (tt & 8) != 0;
      const v4u m = load_rows(w, p < 4 ? (v ? 3 : 0) : (v ? 2 : 1), v ? Wv : Wq, v ? Wo : Wk, x, h, wv_, lds0 + (p < 4 ? 0 : PJ_X + 2 * PJ_BUF), ws, b, it == 0);
      PH(21 + (p >> 2))
      go = wait_go(wv_, ws, b, m, m);
      if (go.rot == rot || it) break;
      rot = go.rot;
    }
    Pool pool = go.pool;
    const int pl = (b + rot) & 7, role = pl < 4 ? (tt & 8) * 4 + pl * 8 + (tt & 7) : pl * 32 + tt;
    const int hd = (pl - 4) * 8 + (tt & 7);
#pragma unroll 1
    for (int layer = 0; layer < nlayer; ++layer) {
      int ro = role, xs = pl, wq = wv_, h2 = hd | (tt & 8) << 8;
      asm volatile("" : "+s"(ro), "+s"(xs), "+s"(wq), "+s"(h2), "+s"(ws), "+s"(pool.base));
      if (layer > 0) {
        const int jb = ro < 64 ? ro + 57 : (xs - 4) * 16 + (ro & 31);
        late_wait(wq, ws);
        if (ro < 64 || jb < 57) {
          job_col(wq, ws, ws_rsrc(ws), lds_base(), jb, fan_off(0, 1, 0), fan_off(1, 0, 0), false);
          __syncthreads();
          RSTAMP_(5)
          PH(7)
        } else fan_wait<64, 20>(wq, ws, fan_off(0, 1, 0), jb);
      }
      if (ro < 32) { rec_p1(wq, w, ws, pool, layer, ro, xs, tn, dbg); PH(4 + layer * 6) }
      else if (ro < 64) rec_pv(wq, w, ws, pool, layer, ro - 32, xs, tn, dbg);
      else {
        if (h2 >> 8) job_proj<true>(wq, w, ws, pool, layer, h2 & 31);
        else job_proj<false>(wq, w, ws, pool, layer, h2 & 31);
        RSTAMP_(2)
      }
    }
    lpl = pl;
    ljb = role < 64 ? role + 57 : (pl - 4) * 16 + (role & 31);
    if (role >= 64 && ljb >= 57) ljb = -1;
  } else {
  const Go go = b == 255 ? lead(wv_, ws, x, mem0, rot) : wait_go(wv_, ws, b, prep_jobs(wv_, ws, rs, x, mem0, b - 128), v4u{0u, 0u, 0u, 0u});
  Pool pool = go.pool;
  const int pl = (b + go.rot) & 7;
  const int role = pl < 4 ? (tt < 26 ? 64 + pl * 10 + tt - 16 : 104 + pl * 6 + tt - 26) : pl * 32 + tt;
  if (pl < 4 || tt < 27) late_jobs(wv_, ws, rs, wr, wi, pl < 4 ? 44 + pl * 16 + tt - 16 : (pl - 4) * 11 + tt - 16);
  PH(23)
  lpl = pl;
  if (role >= 104 && role < 128) {
    while (rtc() - t00 < 100u * SPARE_US) __builtin_amdgcn_s_sleep(127);
    late_wait(wv_, ws);
    prog_prewait(wv_, ws, kqf_off((int)nlayer - 1, NSEG - 1, ((pl + 1) & 3) * 8 + tt - 26));
    __syncthreads();
    __builtin_amdgcn_s_sleep(SPARE_NAP);
  } else if (role >= 64 && role < 104) {
#pragma unroll 1
    for (int layer = 0; layer < nlayer; ++layer) {
      if (layer > 0) {
        late_wait(wv_, ws);
        job_col(wv_, ws, rs, lds_base(), role + 57, fan_off(0, 1, 0), fan_off(1, 0, 0), false);
        __syncthreads();
        PH(7)
      }
      ((unsigned*)(dyn_lds + L_T))[lane_id() & 15] = 0u;
      __syncthreads();
      if (wv_ == 3) rec_fin(ws, pool, layer, role - 64, pl, tn, dbg);
      else if (wv_ == 0) rec_sched(ws, pool, layer, role - 64, pl, tn, dbg);
      __syncthreads();
    }
    ljb = role + 57;
  } else {
#pragma unroll 1
  for (int layer = 0; layer < nlayer; ++layer) {
    int wq = wv_;
    asm volatile("" : "+s"(wq));
    if (layer > 0) late_wait(wq, ws);
    if (layer > 0) job_idft(wq, ws, rs, smem, ((tt - 16) >> 1) * 4 + pl - 4, tt & 1, dbg + 60000 + role * 8);
    RSTAMP_(2)
    __syncthreads();
    if (tt >= 27) {
      job_dft(wq, ws, layer, tt - 27, pl - 4);
      fan_signal<64>(wq, ws, fan_off(0, 1, 0), layer * 20 + (pl - 4) * 5 + tt - 27);
      if (layer) { PH(14) }
    } else {
#pragma unroll 1
      for (int j = (tt - 16) * 4 + pl - 4; j < 320; j += NOUT) {
        flags32_wait(wq, ws, kqf_off(layer, j / 5, 32));
        job_out(wq, ws, rs, smem, pool.ysel, layer, j / 5, j % 5);
        dep_signal(wq, ws, cnt_off(layer, CNT_OUT, j / 5));
      }
    }
    RSTAMP_(3)
    PH((tt >= 27 ? 6 : 5) + layer * 6)
  }
  }
  }
  int wq = wv_, tl = tt;
  asm volatile("" : "+s"(wq), "+s"(ljb), "+s"(lpl), "+s"(tl), "+s"(ws));
  job_last(wq, ws, out, ljb, lpl * 16 + (tl >> 1), tl & 1);
}

extern "C" void kernel_launch(void* const* d_in, const int* in_sizes, int n_in, void* d_out, int out_size, void* d_ws, size_t ws_size,
                              hipStream_t stream) {
  const u16* x = (const u16*)d_in[0];
  const u16* mem0 = (const u16*)d_in[1];
  const u16* Wq = (const u16*)d_in[2];
  const u16* Wk = (const u16*)d_in[3];
  const u16* Wv = (const u16*)d_in[4];
  const u16* Wo = (const u16*)d_in[5];
  const u16* wr = (const u16*)d_in[8];
  const u16* wi = (const u16*)d_in[9];
  unsigned char* ws = (unsigned char*)d_ws;
  u16* out = (u16*)d_out;
  (void)hipFuncSetAttribute((const void*)k_main, hipFuncAttributeMaxDynamicSharedMemorySize, MAIN_LDS);
  k_main<<<256, 256, MAIN_LDS, stream>>>(ws, x, mem0, Wq, Wk, Wv, Wo, wr, wi, out, 2);
}
```

```cpp
#include <hip/hip_runtime.h>
#include <hip/hip_bf16.h>
#include <stdint.h>

typedef unsigned v4u __attribute__((ext_vector_type(4)));
typedef unsigned v2u __attribute__((ext_vector_type(2)));
typedef float f32x4 __attribute__((ext_vector_type(4)));
typedef float f32x2 __attribute__((ext_vector_type(2)));
typedef __bf16 bf16x8 __attribute__((ext_vector_type(8)));
typedef __bf16 bf16x2 __attribute__((ext_vector_type(2)));
typedef unsigned short u16;

constexpr int S_ = 2048, D_ = 640, NSEG = 64, KT = 20, RT = 128;
constexpr int NFT = 40;
constexpr int NFK = 20;
constexpr int NCOL = 161;
constexpr int WIN = 32768, REG = 16 * WIN;
constexpr int W_MEM = 10 * WIN, W_MISC = 15 * WIN;
__host__ __device__ constexpr int copy_off(int c) { return (c & 3) * 2048 + (c >> 2) * 16384; }
constexpr int M_STAMP = W_MISC + copy_off(4), M_TM = M_STAMP + 40 * 64, M_CAL = W_MISC + copy_off(3);
constexpr int PAGE = 4 * REG, NYP = 22, P_Y = NSEG * REG;
constexpr size_t SZ_POOL = (size_t)P_Y + (size_t)(NYP + 1) * PAGE;

constexpr size_t OFF_CNT = 0;
constexpr int CNT_IDFT = 0, CNT_OUT = 1;
constexpr size_t OFF_PROG = OFF_CNT + (size_t)3 * 32 * 128;
constexpr size_t OFF_KQF = OFF_PROG + (size_t)NSEG * 4096;
#define FNAP 6
#define FSP 16
#define FNG 32
constexpr int FAN_G = (161 * FSP + 127) / 128 * 128 > 3840 ? (161 * FSP + 127) / 128 * 128 : 3840;
constexpr size_t OFF_FAN = OFF_KQF + (size_t)NSEG * 4096;
constexpr size_t HO_LAYER = OFF_FAN + (size_t)2 * FNG * FAN_G;
constexpr size_t OFF_HO = SZ_POOL;
constexpr size_t HO_FIRST = OFF_FAN + (size_t)FNG * FAN_G;
constexpr size_t OFF_HS = OFF_HO + 2 * HO_LAYER;
constexpr int HS_GO = (int)OFF_HS, HS_PAY = HS_GO + 4096, HS_ASK = HS_PAY + 1024, HS_HI = HS_ASK + 2048, HS_CAL = HS_HI + 2048;
__host__ __device__ constexpr int prog_off(int layer, int rb, int slot) { return (int)(OFF_HO + layer * HO_LAYER + OFF_PROG) + (rb * 64 + slot) * 64; }
__host__ __device__ constexpr int kqf_off(int layer, int sg, int h) { return (int)(OFF_HO + layer * HO_LAYER + OFF_KQF) + (sg * 64 + h) * 64; }
__host__ __device__ constexpr int cnt_off(int layer, int kind, int rb) { return (int)(OFF_HO + layer * HO_LAYER + OFF_CNT) + (kind * 32 + rb) * 128; }
__host__ __device__ constexpr int fan_off(int layer, int set, int i) { return (int)(OFF_HO + layer * HO_LAYER + OFF_FAN) + ((set ^ layer ^ 1) * FNG + i) * FAN_G; }
constexpr size_t OFF_XR = (OFF_HS + 65536 + 4095) / 4096 * 4096;
constexpr size_t OFF_ST = OFF_XR + (size_t)RT * KT * 1024;
constexpr size_t OFF_QF = OFF_ST + (size_t)S_ * 48;
constexpr size_t OFF_KF = OFF_QF + (size_t)32 * RT * 4 * 1024;
constexpr size_t OFF_UF = OFF_KF + (size_t)32 * RT * 4 * 1024;
constexpr size_t OFF_PXF = OFF_UF + (size_t)NSEG * 80 * 16 * 1024;
constexpr size_t OFF_PXM = OFF_PXF + (size_t)NSEG * 32 * 2 * 1024;
constexpr size_t OFF_INV = OFF_PXM + (size_t)NSEG * 8 * 2 * 1024;
constexpr size_t OFF_X1T = OFF_INV + (size_t)20 * NFK * 1024;
#define ZPAD 32
#define XPAD 32
constexpr int XS = S_ + XPAD, ZS = S_ + ZPAD;
constexpr size_t OFF_ZT = OFF_X1T + (size_t)160 * XS * 8;
constexpr size_t OFF_WT = OFF_ZT + (size_t)161 * ZS * 8;
constexpr size_t OFF_DBG = OFF_WT + (size_t)321 * S_ * 4;
constexpr size_t OFF_END = OFF_DBG + (size_t)1048576;
static_assert(OFF_END <= ((size_t)256 << 20), "workspace too large");

constexpr int AUX_SC1 = 16;
#define PH(k)
#define PHV(k, v)
#define PHT(n)

__device__ __forceinline__ f32x4 mfma(v4u a, v4u b, f32x4 c) {
  return __builtin_amdgcn_mfma_f32_16x16x32_bf16(__builtin_bit_cast(bf16x8, a), __builtin_bit_cast(bf16x8, b), c, 0, 0, 0);
}
__device__ __forceinline__ unsigned pk(float a, float b) {
  f32x2 v = {a, b};
  return __builtin_bit_cast(unsigned, __builtin_convertvector(v, bf16x2));
}
__device__ __forceinline__ float lo_f(unsigned u) { return __builtin_bit_cast(float, u << 16); }
__device__ __forceinline__ float hi_f(unsigned u) { return __builtin_bit_cast(float, u & 0xFFFF0000u); }
__device__ __forceinline__ float bfr(float x) { return lo_f(pk(x, x)); }
__device__ __forceinline__ v4u pk8(f32x4 a, f32x4 b) { return v4u{pk(a.x, a.y), pk(a.z, a.w), pk(b.x, b.y), pk(b.z, b.w)}; }
__device__ __forceinline__ int sigma(int g, int jj) { return 16 * (jj >> 2) + 4 * g + (jj & 3); }
__device__ __forceinline__ int colmap(int c8, int jj) {
  const int p = c8 >> 3, ga = (c8 >> 1) & 3, half = c8 & 1;
  return 32 * (2 * p + (ga >> 1)) + 16 * ((jj >> 1) & 1) + 4 * (2 * (ga & 1) + (jj >> 2)) + 2 * half + (jj & 1);
}
__device__ __forceinline__ int pool_base(const void* ws) { return (int)((0u - (unsigned)(uintptr_t)ws) & (unsigned)(PAGE - 1)); }
__device__ __forceinline__ unsigned xcc_id() { return (unsigned)__builtin_amdgcn_s_getreg(20 | (3 << 11)) & 7u; }
__device__ __forceinline__ float shx(float v, int m) { return __shfl_xor(v, m); }
__device__ __forceinline__ unsigned nosent(unsigned x) { return x < 0xFFFFFFFEu ? x : 0xFFFFFFFEu; }

__device__ __forceinline__ f32x2 frag_stat(v4u c) {
  const float v[8] = {lo_f(c.x), hi_f(c.x), lo_f(c.y), hi_f(c.y), lo_f(c.z), hi_f(c.z), lo_f(c.w), hi_f(c.w)};
  float q = 0.f;
#pragma unroll
  for (int j = 0; j < 8; ++j) q = __builtin_fmaf(v[j], v[j], q);
  return f32x2{((v[0] + v[1]) + (v[2] + v[3])) + ((v[4] + v[5]) + (v[6] + v[7])), q};
}
__device__ __forceinline__ f32x2 rows_sum(f32x2 a) {
  a.x += shx(a.x, 16); a.y += shx(a.y, 16); a.x += shx(a.x, 32); a.y += shx(a.y, 32);
  return a;
}

typedef int v4i __attribute__((ext_vector_type(4)));
__device__ __forceinline__ int y_region(unsigned long long sel, int s) { return (s * 80 + (s * 43 >> 7) * 16 + (int)((sel >> s) & 1ull)) * 8192; }

__device__ __forceinline__ int lane_id() {
  int l;
  asm volatile("v_mbcnt_lo_u32_b32 %0, -1, 0\n\tv_mbcnt_hi_u32_b32 %0, -1, %0" : "=v"(l));
  return l;
}
#define TIDX (wv_ * 64 + lane_id())
typedef __amdgpu_buffer_rsrc_t rsrc_t;
__device__ __forceinline__ rsrc_t ws_rsrc(unsigned char* ws) { return __builtin_amdgcn_make_buffer_rsrc(ws, 0, (int)OFF_END, 0x00020000); }
__device__ __forceinline__ v4u ldc(rsrc_t rs, int off) { return __builtin_amdgcn_raw_buffer_load_b128(rs, off, 0, AUX_SC1); }
__device__ __forceinline__ void stc(rsrc_t rs, int off, v4u v) { __builtin_amdgcn_raw_buffer_store_b128(v, rs, off, 0, AUX_SC1); }

extern __shared__ __attribute__((aligned(16))) unsigned char dyn_lds[];
__device__ __forceinline__ unsigned lds_base() { return (unsigned)(uintptr_t)(__attribute__((address_space(3))) unsigned char*)dyn_lds; }
__device__ __forceinline__ unsigned rtc() { return (unsigned)__builtin_amdgcn_s_memrealtime(); }
__device__ __forceinline__ int wave_imax(int v) {
  asm volatile("s_nop 1\n\tv_max_i32_dpp %0, %0, %0 quad_perm:[1,0,3,2] row_mask:0xf bank_mask:0xf\n\t"
               "s_nop 1\n\tv_max_i32_dpp %0, %0, %0 quad_perm:[2,3,0,1] row_mask:0xf bank_mask:0xf\n\t"
               "s_nop 1\n\tv_max_i32_dpp %0, %0, %0 row_half_mirror row_mask:0xf bank_mask:0xf\n\t"
               "s_nop 1\n\tv_max_i32_dpp %0, %0, %0 row_mirror row_mask:0xf bank_mask:0xf\n\ts_nop 0"
               : "+v"(v));
  const int a = __builtin_amdgcn_readlane(v, 0), b = __builtin_amdgcn_readlane(v, 16), c = __builtin_amdgcn_readlane(v, 32),
            d = __builtin_amdgcn_readlane(v, 48);
  return max(max(a, b), max(c, d));
}

typedef __attribute__((address_space(1))) unsigned gu32;
__device__ __forceinline__ void prog_prewait(int wv_, unsigned char* ws, int off) {
  if (TIDX == 0) {
    gu32* p = (gu32*)(ws + off);
    for (unsigned spins = 0; spins < (1u << 16); ++spins) {
      if (__hip_atomic_load(p, __ATOMIC_RELAXED, __HIP_MEMORY_SCOPE_AGENT) != 0xFFFFFFFFu) break;
      __builtin_amdgcn_s_sleep(48);
    }
  }
}
__device__ __forceinline__ void prog_wait(int wv_, unsigned char* ws, int off) {
  if (wv_ == 0) {
    const int l = lane_id();
    gu32* p0 = (gu32*)(ws + off + l * 64);
    for (unsigned spins = 0; spins < (1u << 17); ++spins) {
      const unsigned a = __hip_atomic_load(p0, __ATOMIC_RELAXED, __HIP_MEMORY_SCOPE_AGENT);
      if (!__any(a == 0xFFFFFFFFu)) break;
      __builtin_amdgcn_s_sleep(8);
    }
  }
  __syncthreads();
}
__device__ __forceinline__ void flags32_wait(int wv_, unsigned char* ws, int off) {
  if (wv_ == 0) {
    const int l = lane_id();
    __attribute__((address_space(1))) unsigned long long* p0 = (__attribute__((address_space(1))) unsigned long long*)(ws + off + (l & 31) * 64 + (l >> 5) * 8);
    for (unsigned spins = 0; spins < (1u << 17); ++spins) {
      const unsigned long long v = __hip_atomic_load(p0, __ATOMIC_RELAXED, __HIP_MEMORY_SCOPE_AGENT);
      if (!__any(max((unsigned)v, (unsigned)(v >> 32)) == 0xFFFFFFFFu)) break;
      __builtin_amdgcn_s_sleep(16);
    }
  }
  __syncthreads();
}
__device__ __forceinline__ void dep_signal(int wv_, unsigned char* ws, int off, unsigned inc = 1u) {
  asm volatile("s_waitcnt vmcnt(0)" ::: "memory");
  __syncthreads();
  if (TIDX == 0) __hip_atomic_fetch_add((gu32*)(ws + off), inc, __ATOMIC_RELAXED, __HIP_MEMORY_SCOPE_AGENT);
}

template <int SP> __device__ __forceinline__ void fan_signal(int wv_, unsigned char* ws, int fan, int i) {
  asm volatile("s_waitcnt vmcnt(0)" ::: "memory");
  __syncthreads();
  const int l = lane_id();
  if (wv_ == 0 && l < FNG) __hip_atomic_store((gu32*)(ws + fan + l * FAN_G + i * SP), nosent(rtc()), __ATOMIC_RELAXED, __HIP_MEMORY_SCOPE_AGENT);
}
constexpr int L_WHEN = 90112;
template <int SP, int N> __device__ __forceinline__ void fan_wait(int wv_, unsigned char* ws, int fan, int salt) {
  if (wv_ * 64 < N) {
    gu32* p = (gu32*)(ws + fan + (((int)xcc_id() * (FNG / 8) + (salt & (FNG / 8 - 1))) * FAN_G + min(TIDX, N - 1) * SP));
    unsigned v;
    for (unsigned spins = 0; spins < (1u << 17); ++spins) {
      v = __hip_atomic_load(p, __ATOMIC_RELAXED, __HIP_MEMORY_SCOPE_AGENT);
      const unsigned long long m = __ballot(v == 0xFFFFFFFFu);
      if (m == 0) break;
      __builtin_amdgcn_s_sleep(N == NCOL ? 24 : FNAP);
      if (N == 20 && (m & 0x3FF)) __builtin_amdgcn_s_sleep(40);
    }
    if (N == 20) {
      const unsigned t = rtc();
      *(unsigned*)(dyn_lds + L_WHEN) = t + (unsigned)wave_imax((int)(v - t));
    }
  }
  __syncthreads();
}

typedef unsigned v8u __attribute__((ext_vector_type(8)));
typedef unsigned v16u __attribute__((ext_vector_type(16)));
typedef unsigned v32u __attribute__((ext_vector_type(32)));
__device__ __forceinline__ float cosi(int i) { return __builtin_amdgcn_cosf((float)i * (1.f / 2048)); }
__device__ __forceinline__ float sini(int i) { return __builtin_amdgcn_sinf((float)i * (1.f / 2048)); }
__device__ __forceinline__ void cis8(v32u& d, int x0, int dx, int d4, unsigned m) {
#pragma unroll
  for (int q = 0; q < 4; ++q) {
    const int x = x0 + dx * 2 * (q & 1) + d4 * (q >> 1);
    d[q] = pk(cosi(x), cosi(x + dx)); d[4 + q] = d[q] ^ m;
    d[8 + q] = pk(sini(x), sini(x + dx)); d[12 + q] = d[8 + q] ^ m;
  }
}
#define LDS_V4(a) (*(__attribute__((address_space(3))) v4u*)(uintptr_t)(a))
#define LDS_U32(a) (*(__attribute__((address_space(3))) unsigned*)(uintptr_t)(a))
#define LDS_V2(a) (*(__attribute__((address_space(3))) v2u*)(uintptr_t)(a))
#define COL_ASM \
   "s_waitcnt lgkmcnt(0)\n\t" "s_barrier\n\t" "ds_read_b64_tr_b16 v[136:137], v16 offset:0\n\t" "ds_read_b64_tr_b16 v[138:139], v16 offset:1056\n\t" \
   "ds_read_b64_tr_b16 v[140:141], v16 offset:8448\n\t" "ds_read_b64_tr_b16 v[142:143], v16 offset:9504\n\t" \
   "ds_read_b64_tr_b16 v[144:145], v16 offset:8\n\t" "ds_read_b64_tr_b16 v[146:147], v16 offset:1064\n\t" \
   "ds_read_b64_tr_b16 v[148:149], v16 offset:8456\n\t" "ds_read_b64_tr_b16 v[150:151], v16 offset:9512\n\t" "s_waitcnt lgkmcnt(4)\n\t" \
   "v_mfma_f32_16x16x32_bf16 v[160:163], v[136:139], v[32:35], 0\n\t" "v_mfma_f32_16x16x32_bf16 v[164:167], v[136:139], v[40:43], 0\n\t" \
   "v_mfma_f32_16x16x32_bf16 v[160:163], v[140:143], v[36:39], v[160:163]\n\t" \
   "v_mfma_f32_16x16x32_bf16 v[164:167], v[140:143], v[44:47], v[164:167]\n\t" "ds_read_b64_tr_b16 v[152:153], v16 offset:16\n\t" \
   "ds_read_b64_tr_b16 v[154:155], v16 offset:1072\n\t" "ds_read_b64_tr_b16 v[156:157], v16 offset:8464\n\t" \
   "ds_read_b64_tr_b16 v[158:159], v16 offset:9520\n\t" "s_nop 3\n\t" "s_waitcnt lgkmcnt(4)\n\t" \
   "v_mfma_f32_16x16x32_bf16 v[168:171], v[144:147], v[32:35], 0\n\t" "v_add_f32 v160, v160, v165\n\t" "v_sub_f32 v161, v161, v164\n\t" \
   "v_mfma_f32_16x16x32_bf16 v[172:175], v[144:147], v[40:43], 0\n\t" "v_add_f32 v162, v162, v167\n\t" "v_sub_f32 v163, v163, v166\n\t" \
   "v_mfma_f32_16x16x32_bf16 v[168:171], v[148:151], v[36:39], v[168:171]\n\t" "v_mul_f32 v24, v161, v49\n\t" "v_mul_f32 v25, v161, v48\n\t" \
   "v_mfma_f32_16x16x32_bf16 v[172:175], v[148:151], v[44:47], v[172:175]\n\t" "v_mul_f32 v26, v163, v49\n\t" "v_mul_f32 v27, v163, v48\n\t" \
   "v_fma_f32 v177, v160, v49, v25\n\t" "v_fma_f32 v176, v160, v48, -v24\n\t" "v_fma_f32 v179, v162, v49, v27\n\t" \
   "v_fma_f32 v178, v162, v48, -v26\n\t" "ds_read_b64_tr_b16 v[136:137], v16 offset:24\n\t" "ds_read_b64_tr_b16 v[138:139], v16 offset:1080\n\t" \
   "ds_read_b64_tr_b16 v[140:141], v16 offset:8472\n\t" "ds_read_b64_tr_b16 v[142:143], v16 offset:9528\n\t" "s_waitcnt lgkmcnt(4)\n\t" \
   "v_mfma_f32_16x16x32_bf16 v[160:163], v[152:155], v[32:35], 0\n\t" "v_add_f32 v168, v168, v173\n\t" "v_sub_f32 v169, v169, v172\n\t" \
   "v_mfma_f32_16x16x32_bf16 v[164:167], v[152:155], v[40:43], 0\n\t" "v_add_f32 v170, v170, v175\n\t" "v_sub_f32 v171, v171, v174\n\t" \
   "v_mfma_f32_16x16x32_bf16 v[160:163], v[156:159], v[36:39], v[160:163]\n\t" "v_mul_f32 v24, v169, v51\n\t" "v_mul_f32 v25, v169, v50\n\t" \
   "v_mfma_f32_16x16x32_bf16 v[164:167], v[156:159], v[44:47], v[164:167]\n\t" "v_mul_f32 v26, v171, v51\n\t" "v_mul_f32 v27, v171, v50\n\t" \
   "v_fma_f32 v181, v168, v51, v25\n\t" "v_fma_f32 v180, v168, v50, -v24\n\t" "v_fma_f32 v183, v170, v51, v27\n\t" \
   "v_fma_f32 v182, v170, v50, -v26\n\t" "v_cvt_pk_bf16_f32 v12, v177, v181\n\t" "v_cvt_pk_bf16_f32 v8, v176, v180\n\t" \
   "v_cvt_pk_bf16_f32 v188, v179, v183\n\t" "v_cvt_pk_bf16_f32 v184, v178, v182\n\t" "ds_read_b64_tr_b16 v[144:145], v16 offset:128\n\t" \
   "ds_read_b64_tr_b16 v[146:147], v16 offset:1184\n\t" "ds_read_b64_tr_b16 v[148:149], v16 offset:8576\n\t" \
   "ds_read_b64_tr_b16 v[150:151], v16 offset:9632\n\t" "s_waitcnt lgkmcnt(4)\n\t" "v_mfma_f32_16x16x32_bf16 v[168:171], v[136:139], v[32:35], 0\n\t" \
   "v_add_f32 v160, v160, v165\n\t" "v_sub_f32 v161, v161, v164\n\t" "v_mfma_f32_16x16x32_bf16 v[172:175], v[136:139], v[40:43], 0\n\t" \
   "v_add_f32 v162, v162, v167\n\t" "v_sub_f32 v163, v163, v166\n\t" "v_mfma_f32_16x16x32_bf16 v[168:171], v[140:143], v[36:39], v[168:171]\n\t" \
   "v_mul_f32 v24, v161, v53\n\t" "v_mul_f32 v25, v161, v52\n\t" "v_mfma_f32_16x16x32_bf16 v[172:175], v[140:143], v[44:47], v[172:175]\n\t" \
   "v_mul_f32 v26, v163, v53\n\t" "v_mul_f32 v27, v163, v52\n\t" "v_fma_f32 v177, v160, v53, v25\n\t" "v_fma_f32 v176, v160, v52, -v24\n\t" \
   "v_fma_f32 v179, v162, v53, v27\n\t" "v_fma_f32 v178, v162, v52, -v26\n\t" "ds_read_b64_tr_b16 v[152:153], v16 offset:136\n\t" \
   "ds_read_b64_tr_b16 v[154:155], v16 offset:1192\n\t" "ds_read_b64_tr_b16 v[156:157], v16 offset:8584\n\t" \
   "ds_read_b64_tr_b16 v[158:159], v16 offset:9640\n\t" "s_waitcnt lgkmcnt(4)\n\t" "v_mfma_f32_16x16x32_bf16 v[160:163], v[144:147], v[32:35], 0\n\t" \
   "v_add_f32 v168, v168, v173\n\t" "v_sub_f32 v169, v169, v172\n\t" "v_mfma_f32_16x16x32_bf16 v[164:167], v[144:147], v[40:43], 0\n\t" \
   "v_add_f32 v170, v170, v175\n\t" "v_sub_f32 v171, v171, v174\n\t" "v_mfma_f32_16x16x32_bf16 v[160:163], v[148:151], v[36:39], v[160:163]\n\t" \
   "v_mul_f32 v24, v169, v55\n\t" "v_mul_f32 v25, v169, v54\n\t" "v_mfma_f32_16x16x32_bf16 v[164:167], v[148:151], v[44:47], v[164:167]\n\t" \
   "v_mul_f32 v26, v171, v55\n\t" "v_mul_f32 v27, v171, v54\n\t" "v_fma_f32 v181, v168, v55, v25\n\t" "v_fma_f32 v180, v168, v54, -v24\n\t" \
   "v_fma_f32 v183, v170, v55, v27\n\t" "v_fma_f32 v182, v170, v54, -v26\n\t" "v_cvt_pk_bf16_f32 v13, v177, v181\n\t" \
   "v_cvt_pk_bf16_f32 v9, v176, v180\n\t" "v_cvt_pk_bf16_f32 v189, v179, v183\n\t" "v_cvt_pk_bf16_f32 v185, v178, v182\n\t" \
   "ds_read_b64_tr_b16 v[136:137], v16 offset:144\n\t" "ds_read_b64_tr_b16 v[138:139], v16 offset:1200\n\t" \
   "ds_read_b64_tr_b16 v[140:141], v16 offset:8592\n\t" "ds_read_b64_tr_b16 v[142:143], v16 offset:9648\n\t" "s_waitcnt lgkmcnt(4)\n\t" \
   "v_mfma_f32_16x16x32_bf16 v[168:171], v[152:155], v[32:35], 0\n\t" "v_add_f32 v160, v160, v165\n\t" "v_sub_f32 v161, v161, v164\n\t" \
   "v_mfma_f32_16x16x32_bf16 v[172:175], v[152:155], v[40:43], 0\n\t" "v_add_f32 v162, v162, v167\n\t" "v_sub_f32 v163, v163, v166\n\t" \
   "v_mfma_f32_16x16x32_bf16 v[168:171], v[156:159], v[36:39], v[168:171]\n\t" "v_mul_f32 v24, v161, v57\n\t" "v_mul_f32 v25, v161, v56\n\t" \
   "v_mfma_f32_16x16x32_bf16 v[172:175], v[156:159], v[44:47], v[172:175]\n\t" "v_mul_f32 v26, v163, v57\n\t" "v_mul_f32 v27, v163, v56\n\t" \
   "v_fma_f32 v177, v160, v57, v25\n\t" "v_fma_f32 v176, v160, v56, -v24\n\t" "v_fma_f32 v179, v162, v57, v27\n\t" \
   "v_fma_f32 v178, v162, v56, -v26\n\t" "ds_read_b64_tr_b16 v[144:145], v16 offset:152\n\t" "ds_read_b64_tr_b16 v[146:147], v16 offset:1208\n\t" \
   "ds_read_b64_tr_b16 v[148:149], v16 offset:8600\n\t" "ds_read_b64_tr_b16 v[150:151], v16 offset:9656\n\t" "s_waitcnt lgkmcnt(4)\n\t" \
   "v_mfma_f32_16x16x32_bf16 v[160:163], v[136:139], v[32:35], 0\n\t" "v_add_f32 v168, v168, v173\n\t" "v_sub_f32 v169, v169, v172\n\t" \
   "v_mfma_f32_16x16x32_bf16 v[164:167], v[136:139], v[40:43], 0\n\t" "v_add_f32 v170, v170, v175\n\t" "v_sub_f32 v171, v171, v174\n\t" \
   "v_mfma_f32_16x16x32_bf16 v[160:163], v[140:143], v[36:39], v[160:163]\n\t" "v_mul_f32 v24, v169, v59\n\t" "v_mul_f32 v25, v169, v58\n\t" \
   "v_mfma_f32_16x16x32_bf16 v[164:167], v[140:143], v[44:47], v[164:167]\n\t" "v_mul_f32 v26, v171, v59\n\t" "v_mul_f32 v27, v171, v58\n\t" \
   "v_fma_f32 v181, v168, v59, v25\n\t" "v_fma_f32 v180, v168, v58, -v24\n\t" "v_fma_f32 v183, v170, v59, v27\n\t" \
   "v_fma_f32 v182, v170, v58, -v26\n\t" "v_cvt_pk_bf16_f32 v14, v177, v181\n\t" "v_cvt_pk_bf16_f32 v10, v176, v180\n\t" \
   "v_cvt_pk_bf16_f32 v190, v179, v183\n\t" "v_cvt_pk_bf16_f32 v186, v178, v182\n\t" "s_nop 1\n\t" "s_waitcnt lgkmcnt(0)\n\t" \
   "v_mfma_f32_16x16x32_bf16 v[168:171], v[144:147], v[32:35], 0\n\t" "v_add_f32 v160, v160, v165\n\t" "v_sub_f32 v161, v161, v164\n\t" \
   "v_mfma_f32_16x16x32_bf16 v[172:175], v[144:147], v[40:43], 0\n\t" "v_add_f32 v162, v162, v167\n\t" "v_sub_f32 v163, v163, v166\n\t" \
   "v_mfma_f32_16x16x32_bf16 v[168:171], v[148:151], v[36:39], v[168:171]\n\t" "v_mul_f32 v24, v161, v61\n\t" "v_mul_f32 v25, v161, v60\n\t" \
   "v_mfma_f32_16x16x32_bf16 v[172:175], v[148:151], v[44:47], v[172:175]\n\t" "v_mul_f32 v26, v163, v61\n\t" "v_mul_f32 v27, v163, v60\n\t" \
   "v_fma_f32 v177, v160, v61, v25\n\t" "v_fma_f32 v176, v160, v60, -v24\n\t" "v_fma_f32 v179, v162, v61, v27\n\t" \
   "v_fma_f32 v178, v162, v60, -v26\n\t" "s_nop 1\n\t" "v_add_f32 v168, v168, v173\n\t" "v_sub_f32 v169, v169, v172\n\t" "v_mul_f32 v24, v169, v63\n\t" \
   "v_mul_f32 v25, v169, v62\n\t" "v_fma_f32 v181, v168, v63, v25\n\t" "v_fma_f32 v180, v168, v62, -v24\n\t" "v_cvt_pk_bf16_f32 v15, v177, v181\n\t" \
   "v_cvt_pk_bf16_f32 v11, v176, v180\n\t" "s_nop 1\n\t" "v_mfma_f32_16x16x32_bf16 v[160:163], v[64:67], v[8:11], 0\n\t" \
   "v_add_f32 v170, v170, v175\n\t" "v_sub_f32 v171, v171, v174\n\t" "v_mfma_f32_16x16x32_bf16 v[164:167], v[64:67], v[12:15], 0\n\t" \
   "v_mul_f32 v24, v171, v63\n\t" "v_mfma_f32_16x16x32_bf16 v[160:163], v[72:75], v[12:15], v[160:163]\n\t" "v_mul_f32 v25, v171, v62\n\t" \
   "v_mfma_f32_16x16x32_bf16 v[164:167], v[128:131], v[8:11], v[164:167]\n\t" "v_fma_f32 v183, v170, v63, v25\n\t" \
   "v_fma_f32 v182, v170, v62, -v24\n\t" "v_cvt_pk_bf16_f32 v191, v179, v183\n\t" "v_cvt_pk_bf16_f32 v187, v178, v182\n\t" "s_nop 4\n\t" \
   "v_mfma_f32_16x16x32_bf16 v[168:171], v[68:71], v[8:11], 0\n\t" "v_mul_f32 v24, v164, v97\n\t" "v_mul_f32 v25, v164, v96\n\t" \
   "v_mul_f32 v26, v165, v99\n\t" "v_mfma_f32_16x16x32_bf16 v[172:175], v[68:71], v[12:15], 0\n\t" "v_mul_f32 v27, v165, v98\n\t" \
   "v_mul_f32 v28, v166, v101\n\t" "v_mul_f32 v29, v166, v100\n\t" "v_mfma_f32_16x16x32_bf16 v[168:171], v[76:79], v[12:15], v[168:171]\n\t" \
   "v_mul_f32 v30, v167, v103\n\t" "v_mul_f32 v31, v167, v102\n\t" "v_fma_f32 v164, v160, v97, v25\n\t" \
   "v_mfma_f32_16x16x32_bf16 v[172:175], v[132:135], v[8:11], v[172:175]\n\t" "v_fma_f32 v160, v160, v96, -v24\n\t" \
   "v_fma_f32 v165, v161, v99, v27\n\t" "v_fma_f32 v161, v161, v98, -v26\n\t" "v_fma_f32 v166, v162, v101, v29\n\t" \
   "v_fma_f32 v162, v162, v100, -v28\n\t" "v_fma_f32 v167, v163, v103, v31\n\t" "v_fma_f32 v163, v163, v102, -v30\n\t" \
   "v_cvt_pk_bf16_f32 v140, v164, v165\n\t" "v_cvt_pk_bf16_f32 v136, v160, v161\n\t" "v_cvt_pk_bf16_f32 v141, v166, v167\n\t" \
   "v_cvt_pk_bf16_f32 v137, v162, v163\n\t" "v_mfma_f32_16x16x32_bf16 v[160:163], v[64:67], v[184:187], 0\n\t" "v_mul_f32 v24, v172, v105\n\t" \
   "v_mul_f32 v25, v172, v104\n\t" "v_mul_f32 v26, v173, v107\n\t" "v_mfma_f32_16x16x32_bf16 v[164:167], v[64:67], v[188:191], 0\n\t" \
   "v_mul_f32 v27, v173, v106\n\t" "v_mul_f32 v28, v174, v109\n\t" "v_mul_f32 v29, v174, v108\n\t" \
   "v_mfma_f32_16x16x32_bf16 v[160:163], v[72:75], v[188:191], v[160:163]\n\t" "v_mul_f32 v30, v175, v111\n\t" "v_mul_f32 v31, v175, v110\n\t" \
   "v_fma_f32 v172, v168, v105, v25\n\t" "v_mfma_f32_16x16x32_bf16 v[164:167], v[128:131], v[184:187], v[164:167]\n\t" \
   "v_fma_f32 v168, v168, v104, -v24\n\t" "v_fma_f32 v173, v169, v107, v27\n\t" "v_fma_f32 v169, v169, v106, -v26\n\t" \
   "v_fma_f32 v174, v170, v109, v29\n\t" "v_fma_f32 v170, v170, v108, -v28\n\t" "v_fma_f32 v175, v171, v111, v31\n\t" \
   "v_fma_f32 v171, v171, v110, -v30\n\t" "v_cvt_pk_bf16_f32 v142, v172, v173\n\t" "v_cvt_pk_bf16_f32 v138, v168, v169\n\t" \
   "v_cvt_pk_bf16_f32 v143, v174, v175\n\t" "v_cvt_pk_bf16_f32 v139, v170, v171\n\t" "v_mfma_f32_16x16x32_bf16 v[168:171], v[68:71], v[184:187], 0\n\t" \
   "v_mul_f32 v24, v164, v113\n\t" "v_mul_f32 v25, v164, v112\n\t" "v_mul_f32 v26, v165, v115\n\t" \
   "v_mfma_f32_16x16x32_bf16 v[172:175], v[68:71], v[188:191], 0\n\t" "v_mul_f32 v27, v165, v114\n\t" "v_mul_f32 v28, v166, v117\n\t" \
   "v_mul_f32 v29, v166, v116\n\t" "v_mfma_f32_16x16x32_bf16 v[168:171], v[76:79], v[188:191], v[168:171]\n\t" "v_mul_f32 v30, v167, v119\n\t" \
   "v_mul_f32 v31, v167, v118\n\t" "v_fma_f32 v164, v160, v113, v25\n\t" "v_mfma_f32_16x16x32_bf16 v[172:175], v[132:135], v[184:187], v[172:175]\n\t" \
   "v_fma_f32 v160, v160, v112, -v24\n\t" "v_fma_f32 v165, v161, v115, v27\n\t" "v_fma_f32 v161, v161, v114, -v26\n\t" \
   "v_fma_f32 v166, v162, v117, v29\n\t" "v_fma_f32 v162, v162, v116, -v28\n\t" "v_fma_f32 v167, v163, v119, v31\n\t" \
   "v_fma_f32 v163, v163, v118, -v30\n\t" "v_cvt_pk_bf16_f32 v148, v164, v165\n\t" "v_cvt_pk_bf16_f32 v144, v160, v161\n\t" \
   "v_cvt_pk_bf16_f32 v149, v166, v167\n\t" "v_cvt_pk_bf16_f32 v145, v162, v163\n\t" "v_mfma_f32_16x16x32_bf16 v[160:163], v[136:139], v[64:67], 0\n\t" \
   "v_mul_f32 v24, v172, v121\n\t" "v_mul_f32 v25, v172, v120\n\t" "v_mul_f32 v26, v173, v123\n\t" \
   "v_mfma_f32_16x16x32_bf16 v[164:167], v[140:143], v[64:67], 0\n\t" "v_mul_f32 v27, v173, v122\n\t" "v_mul_f32 v28, v174, v125\n\t" \
   "v_mul_f32 v29, v174, v124\n\t" "v_mfma_f32_16x16x32_bf16 v[160:163], v[140:143], v[128:131], v[160:163]\n\t" "v_mul_f32 v30, v175, v127\n\t" \
   "v_mul_f32 v31, v175, v126\n\t" "v_fma_f32 v172, v168, v121, v25\n\t" "v_mfma_f32_16x16x32_bf16 v[164:167], v[136:139], v[72:75], v[164:167]\n\t" \
   "v_fma_f32 v168, v168, v120, -v24\n\t" "v_fma_f32 v173, v169, v123, v27\n\t" "v_fma_f32 v169, v169, v122, -v26\n\t" \
   "v_fma_f32 v174, v170, v125, v29\n\t" "v_fma_f32 v170, v170, v124, -v28\n\t" "v_fma_f32 v175, v171, v127, v31\n\t" \
   "v_fma_f32 v171, v171, v126, -v30\n\t" "v_cvt_pk_bf16_f32 v150, v172, v173\n\t" "v_cvt_pk_bf16_f32 v146, v168, v169\n\t" \
   "v_cvt_pk_bf16_f32 v151, v174, v175\n\t" "v_cvt_pk_bf16_f32 v147, v170, v171\n\t" "v_mfma_f32_16x16x32_bf16 v[168:171], v[136:139], v[68:71], 0\n\t" \
   "v_mul_f32 v24, v164, v81\n\t" "v_mul_f32 v25, v164, v80\n\t" "v_mul_f32 v26, v165, v83\n\t" \
   "v_mfma_f32_16x16x32_bf16 v[172:175], v[140:143], v[68:71], 0\n\t" "v_mul_f32 v27, v165, v82\n\t" "v_mul_f32 v28, v166, v85\n\t" \
   "v_mul_f32 v29, v166, v84\n\t" "v_mfma_f32_16x16x32_bf16 v[168:171], v[140:143], v[132:135], v[168:171]\n\t" "v_mul_f32 v30, v167, v87\n\t" \
   "v_mul_f32 v31, v167, v86\n\t" "v_fma_f32 v164, v160, v81, v25\n\t" "v_mfma_f32_16x16x32_bf16 v[172:175], v[136:139], v[76:79], v[172:175]\n\t" \
   "v_fma_f32 v160, v160, v80, -v24\n\t" "v_fma_f32 v165, v161, v83, v27\n\t" "v_fma_f32 v161, v161, v82, -v26\n\t" \
   "v_fma_f32 v166, v162, v85, v29\n\t" "v_fma_f32 v162, v162, v84, -v28\n\t" "v_fma_f32 v167, v163, v87, v31\n\t" \
   "v_fma_f32 v163, v163, v86, -v30\n\t" "v_cvt_pk_bf16_f32 v178, v164, v165\n\t" "v_cvt_pk_bf16_f32 v176, v160, v161\n\t" \
   "v_cvt_pk_bf16_f32 v179, v166, v167\n\t" "v_cvt_pk_bf16_f32 v177, v162, v163\n\t" "ds_write_b64 v17, v[178:179] offset:128\n\t" \
   "ds_write_b64 v17, v[176:177] offset:0\n\t" "v_mfma_f32_16x16x32_bf16 v[160:163], v[144:147], v[64:67], 0\n\t" "v_mul_f32 v24, v172, v89\n\t" \
   "v_mul_f32 v25, v172, v88\n\t" "v_mul_f32 v26, v173, v91\n\t" "v_mfma_f32_16x16x32_bf16 v[164:167], v[148:151], v[64:67], 0\n\t" \
   "v_mul_f32 v27, v173, v90\n\t" "v_mul_f32 v28, v174, v93\n\t" "v_mul_f32 v29, v174, v92\n\t" \
   "v_mfma_f32_16x16x32_bf16 v[160:163], v[148:151], v[128:131], v[160:163]\n\t" "v_mul_f32 v30, v175, v95\n\t" "v_mul_f32 v31, v175, v94\n\t" \
   "v_fma_f32 v172, v168, v89, v25\n\t" "v_mfma_f32_16x16x32_bf16 v[164:167], v[144:147], v[72:75], v[164:167]\n\t" \
   "v_fma_f32 v168, v168, v88, -v24\n\t" "v_fma_f32 v173, v169, v91, v27\n\t" "v_fma_f32 v169, v169, v90, -v26\n\t" \
   "v_fma_f32 v174, v170, v93, v29\n\t" "v_fma_f32 v170, v170, v92, -v28\n\t" "v_fma_f32 v175, v171, v95, v31\n\t" \
   "v_fma_f32 v171, v171, v94, -v30\n\t" "v_cvt_pk_bf16_f32 v182, v172, v173\n\t" "v_cvt_pk_bf16_f32 v180, v168, v169\n\t" \
   "v_cvt_pk_bf16_f32 v183, v174, v175\n\t" "v_cvt_pk_bf16_f32 v181, v170, v171\n\t" "ds_write_b64 v17, v[182:183] offset:8384\n\t" \
   "ds_write_b64 v17, v[180:181] offset:8256\n\t" "v_mfma_f32_16x16x32_bf16 v[168:171], v[144:147], v[68:71], 0\n\t" "v_mul_f32 v24, v164, v81\n\t" \
   "v_mul_f32 v25, v164, v80\n\t" "v_mul_f32 v26, v165, v83\n\t" "v_mfma_f32_16x16x32_bf16 v[172:175], v[148:151], v[68:71], 0\n\t" \
   "v_mul_f32 v27, v165, v82\n\t" "v_mul_f32 v28, v166, v85\n\t" "v_mul_f32 v29, v166, v84\n\t" \
   "v_mfma_f32_16x16x32_bf16 v[168:171], v[148:151], v[132:135], v[168:171]\n\t" "v_mul_f32 v30, v167, v87\n\t" "v_mul_f32 v31, v167, v86\n\t" \
   "v_fma_f32 v164, v160, v81, v25\n\t" "v_mfma_f32_16x16x32_bf16 v[172:175], v[144:147], v[76:79], v[172:175]\n\t" \
   "v_fma_f32 v160, v160, v80, -v24\n\t" "v_fma_f32 v165, v161, v83, v27\n\t" "v_fma_f32 v161, v161, v82, -v26\n\t" \
   "v_fma_f32 v166, v162, v85, v29\n\t" "v_fma_f32 v162, v162, v84, -v28\n\t" "v_fma_f32 v167, v163, v87, v31\n\t" \
   "v_fma_f32 v163, v163, v86, -v30\n\t" "v_cvt_pk_bf16_f32 v178, v164, v165\n\t" "v_cvt_pk_bf16_f32 v176, v160, v161\n\t" \
   "v_cvt_pk_bf16_f32 v179, v166, v167\n\t" "v_cvt_pk_bf16_f32 v177, v162, v163\n\t" "ds_write_b64 v18, v[178:179] offset:128\n\t" \
   "ds_write_b64 v18, v[176:177] offset:0\n\t" "v_mul_f32 v24, v172, v89\n\t" "v_mul_f32 v25, v172, v88\n\t" "v_mul_f32 v26, v173, v91\n\t" \
   "v_mul_f32 v27, v173, v90\n\t" "v_mul_f32 v28, v174, v93\n\t" "v_mul_f32 v29, v174, v92\n\t" "v_mul_f32 v30, v175, v95\n\t" \
   "v_mul_f32 v31, v175, v94\n\t" "v_fma_f32 v172, v168, v89, v25\n\t" "v_fma_f32 v168, v168, v88, -v24\n\t" "v_fma_f32 v173, v169, v91, v27\n\t" \
   "v_fma_f32 v169, v169, v90, -v26\n\t" "v_fma_f32 v174, v170, v93, v29\n\t" "v_fma_f32 v170, v170, v92, -v28\n\t" \
   "v_fma_f32 v175, v171, v95, v31\n\t" "v_fma_f32 v171, v171, v94, -v30\n\t" "v_cvt_pk_bf16_f32 v182, v172, v173\n\t" \
   "v_cvt_pk_bf16_f32 v180, v168, v169\n\t" "v_cvt_pk_bf16_f32 v183, v174, v175\n\t" "v_cvt_pk_bf16_f32 v181, v170, v171\n\t" \
   "ds_write_b64 v18, v[182:183] offset:8384\n\t" "ds_write_b64 v18, v[180:181] offset:8256\n\t" "s_waitcnt lgkmcnt(0)\n\t" "s_barrier\n\t" \
   "ds_read_b128 v[136:139], v19 offset:0\n\t" "ds_read_b128 v[140:143], v20 offset:0\n\t" "ds_read_b128 v[144:147], v19 offset:2064\n\t" \
   "ds_read_b128 v[148:151], v20 offset:2064\n\t" "s_waitcnt lgkmcnt(2)\n\t" "v_mfma_f32_16x16x32_bf16 v[160:163], v[136:139], v[32:35], 0\n\t" \
   "ds_read_b128 v[152:155], v19 offset:4128\n\t" "ds_read_b128 v[156:159], v20 offset:4128\n\t" \
   "v_mfma_f32_16x16x32_bf16 v[164:167], v[136:139], v[40:43], 0\n\t" "v_mfma_f32_16x16x32_bf16 v[160:163], v[140:143], v[36:39], v[160:163]\n\t" \
   "v_mfma_f32_16x16x32_bf16 v[164:167], v[140:143], v[44:47], v[164:167]\n\t" "s_waitcnt lgkmcnt(2)\n\t" \
   "v_mfma_f32_16x16x32_bf16 v[168:171], v[144:147], v[32:35], 0\n\t" "ds_read_b128 v[136:139], v19 offset:6192\n\t" \
   "ds_read_b128 v[140:143], v20 offset:6192\n\t" "v_mfma_f32_16x16x32_bf16 v[172:175], v[144:147], v[40:43], 0\n\t" "v_sub_f32 v160, v160, v165\n\t" \
   "v_add_f32 v161, v161, v164\n\t" "v_sub_f32 v162, v162, v167\n\t" "v_mfma_f32_16x16x32_bf16 v[168:171], v[148:151], v[36:39], v[168:171]\n\t" \
   "v_add_f32 v163, v163, v166\n\t" "v_cvt_pk_bf16_f32 v8, v160, v161\n\t" "v_cvt_pk_bf16_f32 v9, v162, v163\n\t" \
   "v_mfma_f32_16x16x32_bf16 v[172:175], v[148:151], v[44:47], v[172:175]\n\t" "s_waitcnt lgkmcnt(2)\n\t" \
   "v_mfma_f32_16x16x32_bf16 v[160:163], v[152:155], v[32:35], 0\n\t" "ds_read_b128 v[144:147], v19 offset:8256\n\t" \
   "ds_read_b128 v[148:151], v20 offset:8256\n\t" "v_mfma_f32_16x16x32_bf16 v[164:167], v[152:155], v[40:43], 0\n\t" "v_sub_f32 v168, v168, v173\n\t" \
   "v_add_f32 v169, v169, v172\n\t" "v_sub_f32 v170, v170, v175\n\t" "v_mfma_f32_16x16x32_bf16 v[160:163], v[156:159], v[36:39], v[160:163]\n\t" \
   "v_add_f32 v171, v171, v174\n\t" "v_cvt_pk_bf16_f32 v10, v168, v169\n\t" "v_cvt_pk_bf16_f32 v11, v170, v171\n\t" \
   "v_mfma_f32_16x16x32_bf16 v[164:167], v[156:159], v[44:47], v[164:167]\n\t" "buffer_store_dwordx4 v[8:11], v21, %[rs], 0 offen offset:0 sc1\n\t" \
   "s_waitcnt lgkmcnt(2)\n\t" "v_mfma_f32_16x16x32_bf16 v[168:171], v[136:139], v[32:35], 0\n\t" "ds_read_b128 v[152:155], v19 offset:10320\n\t" \
   "ds_read_b128 v[156:159], v20 offset:10320\n\t" "v_mfma_f32_16x16x32_bf16 v[172:175], v[136:139], v[40:43], 0\n\t" "v_sub_f32 v160, v160, v165\n\t" \
   "v_add_f32 v161, v161, v164\n\t" "v_sub_f32 v162, v162, v167\n\t" "v_mfma_f32_16x16x32_bf16 v[168:171], v[140:143], v[36:39], v[168:171]\n\t" \
   "v_add_f32 v163, v163, v166\n\t" "v_cvt_pk_bf16_f32 v12, v160, v161\n\t" "v_cvt_pk_bf16_f32 v13, v162, v163\n\t" \
   "v_mfma_f32_16x16x32_bf16 v[172:175], v[140:143], v[44:47], v[172:175]\n\t" "s_waitcnt lgkmcnt(2)\n\t" \
   "v_mfma_f32_16x16x32_bf16 v[160:163], v[144:147], v[32:35], 0\n\t" "ds_read_b128 v[136:139], v19 offset:12384\n\t" \
   "ds_read_b128 v[140:143], v20 offset:12384\n\t" "v_mfma_f32_16x16x32_bf16 v[164:167], v[144:147], v[40:43], 0\n\t" "v_sub_f32 v168, v168, v173\n\t" \
   "v_add_f32 v169, v169, v172\n\t" "v_sub_f32 v170, v170, v175\n\t" "v_mfma_f32_16x16x32_bf16 v[160:163], v[148:151], v[36:39], v[160:163]\n\t" \
   "v_add_f32 v171, v171, v174\n\t" "v_cvt_pk_bf16_f32 v14, v168, v169\n\t" "v_cvt_pk_bf16_f32 v15, v170, v171\n\t" \
   "v_mfma_f32_16x16x32_bf16 v[164:167], v[148:151], v[44:47], v[164:167]\n\t" "buffer_store_dwordx4 v[12:15], v21, %[rs], 0 offen offset:64 sc1\n\t" \
   "s_waitcnt lgkmcnt(2)\n\t" "v_mfma_f32_16x16x32_bf16 v[168:171], v[152:155], v[32:35], 0\n\t" "ds_read_b128 v[144:147], v19 offset:14448\n\t" \
   "ds_read_b128 v[148:151], v20 offset:14448\n\t" "v_mfma_f32_16x16x32_bf16 v[172:175], v[152:155], v[40:43], 0\n\t" "v_sub_f32 v160, v160, v165\n\t" \
   "v_add_f32 v161, v161, v164\n\t" "v_sub_f32 v162, v162, v167\n\t" "v_mfma_f32_16x16x32_bf16 v[168:171], v[156:159], v[36:39], v[168:171]\n\t" \
   "v_add_f32 v163, v163, v166\n\t" "v_cvt_pk_bf16_f32 v8, v160, v161\n\t" "v_cvt_pk_bf16_f32 v9, v162, v163\n\t" \
   "v_mfma_f32_16x16x32_bf16 v[172:175], v[156:159], v[44:47], v[172:175]\n\t" "s_waitcnt lgkmcnt(2)\n\t" \
   "v_mfma_f32_16x16x32_bf16 v[160:163], v[136:139], v[32:35], 0\n\t" "s_nop 0\n\t" "s_nop 0\n\t" \
   "v_mfma_f32_16x16x32_bf16 v[164:167], v[136:139], v[40:43], 0\n\t" "v_sub_f32 v168, v168, v173\n\t" "v_add_f32 v169, v169, v172\n\t" \
   "v_sub_f32 v170, v170, v175\n\t" "v_mfma_f32_16x16x32_bf16 v[160:163], v[140:143], v[36:39], v[160:163]\n\t" "v_add_f32 v171, v171, v174\n\t" \
   "v_cvt_pk_bf16_f32 v10, v168, v169\n\t" "v_cvt_pk_bf16_f32 v11, v170, v171\n\t" \
   "v_mfma_f32_16x16x32_bf16 v[164:167], v[140:143], v[44:47], v[164:167]\n\t" "buffer_store_dwordx4 v[8:11], v21, %[rs], 0 offen offset:128 sc1\n\t" \
   "s_waitcnt lgkmcnt(0)\n\t" "v_mfma_f32_16x16x32_bf16 v[168:171], v[144:147], v[32:35], 0\n\t" "s_nop 0\n\t" "s_nop 0\n\t" \
   "v_mfma_f32_16x16x32_bf16 v[172:175], v[144:147], v[40:43], 0\n\t" "v_sub_f32 v160, v160, v165\n\t" "v_add_f32 v161, v161, v164\n\t" \
   "v_sub_f32 v162, v162, v167\n\t" "v_mfma_f32_16x16x32_bf16 v[168:171], v[148:151], v[36:39], v[168:171]\n\t" "v_add_f32 v163, v163, v166\n\t" \
   "v_cvt_pk_bf16_f32 v12, v160, v161\n\t" "v_cvt_pk_bf16_f32 v13, v162, v163\n\t" \
   "v_mfma_f32_16x16x32_bf16 v[172:175], v[148:151], v[44:47], v[172:175]\n\t" "s_nop 7\n\t" "v_sub_f32 v168, v168, v173\n\t" \
   "v_add_f32 v169, v169, v172\n\t" "v_sub_f32 v170, v170, v175\n\t" "v_add_f32 v171, v171, v174\n\t" "v_cvt_pk_bf16_f32 v14, v168, v169\n\t" \
   "v_cvt_pk_bf16_f32 v15, v170, v171\n\t" "buffer_store_dwordx4 v[12:15], v21, %[rs], 0 offen offset:192 sc1\n\t" "s_nop 1"
#define COL_CLOB "v8", "v9", "v10", "v11", "v12", "v13", "v14", "v15", "v24", "v25", "v26", "v27", "v28", "v29", "v30", "v31", "v136", "v137", "v138", "v139", "v140", "v141", "v142", "v143", "v144", "v145", "v146", "v147", "v148", "v149", "v150", "v151", "v152", "v153", "v154", "v155", "v156", "v157", "v158", "v159", "v160", "v161", "v162", "v163", "v164", "v165", "v166", "v167", "v168", "v169", "v170", "v171", "v172", "v173", "v174", "v175", "v176", "v177", "v178", "v179", "v180", "v181", "v182", "v183", "v184", "v185", "v186", "v187", "v188", "v189", "v190", "v191"
#define IM_ASM \
   "s_mov_b32 s40, 0\n\t" "buffer_load_dwordx4 a[0:3], %[o0], %[rs], s40 offen offset:0 sc1\n\t" "s_mov_b32 s41, 20480\n\t" \
   "buffer_load_dwordx4 a[80:83], %[o0], %[rs], s41 offen offset:0 sc1\n\t" "s_mov_b32 s42, 40960\n\t" \
   "buffer_load_dwordx4 a[160:163], %[o1], %[rs], s42 offen offset:0 sc1\n\t" "buffer_load_dwordx4 a[4:7], %[o0], %[rs], s40 offen offset:1024 sc1\n\t" \
   "buffer_load_dwordx4 a[84:87], %[o0], %[rs], s41 offen offset:1024 sc1\n\t" \
   "buffer_load_dwordx4 a[164:167], %[o1], %[rs], s42 offen offset:1024 sc1\n\t" \
   "buffer_load_dwordx4 a[8:11], %[o0], %[rs], s40 offen offset:2048 sc1\n\t" \
   "buffer_load_dwordx4 a[88:91], %[o0], %[rs], s41 offen offset:2048 sc1\n\t" \
   "buffer_load_dwordx4 a[168:171], %[o1], %[rs], s42 offen offset:2048 sc1\n\t" \
   "buffer_load_dwordx4 a[12:15], %[o0], %[rs], s40 offen offset:3072 sc1\n\t" \
   "buffer_load_dwordx4 a[92:95], %[o0], %[rs], s41 offen offset:3072 sc1\n\t" \
   "buffer_load_dwordx4 a[172:175], %[o1], %[rs], s42 offen offset:3072 sc1\n\t" "s_mov_b32 s40, 4096\n\t" \
   "buffer_load_dwordx4 a[16:19], %[o0], %[rs], s40 offen offset:0 sc1\n\t" "s_mov_b32 s41, 24576\n\t" \
   "buffer_load_dwordx4 a[96:99], %[o0], %[rs], s41 offen offset:0 sc1\n\t" "s_mov_b32 s42, 45056\n\t" \
   "buffer_load_dwordx4 a[176:179], %[o1], %[rs], s42 offen offset:0 sc1\n\t" \
   "buffer_load_dwordx4 a[20:23], %[o0], %[rs], s40 offen offset:1024 sc1\n\t" \
   "buffer_load_dwordx4 a[100:103], %[o0], %[rs], s41 offen offset:1024 sc1\n\t" \
   "buffer_load_dwordx4 a[180:183], %[o1], %[rs], s42 offen offset:1024 sc1\n\t" \
   "buffer_load_dwordx4 a[24:27], %[o0], %[rs], s40 offen offset:2048 sc1\n\t" \
   "buffer_load_dwordx4 a[104:107], %[o0], %[rs], s41 offen offset:2048 sc1\n\t" \
   "buffer_load_dwordx4 a[184:187], %[o1], %[rs], s42 offen offset:2048 sc1\n\t" \
   "buffer_load_dwordx4 a[28:31], %[o0], %[rs], s40 offen offset:3072 sc1\n\t" \
   "buffer_load_dwordx4 a[108:111], %[o0], %[rs], s41 offen offset:3072 sc1\n\t" \
   "buffer_load_dwordx4 a[188:191], %[o1], %[rs], s42 offen offset:3072 sc1\n\t" "s_mov_b32 s40, 8192\n\t" \
   "buffer_load_dwordx4 a[32:35], %[o0], %[rs], s40 offen offset:0 sc1\n\t" "s_mov_b32 s41, 28672\n\t" \
   "buffer_load_dwordx4 a[112:115], %[o0], %[rs], s41 offen offset:0 sc1\n\t" "s_mov_b32 s42, 49152\n\t" \
   "buffer_load_dwordx4 a[192:195], %[o1], %[rs], s42 offen offset:0 sc1\n\t" \
   "buffer_load_dwordx4 a[36:39], %[o0], %[rs], s40 offen offset:1024 sc1\n\t" \
   "buffer_load_dwordx4 a[116:119], %[o0], %[rs], s41 offen offset:1024 sc1\n\t" \
   "buffer_load_dwordx4 a[196:199], %[o1], %[rs], s42 offen offset:1024 sc1\n\t" \
   "buffer_load_dwordx4 a[40:43], %[o0], %[rs], s40 offen offset:2048 sc1\n\t" \
   "buffer_load_dwordx4 a[120:123], %[o0], %[rs], s41 offen offset:2048 sc1\n\t" \
   "buffer_load_dwordx4 a[200:203], %[o1], %[rs], s42 offen offset:2048 sc1\n\t" \
   "buffer_load_dwordx4 a[44:47], %[o0], %[rs], s40 offen offset:3072 sc1\n\t" \
   "buffer_load_dwordx4 a[124:127], %[o0], %[rs], s41 offen offset:3072 sc1\n\t" \
   "buffer_load_dwordx4 a[204:207], %[o1], %[rs], s42 offen offset:3072 sc1\n\t" "s_mov_b32 s40, 12288\n\t" \
   "buffer_load_dwordx4 a[48:51], %[o0], %[rs], s40 offen offset:0 sc1\n\t" "s_mov_b32 s41, 32768\n\t" \
   "buffer_load_dwordx4 a[128:131], %[o0], %[rs], s41 offen offset:0 sc1\n\t" "s_mov_b32 s42, 53248\n\t" \
   "buffer_load_dwordx4 a[208:211], %[o1], %[rs], s42 offen offset:0 sc1\n\t" \
   "buffer_load_dwordx4 a[52:55], %[o0], %[rs], s40 offen offset:1024 sc1\n\t" \
   "buffer_load_dwordx4 a[132:135], %[o0], %[rs], s41 offen offset:1024 sc1\n\t" \
   "buffer_load_dwordx4 a[212:215], %[o1], %[rs], s42 offen offset:1024 sc1\n\t" \
   "buffer_load_dwordx4 a[56:59], %[o0], %[rs], s40 offen offset:2048 sc1\n\t" \
   "buffer_load_dwordx4 a[136:139], %[o0], %[rs], s41 offen offset:2048 sc1\n\t" \
   "buffer_load_dwordx4 a[216:219], %[o1], %[rs], s42 offen offset:2048 sc1\n\t" \
   "buffer_load_dwordx4 a[60:63], %[o0], %[rs], s40 offen offset:3072 sc1\n\t" \
   "buffer_load_dwordx4 a[140:143], %[o0], %[rs], s41 offen offset:3072 sc1\n\t" \
   "buffer_load_dwordx4 a[220:223], %[o1], %[rs], s42 offen offset:3072 sc1\n\t" "s_mov_b32 s40, 16384\n\t" \
   "buffer_load_dwordx4 a[64:67], %[o0], %[rs], s40 offen offset:0 sc1\n\t" "s_mov_b32 s41, 36864\n\t" \
   "buffer_load_dwordx4 a[144:147], %[o0], %[rs], s41 offen offset:0 sc1\n\t" "s_mov_b32 s42, 57344\n\t" \
   "buffer_load_dwordx4 a[224:227], %[o1], %[rs], s42 offen offset:0 sc1\n\t" \
   "buffer_load_dwordx4 a[68:71], %[o0], %[rs], s40 offen offset:1024 sc1\n\t" \
   "buffer_load_dwordx4 a[148:151], %[o0], %[rs], s41 offen offset:1024 sc1\n\t" \
   "buffer_load_dwordx4 a[228:231], %[o1], %[rs], s42 offen offset:1024 sc1\n\t" \
   "buffer_load_dwordx4 a[72:75], %[o0], %[rs], s40 offen offset:2048 sc1\n\t" \
   "buffer_load_dwordx4 a[152:155], %[o0], %[rs], s41 offen offset:2048 sc1\n\t" \
   "buffer_load_dwordx4 a[232:235], %[o1], %[rs], s42 offen offset:2048 sc1\n\t" \
   "buffer_load_dwordx4 a[76:79], %[o0], %[rs], s40 offen offset:3072 sc1\n\t" \
   "buffer_load_dwordx4 a[156:159], %[o0], %[rs], s41 offen offset:3072 sc1\n\t" \
   "buffer_load_dwordx4 a[236:239], %[o1], %[rs], s42 offen offset:3072 sc1\n\t" "s_waitcnt vmcnt(0)"
#define IM_CLOB "s40", "s41", "s42"
#define ID_ASM \
   "s_waitcnt lgkmcnt(0)\n\t" "s_barrier\n\t" "ds_read_b128 v[32:35], %[la] offset:0\n\t" "ds_read_b128 v[36:39], %[la] offset:10240\n\t" \
   "ds_read_b128 v[40:43], %[la] offset:1024\n\t" "ds_read_b128 v[44:47], %[la] offset:11264\n\t" "ds_read_b128 v[48:51], %[la] offset:2048\n\t" \
   "ds_read_b128 v[52:55], %[la] offset:12288\n\t" "ds_read_b128 v[56:59], %[la] offset:3072\n\t" "ds_read_b128 v[60:63], %[la] offset:13312\n\t" \
   "ds_read_b128 v[64:67], %[la] offset:4096\n\t" "ds_read_b128 v[68:71], %[la] offset:14336\n\t" "ds_read_b128 v[72:75], %[la] offset:5120\n\t" \
   "ds_read_b128 v[76:79], %[la] offset:15360\n\t" "ds_read_b128 v[80:83], %[la] offset:6144\n\t" "ds_read_b128 v[84:87], %[la] offset:16384\n\t" \
   "ds_read_b128 v[88:91], %[la] offset:7168\n\t" "ds_read_b128 v[92:95], %[la] offset:17408\n\t" "ds_read_b128 v[96:99], %[la] offset:8192\n\t" \
   "ds_read_b128 v[100:103], %[la] offset:18432\n\t" "ds_read_b128 v[104:107], %[la] offset:9216\n\t" "ds_read_b128 v[108:111], %[la] offset:19456\n\t" \
   "v_lshlrev_b32 v152, 16, v136\n\t" "v_and_b32 v153, 0xffff0000, v136\n\t" "v_lshlrev_b32 v154, 16, v137\n\t" "v_and_b32 v155, 0xffff0000, v137\n\t" \
   "v_lshlrev_b32 v156, 16, v140\n\t" "v_and_b32 v157, 0xffff0000, v140\n\t" "v_lshlrev_b32 v158, 16, v141\n\t" "v_and_b32 v159, 0xffff0000, v141\n\t" \
   "v_lshlrev_b32 v160, 16, v138\n\t" "v_and_b32 v161, 0xffff0000, v138\n\t" "v_lshlrev_b32 v162, 16, v139\n\t" "v_and_b32 v163, 0xffff0000, v139\n\t" \
   "s_waitcnt lgkmcnt(15)\n\t" "v_mfma_f32_16x16x32_bf16 v[112:115], a[0:3], v[32:35], 0\n\t" "v_lshlrev_b32 v164, 16, v142\n\t" \
   "v_and_b32 v165, 0xffff0000, v142\n\t" "v_mfma_f32_16x16x32_bf16 v[116:119], a[4:7], v[36:39], 0\n\t" "v_lshlrev_b32 v166, 16, v143\n\t" \
   "v_and_b32 v167, 0xffff0000, v143\n\t" "v_mfma_f32_16x16x32_bf16 v[112:115], a[8:11], v[40:43], v[112:115]\n\t" "v_lshlrev_b32 v168, 16, v144\n\t" \
   "v_and_b32 v169, 0xffff0000, v144\n\t" "v_mfma_f32_16x16x32_bf16 v[116:119], a[12:15], v[44:47], v[116:119]\n\t" "v_lshlrev_b32 v170, 16, v145\n\t" \
   "v_and_b32 v171, 0xffff0000, v145\n\t" "v_mfma_f32_16x16x32_bf16 v[112:115], a[16:19], v[48:51], v[112:115]\n\t" "v_lshlrev_b32 v172, 16, v146\n\t" \
   "v_and_b32 v173, 0xffff0000, v146\n\t" "s_waitcnt lgkmcnt(14)\n\t" "v_mfma_f32_16x16x32_bf16 v[116:119], a[20:23], v[52:55], v[116:119]\n\t" \
   "v_lshlrev_b32 v174, 16, v147\n\t" "v_and_b32 v175, 0xffff0000, v147\n\t" "s_waitcnt lgkmcnt(13)\n\t" \
   "v_mfma_f32_16x16x32_bf16 v[112:115], a[24:27], v[56:59], v[112:115]\n\t" "s_waitcnt lgkmcnt(12)\n\t" \
   "v_mfma_f32_16x16x32_bf16 v[116:119], a[28:31], v[60:63], v[116:119]\n\t" "s_waitcnt lgkmcnt(11)\n\t" \
   "v_mfma_f32_16x16x32_bf16 v[112:115], a[32:35], v[64:67], v[112:115]\n\t" "s_waitcnt lgkmcnt(10)\n\t" \
   "v_mfma_f32_16x16x32_bf16 v[116:119], a[36:39], v[68:71], v[116:119]\n\t" "s_waitcnt lgkmcnt(9)\n\t" \
   "v_mfma_f32_16x16x32_bf16 v[112:115], a[40:43], v[72:75], v[112:115]\n\t" "s_waitcnt lgkmcnt(8)\n\t" \
   "v_mfma_f32_16x16x32_bf16 v[116:119], a[44:47], v[76:79], v[116:119]\n\t" "s_waitcnt lgkmcnt(7)\n\t" \
   "v_mfma_f32_16x16x32_bf16 v[112:115], a[48:51], v[80:83], v[112:115]\n\t" "s_waitcnt lgkmcnt(6)\n\t" \
   "v_mfma_f32_16x16x32_bf16 v[116:119], a[52:55], v[84:87], v[116:119]\n\t" "s_waitcnt lgkmcnt(5)\n\t" \
   "v_mfma_f32_16x16x32_bf16 v[112:115], a[56:59], v[88:91], v[112:115]\n\t" "s_waitcnt lgkmcnt(4)\n\t" \
   "v_mfma_f32_16x16x32_bf16 v[116:119], a[60:63], v[92:95], v[116:119]\n\t" "s_waitcnt lgkmcnt(3)\n\t" \
   "v_mfma_f32_16x16x32_bf16 v[112:115], a[64:67], v[96:99], v[112:115]\n\t" "s_waitcnt lgkmcnt(2)\n\t" \
   "v_mfma_f32_16x16x32_bf16 v[116:119], a[68:71], v[100:103], v[116:119]\n\t" "s_waitcnt lgkmcnt(1)\n\t" \
   "v_mfma_f32_16x16x32_bf16 v[112:115], a[72:75], v[104:107], v[112:115]\n\t" "s_waitcnt lgkmcnt(0)\n\t" \
   "v_mfma_f32_16x16x32_bf16 v[116:119], a[76:79], v[108:111], v[116:119]\n\t" "v_mfma_f32_16x16x32_bf16 v[120:123], a[80:83], v[32:35], 0\n\t" \
   "v_mfma_f32_16x16x32_bf16 v[124:127], a[84:87], v[36:39], 0\n\t" "v_mfma_f32_16x16x32_bf16 v[120:123], a[88:91], v[40:43], v[120:123]\n\t" \
   "s_nop 1\n\t" "v_add_f32 v176, v112, v116\n\t" "v_sub_f32 v180, v112, v116\n\t" \
   "v_mfma_f32_16x16x32_bf16 v[124:127], a[92:95], v[44:47], v[124:127]\n\t" "v_add_f32 v177, v113, v117\n\t" "v_sub_f32 v181, v113, v117\n\t" \
   "v_mfma_f32_16x16x32_bf16 v[120:123], a[96:99], v[48:51], v[120:123]\n\t" "v_add_f32 v178, v114, v118\n\t" "v_sub_f32 v182, v114, v118\n\t" \
   "v_mfma_f32_16x16x32_bf16 v[124:127], a[100:103], v[52:55], v[124:127]\n\t" "v_add_f32 v179, v115, v119\n\t" "v_sub_f32 v183, v115, v119\n\t" \
   "v_mfma_f32_16x16x32_bf16 v[120:123], a[104:107], v[56:59], v[120:123]\n\t" "v_cvt_pk_bf16_f32 v184, v176, v177\n\t" \
   "v_cvt_pk_bf16_f32 v185, v178, v179\n\t" "v_mfma_f32_16x16x32_bf16 v[124:127], a[108:111], v[60:63], v[124:127]\n\t" \
   "v_cvt_pk_bf16_f32 v186, v180, v181\n\t" "v_cvt_pk_bf16_f32 v187, v182, v183\n\t" \
   "v_mfma_f32_16x16x32_bf16 v[120:123], a[112:115], v[64:67], v[120:123]\n\t" "v_lshlrev_b32 v176, 16, v184\n\t" \
   "v_and_b32 v177, 0xffff0000, v184\n\t" "v_mfma_f32_16x16x32_bf16 v[124:127], a[116:119], v[68:71], v[124:127]\n\t" \
   "v_lshlrev_b32 v178, 16, v185\n\t" "v_and_b32 v179, 0xffff0000, v185\n\t" \
   "v_mfma_f32_16x16x32_bf16 v[120:123], a[120:123], v[72:75], v[120:123]\n\t" "v_lshlrev_b32 v180, 16, v186\n\t" \
   "v_and_b32 v181, 0xffff0000, v186\n\t" "v_mfma_f32_16x16x32_bf16 v[124:127], a[124:127], v[76:79], v[124:127]\n\t" \
   "v_lshlrev_b32 v182, 16, v187\n\t" "v_and_b32 v183, 0xffff0000, v187\n\t" \
   "v_mfma_f32_16x16x32_bf16 v[120:123], a[128:131], v[80:83], v[120:123]\n\t" "v_add_f32 v176, v176, v152\n\t" "v_add_f32 v177, v177, v153\n\t" \
   "v_mfma_f32_16x16x32_bf16 v[124:127], a[132:135], v[84:87], v[124:127]\n\t" "v_add_f32 v178, v178, v154\n\t" "v_add_f32 v179, v179, v155\n\t" \
   "v_mfma_f32_16x16x32_bf16 v[120:123], a[136:139], v[88:91], v[120:123]\n\t" "v_add_f32 v180, v180, v156\n\t" "v_add_f32 v181, v181, v157\n\t" \
   "v_mfma_f32_16x16x32_bf16 v[124:127], a[140:143], v[92:95], v[124:127]\n\t" "v_add_f32 v182, v182, v158\n\t" "v_add_f32 v183, v183, v159\n\t" \
   "v_mfma_f32_16x16x32_bf16 v[120:123], a[144:147], v[96:99], v[120:123]\n\t" "v_cvt_pk_bf16_f32 v136, v176, v177\n\t" \
   "v_cvt_pk_bf16_f32 v137, v178, v179\n\t" "v_mfma_f32_16x16x32_bf16 v[124:127], a[148:151], v[100:103], v[124:127]\n\t" \
   "v_cvt_pk_bf16_f32 v140, v180, v181\n\t" "v_cvt_pk_bf16_f32 v141, v182, v183\n\t" \
   "v_mfma_f32_16x16x32_bf16 v[120:123], a[152:155], v[104:107], v[120:123]\n\t" \
   "v_mfma_f32_16x16x32_bf16 v[124:127], a[156:159], v[108:111], v[124:127]\n\t" "v_mfma_f32_16x16x32_bf16 v[128:131], a[160:163], v[32:35], 0\n\t" \
   "v_mfma_f32_16x16x32_bf16 v[132:135], a[164:167], v[36:39], 0\n\t" "v_mfma_f32_16x16x32_bf16 v[128:131], a[168:171], v[40:43], v[128:131]\n\t" \
   "s_nop 1\n\t" "v_add_f32 v176, v120, v124\n\t" "v_sub_f32 v180, v120, v124\n\t" \
   "v_mfma_f32_16x16x32_bf16 v[132:135], a[172:175], v[44:47], v[132:135]\n\t" "v_add_f32 v177, v121, v125\n\t" "v_sub_f32 v181, v121, v125\n\t" \
   "v_mfma_f32_16x16x32_bf16 v[128:131], a[176:179], v[48:51], v[128:131]\n\t" "v_add_f32 v178, v122, v126\n\t" "v_sub_f32 v182, v122, v126\n\t" \
   "v_mfma_f32_16x16x32_bf16 v[132:135], a[180:183], v[52:55], v[132:135]\n\t" "v_add_f32 v179, v123, v127\n\t" "v_sub_f32 v183, v123, v127\n\t" \
   "v_mfma_f32_16x16x32_bf16 v[128:131], a[184:187], v[56:59], v[128:131]\n\t" "v_cvt_pk_bf16_f32 v184, v176, v177\n\t" \
   "v_cvt_pk_bf16_f32 v185, v178, v179\n\t" "v_mfma_f32_16x16x32_bf16 v[132:135], a[188:191], v[60:63], v[132:135]\n\t" \
   "v_cvt_pk_bf16_f32 v186, v180, v181\n\t" "v_cvt_pk_bf16_f32 v187, v182, v183\n\t" \
   "v_mfma_f32_16x16x32_bf16 v[128:131], a[192:195], v[64:67], v[128:131]\n\t" "v_lshlrev_b32 v176, 16, v184\n\t" \
   "v_and_b32 v177, 0xffff0000, v184\n\t" "v_mfma_f32_16x16x32_bf16 v[132:135], a[196:199], v[68:71], v[132:135]\n\t" \
   "v_lshlrev_b32 v178, 16, v185\n\t" "v_and_b32 v179, 0xffff0000, v185\n\t" \
   "v_mfma_f32_16x16x32_bf16 v[128:131], a[200:203], v[72:75], v[128:131]\n\t" "v_lshlrev_b32 v180, 16, v186\n\t" \
   "v_and_b32 v181, 0xffff0000, v186\n\t" "v_mfma_f32_16x16x32_bf16 v[132:135], a[204:207], v[76:79], v[132:135]\n\t" \
   "v_lshlrev_b32 v182, 16, v187\n\t" "v_and_b32 v183, 0xffff0000, v187\n\t" \
   "v_mfma_f32_16x16x32_bf16 v[128:131], a[208:211], v[80:83], v[128:131]\n\t" "v_add_f32 v176, v176, v160\n\t" "v_add_f32 v177, v177, v161\n\t" \
   "v_mfma_f32_16x16x32_bf16 v[132:135], a[212:215], v[84:87], v[132:135]\n\t" "v_add_f32 v178, v178, v162\n\t" "v_add_f32 v179, v179, v163\n\t" \
   "v_mfma_f32_16x16x32_bf16 v[128:131], a[216:219], v[88:91], v[128:131]\n\t" "v_add_f32 v180, v180, v164\n\t" "v_add_f32 v181, v181, v165\n\t" \
   "v_mfma_f32_16x16x32_bf16 v[132:135], a[220:223], v[92:95], v[132:135]\n\t" "v_add_f32 v182, v182, v166\n\t" "v_add_f32 v183, v183, v167\n\t" \
   "v_mfma_f32_16x16x32_bf16 v[128:131], a[224:227], v[96:99], v[128:131]\n\t" "v_cvt_pk_bf16_f32 v138, v176, v177\n\t" \
   "v_cvt_pk_bf16_f32 v139, v178, v179\n\t" "v_mfma_f32_16x16x32_bf16 v[132:135], a[228:231], v[100:103], v[132:135]\n\t" \
   "v_cvt_pk_bf16_f32 v142, v180, v181\n\t" "v_cvt_pk_bf16_f32 v143, v182, v183\n\t" \
   "v_mfma_f32_16x16x32_bf16 v[128:131], a[232:235], v[104:107], v[128:131]\n\t" \
   "v_mfma_f32_16x16x32_bf16 v[132:135], a[236:239], v[108:111], v[132:135]\n\t" "s_nop 7\n\t" "v_add_f32 v176, v128, v132\n\t" \
   "v_sub_f32 v180, v128, v132\n\t" "v_add_f32 v177, v129, v133\n\t" "v_sub_f32 v181, v129, v133\n\t" "v_add_f32 v178, v130, v134\n\t" \
   "v_sub_f32 v182, v130, v134\n\t" "v_add_f32 v179, v131, v135\n\t" "v_sub_f32 v183, v131, v135\n\t" "v_cvt_pk_bf16_f32 v184, v176, v177\n\t" \
   "v_cvt_pk_bf16_f32 v185, v178, v179\n\t" "v_cvt_pk_bf16_f32 v186, v180, v181\n\t" "v_cvt_pk_bf16_f32 v187, v182, v183\n\t" \
   "v_lshlrev_b32 v176, 16, v184\n\t" "v_and_b32 v177, 0xffff0000, v184\n\t" "v_lshlrev_b32 v178, 16, v185\n\t" "v_and_b32 v179, 0xffff0000, v185\n\t" \
   "v_lshlrev_b32 v180, 16, v186\n\t" "v_and_b32 v181, 0xffff0000, v186\n\t" "v_lshlrev_b32 v182, 16, v187\n\t" "v_and_b32 v183, 0xffff0000, v187\n\t" \
   "v_add_f32 v176, v176, v168\n\t" "v_add_f32 v177, v177, v169\n\t" "v_add_f32 v178, v178, v170\n\t" "v_add_f32 v179, v179, v171\n\t" \
   "v_add_f32 v180, v180, v172\n\t" "v_add_f32 v181, v181, v173\n\t" "v_add_f32 v182, v182, v174\n\t" "v_add_f32 v183, v183, v175\n\t" \
   "v_cvt_pk_bf16_f32 v144, v176, v177\n\t" "v_cvt_pk_bf16_f32 v145, v178, v179\n\t" "v_cvt_pk_bf16_f32 v146, v180, v181\n\t" \
   "v_cvt_pk_bf16_f32 v147, v182, v183\n\t" "s_nop 1"
#define ID_CLOB "v32", "v33", "v34", "v35", "v36", "v37", "v38", "v39", "v40", "v41", "v42", "v43", "v44", "v45", "v46", "v47", "v48", "v49", "v50", "v51", "v52", "v53", "v54", "v55", "v56", "v57", "v58", "v59", "v60", "v61", "v62", "v63", "v64", "v65", "v66", "v67", "v68", "v69", "v70", "v71", "v72", "v73", "v74", "v75", "v76", "v77", "v78", "v79", "v80", "v81", "v82", "v83", "v84", "v85", "v86", "v87", "v88", "v89", "v90", "v91", "v92", "v93", "v94", "v95", "v96", "v97", "v98", "v99", "v100", "v101", "v102", "v103", "v104", "v105", "v106", "v107", "v108", "v109", "v110", "v111", "v112", "v113", "v114", "v115", "v116", "v117", "v118", "v119", "v120", "v121", "v122", "v123", "v124", "v125", "v126", "v127", "v128", "v129", "v130", "v131", "v132", "v133", "v134", "v135", "v152", "v153", "v154", "v155", "v156", "v157", "v158", "v159", "v160", "v161", "v162", "v163", "v164", "v165", "v166", "v167", "v168", "v169", "v170", "v171", "v172", "v173", "v174", "v175", "v176", "v177", "v178", "v179", "v180", "v181", "v182", "v183", "v184", "v185", "v186", "v187"
#define OU_ASM \
   "s_add_u32 s40, %[so], 0\n\t" "buffer_load_dwordx4 a[0:3], %[o], %[rs], s40 offen offset:0 sc1\n\t" \
   "buffer_load_dwordx4 a[32:35], %[o], %[rs], s40 offen offset:1024 sc1\n\t" \
   "buffer_load_dwordx4 a[64:67], %[o], %[rs], s40 offen offset:2048 sc1\n\t" \
   "buffer_load_dwordx4 a[96:99], %[o], %[rs], s40 offen offset:3072 sc1\n\t" "s_add_u32 s40, %[so], 4096\n\t" \
   "buffer_load_dwordx4 a[128:131], %[o], %[rs], s40 offen offset:0 sc1\n\t" \
   "buffer_load_dwordx4 a[160:163], %[o], %[rs], s40 offen offset:1024 sc1\n\t" \
   "buffer_load_dwordx4 a[192:195], %[o], %[rs], s40 offen offset:2048 sc1\n\t" \
   "buffer_load_dwordx4 a[224:227], %[o], %[rs], s40 offen offset:3072 sc1\n\t" "s_add_u32 s40, %[so], 32768\n\t" \
   "buffer_load_dwordx4 a[4:7], %[o], %[rs], s40 offen offset:0 sc1\n\t" "buffer_load_dwordx4 a[36:39], %[o], %[rs], s40 offen offset:1024 sc1\n\t" \
   "buffer_load_dwordx4 a[68:71], %[o], %[rs], s40 offen offset:2048 sc1\n\t" \
   "buffer_load_dwordx4 a[100:103], %[o], %[rs], s40 offen offset:3072 sc1\n\t" "s_add_u32 s40, %[so], 36864\n\t" \
   "buffer_load_dwordx4 a[132:135], %[o], %[rs], s40 offen offset:0 sc1\n\t" \
   "buffer_load_dwordx4 a[164:167], %[o], %[rs], s40 offen offset:1024 sc1\n\t" \
   "buffer_load_dwordx4 a[196:199], %[o], %[rs], s40 offen offset:2048 sc1\n\t" \
   "buffer_load_dwordx4 a[228:231], %[o], %[rs], s40 offen offset:3072 sc1\n\t" "s_add_u32 s40, %[so], 65536\n\t" \
   "buffer_load_dwordx4 a[8:11], %[o], %[rs], s40 offen offset:0 sc1\n\t" "buffer_load_dwordx4 a[40:43], %[o], %[rs], s40 offen offset:1024 sc1\n\t" \
   "buffer_load_dwordx4 a[72:75], %[o], %[rs], s40 offen offset:2048 sc1\n\t" \
   "buffer_load_dwordx4 a[104:107], %[o], %[rs], s40 offen offset:3072 sc1\n\t" "s_add_u32 s40, %[so], 69632\n\t" \
   "buffer_load_dwordx4 a[136:139], %[o], %[rs], s40 offen offset:0 sc1\n\t" \
   "buffer_load_dwordx4 a[168:171], %[o], %[rs], s40 offen offset:1024 sc1\n\t" \
   "buffer_load_dwordx4 a[200:203], %[o], %[rs], s40 offen offset:2048 sc1\n\t" \
   "buffer_load_dwordx4 a[232:235], %[o], %[rs], s40 offen offset:3072 sc1\n\t" "s_add_u32 s40, %[so], 98304\n\t" \
   "buffer_load_dwordx4 a[12:15], %[o], %[rs], s40 offen offset:0 sc1\n\t" "buffer_load_dwordx4 a[44:47], %[o], %[rs], s40 offen offset:1024 sc1\n\t" \
   "buffer_load_dwordx4 a[76:79], %[o], %[rs], s40 offen offset:2048 sc1\n\t" \
   "buffer_load_dwordx4 a[108:111], %[o], %[rs], s40 offen offset:3072 sc1\n\t" "s_add_u32 s40, %[so], 102400\n\t" \
   "buffer_load_dwordx4 a[140:143], %[o], %[rs], s40 offen offset:0 sc1\n\t" \
   "buffer_load_dwordx4 a[172:175], %[o], %[rs], s40 offen offset:1024 sc1\n\t" \
   "buffer_load_dwordx4 a[204:207], %[o], %[rs], s40 offen offset:2048 sc1\n\t" \
   "buffer_load_dwordx4 a[236:239], %[o], %[rs], s40 offen offset:3072 sc1\n\t" "s_add_u32 s40, %[so], 131072\n\t" \
   "buffer_load_dwordx4 a[16:19], %[o], %[rs], s40 offen offset:0 sc1\n\t" "buffer_load_dwordx4 a[48:51], %[o], %[rs], s40 offen offset:1024 sc1\n\t" \
   "buffer_load_dwordx4 a[80:83], %[o], %[rs], s40 offen offset:2048 sc1\n\t" \
   "buffer_load_dwordx4 a[112:115], %[o], %[rs], s40 offen offset:3072 sc1\n\t" "s_add_u32 s40, %[so], 135168\n\t" \
   "buffer_load_dwordx4 a[144:147], %[o], %[rs], s40 offen offset:0 sc1\n\t" \
   "buffer_load_dwordx4 a[176:179], %[o], %[rs], s40 offen offset:1024 sc1\n\t" \
   "buffer_load_dwordx4 a[208:211], %[o], %[rs], s40 offen offset:2048 sc1\n\t" \
   "buffer_load_dwordx4 a[240:243], %[o], %[rs], s40 offen offset:3072 sc1\n\t" "s_add_u32 s40, %[so], 163840\n\t" \
   "buffer_load_dwordx4 a[20:23], %[o], %[rs], s40 offen offset:0 sc1\n\t" "buffer_load_dwordx4 a[52:55], %[o], %[rs], s40 offen offset:1024 sc1\n\t" \
   "buffer_load_dwordx4 a[84:87], %[o], %[rs], s40 offen offset:2048 sc1\n\t" \
   "buffer_load_dwordx4 a[116:119], %[o], %[rs], s40 offen offset:3072 sc1\n\t" "s_add_u32 s40, %[so], 167936\n\t" \
   "buffer_load_dwordx4 a[148:151], %[o], %[rs], s40 offen offset:0 sc1\n\t" \
   "buffer_load_dwordx4 a[180:183], %[o], %[rs], s40 offen offset:1024 sc1\n\t" \
   "buffer_load_dwordx4 a[212:215], %[o], %[rs], s40 offen offset:2048 sc1\n\t" \
   "buffer_load_dwordx4 a[244:247], %[o], %[rs], s40 offen offset:3072 sc1\n\t" "s_add_u32 s40, %[so], 196608\n\t" \
   "buffer_load_dwordx4 a[24:27], %[o], %[rs], s40 offen offset:0 sc1\n\t" "buffer_load_dwordx4 a[56:59], %[o], %[rs], s40 offen offset:1024 sc1\n\t" \
   "buffer_load_dwordx4 a[88:91], %[o], %[rs], s40 offen offset:2048 sc1\n\t" \
   "buffer_load_dwordx4 a[120:123], %[o], %[rs], s40 offen offset:3072 sc1\n\t" "s_add_u32 s40, %[so], 200704\n\t" \
   "buffer_load_dwordx4 a[152:155], %[o], %[rs], s40 offen offset:0 sc1\n\t" \
   "buffer_load_dwordx4 a[184:187], %[o], %[rs], s40 offen offset:1024 sc1\n\t" \
   "buffer_load_dwordx4 a[216:219], %[o], %[rs], s40 offen offset:2048 sc1\n\t" \
   "buffer_load_dwordx4 a[248:251], %[o], %[rs], s40 offen offset:3072 sc1\n\t" "s_add_u32 s40, %[so], 229376\n\t" \
   "buffer_load_dwordx4 a[28:31], %[o], %[rs], s40 offen offset:0 sc1\n\t" "buffer_load_dwordx4 a[60:63], %[o], %[rs], s40 offen offset:1024 sc1\n\t" \
   "buffer_load_dwordx4 a[92:95], %[o], %[rs], s40 offen offset:2048 sc1\n\t" \
   "buffer_load_dwordx4 a[124:127], %[o], %[rs], s40 offen offset:3072 sc1\n\t" "s_add_u32 s40, %[so], 233472\n\t" \
   "buffer_load_dwordx4 a[156:159], %[o], %[rs], s40 offen offset:0 sc1\n\t" \
   "buffer_load_dwordx4 a[188:191], %[o], %[rs], s40 offen offset:1024 sc1\n\t" \
   "buffer_load_dwordx4 a[220:223], %[o], %[rs], s40 offen offset:2048 sc1\n\t" \
   "buffer_load_dwordx4 a[252:255], %[o], %[rs], s40 offen offset:3072 sc1\n\t" "s_waitcnt vmcnt(0)"
#define OU_CLOB "s40"
#define OY_ASM \
   "s_add_u32 s40, %[sy], 0\n\t" "buffer_load_dwordx4 v[192:195], %[yo], %[rs], s40 offen offset:0 sc1\n\t" "s_add_u32 s40, %[sy], 16384\n\t" \
   "buffer_load_dwordx4 v[196:199], %[yo], %[rs], s40 offen offset:0 sc1\n\t" "s_add_u32 s40, %[sy], 32768\n\t" \
   "buffer_load_dwordx4 v[200:203], %[yo], %[rs], s40 offen offset:0 sc1\n\t" "s_add_u32 s40, %[sy], 49152\n\t" \
   "buffer_load_dwordx4 v[204:207], %[yo], %[rs], s40 offen offset:0 sc1\n\t" "s_add_u32 s40, %[sy], 65536\n\t" \
   "buffer_load_dwordx4 v[208:211], %[yo], %[rs], s40 offen offset:0 sc1\n\t" "s_add_u32 s40, %[sy], 81920\n\t" \
   "buffer_load_dwordx4 v[212:215], %[yo], %[rs], s40 offen offset:0 sc1\n\t" "s_add_u32 s40, %[sy], 98304\n\t" \
   "buffer_load_dwordx4 v[216:219], %[yo], %[rs], s40 offen offset:0 sc1\n\t" "s_add_u32 s40, %[sy], 114688\n\t" \
   "buffer_load_dwordx4 v[220:223], %[yo], %[rs], s40 offen offset:0 sc1\n\t" "s_add_u32 s40, %[sy], 0\n\t" \
   "buffer_load_dwordx4 v[224:227], %[yo], %[rs], s40 offen offset:1024 sc1\n\t" "s_add_u32 s40, %[sy], 16384\n\t" \
   "buffer_load_dwordx4 v[228:231], %[yo], %[rs], s40 offen offset:1024 sc1\n\t" "s_add_u32 s40, %[sy], 32768\n\t" \
   "buffer_load_dwordx4 v[232:235], %[yo], %[rs], s40 offen offset:1024 sc1\n\t" "s_add_u32 s40, %[sy], 49152\n\t" \
   "buffer_load_dwordx4 v[236:239], %[yo], %[rs], s40 offen offset:1024 sc1\n\t" "s_add_u32 s40, %[sy], 65536\n\t" \
   "buffer_load_dwordx4 v[240:243], %[yo], %[rs], s40 offen offset:1024 sc1\n\t" "s_add_u32 s40, %[sy], 81920\n\t" \
   "buffer_load_dwordx4 v[244:247], %[yo], %[rs], s40 offen offset:1024 sc1\n\t" "s_add_u32 s40, %[sy], 98304\n\t" \
   "buffer_load_dwordx4 v[248:251], %[yo], %[rs], s40 offen offset:1024 sc1\n\t" "s_add_u32 s40, %[sy], 114688\n\t" \
   "buffer_load_dwordx4 v[252:255], %[yo], %[rs], s40 offen offset:1024 sc1\n\t" "v_mov_b32 %[m], 0\n\t" "s_waitcnt vmcnt(15)\n\t" \
   "v_max3_u32 %[m], %[m], v192, v194\n\t" "s_waitcnt vmcnt(14)\n\t" "v_max3_u32 %[m], %[m], v196, v198\n\t" "s_waitcnt vmcnt(13)\n\t" \
   "v_max3_u32 %[m], %[m], v200, v202\n\t" "s_waitcnt vmcnt(12)\n\t" "v_max3_u32 %[m], %[m], v204, v206\n\t" "s_waitcnt vmcnt(11)\n\t" \
   "v_max3_u32 %[m], %[m], v208, v210\n\t" "s_waitcnt vmcnt(10)\n\t" "v_max3_u32 %[m], %[m], v212, v214\n\t" "s_waitcnt vmcnt(9)\n\t" \
   "v_max3_u32 %[m], %[m], v216, v218\n\t" "s_waitcnt vmcnt(8)\n\t" "v_max3_u32 %[m], %[m], v220, v222\n\t" "s_waitcnt vmcnt(7)\n\t" \
   "v_max3_u32 %[m], %[m], v224, v226\n\t" "s_waitcnt vmcnt(6)\n\t" "v_max3_u32 %[m], %[m], v228, v230\n\t" "s_waitcnt vmcnt(5)\n\t" \
   "v_max3_u32 %[m], %[m], v232, v234\n\t" "s_waitcnt vmcnt(4)\n\t" "v_max3_u32 %[m], %[m], v236, v238\n\t" "s_waitcnt vmcnt(3)\n\t" \
   "v_max3_u32 %[m], %[m], v240, v242\n\t" "s_waitcnt vmcnt(2)\n\t" "v_max3_u32 %[m], %[m], v244, v246\n\t" "s_waitcnt vmcnt(1)\n\t" \
   "v_max3_u32 %[m], %[m], v248, v250\n\t" "s_waitcnt vmcnt(0)\n\t" "v_max3_u32 %[m], %[m], v252, v254\n\t" "s_nop 0"
#define OY_CLOB "s40"
#define O1_ASM \
   "s_add_u32 s40, %[sp], 0\n\t" "buffer_load_dwordx4 v[112:115], %[po], %[rs], s40 offen offset:0 sc1\n\t" \
   "buffer_load_dwordx4 v[116:119], %[po], %[rs], s40 offen offset:2048 sc1\n\t" "s_add_u32 s40, %[sp], 4096\n\t" \
   "buffer_load_dwordx4 v[120:123], %[po], %[rs], s40 offen offset:0 sc1\n\t" \
   "buffer_load_dwordx4 v[124:127], %[po], %[rs], s40 offen offset:2048 sc1\n\t" "s_add_u32 s40, %[sp], 8192\n\t" \
   "buffer_load_dwordx4 v[128:131], %[po], %[rs], s40 offen offset:0 sc1\n\t" \
   "buffer_load_dwordx4 v[132:135], %[po], %[rs], s40 offen offset:2048 sc1\n\t" "s_add_u32 s40, %[sp], 12288\n\t" \
   "buffer_load_dwordx4 v[136:139], %[po], %[rs], s40 offen offset:0 sc1\n\t" \
   "buffer_load_dwordx4 v[140:143], %[po], %[rs], s40 offen offset:2048 sc1\n\t" \
   "buffer_load_dwordx4 v[144:147], %[mo], %[rs], %[sm] offen offset:0 sc1\n\t" \
   "buffer_load_dwordx4 v[148:151], %[mo], %[rs], %[sm] offen offset:2048 sc1\n\t" "s_waitcnt vmcnt(9)\n\t" \
   "v_mfma_f32_16x16x32_bf16 v[32:35], a[0:3], v[112:115], 0\n\t" "v_mfma_f32_16x16x32_bf16 v[36:39], a[4:7], v[112:115], 0\n\t" \
   "v_mfma_f32_16x16x32_bf16 v[40:43], a[8:11], v[112:115], 0\n\t" "v_mfma_f32_16x16x32_bf16 v[44:47], a[12:15], v[112:115], 0\n\t" \
   "v_mfma_f32_16x16x32_bf16 v[48:51], a[16:19], v[112:115], 0\n\t" "v_mfma_f32_16x16x32_bf16 v[52:55], a[20:23], v[112:115], 0\n\t" \
   "v_mfma_f32_16x16x32_bf16 v[56:59], a[24:27], v[112:115], 0\n\t" "v_mfma_f32_16x16x32_bf16 v[60:63], a[28:31], v[112:115], 0\n\t" \
   "s_waitcnt vmcnt(8)\n\t" "v_mfma_f32_16x16x32_bf16 v[32:35], a[32:35], v[116:119], v[32:35]\n\t" \
   "v_mfma_f32_16x16x32_bf16 v[36:39], a[36:39], v[116:119], v[36:39]\n\t" "v_mfma_f32_16x16x32_bf16 v[40:43], a[40:43], v[116:119], v[40:43]\n\t" \
   "v_mfma_f32_16x16x32_bf16 v[44:47], a[44:47], v[116:119], v[44:47]\n\t" "v_mfma_f32_16x16x32_bf16 v[48:51], a[48:51], v[116:119], v[48:51]\n\t" \
   "v_mfma_f32_16x16x32_bf16 v[52:55], a[52:55], v[116:119], v[52:55]\n\t" "v_mfma_f32_16x16x32_bf16 v[56:59], a[56:59], v[116:119], v[56:59]\n\t" \
   "v_mfma_f32_16x16x32_bf16 v[60:63], a[60:63], v[116:119], v[60:63]\n\t" "s_waitcnt vmcnt(7)\n\t" \
   "v_mfma_f32_16x16x32_bf16 v[32:35], a[64:67], v[120:123], v[32:35]\n\t" "v_mfma_f32_16x16x32_bf16 v[36:39], a[68:71], v[120:123], v[36:39]\n\t" \
   "v_mfma_f32_16x16x32_bf16 v[40:43], a[72:75], v[120:123], v[40:43]\n\t" "v_mfma_f32_16x16x32_bf16 v[44:47], a[76:79], v[120:123], v[44:47]\n\t" \
   "v_mfma_f32_16x16x32_bf16 v[48:51], a[80:83], v[120:123], v[48:51]\n\t" "v_mfma_f32_16x16x32_bf16 v[52:55], a[84:87], v[120:123], v[52:55]\n\t" \
   "v_mfma_f32_16x16x32_bf16 v[56:59], a[88:91], v[120:123], v[56:59]\n\t" "v_mfma_f32_16x16x32_bf16 v[60:63], a[92:95], v[120:123], v[60:63]\n\t" \
   "s_waitcnt vmcnt(6)\n\t" "v_mfma_f32_16x16x32_bf16 v[32:35], a[96:99], v[124:127], v[32:35]\n\t" \
   "v_mfma_f32_16x16x32_bf16 v[36:39], a[100:103], v[124:127], v[36:39]\n\t" "v_mfma_f32_16x16x32_bf16 v[40:43], a[104:107], v[124:127], v[40:43]\n\t" \
   "v_mfma_f32_16x16x32_bf16 v[44:47], a[108:111], v[124:127], v[44:47]\n\t" "v_mfma_f32_16x16x32_bf16 v[48:51], a[112:115], v[124:127], v[48:51]\n\t" \
   "v_mfma_f32_16x16x32_bf16 v[52:55], a[116:119], v[124:127], v[52:55]\n\t" "v_mfma_f32_16x16x32_bf16 v[56:59], a[120:123], v[124:127], v[56:59]\n\t" \
   "v_mfma_f32_16x16x32_bf16 v[60:63], a[124:127], v[124:127], v[60:63]\n\t" "s_waitcnt vmcnt(5)\n\t" \
   "v_mfma_f32_16x16x32_bf16 v[32:35], a[128:131], v[128:131], v[32:35]\n\t" "v_mfma_f32_16x16x32_bf16 v[36:39], a[132:135], v[128:131], v[36:39]\n\t" \
   "v_mfma_f32_16x16x32_bf16 v[40:43], a[136:139], v[128:131], v[40:43]\n\t" "v_mfma_f32_16x16x32_bf16 v[44:47], a[140:143], v[128:131], v[44:47]\n\t" \
   "v_mfma_f32_16x16x32_bf16 v[48:51], a[144:147], v[128:131], v[48:51]\n\t" "v_mfma_f32_16x16x32_bf16 v[52:55], a[148:151], v[128:131], v[52:55]\n\t" \
   "v_mfma_f32_16x16x32_bf16 v[56:59], a[152:155], v[128:131], v[56:59]\n\t" "v_mfma_f32_16x16x32_bf16 v[60:63], a[156:159], v[128:131], v[60:63]\n\t" \
   "s_waitcnt vmcnt(4)\n\t" "v_mfma_f32_16x16x32_bf16 v[32:35], a[160:163], v[132:135], v[32:35]\n\t" \
   "v_mfma_f32_16x16x32_bf16 v[36:39], a[164:167], v[132:135], v[36:39]\n\t" "v_mfma_f32_16x16x32_bf16 v[40:43], a[168:171], v[132:135], v[40:43]\n\t" \
   "v_mfma_f32_16x16x32_bf16 v[44:47], a[172:175], v[132:135], v[44:47]\n\t" "v_mfma_f32_16x16x32_bf16 v[48:51], a[176:179], v[132:135], v[48:51]\n\t" \
   "v_mfma_f32_16x16x32_bf16 v[52:55], a[180:183], v[132:135], v[52:55]\n\t" "v_mfma_f32_16x16x32_bf16 v[56:59], a[184:187], v[132:135], v[56:59]\n\t" \
   "v_mfma_f32_16x16x32_bf16 v[60:63], a[188:191], v[132:135], v[60:63]\n\t" "s_waitcnt vmcnt(3)\n\t" \
   "v_mfma_f32_16x16x32_bf16 v[32:35], a[192:195], v[136:139], v[32:35]\n\t" "v_mfma_f32_16x16x32_bf16 v[36:39], a[196:199], v[136:139], v[36:39]\n\t" \
   "v_mfma_f32_16x16x32_bf16 v[40:43], a[200:203], v[136:139], v[40:43]\n\t" "v_mfma_f32_16x16x32_bf16 v[44:47], a[204:207], v[136:139], v[44:47]\n\t" \
   "v_mfma_f32_16x16x32_bf16 v[48:51], a[208:211], v[136:139], v[48:51]\n\t" "v_mfma_f32_16x16x32_bf16 v[52:55], a[212:215], v[136:139], v[52:55]\n\t" \
   "v_mfma_f32_16x16x32_bf16 v[56:59], a[216:219], v[136:139], v[56:59]\n\t" "v_mfma_f32_16x16x32_bf16 v[60:63], a[220:223], v[136:139], v[60:63]\n\t" \
   "s_waitcnt vmcnt(2)\n\t" "s_waitcnt vmcnt(0)\n\t" "v_mfma_f32_16x16x32_bf16 v[32:35], a[224:227], v[140:143], v[32:35]\n\t" \
   "v_swap_b32 v145, v146\n\t" "v_mfma_f32_16x16x32_bf16 v[36:39], a[228:231], v[140:143], v[36:39]\n\t" "v_swap_b32 v149, v150\n\t" \
   "v_mfma_f32_16x16x32_bf16 v[40:43], a[232:235], v[140:143], v[40:43]\n\t" "v_mfma_f32_16x16x32_bf16 v[44:47], a[236:239], v[140:143], v[44:47]\n\t" \
   "v_mfma_f32_16x16x32_bf16 v[48:51], a[240:243], v[140:143], v[48:51]\n\t" "v_mfma_f32_16x16x32_bf16 v[52:55], a[244:247], v[140:143], v[52:55]\n\t" \
   "v_mfma_f32_16x16x32_bf16 v[56:59], a[248:251], v[140:143], v[56:59]\n\t" "v_mfma_f32_16x16x32_bf16 v[60:63], a[252:255], v[140:143], v[60:63]\n\t" \
   "v_mfma_f32_16x16x32_bf16 v[32:35], v[192:195], v[144:147], v[32:35]\n\t" "v_mfma_f32_16x16x32_bf16 v[36:39], v[196:199], v[144:147], v[36:39]\n\t" \
   "v_mfma_f32_16x16x32_bf16 v[40:43], v[200:203], v[144:147], v[40:43]\n\t" "v_mfma_f32_16x16x32_bf16 v[44:47], v[204:207], v[144:147], v[44:47]\n\t" \
   "v_mfma_f32_16x16x32_bf16 v[48:51], v[208:211], v[144:147], v[48:51]\n\t" "v_mfma_f32_16x16x32_bf16 v[52:55], v[212:215], v[144:147], v[52:55]\n\t" \
   "v_mfma_f32_16x16x32_bf16 v[56:59], v[216:219], v[144:147], v[56:59]\n\t" "v_mfma_f32_16x16x32_bf16 v[60:63], v[220:223], v[144:147], v[60:63]\n\t" \
   "v_mfma_f32_16x16x32_bf16 v[32:35], v[224:227], v[148:151], v[32:35]\n\t" "v_mfma_f32_16x16x32_bf16 v[36:39], v[228:231], v[148:151], v[36:39]\n\t" \
   "v_mfma_f32_16x16x32_bf16 v[40:43], v[232:235], v[148:151], v[40:43]\n\t" "v_mfma_f32_16x16x32_bf16 v[44:47], v[236:239], v[148:151], v[44:47]\n\t" \
   "ds_write_b128 %[lw], v[32:35] offset:0\n\t" "v_mfma_f32_16x16x32_bf16 v[48:51], v[240:243], v[148:151], v[48:51]\n\t" \
   "ds_write_b128 %[lw], v[36:39] offset:1024\n\t" "v_mfma_f32_16x16x32_bf16 v[52:55], v[244:247], v[148:151], v[52:55]\n\t" \
   "ds_write_b128 %[lw], v[40:43] offset:2048\n\t" "v_mfma_f32_16x16x32_bf16 v[56:59], v[248:251], v[148:151], v[56:59]\n\t" \
   "ds_write_b128 %[lw], v[44:47] offset:3072\n\t" "v_mfma_f32_16x16x32_bf16 v[60:63], v[252:255], v[148:151], v[60:63]\n\t" \
   "ds_write_b128 %[lw], v[48:51] offset:4096\n\t" "s_nop 1\n\t" "s_nop 3\n\t" "ds_write_b128 %[lw], v[52:55] offset:5120\n\t" "s_nop 3\n\t" \
   "ds_write_b128 %[lw], v[56:59] offset:6144\n\t" "s_nop 3\n\t" "ds_write_b128 %[lw], v[60:63] offset:7168\n\t" "s_waitcnt lgkmcnt(0)\n\t" \
   "s_barrier\n\t" "ds_read_b128 v[112:115], %[lr] offset:0\n\t" "ds_read_b128 v[116:119], %[lr] offset:256\n\t" \
   "ds_read_b128 v[120:123], %[lr] offset:8192\n\t" "ds_read_b128 v[124:127], %[lr] offset:8448\n\t" "ds_read_b128 v[128:131], %[lr] offset:16384\n\t" \
   "ds_read_b128 v[132:135], %[lr] offset:16640\n\t" "ds_read_b128 v[136:139], %[lr] offset:24576\n\t" \
   "ds_read_b128 v[140:143], %[lr] offset:24832\n\t" "s_waitcnt lgkmcnt(4)\n\t" "v_add_f32 v112, v112, v120\n\t" "v_add_f32 v113, v113, v121\n\t" \
   "v_add_f32 v114, v114, v122\n\t" "v_add_f32 v115, v115, v123\n\t" "v_add_f32 v116, v116, v124\n\t" "v_add_f32 v117, v117, v125\n\t" \
   "v_add_f32 v118, v118, v126\n\t" "v_add_f32 v119, v119, v127\n\t" "s_waitcnt lgkmcnt(0)\n\t" "v_add_f32 v128, v128, v136\n\t" \
   "v_add_f32 v129, v129, v137\n\t" "v_add_f32 v130, v130, v138\n\t" "v_add_f32 v131, v131, v139\n\t" "v_add_f32 v132, v132, v140\n\t" \
   "v_add_f32 v133, v133, v141\n\t" "v_add_f32 v134, v134, v142\n\t" "v_add_f32 v135, v135, v143\n\t" "v_add_f32 v112, v112, v128\n\t" \
   "v_add_f32 v113, v113, v129\n\t" "v_add_f32 v114, v114, v130\n\t" "v_add_f32 v115, v115, v131\n\t" "v_add_f32 v116, v116, v132\n\t" \
   "v_add_f32 v117, v117, v133\n\t" "v_add_f32 v118, v118, v134\n\t" "v_add_f32 v119, v119, v135\n\t" "v_cvt_pk_bf16_f32 v96, v112, v113\n\t" \
   "v_cvt_pk_bf16_f32 v97, v114, v115\n\t" "v_cvt_pk_bf16_f32 v98, v116, v117\n\t" "v_cvt_pk_bf16_f32 v99, v118, v119\n\t" "s_nop 1"
#define O1_CLOB "v32", "v33", "v34", "v35", "v36", "v37", "v38", "v39", "v40", "v41", "v42", "v43", "v44", "v45", "v46", "v47", "v48", "v49", "v50", "v51", "v52", "v53", "v54", "v55", "v56", "v57", "v58", "v59", "v60", "v61", "v62", "v63", "v64", "v65", "v66", "v67", "v68", "v69", "v70", "v71", "v72", "v73", "v74", "v75", "v76", "v77", "v78", "v79", "v80", "v81", "v82", "v83", "v84", "v85", "v86", "v87", "v88", "v89", "v90", "v91", "v92", "v93", "v94", "v95", "v112", "v113", "v114", "v115", "v116", "v117", "v118", "v119", "v120", "v121", "v122", "v123", "v124", "v125", "v126", "v127", "v128", "v129", "v130", "v131", "v132", "v133", "v134", "v135", "v136", "v137", "v138", "v139", "v140", "v141", "v142", "v143", "v144", "v145", "v146", "v147", "v148", "v149", "v150", "v151", "v152", "v153", "v154", "v155", "v156", "v157", "v158", "v159", "v160", "v161", "v162", "v163", "v164", "v165", "v166", "v167", "v168", "v169", "v170", "v171", "v172", "v173", "v174", "v175", "v176", "v177", "v178", "v179", "v180", "v181", "v182", "v183", "v184", "v185", "v186", "v187", "v188", "v189", "v190", "v191", "s40"
#define O2_ASM \
   "s_add_u32 s40, %[sp], 0\n\t" "buffer_load_dwordx4 v[112:115], %[po], %[rs], s40 offen offset:0 sc1\n\t" \
   "buffer_load_dwordx4 v[116:119], %[po], %[rs], s40 offen offset:2048 sc1\n\t" "s_add_u32 s40, %[sp], 4096\n\t" \
   "buffer_load_dwordx4 v[120:123], %[po], %[rs], s40 offen offset:0 sc1\n\t" \
   "buffer_load_dwordx4 v[124:127], %[po], %[rs], s40 offen offset:2048 sc1\n\t" "s_add_u32 s40, %[sp], 8192\n\t" \
   "buffer_load_dwordx4 v[128:131], %[po], %[rs], s40 offen offset:0 sc1\n\t" \
   "buffer_load_dwordx4 v[132:135], %[po], %[rs], s40 offen offset:2048 sc1\n\t" "s_add_u32 s40, %[sp], 12288\n\t" \
   "buffer_load_dwordx4 v[136:139], %[po], %[rs], s40 offen offset:0 sc1\n\t" \
   "buffer_load_dwordx4 v[140:143], %[po], %[rs], s40 offen offset:2048 sc1\n\t" "s_add_u32 s40, %[sp], 0\n\t" \
   "buffer_load_dwordx4 v[152:155], %[po], %[rs], s40 offen offset:1024 sc1\n\t" \
   "buffer_load_dwordx4 v[156:159], %[po], %[rs], s40 offen offset:3072 sc1\n\t" "s_add_u32 s40, %[sp], 4096\n\t" \
   "buffer_load_dwordx4 v[160:163], %[po], %[rs], s40 offen offset:1024 sc1\n\t" \
   "buffer_load_dwordx4 v[164:167], %[po], %[rs], s40 offen offset:3072 sc1\n\t" "s_add_u32 s40, %[sp], 8192\n\t" \
   "buffer_load_dwordx4 v[168:171], %[po], %[rs], s40 offen offset:1024 sc1\n\t" \
   "buffer_load_dwordx4 v[172:175], %[po], %[rs], s40 offen offset:3072 sc1\n\t" "s_add_u32 s40, %[sp], 12288\n\t" \
   "buffer_load_dwordx4 v[176:179], %[po], %[rs], s40 offen offset:1024 sc1\n\t" \
   "buffer_load_dwordx4 v[180:183], %[po], %[rs], s40 offen offset:3072 sc1\n\t" \
   "buffer_load_dwordx4 v[144:147], %[mo], %[rs], %[sm] offen offset:0 sc1\n\t" \
   "buffer_load_dwordx4 v[148:151], %[mo], %[rs], %[sm] offen offset:2048 sc1\n\t" \
   "buffer_load_dwordx4 v[184:187], %[mo], %[rs], %[sm] offen offset:1024 sc1\n\t" \
   "buffer_load_dwordx4 v[188:191], %[mo], %[rs], %[sm] offen offset:3072 sc1\n\t" "s_add_u32 s40, %[sy], 0\n\t" \
   "buffer_load_dwordx4 v[192:195], %[yo], %[rs], s40 offen offset:0 sc1\n\t" "s_add_u32 s40, %[sy], 16384\n\t" \
   "buffer_load_dwordx4 v[196:199], %[yo], %[rs], s40 offen offset:0 sc1\n\t" "s_add_u32 s40, %[sy], 32768\n\t" \
   "buffer_load_dwordx4 v[200:203], %[yo], %[rs], s40 offen offset:0 sc1\n\t" "s_add_u32 s40, %[sy], 49152\n\t" \
   "buffer_load_dwordx4 v[204:207], %[yo], %[rs], s40 offen offset:0 sc1\n\t" "s_add_u32 s40, %[sy], 65536\n\t" \
   "buffer_load_dwordx4 v[208:211], %[yo], %[rs], s40 offen offset:0 sc1\n\t" "s_add_u32 s40, %[sy], 81920\n\t" \
   "buffer_load_dwordx4 v[212:215], %[yo], %[rs], s40 offen offset:0 sc1\n\t" "s_add_u32 s40, %[sy], 98304\n\t" \
   "buffer_load_dwordx4 v[216:219], %[yo], %[rs], s40 offen offset:0 sc1\n\t" "s_add_u32 s40, %[sy], 114688\n\t" \
   "buffer_load_dwordx4 v[220:223], %[yo], %[rs], s40 offen offset:0 sc1\n\t" "s_add_u32 s40, %[sy], 0\n\t" \
   "buffer_load_dwordx4 v[224:227], %[yo], %[rs], s40 offen offset:1024 sc1\n\t" "s_add_u32 s40, %[sy], 16384\n\t" \
   "buffer_load_dwordx4 v[228:231], %[yo], %[rs], s40 offen offset:1024 sc1\n\t" "s_add_u32 s40, %[sy], 32768\n\t" \
   "buffer_load_dwordx4 v[232:235], %[yo], %[rs], s40 offen offset:1024 sc1\n\t" "s_add_u32 s40, %[sy], 49152\n\t" \
   "buffer_load_dwordx4 v[236:239], %[yo], %[rs], s40 offen offset:1024 sc1\n\t" "s_add_u32 s40, %[sy], 65536\n\t" \
   "buffer_load_dwordx4 v[240:243], %[yo], %[rs], s40 offen offset:1024 sc1\n\t" "s_add_u32 s40, %[sy], 81920\n\t" \
   "buffer_load_dwordx4 v[244:247], %[yo], %[rs], s40 offen offset:1024 sc1\n\t" "s_add_u32 s40, %[sy], 98304\n\t" \
   "buffer_load_dwordx4 v[248:251], %[yo], %[rs], s40 offen offset:1024 sc1\n\t" "s_add_u32 s40, %[sy], 114688\n\t" \
   "buffer_load_dwordx4 v[252:255], %[yo], %[rs], s40 offen offset:1024 sc1\n\t" "s_waitcnt vmcnt(35)\n\t" \
   "v_mfma_f32_16x16x32_bf16 v[32:35], a[0:3], v[112:115], 0\n\t" "v_mfma_f32_16x16x32_bf16 v[36:39], a[4:7], v[112:115], 0\n\t" \
   "v_mfma_f32_16x16x32_bf16 v[40:43], a[8:11], v[112:115], 0\n\t" "v_mfma_f32_16x16x32_bf16 v[44:47], a[12:15], v[112:115], 0\n\t" \
   "v_mfma_f32_16x16x32_bf16 v[48:51], a[16:19], v[112:115], 0\n\t" "v_mfma_f32_16x16x32_bf16 v[52:55], a[20:23], v[112:115], 0\n\t" \
   "v_mfma_f32_16x16x32_bf16 v[56:59], a[24:27], v[112:115], 0\n\t" "v_mfma_f32_16x16x32_bf16 v[60:63], a[28:31], v[112:115], 0\n\t" \
   "s_waitcnt vmcnt(34)\n\t" "v_mfma_f32_16x16x32_bf16 v[32:35], a[32:35], v[116:119], v[32:35]\n\t" \
   "v_mfma_f32_16x16x32_bf16 v[36:39], a[36:39], v[116:119], v[36:39]\n\t" "v_mfma_f32_16x16x32_bf16 v[40:43], a[40:43], v[116:119], v[40:43]\n\t" \
   "v_mfma_f32_16x16x32_bf16 v[44:47], a[44:47], v[116:119], v[44:47]\n\t" "v_mfma_f32_16x16x32_bf16 v[48:51], a[48:51], v[116:119], v[48:51]\n\t" \
   "v_mfma_f32_16x16x32_bf16 v[52:55], a[52:55], v[116:119], v[52:55]\n\t" "v_mfma_f32_16x16x32_bf16 v[56:59], a[56:59], v[116:119], v[56:59]\n\t" \
   "v_mfma_f32_16x16x32_bf16 v[60:63], a[60:63], v[116:119], v[60:63]\n\t" "s_waitcnt vmcnt(33)\n\t" \
   "v_mfma_f32_16x16x32_bf16 v[32:35], a[64:67], v[120:123], v[32:35]\n\t" "v_mfma_f32_16x16x32_bf16 v[36:39], a[68:71], v[120:123], v[36:39]\n\t" \
   "v_mfma_f32_16x16x32_bf16 v[40:43], a[72:75], v[120:123], v[40:43]\n\t" "v_mfma_f32_16x16x32_bf16 v[44:47], a[76:79], v[120:123], v[44:47]\n\t" \
   "v_mfma_f32_16x16x32_bf16 v[48:51], a[80:83], v[120:123], v[48:51]\n\t" "v_mfma_f32_16x16x32_bf16 v[52:55], a[84:87], v[120:123], v[52:55]\n\t" \
   "v_mfma_f32_16x16x32_bf16 v[56:59], a[88:91], v[120:123], v[56:59]\n\t" "v_mfma_f32_16x16x32_bf16 v[60:63], a[92:95], v[120:123], v[60:63]\n\t" \
   "s_waitcnt vmcnt(32)\n\t" "v_mfma_f32_16x16x32_bf16 v[32:35], a[96:99], v[124:127], v[32:35]\n\t" \
   "v_mfma_f32_16x16x32_bf16 v[36:39], a[100:103], v[124:127], v[36:39]\n\t" "v_mfma_f32_16x16x32_bf16 v[40:43], a[104:107], v[124:127], v[40:43]\n\t" \
   "v_mfma_f32_16x16x32_bf16 v[44:47], a[108:111], v[124:127], v[44:47]\n\t" "v_mfma_f32_16x16x32_bf16 v[48:51], a[112:115], v[124:127], v[48:51]\n\t" \
   "v_mfma_f32_16x16x32_bf16 v[52:55], a[116:119], v[124:127], v[52:55]\n\t" "v_mfma_f32_16x16x32_bf16 v[56:59], a[120:123], v[124:127], v[56:59]\n\t" \
   "v_mfma_f32_16x16x32_bf16 v[60:63], a[124:127], v[124:127], v[60:63]\n\t" "s_waitcnt vmcnt(31)\n\t" \
   "v_mfma_f32_16x16x32_bf16 v[32:35], a[128:131], v[128:131], v[32:35]\n\t" "v_mfma_f32_16x16x32_bf16 v[36:39], a[132:135], v[128:131], v[36:39]\n\t" \
   "v_mfma_f32_16x16x32_bf16 v[40:43], a[136:139], v[128:131], v[40:43]\n\t" "v_mfma_f32_16x16x32_bf16 v[44:47], a[140:143], v[128:131], v[44:47]\n\t" \
   "v_mfma_f32_16x16x32_bf16 v[48:51], a[144:147], v[128:131], v[48:51]\n\t" "v_mfma_f32_16x16x32_bf16 v[52:55], a[148:151], v[128:131], v[52:55]\n\t" \
   "v_mfma_f32_16x16x32_bf16 v[56:59], a[152:155], v[128:131], v[56:59]\n\t" "v_mfma_f32_16x16x32_bf16 v[60:63], a[156:159], v[128:131], v[60:63]\n\t" \
   "s_waitcnt vmcnt(30)\n\t" "v_mfma_f32_16x16x32_bf16 v[32:35], a[160:163], v[132:135], v[32:35]\n\t" \
   "v_mfma_f32_16x16x32_bf16 v[36:39], a[164:167], v[132:135], v[36:39]\n\t" "v_mfma_f32_16x16x32_bf16 v[40:43], a[168:171], v[132:135], v[40:43]\n\t" \
   "v_mfma_f32_16x16x32_bf16 v[44:47], a[172:175], v[132:135], v[44:47]\n\t" "v_mfma_f32_16x16x32_bf16 v[48:51], a[176:179], v[132:135], v[48:51]\n\t" \
   "v_mfma_f32_16x16x32_bf16 v[52:55], a[180:183], v[132:135], v[52:55]\n\t" "v_mfma_f32_16x16x32_bf16 v[56:59], a[184:187], v[132:135], v[56:59]\n\t" \
   "v_mfma_f32_16x16x32_bf16 v[60:63], a[188:191], v[132:135], v[60:63]\n\t" "s_waitcnt vmcnt(29)\n\t" \
   "v_mfma_f32_16x16x32_bf16 v[32:35], a[192:195], v[136:139], v[32:35]\n\t" "v_mfma_f32_16x16x32_bf16 v[36:39], a[196:199], v[136:139], v[36:39]\n\t" \
   "v_mfma_f32_16x16x32_bf16 v[40:43], a[200:203], v[136:139], v[40:43]\n\t" "v_mfma_f32_16x16x32_bf16 v[44:47], a[204:207], v[136:139], v[44:47]\n\t" \
   "v_mfma_f32_16x16x32_bf16 v[48:51], a[208:211], v[136:139], v[48:51]\n\t" "v_mfma_f32_16x16x32_bf16 v[52:55], a[212:215], v[136:139], v[52:55]\n\t" \
   "v_mfma_f32_16x16x32_bf16 v[56:59], a[216:219], v[136:139], v[56:59]\n\t" "v_mfma_f32_16x16x32_bf16 v[60:63], a[220:223], v[136:139], v[60:63]\n\t" \
   "s_waitcnt vmcnt(28)\n\t" "v_mfma_f32_16x16x32_bf16 v[32:35], a[224:227], v[140:143], v[32:35]\n\t" \
   "v_mfma_f32_16x16x32_bf16 v[36:39], a[228:231], v[140:143], v[36:39]\n\t" "v_mfma_f32_16x16x32_bf16 v[40:43], a[232:235], v[140:143], v[40:43]\n\t" \
   "v_mfma_f32_16x16x32_bf16 v[44:47], a[236:239], v[140:143], v[44:47]\n\t" "v_mfma_f32_16x16x32_bf16 v[48:51], a[240:243], v[140:143], v[48:51]\n\t" \
   "v_mfma_f32_16x16x32_bf16 v[52:55], a[244:247], v[140:143], v[52:55]\n\t" "v_mfma_f32_16x16x32_bf16 v[56:59], a[248:251], v[140:143], v[56:59]\n\t" \
   "v_mfma_f32_16x16x32_bf16 v[60:63], a[252:255], v[140:143], v[60:63]\n\t" "s_waitcnt vmcnt(27)\n\t" \
   "v_mfma_f32_16x16x32_bf16 v[64:67], a[0:3], v[152:155], 0\n\t" "v_mfma_f32_16x16x32_bf16 v[68:71], a[4:7], v[152:155], 0\n\t" \
   "v_mfma_f32_16x16x32_bf16 v[72:75], a[8:11], v[152:155], 0\n\t" "v_mfma_f32_16x16x32_bf16 v[76:79], a[12:15], v[152:155], 0\n\t" \
   "v_mfma_f32_16x16x32_bf16 v[80:83], a[16:19], v[152:155], 0\n\t" "v_mfma_f32_16x16x32_bf16 v[84:87], a[20:23], v[152:155], 0\n\t" \
   "v_mfma_f32_16x16x32_bf16 v[88:91], a[24:27], v[152:155], 0\n\t" "v_mfma_f32_16x16x32_bf16 v[92:95], a[28:31], v[152:155], 0\n\t" \
   "s_waitcnt vmcnt(26)\n\t" "v_mfma_f32_16x16x32_bf16 v[64:67], a[32:35], v[156:159], v[64:67]\n\t" \
   "v_mfma_f32_16x16x32_bf16 v[68:71], a[36:39], v[156:159], v[68:71]\n\t" "v_mfma_f32_16x16x32_bf16 v[72:75], a[40:43], v[156:159], v[72:75]\n\t" \
   "v_mfma_f32_16x16x32_bf16 v[76:79], a[44:47], v[156:159], v[76:79]\n\t" "v_mfma_f32_16x16x32_bf16 v[80:83], a[48:51], v[156:159], v[80:83]\n\t" \
   "v_mfma_f32_16x16x32_bf16 v[84:87], a[52:55], v[156:159], v[84:87]\n\t" "v_mfma_f32_16x16x32_bf16 v[88:91], a[56:59], v[156:159], v[88:91]\n\t" \
   "v_mfma_f32_16x16x32_bf16 v[92:95], a[60:63], v[156:159], v[92:95]\n\t" "s_waitcnt vmcnt(25)\n\t" \
   "v_mfma_f32_16x16x32_bf16 v[64:67], a[64:67], v[160:163], v[64:67]\n\t" "v_mfma_f32_16x16x32_bf16 v[68:71], a[68:71], v[160:163], v[68:71]\n\t" \
   "v_mfma_f32_16x16x32_bf16 v[72:75], a[72:75], v[160:163], v[72:75]\n\t" "v_mfma_f32_16x16x32_bf16 v[76:79], a[76:79], v[160:163], v[76:79]\n\t" \
   "v_mfma_f32_16x16x32_bf16 v[80:83], a[80:83], v[160:163], v[80:83]\n\t" "v_mfma_f32_16x16x32_bf16 v[84:87], a[84:87], v[160:163], v[84:87]\n\t" \
   "v_mfma_f32_16x16x32_bf16 v[88:91], a[88:91], v[160:163], v[88:91]\n\t" "v_mfma_f32_16x16x32_bf16 v[92:95], a[92:95], v[160:163], v[92:95]\n\t" \
   "s_waitcnt vmcnt(24)\n\t" "v_mfma_f32_16x16x32_bf16 v[64:67], a[96:99], v[164:167], v[64:67]\n\t" \
   "v_mfma_f32_16x16x32_bf16 v[68:71], a[100:103], v[164:167], v[68:71]\n\t" "v_mfma_f32_16x16x32_bf16 v[72:75], a[104:107], v[164:167], v[72:75]\n\t" \
   "v_mfma_f32_16x16x32_bf16 v[76:79], a[108:111], v[164:167], v[76:79]\n\t" "v_mfma_f32_16x16x32_bf16 v[80:83], a[112:115], v[164:167], v[80:83]\n\t" \
   "v_mfma_f32_16x16x32_bf16 v[84:87], a[116:119], v[164:167], v[84:87]\n\t" "v_mfma_f32_16x16x32_bf16 v[88:91], a[120:123], v[164:167], v[88:91]\n\t" \
   "v_mfma_f32_16x16x32_bf16 v[92:95], a[124:127], v[164:167], v[92:95]\n\t" "s_waitcnt vmcnt(23)\n\t" \
   "v_mfma_f32_16x16x32_bf16 v[64:67], a[128:131], v[168:171], v[64:67]\n\t" "v_mfma_f32_16x16x32_bf16 v[68:71], a[132:135], v[168:171], v[68:71]\n\t" \
   "v_mfma_f32_16x16x32_bf16 v[72:75], a[136:139], v[168:171], v[72:75]\n\t" "v_mfma_f32_16x16x32_bf16 v[76:79], a[140:143], v[168:171], v[76:79]\n\t" \
   "v_mfma_f32_16x16x32_bf16 v[80:83], a[144:147], v[168:171], v[80:83]\n\t" "v_mfma_f32_16x16x32_bf16 v[84:87], a[148:151], v[168:171], v[84:87]\n\t" \
   "v_mfma_f32_16x16x32_bf16 v[88:91], a[152:155], v[168:171], v[88:91]\n\t" "v_mfma_f32_16x16x32_bf16 v[92:95], a[156:159], v[168:171], v[92:95]\n\t" \
   "s_waitcnt vmcnt(22)\n\t" "v_mfma_f32_16x16x32_bf16 v[64:67], a[160:163], v[172:175], v[64:67]\n\t" \
   "v_mfma_f32_16x16x32_bf16 v[68:71], a[164:167], v[172:175], v[68:71]\n\t" "v_mfma_f32_16x16x32_bf16 v[72:75], a[168:171], v[172:175], v[72:75]\n\t" \
   "v_mfma_f32_16x16x32_bf16 v[76:79], a[172:175], v[172:175], v[76:79]\n\t" "v_mfma_f32_16x16x32_bf16 v[80:83], a[176:179], v[172:175], v[80:83]\n\t" \
   "v_mfma_f32_16x16x32_bf16 v[84:87], a[180:183], v[172:175], v[84:87]\n\t" "v_mfma_f32_16x16x32_bf16 v[88:91], a[184:187], v[172:175], v[88:91]\n\t" \
   "v_mfma_f32_16x16x32_bf16 v[92:95], a[188:191], v[172:175], v[92:95]\n\t" "s_waitcnt vmcnt(21)\n\t" \
   "v_mfma_f32_16x16x32_bf16 v[64:67], a[192:195], v[176:179], v[64:67]\n\t" "v_mfma_f32_16x16x32_bf16 v[68:71], a[196:199], v[176:179], v[68:71]\n\t" \
   "v_mfma_f32_16x16x32_bf16 v[72:75], a[200:203], v[176:179], v[72:75]\n\t" "v_mfma_f32_16x16x32_bf16 v[76:79], a[204:207], v[176:179], v[76:79]\n\t" \
   "v_mfma_f32_16x16x32_bf16 v[80:83], a[208:211], v[176:179], v[80:83]\n\t" "v_mfma_f32_16x16x32_bf16 v[84:87], a[212:215], v[176:179], v[84:87]\n\t" \
   "v_mfma_f32_16x16x32_bf16 v[88:91], a[216:219], v[176:179], v[88:91]\n\t" "v_mfma_f32_16x16x32_bf16 v[92:95], a[220:223], v[176:179], v[92:95]\n\t" \
   "s_waitcnt vmcnt(20)\n\t" "s_waitcnt vmcnt(16)\n\t" "v_mfma_f32_16x16x32_bf16 v[64:67], a[224:227], v[180:183], v[64:67]\n\t" \
   "v_swap_b32 v145, v146\n\t" "v_mfma_f32_16x16x32_bf16 v[68:71], a[228:231], v[180:183], v[68:71]\n\t" "v_swap_b32 v149, v150\n\t" \
   "v_mfma_f32_16x16x32_bf16 v[72:75], a[232:235], v[180:183], v[72:75]\n\t" "v_swap_b32 v185, v186\n\t" \
   "v_mfma_f32_16x16x32_bf16 v[76:79], a[236:239], v[180:183], v[76:79]\n\t" "v_swap_b32 v189, v190\n\t" \
   "v_mfma_f32_16x16x32_bf16 v[80:83], a[240:243], v[180:183], v[80:83]\n\t" "v_mfma_f32_16x16x32_bf16 v[84:87], a[244:247], v[180:183], v[84:87]\n\t" \
   "v_mfma_f32_16x16x32_bf16 v[88:91], a[248:251], v[180:183], v[88:91]\n\t" "v_mfma_f32_16x16x32_bf16 v[92:95], a[252:255], v[180:183], v[92:95]\n\t" \
   "s_waitcnt vmcnt(15)\n\t" "v_mfma_f32_16x16x32_bf16 v[32:35], v[192:195], v[144:147], v[32:35]\n\t" "s_waitcnt vmcnt(14)\n\t" \
   "v_mfma_f32_16x16x32_bf16 v[36:39], v[196:199], v[144:147], v[36:39]\n\t" "s_waitcnt vmcnt(13)\n\t" \
   "v_mfma_f32_16x16x32_bf16 v[40:43], v[200:203], v[144:147], v[40:43]\n\t" "s_waitcnt vmcnt(12)\n\t" \
   "v_mfma_f32_16x16x32_bf16 v[44:47], v[204:207], v[144:147], v[44:47]\n\t" "s_waitcnt vmcnt(11)\n\t" \
   "v_mfma_f32_16x16x32_bf16 v[48:51], v[208:211], v[144:147], v[48:51]\n\t" "s_waitcnt vmcnt(10)\n\t" \
   "v_mfma_f32_16x16x32_bf16 v[52:55], v[212:215], v[144:147], v[52:55]\n\t" "s_waitcnt vmcnt(9)\n\t" \
   "v_mfma_f32_16x16x32_bf16 v[56:59], v[216:219], v[144:147], v[56:59]\n\t" "s_waitcnt vmcnt(8)\n\t" \
   "v_mfma_f32_16x16x32_bf16 v[60:63], v[220:223], v[144:147], v[60:63]\n\t" "s_waitcnt vmcnt(7)\n\t" \
   "v_mfma_f32_16x16x32_bf16 v[32:35], v[224:227], v[148:151], v[32:35]\n\t" "s_waitcnt vmcnt(6)\n\t" \
   "v_mfma_f32_16x16x32_bf16 v[36:39], v[228:231], v[148:151], v[36:39]\n\t" "s_waitcnt vmcnt(5)\n\t" \
   "v_mfma_f32_16x16x32_bf16 v[40:43], v[232:235], v[148:151], v[40:43]\n\t" "s_waitcnt vmcnt(4)\n\t" \
   "v_mfma_f32_16x16x32_bf16 v[44:47], v[236:239], v[148:151], v[44:47]\n\t" "s_waitcnt vmcnt(3)\n\t" \
   "v_mfma_f32_16x16x32_bf16 v[48:51], v[240:243], v[148:151], v[48:51]\n\t" "s_waitcnt vmcnt(2)\n\t" \
   "v_mfma_f32_16x16x32_bf16 v[52:55], v[244:247], v[148:151], v[52:55]\n\t" "s_waitcnt vmcnt(1)\n\t" \
   "v_mfma_f32_16x16x32_bf16 v[56:59], v[248:251], v[148:151], v[56:59]\n\t" "s_waitcnt vmcnt(0)\n\t" \
   "v_mfma_f32_16x16x32_bf16 v[60:63], v[252:255], v[148:151], v[60:63]\n\t" "v_mfma_f32_16x16x32_bf16 v[64:67], v[192:195], v[184:187], v[64:67]\n\t" \
   "v_mfma_f32_16x16x32_bf16 v[68:71], v[196:199], v[184:187], v[68:71]\n\t" "v_mfma_f32_16x16x32_bf16 v[72:75], v[200:203], v[184:187], v[72:75]\n\t" \
   "v_mfma_f32_16x16x32_bf16 v[76:79], v[204:207], v[184:187], v[76:79]\n\t" "v_mfma_f32_16x16x32_bf16 v[80:83], v[208:211], v[184:187], v[80:83]\n\t" \
   "v_mfma_f32_16x16x32_bf16 v[84:87], v[212:215], v[184:187], v[84:87]\n\t" "v_mfma_f32_16x16x32_bf16 v[88:91], v[216:219], v[184:187], v[88:91]\n\t" \
   "v_mfma_f32_16x16x32_bf16 v[92:95], v[220:223], v[184:187], v[92:95]\n\t" "v_mfma_f32_16x16x32_bf16 v[64:67], v[224:227], v[188:191], v[64:67]\n\t" \
   "ds_write_b128 %[lw], v[32:35] offset:0\n\t" "v_mfma_f32_16x16x32_bf16 v[68:71], v[228:231], v[188:191], v[68:71]\n\t" \
   "ds_write_b128 %[lw], v[36:39] offset:1024\n\t" "v_mfma_f32_16x16x32_bf16 v[72:75], v[232:235], v[188:191], v[72:75]\n\t" \
   "ds_write_b128 %[lw], v[40:43] offset:2048\n\t" "v_mfma_f32_16x16x32_bf16 v[76:79], v[236:239], v[188:191], v[76:79]\n\t" \
   "ds_write_b128 %[lw], v[44:47] offset:3072\n\t" "ds_write_b128 %[lw], v[64:67] offset:32768\n\t" \
   "v_mfma_f32_16x16x32_bf16 v[80:83], v[240:243], v[188:191], v[80:83]\n\t" "ds_write_b128 %[lw], v[48:51] offset:4096\n\t" \
   "ds_write_b128 %[lw], v[68:71] offset:33792\n\t" "v_mfma_f32_16x16x32_bf16 v[84:87], v[244:247], v[188:191], v[84:87]\n\t" \
   "ds_write_b128 %[lw], v[52:55] offset:5120\n\t" "ds_write_b128 %[lw], v[72:75] offset:34816\n\t" \
   "v_mfma_f32_16x16x32_bf16 v[88:91], v[248:251], v[188:191], v[88:91]\n\t" "ds_write_b128 %[lw], v[56:59] offset:6144\n\t" \
   "ds_write_b128 %[lw], v[76:79] offset:35840\n\t" "v_mfma_f32_16x16x32_bf16 v[92:95], v[252:255], v[188:191], v[92:95]\n\t" \
   "ds_write_b128 %[lw], v[60:63] offset:7168\n\t" "ds_write_b128 %[lw], v[80:83] offset:36864\n\t" "s_nop 1\n\t" "s_nop 3\n\t" \
   "ds_write_b128 %[lw], v[84:87] offset:37888\n\t" "s_nop 3\n\t" "ds_write_b128 %[lw], v[88:91] offset:38912\n\t" "s_nop 3\n\t" \
   "ds_write_b128 %[lw], v[92:95] offset:39936\n\t" "s_waitcnt lgkmcnt(0)\n\t" "s_barrier\n\t" "ds_read_b128 v[112:115], %[lr] offset:0\n\t" \
   "ds_read_b128 v[116:119], %[lr] offset:256\n\t" "ds_read_b128 v[120:123], %[lr] offset:8192\n\t" "ds_read_b128 v[124:127], %[lr] offset:8448\n\t" \
   "ds_read_b128 v[128:131], %[lr] offset:16384\n\t" "ds_read_b128 v[132:135], %[lr] offset:16640\n\t" \
   "ds_read_b128 v[136:139], %[lr] offset:24576\n\t" "ds_read_b128 v[140:143], %[lr] offset:24832\n\t" \
   "ds_read_b128 v[144:147], %[lr] offset:32768\n\t" "ds_read_b128 v[148:151], %[lr] offset:33024\n\t" \
   "ds_read_b128 v[152:155], %[lr] offset:40960\n\t" "ds_read_b128 v[156:159], %[lr] offset:41216\n\t" \
   "ds_read_b128 v[160:163], %[lr] offset:49152\n\t" "ds_read_b128 v[164:167], %[lr] offset:49408\n\t" \
   "ds_read_b128 v[168:171], %[lr] offset:57344\n\t" "ds_read_b128 v[172:175], %[lr] offset:57600\n\t" "s_waitcnt lgkmcnt(12)\n\t" \
   "v_add_f32 v112, v112, v120\n\t" "v_add_f32 v113, v113, v121\n\t" "v_add_f32 v114, v114, v122\n\t" "v_add_f32 v115, v115, v123\n\t" \
   "v_add_f32 v116, v116, v124\n\t" "v_add_f32 v117, v117, v125\n\t" "v_add_f32 v118, v118, v126\n\t" "v_add_f32 v119, v119, v127\n\t" \
   "s_waitcnt lgkmcnt(8)\n\t" "v_add_f32 v128, v128, v136\n\t" "v_add_f32 v129, v129, v137\n\t" "v_add_f32 v130, v130, v138\n\t" \
   "v_add_f32 v131, v131, v139\n\t" "v_add_f32 v132, v132, v140\n\t" "v_add_f32 v133, v133, v141\n\t" "v_add_f32 v134, v134, v142\n\t" \
   "v_add_f32 v135, v135, v143\n\t" "v_add_f32 v112, v112, v128\n\t" "v_add_f32 v113, v113, v129\n\t" "v_add_f32 v114, v114, v130\n\t" \
   "v_add_f32 v115, v115, v131\n\t" "v_add_f32 v116, v116, v132\n\t" "v_add_f32 v117, v117, v133\n\t" "v_add_f32 v118, v118, v134\n\t" \
   "v_add_f32 v119, v119, v135\n\t" "v_cvt_pk_bf16_f32 v96, v112, v113\n\t" "v_cvt_pk_bf16_f32 v97, v114, v115\n\t" \
   "v_cvt_pk_bf16_f32 v98, v116, v117\n\t" "v_cvt_pk_bf16_f32 v99, v118, v119\n\t" "s_waitcnt lgkmcnt(4)\n\t" "v_add_f32 v144, v144, v152\n\t" \
   "v_add_f32 v145, v145, v153\n\t" "v_add_f32 v146, v146, v154\n\t" "v_add_f32 v147, v147, v155\n\t" "v_add_f32 v148, v148, v156\n\t" \
   "v_add_f32 v149, v149, v157\n\t" "v_add_f32 v150, v150, v158\n\t" "v_add_f32 v151, v151, v159\n\t" "s_waitcnt lgkmcnt(0)\n\t" \
   "v_add_f32 v160, v160, v168\n\t" "v_add_f32 v161, v161, v169\n\t" "v_add_f32 v162, v162, v170\n\t" "v_add_f32 v163, v163, v171\n\t" \
   "v_add_f32 v164, v164, v172\n\t" "v_add_f32 v165, v165, v173\n\t" "v_add_f32 v166, v166, v174\n\t" "v_add_f32 v167, v167, v175\n\t" \
   "v_add_f32 v144, v144, v160\n\t" "v_add_f32 v145, v145, v161\n\t" "v_add_f32 v146, v146, v162\n\t" "v_add_f32 v147, v147, v163\n\t" \
   "v_add_f32 v148, v148, v164\n\t" "v_add_f32 v149, v149, v165\n\t" "v_add_f32 v150, v150, v166\n\t" "v_add_f32 v151, v151, v167\n\t" \
   "v_cvt_pk_bf16_f32 v100, v144, v145\n\t" "v_cvt_pk_bf16_f32 v101, v146, v147\n\t" "v_cvt_pk_bf16_f32 v102, v148, v149\n\t" \
   "v_cvt_pk_bf16_f32 v103, v150, v151\n\t" "s_nop 1"
#define O2_CLOB "v32", "v33", "v34", "v35", "v36", "v37", "v38", "v39", "v40", "v41", "v42", "v43", "v44", "v45", "v46", "v47", "v48", "v49", "v50", "v51", "v52", "v53", "v54", "v55", "v56", "v57", "v58", "v59", "v60", "v61", "v62", "v63", "v64", "v65", "v66", "v67", "v68", "v69", "v70", "v71", "v72", "v73", "v74", "v75", "v76", "v77", "v78", "v79", "v80", "v81", "v82", "v83", "v84", "v85", "v86", "v87", "v88", "v89", "v90", "v91", "v92", "v93", "v94", "v95", "v112", "v113", "v114", "v115", "v116", "v117", "v118", "v119", "v120", "v121", "v122", "v123", "v124", "v125", "v126", "v127", "v128", "v129", "v130", "v131", "v132", "v133", "v134", "v135", "v136", "v137", "v138", "v139", "v140", "v141", "v142", "v143", "v144", "v145", "v146", "v147", "v148", "v149", "v150", "v151", "v152", "v153", "v154", "v155", "v156", "v157", "v158", "v159", "v160", "v161", "v162", "v163", "v164", "v165", "v166", "v167", "v168", "v169", "v170", "v171", "v172", "v173", "v174", "v175", "v176", "v177", "v178", "v179", "v180", "v181", "v182", "v183", "v184", "v185", "v186", "v187", "v188", "v189", "v190", "v191", "s40"
#define TZ 336
#define TZM 368
constexpr int COL_I2 = 64 * 264, COL_LDS = COL_I2 + 8 * 2064;
__device__ __forceinline__ void job_col(int wv, unsigned char* ws, rsrc_t rs, unsigned lds, int jb, int go, int clr, bool all) {
  const int lane = lane_id(), g = lane >> 4, r = lane & 15, j = wv * 64 + lane;
  const int xin = (int)OFF_X1T + (jb == 160 ? 0 : jb) * XS * 8 + j * 16;
  v4u x[4], wf[4];
#pragma unroll
  for (int q = 0; q < 4; ++q) stc(rs, (int)OFF_ZT + jb * ZS * 8 + q * 4096 + j * 16, v4u{0xFFFFFFFFu, 0xFFFFFFFFu, 0xFFFFFFFFu, 0xFFFFFFFFu});
#pragma unroll
  for (int q = 0; q < 4; ++q) {
    const int o = (int)OFF_WT + (jb == 160 ? 320 : 2 * jb + (q >> 1)) * S_ * 4 + ((wv * 2 + (q & 1)) * 64 + lane) * 16;
    wf[q] = ldc(rs, o);
  }
  v32u k0, k1, k2;
  v8u k3, ad = (unsigned)lane;
  {
    const int n = 16 * wv + r;
    cis8(k0, n * (512 * (g & 1) + 256 * (g >> 1)), n * 32, n * 128, (r & 1) ? 0x80008000u : 0u);
    cis8(k1, r * g * 256, r * 64, r * 1024, 0x80000000u);
#pragma unroll
    for (int q = 0; q < 8; ++q) {
      const int i = -n * sigma(g, q), k = (r + 16 * (q >> 2)) * (16 * wv + 4 * g + (q & 3));
      const float sc = 1.0f / ((float)S_ * (float)D_);
      k0[16 + 2 * q] = __builtin_bit_cast(unsigned, cosi(i)); k0[17 + 2 * q] = __builtin_bit_cast(unsigned, sini(i));
      k1[16 + 2 * q] = __builtin_bit_cast(unsigned, cosi(k) * sc); k1[17 + 2 * q] = __builtin_bit_cast(unsigned, sini(k) * sc);
      k3[q] = k1[8 + q] ^ 0x80008000u;
    }
    ad[0] = lds + 264 * (16 * (g & 1) + 8 * (g >> 1) + (r >> 2)) + 32 * (r & 3);
    ad[1] = lds + COL_I2 + (2 * (r >> 3) + (r & 1)) * 2064 + ((r >> 1) & 3) * 512 + (g & 1) * 8;
    ad[2] = ad[1] + 256 + (((4 * (wv >> 1) + 2 * (g >> 1) + (wv & 1)) ^ ((r & 6) + 1)) << 4);
    ad[1] += ((4 * (wv >> 1) + 2 * (g >> 1) + (wv & 1)) ^ (r & 6)) << 4;
    ad[3] = lds + COL_I2 + r * 128 + ((g ^ (r >> 1)) << 4);
    ad[4] = lds + COL_I2 + r * 128 + (((4 + g) ^ (r >> 1)) << 4);
    ad[5] = (int)OFF_ZT + (jb * ZS + 32 * (16 * wv + r) + 2 * g) * 8;
    asm volatile("" : "+{v[32:63]}"(k0), "+{v[64:95]}"(k1), "+{v[128:135]}"(k3), "+{v[16:23]}"(ad));
  }
  const unsigned sh = jb == 160 ? 16u : 0u, mk = jb == 0 ? 0xFFFFu : ~0u, d = lds + (j >> 4) * 264 + r * 16;
#pragma unroll
  for (int q = 0; q < 16; ++q) { k2[2 * q] = wf[q >> 2][q & 3] << 16; k2[2 * q + 1] = wf[q >> 2][q & 3] & 0xFFFF0000u; }
  asm volatile("" : "+{v[96:127]}"(k2));
  fan_signal<FSP>(wv, ws, clr, jb);
  if (all) fan_wait<FSP, NCOL>(wv, ws, clr, jb);
  fan_wait<64, 20>(wv, ws, go, jb);
  PH(27)
#pragma unroll
  for (int q = 0; q < 4; ++q) x[q] = ldc(rs, xin + q * 4096);
#define CSTAMP(i)
  CSTAMP(0)
#pragma unroll
  for (int q = 0; q < 4; ++q) {
    LDS_V2(d + q * 16 * 264) = v2u{(x[q].x >> sh) & mk, x[q].y};
    LDS_V2(d + q * 16 * 264 + 8) = v2u{(x[q].z >> sh) & mk, x[q].w};
  }
  CSTAMP(1)
  PH(28)
  asm volatile(COL_ASM ::"{v[32:63]}"(k0), "{v[64:95]}"(k1), "{v[96:127]}"(k2), "{v[128:135]}"(k3), "{v[16:23]}"(ad), [rs] "s"(rs) : COL_CLOB, "memory");
  CSTAMP(2)
  PH(29)
  CSTAMP(3)
}
constexpr int OOB = 0x7FFFFFF0;
struct XRows { v4u p[2]; v2u q[2]; };
__device__ __forceinline__ int xr_off(int rt, int hf, int wv, int lane, bool third) {
  const int o = (int)OFF_XR + ((rt * KT + hf * 5) * 64 + (lane & 48) + 2 * (lane & 7) + ((lane >> 3) & 1)) * 16;
  return third ? (wv < 2 ? o + 4096 + wv * 8 : OOB) : o + wv * 1024;
}
__device__ __forceinline__ void idft_x(XRows& x, rsrc_t rs, int rt, int hf, int wv, int lane) {
#pragma unroll
  for (int m = 0; m < 2; ++m) {
    x.p[m] = __builtin_amdgcn_raw_buffer_load_b128(rs, xr_off(rt, hf, wv, lane, false), m * 10240, AUX_SC1);
    x.q[m] = __builtin_amdgcn_raw_buffer_load_b64(rs, xr_off(rt, hf, wv, lane, true), m * 10240, AUX_SC1);
  }
}
__device__ __forceinline__ void idft_stage(rsrc_t rs, v4u* lds, int rt, int wv, int lane) {
  const int o = (int)OFF_ZT + ((lane >> 3) * ZS + rt * 16 + 2 * (lane & 7)) * 8;
  const bool more = lane >> 4 == wv;
  int off[9];
  v4u t[9];
#pragma unroll
  for (int i = 0; i < 9; ++i) {
    off[i] = i < 4 ? o + (wv * 32 + i * 8) * ZS * 8 : (more && (i < 8 || lane < 8) ? o + (96 + i * 8) * ZS * 8 : OOB);
    t[i] = ldc(rs, off[i]);
  }
  for (unsigned spins = 0; spins < (1u << 16); ++spins) {
    unsigned m = 0;
#pragma unroll
    for (int i = 0; i < 9; ++i) m = max(m, max(t[i].x, t[i].z));
    if (__builtin_expect(!__any(m == 0xFFFFFFFFu), 1)) break;
    __builtin_amdgcn_s_sleep(2);
#pragma unroll
    for (int i = 0; i < 9; ++i) {
      const bool again = max(t[i].x, t[i].z) == 0xFFFFFFFFu;
      const v4u u = ldc(rs, again ? off[i] : OOB);
      if (again) t[i] = u;
    }
    asm volatile("" ::: "memory");
  }
  if (more && lane < 8) {
    t[0].x = (t[0].x & 0xFFFFu) | (t[8].x << 16);
    t[0].z = (t[0].z & 0xFFFFu) | (t[8].z << 16);
  }
  v4u* dst = lds + (lane >> 3) * 16 + (lane & 7);
#pragma unroll
  for (int c = 0; c < 4; ++c) dst[wv * 128 + (c & 1) * 640 + (c >> 1) * 8] = v4u{t[0][c], t[1][c], t[2][c], t[3][c]};
  if (more) {
#pragma unroll
    for (int c = 0; c < 4; ++c) dst[512 + (c & 1) * 640 + (c >> 1) * 8] = v4u{t[4][c], t[5][c], t[6][c], t[7][c]};
  }
}
#define A_IN(a) "{a[0:31]}"(a[0]), "{a[32:63]}"(a[1]), "{a[64:95]}"(a[2]), "{a[96:127]}"(a[3]), "{a[128:159]}"(a[4]), "{a[160:191]}"(a[5]), "{a[192:223]}"(a[6]), "{a[224:255]}"(a[7])
#define A_OUT(a) "={a[0:31]}"(a[0]), "={a[32:63]}"(a[1]), "={a[64:95]}"(a[2]), "={a[96:127]}"(a[3]), "={a[128:159]}"(a[4]), "={a[160:191]}"(a[5]), \
                 "={a[192:223]}"(a[6]), "={a[224:255]}"(a[7])
__device__ __forceinline__ void idft_mul(XRows& x, const v32u (&a)[8], unsigned la) {
  asm volatile(ID_ASM : "+{v[136:139]}"(x.p[0]), "+{v[140:143]}"(x.p[1]), "+{v[144:145]}"(x.q[0]), "+{v[146:147]}"(x.q[1])
               : A_IN(a), [la] "v"(la) : ID_CLOB, "memory");
}
__device__ __forceinline__ void idft_matrix(v32u (&a)[8], rsrc_t rs, int hf, int wv, int lane) {
  const int o = (int)OFF_INV + hf * 10 * NFK * 1024 + lane * 16;
  asm volatile(IM_ASM : A_OUT(a)
               : [o0] "v"(o + wv * 2 * NFK * 1024), [o1] "v"(wv < 2 ? o + (wv + 6) * NFK * 1024 : OOB), [rs] "s"(rs) : IM_CLOB, "memory");
}
__device__ __forceinline__ void idft_when(unsigned tz) {
  const unsigned t = __builtin_amdgcn_readfirstlane(*(unsigned*)(dyn_lds + L_WHEN)) + tz;
  while ((int)(t - rtc()) > 0 && (int)(t - rtc()) < 1000) __builtin_amdgcn_s_sleep(1);
}
constexpr int NFIRST = 40;
__device__ __forceinline__ void job_idft(int wv_, unsigned char* ws, rsrc_t rs, v4u* lds, int rt0, int hf, unsigned long long* dbg) {
  const int lane = lane_id(), wv = wv_;
  v32u a[8];
  idft_matrix(a, rs, hf, wv, lane);
  fan_wait<FSP, NCOL>(wv_, ws, fan_off(1, 0, 0), rt0 >> 2);
  fan_wait<64, 20>(wv_, ws, fan_off(0, 1, 0), rt0 >> 2);
#pragma unroll 1
  for (int rt = rt0; rt < RT; rt += 32) {
    XRows x;
    idft_x(x, rs, rt, hf, wv, lane);
    if (rt == rt0) { idft_when(TZM); PH(30) }
    idft_stage(rs, lds, rt, wv, lane);
    idft_mul(x, a, lds_base() + lane * 16);
#pragma unroll
    for (int m = 0; m < 2; ++m) {
      __builtin_amdgcn_raw_buffer_store_b128(x.p[m], rs, xr_off(rt, hf, wv, lane, false), m * 10240, AUX_SC1);
      __builtin_amdgcn_raw_buffer_store_b64(x.q[m], rs, xr_off(rt, hf, wv, lane, true), m * 10240, AUX_SC1);
    }
    const f32x2 st = rows_sum(frag_stat(x.p[0]) + frag_stat(x.p[1]) + frag_stat(v4u{x.q[0].x, x.q[0].y, x.q[1].x, x.q[1].y}));
    f32x2* sp = (f32x2*)(lds + 5120) + (lane & 15);
    if (lane < 16) sp[wv * 16] = st;
    asm volatile("s_waitcnt lgkmcnt(0)\n\ts_barrier" ::: "memory");
    if (wv == 0) {
      const f32x2 s0 = sp[0] + sp[16], s1 = sp[32] + sp[48];
      const f32x4 sv = lane < 16 ? f32x4{s0.x, s0.y, s1.x, s1.y} : f32x4{0, 0, 0, 0};
      stc(rs, lane < 16 + hf * 16 ? (int)OFF_ST + (rt * 16 + 2 * (lane & 7) + ((lane >> 3) & 1)) * 48 + (hf + (lane >> 4)) * 16 : OOB, __builtin_bit_cast(v4u, sv));
    }
    if (rt == rt0) { PH(31) }
    if (rt < NFIRST / 2) fan_signal<64>(wv_, ws, fan_off(1, 1, 0), rt * 2 + hf);
    else dep_signal(wv_, ws, cnt_off(1, CNT_IDFT, rt >> 2));
  }
}

constexpr int NFAST = 62;
#define TYL 40
__device__ __forceinline__ void job_out(int wv_, unsigned char* ws, rsrc_t rs, unsigned long long ysel, int layer, int s, int cg, int qt) {
  const int lane = lane_id(), wv = wv_, g = lane >> 4, r = lane & 15, q0 = max(qt, 0);
  const int yo = wv * 2048 + lane * 16;
  const int sy = __builtin_amdgcn_readfirstlane(pool_base(ws) + P_Y + y_region(ysel, s) + cg * 131072);
  const int xo = (int)OFF_XR + (((2 * s + q0) * KT + cg * 4 + wv) * 64 + lane) * 16;
  const v4u x0 = ldc(rs, xo), x1 = ldc(rs, qt < 0 ? xo + KT * 1024 : OOB);
  v32u u[8], y0, y1;
  asm volatile(OU_ASM : A_OUT(u) : [o] "v"(wv * 8192 + lane * 16), [so] "s"((int)OFF_UF + (s * 40 + cg * 8) * 32768), [rs] "s"(rs) : OU_CLOB, "scc", "memory");
  const int sw = min(s, NFAST - 1);
  if (s > 1) prog_prewait(wv_, ws, prog_off(layer, sw - 2, (s + cg * 7) & 31));
  prog_wait(wv_, ws, prog_off(layer, sw, 0));
  v8u acc;
#define OM_OPS A_IN(u), [po] "v"(wv * 16384 + lane * 16), [mo] "v"(wv * 4096 + lane * 16), [sp] "s"((int)OFF_PXF + s * 65536 + q0 * 1024),         \
               [sm] "s"((int)OFF_PXM + s * 16384 + q0 * 1024), [lw] "v"(lds_base() + wv * 8192 + lane * 16), [lr] "v"(lds_base() + wv * 2048 + g * 512 + r * 16), [rs] "s"(rs)
  PHT(1)
  if (qt >= 0) {
    if (s > NFAST) __builtin_amdgcn_s_sleep(TYL);
    for (int i = 0; i < 2; ++i) {
      unsigned m;
      asm volatile(OY_ASM : "={v[192:223]}"(y0), "={v[224:255]}"(y1), [m] "=&v"(m) : [yo] "v"(yo), [sy] "s"(sy), [rs] "s"(rs) : OY_CLOB, "scc", "memory");
      if (i == 0) prog_wait(wv_, ws, prog_off(layer, s, 0));
      if (!__any(m == 0xFFFFFFFFu)) break;
    }
    PHT(0)
    asm volatile(O1_ASM : "=&{v[96:103]}"(acc) : OM_OPS, "{v[192:223]}"(y0), "{v[224:255]}"(y1) : O1_CLOB, "scc", "memory");
    PHV(32 + (s - 61) * 4, pht0)
  } else {
    asm volatile(O2_ASM : "=&{v[96:103]}"(acc), "=&{v[192:223]}"(y0), "=&{v[224:255]}"(y1) : OM_OPS, [yo] "v"(yo), [sy] "s"(sy) : O2_CLOB, "scc", "memory");
  }
  f32x2* sp = (f32x2*)(dyn_lds + 81920);
#pragma unroll
  for (int q = 0; q < 2; ++q) {
    if (q && qt >= 0) break;
    const v4u x = q ? x1 : x0;
    const auto s0 = __builtin_amdgcn_permlane16_swap(acc[4 * q], acc[4 * q + 2], false, false);
    const auto s1 = __builtin_amdgcn_permlane16_swap(acc[4 * q + 1], acc[4 * q + 3], false, false);
    v4u y;
    y.x = pk(lo_f(x.x) + lo_f(s0[0]), hi_f(x.x) + hi_f(s0[0]));
    y.y = pk(lo_f(x.y) + lo_f(s0[1]), hi_f(x.y) + hi_f(s0[1]));
    y.z = pk(lo_f(x.z) + lo_f(s1[0]), hi_f(x.z) + hi_f(s1[0]));
    y.w = pk(lo_f(x.w) + lo_f(s1[1]), hi_f(x.w) + hi_f(s1[1]));
    stc(rs, xo + q * KT * 1024, y);
    const f32x2 st = rows_sum(frag_stat(y));
    if (lane < 16) sp[(wv * 2 + q) * 16 + lane] = st;
  }
  PHT(2)
  asm volatile("s_waitcnt lgkmcnt(0)\n\ts_barrier" ::: "memory");
  if (wv == 0) {
    sp += lane & 31;
    __builtin_amdgcn_raw_buffer_store_b64(__builtin_bit_cast(v2u, (sp[0] + sp[32]) + (sp[64] + sp[96])), rs,
                                          lane < (qt < 0 ? 32 : 16) ? (int)OFF_ST + ((s * 32 + q0 * 16 + lane) * 6 + cg) * 8 : OOB, 0, AUX_SC1);
  }
  dep_signal(wv_, ws, cnt_off(layer, CNT_OUT, s), qt < 0 ? 2u : 1u);
  PHT(3)
  if (s > 60) { PHV(33 + (s - 61) * 4, pht1) PHV(34 + (s - 61) * 4, pht2) PHV(35 + (s - 61) * 4, pht3) }
}
constexpr int JOB_LDS = 4 * NFK * 1024;
constexpr int FAN_LAST = fan_off(0, 1, 0) + 20 * 64;
#define OAUX 0
__device__ __forceinline__ void job_last(int wv_, unsigned char* ws, u16* out, int jb, int rt, int hf) {
  const rsrc_t rs = ws_rsrc(ws), ro = __builtin_amdgcn_make_buffer_rsrc(out, 0, S_ * D_ * 2, 0x00020000);
  const int wv = wv_;
  v32u a[8];
  idft_matrix(a, rs, hf, wv, lane_id());
  PH(26)
  if (jb >= 0) {
    job_col(wv_, ws, rs, lds_base(), jb, FAN_LAST, fan_off(0, 0, 0), true);
    PH(13)
  } else {
    fan_wait<FSP, NCOL>(wv_, ws, fan_off(0, 0, 0), rt);
    fan_wait<64, 20>(wv_, ws, FAN_LAST, rt);
  }
  const int lane = lane_id();
  v4u* lds = (v4u*)dyn_lds + 3072;
  XRows x;
  idft_x(x, rs, rt, hf, wv, lane);
  idft_when(TZ);
  PHT(0)
  idft_stage(rs, lds, rt, wv, lane);
  PHT(1)
  idft_mul(x, a, lds_base() + 49152 + lane * 16);
  const int o = ((rt * 16 + 2 * (lane & 7) + ((lane >> 3) & 1)) * D_ + hf * 160 + (lane >> 5) * 8) * 2;
#pragma unroll
  for (int m = 0; m < 3; ++m) {
    const v4u y = m < 2 ? x.p[m] : v4u{x.q[0].x, x.q[0].y, x.q[1].x, x.q[1].y};
    const auto s0 = __builtin_amdgcn_permlane16_swap(y.x, y.z, false, false), s1 = __builtin_amdgcn_permlane16_swap(y.y, y.w, false, false);
    __builtin_amdgcn_raw_buffer_store_b128(v4u{s0[0], s1[0], s0[1], s1[1]}, ro,
                                           m < 2 ? o + m * 640 + wv * 64 + (lane & 16) * 2 : (wv < 2 ? o + 256 + wv * 32 + (lane & 16) * 40 : OOB), 0, OAUX);
  }
  PH(15)
  PHV(24, pht0) PHV(25, pht1)
}

constexpr unsigned SPIN_CAP = 1u << 16;

#define VM_LD128(dst, off, so, IMM) asm volatile("buffer_load_dwordx4 %0, %1, %2, %3 offen offset:" #IMM " sc1" : "=v"(dst) : "v"(off), "s"(rs), "s"(so) : "memory")
#define VM_LD32(dst, off, so) asm volatile("buffer_load_dword %0, %1, %2, %3 offen sc1" : "=v"(dst) : "v"(off), "s"(rs), "s"(so) : "memory")
#define VM_ST32(val, off, so) asm volatile("buffer_store_dword %0, %1, %2, %3 offen sc1" ::"v"(val), "v"(off), "s"(rs), "s"(so) : "memory")
#define VM_ST128(val, off, so) asm volatile("buffer_store_dwordx4 %0, %1, %2, %3 offen sc1\n\ts_nop 1" ::"v"(val), "v"(off), "s"(rs), "s"(so) : "memory")
#define VM_WAIT(N) asm volatile("s_waitcnt vmcnt(" #N ")" ::: "memory")
#define VM_WAIT1(N, a) asm volatile("s_waitcnt vmcnt(" #N ")" : "+v"(a)::"memory")
__device__ __forceinline__ v4u make_rs(const void* p, unsigned bytes) {
  const unsigned long long a = (unsigned long long)p;
  v4u r;
  r.x = (unsigned)__builtin_amdgcn_readfirstlane((unsigned)a);
  r.y = (unsigned)__builtin_amdgcn_readfirstlane((unsigned)(a >> 32) & 0xFFFFu);
  r.z = bytes;
  r.w = 0x00020000u;
  return r;
}
struct Clk0 { unsigned r, c; };
__device__ __forceinline__ Clk0 clk_sample() {
  unsigned long long r, c;
  asm volatile("s_memrealtime %0\n\ts_memtime %1\n\ts_waitcnt lgkmcnt(0)" : "=s"(r), "=s"(c)::"memory");
  return Clk0{(unsigned)r, (unsigned)c};
}
__device__ __forceinline__ v4u clk_init(Clk0 a) {
  const Clk0 b = clk_sample();
  unsigned k = 1400u;
  if (b.r - a.r >= 100u) k = (unsigned)((float)(b.r - a.r) * 32768.f / (float)(b.c - a.c));
  k = (unsigned)__builtin_amdgcn_readfirstlane((int)min(max(k, 1200u), 3000u));
  asm volatile("" : "+s"(k));
  return v4u{b.r * 8u + 16u, b.c, k, 0u};
}
#define LDSBAR() asm volatile("s_waitcnt lgkmcnt(0)\n\ts_barrier" ::: "memory")
#define MEMPIN() asm volatile("" ::: "memory")

__device__ __forceinline__ int ladder(int x, int i) { return copy_off(((x + (i >> 1)) & 3) | ((i & 1) << 2)); }

struct RecTune { int m1, m2, dl1, rob, c2, vis, dlt, du; };

#define STAT(r)
__device__ __forceinline__ unsigned max4(v4u v) { return max(max(v.x, v.y), max(v.z, v.w)); }
template <int NAP = 6>
__device__ __forceinline__ unsigned flag_finish(const v4u rs, int off, unsigned cv) {
  for (unsigned spins = 0; __any(cv == 0xFFFFFFFFu) && spins < SPIN_CAP; ++spins) {
    __builtin_amdgcn_s_sleep(NAP);
    v4u t;
    VM_LD128(t, off, 0, 0);
    VM_WAIT1(0, t);
    cv = max4(t);
  }
  return cv;
}
struct Pool {
  unsigned long long sel, s3, ysel; int base;
  __device__ __forceinline__ int near(int s) const { return (int)((s3 >> s) & 1ull) * 16384; }
  __device__ __forceinline__ int region(int s) const { return base + s * REG + (int)((sel >> s) & 1ull) * 8192; }
};
constexpr int L_QT = 10240, L_KT = 12288, L_CL = 14336, L_KQ = 16384;
constexpr int L_T = 14336;
constexpr int L_M2 = L_KQ + 65536;
constexpr int REC_ST = L_M2 + 12288;
constexpr int REC_LDS = REC_ST + 4096 + 64;

#define RSTAMP(i)
#define YSTAMP(n)
#define RST_CLOB
#define P1TRACE
#define PVTRACE
#define FINTRACE
#define P1_ASM \
   "s_mov_b32 s64, -1\n\t" "s_mov_b32 s76, 0\n\t" "s_mov_b32 s77, 0\n\t" "s_mov_b32 s71, 63\n\t" "s_mov_b32 s47, 1300\n\t" "s_mov_b32 s49, 0\n\t" \
   "s_mov_b32 s51, 0\n\t" "s_mov_b32 s52, 0\n\t" "s_mov_b32 s70, 0\n\t" "s_mov_b32 s69, 0\n\t" "v_cmp_gt_i32 s[20:21], 0, v126\n\t" \
   "v_cmp_gt_i32 s[22:23], 1, v126\n\t" "v_cmp_gt_i32 s[24:25], 2, v126\n\t" "v_cmp_gt_i32 s[26:27], 3, v126\n\t" "v_cmp_gt_i32 s[28:29], 16, v126\n\t" \
   "v_cmp_gt_i32 s[30:31], 17, v126\n\t" "v_cmp_gt_i32 s[32:33], 18, v126\n\t" "v_cmp_gt_i32 s[34:35], 19, v126\n\t" "s_waitcnt vmcnt(0)\n\t" \
   "s_lshr_b64 s[72:73], %[sel], s71\n\t" "s_lshl_b32 s65, s71, 19\n\t" "s_and_b32 s72, s72, 1\n\t" "s_add_u32 s65, s65, %[pool]\n\t" \
   "s_lshl_b32 s72, s72, 13\n\t" "s_add_u32 s65, s65, s72\n\t" "s_lshr_b64 s[72:73], %[s3], s71\n\t" "s_and_b32 s54, s72, 1\n\t" \
   "s_lshl_b32 s54, s54, 14\n\t" "s_memtime s[62:63]\n\t" "s_memrealtime s[60:61]\n\t" "40:\n\t" "s_cmp_eq_u32 s64, 0\n\t" "s_cbranch_scc1 95f\n\t" \
   "96:\n\t" "s_add_i32 s72, s64, 2\n\t" "s_min_i32 s72, s72, 63\n\t" "s_lshl_b32 s59, s72, 12\n\t" "s_lshl_b32 s79, s72, 13\n\t" \
   "buffer_load_dword v108, v139, %[rs], s65 offen sc1\n\t" "s_cmp_gt_u32 %[wv], 1\n\t" "s_waitcnt vmcnt(4)\n\t" "s_cbranch_scc1 44f\n\t" \
   "buffer_load_dword v15, v140, %[rs], s59 offen sc1\n\t" "ds_write_b128 v127, v[16:19] offset:0\n\t" "ds_write_b128 v127, v[20:23] offset:1024\n\t" \
   "ds_write_b128 v127, v[24:27] offset:2048\n\t" "ds_write_b128 v127, v[28:31] offset:3072\n\t" "s_waitcnt lgkmcnt(0)\n\t" \
   "s_sub_u32 s74, s62, s57\n\t" "s_mul_i32 s74, s74, s58\n\t" "s_lshr_b32 s74, s74, 12\n\t" "s_add_u32 s74, s74, s56\n\t" "s_lshl_b32 s60, s60, 3\n\t" \
   "s_sub_u32 s75, s74, s60\n\t" "s_cmp_lt_u32 s75, 32\n\t" "s_cbranch_scc1 38f\n\t" "s_cmp_lt_i32 s75, 0\n\t" "s_cselect_b32 s61, 0, 31\n\t" \
   "s_add_u32 s74, s60, s61\n\t" "s_sub_i32 s75, s61, s75\n\t" "s_add_i32 s75, s75, 1\n\t" "s_ashr_i32 s75, s75, 1\n\t" "s_min_i32 s75, s75, 8\n\t" \
   "s_max_i32 s75, s75, -8\n\t" "s_mov_b32 s56, s74\n\t" "s_mov_b32 s57, s62\n\t" "s_add_u32 s58, s58, s75\n\t" "38:\n\t" "s_min_u32 s72, s70, 1\n\t" \
   "s_and_b32 s71, s74, -4\n\t" "s_or_b32 s71, s71, s72\n\t" "v_mov_b32 v111, s71\n\t" "buffer_store_dword v111, v131, %[rs], s65 offen sc1\n\t" \
   P1TRACE "s_barrier\n\t" "ds_read_b128 v[160:163], v122 offset:0\n\t" "ds_read_b128 v[164:167], v122 offset:1024\n\t" \
   "ds_read_b128 v[168:171], v122 offset:2048\n\t" "ds_read_b128 v[172:175], v122 offset:3072\n\t" "ds_read_b128 v[176:179], v122 offset:4096\n\t" \
   "ds_read_b128 v[180:183], v122 offset:5120\n\t" "ds_read_b128 v[184:187], v122 offset:6144\n\t" "ds_read_b128 v[188:191], v122 offset:7168\n\t" \
   "s_cmp_lt_i32 s64, 1\n\t" "s_cselect_b32 s72, 0x7ffffff0, 0\n\t" "s_max_i32 s71, s64, 1\n\t" "v_or_b32 v109, s72, v143\n\t" \
   "s_lshl_b32 s71, s71, 12\n\t" "v_mov_b32 v110, 1\n\t" "s_sub_u32 s71, s71, 4096\n\t" "buffer_store_dword v110, v109, %[rs], s71 offen sc1\n\t" \
   "s_add_u32 s48, s69, s47\n\t" "s_cmp_eq_u32 s70, 0\n\t" "s_cselect_b32 s48, s48, -1\n\t" "s_mov_b32 s66, s65\n\t" "s_add_i32 s64, s64, 1\n\t" \
   "s_and_b32 s71, s64, 63\n\t" "s_lshr_b64 s[72:73], %[sel], s71\n\t" "s_lshl_b32 s65, s71, 19\n\t" "s_and_b32 s72, s72, 1\n\t" \
   "s_add_u32 s65, s65, %[pool]\n\t" "s_lshl_b32 s72, s72, 13\n\t" "s_add_u32 s65, s65, s72\n\t" "s_xor_b32 s67, s54, %[lad0]\n\t" \
   "s_xor_b32 s68, s67, 0x4000\n\t" "s_add_u32 s67, s67, s66\n\t" "s_add_u32 s68, s68, s66\n\t" "s_lshr_b64 s[72:73], %[s3], s71\n\t" \
   "s_and_b32 s54, s72, 1\n\t" "s_lshl_b32 s54, s54, 14\n\t" "s_cmp_eq_u32 s64, 0\n\t" "s_cselect_b32 s70, 1, 0\n\t" "s_and_b32 s55, %[wv], 1\n\t" \
   "s_lshl_b32 s55, s55, 14\n\t" "s_xor_b32 s55, s55, s54\n\t" "s_xor_b32 s50, s55, 0x4000\n\t" "s_add_u32 s55, s55, s65\n\t" \
   "s_add_u32 s50, s50, s65\n\t" "v_mov_b32 v48, 0\n\t" "v_mov_b32 v49, 0\n\t" "v_mov_b32 v50, 0\n\t" "v_mov_b32 v51, 0\n\t" "v_mov_b32 v52, 0\n\t" \
   "v_mov_b32 v53, 0\n\t" "v_mov_b32 v54, 0\n\t" "v_mov_b32 v55, 0\n\t" "v_mov_b32 v56, 0\n\t" "v_mov_b32 v57, 0\n\t" "v_mov_b32 v58, 0\n\t" \
   "v_mov_b32 v59, 0\n\t" "v_mov_b32 v60, 0\n\t" "v_mov_b32 v61, 0\n\t" "v_mov_b32 v62, 0\n\t" "v_mov_b32 v63, 0\n\t" "s_waitcnt vmcnt(2)\n\t" \
   "v_cmp_eq_u32 vcc, -1, v15\n\t" "s_nop 0\n\t" "s_cbranch_vccnz 141f\n\t" "41:\n\t" "s_branch 48f\n\t" "44:\n\t" \
   "v_mfma_f32_16x16x32_bf16 v[32:35], v[160:163], v[16:19], 0\n\t" "s_max_i32 s51, s64, 0\n\t" "s_lshl_b32 s52, s51, 16\n\t" \
   "s_lshl_b32 s51, s51, 14\n\t" "v_mfma_f32_16x16x32_bf16 v[36:39], v[176:179], v[16:19], 0\n\t" "s_cmp_lt_i32 s64, 0\n\t" \
   "s_cselect_b32 s49, 0x7ffffff0, 0\n\t" "s_add_i32 s72, s64, 1\n\t" "v_mfma_f32_16x16x32_bf16 v[32:35], v[164:167], v[20:23], v[32:35]\n\t" \
   "s_min_i32 s72, s72, 63\n\t" "s_lshl_b32 s79, s72, 13\n\t" "v_mfma_f32_16x16x32_bf16 v[36:39], v[180:183], v[20:23], v[36:39]\n\t" \
   "v_mfma_f32_16x16x32_bf16 v[32:35], v[168:171], v[24:27], v[32:35]\n\t" "s_waitcnt lgkmcnt(0)\n\t" \
   "v_mfma_f32_16x16x32_bf16 v[36:39], v[184:187], v[24:27], v[36:39]\n\t" "s_sub_u32 s74, s62, s57\n\t" "s_mul_i32 s74, s74, s58\n\t" \
   "s_lshr_b32 s74, s74, 12\n\t" "v_mfma_f32_16x16x32_bf16 v[32:35], v[172:175], v[28:31], v[32:35]\n\t" "s_add_u32 s74, s74, s56\n\t" \
   "s_lshl_b32 s60, s60, 3\n\t" "s_sub_u32 s75, s74, s60\n\t" "v_mfma_f32_16x16x32_bf16 v[36:39], v[188:191], v[28:31], v[36:39]\n\t" \
   "s_cmp_lt_u32 s75, 32\n\t" "s_cbranch_scc1 39f\n\t" "s_cmp_lt_i32 s75, 0\n\t" "s_cselect_b32 s61, 0, 31\n\t" "s_add_u32 s74, s60, s61\n\t" \
   "s_sub_i32 s75, s61, s75\n\t" "s_add_i32 s75, s75, 1\n\t" "s_ashr_i32 s75, s75, 1\n\t" "s_min_i32 s75, s75, 8\n\t" "s_max_i32 s75, s75, -8\n\t" \
   "s_mov_b32 s56, s74\n\t" "s_mov_b32 s57, s62\n\t" "s_add_u32 s58, s58, s75\n\t" "39:\n\t" "s_barrier\n\t" \
   "v_mfma_f32_16x16x32_bf16 v[40:43], v[144:147], v[16:19], 0\n\t" "s_min_u32 s72, s70, 1\n\t" "s_and_b32 s71, s74, -4\n\t" \
   "s_or_b32 s71, s71, s72\n\t" "v_mov_b32 v111, s71\n\t" "v_mfma_f32_16x16x32_bf16 v[40:43], v[148:151], v[20:23], v[40:43]\n\t" \
   "buffer_store_dword v111, v131, %[rs], s65 offen sc1\n\t" P1TRACE "v_mov_b32 v48, 0\n\t" "v_mov_b32 v49, 0\n\t" \
   "v_mfma_f32_16x16x32_bf16 v[40:43], v[152:155], v[24:27], v[40:43]\n\t" "v_mov_b32 v50, 0\n\t" "v_mov_b32 v51, 0\n\t" "v_mov_b32 v52, 0\n\t" \
   "v_mov_b32 v53, 0\n\t" "v_mfma_f32_16x16x32_bf16 v[40:43], v[156:159], v[28:31], v[40:43]\n\t" "v_mov_b32 v54, 0\n\t" "v_mov_b32 v55, 0\n\t" \
   "v_cndmask_b32 v32, v32, v125, s[20:21]\n\t" "v_cndmask_b32 v33, v33, v125, s[22:23]\n\t" "v_cndmask_b32 v34, v34, v125, s[24:25]\n\t" \
   "v_cndmask_b32 v35, v35, v125, s[26:27]\n\t" "v_cndmask_b32 v36, v36, v125, s[28:29]\n\t" "v_cndmask_b32 v37, v37, v125, s[30:31]\n\t" \
   "v_cndmask_b32 v38, v38, v125, s[32:33]\n\t" "v_cndmask_b32 v39, v39, v125, s[34:35]\n\t" "v_exp_f32 v64, v32\n\t" "s_add_u32 s48, s69, s47\n\t" \
   "s_cmp_eq_u32 s70, 0\n\t" "s_cselect_b32 s48, s48, -1\n\t" "v_exp_f32 v65, v33\n\t" "s_mov_b32 s66, s65\n\t" "s_add_i32 s64, s64, 1\n\t" \
   "s_and_b32 s71, s64, 63\n\t" "v_exp_f32 v66, v34\n\t" "s_lshr_b64 s[72:73], %[sel], s71\n\t" "s_lshl_b32 s65, s71, 19\n\t" \
   "s_and_b32 s72, s72, 1\n\t" "v_exp_f32 v67, v35\n\t" "s_add_u32 s65, s65, %[pool]\n\t" "s_lshl_b32 s72, s72, 13\n\t" "s_add_u32 s65, s65, s72\n\t" \
   "v_exp_f32 v68, v36\n\t" "s_xor_b32 s67, s54, %[lad0]\n\t" "s_xor_b32 s68, s67, 0x4000\n\t" "s_add_u32 s67, s67, s66\n\t" "v_exp_f32 v69, v37\n\t" \
   "s_add_u32 s68, s68, s66\n\t" "s_lshr_b64 s[72:73], %[s3], s71\n\t" "s_and_b32 s54, s72, 1\n\t" "v_exp_f32 v70, v38\n\t" \
   "s_lshl_b32 s54, s54, 14\n\t" "s_cmp_eq_u32 s64, 0\n\t" "s_cselect_b32 s70, 1, 0\n\t" "v_exp_f32 v71, v39\n\t" "s_and_b32 s55, %[wv], 1\n\t" \
   "s_lshl_b32 s55, s55, 14\n\t" "s_xor_b32 s55, s55, s54\n\t" "v_exp_f32 v72, v40\n\t" "s_xor_b32 s50, s55, 0x4000\n\t" "s_add_u32 s55, s55, s65\n\t" \
   "s_add_u32 s50, s50, s65\n\t" "v_exp_f32 v73, v41\n\t" "v_mov_b32 v56, 0\n\t" "v_mov_b32 v57, 0\n\t" "v_mov_b32 v58, 0\n\t" "v_exp_f32 v74, v42\n\t" \
   "v_mov_b32 v59, 0\n\t" "v_mov_b32 v60, 0\n\t" "v_mov_b32 v61, 0\n\t" "v_exp_f32 v75, v43\n\t" "v_mov_b32 v62, 0\n\t" "v_mov_b32 v63, 0\n\t" \
   "v_add_f32 v80, v64, v65\n\t" "v_add_f32 v81, v66, v67\n\t" "v_add_f32 v82, v68, v69\n\t" "v_add_f32 v83, v70, v71\n\t" \
   "v_add_f32 v84, v72, v73\n\t" "v_add_f32 v85, v74, v75\n\t" "v_add_f32 v80, v80, v81\n\t" "v_add_f32 v82, v82, v83\n\t" \
   "v_add_f32 v84, v84, v85\n\t" "v_add_f32 v80, v80, v82\n\t" "v_fma_f32 v82, v80, 2.0, v84\n\t" "v_fma_f32 v80, v80, 2.0, v84\n\t" \
   "ds_read_b128 v[160:163], v122 offset:0\n\t" "ds_read_b128 v[164:167], v122 offset:1024\n\t" "v_permlane16_swap_b32 v80, v82\n\t" \
   "v_add_f32 v81, v80, v82\n\t" "v_add_f32 v80, v80, v82\n\t" "ds_read_b128 v[168:171], v122 offset:2048\n\t" \
   "ds_read_b128 v[172:175], v122 offset:3072\n\t" "v_permlane32_swap_b32 v80, v81\n\t" "v_add_f32 v80, v80, v81\n\t" "v_rcp_f32 v90, v80\n\t" \
   "v_add_u32 v81, 0xe0800000, v80\n\t" "v_add_f32 v90, v90, v90\n\t" "v_cmp_lt_u32 vcc, 0x40000000, v81\n\t" \
   "ds_read_b128 v[176:179], v122 offset:4096\n\t" "ds_read_b128 v[180:183], v122 offset:5120\n\t" "ds_read_b128 v[184:187], v122 offset:6144\n\t" \
   "ds_read_b128 v[188:191], v122 offset:7168\n\t" "s_cbranch_vccnz 74f\n\t" "75:\n\t" "v_pk_mul_f32 v[64:65], v[64:65], v[90:91] op_sel_hi:[1,0]\n\t" \
   "v_pk_mul_f32 v[66:67], v[66:67], v[90:91] op_sel_hi:[1,0]\n\t" "v_pk_mul_f32 v[68:69], v[68:69], v[90:91] op_sel_hi:[1,0]\n\t" \
   "v_pk_mul_f32 v[70:71], v[70:71], v[90:91] op_sel_hi:[1,0]\n\t" "v_pk_mul_f32 v[72:73], v[72:73], v[90:91] op_sel_hi:[1,0]\n\t" \
   "v_pk_mul_f32 v[74:75], v[74:75], v[90:91] op_sel_hi:[1,0]\n\t" "v_cvt_pk_bf16_f32 v44, v64, v65\n\t" "v_cvt_pk_bf16_f32 v46, v66, v67\n\t" \
   "v_cvt_pk_bf16_f32 v45, v68, v69\n\t" "v_cvt_pk_bf16_f32 v47, v70, v71\n\t" "v_cvt_pk_bf16_f32 v32, v72, v73\n\t" \
   "v_cvt_pk_bf16_f32 v33, v74, v75\n\t" "s_waitcnt vmcnt(1)\n\t" "48:\n\t" "v_readlane_b32 s69, v108, 0\n\t" "v_readlane_b32 s71, v108, 1\n\t" \
   "s_cmp_eq_u32 s64, 64\n\t" "s_cbranch_scc1 90f\n\t" "s_cmp_eq_u32 s69, -1\n\t" "s_cbranch_scc1 63f\n\t" "s_cmp_eq_u32 s71, -1\n\t" \
   "s_cselect_b32 s47, s47, s71\n\t" "50:\n\t" "s_add_u32 s73, s69, %[m1]\n\t" RSTAMP(5) "51:\n\t" "s_memrealtime s[60:61]\n\t" \
   "s_memtime s[62:63]\n\t" "s_waitcnt lgkmcnt(0)\n\t" "s_sub_u32 s74, s62, s57\n\t" "s_mul_i32 s74, s74, s58\n\t" "s_lshr_b32 s74, s74, 12\n\t" \
   "s_add_u32 s74, s74, s56\n\t" "s_lshl_b32 s60, s60, 3\n\t" "s_sub_u32 s75, s74, s60\n\t" "s_cmp_lt_u32 s75, 32\n\t" "s_cbranch_scc1 52f\n\t" \
   "s_cmp_lt_i32 s75, 0\n\t" "s_cselect_b32 s61, 0, 31\n\t" "s_add_u32 s74, s60, s61\n\t" "s_sub_i32 s75, s61, s75\n\t" "s_add_i32 s75, s75, 1\n\t" \
   "s_ashr_i32 s75, s75, 1\n\t" "s_min_i32 s75, s75, 8\n\t" "s_max_i32 s75, s75, -8\n\t" "s_mov_b32 s56, s74\n\t" "s_mov_b32 s57, s62\n\t" \
   "s_add_u32 s58, s58, s75\n\t" "52:\n\t" "s_sub_i32 s74, s73, s74\n\t" "s_cmp_le_i32 s74, 150\n\t" "s_cbranch_scc1 53f\n\t" \
   "s_cmp_lt_i32 s74, 260\n\t" "s_cbranch_scc1 51b\n\t" "s_sleep 2\n\t" "s_cmp_lt_i32 s74, 32000\n\t" "s_cbranch_scc1 51b\n\t" "s_mov_b32 s74, 0\n\t" \
   "53:\n\t" "s_sub_i32 s74, s74, 56\n\t" "s_max_i32 s74, s74, 0\n\t" "s_mul_i32 s74, s74, 23\n\t" "s_lshr_b32 s74, s74, 5\n\t" \
   "s_and_b32 s74, s74, -4\n\t" "s_getpc_b64 s[60:61]\n\t" "54:\n\t" "s_sub_u32 s74, 55f-54b, s74\n\t" "s_add_u32 s60, s60, s74\n\t" \
   "s_addc_u32 s61, s61, 0\n\t" "s_setpc_b64 s[60:61]\n\t" "s_nop 3\n\t" "s_nop 3\n\t" "s_nop 3\n\t" "s_nop 3\n\t" "s_nop 3\n\t" "s_nop 3\n\t" \
   "s_nop 3\n\t" "s_nop 3\n\t" "s_nop 3\n\t" "s_nop 3\n\t" "s_nop 3\n\t" "s_nop 3\n\t" "s_nop 3\n\t" "s_nop 3\n\t" "s_nop 3\n\t" "s_nop 3\n\t" \
   "s_nop 3\n\t" "s_nop 3\n\t" "s_nop 3\n\t" "s_nop 3\n\t" "s_nop 3\n\t" "s_nop 3\n\t" "s_nop 3\n\t" "s_nop 3\n\t" "55:\n\t" "20:\n\t" RSTAMP(4) \
   "buffer_load_dwordx4 v[64:67], v128, %[rs], s67 offen sc1\n\t" "buffer_load_dwordx4 v[68:71], v129, %[rs], s67 offen sc1\n\t" \
   "buffer_load_dwordx4 v[72:75], v130, %[rs], s67 offen sc1\n\t" "s_nop 7\n\t" "s_nop 7\n\t" "s_nop 7\n\t" "s_cmp_lg_u32 s70, 0\n\t" \
   "s_cbranch_scc1 27f\n\t" "buffer_load_dwordx4 v[16:19], v138, %[rs], s79 offen offset:0 sc1\n\t" \
   "buffer_load_dwordx4 v[20:23], v138, %[rs], s79 offen offset:1024 sc1\n\t" \
   "buffer_load_dwordx4 v[24:27], v138, %[rs], s79 offen offset:2048 sc1\n\t" \
   "buffer_load_dwordx4 v[28:31], v138, %[rs], s79 offen offset:3072 sc1\n\t" "v_or_b32 v108, s49, v141\n\t" "v_or_b32 v109, s49, v142\n\t" \
   "buffer_store_dwordx4 v[44:47], v108, %[rs], s52 offen sc1\n\t" "buffer_store_dwordx2 v[32:33], v109, %[rs], s51 offen sc1\n\t" "28:\n\t" RSTAMP(3) \
   "s_waitcnt vmcnt(8)\n\t" "ds_write_b128 v112, v[64:67]\n\t" "v_smfmac_f32_16x16x64_bf16 v[48:51], v[64:67], a[0:7], v134\n\t" \
   "s_waitcnt vmcnt(7)\n\t" "ds_write_b128 v113, v[68:71]\n\t" "s_waitcnt vmcnt(6)\n\t" "ds_write_b128 v114, v[72:75]\n\t" RSTAMP(7) "2:\n\t" \
   "v_smfmac_f32_16x16x64_bf16 v[52:55], v[64:67], a[64:71], v134\n\t" "v_max3_u32 v110, v64, v66, v68\n\t" "v_max3_u32 v111, v70, v72, v74\n\t" \
   "v_smfmac_f32_16x16x64_bf16 v[48:51], v[68:71], a[8:15], v134\n\t" "v_max_u32 v110, v110, v111\n\t" "v_cmp_eq_u32 vcc, -1, v110\n\t" \
   "v_smfmac_f32_16x16x64_bf16 v[52:55], v[68:71], a[72:79], v134\n\t" "s_cbranch_vccnz 8f\n\t" \
   "v_smfmac_f32_16x16x64_bf16 v[48:51], v[72:75], a[16:23], v134\n\t" "s_waitcnt lgkmcnt(0)\n\t" "s_barrier\n\t" RSTAMP(0) \
   "ds_read_b128 v[76:79], v115\n\t" "ds_read_b128 v[80:83], v116\n\t" "v_smfmac_f32_16x16x64_bf16 v[52:55], v[72:75], a[80:87], v134\n\t" \
   "ds_read_b128 v[84:87], v117\n\t" "ds_read_b128 v[88:91], v118\n\t" "v_smfmac_f32_16x16x64_bf16 v[56:59], v[64:67], a[128:135], v134\n\t" \
   "ds_read_b128 v[92:95], v119\n\t" "ds_read_b128 v[96:99], v120\n\t" "v_smfmac_f32_16x16x64_bf16 v[60:63], v[64:67], a[192:199], v134\n\t" \
   "ds_read_b128 v[100:103], v121\n\t" "v_smfmac_f32_16x16x64_bf16 v[56:59], v[68:71], a[136:143], v134\n\t" \
   "v_smfmac_f32_16x16x64_bf16 v[60:63], v[68:71], a[200:207], v134\n\t" "s_waitcnt lgkmcnt(6)\n\t" \
   "v_smfmac_f32_16x16x64_bf16 v[48:51], v[76:79], a[24:31], v134\n\t" "v_smfmac_f32_16x16x64_bf16 v[52:55], v[76:79], a[88:95], v134\n\t" \
   "s_waitcnt lgkmcnt(5)\n\t" "v_smfmac_f32_16x16x64_bf16 v[48:51], v[80:83], a[32:39], v134\n\t" \
   "v_smfmac_f32_16x16x64_bf16 v[52:55], v[80:83], a[96:103], v134\n\t" "s_waitcnt lgkmcnt(4)\n\t" \
   "v_smfmac_f32_16x16x64_bf16 v[48:51], v[84:87], a[40:47], v134\n\t" "v_smfmac_f32_16x16x64_bf16 v[52:55], v[84:87], a[104:111], v134\n\t" \
   "s_waitcnt lgkmcnt(3)\n\t" "v_smfmac_f32_16x16x64_bf16 v[48:51], v[88:91], a[48:55], v134\n\t" \
   "v_smfmac_f32_16x16x64_bf16 v[52:55], v[88:91], a[112:119], v134\n\t" "s_waitcnt lgkmcnt(2)\n\t" \
   "v_smfmac_f32_16x16x64_bf16 v[48:51], v[92:95], a[56:63], v134\n\t" "v_smfmac_f32_16x16x64_bf16 v[52:55], v[92:95], a[120:127], v134\n\t" \
   "s_waitcnt lgkmcnt(0)\n\t" "v_smfmac_f32_16x16x64_bf16 v[48:51], v[96:99], v[192:199], v134\n\t" \
   "v_smfmac_f32_16x16x64_bf16 v[48:51], v[100:103], v[200:207], v134\n\t" "v_smfmac_f32_16x16x64_bf16 v[52:55], v[96:99], v[208:215], v134\n\t" \
   "v_smfmac_f32_16x16x64_bf16 v[52:55], v[100:103], v[216:223], v134\n\t" "v_smfmac_f32_16x16x64_bf16 v[56:59], v[72:75], a[144:151], v134\n\t" \
   "v_add_f32 v48, v48, v49\n\t" "v_add_f32 v49, v50, v51\n\t" "v_smfmac_f32_16x16x64_bf16 v[60:63], v[72:75], a[208:215], v134\n\t" \
   "v_mul_f32 v48, 0x3e0293ee, v48\n\t" "v_mul_f32 v49, 0x3e0293ee, v49\n\t" "v_smfmac_f32_16x16x64_bf16 v[56:59], v[76:79], a[152:159], v134\n\t" \
   "v_add_f32 v52, v52, v53\n\t" "v_add_f32 v53, v54, v55\n\t" "v_smfmac_f32_16x16x64_bf16 v[56:59], v[80:83], a[160:167], v134\n\t" \
   "v_mul_f32 v52, 0x3e0293ee, v52\n\t" "v_mul_f32 v53, 0x3e0293ee, v53\n\t" "v_smfmac_f32_16x16x64_bf16 v[56:59], v[84:87], a[168:175], v134\n\t" \
   "v_cvt_pk_bf16_f32 v48, v48, v49\n\t" "ds_write_b32 v135, v48 offset:10240\n\t" \
   "v_smfmac_f32_16x16x64_bf16 v[56:59], v[88:91], a[176:183], v134\n\t" "v_cvt_pk_bf16_f32 v52, v52, v53\n\t" \
   "ds_write_b32 v135, v52 offset:10496\n\t" "v_smfmac_f32_16x16x64_bf16 v[56:59], v[92:95], a[184:191], v134\n\t" \
   "v_smfmac_f32_16x16x64_bf16 v[56:59], v[96:99], v[224:231], v134\n\t" "v_smfmac_f32_16x16x64_bf16 v[56:59], v[100:103], v[232:239], v134\n\t" \
   "s_waitcnt lgkmcnt(0)\n\t" "s_barrier\n\t" "ds_read_b64_tr_b16 v[64:65], v136 offset:10240\n\t" "ds_read_b64_tr_b16 v[66:67], v136 offset:10496\n\t" \
   "v_smfmac_f32_16x16x64_bf16 v[60:63], v[76:79], a[216:223], v134\n\t" "ds_read_b64_tr_b16 v[68:69], v136 offset:10752\n\t" \
   "v_smfmac_f32_16x16x64_bf16 v[60:63], v[80:83], a[224:231], v134\n\t" "ds_read_b64_tr_b16 v[70:71], v136 offset:11008\n\t" \
   "v_smfmac_f32_16x16x64_bf16 v[60:63], v[84:87], a[232:239], v134\n\t" "ds_read_b64_tr_b16 v[72:73], v136 offset:11264\n\t" \
   "v_smfmac_f32_16x16x64_bf16 v[60:63], v[88:91], a[240:247], v134\n\t" "ds_read_b64_tr_b16 v[74:75], v136 offset:11520\n\t" \
   "v_add_f32 v56, v56, v57\n\t" "v_add_f32 v57, v58, v59\n\t" "v_smfmac_f32_16x16x64_bf16 v[60:63], v[92:95], a[248:255], v134\n\t" \
   "ds_read_b64_tr_b16 v[104:105], v136 offset:11776\n\t" "ds_read_b64_tr_b16 v[106:107], v136 offset:12032\n\t" \
   "v_smfmac_f32_16x16x64_bf16 v[60:63], v[96:99], v[240:247], v134\n\t" "v_cvt_pk_bf16_f32 v56, v56, v57\n\t" \
   "ds_write_b32 v135, v56 offset:12288\n\t" "v_smfmac_f32_16x16x64_bf16 v[60:63], v[100:103], v[248:255], v134\n\t" RSTAMP(6) \
   "s_waitcnt lgkmcnt(7)\n\t" "v_mfma_f32_16x16x32_bf16 v[48:51], v[160:163], v[64:67], 0\n\t" \
   "v_mfma_f32_16x16x32_bf16 v[52:55], v[176:179], v[64:67], 0\n\t" "s_waitcnt lgkmcnt(3)\n\t" "s_nop 0\n\t" "v_add_f32 v60, v60, v61\n\t" \
   "v_add_f32 v61, v62, v63\n\t" "v_cvt_pk_bf16_f32 v60, v60, v61\n\t" "ds_write_b32 v135, v60 offset:12544\n\t" \
   "v_mfma_f32_16x16x32_bf16 v[48:51], v[164:167], v[68:71], v[48:51]\n\t" "v_mfma_f32_16x16x32_bf16 v[52:55], v[180:183], v[68:71], v[52:55]\n\t" \
   "v_mfma_f32_16x16x32_bf16 v[48:51], v[168:171], v[72:75], v[48:51]\n\t" "v_mfma_f32_16x16x32_bf16 v[52:55], v[184:187], v[72:75], v[52:55]\n\t" \
   "s_waitcnt lgkmcnt(2)\n\t" "v_mfma_f32_16x16x32_bf16 v[48:51], v[172:175], v[104:107], v[48:51]\n\t" \
   "v_mfma_f32_16x16x32_bf16 v[52:55], v[188:191], v[104:107], v[52:55]\n\t" "s_waitcnt lgkmcnt(0)\n\t" "s_barrier\n\t" \
   "ds_read_b64_tr_b16 v[144:145], v136 offset:12288\n\t" "ds_read_b64_tr_b16 v[146:147], v136 offset:12544\n\t" \
   "ds_read_b64_tr_b16 v[148:149], v136 offset:12800\n\t" "ds_read_b64_tr_b16 v[150:151], v136 offset:13056\n\t" \
   "ds_read_b64_tr_b16 v[152:153], v136 offset:13312\n\t" "ds_read_b64_tr_b16 v[154:155], v136 offset:13568\n\t" \
   "ds_read_b64_tr_b16 v[156:157], v136 offset:13824\n\t" "ds_read_b64_tr_b16 v[158:159], v136 offset:14080\n\t" "s_cmp_eq_u32 s64, 0\n\t" \
   "s_cbranch_scc1 92f\n\t" "93:\n\t" RSTAMP(1) "v_cndmask_b32 v48, v48, v52, %[hi8]\n\t" "v_cndmask_b32 v49, v49, v53, %[hi8]\n\t" \
   "v_cndmask_b32 v50, v50, v54, %[hi8]\n\t" "v_cndmask_b32 v51, v51, v55, %[hi8]\n\t" "v_exp_f32 v80, v48\n\t" "v_exp_f32 v81, v49\n\t" \
   "s_waitcnt lgkmcnt(6)\n\t" "v_mfma_f32_16x16x32_bf16 v[76:79], v[144:147], v[64:67], 0\n\t" "v_exp_f32 v82, v50\n\t" "v_exp_f32 v83, v51\n\t" \
   "s_waitcnt lgkmcnt(4)\n\t" "v_mfma_f32_16x16x32_bf16 v[76:79], v[148:151], v[68:71], v[76:79]\n\t" "v_add_f32 v96, v80, v81\n\t" \
   "v_add_f32 v103, v82, v83\n\t" "s_waitcnt lgkmcnt(2)\n\t" "v_mfma_f32_16x16x32_bf16 v[76:79], v[152:155], v[72:75], v[76:79]\n\t" \
   "v_add_f32 v94, v96, v103\n\t" "v_add_f32 v96, v96, v103\n\t" "s_waitcnt lgkmcnt(0)\n\t" \
   "v_mfma_f32_16x16x32_bf16 v[76:79], v[156:159], v[104:107], v[76:79]\n\t" "v_permlane32_swap_b32 v96, v94\n\t" "v_add_f32 v96, v96, v94\n\t" \
   "s_nop 1\n\t" "v_add_f32_dpp v96, v96, v96 row_ror:8 row_mask:0xf bank_mask:0xf\n\t" "s_nop 2\n\t" "v_exp_f32 v88, v76\n\t" "v_exp_f32 v89, v77\n\t" \
   "v_add_f32 v103, v96, v88\n\t" "v_exp_f32 v90, v78\n\t" "v_add_f32 v103, v103, v89\n\t" "v_exp_f32 v91, v79\n\t" "v_add_f32 v103, v103, v90\n\t" \
   "v_add_f32 v97, v103, v91\n\t" "v_add_f32 v103, v103, v91\n\t" "s_nop 1\n\t" "v_permlane16_swap_b32 v103, v97\n\t" "v_add_f32 v103, v103, v97\n\t" \
   "v_rcp_f32 v100, v103\n\t" "v_add_u32 v94, 0xe0800000, v103\n\t" "v_pk_mul_f32 v[80:81], v[80:81], v[100:101] op_sel_hi:[1,0]\n\t" \
   "v_cmp_lt_u32 vcc, 0x40000000, v94\n\t" "v_pk_mul_f32 v[82:83], v[82:83], v[100:101] op_sel_hi:[1,0]\n\t" "s_cbranch_vccnz 70f\n\t" \
   "v_cvt_pk_bf16_f32 v48, v80, v81\n\t" "v_cvt_pk_bf16_f32 v49, v82, v83\n\t" "buffer_store_dwordx2 v[48:49], v132, %[rs], s55 offen sc1\n\t" \
   "v_pk_mul_f32 v[88:89], v[88:89], v[100:101] op_sel_hi:[1,0]\n\t" "v_pk_mul_f32 v[90:91], v[90:91], v[100:101] op_sel_hi:[1,0]\n\t" \
   "v_cvt_pk_bf16_f32 v52, v88, v89\n\t" "v_cvt_pk_bf16_f32 v53, v90, v91\n\t" "71:\n\t" \
   "buffer_store_dwordx2 v[52:53], v133, %[rs], s65 offen sc1\n\t" "s_memtime s[62:63]\n\t" "s_memrealtime s[60:61]\n\t" \
   "buffer_store_dwordx2 v[48:49], v132, %[rs], s50 offen sc1\n\t" RSTAMP(2) "s_branch 40b\n\t" "70:\n\t" "v_max3_f32 v94, v48, v49, v50\n\t" \
   "v_max3_f32 v102, v76, v77, v78\n\t" "v_max3_f32 v94, v94, v51, v79\n\t" "v_max_f32 v92, v94, v102\n\t" "s_nop 1\n\t" \
   "v_max_f32_dpp v102, v92, v92 row_ror:8 row_mask:0xf bank_mask:0xf\n\t" "v_max_f32_dpp v94, v92, v92 row_ror:8 row_mask:0xf bank_mask:0xf\n\t" \
   "s_nop 1\n\t" "v_permlane32_swap_b32 v102, v94\n\t" "v_max_f32 v95, v102, v94\n\t" "v_max_f32 v102, v102, v94\n\t" "s_nop 1\n\t" \
   "v_permlane16_swap_b32 v102, v95\n\t" "v_max_f32 v102, v102, v95\n\t" "v_sub_f32 v80, v48, v102\n\t" "v_sub_f32 v88, v76, v102\n\t" \
   "v_sub_f32 v81, v49, v102\n\t" "v_sub_f32 v89, v77, v102\n\t" "v_sub_f32 v82, v50, v102\n\t" "v_sub_f32 v90, v78, v102\n\t" \
   "v_sub_f32 v83, v51, v102\n\t" "v_sub_f32 v91, v79, v102\n\t" "v_exp_f32 v80, v80\n\t" "v_exp_f32 v88, v88\n\t" "v_exp_f32 v81, v81\n\t" \
   "v_exp_f32 v89, v89\n\t" "v_exp_f32 v82, v82\n\t" "v_exp_f32 v90, v90\n\t" "v_exp_f32 v83, v83\n\t" "v_exp_f32 v91, v91\n\t" "s_nop 0\n\t" \
   "v_add_f32 v96, v80, v81\n\t" "v_add_f32 v103, v82, v83\n\t" "v_add_f32 v94, v96, v103\n\t" "v_add_f32 v96, v96, v103\n\t" "s_nop 1\n\t" \
   "v_permlane32_swap_b32 v96, v94\n\t" "v_add_f32 v96, v96, v94\n\t" "s_nop 1\n\t" \
   "v_add_f32_dpp v96, v96, v96 row_ror:8 row_mask:0xf bank_mask:0xf\n\t" "v_add_f32 v103, v96, v88\n\t" "v_add_f32 v103, v103, v89\n\t" \
   "v_add_f32 v103, v103, v90\n\t" "v_add_f32 v97, v103, v91\n\t" "v_add_f32 v103, v103, v91\n\t" "s_nop 1\n\t" "v_permlane16_swap_b32 v103, v97\n\t" \
   "v_add_f32 v103, v103, v97\n\t" "v_rcp_f32 v100, v103\n\t" "s_nop 0\n\t" "v_pk_mul_f32 v[80:81], v[80:81], v[100:101] op_sel_hi:[1,0]\n\t" \
   "v_pk_mul_f32 v[82:83], v[82:83], v[100:101] op_sel_hi:[1,0]\n\t" "v_pk_mul_f32 v[88:89], v[88:89], v[100:101] op_sel_hi:[1,0]\n\t" \
   "v_pk_mul_f32 v[90:91], v[90:91], v[100:101] op_sel_hi:[1,0]\n\t" "v_cvt_pk_bf16_f32 v48, v80, v81\n\t" "v_cvt_pk_bf16_f32 v49, v82, v83\n\t" \
   "v_cvt_pk_bf16_f32 v52, v88, v89\n\t" "v_cvt_pk_bf16_f32 v53, v90, v91\n\t" "v_min_u32 v48, -2, v48\n\t" "v_min_u32 v52, -2, v52\n\t" \
   "buffer_store_dwordx2 v[48:49], v132, %[rs], s55 offen sc1\n\t" "s_branch 71b\n\t" "74:\n\t" "v_max3_f32 v80, v32, v33, v34\n\t" \
   "v_max3_f32 v81, v35, v36, v37\n\t" "v_max3_f32 v82, v38, v39, v40\n\t" "v_max3_f32 v83, v41, v42, v43\n\t" "v_max3_f32 v80, v80, v81, v82\n\t" \
   "v_max_f32 v81, v80, v83\n\t" "v_max_f32 v80, v80, v83\n\t" "s_nop 1\n\t" "v_permlane16_swap_b32 v80, v81\n\t" "v_max_f32 v82, v80, v81\n\t" \
   "v_max_f32 v80, v80, v81\n\t" "s_nop 1\n\t" "v_permlane32_swap_b32 v80, v82\n\t" "v_max_f32 v80, v80, v82\n\t" "v_sub_f32 v64, v32, v80\n\t" \
   "v_sub_f32 v65, v33, v80\n\t" "v_sub_f32 v66, v34, v80\n\t" "v_sub_f32 v67, v35, v80\n\t" "v_sub_f32 v68, v36, v80\n\t" \
   "v_sub_f32 v69, v37, v80\n\t" "v_sub_f32 v70, v38, v80\n\t" "v_sub_f32 v71, v39, v80\n\t" "v_sub_f32 v72, v40, v80\n\t" \
   "v_sub_f32 v73, v41, v80\n\t" "v_sub_f32 v74, v42, v80\n\t" "v_sub_f32 v75, v43, v80\n\t" "v_exp_f32 v64, v64\n\t" "v_exp_f32 v65, v65\n\t" \
   "v_exp_f32 v66, v66\n\t" "v_exp_f32 v67, v67\n\t" "v_exp_f32 v68, v68\n\t" "v_exp_f32 v69, v69\n\t" "v_exp_f32 v70, v70\n\t" \
   "v_exp_f32 v71, v71\n\t" "v_exp_f32 v72, v72\n\t" "v_exp_f32 v73, v73\n\t" "v_exp_f32 v74, v74\n\t" "v_exp_f32 v75, v75\n\t" "s_nop 0\n\t" \
   "v_add_f32 v80, v64, v65\n\t" "v_add_f32 v81, v66, v67\n\t" "v_add_f32 v82, v68, v69\n\t" "v_add_f32 v83, v70, v71\n\t" \
   "v_add_f32 v84, v72, v73\n\t" "v_add_f32 v85, v74, v75\n\t" "v_add_f32 v80, v80, v81\n\t" "v_add_f32 v82, v82, v83\n\t" \
   "v_add_f32 v84, v84, v85\n\t" "v_add_f32 v80, v80, v82\n\t" "v_fma_f32 v82, v80, 2.0, v84\n\t" "v_fma_f32 v80, v80, 2.0, v84\n\t" "s_nop 0\n\t" \
   "s_nop 0\n\t" "v_permlane16_swap_b32 v80, v82\n\t" "v_add_f32 v81, v80, v82\n\t" "v_add_f32 v80, v80, v82\n\t" "s_nop 0\n\t" "s_nop 0\n\t" \
   "v_permlane32_swap_b32 v80, v81\n\t" "v_add_f32 v80, v80, v81\n\t" "v_rcp_f32 v90, v80\n\t" "s_nop 0\n\t" "v_add_f32 v90, v90, v90\n\t" \
   "s_branch 75b\n\t" "63:\n\t" "s_cmp_lg_u32 s64, 0\n\t" "s_cbranch_scc1 64f\n\t" "s_sub_u32 s69, s56, 0x10000\n\t" "s_and_b32 s69, s69, -2\n\t" \
   "s_branch 50b\n\t" "64:\n\t" "s_mov_b32 s69, s48\n\t" "s_cmp_lt_u32 s64, 3\n\t" "s_cselect_b32 s69, -1, s69\n\t" "s_cmp_lg_u32 s69, -1\n\t" \
   "s_cbranch_scc1 50b\n\t" "s_add_u32 s76, s76, 0x1000000\n\t" "s_mov_b32 s73, 0\n\t" "65:\n\t" "s_add_u32 s73, s73, 1\n\t" \
   "s_min_u32 s72, s73, 7\n\t" "s_mul_i32 s72, s72, 5\n\t" "s_add_u32 s72, s72, %[xs]\n\t" "s_and_b32 s72, s72, 7\n\t" "s_lshl_b32 s71, %[xs], 7\n\t" \
   "v_subrev_u32 v109, s71, v139\n\t" "s_lshl_b32 s72, s72, 7\n\t" "s_add_u32 s72, s72, s66\n\t" "s_cmp_lt_u32 s73, 7\n\t" "s_cbranch_scc1 66f\n\t" \
   "s_sleep 4\n\t" "66:\n\t" "buffer_load_dword v108, v109, %[rs], s72 offen sc1\n\t" "s_waitcnt vmcnt(0)\n\t" "v_readfirstlane_b32 s69, v108\n\t" \
   "s_cmp_gt_u32 s73, 0x10000\n\t" "s_cbranch_scc1 50b\n\t" "s_cmp_eq_u32 s69, -1\n\t" "s_cbranch_scc1 65b\n\t" "s_branch 50b\n\t" "8:\n\t" \
   "s_nop 7\n\t" "s_waitcnt vmcnt(6) lgkmcnt(0)\n\t" "s_add_u32 s70, s70, 1\n\t" "s_cmp_gt_u32 s70, 2\n\t" "s_cbranch_scc1 81f\n\t" \
   "s_lshl_b32 s71, s70, 3\n\t" "s_lshl_b32 s71, 1, s71\n\t" "s_add_u32 s76, s76, s71\n\t" "81:\n\t" "v_mov_b32 v48, 0\n\t" "v_mov_b32 v49, 0\n\t" \
   "v_mov_b32 v50, 0\n\t" "v_mov_b32 v51, 0\n\t" "v_mov_b32 v52, 0\n\t" "v_mov_b32 v53, 0\n\t" "v_mov_b32 v54, 0\n\t" "v_mov_b32 v55, 0\n\t" \
   "v_mov_b32 v56, 0\n\t" "v_mov_b32 v57, 0\n\t" "v_mov_b32 v58, 0\n\t" "v_mov_b32 v59, 0\n\t" "v_mov_b32 v60, 0\n\t" "v_mov_b32 v61, 0\n\t" \
   "v_mov_b32 v62, 0\n\t" "v_mov_b32 v63, 0\n\t" "s_min_u32 s71, s70, 7\n\t" "s_lshr_b32 s67, s71, 1\n\t" "s_add_u32 s67, s67, %[xs]\n\t" \
   "s_and_b32 s67, s67, 3\n\t" "s_lshl_b32 s67, s67, 11\n\t" "s_and_b32 s72, s71, 1\n\t" "s_lshl_b32 s72, s72, 14\n\t" "s_add_u32 s67, s67, s72\n\t" \
   "s_add_u32 s67, s67, s66\n\t" "s_cmp_eq_u32 s70, 1\n\t" "s_cselect_b32 s67, s68, s67\n\t" "s_cmp_lt_u32 s70, 7\n\t" "s_cbranch_scc1 20b\n\t" \
   "s_sleep 3\n\t" "s_cmp_lt_u32 s70, 0x20000\n\t" "s_cbranch_scc1 20b\n\t" "s_mov_b32 s67, %[esc]\n\t" "s_mov_b32 s68, %[esc]\n\t" "s_branch 20b\n\t" \
   "27:\n\t" "v_mov_b32 v98, 0x7ffffff0\n\t" "buffer_store_dword v98, v98, %[rs], 0 offen\n\t" "buffer_store_dword v98, v98, %[rs], 0 offen\n\t" \
   "buffer_store_dword v98, v98, %[rs], 0 offen\n\t" "buffer_store_dword v98, v98, %[rs], 0 offen\n\t" \
   "buffer_store_dword v98, v98, %[rs], 0 offen\n\t" "buffer_store_dword v98, v98, %[rs], 0 offen\n\t" "s_branch 28b\n\t" "141:\n\t" \
   "s_mov_b32 s74, 0\n\t" "s_cmp_eq_u32 s64, 0\n\t" "s_cbranch_scc1 41b\n\t" "42:\n\t" "buffer_load_dword v15, v140, %[rs], s59 offen sc1\n\t" \
   "s_add_u32 s74, s74, 1\n\t" "s_waitcnt vmcnt(0)\n\t" "v_cmp_eq_u32 vcc, -1, v15\n\t" "s_cmp_gt_u32 s74, 0x10000\n\t" "s_cbranch_scc1 41b\n\t" \
   "s_cbranch_vccz 41b\n\t" "s_sleep 2\n\t" "s_branch 42b\n\t" "95:\n\t" "s_movk_i32 s59, 0x1000\n\t" "s_branch 197f\n\t" "97:\n\t" \
   "buffer_load_dwordx4 v[16:19], v138, %[rs], s79 offen offset:0 sc1\n\t" "buffer_load_dwordx4 v[20:23], v138, %[rs], s79 offen offset:1024 sc1\n\t" \
   "buffer_load_dwordx4 v[24:27], v138, %[rs], s79 offen offset:2048 sc1\n\t" \
   "buffer_load_dwordx4 v[28:31], v138, %[rs], s79 offen offset:3072 sc1\n\t" "v_or_b32 v108, s49, v141\n\t" "v_or_b32 v109, s49, v142\n\t" \
   "buffer_store_dwordx4 v[44:47], v108, %[rs], s52 offen sc1\n\t" "buffer_store_dwordx2 v[32:33], v109, %[rs], s51 offen sc1\n\t" \
   "s_waitcnt vmcnt(0)\n\t" "s_branch 96b\n\t" "197:\n\t" "s_mov_b32 s74, 0\n\t" "98:\n\t" "buffer_load_dword v15, v140, %[rs], s59 offen sc1\n\t" \
   "s_add_u32 s74, s74, 1\n\t" "s_waitcnt vmcnt(0)\n\t" "v_cmp_eq_u32 vcc, -1, v15\n\t" "s_cmp_gt_u32 s74, 0x10000\n\t" "s_cbranch_scc1 97b\n\t" \
   "s_cbranch_vccz 97b\n\t" "s_sleep 2\n\t" "s_branch 98b\n\t" "92:\n\t" "s_mov_b32 s59, 0\n\t" "s_branch 145f\n\t" "45:\n\t" \
   "v_mbcnt_lo_u32_b32 v98, -1, 0\n\t" "v_mbcnt_hi_u32_b32 v98, -1, v98\n\t" "v_lshlrev_b32 v98, 4, v98\n\t" "s_add_u32 s72, %[kf0], 4096\n\t" \
   "buffer_load_dwordx4 v[160:163], v98, %[rs], %[kf0] offen offset:0 sc1\n\t" \
   "buffer_load_dwordx4 v[164:167], v98, %[rs], %[kf0] offen offset:1024 sc1\n\t" \
   "buffer_load_dwordx4 v[168:171], v98, %[rs], %[kf0] offen offset:2048 sc1\n\t" \
   "buffer_load_dwordx4 v[172:175], v98, %[rs], %[kf0] offen offset:3072 sc1\n\t" \
   "buffer_load_dwordx4 v[176:179], v98, %[rs], s72 offen offset:0 sc1\n\t" "buffer_load_dwordx4 v[180:183], v98, %[rs], s72 offen offset:1024 sc1\n\t" \
   "buffer_load_dwordx4 v[184:187], v98, %[rs], s72 offen offset:2048 sc1\n\t" \
   "buffer_load_dwordx4 v[188:191], v98, %[rs], s72 offen offset:3072 sc1\n\t" "s_waitcnt vmcnt(0)\n\t" \
   "v_mfma_f32_16x16x32_bf16 v[48:51], v[160:163], v[64:67], 0\n\t" "v_mfma_f32_16x16x32_bf16 v[52:55], v[176:179], v[64:67], 0\n\t" \
   "v_mfma_f32_16x16x32_bf16 v[48:51], v[164:167], v[68:71], v[48:51]\n\t" "v_mfma_f32_16x16x32_bf16 v[52:55], v[180:183], v[68:71], v[52:55]\n\t" \
   "v_mfma_f32_16x16x32_bf16 v[48:51], v[168:171], v[72:75], v[48:51]\n\t" "v_mfma_f32_16x16x32_bf16 v[52:55], v[184:187], v[72:75], v[52:55]\n\t" \
   "v_mfma_f32_16x16x32_bf16 v[48:51], v[172:175], v[104:107], v[48:51]\n\t" "v_mfma_f32_16x16x32_bf16 v[52:55], v[188:191], v[104:107], v[52:55]\n\t" \
   "s_nop 7\n\t" "s_branch 93b\n\t" "145:\n\t" "s_mov_b32 s74, 0\n\t" "46:\n\t" "buffer_load_dword v15, v140, %[rs], s59 offen sc1\n\t" \
   "s_add_u32 s74, s74, 1\n\t" "s_waitcnt vmcnt(0)\n\t" "v_cmp_eq_u32 vcc, -1, v15\n\t" "s_cmp_gt_u32 s74, 0x10000\n\t" "s_cbranch_scc1 45b\n\t" \
   "s_cbranch_vccz 45b\n\t" "s_sleep 2\n\t" "s_branch 46b\n\t" "90:\n\t" "v_or_b32 v108, s49, v141\n\t" "v_or_b32 v109, s49, v142\n\t" \
   "buffer_store_dwordx4 v[44:47], v108, %[rs], s52 offen sc1\n\t" "buffer_store_dwordx2 v[32:33], v109, %[rs], s51 offen sc1\n\t" \
   "s_waitcnt vmcnt(0) lgkmcnt(0)"
#define P1_CLOB "v15", "v16", "v17", "v18", "v19", "v20", "v21", "v22", "v23", "v24", "v25", "v26", "v27", "v28", "v29", "v30", "v31", "v32", "v33", "v34", "v35", "v36", "v37", "v38", "v39", "v40", "v41", "v42", "v43", "v44", "v45", "v46", "v47", "v48", "v49", "v50", "v51", "v52", "v53", "v54", "v55", "v56", "v57", "v58", "v59", "v60", "v61", "v62", "v63", "v64", "v65", "v66", "v67", "v68", "v69", "v70", "v71", "v72", "v73", "v74", "v75", "v76", "v77", "v78", "v79", "v80", "v81", "v82", "v83", "v84", "v85", "v86", "v87", "v88", "v89", "v90", "v91", "v92", "v93", "v94", "v95", "v96", "v97", "v98", "v99", "v100", "v101", "v102", "v103", "v104", "v105", "v106", "v107", "v108", "v109", "v110", "v111", "v144", "v145", "v146", "v147", "v148", "v149", "v150", "v151", "v152", "v153", "v154", "v155", "v156", "v157", "v158", "v159", "v160", "v161", "v162", "v163", "v164", "v165", "v166", "v167", "v168", "v169", "v170", "v171", "v172", "v173", "v174", "v175", "v176", "v177", "v178", "v179", "v180", "v181", "v182", "v183", "v184", "v185", "v186", "v187", "v188", "v189", "v190", "v191", "s60", "s61", "s62", "s63", "s64", "s65", "s66", "s67", "s68", "s69", "s70", "s71", "s72", "s73", "s74", "s75", "s47", "s48", "s49", "s50", "s51", "s52", "s54", "s55", "s79", "s20", "s21", "s22", "s23", "s24", "s25", "s26", "s27", "s28", "s29", "s30", "s31", "s32", "s33", "s34", "s35"
#define PV_ASM \
   "s_mov_b32 s64, -1\n\t" "s_mov_b32 s76, 0\n\t" "s_mov_b32 s77, 0\n\t" "s_mov_b32 s71, 63\n\t" "s_mov_b32 s47, 1300\n\t" "s_mov_b32 s70, 0\n\t" \
   "s_mov_b32 s69, 0\n\t" "s_lshr_b64 s[72:73], %[sel], s71\n\t" "s_lshl_b32 s65, s71, 19\n\t" "s_and_b32 s72, s72, 1\n\t" \
   "s_add_u32 s65, s65, %[pool]\n\t" "s_lshl_b32 s72, s72, 13\n\t" "s_add_u32 s65, s65, s72\n\t" "s_lshr_b64 s[72:73], %[s3], s71\n\t" \
   "s_and_b32 s54, s72, 1\n\t" "s_lshl_b32 s54, s54, 14\n\t" "40:\n\t" "s_cmp_lt_i32 s64, 0\n\t" "s_cselect_b32 s72, 0x7ffffff0, 0\n\t" \
   "s_max_i32 s71, s64, 0\n\t" "v_or_b32 v109, s72, v143\n\t" "s_lshl_b32 s71, s71, 12\n\t" "v_mov_b32 v110, 1\n\t" "s_cmp_eq_u32 s64, 63\n\t" \
   "s_cbranch_scc1 70f\n\t" "71:\n\t" "s_waitcnt vmcnt(0)\n\t" RSTAMP(3) "s_barrier\n\t" "buffer_load_dword v108, v139, %[rs], s65 offen sc1\n\t" \
   "buffer_store_dword v110, v109, %[rs], s71 offen sc1\n\t" PVTRACE "s_add_u32 s48, s69, s47\n\t" "s_cmp_eq_u32 s70, 0\n\t" \
   "s_cselect_b32 s48, s48, -1\n\t" "s_mov_b32 s66, s65\n\t" "s_add_i32 s64, s64, 1\n\t" "s_and_b32 s71, s64, 63\n\t" \
   "s_lshr_b64 s[72:73], %[sel], s71\n\t" "s_lshl_b32 s65, s71, 19\n\t" "s_and_b32 s72, s72, 1\n\t" "s_add_u32 s65, s65, %[pool]\n\t" \
   "s_lshl_b32 s72, s72, 13\n\t" "s_add_u32 s65, s65, s72\n\t" "s_xor_b32 s67, s54, %[lad0]\n\t" "s_xor_b32 s68, s67, 0x4000\n\t" \
   "s_add_u32 s67, s67, s66\n\t" "s_add_u32 s68, s68, s66\n\t" "s_lshr_b64 s[72:73], %[s3], s71\n\t" "s_and_b32 s54, s72, 1\n\t" \
   "s_lshl_b32 s54, s54, 14\n\t" "s_mov_b32 s70, 0\n\t" "s_lshr_b64 s[72:73], %[ysel], s71\n\t" "s_mul_i32 s73, s71, 43\n\t" \
   "s_mul_i32 s49, s71, 80\n\t" "s_and_b32 s72, s72, 1\n\t" "s_lshr_b32 s73, s73, 7\n\t" "s_add_u32 s49, s49, s72\n\t" \
   "s_lshl4_add_u32 s49, s73, s49\n\t" "s_lshl_b32 s49, s49, 13\n\t" "s_add_u32 s49, s49, %[yb]\n\t" "s_add_u32 s50, s49, 32768\n\t" \
   "s_add_u32 s51, s49, 65536\n\t" "s_add_u32 s52, s49, 98304\n\t" "s_add_u32 s53, s49, 131072\n\t" "v_mov_b32 v48, 0\n\t" "v_mov_b32 v49, 0\n\t" \
   "v_mov_b32 v50, 0\n\t" "v_mov_b32 v51, 0\n\t" "v_mov_b32 v52, 0\n\t" "v_mov_b32 v53, 0\n\t" "v_mov_b32 v54, 0\n\t" "v_mov_b32 v55, 0\n\t" \
   "v_mov_b32 v144, 0\n\t" "v_mov_b32 v145, 0\n\t" "v_mov_b32 v146, 0\n\t" "v_mov_b32 v147, 0\n\t" "v_mov_b32 v148, 0\n\t" "v_mov_b32 v149, 0\n\t" \
   "v_mov_b32 v150, 0\n\t" "v_mov_b32 v151, 0\n\t" "v_mov_b32 v152, 0\n\t" "v_mov_b32 v153, 0\n\t" "v_mov_b32 v154, 0\n\t" "v_mov_b32 v155, 0\n\t" \
   "v_mov_b32 v156, 0\n\t" "v_mov_b32 v157, 0\n\t" "v_mov_b32 v158, 0\n\t" "v_mov_b32 v159, 0\n\t" "v_mov_b32 v160, 0\n\t" "v_mov_b32 v161, 0\n\t" \
   "v_mov_b32 v162, 0\n\t" "v_mov_b32 v163, 0\n\t" "s_cmp_eq_u32 s64, 64\n\t" "s_cbranch_scc1 90f\n\t" "s_waitcnt vmcnt(1)\n\t" \
   "v_readlane_b32 s69, v108, 0\n\t" "v_readlane_b32 s71, v108, 1\n\t" "s_cmp_eq_u32 s69, -1\n\t" "s_cbranch_scc1 63f\n\t" "s_cmp_eq_u32 s71, -1\n\t" \
   "s_cselect_b32 s47, s47, s71\n\t" "50:\n\t" "s_add_u32 s73, s69, %[m1c]\n\t" RSTAMP(5) "51:\n\t" "s_memrealtime s[60:61]\n\t" \
   "s_memtime s[62:63]\n\t" "s_waitcnt lgkmcnt(0)\n\t" "s_sub_u32 s74, s62, s57\n\t" "s_mul_i32 s74, s74, s58\n\t" "s_lshr_b32 s74, s74, 12\n\t" \
   "s_add_u32 s74, s74, s56\n\t" "s_lshl_b32 s60, s60, 3\n\t" "s_sub_u32 s75, s74, s60\n\t" "s_cmp_lt_u32 s75, 32\n\t" "s_cbranch_scc1 52f\n\t" \
   "s_cmp_lt_i32 s75, 0\n\t" "s_cselect_b32 s61, 0, 31\n\t" "s_add_u32 s74, s60, s61\n\t" "s_sub_i32 s75, s61, s75\n\t" "s_add_i32 s75, s75, 1\n\t" \
   "s_ashr_i32 s75, s75, 1\n\t" "s_min_i32 s75, s75, 8\n\t" "s_max_i32 s75, s75, -8\n\t" "s_mov_b32 s56, s74\n\t" "s_mov_b32 s57, s62\n\t" \
   "s_add_u32 s58, s58, s75\n\t" "52:\n\t" "s_sub_i32 s74, s73, s74\n\t" "s_cmp_le_i32 s74, 150\n\t" "s_cbranch_scc1 53f\n\t" \
   "s_cmp_lt_i32 s74, 260\n\t" "s_cbranch_scc1 51b\n\t" "s_sleep 2\n\t" "s_cmp_lt_i32 s74, 32000\n\t" "s_cbranch_scc1 51b\n\t" "s_mov_b32 s74, 0\n\t" \
   "53:\n\t" "s_sub_i32 s74, s74, 56\n\t" "s_max_i32 s74, s74, 0\n\t" "s_mul_i32 s74, s74, 23\n\t" "s_lshr_b32 s74, s74, 5\n\t" \
   "s_and_b32 s74, s74, -4\n\t" "s_getpc_b64 s[60:61]\n\t" "54:\n\t" "s_sub_u32 s74, 55f-54b, s74\n\t" "s_add_u32 s60, s60, s74\n\t" \
   "s_addc_u32 s61, s61, 0\n\t" "s_setpc_b64 s[60:61]\n\t" "s_nop 3\n\t" "s_nop 3\n\t" "s_nop 3\n\t" "s_nop 3\n\t" "s_nop 3\n\t" "s_nop 3\n\t" \
   "s_nop 3\n\t" "s_nop 3\n\t" "s_nop 3\n\t" "s_nop 3\n\t" "s_nop 3\n\t" "s_nop 3\n\t" "s_nop 3\n\t" "s_nop 3\n\t" "s_nop 3\n\t" "s_nop 3\n\t" \
   "s_nop 3\n\t" "s_nop 3\n\t" "s_nop 3\n\t" "s_nop 3\n\t" "s_nop 3\n\t" "s_nop 3\n\t" "s_nop 3\n\t" "s_nop 3\n\t" "55:\n\t" "20:\n\t" RSTAMP(4) \
   "buffer_load_dwordx4 v[64:67], v128, %[rs], s67 offen sc1\n\t" "buffer_load_dwordx4 v[68:71], v129, %[rs], s67 offen sc1\n\t" \
   "buffer_load_dwordx4 v[72:75], v130, %[rs], s67 offen sc1\n\t" "s_waitcnt vmcnt(2)\n\t" "ds_write_b128 v112, v[64:67]\n\t" \
   "v_smfmac_f32_16x16x64_bf16 v[48:51], v[64:67], a[0:7], v134\n\t" "s_waitcnt vmcnt(1)\n\t" "ds_write_b128 v113, v[68:71]\n\t" \
   "s_waitcnt vmcnt(0)\n\t" "ds_write_b128 v114, v[72:75]\n\t" RSTAMP(7) "v_smfmac_f32_16x16x64_bf16 v[52:55], v[64:67], a[64:71], v134\n\t" \
   "v_max3_u32 v110, v64, v66, v68\n\t" "v_max3_u32 v111, v70, v72, v74\n\t" "v_smfmac_f32_16x16x64_bf16 v[48:51], v[68:71], a[8:15], v134\n\t" \
   "v_max_u32 v110, v110, v111\n\t" "v_cmp_eq_u32 vcc, -1, v110\n\t" "v_smfmac_f32_16x16x64_bf16 v[52:55], v[68:71], a[72:79], v134\n\t" \
   "s_cbranch_vccnz 8f\n\t" "s_waitcnt lgkmcnt(0)\n\t" "s_barrier\n\t" RSTAMP(0) "ds_read_b128 v[76:79], v115\n\t" "ds_read_b128 v[80:83], v116\n\t" \
   "ds_read_b128 v[84:87], v117\n\t" "v_smfmac_f32_16x16x64_bf16 v[48:51], v[72:75], a[16:23], v134\n\t" "ds_read_b128 v[88:91], v118\n\t" \
   "ds_read_b128 v[92:95], v119\n\t" "v_smfmac_f32_16x16x64_bf16 v[52:55], v[72:75], a[80:87], v134\n\t" "ds_read_b128 v[96:99], v120\n\t" \
   "ds_read_b128 v[100:103], v121\n\t" "s_waitcnt lgkmcnt(6)\n\t" "v_smfmac_f32_16x16x64_bf16 v[48:51], v[76:79], a[24:31], v134\n\t" \
   "v_smfmac_f32_16x16x64_bf16 v[52:55], v[76:79], a[88:95], v134\n\t" "s_waitcnt lgkmcnt(5)\n\t" \
   "v_smfmac_f32_16x16x64_bf16 v[48:51], v[80:83], a[32:39], v134\n\t" "v_smfmac_f32_16x16x64_bf16 v[52:55], v[80:83], a[96:103], v134\n\t" \
   "s_waitcnt lgkmcnt(4)\n\t" "v_smfmac_f32_16x16x64_bf16 v[48:51], v[84:87], a[40:47], v134\n\t" \
   "v_smfmac_f32_16x16x64_bf16 v[52:55], v[84:87], a[104:111], v134\n\t" "s_waitcnt lgkmcnt(3)\n\t" \
   "v_smfmac_f32_16x16x64_bf16 v[48:51], v[88:91], a[48:55], v134\n\t" "v_smfmac_f32_16x16x64_bf16 v[52:55], v[88:91], a[112:119], v134\n\t" \
   "s_waitcnt lgkmcnt(0)\n\t" "v_smfmac_f32_16x16x64_bf16 v[48:51], v[92:95], a[56:63], v134\n\t" \
   "v_smfmac_f32_16x16x64_bf16 v[48:51], v[96:99], v[192:199], v134\n\t" "v_smfmac_f32_16x16x64_bf16 v[48:51], v[100:103], v[200:207], v134\n\t" \
   "v_smfmac_f32_16x16x64_bf16 v[52:55], v[92:95], a[120:127], v134\n\t" "v_smfmac_f32_16x16x64_bf16 v[52:55], v[96:99], v[208:215], v134\n\t" \
   "v_smfmac_f32_16x16x64_bf16 v[52:55], v[100:103], v[216:223], v134\n\t" "v_add_f32 v48, v48, v49\n\t" "v_add_f32 v49, v50, v51\n\t" \
   "v_cvt_pk_bf16_f32 v48, v48, v49\n\t" "ds_write_b32 v135, v48 offset:10240\n\t" "s_nop 3\n\t" "v_add_f32 v52, v52, v53\n\t" \
   "v_add_f32 v53, v54, v55\n\t" "v_cvt_pk_bf16_f32 v52, v52, v53\n\t" "ds_write_b32 v135, v52 offset:10496\n\t" "s_waitcnt lgkmcnt(0)\n\t" \
   "s_barrier\n\t" RSTAMP(6) "ds_read_b64_tr_b16 v[16:17], v136 offset:10240\n\t" "ds_read_b64_tr_b16 v[18:19], v136 offset:10496\n\t" \
   "ds_read_b64_tr_b16 v[20:21], v136 offset:10752\n\t" "ds_read_b64_tr_b16 v[22:23], v136 offset:11008\n\t" \
   "ds_read_b64_tr_b16 v[24:25], v136 offset:11264\n\t" "ds_read_b64_tr_b16 v[26:27], v136 offset:11520\n\t" \
   "ds_read_b64_tr_b16 v[28:29], v136 offset:11776\n\t" "ds_read_b64_tr_b16 v[30:31], v136 offset:12032\n\t" "s_waitcnt lgkmcnt(6)\n\t" \
   "v_smfmac_f32_16x16x64_bf16 v[144:147], v[16:19], a[128:135], v131\n\t" "v_smfmac_f32_16x16x64_bf16 v[148:151], v[16:19], a[160:167], v131\n\t" \
   "v_smfmac_f32_16x16x64_bf16 v[152:155], v[16:19], a[192:199], v131\n\t" "v_smfmac_f32_16x16x64_bf16 v[156:159], v[16:19], a[224:231], v131\n\t" \
   "v_smfmac_f32_16x16x64_bf16 v[160:163], v[16:19], v[224:231], v131\n\t" "s_waitcnt lgkmcnt(0)\n\t" \
   "v_smfmac_f32_16x16x64_bf16 v[144:147], v[20:23], a[136:143], v131\n\t" "v_smfmac_f32_16x16x64_bf16 v[144:147], v[24:27], a[144:151], v131\n\t" \
   "v_smfmac_f32_16x16x64_bf16 v[144:147], v[28:31], a[152:159], v131\n\t" "v_smfmac_f32_16x16x64_bf16 v[148:151], v[20:23], a[168:175], v131\n\t" \
   "v_smfmac_f32_16x16x64_bf16 v[148:151], v[24:27], a[176:183], v131\n\t" "v_smfmac_f32_16x16x64_bf16 v[148:151], v[28:31], a[184:191], v131\n\t" \
   "v_smfmac_f32_16x16x64_bf16 v[152:155], v[20:23], a[200:207], v131\n\t" "v_cvt_pk_bf16_f32 v164, v144, v145\n\t" \
   "v_cvt_pk_bf16_f32 v165, v146, v147\n\t" "v_smfmac_f32_16x16x64_bf16 v[152:155], v[24:27], a[208:215], v131\n\t" "v_min_u32 v164, -2, v164\n\t" \
   "buffer_store_dwordx2 v[164:165], v132, %[rs], s49 offen sc1\n\t" "v_smfmac_f32_16x16x64_bf16 v[152:155], v[28:31], a[216:223], v131\n\t" \
   "v_smfmac_f32_16x16x64_bf16 v[156:159], v[20:23], a[232:239], v131\n\t" "v_cvt_pk_bf16_f32 v166, v148, v149\n\t" \
   "v_cvt_pk_bf16_f32 v167, v150, v151\n\t" "v_smfmac_f32_16x16x64_bf16 v[156:159], v[24:27], a[240:247], v131\n\t" "v_min_u32 v166, -2, v166\n\t" \
   "buffer_store_dwordx2 v[166:167], v132, %[rs], s50 offen sc1\n\t" "v_smfmac_f32_16x16x64_bf16 v[156:159], v[28:31], a[248:255], v131\n\t" \
   "v_smfmac_f32_16x16x64_bf16 v[160:163], v[20:23], v[232:239], v131\n\t" "v_cvt_pk_bf16_f32 v168, v152, v153\n\t" \
   "v_cvt_pk_bf16_f32 v169, v154, v155\n\t" "v_smfmac_f32_16x16x64_bf16 v[160:163], v[24:27], v[240:247], v131\n\t" "v_min_u32 v168, -2, v168\n\t" \
   "buffer_store_dwordx2 v[168:169], v132, %[rs], s51 offen sc1\n\t" "v_smfmac_f32_16x16x64_bf16 v[160:163], v[28:31], v[248:255], v131\n\t" \
   "v_cvt_pk_bf16_f32 v170, v156, v157\n\t" "v_cvt_pk_bf16_f32 v171, v158, v159\n\t" "v_min_u32 v170, -2, v170\n\t" \
   "buffer_store_dwordx2 v[170:171], v132, %[rs], s52 offen sc1\n\t" "s_nop 3\n\t" "v_cvt_pk_bf16_f32 v172, v160, v161\n\t" \
   "v_cvt_pk_bf16_f32 v173, v162, v163\n\t" "v_min_u32 v172, -2, v172\n\t" "buffer_store_dwordx2 v[172:173], v132, %[rs], s53 offen sc1\n\t" RSTAMP(2) \
   "s_branch 40b\n\t" "70:\n\t" "s_mov_b32 s46, 0\n\t" "s_lshr_b64 s[72:73], %[sel], s46\n\t" "s_lshl_b32 s45, s46, 19\n\t" "s_and_b32 s72, s72, 1\n\t" \
   "s_add_u32 s45, s45, %[pool]\n\t" "s_lshl_b32 s72, s72, 13\n\t" "s_add_u32 s45, s45, s72\n\t" "v_mov_b32 v76, -1\n\t" "v_mov_b32 v77, -1\n\t" \
   "v_mov_b32 v78, -1\n\t" "v_mov_b32 v79, -1\n\t" "buffer_store_dwordx4 v[76:79], v133, %[rs], s45 offen sc1\n\t" \
   "buffer_store_dwordx4 v[76:79], v133, %[rs], s45 offen offset:1024 sc1\n\t" "s_branch 71b\n\t" "63:\n\t" "s_cmp_lg_u32 s64, 0\n\t" \
   "s_cbranch_scc1 64f\n\t" "s_sub_u32 s69, s56, 0x10000\n\t" "s_and_b32 s69, s69, -2\n\t" "s_branch 50b\n\t" "64:\n\t" "s_mov_b32 s69, s48\n\t" \
   "s_cmp_lt_u32 s64, 3\n\t" "s_cselect_b32 s69, -1, s69\n\t" "s_cmp_lg_u32 s69, -1\n\t" "s_cbranch_scc1 50b\n\t" "s_add_u32 s76, s76, 0x1000000\n\t" \
   "s_mov_b32 s73, 0\n\t" "65:\n\t" "s_add_u32 s73, s73, 1\n\t" "s_sleep 6\n\t" "s_and_b32 s72, s73, 3\n\t" "s_add_u32 s72, s72, 8\n\t" \
   "s_sub_u32 s72, s72, %[xs]\n\t" "s_lshl_b32 s72, s72, 7\n\t" "s_add_u32 s72, s72, s66\n\t" "buffer_load_dword v108, v139, %[rs], s72 offen sc1\n\t" \
   "s_waitcnt vmcnt(0)\n\t" "v_readfirstlane_b32 s69, v108\n\t" "s_cmp_gt_u32 s73, 0x10000\n\t" "s_cbranch_scc1 50b\n\t" "s_cmp_eq_u32 s69, -1\n\t" \
   "s_cbranch_scc1 65b\n\t" "s_branch 50b\n\t" "8:\n\t" "s_nop 7\n\t" "s_waitcnt lgkmcnt(0)\n\t" "s_add_u32 s70, s70, 1\n\t" "s_cmp_gt_u32 s70, 2\n\t" \
   "s_cbranch_scc1 81f\n\t" "s_lshl_b32 s71, s70, 3\n\t" "s_lshl_b32 s71, 1, s71\n\t" "s_add_u32 s76, s76, s71\n\t" "81:\n\t" "v_mov_b32 v48, 0\n\t" \
   "v_mov_b32 v49, 0\n\t" "v_mov_b32 v50, 0\n\t" "v_mov_b32 v51, 0\n\t" "v_mov_b32 v52, 0\n\t" "v_mov_b32 v53, 0\n\t" "v_mov_b32 v54, 0\n\t" \
   "v_mov_b32 v55, 0\n\t" "s_add_u32 s71, s70, 1\n\t" "s_min_u32 s71, s71, 7\n\t" "s_lshr_b32 s67, s71, 1\n\t" "s_add_u32 s67, s67, %[xs]\n\t" \
   "s_and_b32 s67, s67, 3\n\t" "s_lshl_b32 s67, s67, 11\n\t" "s_and_b32 s72, s71, 1\n\t" "s_lshl_b32 s72, s72, 14\n\t" "s_add_u32 s67, s67, s72\n\t" \
   "s_add_u32 s67, s67, s66\n\t" "s_cmp_lt_u32 s70, 6\n\t" "s_cbranch_scc1 20b\n\t" "s_sleep 3\n\t" "s_cmp_lt_u32 s70, 0x20000\n\t" \
   "s_cbranch_scc1 20b\n\t" "s_mov_b32 s67, %[esc]\n\t" "s_branch 20b\n\t" "90:\n\t" "s_waitcnt vmcnt(0) lgkmcnt(0)"
#define PV_CLOB "v16", "v17", "v18", "v19", "v20", "v21", "v22", "v23", "v24", "v25", "v26", "v27", "v28", "v29", "v30", "v31", "v48", "v49", "v50", "v51", "v52", "v53", "v54", "v55", "v56", "v57", "v58", "v59", "v60", "v61", "v62", "v63", "v64", "v65", "v66", "v67", "v68", "v69", "v70", "v71", "v72", "v73", "v74", "v75", "v76", "v77", "v78", "v79", "v80", "v81", "v82", "v83", "v84", "v85", "v86", "v87", "v88", "v89", "v90", "v91", "v92", "v93", "v94", "v95", "v96", "v97", "v98", "v99", "v100", "v101", "v102", "v103", "v104", "v105", "v106", "v107", "v108", "v109", "v110", "v111", "v144", "v145", "v146", "v147", "v148", "v149", "v150", "v151", "v152", "v153", "v154", "v155", "v156", "v157", "v158", "v159", "v160", "v161", "v162", "v163", "v164", "v165", "v166", "v167", "v168", "v169", "v170", "v171", "v172", "v173", "v174", "v175", "s60", "s61", "s62", "s63", "s64", "s65", "s66", "s67", "s68", "s69", "s70", "s71", "s72", "s73", "s74", "s75", "s45", "s46", "s47", "s48", "s49", "s50", "s51", "s52", "s53", "s54"
#define WAIT_ASM \
   "11:\n\t" "s_memrealtime s[60:61]\n\t" "s_memtime s[62:63]\n\t" "s_waitcnt lgkmcnt(0)\n\t" "s_sub_u32 s74, s62, s57\n\t" \
   "s_mul_i32 s74, s74, s58\n\t" "s_lshr_b32 s74, s74, 12\n\t" "s_add_u32 s74, s74, s56\n\t" "s_lshl_b32 s60, s60, 3\n\t" "s_sub_u32 s75, s74, s60\n\t" \
   "s_cmp_lt_u32 s75, 32\n\t" "s_cbranch_scc1 12f\n\t" "s_cmp_lt_i32 s75, 0\n\t" "s_cselect_b32 s61, 0, 31\n\t" "s_add_u32 s74, s60, s61\n\t" \
   "s_sub_i32 s75, s61, s75\n\t" "s_add_i32 s75, s75, 1\n\t" "s_ashr_i32 s75, s75, 1\n\t" "s_min_i32 s75, s75, 8\n\t" "s_max_i32 s75, s75, -8\n\t" \
   "s_mov_b32 s56, s74\n\t" "s_mov_b32 s57, s62\n\t" "s_add_u32 s58, s58, s75\n\t" "12:\n\t" "s_sub_i32 s74, %[ta], s74\n\t" \
   "s_cmp_le_i32 s74, 150\n\t" "s_cbranch_scc1 13f\n\t" "s_cmp_lt_i32 s74, 260\n\t" "s_cbranch_scc1 11b\n\t" "s_sleep 2\n\t" \
   "s_cmp_lt_i32 s74, 32000\n\t" "s_cbranch_scc1 11b\n\t" "s_mov_b32 s74, 0\n\t" "13:\n\t" "s_sub_i32 s74, s74, 56\n\t" "s_max_i32 s74, s74, 0\n\t" \
   "s_mul_i32 s74, s74, 23\n\t" "s_lshr_b32 s74, s74, 5\n\t" "s_and_b32 s74, s74, -4\n\t" "s_getpc_b64 s[60:61]\n\t" "14:\n\t" \
   "s_sub_u32 s74, 15f-14b, s74\n\t" "s_add_u32 s60, s60, s74\n\t" "s_addc_u32 s61, s61, 0\n\t" "s_setpc_b64 s[60:61]\n\t" "s_nop 3\n\t" "s_nop 3\n\t" \
   "s_nop 3\n\t" "s_nop 3\n\t" "s_nop 3\n\t" "s_nop 3\n\t" "s_nop 3\n\t" "s_nop 3\n\t" "s_nop 3\n\t" "s_nop 3\n\t" "s_nop 3\n\t" "s_nop 3\n\t" \
   "s_nop 3\n\t" "s_nop 3\n\t" "s_nop 3\n\t" "s_nop 3\n\t" "s_nop 3\n\t" "s_nop 3\n\t" "s_nop 3\n\t" "s_nop 3\n\t" "s_nop 3\n\t" "s_nop 3\n\t" \
   "s_nop 3\n\t" "s_nop 3\n\t" "15:\n\t" ""
#define WAIT_CLOB "s60", "s61", "s62", "s63", "s74", "s75"
#define FIN_ASM \
   "s_mov_b32 s40, 0\n\t" "s_mov_b32 s76, 0\n\t" "s_mov_b32 s77, 0\n\t" "s_sub_u32 s45, s56, 0x10000\n\t" "v_mov_b32 v13, -2\n\t" \
   "v_mov_b32 v11, -1\n\t" "30:\n\t" "s_lshr_b64 s[72:73], %[sel], s40\n\t" "s_lshl_b32 s41, s40, 19\n\t" "s_and_b32 s72, s72, 1\n\t" \
   "s_add_u32 s41, s41, %[pool]\n\t" "s_lshl_b32 s72, s72, 13\n\t" "s_add_u32 s41, s41, s72\n\t" "s_lshr_b64 s[72:73], %[s3], s40\n\t" \
   "s_and_b32 s47, s72, 1\n\t" "s_lshl_b32 s47, s47, 14\n\t" "s_mul_i32 s49, s40, 1310720\n\t" "s_add_u32 s49, s49, %[su]\n\t" \
   "s_lshr_b64 s[72:73], %[ysel], s40\n\t" "s_mul_i32 s73, s40, 43\n\t" "s_mul_i32 s50, s40, 80\n\t" "s_and_b32 s72, s72, 1\n\t" \
   "s_lshr_b32 s73, s73, 7\n\t" "s_add_u32 s50, s50, s72\n\t" "s_lshl4_add_u32 s50, s73, s50\n\t" "s_lshl_b32 s50, s50, 13\n\t" \
   "s_add_u32 s50, s50, %[yb]\n\t" "s_add_u32 s51, s50, 4096\n\t" "s_lshl_b32 s52, s40, 12\n\t" "s_add_u32 s52, s52, %[uf]\n\t" \
   "s_add_u32 s69, s45, %[tu]\n\t" "v_max3_u32 v15, v8, v9, v10\n\t" "v_max_u32 v15, v15, v11\n\t" "v_cmp_eq_u32 vcc, -1, v15\n\t" \
   "s_add_u32 s40, s40, 1\n\t" "s_cbranch_vccnz 95f\n\t" "96:\n\t" "11:\n\t" "s_memrealtime s[60:61]\n\t" "s_memtime s[62:63]\n\t" \
   "s_waitcnt lgkmcnt(0)\n\t" "s_sub_u32 s74, s62, s57\n\t" "s_mul_i32 s74, s74, s58\n\t" "s_lshr_b32 s74, s74, 12\n\t" "s_add_u32 s74, s74, s56\n\t" \
   "s_lshl_b32 s60, s60, 3\n\t" "s_sub_u32 s75, s74, s60\n\t" "s_cmp_lt_u32 s75, 32\n\t" "s_cbranch_scc1 12f\n\t" "s_cmp_lt_i32 s75, 0\n\t" \
   "s_cselect_b32 s61, 0, 31\n\t" "s_add_u32 s74, s60, s61\n\t" "s_sub_i32 s75, s61, s75\n\t" "s_add_i32 s75, s75, 1\n\t" "s_ashr_i32 s75, s75, 1\n\t" \
   "s_min_i32 s75, s75, 8\n\t" "s_max_i32 s75, s75, -8\n\t" "s_mov_b32 s56, s74\n\t" "s_mov_b32 s57, s62\n\t" "s_add_u32 s58, s58, s75\n\t" "12:\n\t" \
   "s_sub_i32 s74, s69, s74\n\t" "s_cmp_le_i32 s74, 150\n\t" "s_cbranch_scc1 13f\n\t" "s_cmp_lt_i32 s74, 260\n\t" "s_cbranch_scc1 11b\n\t" \
   "s_sleep 2\n\t" "s_cmp_lt_i32 s74, 32000\n\t" "s_cbranch_scc1 11b\n\t" "s_mov_b32 s74, 0\n\t" "13:\n\t" "s_sub_i32 s74, s74, 56\n\t" \
   "s_max_i32 s74, s74, 0\n\t" "s_mul_i32 s74, s74, 23\n\t" "s_lshr_b32 s74, s74, 5\n\t" "s_and_b32 s74, s74, -4\n\t" "s_getpc_b64 s[60:61]\n\t" \
   "14:\n\t" "s_sub_u32 s74, 15f-14b, s74\n\t" "s_add_u32 s60, s60, s74\n\t" "s_addc_u32 s61, s61, 0\n\t" "s_setpc_b64 s[60:61]\n\t" "s_nop 3\n\t" \
   "s_nop 3\n\t" "s_nop 3\n\t" "s_nop 3\n\t" "s_nop 3\n\t" "s_nop 3\n\t" "s_nop 3\n\t" "s_nop 3\n\t" "s_nop 3\n\t" "s_nop 3\n\t" "s_nop 3\n\t" \
   "s_nop 3\n\t" "s_nop 3\n\t" "s_nop 3\n\t" "s_nop 3\n\t" "s_nop 3\n\t" "s_nop 3\n\t" "s_nop 3\n\t" "s_nop 3\n\t" "s_nop 3\n\t" "s_nop 3\n\t" \
   "s_nop 3\n\t" "s_nop 3\n\t" "s_nop 3\n\t" "15:\n\t" RSTAMP(4) "buffer_load_dwordx4 a[0:3], v16, %[rs], s49 offen offset:0 sc1\n\t" "s_nop 0\n\t" \
   "buffer_load_dwordx4 a[4:7], v16, %[rs], s49 offen offset:1024 sc1\n\t" "s_nop 0\n\t" \
   "buffer_load_dwordx4 a[8:11], v16, %[rs], s49 offen offset:2048 sc1\n\t" "s_nop 0\n\t" \
   "buffer_load_dwordx4 a[12:15], v16, %[rs], s49 offen offset:3072 sc1\n\t" "s_nop 0\n\t" "s_add_u32 s49, s49, 4096\n\t" \
   "buffer_load_dwordx4 a[16:19], v16, %[rs], s49 offen offset:0 sc1\n\t" "s_nop 0\n\t" \
   "buffer_load_dwordx4 a[20:23], v16, %[rs], s49 offen offset:1024 sc1\n\t" "s_nop 0\n\t" \
   "buffer_load_dwordx4 a[24:27], v16, %[rs], s49 offen offset:2048 sc1\n\t" "s_nop 0\n\t" \
   "buffer_load_dwordx4 a[28:31], v16, %[rs], s49 offen offset:3072 sc1\n\t" "s_nop 0\n\t" "s_add_u32 s49, s49, 4096\n\t" \
   "buffer_load_dwordx4 a[32:35], v16, %[rs], s49 offen offset:0 sc1\n\t" "s_nop 0\n\t" \
   "buffer_load_dwordx4 a[36:39], v16, %[rs], s49 offen offset:1024 sc1\n\t" "s_nop 0\n\t" \
   "buffer_load_dwordx4 a[40:43], v16, %[rs], s49 offen offset:2048 sc1\n\t" "s_nop 0\n\t" \
   "buffer_load_dwordx4 a[44:47], v16, %[rs], s49 offen offset:3072 sc1\n\t" "s_nop 0\n\t" "s_add_u32 s49, s49, 4096\n\t" \
   "buffer_load_dwordx4 a[48:51], v16, %[rs], s49 offen offset:0 sc1\n\t" "s_nop 0\n\t" \
   "buffer_load_dwordx4 a[52:55], v16, %[rs], s49 offen offset:1024 sc1\n\t" "s_nop 0\n\t" \
   "buffer_load_dwordx4 a[56:59], v16, %[rs], s49 offen offset:2048 sc1\n\t" "s_nop 0\n\t" \
   "buffer_load_dwordx4 a[60:63], v16, %[rs], s49 offen offset:3072 sc1\n\t" "s_nop 0\n\t" "s_add_u32 s49, s49, 4096\n\t" \
   "buffer_load_dwordx4 a[64:67], v16, %[rs], s49 offen offset:0 sc1\n\t" "s_nop 0\n\t" \
   "buffer_load_dwordx4 a[68:71], v16, %[rs], s49 offen offset:1024 sc1\n\t" "s_nop 0\n\t" \
   "buffer_load_dwordx4 a[72:75], v16, %[rs], s49 offen offset:2048 sc1\n\t" "s_nop 0\n\t" \
   "buffer_load_dwordx4 a[76:79], v16, %[rs], s49 offen offset:3072 sc1\n\t" "s_nop 0\n\t" "s_add_u32 s49, s49, 4096\n\t" \
   "buffer_load_dwordx4 a[80:83], v16, %[rs], s49 offen offset:0 sc1\n\t" "s_nop 0\n\t" \
   "buffer_load_dwordx4 a[84:87], v16, %[rs], s49 offen offset:1024 sc1\n\t" "s_nop 0\n\t" \
   "buffer_load_dwordx4 a[88:91], v16, %[rs], s49 offen offset:2048 sc1\n\t" "s_nop 0\n\t" \
   "buffer_load_dwordx4 a[92:95], v16, %[rs], s49 offen offset:3072 sc1\n\t" "s_nop 0\n\t" "s_add_u32 s49, s49, 4096\n\t" \
   "buffer_load_dwordx4 a[96:99], v16, %[rs], s49 offen offset:0 sc1\n\t" "s_nop 0\n\t" \
   "buffer_load_dwordx4 a[100:103], v16, %[rs], s49 offen offset:1024 sc1\n\t" "s_nop 0\n\t" \
   "buffer_load_dwordx4 a[104:107], v16, %[rs], s49 offen offset:2048 sc1\n\t" "s_nop 0\n\t" \
   "buffer_load_dwordx4 a[108:111], v16, %[rs], s49 offen offset:3072 sc1\n\t" "s_nop 0\n\t" "s_add_u32 s49, s49, 4096\n\t" \
   "buffer_load_dwordx4 a[112:115], v16, %[rs], s49 offen offset:0 sc1\n\t" "s_nop 0\n\t" \
   "buffer_load_dwordx4 a[116:119], v16, %[rs], s49 offen offset:1024 sc1\n\t" "s_nop 0\n\t" \
   "buffer_load_dwordx4 a[120:123], v16, %[rs], s49 offen offset:2048 sc1\n\t" "s_nop 0\n\t" \
   "buffer_load_dwordx4 a[124:127], v16, %[rs], s49 offen offset:3072 sc1\n\t" "s_nop 0\n\t" "s_add_u32 s52, s52, 4096\n\t" \
   "buffer_load_dwordx4 v[8:11], v19, %[rs], s52 offen sc1\n\t" RSTAMP(0) "s_mov_b32 s46, 0\n\t" "s_xor_b32 s64, s47, %[lad0]\n\t" \
   "s_and_b32 s65, s64, 0x4000\n\t" "s_xor_b32 s64, s64, s65\n\t" "v_or_b32 v224, s65, v16\n\t" "v_or_b32 v225, s65, v17\n\t" \
   "v_xor_b32 v226, 0x4000, v224\n\t" "v_xor_b32 v227, 0x4000, v225\n\t" "s_add_u32 s64, s64, s41\n\t" "v_xor_b32 v14, s47, v20\n\t" \
   "v_xor_b32 v15, 0x4000, v14\n\t" "s_add_u32 s65, s64, 32768\n\t" "s_add_u32 s66, s64, 65536\n\t" "s_add_u32 s67, s64, 98304\n\t" \
   "s_add_u32 s68, s64, 131072\n\t" "s_add_u32 s69, s64, 163840\n\t" "s_add_u32 s70, s64, 196608\n\t" "s_add_u32 s71, s64, 229376\n\t" \
   "s_add_u32 s72, s64, 262144\n\t" "s_add_u32 s73, s64, 294912\n\t" "s_add_u32 s53, s41, 4096\n\t" "v_mov_b32 v28, 0\n\t" "v_mov_b32 v29, 0\n\t" \
   "v_mov_b32 v30, 0\n\t" "v_mov_b32 v31, 0\n\t" "19:\n\t" "ds_read_b128 v[24:27], v23\n\t" "s_waitcnt lgkmcnt(0)\n\t" \
   "v_readfirstlane_b32 s74, v26\n\t" "v_readfirstlane_b32 s44, v24\n\t" "v_readfirstlane_b32 s45, v25\n\t" "s_sub_u32 s48, s44, 110\n\t" \
   "s_cmp_lt_u32 s74, s40\n\t" "s_cbranch_scc1 18f\n\t" RSTAMP(7) "21:\n\t" "s_memrealtime s[60:61]\n\t" "s_memtime s[62:63]\n\t" \
   "s_waitcnt lgkmcnt(0)\n\t" "s_sub_u32 s74, s62, s57\n\t" "s_mul_i32 s74, s74, s58\n\t" "s_lshr_b32 s74, s74, 12\n\t" "s_add_u32 s74, s74, s56\n\t" \
   "s_lshl_b32 s60, s60, 3\n\t" "s_sub_u32 s75, s74, s60\n\t" "s_cmp_lt_u32 s75, 32\n\t" "s_cbranch_scc1 22f\n\t" "s_cmp_lt_i32 s75, 0\n\t" \
   "s_cselect_b32 s61, 0, 31\n\t" "s_add_u32 s74, s60, s61\n\t" "s_sub_i32 s75, s61, s75\n\t" "s_add_i32 s75, s75, 1\n\t" "s_ashr_i32 s75, s75, 1\n\t" \
   "s_min_i32 s75, s75, 8\n\t" "s_max_i32 s75, s75, -8\n\t" "s_mov_b32 s56, s74\n\t" "s_mov_b32 s57, s62\n\t" "s_add_u32 s58, s58, s75\n\t" "22:\n\t" \
   "s_sub_i32 s74, s48, s74\n\t" "s_cmp_le_i32 s74, 150\n\t" "s_cbranch_scc1 23f\n\t" "s_cmp_lt_i32 s74, 260\n\t" "s_cbranch_scc1 21b\n\t" \
   "s_sleep 2\n\t" "s_cmp_lt_i32 s74, 32000\n\t" "s_cbranch_scc1 21b\n\t" "s_mov_b32 s74, 0\n\t" "23:\n\t" "s_sub_i32 s74, s74, 56\n\t" \
   "s_max_i32 s74, s74, 0\n\t" "s_mul_i32 s74, s74, 23\n\t" "s_lshr_b32 s74, s74, 5\n\t" "s_and_b32 s74, s74, -4\n\t" "s_getpc_b64 s[60:61]\n\t" \
   "24:\n\t" "s_sub_u32 s74, 25f-24b, s74\n\t" "s_add_u32 s60, s60, s74\n\t" "s_addc_u32 s61, s61, 0\n\t" "s_setpc_b64 s[60:61]\n\t" "s_nop 3\n\t" \
   "s_nop 3\n\t" "s_nop 3\n\t" "s_nop 3\n\t" "s_nop 3\n\t" "s_nop 3\n\t" "s_nop 3\n\t" "s_nop 3\n\t" "s_nop 3\n\t" "s_nop 3\n\t" "s_nop 3\n\t" \
   "s_nop 3\n\t" "s_nop 3\n\t" "s_nop 3\n\t" "s_nop 3\n\t" "s_nop 3\n\t" "s_nop 3\n\t" "s_nop 3\n\t" "s_nop 3\n\t" "s_nop 3\n\t" "s_nop 3\n\t" \
   "s_nop 3\n\t" "s_nop 3\n\t" "s_nop 3\n\t" "25:\n\t" "buffer_load_dwordx4 v[32:35], v16, %[rs], s50 offen offset:0 sc1\n\t" \
   "buffer_load_dwordx4 v[36:39], v16, %[rs], s50 offen offset:1024 sc1\n\t" "buffer_load_dwordx4 v[40:43], v16, %[rs], s50 offen offset:2048 sc1\n\t" \
   "buffer_load_dwordx4 v[44:47], v16, %[rs], s50 offen offset:3072 sc1\n\t" "buffer_load_dwordx4 v[48:51], v16, %[rs], s51 offen offset:0 sc1\n\t" \
   "buffer_load_dwordx4 v[52:55], v16, %[rs], s51 offen offset:1024 sc1\n\t" "buffer_load_dwordx4 v[56:59], v16, %[rs], s51 offen offset:2048 sc1\n\t" \
   "buffer_load_dwordx4 v[60:63], v16, %[rs], s51 offen offset:3072 sc1\n\t" "s_nop 3\n\t" "s_nop 3\n\t" "s_nop 3\n\t" "s_nop 3\n\t" "s_nop 3\n\t" \
   "s_nop 3\n\t" "s_nop 3\n\t" "s_nop 3\n\t" "s_nop 3\n\t" "s_nop 3\n\t" "s_nop 3\n\t" "s_nop 3\n\t" "s_nop 3\n\t" "s_nop 3\n\t" "s_nop 3\n\t" \
   "s_nop 3\n\t" RSTAMP(6) "buffer_load_dwordx4 v[64:67], v224, %[rs], s64 offen offset:0 sc1\n\t" \
   "buffer_load_dwordx4 v[68:71], v224, %[rs], s64 offen offset:1024 sc1\n\t" "buffer_load_dwordx4 v[72:75], v224, %[rs], s65 offen offset:0 sc1\n\t" \
   "buffer_load_dwordx4 v[76:79], v224, %[rs], s65 offen offset:1024 sc1\n\t" "buffer_load_dwordx4 v[80:83], v224, %[rs], s66 offen offset:0 sc1\n\t" \
   "buffer_load_dwordx4 v[84:87], v224, %[rs], s66 offen offset:1024 sc1\n\t" "buffer_load_dwordx4 v[88:91], v224, %[rs], s67 offen offset:0 sc1\n\t" \
   "buffer_load_dwordx4 v[92:95], v224, %[rs], s67 offen offset:1024 sc1\n\t" "buffer_load_dwordx4 v[96:99], v224, %[rs], s68 offen offset:0 sc1\n\t" \
   "buffer_load_dwordx4 v[100:103], v224, %[rs], s68 offen offset:1024 sc1\n\t" \
   "buffer_load_dwordx4 v[104:107], v224, %[rs], s69 offen offset:0 sc1\n\t" \
   "buffer_load_dwordx4 v[108:111], v224, %[rs], s69 offen offset:1024 sc1\n\t" \
   "buffer_load_dwordx4 v[112:115], v224, %[rs], s70 offen offset:0 sc1\n\t" \
   "buffer_load_dwordx4 v[116:119], v224, %[rs], s70 offen offset:1024 sc1\n\t" \
   "buffer_load_dwordx4 v[120:123], v224, %[rs], s71 offen offset:0 sc1\n\t" \
   "buffer_load_dwordx4 v[124:127], v224, %[rs], s71 offen offset:1024 sc1\n\t" \
   "buffer_load_dwordx2 v[128:129], v225, %[rs], s72 offen offset:0 sc1\n\t" \
   "buffer_load_dwordx2 v[130:131], v225, %[rs], s72 offen offset:128 sc1\n\t" \
   "buffer_load_dwordx2 v[132:133], v225, %[rs], s72 offen offset:1024 sc1\n\t" \
   "buffer_load_dwordx2 v[134:135], v225, %[rs], s72 offen offset:1152 sc1\n\t" \
   "buffer_load_dwordx2 v[136:137], v225, %[rs], s73 offen offset:0 sc1\n\t" \
   "buffer_load_dwordx2 v[138:139], v225, %[rs], s73 offen offset:128 sc1\n\t" \
   "buffer_load_dwordx2 v[140:141], v225, %[rs], s73 offen offset:1024 sc1\n\t" \
   "buffer_load_dwordx2 v[142:143], v225, %[rs], s73 offen offset:1152 sc1\n\t" YSTAMP(24) "s_waitcnt vmcnt(23)\n\t" RSTAMP(1) \
   "v_smfmac_f32_16x16x64_bf16 v[28:31], v[64:67], a[0:7], v22\n\t" "s_waitcnt vmcnt(22)\n\t" \
   "v_smfmac_f32_16x16x64_bf16 v[28:31], v[68:71], a[8:15], v22\n\t" "s_waitcnt vmcnt(21)\n\t" \
   "v_smfmac_f32_16x16x64_bf16 v[28:31], v[72:75], a[16:23], v22\n\t" "s_waitcnt vmcnt(20)\n\t" \
   "v_smfmac_f32_16x16x64_bf16 v[28:31], v[76:79], a[24:31], v22\n\t" "s_waitcnt vmcnt(19)\n\t" \
   "v_smfmac_f32_16x16x64_bf16 v[28:31], v[80:83], a[32:39], v22\n\t" "s_waitcnt vmcnt(18)\n\t" \
   "v_smfmac_f32_16x16x64_bf16 v[28:31], v[84:87], a[40:47], v22\n\t" "s_waitcnt vmcnt(17)\n\t" \
   "v_smfmac_f32_16x16x64_bf16 v[28:31], v[88:91], a[48:55], v22\n\t" "s_waitcnt vmcnt(16)\n\t" \
   "v_smfmac_f32_16x16x64_bf16 v[28:31], v[92:95], a[56:63], v22\n\t" "s_waitcnt vmcnt(15)\n\t" \
   "v_smfmac_f32_16x16x64_bf16 v[28:31], v[96:99], a[64:71], v22\n\t" "s_waitcnt vmcnt(14)\n\t" \
   "v_smfmac_f32_16x16x64_bf16 v[28:31], v[100:103], a[72:79], v22\n\t" "s_waitcnt vmcnt(13)\n\t" \
   "v_smfmac_f32_16x16x64_bf16 v[28:31], v[104:107], a[80:87], v22\n\t" "s_waitcnt vmcnt(12)\n\t" \
   "v_smfmac_f32_16x16x64_bf16 v[28:31], v[108:111], a[88:95], v22\n\t" "s_waitcnt vmcnt(11)\n\t" \
   "v_smfmac_f32_16x16x64_bf16 v[28:31], v[112:115], a[96:103], v22\n\t" "s_waitcnt vmcnt(10)\n\t" \
   "v_smfmac_f32_16x16x64_bf16 v[28:31], v[116:119], a[104:111], v22\n\t" "s_waitcnt vmcnt(9)\n\t" \
   "v_smfmac_f32_16x16x64_bf16 v[28:31], v[120:123], a[112:119], v22\n\t" "s_waitcnt vmcnt(8)\n\t" \
   "v_smfmac_f32_16x16x64_bf16 v[28:31], v[124:127], a[120:127], v22\n\t" "s_waitcnt vmcnt(6)\n\t" \
   "v_smfmac_f32_16x16x64_bf16 v[28:31], v[128:131], v[32:39], v22\n\t" "s_waitcnt vmcnt(4)\n\t" \
   "v_smfmac_f32_16x16x64_bf16 v[28:31], v[132:135], v[40:47], v22\n\t" "s_waitcnt vmcnt(2)\n\t" \
   "v_smfmac_f32_16x16x64_bf16 v[28:31], v[136:139], v[48:55], v22\n\t" "s_waitcnt vmcnt(0)\n\t" \
   "v_smfmac_f32_16x16x64_bf16 v[28:31], v[140:143], v[56:63], v22\n\t" "s_nop 7\n\t" "v_add_f32 v28, v28, v29\n\t" "v_add_f32 v30, v30, v31\n\t" \
   "v_cvt_pk_bf16_f32 v24, v28, v30\n\t" "v_cmp_u_f32 vcc, v28, v30\n\t" "s_nop 0\n\t" \
   "v_mov_b32_dpp v25, v24 quad_perm:[1,0,3,2] row_mask:0xf bank_mask:0xf\n\t" "s_cbranch_vccnz 8f\n\t" "2:\n\t" RSTAMP(2) \
   "v_perm_b32 v26, v25, v24, v18\n\t" "s_nop 1\n\t" "v_min_u32_dpp v24, v26, v13 quad_perm:[0,1,0,1] row_mask:0xf bank_mask:0xf\n\t" \
   "v_mov_b32_dpp v25, v26 quad_perm:[2,3,2,3] row_mask:0xf bank_mask:0xf\n\t" "buffer_store_dwordx2 v[24:25], v14, %[rs], s41 offen sc1\n\t" \
   "buffer_store_dwordx2 v[24:25], v14, %[rs], s53 offen sc1\n\t" "s_memtime s[62:63]\n\t" "s_memrealtime s[60:61]\n\t" \
   "buffer_store_dwordx2 v[24:25], v15, %[rs], s41 offen sc1\n\t" "buffer_store_dwordx2 v[24:25], v15, %[rs], s53 offen sc1\n\t" RSTAMP(3) \
   "s_waitcnt lgkmcnt(0)\n\t" "s_sub_u32 s74, s62, s57\n\t" "s_mul_i32 s74, s74, s58\n\t" "s_lshr_b32 s74, s74, 12\n\t" "s_add_u32 s74, s74, s56\n\t" \
   "s_lshl_b32 s60, s60, 3\n\t" "s_sub_u32 s75, s74, s60\n\t" "s_cmp_lt_u32 s75, 32\n\t" "s_cbranch_scc1 38f\n\t" "s_cmp_lt_i32 s75, 0\n\t" \
   "s_cselect_b32 s61, 0, 31\n\t" "s_add_u32 s74, s60, s61\n\t" "s_sub_i32 s75, s61, s75\n\t" "s_add_i32 s75, s75, 1\n\t" "s_ashr_i32 s75, s75, 1\n\t" \
   "s_min_i32 s75, s75, 8\n\t" "s_max_i32 s75, s75, -8\n\t" "s_mov_b32 s56, s74\n\t" "s_mov_b32 s57, s62\n\t" "s_add_u32 s58, s58, s75\n\t" "38:\n\t" \
   "s_min_u32 s75, s46, 1\n\t" "s_and_b32 s74, s74, -4\n\t" "s_or_b32 s74, s74, s75\n\t" "v_mov_b32 v26, s74\n\t" \
   "buffer_store_dword v26, v21, %[rs], s41 offen sc1\n\t" FINTRACE "v_xor_b32 v23, 32, v23\n\t" "s_cmp_lt_u32 s40, 63\n\t" "s_cbranch_scc1 30b\n\t" \
   "s_branch 90f\n\t" "18:\n\t" "s_sleep 2\n\t" "s_branch 19b\n\t" "8:\n\t" "s_mov_b32 s74, 0\n\t" "87:\n\t" "v_max3_u32 v12, v32, v34, v36\n\t" \
   "v_max3_u32 v12, v12, v38, v40\n\t" "v_max3_u32 v12, v12, v42, v44\n\t" "v_max3_u32 v12, v12, v46, v48\n\t" "v_max3_u32 v12, v12, v50, v52\n\t" \
   "v_max3_u32 v12, v12, v54, v56\n\t" "v_max3_u32 v12, v12, v58, v60\n\t" "v_max_u32 v12, v12, v62\n\t" "v_cmp_eq_u32 vcc, -1, v12\n\t" \
   "s_cmp_gt_u32 s74, 0x10000\n\t" "s_cbranch_scc1 86f\n\t" "s_cbranch_vccz 86f\n\t" "s_cmp_eq_u32 s74, 0\n\t" "s_cselect_b32 s75, 0x100, 0\n\t" \
   "s_cbranch_scc1 88f\n\t" "s_sleep 3\n\t" "88:\n\t" "buffer_load_dwordx4 v[32:35], v16, %[rs], s50 offen offset:0 sc1\n\t" \
   "buffer_load_dwordx4 v[36:39], v16, %[rs], s50 offen offset:1024 sc1\n\t" "buffer_load_dwordx4 v[40:43], v16, %[rs], s50 offen offset:2048 sc1\n\t" \
   "buffer_load_dwordx4 v[44:47], v16, %[rs], s50 offen offset:3072 sc1\n\t" "buffer_load_dwordx4 v[48:51], v16, %[rs], s51 offen offset:0 sc1\n\t" \
   "buffer_load_dwordx4 v[52:55], v16, %[rs], s51 offen offset:1024 sc1\n\t" "buffer_load_dwordx4 v[56:59], v16, %[rs], s51 offen offset:2048 sc1\n\t" \
   "buffer_load_dwordx4 v[60:63], v16, %[rs], s51 offen offset:3072 sc1\n\t" "s_add_u32 s76, s76, s75\n\t" "s_add_u32 s74, s74, 1\n\t" \
   "s_waitcnt vmcnt(0)\n\t" "s_branch 87b\n\t" "86:\n\t" "v_mov_b32 v27, 0\n\t" "v_mov_b32 v28, 0\n\t" "v_mov_b32 v29, 0\n\t" "v_mov_b32 v30, 0\n\t" \
   "v_mov_b32 v31, 0\n\t" "v_max3_u32 v27, v27, v64, v66\n\t" "v_max3_u32 v27, v27, v68, v70\n\t" "v_max3_u32 v27, v27, v72, v74\n\t" \
   "v_max3_u32 v27, v27, v76, v78\n\t" "v_max3_u32 v27, v27, v80, v82\n\t" "v_max3_u32 v27, v27, v84, v86\n\t" "v_max3_u32 v27, v27, v88, v90\n\t" \
   "v_max3_u32 v27, v27, v92, v94\n\t" "v_max3_u32 v27, v27, v96, v98\n\t" "v_max3_u32 v27, v27, v100, v102\n\t" "v_max3_u32 v27, v27, v104, v106\n\t" \
   "v_max3_u32 v27, v27, v108, v110\n\t" "v_max3_u32 v27, v27, v112, v114\n\t" "v_max3_u32 v27, v27, v116, v118\n\t" \
   "v_max3_u32 v27, v27, v120, v122\n\t" "v_max3_u32 v27, v27, v124, v126\n\t" "v_max3_u32 v27, v27, v128, v130\n\t" \
   "v_max3_u32 v27, v27, v132, v134\n\t" "v_max3_u32 v27, v27, v136, v138\n\t" "v_max3_u32 v27, v27, v140, v142\n\t" "v_cmp_eq_u32 vcc, -1, v27\n\t" \
   "s_nop 0\n\t" "s_cbranch_vccnz 85f\n\t" "v_smfmac_f32_16x16x64_bf16 v[28:31], v[64:67], a[0:7], v22\n\t" \
   "v_smfmac_f32_16x16x64_bf16 v[28:31], v[68:71], a[8:15], v22\n\t" "v_smfmac_f32_16x16x64_bf16 v[28:31], v[72:75], a[16:23], v22\n\t" \
   "v_smfmac_f32_16x16x64_bf16 v[28:31], v[76:79], a[24:31], v22\n\t" "v_smfmac_f32_16x16x64_bf16 v[28:31], v[80:83], a[32:39], v22\n\t" \
   "v_smfmac_f32_16x16x64_bf16 v[28:31], v[84:87], a[40:47], v22\n\t" "v_smfmac_f32_16x16x64_bf16 v[28:31], v[88:91], a[48:55], v22\n\t" \
   "v_smfmac_f32_16x16x64_bf16 v[28:31], v[92:95], a[56:63], v22\n\t" "v_smfmac_f32_16x16x64_bf16 v[28:31], v[96:99], a[64:71], v22\n\t" \
   "v_smfmac_f32_16x16x64_bf16 v[28:31], v[100:103], a[72:79], v22\n\t" "v_smfmac_f32_16x16x64_bf16 v[28:31], v[104:107], a[80:87], v22\n\t" \
   "v_smfmac_f32_16x16x64_bf16 v[28:31], v[108:111], a[88:95], v22\n\t" "v_smfmac_f32_16x16x64_bf16 v[28:31], v[112:115], a[96:103], v22\n\t" \
   "v_smfmac_f32_16x16x64_bf16 v[28:31], v[116:119], a[104:111], v22\n\t" "v_smfmac_f32_16x16x64_bf16 v[28:31], v[120:123], a[112:119], v22\n\t" \
   "v_smfmac_f32_16x16x64_bf16 v[28:31], v[124:127], a[120:127], v22\n\t" "v_smfmac_f32_16x16x64_bf16 v[28:31], v[128:131], v[32:39], v22\n\t" \
   "v_smfmac_f32_16x16x64_bf16 v[28:31], v[132:135], v[40:47], v22\n\t" "v_smfmac_f32_16x16x64_bf16 v[28:31], v[136:139], v[48:55], v22\n\t" \
   "v_smfmac_f32_16x16x64_bf16 v[28:31], v[140:143], v[56:63], v22\n\t" "84:\n\t" "v_cmp_eq_u32 vcc, -1, v27\n\t" "s_cmp_gt_u32 s46, 0x10000\n\t" \
   "s_cbranch_scc1 83f\n\t" "s_cbranch_vccz 83f\n\t" "85:\n\t" "s_add_u32 s46, s46, 1\n\t" "s_cmp_gt_u32 s46, 2\n\t" "s_cbranch_scc1 81f\n\t" \
   "s_lshl_b32 s74, s46, 3\n\t" "s_lshl_b32 s74, 1, s74\n\t" "s_add_u32 s77, s77, s74\n\t" "81:\n\t" "s_min_u32 s74, s46, 7\n\t" \
   "s_lshr_b32 s64, s74, 1\n\t" "s_add_u32 s64, s64, %[xs]\n\t" "s_and_b32 s64, s64, 3\n\t" "s_lshl_b32 s64, s64, 11\n\t" "s_and_b32 s75, s74, 1\n\t" \
   "s_lshl_b32 s75, s75, 14\n\t" "s_xor_b32 s75, s75, s47\n\t" "s_xor_b32 s75, s75, %[lad0]\n\t" "s_and_b32 s75, s75, 0x4000\n\t" \
   "s_add_u32 s64, s64, s75\n\t" "s_add_u32 s64, s64, s41\n\t" "s_add_u32 s65, s64, 32768\n\t" "s_add_u32 s66, s64, 65536\n\t" \
   "s_add_u32 s67, s64, 98304\n\t" "s_add_u32 s68, s64, 131072\n\t" "s_add_u32 s69, s64, 163840\n\t" "s_add_u32 s70, s64, 196608\n\t" \
   "s_add_u32 s71, s64, 229376\n\t" "s_add_u32 s72, s64, 262144\n\t" "s_add_u32 s73, s64, 294912\n\t" "v_mov_b32 v28, 0\n\t" "v_mov_b32 v29, 0\n\t" \
   "v_mov_b32 v30, 0\n\t" "v_mov_b32 v31, 0\n\t" "v_mov_b32 v27, 0\n\t" "s_cmp_lt_u32 s46, 7\n\t" "s_cbranch_scc1 82f\n\t" "s_sleep 3\n\t" "82:\n\t" \
   RSTAMP(6) "buffer_load_dwordx4 v[64:67], v16, %[rs], s64 offen offset:0 sc1\n\t" \
   "buffer_load_dwordx4 v[68:71], v16, %[rs], s64 offen offset:1024 sc1\n\t" "buffer_load_dwordx4 v[72:75], v16, %[rs], s65 offen offset:0 sc1\n\t" \
   "buffer_load_dwordx4 v[76:79], v16, %[rs], s65 offen offset:1024 sc1\n\t" "buffer_load_dwordx4 v[80:83], v16, %[rs], s66 offen offset:0 sc1\n\t" \
   "buffer_load_dwordx4 v[84:87], v16, %[rs], s66 offen offset:1024 sc1\n\t" "buffer_load_dwordx4 v[88:91], v16, %[rs], s67 offen offset:0 sc1\n\t" \
   "buffer_load_dwordx4 v[92:95], v16, %[rs], s67 offen offset:1024 sc1\n\t" "buffer_load_dwordx4 v[96:99], v16, %[rs], s68 offen offset:0 sc1\n\t" \
   "buffer_load_dwordx4 v[100:103], v16, %[rs], s68 offen offset:1024 sc1\n\t" "buffer_load_dwordx4 v[104:107], v16, %[rs], s69 offen offset:0 sc1\n\t" \
   "buffer_load_dwordx4 v[108:111], v16, %[rs], s69 offen offset:1024 sc1\n\t" "buffer_load_dwordx4 v[112:115], v16, %[rs], s70 offen offset:0 sc1\n\t" \
   "buffer_load_dwordx4 v[116:119], v16, %[rs], s70 offen offset:1024 sc1\n\t" "buffer_load_dwordx4 v[120:123], v16, %[rs], s71 offen offset:0 sc1\n\t" \
   "buffer_load_dwordx4 v[124:127], v16, %[rs], s71 offen offset:1024 sc1\n\t" "buffer_load_dwordx2 v[128:129], v17, %[rs], s72 offen offset:0 sc1\n\t" \
   "buffer_load_dwordx2 v[130:131], v17, %[rs], s72 offen offset:128 sc1\n\t" \
   "buffer_load_dwordx2 v[132:133], v17, %[rs], s72 offen offset:1024 sc1\n\t" \
   "buffer_load_dwordx2 v[134:135], v17, %[rs], s72 offen offset:1152 sc1\n\t" "buffer_load_dwordx2 v[136:137], v17, %[rs], s73 offen offset:0 sc1\n\t" \
   "buffer_load_dwordx2 v[138:139], v17, %[rs], s73 offen offset:128 sc1\n\t" \
   "buffer_load_dwordx2 v[140:141], v17, %[rs], s73 offen offset:1024 sc1\n\t" \
   "buffer_load_dwordx2 v[142:143], v17, %[rs], s73 offen offset:1152 sc1\n\t" "s_waitcnt vmcnt(23)\n\t" \
   "v_smfmac_f32_16x16x64_bf16 v[28:31], v[64:67], a[0:7], v22\n\t" "v_max3_u32 v27, v27, v64, v66\n\t" "s_waitcnt vmcnt(22)\n\t" \
   "v_smfmac_f32_16x16x64_bf16 v[28:31], v[68:71], a[8:15], v22\n\t" "v_max3_u32 v27, v27, v68, v70\n\t" "s_waitcnt vmcnt(21)\n\t" \
   "v_smfmac_f32_16x16x64_bf16 v[28:31], v[72:75], a[16:23], v22\n\t" "v_max3_u32 v27, v27, v72, v74\n\t" "s_waitcnt vmcnt(20)\n\t" \
   "v_smfmac_f32_16x16x64_bf16 v[28:31], v[76:79], a[24:31], v22\n\t" "v_max3_u32 v27, v27, v76, v78\n\t" "s_waitcnt vmcnt(19)\n\t" \
   "v_smfmac_f32_16x16x64_bf16 v[28:31], v[80:83], a[32:39], v22\n\t" "v_max3_u32 v27, v27, v80, v82\n\t" "s_waitcnt vmcnt(18)\n\t" \
   "v_smfmac_f32_16x16x64_bf16 v[28:31], v[84:87], a[40:47], v22\n\t" "v_max3_u32 v27, v27, v84, v86\n\t" "s_waitcnt vmcnt(17)\n\t" \
   "v_smfmac_f32_16x16x64_bf16 v[28:31], v[88:91], a[48:55], v22\n\t" "v_max3_u32 v27, v27, v88, v90\n\t" "s_waitcnt vmcnt(16)\n\t" \
   "v_smfmac_f32_16x16x64_bf16 v[28:31], v[92:95], a[56:63], v22\n\t" "v_max3_u32 v27, v27, v92, v94\n\t" "s_waitcnt vmcnt(15)\n\t" \
   "v_smfmac_f32_16x16x64_bf16 v[28:31], v[96:99], a[64:71], v22\n\t" "v_max3_u32 v27, v27, v96, v98\n\t" "s_waitcnt vmcnt(14)\n\t" \
   "v_smfmac_f32_16x16x64_bf16 v[28:31], v[100:103], a[72:79], v22\n\t" "v_max3_u32 v27, v27, v100, v102\n\t" "s_waitcnt vmcnt(13)\n\t" \
   "v_smfmac_f32_16x16x64_bf16 v[28:31], v[104:107], a[80:87], v22\n\t" "v_max3_u32 v27, v27, v104, v106\n\t" "s_waitcnt vmcnt(12)\n\t" \
   "v_smfmac_f32_16x16x64_bf16 v[28:31], v[108:111], a[88:95], v22\n\t" "v_max3_u32 v27, v27, v108, v110\n\t" "s_waitcnt vmcnt(11)\n\t" \
   "v_smfmac_f32_16x16x64_bf16 v[28:31], v[112:115], a[96:103], v22\n\t" "v_max3_u32 v27, v27, v112, v114\n\t" "s_waitcnt vmcnt(10)\n\t" \
   "v_smfmac_f32_16x16x64_bf16 v[28:31], v[116:119], a[104:111], v22\n\t" "v_max3_u32 v27, v27, v116, v118\n\t" "s_waitcnt vmcnt(9)\n\t" \
   "v_smfmac_f32_16x16x64_bf16 v[28:31], v[120:123], a[112:119], v22\n\t" "v_max3_u32 v27, v27, v120, v122\n\t" "s_waitcnt vmcnt(8)\n\t" \
   "v_smfmac_f32_16x16x64_bf16 v[28:31], v[124:127], a[120:127], v22\n\t" "v_max3_u32 v27, v27, v124, v126\n\t" "s_waitcnt vmcnt(6)\n\t" \
   "v_smfmac_f32_16x16x64_bf16 v[28:31], v[128:131], v[32:39], v22\n\t" "v_max3_u32 v27, v27, v128, v130\n\t" "s_waitcnt vmcnt(4)\n\t" \
   "v_smfmac_f32_16x16x64_bf16 v[28:31], v[132:135], v[40:47], v22\n\t" "v_max3_u32 v27, v27, v132, v134\n\t" "s_waitcnt vmcnt(2)\n\t" \
   "v_smfmac_f32_16x16x64_bf16 v[28:31], v[136:139], v[48:55], v22\n\t" "v_max3_u32 v27, v27, v136, v138\n\t" "s_waitcnt vmcnt(0)\n\t" \
   "v_smfmac_f32_16x16x64_bf16 v[28:31], v[140:143], v[56:63], v22\n\t" "v_max3_u32 v27, v27, v140, v142\n\t" "s_branch 84b\n\t" "83:\n\t" \
   "s_or_b32 s46, s46, 0x10000000\n\t" "s_nop 4\n\t" "v_add_f32 v28, v28, v29\n\t" "v_add_f32 v30, v30, v31\n\t" "v_cvt_pk_bf16_f32 v24, v28, v30\n\t" \
   "s_nop 1\n\t" "v_mov_b32_dpp v25, v24 quad_perm:[1,0,3,2] row_mask:0xf bank_mask:0xf\n\t" "s_branch 2b\n\t" "95:\n\t" "s_mov_b32 s74, 0\n\t" \
   "97:\n\t" "buffer_load_dwordx4 v[8:11], v19, %[rs], s52 offen sc1\n\t" "s_add_u32 s74, s74, 1\n\t" "s_waitcnt vmcnt(0)\n\t" \
   "v_max3_u32 v15, v8, v9, v10\n\t" "v_max_u32 v15, v15, v11\n\t" "v_cmp_eq_u32 vcc, -1, v15\n\t" "s_cmp_gt_u32 s74, 0x10000\n\t" \
   "s_cbranch_scc1 96b\n\t" "s_cbranch_vccz 96b\n\t" "s_sleep 2\n\t" "s_branch 97b\n\t" "90:\n\t" "s_waitcnt vmcnt(0) lgkmcnt(0)"
#define FIN_CLOB "v8", "v9", "v10", "v11", "v12", "v13", "v14", "v15", "v24", "v25", "v26", "v27", "v28", "v29", "v30", "v31", "v32", "v33", "v34", "v35", "v36", "v37", "v38", "v39", "v40", "v41", "v42", "v43", "v44", "v45", "v46", "v47", "v48", "v49", "v50", "v51", "v52", "v53", "v54", "v55", "v56", "v57", "v58", "v59", "v60", "v61", "v62", "v63", "v64", "v65", "v66", "v67", "v68", "v69", "v70", "v71", "v72", "v73", "v74", "v75", "v76", "v77", "v78", "v79", "v80", "v81", "v82", "v83", "v84", "v85", "v86", "v87", "v88", "v89", "v90", "v91", "v92", "v93", "v94", "v95", "v96", "v97", "v98", "v99", "v100", "v101", "v102", "v103", "v104", "v105", "v106", "v107", "v108", "v109", "v110", "v111", "v112", "v113", "v114", "v115", "v116", "v117", "v118", "v119", "v120", "v121", "v122", "v123", "v124", "v125", "v126", "v127", "v128", "v129", "v130", "v131", "v132", "v133", "v134", "v135", "v136", "v137", "v138", "v139", "v140", "v141", "v142", "v143", "v144", "v145", "v146", "v147", "v148", "v149", "v150", "v151", "v152", "v153", "v154", "v155", "v156", "v157", "v158", "v159", "v160", "v161", "v162", "v163", "v164", "v165", "v166", "v167", "v168", "v169", "v170", "v171", "v172", "v173", "v174", "v175", "v176", "v177", "v178", "v179", "v180", "v181", "v182", "v183", "v184", "v185", "v186", "v187", "v188", "v189", "v190", "v191", "v192", "v193", "v194", "v195", "v196", "v197", "v198", "v199", "v200", "v201", "v202", "v203", "v204", "v205", "v206", "v207", "v208", "v209", "v210", "v211", "v212", "v213", "v214", "v215", "v216", "v217", "v218", "v219", "v220", "v221", "v222", "v223", "v224", "v225", "v226", "v227", "s40", "s41", "s42", "s43", "s44", "s45", "s46", "s47", "s48", "s49", "s50", "s51", "s52", "s53", "s60", "s61", "s62", "s63", "s64", "s65", "s66", "s67", "s68", "s69", "s70", "s71", "s72", "s73", "s74", "s75", "a0", "a1", "a2", "a3", "a4", "a5", "a6", "a7", "a8", "a9", "a10", "a11", "a12", "a13", "a14", "a15", "a16", "a17", "a18", "a19", "a20", "a21", "a22", "a23", "a24", "a25", "a26", "a27", "a28", "a29", "a30", "a31", "a32", "a33", "a34", "a35", "a36", "a37", "a38", "a39", "a40", "a41", "a42", "a43", "a44", "a45", "a46", "a47", "a48", "a49", "a50", "a51", "a52", "a53", "a54", "a55", "a56", "a57", "a58", "a59", "a60", "a61", "a62", "a63", "a64", "a65", "a66", "a67", "a68", "a69", "a70", "a71", "a72", "a73", "a74", "a75", "a76", "a77", "a78", "a79", "a80", "a81", "a82", "a83", "a84", "a85", "a86", "a87", "a88", "a89", "a90", "a91", "a92", "a93", "a94", "a95", "a96", "a97", "a98", "a99", "a100", "a101", "a102", "a103", "a104", "a105", "a106", "a107", "a108", "a109", "a110", "a111", "a112", "a113", "a114", "a115", "a116", "a117", "a118", "a119", "a120", "a121", "a122", "a123", "a124", "a125", "a126", "a127"
__device__ __forceinline__ void put4(v32u& d, int i, v4u v) { d[4 * i] = v.x; d[4 * i + 1] = v.y; d[4 * i + 2] = v.z; d[4 * i + 3] = v.w; }
#define LDP_ASM \
   "buffer_load_dword v26, v113, %[rw], 0 offen sc1\n\t" "s_mov_b32 s41, 0\n\t" "buffer_load_dwordx4 a[0:3], v16, %[ra], s41 offen\n\t" \
   "s_mov_b32 s41, 131072\n\t" "buffer_load_dwordx4 a[64:67], v16, %[ra], s41 offen\n\t" "s_mov_b32 s41, 524288\n\t" \
   "buffer_load_dwordx4 a[8:11], v16, %[ra], s41 offen\n\t" "s_mov_b32 s41, 655360\n\t" "buffer_load_dwordx4 a[72:75], v16, %[ra], s41 offen\n\t" \
   "s_mov_b32 s41, 1048576\n\t" "buffer_load_dwordx4 a[16:19], v16, %[ra], s41 offen\n\t" "s_mov_b32 s41, 1179648\n\t" \
   "buffer_load_dwordx4 a[80:83], v16, %[ra], s41 offen\n\t" "s_mov_b32 s41, 1572864\n\t" "buffer_load_dwordx4 a[24:27], v16, %[ra], s41 offen\n\t" \
   "s_mov_b32 s41, 1703936\n\t" "buffer_load_dwordx4 a[88:91], v16, %[ra], s41 offen\n\t" "s_mov_b32 s41, 2097152\n\t" \
   "buffer_load_dwordx4 a[32:35], v16, %[ra], s41 offen\n\t" "s_mov_b32 s41, 2228224\n\t" "buffer_load_dwordx4 a[96:99], v16, %[ra], s41 offen\n\t" \
   "s_mov_b32 s41, 2621440\n\t" "buffer_load_dwordx4 a[40:43], v16, %[ra], s41 offen\n\t" "s_mov_b32 s41, 2752512\n\t" \
   "buffer_load_dwordx4 a[104:107], v16, %[ra], s41 offen\n\t" "s_mov_b32 s41, 3145728\n\t" "buffer_load_dwordx4 a[48:51], v16, %[ra], s41 offen\n\t" \
   "s_mov_b32 s41, 3276800\n\t" "buffer_load_dwordx4 a[112:115], v16, %[ra], s41 offen\n\t" "s_mov_b32 s41, 3670016\n\t" \
   "buffer_load_dwordx4 a[56:59], v16, %[ra], s41 offen\n\t" "s_mov_b32 s41, 3801088\n\t" "buffer_load_dwordx4 a[120:123], v16, %[ra], s41 offen\n\t" \
   "s_waitcnt vmcnt(16)\n\t" "v_add_u32 v26, 1, v26\n\t" "buffer_store_dword v26, v114, %[rw], 0 offen sc1\n\t" "ds_write_b32 v115, v26\n\t" LDSTAMP(0) \
   "s_waitcnt vmcnt(15)\n\t" LDSTAMP(1) "ds_write_b128 v20, a[0:3] offset:0\n\t" "s_mov_b32 s41, 4194304\n\t" \
   "buffer_load_dwordx4 v[192:195], v16, %[ra], s41 offen\n\t" "s_waitcnt vmcnt(15)\n\t" "ds_write_b128 v20, a[64:67] offset:4096\n\t" \
   "s_mov_b32 s41, 4325376\n\t" "buffer_load_dwordx4 v[208:211], v16, %[ra], s41 offen\n\t" "s_waitcnt vmcnt(15)\n\t" \
   "ds_write_b128 v20, a[8:11] offset:8192\n\t" "s_mov_b32 s41, 4718592\n\t" "buffer_load_dwordx4 v[200:203], v16, %[ra], s41 offen\n\t" \
   "s_waitcnt vmcnt(15)\n\t" "ds_write_b128 v20, a[72:75] offset:12288\n\t" "s_mov_b32 s41, 4849664\n\t" \
   "buffer_load_dwordx4 v[216:219], v16, %[ra], s41 offen\n\t" "s_waitcnt vmcnt(15)\n\t" "ds_write_b128 v20, a[16:19] offset:16384\n\t" \
   "s_mov_b32 s41, 0\n\t" "buffer_load_dwordx4 a[128:131], v16, %[rb], s41 offen\n\t" "s_waitcnt vmcnt(15)\n\t" \
   "ds_write_b128 v20, a[80:83] offset:20480\n\t" "s_mov_b32 s41, 131072\n\t" "buffer_load_dwordx4 a[192:195], v16, %[rb], s41 offen\n\t" \
   "s_waitcnt vmcnt(15)\n\t" "ds_write_b128 v20, a[24:27] offset:24576\n\t" "s_mov_b32 s41, 524288\n\t" \
   "buffer_load_dwordx4 a[136:139], v16, %[rb], s41 offen\n\t" "s_waitcnt vmcnt(15)\n\t" "ds_write_b128 v20, a[88:91] offset:28672\n\t" \
   "s_mov_b32 s41, 655360\n\t" "buffer_load_dwordx4 a[200:203], v16, %[rb], s41 offen\n\t" "s_waitcnt vmcnt(15)\n\t" \
   "ds_write_b128 v20, a[32:35] offset:32768\n\t" "s_mov_b32 s41, 1048576\n\t" "buffer_load_dwordx4 a[144:147], v16, %[rb], s41 offen\n\t" \
   "s_waitcnt vmcnt(15)\n\t" "ds_write_b128 v20, a[96:99] offset:36864\n\t" "s_mov_b32 s41, 1179648\n\t" \
   "buffer_load_dwordx4 a[208:211], v16, %[rb], s41 offen\n\t" "s_waitcnt vmcnt(15)\n\t" "ds_write_b128 v20, a[40:43] offset:40960\n\t" \
   "s_mov_b32 s41, 1572864\n\t" "buffer_load_dwordx4 a[152:155], v16, %[rb], s41 offen\n\t" "s_waitcnt vmcnt(15)\n\t" \
   "ds_write_b128 v20, a[104:107] offset:45056\n\t" "s_mov_b32 s41, 1703936\n\t" "buffer_load_dwordx4 a[216:219], v16, %[rb], s41 offen\n\t" \
   "s_waitcnt vmcnt(15)\n\t" "ds_write_b128 v20, a[48:51] offset:49152\n\t" "s_mov_b32 s41, 2097152\n\t" \
   "buffer_load_dwordx4 a[160:163], v16, %[rb], s41 offen\n\t" "s_waitcnt vmcnt(15)\n\t" "ds_write_b128 v20, a[112:115] offset:53248\n\t" \
   "s_mov_b32 s41, 2228224\n\t" "buffer_load_dwordx4 a[224:227], v16, %[rb], s41 offen\n\t" "s_waitcnt vmcnt(15)\n\t" \
   "ds_write_b128 v20, a[56:59] offset:57344\n\t" "s_mov_b32 s41, 2621440\n\t" "buffer_load_dwordx4 a[168:171], v16, %[rb], s41 offen\n\t" \
   "s_waitcnt vmcnt(15)\n\t" "ds_write_b128 v20, a[120:123] offset:61440\n\t" "s_mov_b32 s41, 2752512\n\t" \
   "buffer_load_dwordx4 a[232:235], v16, %[rb], s41 offen\n\t" "s_waitcnt vmcnt(15)\n\t" "ds_write_b128 v23, v[192:195] offset:0\n\t" \
   "s_mov_b32 s41, 3145728\n\t" "buffer_load_dwordx4 a[176:179], v16, %[rb], s41 offen\n\t" "s_waitcnt vmcnt(15)\n\t" \
   "ds_write_b128 v23, v[208:211] offset:4096\n\t" "s_mov_b32 s41, 3276800\n\t" "buffer_load_dwordx4 a[240:243], v16, %[rb], s41 offen\n\t" \
   "s_waitcnt vmcnt(15)\n\t" "ds_write_b128 v23, v[200:203] offset:8192\n\t" "s_mov_b32 s41, 3670016\n\t" \
   "buffer_load_dwordx4 a[184:187], v16, %[rb], s41 offen\n\t" "s_waitcnt vmcnt(15)\n\t" "ds_write_b128 v23, v[216:219] offset:12288\n\t" \
   "s_mov_b32 s41, 3801088\n\t" "buffer_load_dwordx4 a[248:251], v16, %[rb], s41 offen\n\t" "s_waitcnt lgkmcnt(0)\n\t" "s_barrier\n\t" LDSTAMP(2) \
   "s_lshl_b32 s40, %[p0], 13\n\t" "v_add_u32 v24, s40, v18\n\t" "v_add_u32 v25, s40, v19\n\t" "s_add_u32 s40, s40, 8192\n\t" \
   "s_cmp_eq_u32 s40, 81920\n\t" "s_cselect_b32 s40, 0, s40\n\t" "ds_read_b64_tr_b16 a[0:1], v24 offset:0\n\t" \
   "ds_read_b64_tr_b16 a[2:3], v24 offset:4096\n\t" "ds_read_b64_tr_b16 a[64:65], v25 offset:0\n\t" "ds_read_b64_tr_b16 a[66:67], v25 offset:4096\n\t" \
   "v_add_u32 v24, s40, v18\n\t" "v_add_u32 v25, s40, v19\n\t" "s_add_u32 s40, s40, 8192\n\t" "s_cmp_eq_u32 s40, 81920\n\t" \
   "s_cselect_b32 s40, 0, s40\n\t" "ds_read_b64_tr_b16 a[8:9], v24 offset:0\n\t" "ds_read_b64_tr_b16 a[10:11], v24 offset:4096\n\t" \
   "ds_read_b64_tr_b16 a[72:73], v25 offset:0\n\t" "ds_read_b64_tr_b16 a[74:75], v25 offset:4096\n\t" "v_add_u32 v24, s40, v18\n\t" \
   "v_add_u32 v25, s40, v19\n\t" "s_add_u32 s40, s40, 8192\n\t" "s_cmp_eq_u32 s40, 81920\n\t" "s_cselect_b32 s40, 0, s40\n\t" \
   "ds_read_b64_tr_b16 a[16:17], v24 offset:0\n\t" "ds_read_b64_tr_b16 a[18:19], v24 offset:4096\n\t" "ds_read_b64_tr_b16 a[80:81], v25 offset:0\n\t" \
   "ds_read_b64_tr_b16 a[82:83], v25 offset:4096\n\t" "v_add_u32 v24, s40, v18\n\t" "v_add_u32 v25, s40, v19\n\t" "s_add_u32 s40, s40, 8192\n\t" \
   "s_cmp_eq_u32 s40, 81920\n\t" "s_cselect_b32 s40, 0, s40\n\t" "ds_read_b64_tr_b16 a[24:25], v24 offset:0\n\t" \
   "ds_read_b64_tr_b16 a[26:27], v24 offset:4096\n\t" "ds_read_b64_tr_b16 a[88:89], v25 offset:0\n\t" \
   "ds_read_b64_tr_b16 a[90:91], v25 offset:4096\n\t" "v_add_u32 v24, s40, v18\n\t" "v_add_u32 v25, s40, v19\n\t" "s_add_u32 s40, s40, 8192\n\t" \
   "s_cmp_eq_u32 s40, 81920\n\t" "s_cselect_b32 s40, 0, s40\n\t" "ds_read_b64_tr_b16 a[32:33], v24 offset:0\n\t" \
   "ds_read_b64_tr_b16 a[34:35], v24 offset:4096\n\t" "ds_read_b64_tr_b16 a[96:97], v25 offset:0\n\t" \
   "ds_read_b64_tr_b16 a[98:99], v25 offset:4096\n\t" "v_add_u32 v24, s40, v18\n\t" "v_add_u32 v25, s40, v19\n\t" "s_add_u32 s40, s40, 8192\n\t" \
   "s_cmp_eq_u32 s40, 81920\n\t" "s_cselect_b32 s40, 0, s40\n\t" "ds_read_b64_tr_b16 a[40:41], v24 offset:0\n\t" \
   "ds_read_b64_tr_b16 a[42:43], v24 offset:4096\n\t" "ds_read_b64_tr_b16 a[104:105], v25 offset:0\n\t" \
   "ds_read_b64_tr_b16 a[106:107], v25 offset:4096\n\t" "v_add_u32 v24, s40, v18\n\t" "v_add_u32 v25, s40, v19\n\t" "s_add_u32 s40, s40, 8192\n\t" \
   "s_cmp_eq_u32 s40, 81920\n\t" "s_cselect_b32 s40, 0, s40\n\t" "ds_read_b64_tr_b16 a[48:49], v24 offset:0\n\t" \
   "ds_read_b64_tr_b16 a[50:51], v24 offset:4096\n\t" "ds_read_b64_tr_b16 a[112:113], v25 offset:0\n\t" \
   "ds_read_b64_tr_b16 a[114:115], v25 offset:4096\n\t" "v_add_u32 v24, s40, v18\n\t" "v_add_u32 v25, s40, v19\n\t" "s_add_u32 s40, s40, 8192\n\t" \
   "s_cmp_eq_u32 s40, 81920\n\t" "s_cselect_b32 s40, 0, s40\n\t" "ds_read_b64_tr_b16 a[56:57], v24 offset:0\n\t" \
   "ds_read_b64_tr_b16 a[58:59], v24 offset:4096\n\t" "ds_read_b64_tr_b16 a[120:121], v25 offset:0\n\t" \
   "ds_read_b64_tr_b16 a[122:123], v25 offset:4096\n\t" "v_add_u32 v24, s40, v18\n\t" "v_add_u32 v25, s40, v19\n\t" "s_add_u32 s40, s40, 8192\n\t" \
   "s_cmp_eq_u32 s40, 81920\n\t" "s_cselect_b32 s40, 0, s40\n\t" "ds_read_b64_tr_b16 v[192:193], v24 offset:0\n\t" \
   "ds_read_b64_tr_b16 v[194:195], v24 offset:4096\n\t" "ds_read_b64_tr_b16 v[208:209], v25 offset:0\n\t" \
   "ds_read_b64_tr_b16 v[210:211], v25 offset:4096\n\t" "v_add_u32 v24, s40, v18\n\t" "v_add_u32 v25, s40, v19\n\t" "s_add_u32 s40, s40, 8192\n\t" \
   "s_cmp_eq_u32 s40, 81920\n\t" "s_cselect_b32 s40, 0, s40\n\t" "ds_read_b64_tr_b16 v[200:201], v24 offset:0\n\t" \
   "ds_read_b64_tr_b16 v[202:203], v24 offset:4096\n\t" "ds_read_b64_tr_b16 v[216:217], v25 offset:0\n\t" \
   "ds_read_b64_tr_b16 v[218:219], v25 offset:4096\n\t" "s_waitcnt vmcnt(15)\n\t" "s_waitcnt lgkmcnt(0)\n\t" "s_barrier\n\t" \
   "ds_write_b128 v20, a[128:131] offset:0\n\t" "s_mov_b32 s41, 4194304\n\t" "buffer_load_dwordx4 v[224:227], v16, %[rb], s41 offen\n\t" \
   "s_waitcnt vmcnt(15)\n\t" "ds_write_b128 v20, a[192:195] offset:4096\n\t" "s_mov_b32 s41, 4325376\n\t" \
   "buffer_load_dwordx4 v[240:243], v16, %[rb], s41 offen\n\t" "s_waitcnt vmcnt(15)\n\t" "ds_write_b128 v20, a[136:139] offset:8192\n\t" \
   "s_mov_b32 s41, 4718592\n\t" "buffer_load_dwordx4 v[232:235], v16, %[rb], s41 offen\n\t" "s_waitcnt vmcnt(15)\n\t" \
   "ds_write_b128 v20, a[200:203] offset:12288\n\t" "s_mov_b32 s41, 4849664\n\t" "buffer_load_dwordx4 v[248:251], v16, %[rb], s41 offen\n\t" \
   "s_waitcnt vmcnt(15)\n\t" "ds_write_b128 v20, a[144:147] offset:16384\n\t" "s_mov_b32 s41, 262144\n\t" \
   "buffer_load_dwordx4 a[4:7], v16, %[ra], s41 offen\n\t" "s_waitcnt vmcnt(15)\n\t" "ds_write_b128 v20, a[208:211] offset:20480\n\t" \
   "s_mov_b32 s41, 393216\n\t" "buffer_load_dwordx4 a[68:71], v16, %[ra], s41 offen\n\t" "s_waitcnt vmcnt(15)\n\t" \
   "ds_write_b128 v20, a[152:155] offset:24576\n\t" "s_mov_b32 s41, 786432\n\t" "buffer_load_dwordx4 a[12:15], v16, %[ra], s41 offen\n\t" \
   "s_waitcnt vmcnt(15)\n\t" "ds_write_b128 v20, a[216:219] offset:28672\n\t" "s_mov_b32 s41, 917504\n\t" \
   "buffer_load_dwordx4 a[76:79], v16, %[ra], s41 offen\n\t" "s_waitcnt vmcnt(15)\n\t" "ds_write_b128 v20, a[160:163] offset:32768\n\t" \
   "s_mov_b32 s41, 1310720\n\t" "buffer_load_dwordx4 a[20:23], v16, %[ra], s41 offen\n\t" "s_waitcnt vmcnt(15)\n\t" \
   "ds_write_b128 v20, a[224:227] offset:36864\n\t" "s_mov_b32 s41, 1441792\n\t" "buffer_load_dwordx4 a[84:87], v16, %[ra], s41 offen\n\t" \
   "s_waitcnt vmcnt(15)\n\t" "ds_write_b128 v20, a[168:171] offset:40960\n\t" "s_mov_b32 s41, 1835008\n\t" \
   "buffer_load_dwordx4 a[28:31], v16, %[ra], s41 offen\n\t" "s_waitcnt vmcnt(15)\n\t" "ds_write_b128 v20, a[232:235] offset:45056\n\t" \
   "s_mov_b32 s41, 1966080\n\t" "buffer_load_dwordx4 a[92:95], v16, %[ra], s41 offen\n\t" "s_waitcnt vmcnt(15)\n\t" \
   "ds_write_b128 v20, a[176:179] offset:49152\n\t" "s_mov_b32 s41, 2359296\n\t" "buffer_load_dwordx4 a[36:39], v16, %[ra], s41 offen\n\t" \
   "s_waitcnt vmcnt(15)\n\t" "ds_write_b128 v20, a[240:243] offset:53248\n\t" "s_mov_b32 s41, 2490368\n\t" \
   "buffer_load_dwordx4 a[100:103], v16, %[ra], s41 offen\n\t" "s_waitcnt vmcnt(15)\n\t" "ds_write_b128 v20, a[184:187] offset:57344\n\t" \
   "s_mov_b32 s41, 2883584\n\t" "buffer_load_dwordx4 a[44:47], v16, %[ra], s41 offen\n\t" "s_waitcnt vmcnt(15)\n\t" \
   "ds_write_b128 v20, a[248:251] offset:61440\n\t" "s_mov_b32 s41, 3014656\n\t" "buffer_load_dwordx4 a[108:111], v16, %[ra], s41 offen\n\t" \
   "s_waitcnt vmcnt(15)\n\t" "ds_write_b128 v23, v[224:227] offset:0\n\t" "s_mov_b32 s41, 3407872\n\t" \
   "buffer_load_dwordx4 a[52:55], v16, %[ra], s41 offen\n\t" "s_waitcnt vmcnt(15)\n\t" "ds_write_b128 v23, v[240:243] offset:4096\n\t" \
   "s_mov_b32 s41, 3538944\n\t" "buffer_load_dwordx4 a[116:119], v16, %[ra], s41 offen\n\t" "s_waitcnt vmcnt(15)\n\t" \
   "ds_write_b128 v23, v[232:235] offset:8192\n\t" "s_mov_b32 s41, 3932160\n\t" "buffer_load_dwordx4 a[60:63], v16, %[ra], s41 offen\n\t" \
   "s_waitcnt vmcnt(15)\n\t" "ds_write_b128 v23, v[248:251] offset:12288\n\t" "s_mov_b32 s41, 4063232\n\t" \
   "buffer_load_dwordx4 a[124:127], v16, %[ra], s41 offen\n\t" "s_waitcnt lgkmcnt(0)\n\t" "s_barrier\n\t" LDSTAMP(3) "s_lshl_b32 s40, %[p0], 13\n\t" \
   "v_add_u32 v24, s40, v18\n\t" "v_add_u32 v25, s40, v19\n\t" "s_add_u32 s40, s40, 8192\n\t" "s_cmp_eq_u32 s40, 81920\n\t" \
   "s_cselect_b32 s40, 0, s40\n\t" "ds_read_b64_tr_b16 a[128:129], v24 offset:0\n\t" "ds_read_b64_tr_b16 a[130:131], v24 offset:4096\n\t" \
   "ds_read_b64_tr_b16 a[192:193], v25 offset:0\n\t" "ds_read_b64_tr_b16 a[194:195], v25 offset:4096\n\t" "v_add_u32 v24, s40, v18\n\t" \
   "v_add_u32 v25, s40, v19\n\t" "s_add_u32 s40, s40, 8192\n\t" "s_cmp_eq_u32 s40, 81920\n\t" "s_cselect_b32 s40, 0, s40\n\t" \
   "ds_read_b64_tr_b16 a[136:137], v24 offset:0\n\t" "ds_read_b64_tr_b16 a[138:139], v24 offset:4096\n\t" \
   "ds_read_b64_tr_b16 a[200:201], v25 offset:0\n\t" "ds_read_b64_tr_b16 a[202:203], v25 offset:4096\n\t" "v_add_u32 v24, s40, v18\n\t" \
   "v_add_u32 v25, s40, v19\n\t" "s_add_u32 s40, s40, 8192\n\t" "s_cmp_eq_u32 s40, 81920\n\t" "s_cselect_b32 s40, 0, s40\n\t" \
   "ds_read_b64_tr_b16 a[144:145], v24 offset:0\n\t" "ds_read_b64_tr_b16 a[146:147], v24 offset:4096\n\t" \
   "ds_read_b64_tr_b16 a[208:209], v25 offset:0\n\t" "ds_read_b64_tr_b16 a[210:211], v25 offset:4096\n\t" "v_add_u32 v24, s40, v18\n\t" \
   "v_add_u32 v25, s40, v19\n\t" "s_add_u32 s40, s40, 8192\n\t" "s_cmp_eq_u32 s40, 81920\n\t" "s_cselect_b32 s40, 0, s40\n\t" \
   "ds_read_b64_tr_b16 a[152:153], v24 offset:0\n\t" "ds_read_b64_tr_b16 a[154:155], v24 offset:4096\n\t" \
   "ds_read_b64_tr_b16 a[216:217], v25 offset:0\n\t" "ds_read_b64_tr_b16 a[218:219], v25 offset:4096\n\t" "v_add_u32 v24, s40, v18\n\t" \
   "v_add_u32 v25, s40, v19\n\t" "s_add_u32 s40, s40, 8192\n\t" "s_cmp_eq_u32 s40, 81920\n\t" "s_cselect_b32 s40, 0, s40\n\t" \
   "ds_read_b64_tr_b16 a[160:161], v24 offset:0\n\t" "ds_read_b64_tr_b16 a[162:163], v24 offset:4096\n\t" \
   "ds_read_b64_tr_b16 a[224:225], v25 offset:0\n\t" "ds_read_b64_tr_b16 a[226:227], v25 offset:4096\n\t" "v_add_u32 v24, s40, v18\n\t" \
   "v_add_u32 v25, s40, v19\n\t" "s_add_u32 s40, s40, 8192\n\t" "s_cmp_eq_u32 s40, 81920\n\t" "s_cselect_b32 s40, 0, s40\n\t" \
   "ds_read_b64_tr_b16 a[168:169], v24 offset:0\n\t" "ds_read_b64_tr_b16 a[170:171], v24 offset:4096\n\t" \
   "ds_read_b64_tr_b16 a[232:233], v25 offset:0\n\t" "ds_read_b64_tr_b16 a[234:235], v25 offset:4096\n\t" "v_add_u32 v24, s40, v18\n\t" \
   "v_add_u32 v25, s40, v19\n\t" "s_add_u32 s40, s40, 8192\n\t" "s_cmp_eq_u32 s40, 81920\n\t" "s_cselect_b32 s40, 0, s40\n\t" \
   "ds_read_b64_tr_b16 a[176:177], v24 offset:0\n\t" "ds_read_b64_tr_b16 a[178:179], v24 offset:4096\n\t" \
   "ds_read_b64_tr_b16 a[240:241], v25 offset:0\n\t" "ds_read_b64_tr_b16 a[242:243], v25 offset:4096\n\t" "v_add_u32 v24, s40, v18\n\t" \
   "v_add_u32 v25, s40, v19\n\t" "s_add_u32 s40, s40, 8192\n\t" "s_cmp_eq_u32 s40, 81920\n\t" "s_cselect_b32 s40, 0, s40\n\t" \
   "ds_read_b64_tr_b16 a[184:185], v24 offset:0\n\t" "ds_read_b64_tr_b16 a[186:187], v24 offset:4096\n\t" \
   "ds_read_b64_tr_b16 a[248:249], v25 offset:0\n\t" "ds_read_b64_tr_b16 a[250:251], v25 offset:4096\n\t" "v_add_u32 v24, s40, v18\n\t" \
   "v_add_u32 v25, s40, v19\n\t" "s_add_u32 s40, s40, 8192\n\t" "s_cmp_eq_u32 s40, 81920\n\t" "s_cselect_b32 s40, 0, s40\n\t" \
   "ds_read_b64_tr_b16 v[224:225], v24 offset:0\n\t" "ds_read_b64_tr_b16 v[226:227], v24 offset:4096\n\t" \
   "ds_read_b64_tr_b16 v[240:241], v25 offset:0\n\t" "ds_read_b64_tr_b16 v[242:243], v25 offset:4096\n\t" "v_add_u32 v24, s40, v18\n\t" \
   "v_add_u32 v25, s40, v19\n\t" "s_add_u32 s40, s40, 8192\n\t" "s_cmp_eq_u32 s40, 81920\n\t" "s_cselect_b32 s40, 0, s40\n\t" \
   "ds_read_b64_tr_b16 v[232:233], v24 offset:0\n\t" "ds_read_b64_tr_b16 v[234:235], v24 offset:4096\n\t" \
   "ds_read_b64_tr_b16 v[248:249], v25 offset:0\n\t" "ds_read_b64_tr_b16 v[250:251], v25 offset:4096\n\t" "s_waitcnt vmcnt(15)\n\t" \
   "s_waitcnt lgkmcnt(0)\n\t" "s_barrier\n\t" "ds_write_b128 v20, a[4:7] offset:0\n\t" "s_mov_b32 s41, 4456448\n\t" \
   "buffer_load_dwordx4 v[196:199], v16, %[ra], s41 offen\n\t" "s_waitcnt vmcnt(15)\n\t" "ds_write_b128 v20, a[68:71] offset:4096\n\t" \
   "s_mov_b32 s41, 4587520\n\t" "buffer_load_dwordx4 v[212:215], v16, %[ra], s41 offen\n\t" "s_waitcnt vmcnt(15)\n\t" \
   "ds_write_b128 v20, a[12:15] offset:8192\n\t" "s_mov_b32 s41, 4980736\n\t" "buffer_load_dwordx4 v[204:207], v16, %[ra], s41 offen\n\t" \
   "s_waitcnt vmcnt(15)\n\t" "ds_write_b128 v20, a[76:79] offset:12288\n\t" "s_mov_b32 s41, 5111808\n\t" \
   "buffer_load_dwordx4 v[220:223], v16, %[ra], s41 offen\n\t" "s_waitcnt vmcnt(15)\n\t" "ds_write_b128 v20, a[20:23] offset:16384\n\t" \
   "s_mov_b32 s41, 262144\n\t" "buffer_load_dwordx4 a[132:135], v16, %[rb], s41 offen\n\t" "s_waitcnt vmcnt(15)\n\t" \
   "ds_write_b128 v20, a[84:87] offset:20480\n\t" "s_mov_b32 s41, 393216\n\t" "buffer_load_dwordx4 a[196:199], v16, %[rb], s41 offen\n\t" \
   "s_waitcnt vmcnt(15)\n\t" "ds_write_b128 v20, a[28:31] offset:24576\n\t" "s_mov_b32 s41, 786432\n\t" \
   "buffer_load_dwordx4 a[140:143], v16, %[rb], s41 offen\n\t" "s_waitcnt vmcnt(15)\n\t" "ds_write_b128 v20, a[92:95] offset:28672\n\t" \
   "s_mov_b32 s41, 917504\n\t" "buffer_load_dwordx4 a[204:207], v16, %[rb], s41 offen\n\t" "s_waitcnt vmcnt(15)\n\t" \
   "ds_write_b128 v20, a[36:39] offset:32768\n\t" "s_mov_b32 s41, 1310720\n\t" "buffer_load_dwordx4 a[148:151], v16, %[rb], s41 offen\n\t" \
   "s_waitcnt vmcnt(15)\n\t" "ds_write_b128 v20, a[100:103] offset:36864\n\t" "s_mov_b32 s41, 1441792\n\t" \
   "buffer_load_dwordx4 a[212:215], v16, %[rb], s41 offen\n\t" "s_waitcnt vmcnt(15)\n\t" "ds_write_b128 v20, a[44:47] offset:40960\n\t" \
   "s_mov_b32 s41, 1835008\n\t" "buffer_load_dwordx4 a[156:159], v16, %[rb], s41 offen\n\t" "s_waitcnt vmcnt(15)\n\t" \
   "ds_write_b128 v20, a[108:111] offset:45056\n\t" "s_mov_b32 s41, 1966080\n\t" "buffer_load_dwordx4 a[220:223], v16, %[rb], s41 offen\n\t" \
   "s_waitcnt vmcnt(15)\n\t" "ds_write_b128 v20, a[52:55] offset:49152\n\t" "s_mov_b32 s41, 2359296\n\t" \
   "buffer_load_dwordx4 a[164:167], v16, %[rb], s41 offen\n\t" "s_waitcnt vmcnt(15)\n\t" "ds_write_b128 v20, a[116:119] offset:53248\n\t" \
   "s_mov_b32 s41, 2490368\n\t" "buffer_load_dwordx4 a[228:231], v16, %[rb], s41 offen\n\t" "s_waitcnt vmcnt(15)\n\t" \
   "ds_write_b128 v20, a[60:63] offset:57344\n\t" "s_mov_b32 s41, 2883584\n\t" "buffer_load_dwordx4 a[172:175], v16, %[rb], s41 offen\n\t" \
   "s_waitcnt vmcnt(15)\n\t" "ds_write_b128 v20, a[124:127] offset:61440\n\t" "s_mov_b32 s41, 3014656\n\t" \
   "buffer_load_dwordx4 a[236:239], v16, %[rb], s41 offen\n\t" "s_waitcnt vmcnt(15)\n\t" "ds_write_b128 v23, v[196:199] offset:0\n\t" \
   "s_mov_b32 s41, 3407872\n\t" "buffer_load_dwordx4 a[180:183], v16, %[rb], s41 offen\n\t" "s_waitcnt vmcnt(15)\n\t" \
   "ds_write_b128 v23, v[212:215] offset:4096\n\t" "s_mov_b32 s41, 3538944\n\t" "buffer_load_dwordx4 a[244:247], v16, %[rb], s41 offen\n\t" \
   "s_waitcnt vmcnt(15)\n\t" "ds_write_b128 v23, v[204:207] offset:8192\n\t" "s_mov_b32 s41, 3932160\n\t" \
   "buffer_load_dwordx4 a[188:191], v16, %[rb], s41 offen\n\t" "s_waitcnt vmcnt(15)\n\t" "ds_write_b128 v23, v[220:223] offset:12288\n\t" \
   "s_mov_b32 s41, 4063232\n\t" "buffer_load_dwordx4 a[252:255], v16, %[rb], s41 offen\n\t" "s_waitcnt lgkmcnt(0)\n\t" "s_barrier\n\t" LDSTAMP(4) \
   "s_lshl_b32 s40, %[p0], 13\n\t" "v_add_u32 v24, s40, v18\n\t" "v_add_u32 v25, s40, v19\n\t" "s_add_u32 s40, s40, 8192\n\t" \
   "s_cmp_eq_u32 s40, 81920\n\t" "s_cselect_b32 s40, 0, s40\n\t" "ds_read_b64_tr_b16 a[4:5], v24 offset:0\n\t" \
   "ds_read_b64_tr_b16 a[6:7], v24 offset:4096\n\t" "ds_read_b64_tr_b16 a[68:69], v25 offset:0\n\t" "ds_read_b64_tr_b16 a[70:71], v25 offset:4096\n\t" \
   "v_add_u32 v24, s40, v18\n\t" "v_add_u32 v25, s40, v19\n\t" "s_add_u32 s40, s40, 8192\n\t" "s_cmp_eq_u32 s40, 81920\n\t" \
   "s_cselect_b32 s40, 0, s40\n\t" "ds_read_b64_tr_b16 a[12:13], v24 offset:0\n\t" "ds_read_b64_tr_b16 a[14:15], v24 offset:4096\n\t" \
   "ds_read_b64_tr_b16 a[76:77], v25 offset:0\n\t" "ds_read_b64_tr_b16 a[78:79], v25 offset:4096\n\t" "v_add_u32 v24, s40, v18\n\t" \
   "v_add_u32 v25, s40, v19\n\t" "s_add_u32 s40, s40, 8192\n\t" "s_cmp_eq_u32 s40, 81920\n\t" "s_cselect_b32 s40, 0, s40\n\t" \
   "ds_read_b64_tr_b16 a[20:21], v24 offset:0\n\t" "ds_read_b64_tr_b16 a[22:23], v24 offset:4096\n\t" "ds_read_b64_tr_b16 a[84:85], v25 offset:0\n\t" \
   "ds_read_b64_tr_b16 a[86:87], v25 offset:4096\n\t" "v_add_u32 v24, s40, v18\n\t" "v_add_u32 v25, s40, v19\n\t" "s_add_u32 s40, s40, 8192\n\t" \
   "s_cmp_eq_u32 s40, 81920\n\t" "s_cselect_b32 s40, 0, s40\n\t" "ds_read_b64_tr_b16 a[28:29], v24 offset:0\n\t" \
   "ds_read_b64_tr_b16 a[30:31], v24 offset:4096\n\t" "ds_read_b64_tr_b16 a[92:93], v25 offset:0\n\t" \
   "ds_read_b64_tr_b16 a[94:95], v25 offset:4096\n\t" "v_add_u32 v24, s40, v18\n\t" "v_add_u32 v25, s40, v19\n\t" "s_add_u32 s40, s40, 8192\n\t" \
   "s_cmp_eq_u32 s40, 81920\n\t" "s_cselect_b32 s40, 0, s40\n\t" "ds_read_b64_tr_b16 a[36:37], v24 offset:0\n\t" \
   "ds_read_b64_tr_b16 a[38:39], v24 offset:4096\n\t" "ds_read_b64_tr_b16 a[100:101], v25 offset:0\n\t" \
   "ds_read_b64_tr_b16 a[102:103], v25 offset:4096\n\t" "v_add_u32 v24, s40, v18\n\t" "v_add_u32 v25, s40, v19\n\t" "s_add_u32 s40, s40, 8192\n\t" \
   "s_cmp_eq_u32 s40, 81920\n\t" "s_cselect_b32 s40, 0, s40\n\t" "ds_read_b64_tr_b16 a[44:45], v24 offset:0\n\t" \
   "ds_read_b64_tr_b16 a[46:47], v24 offset:4096\n\t" "ds_read_b64_tr_b16 a[108:109], v25 offset:0\n\t" \
   "ds_read_b64_tr_b16 a[110:111], v25 offset:4096\n\t" "v_add_u32 v24, s40, v18\n\t" "v_add_u32 v25, s40, v19\n\t" "s_add_u32 s40, s40, 8192\n\t" \
   "s_cmp_eq_u32 s40, 81920\n\t" "s_cselect_b32 s40, 0, s40\n\t" "ds_read_b64_tr_b16 a[52:53], v24 offset:0\n\t" \
   "ds_read_b64_tr_b16 a[54:55], v24 offset:4096\n\t" "ds_read_b64_tr_b16 a[116:117], v25 offset:0\n\t" \
   "ds_read_b64_tr_b16 a[118:119], v25 offset:4096\n\t" "v_add_u32 v24, s40, v18\n\t" "v_add_u32 v25, s40, v19\n\t" "s_add_u32 s40, s40, 8192\n\t" \
   "s_cmp_eq_u32 s40, 81920\n\t" "s_cselect_b32 s40, 0, s40\n\t" "ds_read_b64_tr_b16 a[60:61], v24 offset:0\n\t" \
   "ds_read_b64_tr_b16 a[62:63], v24 offset:4096\n\t" "ds_read_b64_tr_b16 a[124:125], v25 offset:0\n\t" \
   "ds_read_b64_tr_b16 a[126:127], v25 offset:4096\n\t" "v_add_u32 v24, s40, v18\n\t" "v_add_u32 v25, s40, v19\n\t" "s_add_u32 s40, s40, 8192\n\t" \
   "s_cmp_eq_u32 s40, 81920\n\t" "s_cselect_b32 s40, 0, s40\n\t" "ds_read_b64_tr_b16 v[196:197], v24 offset:0\n\t" \
   "ds_read_b64_tr_b16 v[198:199], v24 offset:4096\n\t" "ds_read_b64_tr_b16 v[212:213], v25 offset:0\n\t" \
   "ds_read_b64_tr_b16 v[214:215], v25 offset:4096\n\t" "v_add_u32 v24, s40, v18\n\t" "v_add_u32 v25, s40, v19\n\t" "s_add_u32 s40, s40, 8192\n\t" \
   "s_cmp_eq_u32 s40, 81920\n\t" "s_cselect_b32 s40, 0, s40\n\t" "ds_read_b64_tr_b16 v[204:205], v24 offset:0\n\t" \
   "ds_read_b64_tr_b16 v[206:207], v24 offset:4096\n\t" "ds_read_b64_tr_b16 v[220:221], v25 offset:0\n\t" \
   "ds_read_b64_tr_b16 v[222:223], v25 offset:4096\n\t" "s_waitcnt vmcnt(15)\n\t" "s_waitcnt lgkmcnt(0)\n\t" "s_barrier\n\t" \
   "ds_write_b128 v20, a[132:135] offset:0\n\t" "s_mov_b32 s41, 4456448\n\t" "buffer_load_dwordx4 v[228:231], v16, %[rb], s41 offen\n\t" \
   "s_waitcnt vmcnt(15)\n\t" "ds_write_b128 v20, a[196:199] offset:4096\n\t" "s_mov_b32 s41, 4587520\n\t" \
   "buffer_load_dwordx4 v[244:247], v16, %[rb], s41 offen\n\t" "s_waitcnt vmcnt(15)\n\t" "ds_write_b128 v20, a[140:143] offset:8192\n\t" \
   "s_mov_b32 s41, 4980736\n\t" "buffer_load_dwordx4 v[236:239], v16, %[rb], s41 offen\n\t" "s_waitcnt vmcnt(15)\n\t" \
   "ds_write_b128 v20, a[204:207] offset:12288\n\t" "s_mov_b32 s41, 5111808\n\t" "buffer_load_dwordx4 v[252:255], v16, %[rb], s41 offen\n\t" \
   "s_waitcnt vmcnt(15)\n\t" "ds_write_b128 v20, a[148:151] offset:16384\n\t" "s_waitcnt vmcnt(14)\n\t" \
   "ds_write_b128 v20, a[212:215] offset:20480\n\t" "s_waitcnt vmcnt(13)\n\t" "ds_write_b128 v20, a[156:159] offset:24576\n\t" \
   "s_waitcnt vmcnt(12)\n\t" "ds_write_b128 v20, a[220:223] offset:28672\n\t" "s_waitcnt vmcnt(11)\n\t" \
   "ds_write_b128 v20, a[164:167] offset:32768\n\t" "s_waitcnt vmcnt(10)\n\t" "ds_write_b128 v20, a[228:231] offset:36864\n\t" \
   "buffer_load_dwordx4 v[28:31], v112, %[rw], 0 offen sc1\n\t" "s_waitcnt vmcnt(10)\n\t" "ds_write_b128 v20, a[172:175] offset:40960\n\t" \
   "s_waitcnt vmcnt(9)\n\t" "ds_write_b128 v20, a[236:239] offset:45056\n\t" "s_waitcnt vmcnt(8)\n\t" "ds_write_b128 v20, a[180:183] offset:49152\n\t" \
   "s_waitcnt vmcnt(7)\n\t" "ds_write_b128 v20, a[244:247] offset:53248\n\t" "s_waitcnt vmcnt(6)\n\t" "ds_write_b128 v20, a[188:191] offset:57344\n\t" \
   "s_waitcnt vmcnt(5)\n\t" "ds_write_b128 v20, a[252:255] offset:61440\n\t" "s_waitcnt vmcnt(4)\n\t" "ds_write_b128 v23, v[228:231] offset:0\n\t" \
   "s_waitcnt vmcnt(3)\n\t" "ds_write_b128 v23, v[244:247] offset:4096\n\t" "s_waitcnt vmcnt(2)\n\t" "ds_write_b128 v23, v[236:239] offset:8192\n\t" \
   "s_waitcnt vmcnt(1)\n\t" "ds_write_b128 v23, v[252:255] offset:12288\n\t" "s_waitcnt lgkmcnt(0)\n\t" "s_barrier\n\t" LDSTAMP(5) \
   "s_lshl_b32 s40, %[p0], 13\n\t" "v_add_u32 v24, s40, v18\n\t" "v_add_u32 v25, s40, v19\n\t" "s_add_u32 s40, s40, 8192\n\t" \
   "s_cmp_eq_u32 s40, 81920\n\t" "s_cselect_b32 s40, 0, s40\n\t" "ds_read_b64_tr_b16 a[132:133], v24 offset:0\n\t" \
   "ds_read_b64_tr_b16 a[134:135], v24 offset:4096\n\t" "ds_read_b64_tr_b16 a[196:197], v25 offset:0\n\t" \
   "ds_read_b64_tr_b16 a[198:199], v25 offset:4096\n\t" "v_add_u32 v24, s40, v18\n\t" "v_add_u32 v25, s40, v19\n\t" "s_add_u32 s40, s40, 8192\n\t" \
   "s_cmp_eq_u32 s40, 81920\n\t" "s_cselect_b32 s40, 0, s40\n\t" "ds_read_b64_tr_b16 a[140:141], v24 offset:0\n\t" \
   "ds_read_b64_tr_b16 a[142:143], v24 offset:4096\n\t" "ds_read_b64_tr_b16 a[204:205], v25 offset:0\n\t" \
   "ds_read_b64_tr_b16 a[206:207], v25 offset:4096\n\t" "v_add_u32 v24, s40, v18\n\t" "v_add_u32 v25, s40, v19\n\t" "s_add_u32 s40, s40, 8192\n\t" \
   "s_cmp_eq_u32 s40, 81920\n\t" "s_cselect_b32 s40, 0, s40\n\t" "ds_read_b64_tr_b16 a[148:149], v24 offset:0\n\t" \
   "ds_read_b64_tr_b16 a[150:151], v24 offset:4096\n\t" "ds_read_b64_tr_b16 a[212:213], v25 offset:0\n\t" \
   "ds_read_b64_tr_b16 a[214:215], v25 offset:4096\n\t" "v_add_u32 v24, s40, v18\n\t" "v_add_u32 v25, s40, v19\n\t" "s_add_u32 s40, s40, 8192\n\t" \
   "s_cmp_eq_u32 s40, 81920\n\t" "s_cselect_b32 s40, 0, s40\n\t" "ds_read_b64_tr_b16 a[156:157], v24 offset:0\n\t" \
   "ds_read_b64_tr_b16 a[158:159], v24 offset:4096\n\t" "ds_read_b64_tr_b16 a[220:221], v25 offset:0\n\t" \
   "ds_read_b64_tr_b16 a[222:223], v25 offset:4096\n\t" "v_add_u32 v24, s40, v18\n\t" "v_add_u32 v25, s40, v19\n\t" "s_add_u32 s40, s40, 8192\n\t" \
   "s_cmp_eq_u32 s40, 81920\n\t" "s_cselect_b32 s40, 0, s40\n\t" "ds_read_b64_tr_b16 a[164:165], v24 offset:0\n\t" \
   "ds_read_b64_tr_b16 a[166:167], v24 offset:4096\n\t" "ds_read_b64_tr_b16 a[228:229], v25 offset:0\n\t" \
   "ds_read_b64_tr_b16 a[230:231], v25 offset:4096\n\t" "v_add_u32 v24, s40, v18\n\t" "v_add_u32 v25, s40, v19\n\t" "s_add_u32 s40, s40, 8192\n\t" \
   "s_cmp_eq_u32 s40, 81920\n\t" "s_cselect_b32 s40, 0, s40\n\t" "ds_read_b64_tr_b16 a[172:173], v24 offset:0\n\t" \
   "ds_read_b64_tr_b16 a[174:175], v24 offset:4096\n\t" "ds_read_b64_tr_b16 a[236:237], v25 offset:0\n\t" \
   "ds_read_b64_tr_b16 a[238:239], v25 offset:4096\n\t" "v_add_u32 v24, s40, v18\n\t" "v_add_u32 v25, s40, v19\n\t" "s_add_u32 s40, s40, 8192\n\t" \
   "s_cmp_eq_u32 s40, 81920\n\t" "s_cselect_b32 s40, 0, s40\n\t" "ds_read_b64_tr_b16 a[180:181], v24 offset:0\n\t" \
   "ds_read_b64_tr_b16 a[182:183], v24 offset:4096\n\t" "ds_read_b64_tr_b16 a[244:245], v25 offset:0\n\t" \
   "ds_read_b64_tr_b16 a[246:247], v25 offset:4096\n\t" "v_add_u32 v24, s40, v18\n\t" "v_add_u32 v25, s40, v19\n\t" "s_add_u32 s40, s40, 8192\n\t" \
   "s_cmp_eq_u32 s40, 81920\n\t" "s_cselect_b32 s40, 0, s40\n\t" "ds_read_b64_tr_b16 a[188:189], v24 offset:0\n\t" \
   "ds_read_b64_tr_b16 a[190:191], v24 offset:4096\n\t" "ds_read_b64_tr_b16 a[252:253], v25 offset:0\n\t" \
   "ds_read_b64_tr_b16 a[254:255], v25 offset:4096\n\t" "v_add_u32 v24, s40, v18\n\t" "v_add_u32 v25, s40, v19\n\t" "s_add_u32 s40, s40, 8192\n\t" \
   "s_cmp_eq_u32 s40, 81920\n\t" "s_cselect_b32 s40, 0, s40\n\t" "ds_read_b64_tr_b16 v[228:229], v24 offset:0\n\t" \
   "ds_read_b64_tr_b16 v[230:231], v24 offset:4096\n\t" "ds_read_b64_tr_b16 v[244:245], v25 offset:0\n\t" \
   "ds_read_b64_tr_b16 v[246:247], v25 offset:4096\n\t" "v_add_u32 v24, s40, v18\n\t" "v_add_u32 v25, s40, v19\n\t" "s_add_u32 s40, s40, 8192\n\t" \
   "s_cmp_eq_u32 s40, 81920\n\t" "s_cselect_b32 s40, 0, s40\n\t" "ds_read_b64_tr_b16 v[236:237], v24 offset:0\n\t" \
   "ds_read_b64_tr_b16 v[238:239], v24 offset:4096\n\t" "ds_read_b64_tr_b16 v[252:253], v25 offset:0\n\t" \
   "ds_read_b64_tr_b16 v[254:255], v25 offset:4096\n\t" "s_waitcnt vmcnt(0) lgkmcnt(0)\n\t" "s_barrier"
#define LDP_CLOB "v24", "v25", "v26", "v32", "v33", "v34", "v35", "v36", "v37", "v38", "v39", "v40", "v41", "v42", "v43", "v44", "v45", "v46", "v47", "v48", "v49", "v50", "v51", "v52", "v53", "v54", "v55", "v56", "v57", "v58", "v59", "v60", "v61", "v62", "v63", "v64", "v65", "v66", "v67", "v68", "v69", "v70", "v71", "v72", "v73", "v74", "v75", "v76", "v77", "v78", "v79", "v80", "v81", "v82", "v83", "v84", "v85", "v86", "v87", "v88", "v89", "v90", "v91", "v92", "v93", "v94", "v95", "v96", "v97", "v98", "v99", "v100", "v101", "v102", "v103", "v104", "v105", "v106", "v107", "v108", "v109", "v110", "v111", "v120", "v121", "v122", "v123", "s40", "s41"
#define LDK_ASM \
   "buffer_load_dword v26, v113, %[rw], 0 offen sc1\n\t" "s_mov_b32 s41, 0\n\t" "buffer_load_dwordx4 v[32:35], v116, %[rx], s41 offen\n\t" \
   "s_mov_b32 s41, 10240\n\t" "buffer_load_dwordx4 v[36:39], v116, %[rx], s41 offen\n\t" "s_mov_b32 s41, 128\n\t" \
   "buffer_load_dwordx4 v[40:43], v116, %[rx], s41 offen\n\t" "s_mov_b32 s41, 10368\n\t" "buffer_load_dwordx4 v[44:47], v116, %[rx], s41 offen\n\t" \
   "s_mov_b32 s41, 256\n\t" "buffer_load_dwordx4 v[48:51], v116, %[rx], s41 offen\n\t" "s_mov_b32 s41, 10496\n\t" \
   "buffer_load_dwordx4 v[52:55], v116, %[rx], s41 offen\n\t" "s_mov_b32 s41, 384\n\t" "buffer_load_dwordx4 v[56:59], v116, %[rx], s41 offen\n\t" \
   "s_mov_b32 s41, 10624\n\t" "buffer_load_dwordx4 v[60:63], v116, %[rx], s41 offen\n\t" "s_mov_b32 s41, 512\n\t" \
   "buffer_load_dwordx4 v[64:67], v116, %[rx], s41 offen\n\t" "s_mov_b32 s41, 10752\n\t" "buffer_load_dwordx4 v[68:71], v116, %[rx], s41 offen\n\t" \
   "s_mov_b32 s41, 640\n\t" "buffer_load_dwordx4 v[72:75], v116, %[rx], s41 offen\n\t" "s_mov_b32 s41, 10880\n\t" \
   "buffer_load_dwordx4 v[76:79], v116, %[rx], s41 offen\n\t" "s_mov_b32 s41, 768\n\t" "buffer_load_dwordx4 v[80:83], v116, %[rx], s41 offen\n\t" \
   "s_mov_b32 s41, 11008\n\t" "buffer_load_dwordx4 v[84:87], v116, %[rx], s41 offen\n\t" "s_mov_b32 s41, 896\n\t" \
   "buffer_load_dwordx4 v[88:91], v116, %[rx], s41 offen\n\t" "s_mov_b32 s41, 11136\n\t" "buffer_load_dwordx4 v[92:95], v116, %[rx], s41 offen\n\t" \
   "s_mov_b32 s40, 0x3f803f80\n\t" "v_mov_b32 v120, 0\n\t" "v_mov_b32 v121, 0\n\t" "v_mov_b32 v122, 0\n\t" "v_mov_b32 v123, 0\n\t" \
   "s_waitcnt vmcnt(16)\n\t" "v_add_u32 v26, 1, v26\n\t" "buffer_store_dword v26, v114, %[rw], 0 offen sc1\n\t" "ds_write_b32 v115, v26\n\t" LDSTAMP(0) \
   "s_waitcnt vmcnt(15)\n\t" LDSTAMP(1) "ds_write_b64 v117, v[32:33] offset:0\n\t" "ds_write_b64 v117, v[34:35] offset:256\n\t" \
   "s_mov_b32 s41, 1024\n\t" "buffer_load_dwordx4 v[96:99], v116, %[rx], s41 offen\n\t" "v_dot2c_f32_bf16 v120, s40, v32\n\t" \
   "v_dot2c_f32_bf16 v121, v32, v32\n\t" "v_dot2c_f32_bf16 v120, s40, v33\n\t" "v_dot2c_f32_bf16 v121, v33, v33\n\t" \
   "v_dot2c_f32_bf16 v120, s40, v34\n\t" "v_dot2c_f32_bf16 v121, v34, v34\n\t" "v_dot2c_f32_bf16 v120, s40, v35\n\t" \
   "v_dot2c_f32_bf16 v121, v35, v35\n\t" "s_waitcnt vmcnt(15)\n\t" "ds_write_b64 v117, v[36:37] offset:128\n\t" \
   "ds_write_b64 v117, v[38:39] offset:384\n\t" "s_mov_b32 s41, 11264\n\t" "buffer_load_dwordx4 v[100:103], v116, %[rx], s41 offen\n\t" \
   "v_dot2c_f32_bf16 v122, s40, v36\n\t" "v_dot2c_f32_bf16 v123, v36, v36\n\t" "v_dot2c_f32_bf16 v122, s40, v37\n\t" \
   "v_dot2c_f32_bf16 v123, v37, v37\n\t" "v_dot2c_f32_bf16 v122, s40, v38\n\t" "v_dot2c_f32_bf16 v123, v38, v38\n\t" \
   "v_dot2c_f32_bf16 v122, s40, v39\n\t" "v_dot2c_f32_bf16 v123, v39, v39\n\t" "s_waitcnt vmcnt(15)\n\t" "ds_write_b64 v117, v[40:41] offset:2048\n\t" \
   "ds_write_b64 v117, v[42:43] offset:2304\n\t" "s_mov_b32 s41, 1152\n\t" "buffer_load_dwordx4 v[104:107], v116, %[rx], s41 offen\n\t" \
   "v_dot2c_f32_bf16 v120, s40, v40\n\t" "v_dot2c_f32_bf16 v121, v40, v40\n\t" "v_dot2c_f32_bf16 v120, s40, v41\n\t" \
   "v_dot2c_f32_bf16 v121, v41, v41\n\t" "v_dot2c_f32_bf16 v120, s40, v42\n\t" "v_dot2c_f32_bf16 v121, v42, v42\n\t" \
   "v_dot2c_f32_bf16 v120, s40, v43\n\t" "v_dot2c_f32_bf16 v121, v43, v43\n\t" "s_waitcnt vmcnt(15)\n\t" "ds_write_b64 v117, v[44:45] offset:2176\n\t" \
   "ds_write_b64 v117, v[46:47] offset:2432\n\t" "s_mov_b32 s41, 11392\n\t" "buffer_load_dwordx4 v[108:111], v116, %[rx], s41 offen\n\t" \
   "v_dot2c_f32_bf16 v122, s40, v44\n\t" "v_dot2c_f32_bf16 v123, v44, v44\n\t" "v_dot2c_f32_bf16 v122, s40, v45\n\t" \
   "v_dot2c_f32_bf16 v123, v45, v45\n\t" "v_dot2c_f32_bf16 v122, s40, v46\n\t" "v_dot2c_f32_bf16 v123, v46, v46\n\t" \
   "v_dot2c_f32_bf16 v122, s40, v47\n\t" "v_dot2c_f32_bf16 v123, v47, v47\n\t" "s_waitcnt vmcnt(15)\n\t" "ds_write_b64 v117, v[48:49] offset:4096\n\t" \
   "ds_write_b64 v117, v[50:51] offset:4352\n\t" "s_mov_b32 s41, 0\n\t" "buffer_load_dwordx4 a[0:3], v16, %[ra], s41 offen\n\t" \
   "v_dot2c_f32_bf16 v120, s40, v48\n\t" "v_dot2c_f32_bf16 v121, v48, v48\n\t" "v_dot2c_f32_bf16 v120, s40, v49\n\t" \
   "v_dot2c_f32_bf16 v121, v49, v49\n\t" "v_dot2c_f32_bf16 v120, s40, v50\n\t" "v_dot2c_f32_bf16 v121, v50, v50\n\t" \
   "v_dot2c_f32_bf16 v120, s40, v51\n\t" "v_dot2c_f32_bf16 v121, v51, v51\n\t" "s_waitcnt vmcnt(15)\n\t" "ds_write_b64 v117, v[52:53] offset:4224\n\t" \
   "ds_write_b64 v117, v[54:55] offset:4480\n\t" "s_mov_b32 s41, 131072\n\t" "buffer_load_dwordx4 a[4:7], v16, %[ra], s41 offen\n\t" \
   "v_dot2c_f32_bf16 v122, s40, v52\n\t" "v_dot2c_f32_bf16 v123, v52, v52\n\t" "v_dot2c_f32_bf16 v122, s40, v53\n\t" \
   "v_dot2c_f32_bf16 v123, v53, v53\n\t" "v_dot2c_f32_bf16 v122, s40, v54\n\t" "v_dot2c_f32_bf16 v123, v54, v54\n\t" \
   "v_dot2c_f32_bf16 v122, s40, v55\n\t" "v_dot2c_f32_bf16 v123, v55, v55\n\t" "s_waitcnt vmcnt(15)\n\t" "ds_write_b64 v117, v[56:57] offset:6144\n\t" \
   "ds_write_b64 v117, v[58:59] offset:6400\n\t" "s_mov_b32 s41, 262144\n\t" "buffer_load_dwordx4 a[16:19], v16, %[ra], s41 offen\n\t" \
   "v_dot2c_f32_bf16 v120, s40, v56\n\t" "v_dot2c_f32_bf16 v121, v56, v56\n\t" "v_dot2c_f32_bf16 v120, s40, v57\n\t" \
   "v_dot2c_f32_bf16 v121, v57, v57\n\t" "v_dot2c_f32_bf16 v120, s40, v58\n\t" "v_dot2c_f32_bf16 v121, v58, v58\n\t" \
   "v_dot2c_f32_bf16 v120, s40, v59\n\t" "v_dot2c_f32_bf16 v121, v59, v59\n\t" "s_waitcnt vmcnt(15)\n\t" "ds_write_b64 v117, v[60:61] offset:6272\n\t" \
   "ds_write_b64 v117, v[62:63] offset:6528\n\t" "s_mov_b32 s41, 393216\n\t" "buffer_load_dwordx4 a[20:23], v16, %[ra], s41 offen\n\t" \
   "v_dot2c_f32_bf16 v122, s40, v60\n\t" "v_dot2c_f32_bf16 v123, v60, v60\n\t" "v_dot2c_f32_bf16 v122, s40, v61\n\t" \
   "v_dot2c_f32_bf16 v123, v61, v61\n\t" "v_dot2c_f32_bf16 v122, s40, v62\n\t" "v_dot2c_f32_bf16 v123, v62, v62\n\t" \
   "v_dot2c_f32_bf16 v122, s40, v63\n\t" "v_dot2c_f32_bf16 v123, v63, v63\n\t" "s_waitcnt vmcnt(15)\n\t" "ds_write_b64 v117, v[64:65] offset:8192\n\t" \
   "ds_write_b64 v117, v[66:67] offset:8448\n\t" "s_mov_b32 s41, 524288\n\t" "buffer_load_dwordx4 a[32:35], v16, %[ra], s41 offen\n\t" \
   "v_dot2c_f32_bf16 v120, s40, v64\n\t" "v_dot2c_f32_bf16 v121, v64, v64\n\t" "v_dot2c_f32_bf16 v120, s40, v65\n\t" \
   "v_dot2c_f32_bf16 v121, v65, v65\n\t" "v_dot2c_f32_bf16 v120, s40, v66\n\t" "v_dot2c_f32_bf16 v121, v66, v66\n\t" \
   "v_dot2c_f32_bf16 v120, s40, v67\n\t" "v_dot2c_f32_bf16 v121, v67, v67\n\t" "s_waitcnt vmcnt(15)\n\t" "ds_write_b64 v117, v[68:69] offset:8320\n\t" \
   "ds_write_b64 v117, v[70:71] offset:8576\n\t" "s_mov_b32 s41, 655360\n\t" "buffer_load_dwordx4 a[36:39], v16, %[ra], s41 offen\n\t" \
   "v_dot2c_f32_bf16 v122, s40, v68\n\t" "v_dot2c_f32_bf16 v123, v68, v68\n\t" "v_dot2c_f32_bf16 v122, s40, v69\n\t" \
   "v_dot2c_f32_bf16 v123, v69, v69\n\t" "v_dot2c_f32_bf16 v122, s40, v70\n\t" "v_dot2c_f32_bf16 v123, v70, v70\n\t" \
   "v_dot2c_f32_bf16 v122, s40, v71\n\t" "v_dot2c_f32_bf16 v123, v71, v71\n\t" "s_waitcnt vmcnt(15)\n\t" "ds_write_b64 v117, v[72:73] offset:10240\n\t" \
   "ds_write_b64 v117, v[74:75] offset:10496\n\t" "s_mov_b32 s41, 786432\n\t" "buffer_load_dwordx4 a[48:51], v16, %[ra], s41 offen\n\t" \
   "v_dot2c_f32_bf16 v120, s40, v72\n\t" "v_dot2c_f32_bf16 v121, v72, v72\n\t" "v_dot2c_f32_bf16 v120, s40, v73\n\t" \
   "v_dot2c_f32_bf16 v121, v73, v73\n\t" "v_dot2c_f32_bf16 v120, s40, v74\n\t" "v_dot2c_f32_bf16 v121, v74, v74\n\t" \
   "v_dot2c_f32_bf16 v120, s40, v75\n\t" "v_dot2c_f32_bf16 v121, v75, v75\n\t" "s_waitcnt vmcnt(15)\n\t" "ds_write_b64 v117, v[76:77] offset:10368\n\t" \
   "ds_write_b64 v117, v[78:79] offset:10624\n\t" "s_mov_b32 s41, 917504\n\t" "buffer_load_dwordx4 a[52:55], v16, %[ra], s41 offen\n\t" \
   "v_dot2c_f32_bf16 v122, s40, v76\n\t" "v_dot2c_f32_bf16 v123, v76, v76\n\t" "v_dot2c_f32_bf16 v122, s40, v77\n\t" \
   "v_dot2c_f32_bf16 v123, v77, v77\n\t" "v_dot2c_f32_bf16 v122, s40, v78\n\t" "v_dot2c_f32_bf16 v123, v78, v78\n\t" \
   "v_dot2c_f32_bf16 v122, s40, v79\n\t" "v_dot2c_f32_bf16 v123, v79, v79\n\t" "s_waitcnt vmcnt(15)\n\t" "ds_write_b64 v117, v[80:81] offset:12288\n\t" \
   "ds_write_b64 v117, v[82:83] offset:12544\n\t" "s_mov_b32 s41, 1048576\n\t" "buffer_load_dwordx4 a[64:67], v16, %[ra], s41 offen\n\t" \
   "v_dot2c_f32_bf16 v120, s40, v80\n\t" "v_dot2c_f32_bf16 v121, v80, v80\n\t" "v_dot2c_f32_bf16 v120, s40, v81\n\t" \
   "v_dot2c_f32_bf16 v121, v81, v81\n\t" "v_dot2c_f32_bf16 v120, s40, v82\n\t" "v_dot2c_f32_bf16 v121, v82, v82\n\t" \
   "v_dot2c_f32_bf16 v120, s40, v83\n\t" "v_dot2c_f32_bf16 v121, v83, v83\n\t" "s_waitcnt vmcnt(15)\n\t" "ds_write_b64 v117, v[84:85] offset:12416\n\t" \
   "ds_write_b64 v117, v[86:87] offset:12672\n\t" "s_mov_b32 s41, 1179648\n\t" "buffer_load_dwordx4 a[68:71], v16, %[ra], s41 offen\n\t" \
   "v_dot2c_f32_bf16 v122, s40, v84\n\t" "v_dot2c_f32_bf16 v123, v84, v84\n\t" "v_dot2c_f32_bf16 v122, s40, v85\n\t" \
   "v_dot2c_f32_bf16 v123, v85, v85\n\t" "v_dot2c_f32_bf16 v122, s40, v86\n\t" "v_dot2c_f32_bf16 v123, v86, v86\n\t" \
   "v_dot2c_f32_bf16 v122, s40, v87\n\t" "v_dot2c_f32_bf16 v123, v87, v87\n\t" "s_waitcnt vmcnt(15)\n\t" "ds_write_b64 v117, v[88:89] offset:14336\n\t" \
   "ds_write_b64 v117, v[90:91] offset:14592\n\t" "s_mov_b32 s41, 0\n\t" "buffer_load_dwordx4 a[8:11], v16, %[rb], s41 offen\n\t" \
   "v_dot2c_f32_bf16 v120, s40, v88\n\t" "v_dot2c_f32_bf16 v121, v88, v88\n\t" "v_dot2c_f32_bf16 v120, s40, v89\n\t" \
   "v_dot2c_f32_bf16 v121, v89, v89\n\t" "v_dot2c_f32_bf16 v120, s40, v90\n\t" "v_dot2c_f32_bf16 v121, v90, v90\n\t" \
   "v_dot2c_f32_bf16 v120, s40, v91\n\t" "v_dot2c_f32_bf16 v121, v91, v91\n\t" "s_waitcnt vmcnt(15)\n\t" "ds_write_b64 v117, v[92:93] offset:14464\n\t" \
   "ds_write_b64 v117, v[94:95] offset:14720\n\t" "s_mov_b32 s41, 131072\n\t" "buffer_load_dwordx4 a[12:15], v16, %[rb], s41 offen\n\t" \
   "v_dot2c_f32_bf16 v122, s40, v92\n\t" "v_dot2c_f32_bf16 v123, v92, v92\n\t" "v_dot2c_f32_bf16 v122, s40, v93\n\t" \
   "v_dot2c_f32_bf16 v123, v93, v93\n\t" "v_dot2c_f32_bf16 v122, s40, v94\n\t" "v_dot2c_f32_bf16 v123, v94, v94\n\t" \
   "v_dot2c_f32_bf16 v122, s40, v95\n\t" "v_dot2c_f32_bf16 v123, v95, v95\n\t" "s_waitcnt vmcnt(15)\n\t" "ds_write_b64 v117, v[96:97] offset:16384\n\t" \
   "ds_write_b64 v117, v[98:99] offset:16640\n\t" "s_mov_b32 s41, 262144\n\t" "buffer_load_dwordx4 a[24:27], v16, %[rb], s41 offen\n\t" \
   "v_dot2c_f32_bf16 v120, s40, v96\n\t" "v_dot2c_f32_bf16 v121, v96, v96\n\t" "v_dot2c_f32_bf16 v120, s40, v97\n\t" \
   "v_dot2c_f32_bf16 v121, v97, v97\n\t" "v_dot2c_f32_bf16 v120, s40, v98\n\t" "v_dot2c_f32_bf16 v121, v98, v98\n\t" \
   "v_dot2c_f32_bf16 v120, s40, v99\n\t" "v_dot2c_f32_bf16 v121, v99, v99\n\t" "s_waitcnt vmcnt(15)\n\t" \
   "ds_write_b64 v117, v[100:101] offset:16512\n\t" "ds_write_b64 v117, v[102:103] offset:16768\n\t" "s_mov_b32 s41, 393216\n\t" \
   "buffer_load_dwordx4 a[28:31], v16, %[rb], s41 offen\n\t" "v_dot2c_f32_bf16 v122, s40, v100\n\t" "v_dot2c_f32_bf16 v123, v100, v100\n\t" \
   "v_dot2c_f32_bf16 v122, s40, v101\n\t" "v_dot2c_f32_bf16 v123, v101, v101\n\t" "v_dot2c_f32_bf16 v122, s40, v102\n\t" \
   "v_dot2c_f32_bf16 v123, v102, v102\n\t" "v_dot2c_f32_bf16 v122, s40, v103\n\t" "v_dot2c_f32_bf16 v123, v103, v103\n\t" "s_waitcnt vmcnt(15)\n\t" \
   "ds_write_b64 v117, v[104:105] offset:18432\n\t" "ds_write_b64 v117, v[106:107] offset:18688\n\t" "s_mov_b32 s41, 524288\n\t" \
   "buffer_load_dwordx4 a[40:43], v16, %[rb], s41 offen\n\t" "v_dot2c_f32_bf16 v120, s40, v104\n\t" "v_dot2c_f32_bf16 v121, v104, v104\n\t" \
   "v_dot2c_f32_bf16 v120, s40, v105\n\t" "v_dot2c_f32_bf16 v121, v105, v105\n\t" "v_dot2c_f32_bf16 v120, s40, v106\n\t" \
   "v_dot2c_f32_bf16 v121, v106, v106\n\t" "v_dot2c_f32_bf16 v120, s40, v107\n\t" "v_dot2c_f32_bf16 v121, v107, v107\n\t" "s_waitcnt vmcnt(15)\n\t" \
   "ds_write_b64 v117, v[108:109] offset:18560\n\t" "ds_write_b64 v117, v[110:111] offset:18816\n\t" "s_mov_b32 s41, 655360\n\t" \
   "buffer_load_dwordx4 a[44:47], v16, %[rb], s41 offen\n\t" "v_dot2c_f32_bf16 v122, s40, v108\n\t" "v_dot2c_f32_bf16 v123, v108, v108\n\t" \
   "v_dot2c_f32_bf16 v122, s40, v109\n\t" "v_dot2c_f32_bf16 v123, v109, v109\n\t" "v_dot2c_f32_bf16 v122, s40, v110\n\t" \
   "v_dot2c_f32_bf16 v123, v110, v110\n\t" "v_dot2c_f32_bf16 v122, s40, v111\n\t" "v_dot2c_f32_bf16 v123, v111, v111\n\t" "s_nop 3\n\t" \
   "v_add_f32_dpp v120, v120, v120 quad_perm:[1,0,3,2] row_mask:0xf bank_mask:0xf\n\t" \
   "v_add_f32_dpp v121, v121, v121 quad_perm:[1,0,3,2] row_mask:0xf bank_mask:0xf\n\t" \
   "v_add_f32_dpp v122, v122, v122 quad_perm:[1,0,3,2] row_mask:0xf bank_mask:0xf\n\t" \
   "v_add_f32_dpp v123, v123, v123 quad_perm:[1,0,3,2] row_mask:0xf bank_mask:0xf\n\t" \
   "v_add_f32_dpp v120, v120, v120 quad_perm:[2,3,0,1] row_mask:0xf bank_mask:0xf\n\t" \
   "v_add_f32_dpp v121, v121, v121 quad_perm:[2,3,0,1] row_mask:0xf bank_mask:0xf\n\t" \
   "v_add_f32_dpp v122, v122, v122 quad_perm:[2,3,0,1] row_mask:0xf bank_mask:0xf\n\t" \
   "v_add_f32_dpp v123, v123, v123 quad_perm:[2,3,0,1] row_mask:0xf bank_mask:0xf\n\t" \
   "v_add_f32_dpp v120, v120, v120 row_half_mirror row_mask:0xf bank_mask:0xf\n\t" \
   "v_add_f32_dpp v121, v121, v121 row_half_mirror row_mask:0xf bank_mask:0xf\n\t" \
   "v_add_f32_dpp v122, v122, v122 row_half_mirror row_mask:0xf bank_mask:0xf\n\t" \
   "v_add_f32_dpp v123, v123, v123 row_half_mirror row_mask:0xf bank_mask:0xf\n\t" "v_and_b32 v120, v119, v120\n\t" "v_and_b32 v121, v119, v121\n\t" \
   "v_and_b32 v122, v119, v122\n\t" "v_and_b32 v123, v119, v123\n\t" "ds_write_b64 v118, v[120:121]\n\t" "ds_write_b64 v118, v[122:123] offset:384\n\t" \
   "s_waitcnt vmcnt(15)\n\t" "ds_write_b128 v20, a[0:3] offset:0\n\t" "s_mov_b32 s41, 786432\n\t" \
   "buffer_load_dwordx4 a[56:59], v16, %[rb], s41 offen\n\t" "s_waitcnt vmcnt(15)\n\t" "ds_write_b128 v20, a[4:7] offset:4096\n\t" \
   "s_mov_b32 s41, 917504\n\t" "buffer_load_dwordx4 a[60:63], v16, %[rb], s41 offen\n\t" "s_waitcnt vmcnt(15)\n\t" \
   "ds_write_b128 v20, a[16:19] offset:8192\n\t" "s_mov_b32 s41, 1048576\n\t" "buffer_load_dwordx4 a[72:75], v16, %[rb], s41 offen\n\t" \
   "s_waitcnt vmcnt(15)\n\t" "ds_write_b128 v20, a[20:23] offset:12288\n\t" "s_mov_b32 s41, 1179648\n\t" \
   "buffer_load_dwordx4 a[76:79], v16, %[rb], s41 offen\n\t" "s_waitcnt vmcnt(15)\n\t" "ds_write_b128 v20, a[32:35] offset:16384\n\t" \
   "s_mov_b32 s41, 1310720\n\t" "buffer_load_dwordx4 a[80:83], v16, %[ra], s41 offen\n\t" "s_waitcnt vmcnt(15)\n\t" \
   "ds_write_b128 v20, a[36:39] offset:20480\n\t" "s_mov_b32 s41, 1441792\n\t" "buffer_load_dwordx4 a[84:87], v16, %[ra], s41 offen\n\t" \
   "s_waitcnt vmcnt(15)\n\t" "ds_write_b128 v20, a[48:51] offset:24576\n\t" "s_mov_b32 s41, 1572864\n\t" \
   "buffer_load_dwordx4 a[96:99], v16, %[ra], s41 offen\n\t" "s_waitcnt vmcnt(15)\n\t" "ds_write_b128 v20, a[52:55] offset:28672\n\t" \
   "s_mov_b32 s41, 1703936\n\t" "buffer_load_dwordx4 a[100:103], v16, %[ra], s41 offen\n\t" "s_waitcnt vmcnt(15)\n\t" \
   "ds_write_b128 v20, a[64:67] offset:32768\n\t" "s_mov_b32 s41, 1835008\n\t" "buffer_load_dwordx4 a[112:115], v16, %[ra], s41 offen\n\t" \
   "s_waitcnt vmcnt(15)\n\t" "ds_write_b128 v20, a[68:71] offset:36864\n\t" "s_mov_b32 s41, 1966080\n\t" \
   "buffer_load_dwordx4 a[116:119], v16, %[ra], s41 offen\n\t" "s_waitcnt lgkmcnt(0)\n\t" "s_barrier\n\t" LDSTAMP(2) \
   "ds_read_b64_tr_b16 a[0:1], v18 offset:0\n\t" "ds_read_b64_tr_b16 a[2:3], v18 offset:4096\n\t" "ds_read_b64_tr_b16 a[4:5], v19 offset:0\n\t" \
   "ds_read_b64_tr_b16 a[6:7], v19 offset:4096\n\t" "ds_read_b64_tr_b16 a[16:17], v18 offset:8192\n\t" \
   "ds_read_b64_tr_b16 a[18:19], v18 offset:12288\n\t" "ds_read_b64_tr_b16 a[20:21], v19 offset:8192\n\t" \
   "ds_read_b64_tr_b16 a[22:23], v19 offset:12288\n\t" "ds_read_b64_tr_b16 a[32:33], v18 offset:16384\n\t" \
   "ds_read_b64_tr_b16 a[34:35], v18 offset:20480\n\t" "ds_read_b64_tr_b16 a[36:37], v19 offset:16384\n\t" \
   "ds_read_b64_tr_b16 a[38:39], v19 offset:20480\n\t" "ds_read_b64_tr_b16 a[48:49], v18 offset:24576\n\t" \
   "ds_read_b64_tr_b16 a[50:51], v18 offset:28672\n\t" "ds_read_b64_tr_b16 a[52:53], v19 offset:24576\n\t" \
   "ds_read_b64_tr_b16 a[54:55], v19 offset:28672\n\t" "ds_read_b64_tr_b16 a[64:65], v18 offset:32768\n\t" \
   "ds_read_b64_tr_b16 a[66:67], v18 offset:36864\n\t" "ds_read_b64_tr_b16 a[68:69], v19 offset:32768\n\t" \
   "ds_read_b64_tr_b16 a[70:71], v19 offset:36864\n\t" "s_waitcnt vmcnt(15)\n\t" "s_waitcnt lgkmcnt(0)\n\t" "s_barrier\n\t" \
   "ds_write_b128 v20, a[8:11] offset:0\n\t" "s_mov_b32 s41, 2097152\n\t" "buffer_load_dwordx4 a[128:131], v16, %[ra], s41 offen\n\t" \
   "s_waitcnt vmcnt(15)\n\t" "ds_write_b128 v20, a[12:15] offset:4096\n\t" "s_mov_b32 s41, 2228224\n\t" \
   "buffer_load_dwordx4 a[132:135], v16, %[ra], s41 offen\n\t" "s_waitcnt vmcnt(15)\n\t" "ds_write_b128 v20, a[24:27] offset:8192\n\t" \
   "s_mov_b32 s41, 2359296\n\t" "buffer_load_dwordx4 a[144:147], v16, %[ra], s41 offen\n\t" "s_waitcnt vmcnt(15)\n\t" \
   "ds_write_b128 v20, a[28:31] offset:12288\n\t" "s_mov_b32 s41, 2490368\n\t" "buffer_load_dwordx4 a[148:151], v16, %[ra], s41 offen\n\t" \
   "s_waitcnt vmcnt(15)\n\t" "ds_write_b128 v20, a[40:43] offset:16384\n\t" "s_mov_b32 s41, 1310720\n\t" \
   "buffer_load_dwordx4 a[88:91], v16, %[rb], s41 offen\n\t" "s_waitcnt vmcnt(15)\n\t" "ds_write_b128 v20, a[44:47] offset:20480\n\t" \
   "s_mov_b32 s41, 1441792\n\t" "buffer_load_dwordx4 a[92:95], v16, %[rb], s41 offen\n\t" "s_waitcnt vmcnt(15)\n\t" \
   "ds_write_b128 v20, a[56:59] offset:24576\n\t" "s_mov_b32 s41, 1572864\n\t" "buffer_load_dwordx4 a[104:107], v16, %[rb], s41 offen\n\t" \
   "s_waitcnt vmcnt(15)\n\t" "ds_write_b128 v20, a[60:63] offset:28672\n\t" "s_mov_b32 s41, 1703936\n\t" \
   "buffer_load_dwordx4 a[108:111], v16, %[rb], s41 offen\n\t" "s_waitcnt vmcnt(15)\n\t" "ds_write_b128 v20, a[72:75] offset:32768\n\t" \
   "s_mov_b32 s41, 1835008\n\t" "buffer_load_dwordx4 a[120:123], v16, %[rb], s41 offen\n\t" "s_waitcnt vmcnt(15)\n\t" \
   "ds_write_b128 v20, a[76:79] offset:36864\n\t" "s_mov_b32 s41, 1966080\n\t" "buffer_load_dwordx4 a[124:127], v16, %[rb], s41 offen\n\t" \
   "s_waitcnt lgkmcnt(0)\n\t" "s_barrier\n\t" LDSTAMP(3) "ds_read_b64_tr_b16 a[8:9], v18 offset:0\n\t" \
   "ds_read_b64_tr_b16 a[10:11], v18 offset:4096\n\t" "ds_read_b64_tr_b16 a[12:13], v19 offset:0\n\t" \
   "ds_read_b64_tr_b16 a[14:15], v19 offset:4096\n\t" "ds_read_b64_tr_b16 a[24:25], v18 offset:8192\n\t" \
   "ds_read_b64_tr_b16 a[26:27], v18 offset:12288\n\t" "ds_read_b64_tr_b16 a[28:29], v19 offset:8192\n\t" \
   "ds_read_b64_tr_b16 a[30:31], v19 offset:12288\n\t" "ds_read_b64_tr_b16 a[40:41], v18 offset:16384\n\t" \
   "ds_read_b64_tr_b16 a[42:43], v18 offset:20480\n\t" "ds_read_b64_tr_b16 a[44:45], v19 offset:16384\n\t" \
   "ds_read_b64_tr_b16 a[46:47], v19 offset:20480\n\t" "ds_read_b64_tr_b16 a[56:57], v18 offset:24576\n\t" \
   "ds_read_b64_tr_b16 a[58:59], v18 offset:28672\n\t" "ds_read_b64_tr_b16 a[60:61], v19 offset:24576\n\t" \
   "ds_read_b64_tr_b16 a[62:63], v19 offset:28672\n\t" "ds_read_b64_tr_b16 a[72:73], v18 offset:32768\n\t" \
   "ds_read_b64_tr_b16 a[74:75], v18 offset:36864\n\t" "ds_read_b64_tr_b16 a[76:77], v19 offset:32768\n\t" \
   "ds_read_b64_tr_b16 a[78:79], v19 offset:36864\n\t" "s_waitcnt vmcnt(15)\n\t" "s_waitcnt lgkmcnt(0)\n\t" "s_barrier\n\t" \
   "ds_write_b128 v20, a[80:83] offset:0\n\t" "s_mov_b32 s41, 2097152\n\t" "buffer_load_dwordx4 a[136:139], v16, %[rb], s41 offen\n\t" \
   "s_waitcnt vmcnt(15)\n\t" "ds_write_b128 v20, a[84:87] offset:4096\n\t" "s_mov_b32 s41, 2228224\n\t" \
   "buffer_load_dwordx4 a[140:143], v16, %[rb], s41 offen\n\t" "s_waitcnt vmcnt(15)\n\t" "ds_write_b128 v20, a[96:99] offset:8192\n\t" \
   "s_mov_b32 s41, 2359296\n\t" "buffer_load_dwordx4 a[152:155], v16, %[rb], s41 offen\n\t" "s_waitcnt vmcnt(15)\n\t" \
   "ds_write_b128 v20, a[100:103] offset:12288\n\t" "s_mov_b32 s41, 2490368\n\t" "buffer_load_dwordx4 a[156:159], v16, %[rb], s41 offen\n\t" \
   "s_waitcnt vmcnt(15)\n\t" "ds_write_b128 v20, a[112:115] offset:16384\n\t" "s_mov_b32 s41, 2621440\n\t" \
   "buffer_load_dwordx4 a[160:163], v16, %[ra], s41 offen\n\t" "s_waitcnt vmcnt(15)\n\t" "ds_write_b128 v20, a[116:119] offset:20480\n\t" \
   "s_mov_b32 s41, 2752512\n\t" "buffer_load_dwordx4 a[164:167], v16, %[ra], s41 offen\n\t" "s_waitcnt vmcnt(15)\n\t" \
   "ds_write_b128 v20, a[128:131] offset:24576\n\t" "s_mov_b32 s41, 2883584\n\t" "buffer_load_dwordx4 a[176:179], v16, %[ra], s41 offen\n\t" \
   "s_waitcnt vmcnt(15)\n\t" "ds_write_b128 v20, a[132:135] offset:28672\n\t" "s_mov_b32 s41, 3014656\n\t" \
   "buffer_load_dwordx4 a[180:183], v16, %[ra], s41 offen\n\t" "s_waitcnt vmcnt(15)\n\t" "ds_write_b128 v20, a[144:147] offset:32768\n\t" \
   "s_mov_b32 s41, 3145728\n\t" "buffer_load_dwordx4 a[192:195], v16, %[ra], s41 offen\n\t" "s_waitcnt vmcnt(15)\n\t" \
   "ds_write_b128 v20, a[148:151] offset:36864\n\t" "s_mov_b32 s41, 3276800\n\t" "buffer_load_dwordx4 a[196:199], v16, %[ra], s41 offen\n\t" \
   "s_waitcnt lgkmcnt(0)\n\t" "s_barrier\n\t" LDSTAMP(4) "ds_read_b64_tr_b16 a[80:81], v18 offset:0\n\t" \
   "ds_read_b64_tr_b16 a[82:83], v18 offset:4096\n\t" "ds_read_b64_tr_b16 a[84:85], v19 offset:0\n\t" \
   "ds_read_b64_tr_b16 a[86:87], v19 offset:4096\n\t" "ds_read_b64_tr_b16 a[96:97], v18 offset:8192\n\t" \
   "ds_read_b64_tr_b16 a[98:99], v18 offset:12288\n\t" "ds_read_b64_tr_b16 a[100:101], v19 offset:8192\n\t" \
   "ds_read_b64_tr_b16 a[102:103], v19 offset:12288\n\t" "ds_read_b64_tr_b16 a[112:113], v18 offset:16384\n\t" \
   "ds_read_b64_tr_b16 a[114:115], v18 offset:20480\n\t" "ds_read_b64_tr_b16 a[116:117], v19 offset:16384\n\t" \
   "ds_read_b64_tr_b16 a[118:119], v19 offset:20480\n\t" "ds_read_b64_tr_b16 a[128:129], v18 offset:24576\n\t" \
   "ds_read_b64_tr_b16 a[130:131], v18 offset:28672\n\t" "ds_read_b64_tr_b16 a[132:133], v19 offset:24576\n\t" \
   "ds_read_b64_tr_b16 a[134:135], v19 offset:28672\n\t" "ds_read_b64_tr_b16 a[144:145], v18 offset:32768\n\t" \
   "ds_read_b64_tr_b16 a[146:147], v18 offset:36864\n\t" "ds_read_b64_tr_b16 a[148:149], v19 offset:32768\n\t" \
   "ds_read_b64_tr_b16 a[150:151], v19 offset:36864\n\t" "s_waitcnt vmcnt(15)\n\t" "s_waitcnt lgkmcnt(0)\n\t" "s_barrier\n\t" \
   "ds_write_b128 v20, a[88:91] offset:0\n\t" "s_mov_b32 s41, 3407872\n\t" "buffer_load_dwordx4 a[208:211], v16, %[ra], s41 offen\n\t" \
   "s_waitcnt vmcnt(15)\n\t" "ds_write_b128 v20, a[92:95] offset:4096\n\t" "s_mov_b32 s41, 3538944\n\t" \
   "buffer_load_dwordx4 a[212:215], v16, %[ra], s41 offen\n\t" "s_waitcnt vmcnt(15)\n\t" "ds_write_b128 v20, a[104:107] offset:8192\n\t" \
   "s_mov_b32 s41, 3670016\n\t" "buffer_load_dwordx4 a[224:227], v16, %[ra], s41 offen\n\t" "s_waitcnt vmcnt(15)\n\t" \
   "ds_write_b128 v20, a[108:111] offset:12288\n\t" "s_mov_b32 s41, 3801088\n\t" "buffer_load_dwordx4 a[228:231], v16, %[ra], s41 offen\n\t" \
   "s_waitcnt vmcnt(15)\n\t" "ds_write_b128 v20, a[120:123] offset:16384\n\t" "s_mov_b32 s41, 2621440\n\t" \
   "buffer_load_dwordx4 a[168:171], v16, %[rb], s41 offen\n\t" "s_waitcnt vmcnt(15)\n\t" "ds_write_b128 v20, a[124:127] offset:20480\n\t" \
   "s_mov_b32 s41, 2752512\n\t" "buffer_load_dwordx4 a[172:175], v16, %[rb], s41 offen\n\t" "s_waitcnt vmcnt(15)\n\t" \
   "ds_write_b128 v20, a[136:139] offset:24576\n\t" "s_mov_b32 s41, 2883584\n\t" "buffer_load_dwordx4 a[184:187], v16, %[rb], s41 offen\n\t" \
   "s_waitcnt vmcnt(15)\n\t" "ds_write_b128 v20, a[140:143] offset:28672\n\t" "s_mov_b32 s41, 3014656\n\t" \
   "buffer_load_dwordx4 a[188:191], v16, %[rb], s41 offen\n\t" "s_waitcnt vmcnt(15)\n\t" "ds_write_b128 v20, a[152:155] offset:32768\n\t" \
   "s_mov_b32 s41, 3145728\n\t" "buffer_load_dwordx4 a[200:203], v16, %[rb], s41 offen\n\t" "s_waitcnt vmcnt(15)\n\t" \
   "ds_write_b128 v20, a[156:159] offset:36864\n\t" "s_mov_b32 s41, 3276800\n\t" "buffer_load_dwordx4 a[204:207], v16, %[rb], s41 offen\n\t" \
   "s_waitcnt lgkmcnt(0)\n\t" "s_barrier\n\t" LDSTAMP(5) "ds_read_b64_tr_b16 a[88:89], v18 offset:0\n\t" \
   "ds_read_b64_tr_b16 a[90:91], v18 offset:4096\n\t" "ds_read_b64_tr_b16 a[92:93], v19 offset:0\n\t" \
   "ds_read_b64_tr_b16 a[94:95], v19 offset:4096\n\t" "ds_read_b64_tr_b16 a[104:105], v18 offset:8192\n\t" \
   "ds_read_b64_tr_b16 a[106:107], v18 offset:12288\n\t" "ds_read_b64_tr_b16 a[108:109], v19 offset:8192\n\t" \
   "ds_read_b64_tr_b16 a[110:111], v19 offset:12288\n\t" "ds_read_b64_tr_b16 a[120:121], v18 offset:16384\n\t" \
   "ds_read_b64_tr_b16 a[122:123], v18 offset:20480\n\t" "ds_read_b64_tr_b16 a[124:125], v19 offset:16384\n\t" \
   "ds_read_b64_tr_b16 a[126:127], v19 offset:20480\n\t" "ds_read_b64_tr_b16 a[136:137], v18 offset:24576\n\t" \
   "ds_read_b64_tr_b16 a[138:139], v18 offset:28672\n\t" "ds_read_b64_tr_b16 a[140:141], v19 offset:24576\n\t" \
   "ds_read_b64_tr_b16 a[142:143], v19 offset:28672\n\t" "ds_read_b64_tr_b16 a[152:153], v18 offset:32768\n\t" \
   "ds_read_b64_tr_b16 a[154:155], v18 offset:36864\n\t" "ds_read_b64_tr_b16 a[156:157], v19 offset:32768\n\t" \
   "ds_read_b64_tr_b16 a[158:159], v19 offset:36864\n\t" "s_waitcnt vmcnt(15)\n\t" "s_waitcnt lgkmcnt(0)\n\t" "s_barrier\n\t" \
   "ds_write_b128 v20, a[160:163] offset:0\n\t" "s_mov_b32 s41, 3407872\n\t" "buffer_load_dwordx4 a[216:219], v16, %[rb], s41 offen\n\t" \
   "s_waitcnt vmcnt(15)\n\t" "ds_write_b128 v20, a[164:167] offset:4096\n\t" "s_mov_b32 s41, 3538944\n\t" \
   "buffer_load_dwordx4 a[220:223], v16, %[rb], s41 offen\n\t" "s_waitcnt vmcnt(15)\n\t" "ds_write_b128 v20, a[176:179] offset:8192\n\t" \
   "s_mov_b32 s41, 3670016\n\t" "buffer_load_dwordx4 a[232:235], v16, %[rb], s41 offen\n\t" "s_waitcnt vmcnt(15)\n\t" \
   "ds_write_b128 v20, a[180:183] offset:12288\n\t" "s_mov_b32 s41, 3801088\n\t" "buffer_load_dwordx4 a[236:239], v16, %[rb], s41 offen\n\t" \
   "s_waitcnt vmcnt(15)\n\t" "ds_write_b128 v20, a[192:195] offset:16384\n\t" "s_mov_b32 s41, 3932160\n\t" \
   "buffer_load_dwordx4 a[240:243], v16, %[ra], s41 offen\n\t" "s_waitcnt vmcnt(15)\n\t" "ds_write_b128 v20, a[196:199] offset:20480\n\t" \
   "s_mov_b32 s41, 4063232\n\t" "buffer_load_dwordx4 a[244:247], v16, %[ra], s41 offen\n\t" "s_waitcnt vmcnt(15)\n\t" \
   "ds_write_b128 v20, a[208:211] offset:24576\n\t" "s_mov_b32 s41, 4194304\n\t" "buffer_load_dwordx4 v[192:195], v16, %[ra], s41 offen\n\t" \
   "s_waitcnt vmcnt(15)\n\t" "ds_write_b128 v20, a[212:215] offset:28672\n\t" "s_mov_b32 s41, 4325376\n\t" \
   "buffer_load_dwordx4 v[196:199], v16, %[ra], s41 offen\n\t" "s_waitcnt vmcnt(15)\n\t" "ds_write_b128 v20, a[224:227] offset:32768\n\t" \
   "s_mov_b32 s41, 4456448\n\t" "buffer_load_dwordx4 v[208:211], v16, %[ra], s41 offen\n\t" "s_waitcnt vmcnt(15)\n\t" \
   "ds_write_b128 v20, a[228:231] offset:36864\n\t" "s_mov_b32 s41, 4587520\n\t" "buffer_load_dwordx4 v[212:215], v16, %[ra], s41 offen\n\t" \
   "s_waitcnt lgkmcnt(0)\n\t" "s_barrier\n\t" LDSTAMP(6) "ds_read_b64_tr_b16 a[160:161], v18 offset:0\n\t" \
   "ds_read_b64_tr_b16 a[162:163], v18 offset:4096\n\t" "ds_read_b64_tr_b16 a[164:165], v19 offset:0\n\t" \
   "ds_read_b64_tr_b16 a[166:167], v19 offset:4096\n\t" "ds_read_b64_tr_b16 a[176:177], v18 offset:8192\n\t" \
   "ds_read_b64_tr_b16 a[178:179], v18 offset:12288\n\t" "ds_read_b64_tr_b16 a[180:181], v19 offset:8192\n\t" \
   "ds_read_b64_tr_b16 a[182:183], v19 offset:12288\n\t" "ds_read_b64_tr_b16 a[192:193], v18 offset:16384\n\t" \
   "ds_read_b64_tr_b16 a[194:195], v18 offset:20480\n\t" "ds_read_b64_tr_b16 a[196:197], v19 offset:16384\n\t" \
   "ds_read_b64_tr_b16 a[198:199], v19 offset:20480\n\t" "ds_read_b64_tr_b16 a[208:209], v18 offset:24576\n\t" \
   "ds_read_b64_tr_b16 a[210:211], v18 offset:28672\n\t" "ds_read_b64_tr_b16 a[212:213], v19 offset:24576\n\t" \
   "ds_read_b64_tr_b16 a[214:215], v19 offset:28672\n\t" "ds_read_b64_tr_b16 a[224:225], v18 offset:32768\n\t" \
   "ds_read_b64_tr_b16 a[226:227], v18 offset:36864\n\t" "ds_read_b64_tr_b16 a[228:229], v19 offset:32768\n\t" \
   "ds_read_b64_tr_b16 a[230:231], v19 offset:36864\n\t" "s_waitcnt vmcnt(15)\n\t" "s_waitcnt lgkmcnt(0)\n\t" "s_barrier\n\t" \
   "ds_write_b128 v20, a[168:171] offset:0\n\t" "s_mov_b32 s41, 4718592\n\t" "buffer_load_dwordx4 v[224:227], v16, %[ra], s41 offen\n\t" \
   "s_waitcnt vmcnt(15)\n\t" "ds_write_b128 v20, a[172:175] offset:4096\n\t" "s_mov_b32 s41, 4849664\n\t" \
   "buffer_load_dwordx4 v[228:231], v16, %[ra], s41 offen\n\t" "s_waitcnt vmcnt(15)\n\t" "ds_write_b128 v20, a[184:187] offset:8192\n\t" \
   "s_mov_b32 s41, 4980736\n\t" "buffer_load_dwordx4 v[240:243], v16, %[ra], s41 offen\n\t" "s_waitcnt vmcnt(15)\n\t" \
   "ds_write_b128 v20, a[188:191] offset:12288\n\t" "s_mov_b32 s41, 5111808\n\t" "buffer_load_dwordx4 v[244:247], v16, %[ra], s41 offen\n\t" \
   "s_waitcnt vmcnt(15)\n\t" "ds_write_b128 v20, a[200:203] offset:16384\n\t" "s_mov_b32 s41, 3932160\n\t" \
   "buffer_load_dwordx4 a[248:251], v16, %[rb], s41 offen\n\t" "s_waitcnt vmcnt(15)\n\t" "ds_write_b128 v20, a[204:207] offset:20480\n\t" \
   "s_mov_b32 s41, 4063232\n\t" "buffer_load_dwordx4 a[252:255], v16, %[rb], s41 offen\n\t" "s_waitcnt vmcnt(15)\n\t" \
   "ds_write_b128 v20, a[216:219] offset:24576\n\t" "s_mov_b32 s41, 4194304\n\t" "buffer_load_dwordx4 v[200:203], v16, %[rb], s41 offen\n\t" \
   "s_waitcnt vmcnt(15)\n\t" "ds_write_b128 v20, a[220:223] offset:28672\n\t" "s_mov_b32 s41, 4325376\n\t" \
   "buffer_load_dwordx4 v[204:207], v16, %[rb], s41 offen\n\t" "s_waitcnt vmcnt(15)\n\t" "ds_write_b128 v20, a[232:235] offset:32768\n\t" \
   "s_mov_b32 s41, 4456448\n\t" "buffer_load_dwordx4 v[216:219], v16, %[rb], s41 offen\n\t" "s_waitcnt vmcnt(15)\n\t" \
   "ds_write_b128 v20, a[236:239] offset:36864\n\t" "s_mov_b32 s41, 4587520\n\t" "buffer_load_dwordx4 v[220:223], v16, %[rb], s41 offen\n\t" \
   "s_waitcnt lgkmcnt(0)\n\t" "s_barrier\n\t" LDSTAMP(7) "ds_read_b64_tr_b16 a[168:169], v18 offset:0\n\t" \
   "ds_read_b64_tr_b16 a[170:171], v18 offset:4096\n\t" "ds_read_b64_tr_b16 a[172:173], v19 offset:0\n\t" \
   "ds_read_b64_tr_b16 a[174:175], v19 offset:4096\n\t" "ds_read_b64_tr_b16 a[184:185], v18 offset:8192\n\t" \
   "ds_read_b64_tr_b16 a[186:187], v18 offset:12288\n\t" "ds_read_b64_tr_b16 a[188:189], v19 offset:8192\n\t" \
   "ds_read_b64_tr_b16 a[190:191], v19 offset:12288\n\t" "ds_read_b64_tr_b16 a[200:201], v18 offset:16384\n\t" \
   "ds_read_b64_tr_b16 a[202:203], v18 offset:20480\n\t" "ds_read_b64_tr_b16 a[204:205], v19 offset:16384\n\t" \
   "ds_read_b64_tr_b16 a[206:207], v19 offset:20480\n\t" "ds_read_b64_tr_b16 a[216:217], v18 offset:24576\n\t" \
   "ds_read_b64_tr_b16 a[218:219], v18 offset:28672\n\t" "ds_read_b64_tr_b16 a[220:221], v19 offset:24576\n\t" \
   "ds_read_b64_tr_b16 a[222:223], v19 offset:28672\n\t" "ds_read_b64_tr_b16 a[232:233], v18 offset:32768\n\t" \
   "ds_read_b64_tr_b16 a[234:235], v18 offset:36864\n\t" "ds_read_b64_tr_b16 a[236:237], v19 offset:32768\n\t" \
   "ds_read_b64_tr_b16 a[238:239], v19 offset:36864\n\t" "s_waitcnt vmcnt(15)\n\t" "s_waitcnt lgkmcnt(0)\n\t" "s_barrier\n\t" \
   "ds_write_b128 v20, a[240:243] offset:0\n\t" "s_mov_b32 s41, 4718592\n\t" "buffer_load_dwordx4 v[232:235], v16, %[rb], s41 offen\n\t" \
   "s_waitcnt vmcnt(15)\n\t" "ds_write_b128 v20, a[244:247] offset:4096\n\t" "s_mov_b32 s41, 4849664\n\t" \
   "buffer_load_dwordx4 v[236:239], v16, %[rb], s41 offen\n\t" "s_waitcnt vmcnt(15)\n\t" "ds_write_b128 v20, v[192:195] offset:8192\n\t" \
   "s_mov_b32 s41, 4980736\n\t" "buffer_load_dwordx4 v[248:251], v16, %[rb], s41 offen\n\t" "s_waitcnt vmcnt(15)\n\t" \
   "ds_write_b128 v20, v[196:199] offset:12288\n\t" "s_mov_b32 s41, 5111808\n\t" "buffer_load_dwordx4 v[252:255], v16, %[rb], s41 offen\n\t" \
   "s_waitcnt vmcnt(15)\n\t" "ds_write_b128 v20, v[208:211] offset:16384\n\t" "s_waitcnt vmcnt(14)\n\t" \
   "ds_write_b128 v20, v[212:215] offset:20480\n\t" "s_waitcnt vmcnt(13)\n\t" "ds_write_b128 v20, v[224:227] offset:24576\n\t" \
   "s_waitcnt vmcnt(12)\n\t" "ds_write_b128 v20, v[228:231] offset:28672\n\t" "s_waitcnt vmcnt(11)\n\t" \
   "ds_write_b128 v20, v[240:243] offset:32768\n\t" "s_waitcnt vmcnt(10)\n\t" "ds_write_b128 v20, v[244:247] offset:36864\n\t" \
   "buffer_load_dwordx4 v[28:31], v112, %[rw], 0 offen sc1\n\t" "s_waitcnt lgkmcnt(0)\n\t" "s_barrier\n\t" LDSTAMP(8) \
   "ds_read_b64_tr_b16 a[240:241], v18 offset:0\n\t" "ds_read_b64_tr_b16 a[242:243], v18 offset:4096\n\t" \
   "ds_read_b64_tr_b16 a[244:245], v19 offset:0\n\t" "ds_read_b64_tr_b16 a[246:247], v19 offset:4096\n\t" \
   "ds_read_b64_tr_b16 v[192:193], v18 offset:8192\n\t" "ds_read_b64_tr_b16 v[194:195], v18 offset:12288\n\t" \
   "ds_read_b64_tr_b16 v[196:197], v19 offset:8192\n\t" "ds_read_b64_tr_b16 v[198:199], v19 offset:12288\n\t" \
   "ds_read_b64_tr_b16 v[208:209], v18 offset:16384\n\t" "ds_read_b64_tr_b16 v[210:211], v18 offset:20480\n\t" \
   "ds_read_b64_tr_b16 v[212:213], v19 offset:16384\n\t" "ds_read_b64_tr_b16 v[214:215], v19 offset:20480\n\t" \
   "ds_read_b64_tr_b16 v[224:225], v18 offset:24576\n\t" "ds_read_b64_tr_b16 v[226:227], v18 offset:28672\n\t" \
   "ds_read_b64_tr_b16 v[228:229], v19 offset:24576\n\t" "ds_read_b64_tr_b16 v[230:231], v19 offset:28672\n\t" \
   "ds_read_b64_tr_b16 v[240:241], v18 offset:32768\n\t" "ds_read_b64_tr_b16 v[242:243], v18 offset:36864\n\t" \
   "ds_read_b64_tr_b16 v[244:245], v19 offset:32768\n\t" "ds_read_b64_tr_b16 v[246:247], v19 offset:36864\n\t" "s_waitcnt vmcnt(10)\n\t" \
   "s_waitcnt lgkmcnt(0)\n\t" "s_barrier\n\t" "ds_write_b128 v20, a[248:251] offset:0\n\t" "s_waitcnt vmcnt(9)\n\t" \
   "ds_write_b128 v20, a[252:255] offset:4096\n\t" "s_waitcnt vmcnt(8)\n\t" "ds_write_b128 v20, v[200:203] offset:8192\n\t" "s_waitcnt vmcnt(7)\n\t" \
   "ds_write_b128 v20, v[204:207] offset:12288\n\t" "s_waitcnt vmcnt(6)\n\t" "ds_write_b128 v20, v[216:219] offset:16384\n\t" "s_waitcnt vmcnt(5)\n\t" \
   "ds_write_b128 v20, v[220:223] offset:20480\n\t" "s_waitcnt vmcnt(4)\n\t" "ds_write_b128 v20, v[232:235] offset:24576\n\t" "s_waitcnt vmcnt(3)\n\t" \
   "ds_write_b128 v20, v[236:239] offset:28672\n\t" "s_waitcnt vmcnt(2)\n\t" "ds_write_b128 v20, v[248:251] offset:32768\n\t" "s_waitcnt vmcnt(1)\n\t" \
   "ds_write_b128 v20, v[252:255] offset:36864\n\t" "s_waitcnt lgkmcnt(0)\n\t" "s_barrier\n\t" LDSTAMP(9) \
   "ds_read_b64_tr_b16 a[248:249], v18 offset:0\n\t" "ds_read_b64_tr_b16 a[250:251], v18 offset:4096\n\t" \
   "ds_read_b64_tr_b16 a[252:253], v19 offset:0\n\t" "ds_read_b64_tr_b16 a[254:255], v19 offset:4096\n\t" \
   "ds_read_b64_tr_b16 v[200:201], v18 offset:8192\n\t" "ds_read_b64_tr_b16 v[202:203], v18 offset:12288\n\t" \
   "ds_read_b64_tr_b16 v[204:205], v19 offset:8192\n\t" "ds_read_b64_tr_b16 v[206:207], v19 offset:12288\n\t" \
   "ds_read_b64_tr_b16 v[216:217], v18 offset:16384\n\t" "ds_read_b64_tr_b16 v[218:219], v18 offset:20480\n\t" \
   "ds_read_b64_tr_b16 v[220:221], v19 offset:16384\n\t" "ds_read_b64_tr_b16 v[222:223], v19 offset:20480\n\t" \
   "ds_read_b64_tr_b16 v[232:233], v18 offset:24576\n\t" "ds_read_b64_tr_b16 v[234:235], v18 offset:28672\n\t" \
   "ds_read_b64_tr_b16 v[236:237], v19 offset:24576\n\t" "ds_read_b64_tr_b16 v[238:239], v19 offset:28672\n\t" \
   "ds_read_b64_tr_b16 v[248:249], v18 offset:32768\n\t" "ds_read_b64_tr_b16 v[250:251], v18 offset:36864\n\t" \
   "ds_read_b64_tr_b16 v[252:253], v19 offset:32768\n\t" "ds_read_b64_tr_b16 v[254:255], v19 offset:36864\n\t" "s_waitcnt vmcnt(0) lgkmcnt(0)\n\t" \
   "s_barrier"
#define LDK_CLOB "v24", "v25", "v26", "v32", "v33", "v34", "v35", "v36", "v37", "v38", "v39", "v40", "v41", "v42", "v43", "v44", "v45", "v46", "v47", "v48", "v49", "v50", "v51", "v52", "v53", "v54", "v55", "v56", "v57", "v58", "v59", "v60", "v61", "v62", "v63", "v64", "v65", "v66", "v67", "v68", "v69", "v70", "v71", "v72", "v73", "v74", "v75", "v76", "v77", "v78", "v79", "v80", "v81", "v82", "v83", "v84", "v85", "v86", "v87", "v88", "v89", "v90", "v91", "v92", "v93", "v94", "v95", "v96", "v97", "v98", "v99", "v100", "v101", "v102", "v103", "v104", "v105", "v106", "v107", "v108", "v109", "v110", "v111", "v120", "v121", "v122", "v123", "s40", "s41"
#define LDV_ASM \
   "buffer_load_dword v26, v113, %[rw], 0 offen sc1\n\t" "s_mov_b32 s41, 0\n\t" "buffer_load_dwordx4 v[32:35], v116, %[rx], s41 offen\n\t" \
   "s_mov_b32 s41, 10240\n\t" "buffer_load_dwordx4 v[36:39], v116, %[rx], s41 offen\n\t" "s_mov_b32 s41, 128\n\t" \
   "buffer_load_dwordx4 v[40:43], v116, %[rx], s41 offen\n\t" "s_mov_b32 s41, 10368\n\t" "buffer_load_dwordx4 v[44:47], v116, %[rx], s41 offen\n\t" \
   "s_mov_b32 s41, 256\n\t" "buffer_load_dwordx4 v[48:51], v116, %[rx], s41 offen\n\t" "s_mov_b32 s41, 10496\n\t" \
   "buffer_load_dwordx4 v[52:55], v116, %[rx], s41 offen\n\t" "s_mov_b32 s41, 384\n\t" "buffer_load_dwordx4 v[56:59], v116, %[rx], s41 offen\n\t" \
   "s_mov_b32 s41, 10624\n\t" "buffer_load_dwordx4 v[60:63], v116, %[rx], s41 offen\n\t" "s_mov_b32 s41, 512\n\t" \
   "buffer_load_dwordx4 v[64:67], v116, %[rx], s41 offen\n\t" "s_mov_b32 s41, 10752\n\t" "buffer_load_dwordx4 v[68:71], v116, %[rx], s41 offen\n\t" \
   "s_mov_b32 s41, 640\n\t" "buffer_load_dwordx4 v[72:75], v116, %[rx], s41 offen\n\t" "s_mov_b32 s41, 10880\n\t" \
   "buffer_load_dwordx4 v[76:79], v116, %[rx], s41 offen\n\t" "s_mov_b32 s41, 768\n\t" "buffer_load_dwordx4 v[80:83], v116, %[rx], s41 offen\n\t" \
   "s_mov_b32 s41, 11008\n\t" "buffer_load_dwordx4 v[84:87], v116, %[rx], s41 offen\n\t" "s_mov_b32 s41, 896\n\t" \
   "buffer_load_dwordx4 v[88:91], v116, %[rx], s41 offen\n\t" "s_mov_b32 s41, 11136\n\t" "buffer_load_dwordx4 v[92:95], v116, %[rx], s41 offen\n\t" \
   "s_mov_b32 s40, 0x3f803f80\n\t" "v_mov_b32 v120, 0\n\t" "v_mov_b32 v121, 0\n\t" "v_mov_b32 v122, 0\n\t" "v_mov_b32 v123, 0\n\t" \
   "s_waitcnt vmcnt(16)\n\t" "v_add_u32 v26, 1, v26\n\t" "buffer_store_dword v26, v114, %[rw], 0 offen sc1\n\t" "ds_write_b32 v115, v26\n\t" LDSTAMP(0) \
   "s_waitcnt vmcnt(15)\n\t" LDSTAMP(1) "ds_write_b64 v117, v[32:33] offset:0\n\t" "ds_write_b64 v117, v[34:35] offset:256\n\t" \
   "s_mov_b32 s41, 1024\n\t" "buffer_load_dwordx4 v[96:99], v116, %[rx], s41 offen\n\t" "v_dot2c_f32_bf16 v120, s40, v32\n\t" \
   "v_dot2c_f32_bf16 v121, v32, v32\n\t" "v_dot2c_f32_bf16 v120, s40, v33\n\t" "v_dot2c_f32_bf16 v121, v33, v33\n\t" \
   "v_dot2c_f32_bf16 v120, s40, v34\n\t" "v_dot2c_f32_bf16 v121, v34, v34\n\t" "v_dot2c_f32_bf16 v120, s40, v35\n\t" \
   "v_dot2c_f32_bf16 v121, v35, v35\n\t" "s_waitcnt vmcnt(15)\n\t" "ds_write_b64 v117, v[36:37] offset:128\n\t" \
   "ds_write_b64 v117, v[38:39] offset:384\n\t" "s_mov_b32 s41, 11264\n\t" "buffer_load_dwordx4 v[100:103], v116, %[rx], s41 offen\n\t" \
   "v_dot2c_f32_bf16 v122, s40, v36\n\t" "v_dot2c_f32_bf16 v123, v36, v36\n\t" "v_dot2c_f32_bf16 v122, s40, v37\n\t" \
   "v_dot2c_f32_bf16 v123, v37, v37\n\t" "v_dot2c_f32_bf16 v122, s40, v38\n\t" "v_dot2c_f32_bf16 v123, v38, v38\n\t" \
   "v_dot2c_f32_bf16 v122, s40, v39\n\t" "v_dot2c_f32_bf16 v123, v39, v39\n\t" "s_waitcnt vmcnt(15)\n\t" "ds_write_b64 v117, v[40:41] offset:2048\n\t" \
   "ds_write_b64 v117, v[42:43] offset:2304\n\t" "s_mov_b32 s41, 1152\n\t" "buffer_load_dwordx4 v[104:107], v116, %[rx], s41 offen\n\t" \
   "v_dot2c_f32_bf16 v120, s40, v40\n\t" "v_dot2c_f32_bf16 v121, v40, v40\n\t" "v_dot2c_f32_bf16 v120, s40, v41\n\t" \
   "v_dot2c_f32_bf16 v121, v41, v41\n\t" "v_dot2c_f32_bf16 v120, s40, v42\n\t" "v_dot2c_f32_bf16 v121, v42, v42\n\t" \
   "v_dot2c_f32_bf16 v120, s40, v43\n\t" "v_dot2c_f32_bf16 v121, v43, v43\n\t" "s_waitcnt vmcnt(15)\n\t" "ds_write_b64 v117, v[44:45] offset:2176\n\t" \
   "ds_write_b64 v117, v[46:47] offset:2432\n\t" "s_mov_b32 s41, 11392\n\t" "buffer_load_dwordx4 v[108:111], v116, %[rx], s41 offen\n\t" \
   "v_dot2c_f32_bf16 v122, s40, v44\n\t" "v_dot2c_f32_bf16 v123, v44, v44\n\t" "v_dot2c_f32_bf16 v122, s40, v45\n\t" \
   "v_dot2c_f32_bf16 v123, v45, v45\n\t" "v_dot2c_f32_bf16 v122, s40, v46\n\t" "v_dot2c_f32_bf16 v123, v46, v46\n\t" \
   "v_dot2c_f32_bf16 v122, s40, v47\n\t" "v_dot2c_f32_bf16 v123, v47, v47\n\t" "s_waitcnt vmcnt(15)\n\t" "ds_write_b64 v117, v[48:49] offset:4096\n\t" \
   "ds_write_b64 v117, v[50:51] offset:4352\n\t" "s_mov_b32 s41, 0\n\t" "buffer_load_dwordx4 a[0:3], v16, %[ra], s41 offen\n\t" \
   "v_dot2c_f32_bf16 v120, s40, v48\n\t" "v_dot2c_f32_bf16 v121, v48, v48\n\t" "v_dot2c_f32_bf16 v120, s40, v49\n\t" \
   "v_dot2c_f32_bf16 v121, v49, v49\n\t" "v_dot2c_f32_bf16 v120, s40, v50\n\t" "v_dot2c_f32_bf16 v121, v50, v50\n\t" \
   "v_dot2c_f32_bf16 v120, s40, v51\n\t" "v_dot2c_f32_bf16 v121, v51, v51\n\t" "s_waitcnt vmcnt(15)\n\t" "ds_write_b64 v117, v[52:53] offset:4224\n\t" \
   "ds_write_b64 v117, v[54:55] offset:4480\n\t" "s_mov_b32 s41, 131072\n\t" "buffer_load_dwordx4 a[4:7], v16, %[ra], s41 offen\n\t" \
   "v_dot2c_f32_bf16 v122, s40, v52\n\t" "v_dot2c_f32_bf16 v123, v52, v52\n\t" "v_dot2c_f32_bf16 v122, s40, v53\n\t" \
   "v_dot2c_f32_bf16 v123, v53, v53\n\t" "v_dot2c_f32_bf16 v122, s40, v54\n\t" "v_dot2c_f32_bf16 v123, v54, v54\n\t" \
   "v_dot2c_f32_bf16 v122, s40, v55\n\t" "v_dot2c_f32_bf16 v123, v55, v55\n\t" "s_waitcnt vmcnt(15)\n\t" "ds_write_b64 v117, v[56:57] offset:6144\n\t" \
   "ds_write_b64 v117, v[58:59] offset:6400\n\t" "s_mov_b32 s41, 262144\n\t" "buffer_load_dwordx4 a[8:11], v16, %[ra], s41 offen\n\t" \
   "v_dot2c_f32_bf16 v120, s40, v56\n\t" "v_dot2c_f32_bf16 v121, v56, v56\n\t" "v_dot2c_f32_bf16 v120, s40, v57\n\t" \
   "v_dot2c_f32_bf16 v121, v57, v57\n\t" "v_dot2c_f32_bf16 v120, s40, v58\n\t" "v_dot2c_f32_bf16 v121, v58, v58\n\t" \
   "v_dot2c_f32_bf16 v120, s40, v59\n\t" "v_dot2c_f32_bf16 v121, v59, v59\n\t" "s_waitcnt vmcnt(15)\n\t" "ds_write_b64 v117, v[60:61] offset:6272\n\t" \
   "ds_write_b64 v117, v[62:63] offset:6528\n\t" "s_mov_b32 s41, 393216\n\t" "buffer_load_dwordx4 a[12:15], v16, %[ra], s41 offen\n\t" \
   "v_dot2c_f32_bf16 v122, s40, v60\n\t" "v_dot2c_f32_bf16 v123, v60, v60\n\t" "v_dot2c_f32_bf16 v122, s40, v61\n\t" \
   "v_dot2c_f32_bf16 v123, v61, v61\n\t" "v_dot2c_f32_bf16 v122, s40, v62\n\t" "v_dot2c_f32_bf16 v123, v62, v62\n\t" \
   "v_dot2c_f32_bf16 v122, s40, v63\n\t" "v_dot2c_f32_bf16 v123, v63, v63\n\t" "s_waitcnt vmcnt(15)\n\t" "ds_write_b64 v117, v[64:65] offset:8192\n\t" \
   "ds_write_b64 v117, v[66:67] offset:8448\n\t" "s_mov_b32 s41, 524288\n\t" "buffer_load_dwordx4 a[16:19], v16, %[ra], s41 offen\n\t" \
   "v_dot2c_f32_bf16 v120, s40, v64\n\t" "v_dot2c_f32_bf16 v121, v64, v64\n\t" "v_dot2c_f32_bf16 v120, s40, v65\n\t" \
   "v_dot2c_f32_bf16 v121, v65, v65\n\t" "v_dot2c_f32_bf16 v120, s40, v66\n\t" "v_dot2c_f32_bf16 v121, v66, v66\n\t" \
   "v_dot2c_f32_bf16 v120, s40, v67\n\t" "v_dot2c_f32_bf16 v121, v67, v67\n\t" "s_waitcnt vmcnt(15)\n\t" "ds_write_b64 v117, v[68:69] offset:8320\n\t" \
   "ds_write_b64 v117, v[70:71] offset:8576\n\t" "s_mov_b32 s41, 655360\n\t" "buffer_load_dwordx4 a[20:23], v16, %[ra], s41 offen\n\t" \
   "v_dot2c_f32_bf16 v122, s40, v68\n\t" "v_dot2c_f32_bf16 v123, v68, v68\n\t" "v_dot2c_f32_bf16 v122, s40, v69\n\t" \
   "v_dot2c_f32_bf16 v123, v69, v69\n\t" "v_dot2c_f32_bf16 v122, s40, v70\n\t" "v_dot2c_f32_bf16 v123, v70, v70\n\t" \
   "v_dot2c_f32_bf16 v122, s40, v71\n\t" "v_dot2c_f32_bf16 v123, v71, v71\n\t" "s_waitcnt vmcnt(15)\n\t" "ds_write_b64 v117, v[72:73] offset:10240\n\t" \
   "ds_write_b64 v117, v[74:75] offset:10496\n\t" "s_mov_b32 s41, 786432\n\t" "buffer_load_dwordx4 a[24:27], v16, %[ra], s41 offen\n\t" \
   "v_dot2c_f32_bf16 v120, s40, v72\n\t" "v_dot2c_f32_bf16 v121, v72, v72\n\t" "v_dot2c_f32_bf16 v120, s40, v73\n\t" \
   "v_dot2c_f32_bf16 v121, v73, v73\n\t" "v_dot2c_f32_bf16 v120, s40, v74\n\t" "v_dot2c_f32_bf16 v121, v74, v74\n\t" \
   "v_dot2c_f32_bf16 v120, s40, v75\n\t" "v_dot2c_f32_bf16 v121, v75, v75\n\t" "s_waitcnt vmcnt(15)\n\t" "ds_write_b64 v117, v[76:77] offset:10368\n\t" \
   "ds_write_b64 v117, v[78:79] offset:10624\n\t" "s_mov_b32 s41, 917504\n\t" "buffer_load_dwordx4 a[28:31], v16, %[ra], s41 offen\n\t" \
   "v_dot2c_f32_bf16 v122, s40, v76\n\t" "v_dot2c_f32_bf16 v123, v76, v76\n\t" "v_dot2c_f32_bf16 v122, s40, v77\n\t" \
   "v_dot2c_f32_bf16 v123, v77, v77\n\t" "v_dot2c_f32_bf16 v122, s40, v78\n\t" "v_dot2c_f32_bf16 v123, v78, v78\n\t" \
   "v_dot2c_f32_bf16 v122, s40, v79\n\t" "v_dot2c_f32_bf16 v123, v79, v79\n\t" "s_waitcnt vmcnt(15)\n\t" "ds_write_b64 v117, v[80:81] offset:12288\n\t" \
   "ds_write_b64 v117, v[82:83] offset:12544\n\t" "s_mov_b32 s41, 1048576\n\t" "buffer_load_dwordx4 a[32:35], v16, %[ra], s41 offen\n\t" \
   "v_dot2c_f32_bf16 v120, s40, v80\n\t" "v_dot2c_f32_bf16 v121, v80, v80\n\t" "v_dot2c_f32_bf16 v120, s40, v81\n\t" \
   "v_dot2c_f32_bf16 v121, v81, v81\n\t" "v_dot2c_f32_bf16 v120, s40, v82\n\t" "v_dot2c_f32_bf16 v121, v82, v82\n\t" \
   "v_dot2c_f32_bf16 v120, s40, v83\n\t" "v_dot2c_f32_bf16 v121, v83, v83\n\t" "s_waitcnt vmcnt(15)\n\t" "ds_write_b64 v117, v[84:85] offset:12416\n\t" \
   "ds_write_b64 v117, v[86:87] offset:12672\n\t" "s_mov_b32 s41, 1179648\n\t" "buffer_load_dwordx4 a[36:39], v16, %[ra], s41 offen\n\t" \
   "v_dot2c_f32_bf16 v122, s40, v84\n\t" "v_dot2c_f32_bf16 v123, v84, v84\n\t" "v_dot2c_f32_bf16 v122, s40, v85\n\t" \
   "v_dot2c_f32_bf16 v123, v85, v85\n\t" "v_dot2c_f32_bf16 v122, s40, v86\n\t" "v_dot2c_f32_bf16 v123, v86, v86\n\t" \
   "v_dot2c_f32_bf16 v122, s40, v87\n\t" "v_dot2c_f32_bf16 v123, v87, v87\n\t" "s_waitcnt vmcnt(15)\n\t" "ds_write_b64 v117, v[88:89] offset:14336\n\t" \
   "ds_write_b64 v117, v[90:91] offset:14592\n\t" "s_mov_b32 s41, 1310720\n\t" "buffer_load_dwordx4 a[40:43], v16, %[ra], s41 offen\n\t" \
   "v_dot2c_f32_bf16 v120, s40, v88\n\t" "v_dot2c_f32_bf16 v121, v88, v88\n\t" "v_dot2c_f32_bf16 v120, s40, v89\n\t" \
   "v_dot2c_f32_bf16 v121, v89, v89\n\t" "v_dot2c_f32_bf16 v120, s40, v90\n\t" "v_dot2c_f32_bf16 v121, v90, v90\n\t" \
   "v_dot2c_f32_bf16 v120, s40, v91\n\t" "v_dot2c_f32_bf16 v121, v91, v91\n\t" "s_waitcnt vmcnt(15)\n\t" "ds_write_b64 v117, v[92:93] offset:14464\n\t" \
   "ds_write_b64 v117, v[94:95] offset:14720\n\t" "s_mov_b32 s41, 1441792\n\t" "buffer_load_dwordx4 a[44:47], v16, %[ra], s41 offen\n\t" \
   "v_dot2c_f32_bf16 v122, s40, v92\n\t" "v_dot2c_f32_bf16 v123, v92, v92\n\t" "v_dot2c_f32_bf16 v122, s40, v93\n\t" \
   "v_dot2c_f32_bf16 v123, v93, v93\n\t" "v_dot2c_f32_bf16 v122, s40, v94\n\t" "v_dot2c_f32_bf16 v123, v94, v94\n\t" \
   "v_dot2c_f32_bf16 v122, s40, v95\n\t" "v_dot2c_f32_bf16 v123, v95, v95\n\t" "s_waitcnt vmcnt(15)\n\t" "ds_write_b64 v117, v[96:97] offset:16384\n\t" \
   "ds_write_b64 v117, v[98:99] offset:16640\n\t" "s_mov_b32 s41, 1572864\n\t" "buffer_load_dwordx4 a[48:51], v16, %[ra], s41 offen\n\t" \
   "v_dot2c_f32_bf16 v120, s40, v96\n\t" "v_dot2c_f32_bf16 v121, v96, v96\n\t" "v_dot2c_f32_bf16 v120, s40, v97\n\t" \
   "v_dot2c_f32_bf16 v121, v97, v97\n\t" "v_dot2c_f32_bf16 v120, s40, v98\n\t" "v_dot2c_f32_bf16 v121, v98, v98\n\t" \
   "v_dot2c_f32_bf16 v120, s40, v99\n\t" "v_dot2c_f32_bf16 v121, v99, v99\n\t" "s_waitcnt vmcnt(15)\n\t" \
   "ds_write_b64 v117, v[100:101] offset:16512\n\t" "ds_write_b64 v117, v[102:103] offset:16768\n\t" "s_mov_b32 s41, 1703936\n\t" \
   "buffer_load_dwordx4 a[52:55], v16, %[ra], s41 offen\n\t" "v_dot2c_f32_bf16 v122, s40, v100\n\t" "v_dot2c_f32_bf16 v123, v100, v100\n\t" \
   "v_dot2c_f32_bf16 v122, s40, v101\n\t" "v_dot2c_f32_bf16 v123, v101, v101\n\t" "v_dot2c_f32_bf16 v122, s40, v102\n\t" \
   "v_dot2c_f32_bf16 v123, v102, v102\n\t" "v_dot2c_f32_bf16 v122, s40, v103\n\t" "v_dot2c_f32_bf16 v123, v103, v103\n\t" "s_waitcnt vmcnt(15)\n\t" \
   "ds_write_b64 v117, v[104:105] offset:18432\n\t" "ds_write_b64 v117, v[106:107] offset:18688\n\t" "s_mov_b32 s41, 1835008\n\t" \
   "buffer_load_dwordx4 a[56:59], v16, %[ra], s41 offen\n\t" "v_dot2c_f32_bf16 v120, s40, v104\n\t" "v_dot2c_f32_bf16 v121, v104, v104\n\t" \
   "v_dot2c_f32_bf16 v120, s40, v105\n\t" "v_dot2c_f32_bf16 v121, v105, v105\n\t" "v_dot2c_f32_bf16 v120, s40, v106\n\t" \
   "v_dot2c_f32_bf16 v121, v106, v106\n\t" "v_dot2c_f32_bf16 v120, s40, v107\n\t" "v_dot2c_f32_bf16 v121, v107, v107\n\t" "s_waitcnt vmcnt(15)\n\t" \
   "ds_write_b64 v117, v[108:109] offset:18560\n\t" "ds_write_b64 v117, v[110:111] offset:18816\n\t" "s_mov_b32 s41, 1966080\n\t" \
   "buffer_load_dwordx4 a[60:63], v16, %[ra], s41 offen\n\t" "v_dot2c_f32_bf16 v122, s40, v108\n\t" "v_dot2c_f32_bf16 v123, v108, v108\n\t" \
   "v_dot2c_f32_bf16 v122, s40, v109\n\t" "v_dot2c_f32_bf16 v123, v109, v109\n\t" "v_dot2c_f32_bf16 v122, s40, v110\n\t" \
   "v_dot2c_f32_bf16 v123, v110, v110\n\t" "v_dot2c_f32_bf16 v122, s40, v111\n\t" "v_dot2c_f32_bf16 v123, v111, v111\n\t" "s_nop 3\n\t" \
   "v_add_f32_dpp v120, v120, v120 quad_perm:[1,0,3,2] row_mask:0xf bank_mask:0xf\n\t" \
   "v_add_f32_dpp v121, v121, v121 quad_perm:[1,0,3,2] row_mask:0xf bank_mask:0xf\n\t" \
   "v_add_f32_dpp v122, v122, v122 quad_perm:[1,0,3,2] row_mask:0xf bank_mask:0xf\n\t" \
   "v_add_f32_dpp v123, v123, v123 quad_perm:[1,0,3,2] row_mask:0xf bank_mask:0xf\n\t" \
   "v_add_f32_dpp v120, v120, v120 quad_perm:[2,3,0,1] row_mask:0xf bank_mask:0xf\n\t" \
   "v_add_f32_dpp v121, v121, v121 quad_perm:[2,3,0,1] row_mask:0xf bank_mask:0xf\n\t" \
   "v_add_f32_dpp v122, v122, v122 quad_perm:[2,3,0,1] row_mask:0xf bank_mask:0xf\n\t" \
   "v_add_f32_dpp v123, v123, v123 quad_perm:[2,3,0,1] row_mask:0xf bank_mask:0xf\n\t" \
   "v_add_f32_dpp v120, v120, v120 row_half_mirror row_mask:0xf bank_mask:0xf\n\t" \
   "v_add_f32_dpp v121, v121, v121 row_half_mirror row_mask:0xf bank_mask:0xf\n\t" \
   "v_add_f32_dpp v122, v122, v122 row_half_mirror row_mask:0xf bank_mask:0xf\n\t" \
   "v_add_f32_dpp v123, v123, v123 row_half_mirror row_mask:0xf bank_mask:0xf\n\t" "v_and_b32 v120, v119, v120\n\t" "v_and_b32 v121, v119, v121\n\t" \
   "v_and_b32 v122, v119, v122\n\t" "v_and_b32 v123, v119, v123\n\t" "ds_write_b64 v118, v[120:121]\n\t" "ds_write_b64 v118, v[122:123] offset:384\n\t" \
   "s_waitcnt vmcnt(15)\n\t" "ds_write_b128 v20, a[0:3] offset:0\n\t" "s_mov_b32 s41, 2097152\n\t" \
   "buffer_load_dwordx4 a[64:67], v16, %[ra], s41 offen\n\t" "s_waitcnt vmcnt(15)\n\t" "ds_write_b128 v20, a[4:7] offset:4096\n\t" \
   "s_mov_b32 s41, 2228224\n\t" "buffer_load_dwordx4 a[68:71], v16, %[ra], s41 offen\n\t" "s_waitcnt vmcnt(15)\n\t" \
   "ds_write_b128 v20, a[8:11] offset:8192\n\t" "s_mov_b32 s41, 2359296\n\t" "buffer_load_dwordx4 a[72:75], v16, %[ra], s41 offen\n\t" \
   "s_waitcnt vmcnt(15)\n\t" "ds_write_b128 v20, a[12:15] offset:12288\n\t" "s_mov_b32 s41, 2490368\n\t" \
   "buffer_load_dwordx4 a[76:79], v16, %[ra], s41 offen\n\t" "s_waitcnt vmcnt(15)\n\t" "ds_write_b128 v20, a[16:19] offset:16384\n\t" \
   "s_mov_b32 s41, 2621440\n\t" "buffer_load_dwordx4 a[80:83], v16, %[ra], s41 offen\n\t" "s_waitcnt vmcnt(15)\n\t" \
   "ds_write_b128 v20, a[20:23] offset:20480\n\t" "s_mov_b32 s41, 2752512\n\t" "buffer_load_dwordx4 a[84:87], v16, %[ra], s41 offen\n\t" \
   "s_waitcnt vmcnt(15)\n\t" "ds_write_b128 v20, a[24:27] offset:24576\n\t" "s_mov_b32 s41, 2883584\n\t" \
   "buffer_load_dwordx4 a[88:91], v16, %[ra], s41 offen\n\t" "s_waitcnt vmcnt(15)\n\t" "ds_write_b128 v20, a[28:31] offset:28672\n\t" \
   "s_mov_b32 s41, 3014656\n\t" "buffer_load_dwordx4 a[92:95], v16, %[ra], s41 offen\n\t" "s_waitcnt vmcnt(15)\n\t" \
   "ds_write_b128 v20, a[32:35] offset:32768\n\t" "s_mov_b32 s41, 3145728\n\t" "buffer_load_dwordx4 a[96:99], v16, %[ra], s41 offen\n\t" \
   "s_waitcnt vmcnt(15)\n\t" "ds_write_b128 v20, a[36:39] offset:36864\n\t" "s_mov_b32 s41, 3276800\n\t" \
   "buffer_load_dwordx4 a[100:103], v16, %[ra], s41 offen\n\t" "s_waitcnt lgkmcnt(0)\n\t" "s_barrier\n\t" LDSTAMP(2) \
   "ds_read_b64_tr_b16 a[0:1], v18 offset:0\n\t" "ds_read_b64_tr_b16 a[2:3], v18 offset:4096\n\t" "ds_read_b64_tr_b16 a[4:5], v19 offset:0\n\t" \
   "ds_read_b64_tr_b16 a[6:7], v19 offset:4096\n\t" "ds_read_b64_tr_b16 a[8:9], v18 offset:8192\n\t" \
   "ds_read_b64_tr_b16 a[10:11], v18 offset:12288\n\t" "ds_read_b64_tr_b16 a[12:13], v19 offset:8192\n\t" \
   "ds_read_b64_tr_b16 a[14:15], v19 offset:12288\n\t" "ds_read_b64_tr_b16 a[16:17], v18 offset:16384\n\t" \
   "ds_read_b64_tr_b16 a[18:19], v18 offset:20480\n\t" "ds_read_b64_tr_b16 a[20:21], v19 offset:16384\n\t" \
   "ds_read_b64_tr_b16 a[22:23], v19 offset:20480\n\t" "ds_read_b64_tr_b16 a[24:25], v18 offset:24576\n\t" \
   "ds_read_b64_tr_b16 a[26:27], v18 offset:28672\n\t" "ds_read_b64_tr_b16 a[28:29], v19 offset:24576\n\t" \
   "ds_read_b64_tr_b16 a[30:31], v19 offset:28672\n\t" "ds_read_b64_tr_b16 a[32:33], v18 offset:32768\n\t" \
   "ds_read_b64_tr_b16 a[34:35], v18 offset:36864\n\t" "ds_read_b64_tr_b16 a[36:37], v19 offset:32768\n\t" \
   "ds_read_b64_tr_b16 a[38:39], v19 offset:36864\n\t" "s_waitcnt vmcnt(15)\n\t" "s_waitcnt lgkmcnt(0)\n\t" "s_barrier\n\t" \
   "ds_write_b128 v20, a[40:43] offset:0\n\t" "s_mov_b32 s41, 3407872\n\t" "buffer_load_dwordx4 a[104:107], v16, %[ra], s41 offen\n\t" \
   "s_waitcnt vmcnt(15)\n\t" "ds_write_b128 v20, a[44:47] offset:4096\n\t" "s_mov_b32 s41, 3538944\n\t" \
   "buffer_load_dwordx4 a[108:111], v16, %[ra], s41 offen\n\t" "s_waitcnt vmcnt(15)\n\t" "ds_write_b128 v20, a[48:51] offset:8192\n\t" \
   "s_mov_b32 s41, 3670016\n\t" "buffer_load_dwordx4 a[112:115], v16, %[ra], s41 offen\n\t" "s_waitcnt vmcnt(15)\n\t" \
   "ds_write_b128 v20, a[52:55] offset:12288\n\t" "s_mov_b32 s41, 3801088\n\t" "buffer_load_dwordx4 a[116:119], v16, %[ra], s41 offen\n\t" \
   "s_waitcnt vmcnt(15)\n\t" "ds_write_b128 v20, a[56:59] offset:16384\n\t" "s_mov_b32 s41, 3932160\n\t" \
   "buffer_load_dwordx4 a[120:123], v16, %[ra], s41 offen\n\t" "s_waitcnt vmcnt(15)\n\t" "ds_write_b128 v20, a[60:63] offset:20480\n\t" \
   "s_mov_b32 s41, 4063232\n\t" "buffer_load_dwordx4 a[124:127], v16, %[ra], s41 offen\n\t" "s_waitcnt vmcnt(15)\n\t" \
   "ds_write_b128 v20, a[64:67] offset:24576\n\t" "s_mov_b32 s41, 4194304\n\t" "buffer_load_dwordx4 a[128:131], v16, %[ra], s41 offen\n\t" \
   "s_waitcnt vmcnt(15)\n\t" "ds_write_b128 v20, a[68:71] offset:28672\n\t" "s_mov_b32 s41, 4325376\n\t" \
   "buffer_load_dwordx4 a[132:135], v16, %[ra], s41 offen\n\t" "s_waitcnt vmcnt(15)\n\t" "ds_write_b128 v20, a[72:75] offset:32768\n\t" \
   "s_mov_b32 s41, 4456448\n\t" "buffer_load_dwordx4 a[136:139], v16, %[ra], s41 offen\n\t" "s_waitcnt vmcnt(15)\n\t" \
   "ds_write_b128 v20, a[76:79] offset:36864\n\t" "s_mov_b32 s41, 4587520\n\t" "buffer_load_dwordx4 a[140:143], v16, %[ra], s41 offen\n\t" \
   "s_waitcnt lgkmcnt(0)\n\t" "s_barrier\n\t" LDSTAMP(3) "ds_read_b64_tr_b16 a[40:41], v18 offset:0\n\t" \
   "ds_read_b64_tr_b16 a[42:43], v18 offset:4096\n\t" "ds_read_b64_tr_b16 a[44:45], v19 offset:0\n\t" \
   "ds_read_b64_tr_b16 a[46:47], v19 offset:4096\n\t" "ds_read_b64_tr_b16 a[48:49], v18 offset:8192\n\t" \
   "ds_read_b64_tr_b16 a[50:51], v18 offset:12288\n\t" "ds_read_b64_tr_b16 a[52:53], v19 offset:8192\n\t" \
   "ds_read_b64_tr_b16 a[54:55], v19 offset:12288\n\t" "ds_read_b64_tr_b16 a[56:57], v18 offset:16384\n\t" \
   "ds_read_b64_tr_b16 a[58:59], v18 offset:20480\n\t" "ds_read_b64_tr_b16 a[60:61], v19 offset:16384\n\t" \
   "ds_read_b64_tr_b16 a[62:63], v19 offset:20480\n\t" "ds_read_b64_tr_b16 a[64:65], v18 offset:24576\n\t" \
   "ds_read_b64_tr_b16 a[66:67], v18 offset:28672\n\t" "ds_read_b64_tr_b16 a[68:69], v19 offset:24576\n\t" \
   "ds_read_b64_tr_b16 a[70:71], v19 offset:28672\n\t" "ds_read_b64_tr_b16 a[72:73], v18 offset:32768\n\t" \
   "ds_read_b64_tr_b16 a[74:75], v18 offset:36864\n\t" "ds_read_b64_tr_b16 a[76:77], v19 offset:32768\n\t" \
   "ds_read_b64_tr_b16 a[78:79], v19 offset:36864\n\t" "s_waitcnt vmcnt(15)\n\t" "s_waitcnt lgkmcnt(0)\n\t" "s_barrier\n\t" \
   "ds_write_b128 v20, a[80:83] offset:0\n\t" "s_mov_b32 s41, 4718592\n\t" "buffer_load_dwordx4 a[144:147], v16, %[ra], s41 offen\n\t" \
   "s_waitcnt vmcnt(15)\n\t" "ds_write_b128 v20, a[84:87] offset:4096\n\t" "s_mov_b32 s41, 4849664\n\t" \
   "buffer_load_dwordx4 a[148:151], v16, %[ra], s41 offen\n\t" "s_waitcnt vmcnt(15)\n\t" "ds_write_b128 v20, a[88:91] offset:8192\n\t" \
   "s_mov_b32 s41, 4980736\n\t" "buffer_load_dwordx4 a[152:155], v16, %[ra], s41 offen\n\t" "s_waitcnt vmcnt(15)\n\t" \
   "ds_write_b128 v20, a[92:95] offset:12288\n\t" "s_mov_b32 s41, 5111808\n\t" "buffer_load_dwordx4 a[156:159], v16, %[ra], s41 offen\n\t" \
   "s_waitcnt vmcnt(15)\n\t" "ds_write_b128 v20, a[96:99] offset:16384\n\t" "s_mov_b32 s41, 0\n\t" \
   "buffer_load_dwordx4 a[160:163], v17, %[rb], s41 offen\n\t" "s_waitcnt vmcnt(15)\n\t" "ds_write_b128 v20, a[100:103] offset:20480\n\t" \
   "s_mov_b32 s41, 256\n\t" "buffer_load_dwordx4 a[176:179], v17, %[rb], s41 offen\n\t" "s_waitcnt vmcnt(15)\n\t" \
   "ds_write_b128 v20, a[104:107] offset:24576\n\t" "s_mov_b32 s41, 512\n\t" "buffer_load_dwordx4 a[192:195], v17, %[rb], s41 offen\n\t" \
   "s_waitcnt vmcnt(15)\n\t" "ds_write_b128 v20, a[108:111] offset:28672\n\t" "s_mov_b32 s41, 768\n\t" \
   "buffer_load_dwordx4 a[208:211], v17, %[rb], s41 offen\n\t" "s_waitcnt vmcnt(15)\n\t" "ds_write_b128 v20, a[112:115] offset:32768\n\t" \
   "s_mov_b32 s41, 1024\n\t" "buffer_load_dwordx4 a[224:227], v17, %[rb], s41 offen\n\t" "s_waitcnt vmcnt(15)\n\t" \
   "ds_write_b128 v20, a[116:119] offset:36864\n\t" "s_mov_b32 s41, 20480\n\t" "buffer_load_dwordx4 a[240:243], v17, %[rb], s41 offen\n\t" \
   "s_waitcnt lgkmcnt(0)\n\t" "s_barrier\n\t" LDSTAMP(4) "ds_read_b64_tr_b16 a[80:81], v18 offset:0\n\t" \
   "ds_read_b64_tr_b16 a[82:83], v18 offset:4096\n\t" "ds_read_b64_tr_b16 a[84:85], v19 offset:0\n\t" \
   "ds_read_b64_tr_b16 a[86:87], v19 offset:4096\n\t" "ds_read_b64_tr_b16 a[88:89], v18 offset:8192\n\t" \
   "ds_read_b64_tr_b16 a[90:91], v18 offset:12288\n\t" "ds_read_b64_tr_b16 a[92:93], v19 offset:8192\n\t" \
   "ds_read_b64_tr_b16 a[94:95], v19 offset:12288\n\t" "ds_read_b64_tr_b16 a[96:97], v18 offset:16384\n\t" \
   "ds_read_b64_tr_b16 a[98:99], v18 offset:20480\n\t" "ds_read_b64_tr_b16 a[100:101], v19 offset:16384\n\t" \
   "ds_read_b64_tr_b16 a[102:103], v19 offset:20480\n\t" "ds_read_b64_tr_b16 a[104:105], v18 offset:24576\n\t" \
   "ds_read_b64_tr_b16 a[106:107], v18 offset:28672\n\t" "ds_read_b64_tr_b16 a[108:109], v19 offset:24576\n\t" \
   "ds_read_b64_tr_b16 a[110:111], v19 offset:28672\n\t" "ds_read_b64_tr_b16 a[112:113], v18 offset:32768\n\t" \
   "ds_read_b64_tr_b16 a[114:115], v18 offset:36864\n\t" "ds_read_b64_tr_b16 a[116:117], v19 offset:32768\n\t" \
   "ds_read_b64_tr_b16 a[118:119], v19 offset:36864\n\t" "s_waitcnt vmcnt(15)\n\t" "s_waitcnt lgkmcnt(0)\n\t" "s_barrier\n\t" \
   "ds_write_b128 v20, a[120:123] offset:0\n\t" "s_mov_b32 s41, 20736\n\t" "buffer_load_dwordx4 v[192:195], v17, %[rb], s41 offen\n\t" \
   "s_waitcnt vmcnt(15)\n\t" "ds_write_b128 v20, a[124:127] offset:4096\n\t" "s_mov_b32 s41, 20992\n\t" \
   "buffer_load_dwordx4 v[208:211], v17, %[rb], s41 offen\n\t" "s_waitcnt vmcnt(15)\n\t" "ds_write_b128 v20, a[128:131] offset:8192\n\t" \
   "s_mov_b32 s41, 21248\n\t" "buffer_load_dwordx4 v[224:227], v17, %[rb], s41 offen\n\t" "s_waitcnt vmcnt(15)\n\t" \
   "ds_write_b128 v20, a[132:135] offset:12288\n\t" "s_mov_b32 s41, 21504\n\t" "buffer_load_dwordx4 v[240:243], v17, %[rb], s41 offen\n\t" \
   "s_waitcnt vmcnt(15)\n\t" "ds_write_b128 v20, a[136:139] offset:16384\n\t" "s_mov_b32 s41, 40960\n\t" \
   "buffer_load_dwordx4 a[164:167], v17, %[rb], s41 offen\n\t" "s_waitcnt vmcnt(15)\n\t" "ds_write_b128 v20, a[140:143] offset:20480\n\t" \
   "s_mov_b32 s41, 41216\n\t" "buffer_load_dwordx4 a[180:183], v17, %[rb], s41 offen\n\t" "s_waitcnt vmcnt(15)\n\t" \
   "ds_write_b128 v20, a[144:147] offset:24576\n\t" "s_mov_b32 s41, 41472\n\t" "buffer_load_dwordx4 a[196:199], v17, %[rb], s41 offen\n\t" \
   "s_waitcnt vmcnt(15)\n\t" "ds_write_b128 v20, a[148:151] offset:28672\n\t" "s_mov_b32 s41, 41728\n\t" \
   "buffer_load_dwordx4 a[212:215], v17, %[rb], s41 offen\n\t" "s_waitcnt vmcnt(15)\n\t" "ds_write_b128 v20, a[152:155] offset:32768\n\t" \
   "s_mov_b32 s41, 41984\n\t" "buffer_load_dwordx4 a[228:231], v17, %[rb], s41 offen\n\t" "s_waitcnt vmcnt(15)\n\t" \
   "ds_write_b128 v20, a[156:159] offset:36864\n\t" "s_mov_b32 s41, 61440\n\t" "buffer_load_dwordx4 a[244:247], v17, %[rb], s41 offen\n\t" \
   "s_waitcnt lgkmcnt(0)\n\t" "s_barrier\n\t" LDSTAMP(5) "ds_read_b64_tr_b16 a[120:121], v18 offset:0\n\t" \
   "ds_read_b64_tr_b16 a[122:123], v18 offset:4096\n\t" "ds_read_b64_tr_b16 a[124:125], v19 offset:0\n\t" \
   "ds_read_b64_tr_b16 a[126:127], v19 offset:4096\n\t" "ds_read_b64_tr_b16 a[128:129], v18 offset:8192\n\t" \
   "ds_read_b64_tr_b16 a[130:131], v18 offset:12288\n\t" "ds_read_b64_tr_b16 a[132:133], v19 offset:8192\n\t" \
   "ds_read_b64_tr_b16 a[134:135], v19 offset:12288\n\t" "ds_read_b64_tr_b16 a[136:137], v18 offset:16384\n\t" \
   "ds_read_b64_tr_b16 a[138:139], v18 offset:20480\n\t" "ds_read_b64_tr_b16 a[140:141], v19 offset:16384\n\t" \
   "ds_read_b64_tr_b16 a[142:143], v19 offset:20480\n\t" "ds_read_b64_tr_b16 a[144:145], v18 offset:24576\n\t" \
   "ds_read_b64_tr_b16 a[146:147], v18 offset:28672\n\t" "ds_read_b64_tr_b16 a[148:149], v19 offset:24576\n\t" \
   "ds_read_b64_tr_b16 a[150:151], v19 offset:28672\n\t" "ds_read_b64_tr_b16 a[152:153], v18 offset:32768\n\t" \
   "ds_read_b64_tr_b16 a[154:155], v18 offset:36864\n\t" "ds_read_b64_tr_b16 a[156:157], v19 offset:32768\n\t" \
   "ds_read_b64_tr_b16 a[158:159], v19 offset:36864\n\t" "s_waitcnt vmcnt(15)\n\t" "s_waitcnt lgkmcnt(0)\n\t" "s_barrier\n\t" \
   "ds_write_b32 v21, a160 offset:0\n\t" "ds_write_b32 v21, a161 offset:21120\n\t" "ds_write_b32 v21, a162 offset:10560\n\t" \
   "ds_write_b32 v21, a163 offset:31680\n\t" "s_mov_b32 s41, 61696\n\t" "buffer_load_dwordx4 v[196:199], v17, %[rb], s41 offen\n\t" \
   "s_waitcnt vmcnt(15)\n\t" "ds_write_b32 v21, a176 offset:64\n\t" "ds_write_b32 v21, a177 offset:21184\n\t" "ds_write_b32 v21, a178 offset:10624\n\t" \
   "ds_write_b32 v21, a179 offset:31744\n\t" "s_mov_b32 s41, 61952\n\t" "buffer_load_dwordx4 v[212:215], v17, %[rb], s41 offen\n\t" \
   "s_waitcnt vmcnt(15)\n\t" "ds_write_b32 v21, a192 offset:128\n\t" "ds_write_b32 v21, a193 offset:21248\n\t" \
   "ds_write_b32 v21, a194 offset:10688\n\t" "ds_write_b32 v21, a195 offset:31808\n\t" "s_mov_b32 s41, 62208\n\t" \
   "buffer_load_dwordx4 v[228:231], v17, %[rb], s41 offen\n\t" "s_waitcnt vmcnt(15)\n\t" "ds_write_b32 v21, a208 offset:192\n\t" \
   "ds_write_b32 v21, a209 offset:21312\n\t" "ds_write_b32 v21, a210 offset:10752\n\t" "ds_write_b32 v21, a211 offset:31872\n\t" \
   "s_mov_b32 s41, 62464\n\t" "buffer_load_dwordx4 v[244:247], v17, %[rb], s41 offen\n\t" "s_waitcnt vmcnt(15)\n\t" \
   "ds_write_b32 v21, a224 offset:256\n\t" "ds_write_b32 v21, a225 offset:21376\n\t" "ds_write_b32 v21, a226 offset:10816\n\t" \
   "ds_write_b32 v21, a227 offset:31936\n\t" "s_mov_b32 s41, 81920\n\t" "buffer_load_dwordx4 a[168:171], v17, %[rb], s41 offen\n\t" \
   "s_waitcnt vmcnt(15)\n\t" "ds_write_b32 v21, a240 offset:5248\n\t" "ds_write_b32 v21, a241 offset:26368\n\t" \
   "ds_write_b32 v21, a242 offset:15808\n\t" "ds_write_b32 v21, a243 offset:36928\n\t" "s_mov_b32 s41, 82176\n\t" \
   "buffer_load_dwordx4 a[184:187], v17, %[rb], s41 offen\n\t" "s_waitcnt vmcnt(15)\n\t" "ds_write_b32 v21, v192 offset:5312\n\t" \
   "ds_write_b32 v21, v193 offset:26432\n\t" "ds_write_b32 v21, v194 offset:15872\n\t" "ds_write_b32 v21, v195 offset:36992\n\t" \
   "s_mov_b32 s41, 82432\n\t" "buffer_load_dwordx4 a[200:203], v17, %[rb], s41 offen\n\t" "s_waitcnt vmcnt(15)\n\t" \
   "ds_write_b32 v21, v208 offset:5376\n\t" "ds_write_b32 v21, v209 offset:26496\n\t" "ds_write_b32 v21, v210 offset:15936\n\t" \
   "ds_write_b32 v21, v211 offset:37056\n\t" "s_mov_b32 s41, 82688\n\t" "buffer_load_dwordx4 a[216:219], v17, %[rb], s41 offen\n\t" \
   "s_waitcnt vmcnt(15)\n\t" "ds_write_b32 v21, v224 offset:5440\n\t" "ds_write_b32 v21, v225 offset:26560\n\t" \
   "ds_write_b32 v21, v226 offset:16000\n\t" "ds_write_b32 v21, v227 offset:37120\n\t" "s_mov_b32 s41, 82944\n\t" \
   "buffer_load_dwordx4 a[232:235], v17, %[rb], s41 offen\n\t" "s_waitcnt vmcnt(15)\n\t" "ds_write_b32 v21, v240 offset:5504\n\t" \
   "ds_write_b32 v21, v241 offset:26624\n\t" "ds_write_b32 v21, v242 offset:16064\n\t" "ds_write_b32 v21, v243 offset:37184\n\t" \
   "s_mov_b32 s41, 102400\n\t" "buffer_load_dwordx4 a[248:251], v17, %[rb], s41 offen\n\t" "s_waitcnt lgkmcnt(0)\n\t" "s_barrier\n\t" LDSTAMP(6) \
   "ds_read_b64_tr_b16 a[160:161], v22 offset:0\n\t" "ds_read_b64_tr_b16 a[162:163], v22 offset:2624\n\t" \
   "ds_read_b64_tr_b16 a[176:177], v22 offset:5248\n\t" "ds_read_b64_tr_b16 a[178:179], v22 offset:7872\n\t" \
   "ds_read_b64_tr_b16 a[192:193], v22 offset:16\n\t" "ds_read_b64_tr_b16 a[194:195], v22 offset:2640\n\t" \
   "ds_read_b64_tr_b16 a[208:209], v22 offset:5264\n\t" "ds_read_b64_tr_b16 a[210:211], v22 offset:7888\n\t" \
   "ds_read_b64_tr_b16 a[224:225], v22 offset:32\n\t" "ds_read_b64_tr_b16 a[226:227], v22 offset:2656\n\t" \
   "ds_read_b64_tr_b16 a[240:241], v22 offset:5280\n\t" "ds_read_b64_tr_b16 a[242:243], v22 offset:7904\n\t" \
   "ds_read_b64_tr_b16 v[192:193], v22 offset:48\n\t" "ds_read_b64_tr_b16 v[194:195], v22 offset:2672\n\t" \
   "ds_read_b64_tr_b16 v[208:209], v22 offset:5296\n\t" "ds_read_b64_tr_b16 v[210:211], v22 offset:7920\n\t" \
   "ds_read_b64_tr_b16 v[224:225], v22 offset:64\n\t" "ds_read_b64_tr_b16 v[226:227], v22 offset:2688\n\t" \
   "ds_read_b64_tr_b16 v[240:241], v22 offset:5312\n\t" "ds_read_b64_tr_b16 v[242:243], v22 offset:7936\n\t" "s_waitcnt vmcnt(15)\n\t" \
   "s_waitcnt lgkmcnt(0)\n\t" "s_barrier\n\t" "ds_write_b32 v21, a164 offset:0\n\t" "ds_write_b32 v21, a165 offset:21120\n\t" \
   "ds_write_b32 v21, a166 offset:10560\n\t" "ds_write_b32 v21, a167 offset:31680\n\t" "s_mov_b32 s41, 102656\n\t" \
   "buffer_load_dwordx4 v[200:203], v17, %[rb], s41 offen\n\t" "s_waitcnt vmcnt(15)\n\t" "ds_write_b32 v21, a180 offset:64\n\t" \
   "ds_write_b32 v21, a181 offset:21184\n\t" "ds_write_b32 v21, a182 offset:10624\n\t" "ds_write_b32 v21, a183 offset:31744\n\t" \
   "s_mov_b32 s41, 102912\n\t" "buffer_load_dwordx4 v[216:219], v17, %[rb], s41 offen\n\t" "s_waitcnt vmcnt(15)\n\t" \
   "ds_write_b32 v21, a196 offset:128\n\t" "ds_write_b32 v21, a197 offset:21248\n\t" "ds_write_b32 v21, a198 offset:10688\n\t" \
   "ds_write_b32 v21, a199 offset:31808\n\t" "s_mov_b32 s41, 103168\n\t" "buffer_load_dwordx4 v[232:235], v17, %[rb], s41 offen\n\t" \
   "s_waitcnt vmcnt(15)\n\t" "ds_write_b32 v21, a212 offset:192\n\t" "ds_write_b32 v21, a213 offset:21312\n\t" \
   "ds_write_b32 v21, a214 offset:10752\n\t" "ds_write_b32 v21, a215 offset:31872\n\t" "s_mov_b32 s41, 103424\n\t" \
   "buffer_load_dwordx4 v[248:251], v17, %[rb], s41 offen\n\t" "s_waitcnt vmcnt(15)\n\t" "ds_write_b32 v21, a228 offset:256\n\t" \
   "ds_write_b32 v21, a229 offset:21376\n\t" "ds_write_b32 v21, a230 offset:10816\n\t" "ds_write_b32 v21, a231 offset:31936\n\t" \
   "s_mov_b32 s41, 122880\n\t" "buffer_load_dwordx4 a[172:175], v17, %[rb], s41 offen\n\t" "s_waitcnt vmcnt(15)\n\t" \
   "ds_write_b32 v21, a244 offset:5248\n\t" "ds_write_b32 v21, a245 offset:26368\n\t" "ds_write_b32 v21, a246 offset:15808\n\t" \
   "ds_write_b32 v21, a247 offset:36928\n\t" "s_mov_b32 s41, 123136\n\t" "buffer_load_dwordx4 a[188:191], v17, %[rb], s41 offen\n\t" \
   "s_waitcnt vmcnt(15)\n\t" "ds_write_b32 v21, v196 offset:5312\n\t" "ds_write_b32 v21, v197 offset:26432\n\t" \
   "ds_write_b32 v21, v198 offset:15872\n\t" "ds_write_b32 v21, v199 offset:36992\n\t" "s_mov_b32 s41, 123392\n\t" \
   "buffer_load_dwordx4 a[204:207], v17, %[rb], s41 offen\n\t" "s_waitcnt vmcnt(15)\n\t" "ds_write_b32 v21, v212 offset:5376\n\t" \
   "ds_write_b32 v21, v213 offset:26496\n\t" "ds_write_b32 v21, v214 offset:15936\n\t" "ds_write_b32 v21, v215 offset:37056\n\t" \
   "s_mov_b32 s41, 123648\n\t" "buffer_load_dwordx4 a[220:223], v17, %[rb], s41 offen\n\t" "s_waitcnt vmcnt(15)\n\t" \
   "ds_write_b32 v21, v228 offset:5440\n\t" "ds_write_b32 v21, v229 offset:26560\n\t" "ds_write_b32 v21, v230 offset:16000\n\t" \
   "ds_write_b32 v21, v231 offset:37120\n\t" "s_mov_b32 s41, 123904\n\t" "buffer_load_dwordx4 a[236:239], v17, %[rb], s41 offen\n\t" \
   "s_waitcnt vmcnt(15)\n\t" "ds_write_b32 v21, v244 offset:5504\n\t" "ds_write_b32 v21, v245 offset:26624\n\t" \
   "ds_write_b32 v21, v246 offset:16064\n\t" "ds_write_b32 v21, v247 offset:37184\n\t" "s_mov_b32 s41, 143360\n\t" \
   "buffer_load_dwordx4 a[252:255], v17, %[rb], s41 offen\n\t" "s_waitcnt lgkmcnt(0)\n\t" "s_barrier\n\t" LDSTAMP(7) \
   "ds_read_b64_tr_b16 a[164:165], v22 offset:0\n\t" "ds_read_b64_tr_b16 a[166:167], v22 offset:2624\n\t" \
   "ds_read_b64_tr_b16 a[180:181], v22 offset:5248\n\t" "ds_read_b64_tr_b16 a[182:183], v22 offset:7872\n\t" \
   "ds_read_b64_tr_b16 a[196:197], v22 offset:16\n\t" "ds_read_b64_tr_b16 a[198:199], v22 offset:2640\n\t" \
   "ds_read_b64_tr_b16 a[212:213], v22 offset:5264\n\t" "ds_read_b64_tr_b16 a[214:215], v22 offset:7888\n\t" \
   "ds_read_b64_tr_b16 a[228:229], v22 offset:32\n\t" "ds_read_b64_tr_b16 a[230:231], v22 offset:2656\n\t" \
   "ds_read_b64_tr_b16 a[244:245], v22 offset:5280\n\t" "ds_read_b64_tr_b16 a[246:247], v22 offset:7904\n\t" \
   "ds_read_b64_tr_b16 v[196:197], v22 offset:48\n\t" "ds_read_b64_tr_b16 v[198:199], v22 offset:2672\n\t" \
   "ds_read_b64_tr_b16 v[212:213], v22 offset:5296\n\t" "ds_read_b64_tr_b16 v[214:215], v22 offset:7920\n\t" \
   "ds_read_b64_tr_b16 v[228:229], v22 offset:64\n\t" "ds_read_b64_tr_b16 v[230:231], v22 offset:2688\n\t" \
   "ds_read_b64_tr_b16 v[244:245], v22 offset:5312\n\t" "ds_read_b64_tr_b16 v[246:247], v22 offset:7936\n\t" "s_waitcnt vmcnt(15)\n\t" \
   "s_waitcnt lgkmcnt(0)\n\t" "s_barrier\n\t" "ds_write_b32 v21, a168 offset:0\n\t" "ds_write_b32 v21, a169 offset:21120\n\t" \
   "ds_write_b32 v21, a170 offset:10560\n\t" "ds_write_b32 v21, a171 offset:31680\n\t" "s_mov_b32 s41, 143616\n\t" \
   "buffer_load_dwordx4 v[204:207], v17, %[rb], s41 offen\n\t" "s_waitcnt vmcnt(15)\n\t" "ds_write_b32 v21, a184 offset:64\n\t" \
   "ds_write_b32 v21, a185 offset:21184\n\t" "ds_write_b32 v21, a186 offset:10624\n\t" "ds_write_b32 v21, a187 offset:31744\n\t" \
   "s_mov_b32 s41, 143872\n\t" "buffer_load_dwordx4 v[220:223], v17, %[rb], s41 offen\n\t" "s_waitcnt vmcnt(15)\n\t" \
   "ds_write_b32 v21, a200 offset:128\n\t" "ds_write_b32 v21, a201 offset:21248\n\t" "ds_write_b32 v21, a202 offset:10688\n\t" \
   "ds_write_b32 v21, a203 offset:31808\n\t" "s_mov_b32 s41, 144128\n\t" "buffer_load_dwordx4 v[236:239], v17, %[rb], s41 offen\n\t" \
   "s_waitcnt vmcnt(15)\n\t" "ds_write_b32 v21, a216 offset:192\n\t" "ds_write_b32 v21, a217 offset:21312\n\t" \
   "ds_write_b32 v21, a218 offset:10752\n\t" "ds_write_b32 v21, a219 offset:31872\n\t" "s_mov_b32 s41, 144384\n\t" \
   "buffer_load_dwordx4 v[252:255], v17, %[rb], s41 offen\n\t" "s_waitcnt vmcnt(15)\n\t" "ds_write_b32 v21, a232 offset:256\n\t" \
   "ds_write_b32 v21, a233 offset:21376\n\t" "ds_write_b32 v21, a234 offset:10816\n\t" "ds_write_b32 v21, a235 offset:31936\n\t" \
   "s_waitcnt vmcnt(14)\n\t" "ds_write_b32 v21, a248 offset:5248\n\t" "ds_write_b32 v21, a249 offset:26368\n\t" \
   "ds_write_b32 v21, a250 offset:15808\n\t" "ds_write_b32 v21, a251 offset:36928\n\t" "s_waitcnt vmcnt(13)\n\t" \
   "ds_write_b32 v21, v200 offset:5312\n\t" "ds_write_b32 v21, v201 offset:26432\n\t" "ds_write_b32 v21, v202 offset:15872\n\t" \
   "ds_write_b32 v21, v203 offset:36992\n\t" "s_waitcnt vmcnt(12)\n\t" "ds_write_b32 v21, v216 offset:5376\n\t" \
   "ds_write_b32 v21, v217 offset:26496\n\t" "ds_write_b32 v21, v218 offset:15936\n\t" "ds_write_b32 v21, v219 offset:37056\n\t" \
   "s_waitcnt vmcnt(11)\n\t" "ds_write_b32 v21, v232 offset:5440\n\t" "ds_write_b32 v21, v233 offset:26560\n\t" \
   "ds_write_b32 v21, v234 offset:16000\n\t" "ds_write_b32 v21, v235 offset:37120\n\t" "s_waitcnt vmcnt(10)\n\t" \
   "ds_write_b32 v21, v248 offset:5504\n\t" "ds_write_b32 v21, v249 offset:26624\n\t" "ds_write_b32 v21, v250 offset:16064\n\t" \
   "ds_write_b32 v21, v251 offset:37184\n\t" "buffer_load_dwordx4 v[28:31], v112, %[rw], 0 offen sc1\n\t" "s_waitcnt lgkmcnt(0)\n\t" "s_barrier\n\t" \
   LDSTAMP(8) "ds_read_b64_tr_b16 a[168:169], v22 offset:0\n\t" "ds_read_b64_tr_b16 a[170:171], v22 offset:2624\n\t" \
   "ds_read_b64_tr_b16 a[184:185], v22 offset:5248\n\t" "ds_read_b64_tr_b16 a[186:187], v22 offset:7872\n\t" \
   "ds_read_b64_tr_b16 a[200:201], v22 offset:16\n\t" "ds_read_b64_tr_b16 a[202:203], v22 offset:2640\n\t" \
   "ds_read_b64_tr_b16 a[216:217], v22 offset:5264\n\t" "ds_read_b64_tr_b16 a[218:219], v22 offset:7888\n\t" \
   "ds_read_b64_tr_b16 a[232:233], v22 offset:32\n\t" "ds_read_b64_tr_b16 a[234:235], v22 offset:2656\n\t" \
   "ds_read_b64_tr_b16 a[248:249], v22 offset:5280\n\t" "ds_read_b64_tr_b16 a[250:251], v22 offset:7904\n\t" \
   "ds_read_b64_tr_b16 v[200:201], v22 offset:48\n\t" "ds_read_b64_tr_b16 v[202:203], v22 offset:2672\n\t" \
   "ds_read_b64_tr_b16 v[216:217], v22 offset:5296\n\t" "ds_read_b64_tr_b16 v[218:219], v22 offset:7920\n\t" \
   "ds_read_b64_tr_b16 v[232:233], v22 offset:64\n\t" "ds_read_b64_tr_b16 v[234:235], v22 offset:2688\n\t" \
   "ds_read_b64_tr_b16 v[248:249], v22 offset:5312\n\t" "ds_read_b64_tr_b16 v[250:251], v22 offset:7936\n\t" "s_waitcnt vmcnt(10)\n\t" \
   "s_waitcnt lgkmcnt(0)\n\t" "s_barrier\n\t" "ds_write_b32 v21, a172 offset:0\n\t" "ds_write_b32 v21, a173 offset:21120\n\t" \
   "ds_write_b32 v21, a174 offset:10560\n\t" "ds_write_b32 v21, a175 offset:31680\n\t" "s_waitcnt vmcnt(9)\n\t" "ds_write_b32 v21, a188 offset:64\n\t" \
   "ds_write_b32 v21, a189 offset:21184\n\t" "ds_write_b32 v21, a190 offset:10624\n\t" "ds_write_b32 v21, a191 offset:31744\n\t" \
   "s_waitcnt vmcnt(8)\n\t" "ds_write_b32 v21, a204 offset:128\n\t" "ds_write_b32 v21, a205 offset:21248\n\t" "ds_write_b32 v21, a206 offset:10688\n\t" \
   "ds_write_b32 v21, a207 offset:31808\n\t" "s_waitcnt vmcnt(7)\n\t" "ds_write_b32 v21, a220 offset:192\n\t" "ds_write_b32 v21, a221 offset:21312\n\t" \
   "ds_write_b32 v21, a222 offset:10752\n\t" "ds_write_b32 v21, a223 offset:31872\n\t" "s_waitcnt vmcnt(6)\n\t" "ds_write_b32 v21, a236 offset:256\n\t" \
   "ds_write_b32 v21, a237 offset:21376\n\t" "ds_write_b32 v21, a238 offset:10816\n\t" "ds_write_b32 v21, a239 offset:31936\n\t" \
   "s_waitcnt vmcnt(5)\n\t" "ds_write_b32 v21, a252 offset:5248\n\t" "ds_write_b32 v21, a253 offset:26368\n\t" \
   "ds_write_b32 v21, a254 offset:15808\n\t" "ds_write_b32 v21, a255 offset:36928\n\t" "s_waitcnt vmcnt(4)\n\t" \
   "ds_write_b32 v21, v204 offset:5312\n\t" "ds_write_b32 v21, v205 offset:26432\n\t" "ds_write_b32 v21, v206 offset:15872\n\t" \
   "ds_write_b32 v21, v207 offset:36992\n\t" "s_waitcnt vmcnt(3)\n\t" "ds_write_b32 v21, v220 offset:5376\n\t" \
   "ds_write_b32 v21, v221 offset:26496\n\t" "ds_write_b32 v21, v222 offset:15936\n\t" "ds_write_b32 v21, v223 offset:37056\n\t" \
   "s_waitcnt vmcnt(2)\n\t" "ds_write_b32 v21, v236 offset:5440\n\t" "ds_write_b32 v21, v237 offset:26560\n\t" \
   "ds_write_b32 v21, v238 offset:16000\n\t" "ds_write_b32 v21, v239 offset:37120\n\t" "s_waitcnt vmcnt(1)\n\t" \
   "ds_write_b32 v21, v252 offset:5504\n\t" "ds_write_b32 v21, v253 offset:26624\n\t" "ds_write_b32 v21, v254 offset:16064\n\t" \
   "ds_write_b32 v21, v255 offset:37184\n\t" "s_waitcnt lgkmcnt(0)\n\t" "s_barrier\n\t" LDSTAMP(9) "ds_read_b64_tr_b16 a[172:173], v22 offset:0\n\t" \
   "ds_read_b64_tr_b16 a[174:175], v22 offset:2624\n\t" "ds_read_b64_tr_b16 a[188:189], v22 offset:5248\n\t" \
   "ds_read_b64_tr_b16 a[190:191], v22 offset:7872\n\t" "ds_read_b64_tr_b16 a[204:205], v22 offset:16\n\t" \
   "ds_read_b64_tr_b16 a[206:207], v22 offset:2640\n\t" "ds_read_b64_tr_b16 a[220:221], v22 offset:5264\n\t" \
   "ds_read_b64_tr_b16 a[222:223], v22 offset:7888\n\t" "ds_read_b64_tr_b16 a[236:237], v22 offset:32\n\t" \
   "ds_read_b64_tr_b16 a[238:239], v22 offset:2656\n\t" "ds_read_b64_tr_b16 a[252:253], v22 offset:5280\n\t" \
   "ds_read_b64_tr_b16 a[254:255], v22 offset:7904\n\t" "ds_read_b64_tr_b16 v[204:205], v22 offset:48\n\t" \
   "ds_read_b64_tr_b16 v[206:207], v22 offset:2672\n\t" "ds_read_b64_tr_b16 v[220:221], v22 offset:5296\n\t" \
   "ds_read_b64_tr_b16 v[222:223], v22 offset:7920\n\t" "ds_read_b64_tr_b16 v[236:237], v22 offset:64\n\t" \
   "ds_read_b64_tr_b16 v[238:239], v22 offset:2688\n\t" "ds_read_b64_tr_b16 v[252:253], v22 offset:5312\n\t" \
   "ds_read_b64_tr_b16 v[254:255], v22 offset:7936\n\t" "s_waitcnt vmcnt(0) lgkmcnt(0)\n\t" "s_barrier"
#define LDV_CLOB "v24", "v25", "v26", "v32", "v33", "v34", "v35", "v36", "v37", "v38", "v39", "v40", "v41", "v42", "v43", "v44", "v45", "v46", "v47", "v48", "v49", "v50", "v51", "v52", "v53", "v54", "v55", "v56", "v57", "v58", "v59", "v60", "v61", "v62", "v63", "v64", "v65", "v66", "v67", "v68", "v69", "v70", "v71", "v72", "v73", "v74", "v75", "v76", "v77", "v78", "v79", "v80", "v81", "v82", "v83", "v84", "v85", "v86", "v87", "v88", "v89", "v90", "v91", "v92", "v93", "v94", "v95", "v96", "v97", "v98", "v99", "v100", "v101", "v102", "v103", "v104", "v105", "v106", "v107", "v108", "v109", "v110", "v111", "v120", "v121", "v122", "v123", "s40", "s41"
#define LDY_ASM \
   "buffer_load_dword v26, v113, %[rw], 0 offen sc1\n\t" "s_mov_b32 s41, 0\n\t" "buffer_load_dwordx4 a[0:3], v16, %[ra], s41 offen\n\t" \
   "s_mov_b32 s41, 131072\n\t" "buffer_load_dwordx4 a[64:67], v16, %[ra], s41 offen\n\t" "s_mov_b32 s41, 524288\n\t" \
   "buffer_load_dwordx4 a[8:11], v16, %[ra], s41 offen\n\t" "s_mov_b32 s41, 655360\n\t" "buffer_load_dwordx4 a[72:75], v16, %[ra], s41 offen\n\t" \
   "s_mov_b32 s41, 1048576\n\t" "buffer_load_dwordx4 a[16:19], v16, %[ra], s41 offen\n\t" "s_mov_b32 s41, 1179648\n\t" \
   "buffer_load_dwordx4 a[80:83], v16, %[ra], s41 offen\n\t" "s_mov_b32 s41, 1572864\n\t" "buffer_load_dwordx4 a[24:27], v16, %[ra], s41 offen\n\t" \
   "s_mov_b32 s41, 1703936\n\t" "buffer_load_dwordx4 a[88:91], v16, %[ra], s41 offen\n\t" "s_mov_b32 s41, 2097152\n\t" \
   "buffer_load_dwordx4 a[32:35], v16, %[ra], s41 offen\n\t" "s_mov_b32 s41, 2228224\n\t" "buffer_load_dwordx4 a[96:99], v16, %[ra], s41 offen\n\t" \
   "s_mov_b32 s41, 2621440\n\t" "buffer_load_dwordx4 a[40:43], v16, %[ra], s41 offen\n\t" "s_mov_b32 s41, 2752512\n\t" \
   "buffer_load_dwordx4 a[104:107], v16, %[ra], s41 offen\n\t" "s_mov_b32 s41, 3145728\n\t" "buffer_load_dwordx4 a[48:51], v16, %[ra], s41 offen\n\t" \
   "s_mov_b32 s41, 3276800\n\t" "buffer_load_dwordx4 a[112:115], v16, %[ra], s41 offen\n\t" "s_mov_b32 s41, 3670016\n\t" \
   "buffer_load_dwordx4 a[56:59], v16, %[ra], s41 offen\n\t" "s_mov_b32 s41, 3801088\n\t" "buffer_load_dwordx4 a[120:123], v16, %[ra], s41 offen\n\t" \
   "s_waitcnt vmcnt(16)\n\t" "v_add_u32 v26, 1, v26\n\t" "buffer_store_dword v26, v114, %[rw], 0 offen sc1\n\t" "ds_write_b32 v115, v26\n\t" LDSTAMP(0) \
   "s_waitcnt vmcnt(15)\n\t" LDSTAMP(1) "ds_write_b128 v20, a[0:3] offset:0\n\t" "s_mov_b32 s41, 4194304\n\t" \
   "buffer_load_dwordx4 v[192:195], v16, %[ra], s41 offen\n\t" "s_waitcnt vmcnt(15)\n\t" "ds_write_b128 v20, a[64:67] offset:4096\n\t" \
   "s_mov_b32 s41, 4325376\n\t" "buffer_load_dwordx4 v[208:211], v16, %[ra], s41 offen\n\t" "s_waitcnt vmcnt(15)\n\t" \
   "ds_write_b128 v20, a[8:11] offset:8192\n\t" "s_mov_b32 s41, 4718592\n\t" "buffer_load_dwordx4 v[200:203], v16, %[ra], s41 offen\n\t" \
   "s_waitcnt vmcnt(15)\n\t" "ds_write_b128 v20, a[72:75] offset:12288\n\t" "s_mov_b32 s41, 4849664\n\t" \
   "buffer_load_dwordx4 v[216:219], v16, %[ra], s41 offen\n\t" "s_waitcnt vmcnt(15)\n\t" "ds_write_b128 v20, a[16:19] offset:16384\n\t" \
   "s_mov_b32 s41, 262144\n\t" "buffer_load_dwordx4 a[4:7], v16, %[ra], s41 offen\n\t" "s_waitcnt vmcnt(15)\n\t" \
   "ds_write_b128 v20, a[80:83] offset:20480\n\t" "s_mov_b32 s41, 393216\n\t" "buffer_load_dwordx4 a[68:71], v16, %[ra], s41 offen\n\t" \
   "s_waitcnt vmcnt(15)\n\t" "ds_write_b128 v20, a[24:27] offset:24576\n\t" "s_mov_b32 s41, 786432\n\t" \
   "buffer_load_dwordx4 a[12:15], v16, %[ra], s41 offen\n\t" "s_waitcnt vmcnt(15)\n\t" "ds_write_b128 v20, a[88:91] offset:28672\n\t" \
   "s_mov_b32 s41, 917504\n\t" "buffer_load_dwordx4 a[76:79], v16, %[ra], s41 offen\n\t" "s_waitcnt vmcnt(15)\n\t" \
   "ds_write_b128 v20, a[32:35] offset:32768\n\t" "s_mov_b32 s41, 1310720\n\t" "buffer_load_dwordx4 a[20:23], v16, %[ra], s41 offen\n\t" \
   "s_waitcnt vmcnt(15)\n\t" "ds_write_b128 v20, a[96:99] offset:36864\n\t" "s_mov_b32 s41, 1441792\n\t" \
   "buffer_load_dwordx4 a[84:87], v16, %[ra], s41 offen\n\t" "s_waitcnt vmcnt(15)\n\t" "ds_write_b128 v20, a[40:43] offset:40960\n\t" \
   "s_mov_b32 s41, 1835008\n\t" "buffer_load_dwordx4 a[28:31], v16, %[ra], s41 offen\n\t" "s_waitcnt vmcnt(15)\n\t" \
   "ds_write_b128 v20, a[104:107] offset:45056\n\t" "s_mov_b32 s41, 1966080\n\t" "buffer_load_dwordx4 a[92:95], v16, %[ra], s41 offen\n\t" \
   "s_waitcnt vmcnt(15)\n\t" "ds_write_b128 v20, a[48:51] offset:49152\n\t" "s_mov_b32 s41, 2359296\n\t" \
   "buffer_load_dwordx4 a[36:39], v16, %[ra], s41 offen\n\t" "s_waitcnt vmcnt(15)\n\t" "ds_write_b128 v20, a[112:115] offset:53248\n\t" \
   "s_mov_b32 s41, 2490368\n\t" "buffer_load_dwordx4 a[100:103], v16, %[ra], s41 offen\n\t" "s_waitcnt vmcnt(15)\n\t" \
   "ds_write_b128 v20, a[56:59] offset:57344\n\t" "s_mov_b32 s41, 2883584\n\t" "buffer_load_dwordx4 a[44:47], v16, %[ra], s41 offen\n\t" \
   "s_waitcnt vmcnt(15)\n\t" "ds_write_b128 v20, a[120:123] offset:61440\n\t" "s_mov_b32 s41, 3014656\n\t" \
   "buffer_load_dwordx4 a[108:111], v16, %[ra], s41 offen\n\t" "s_waitcnt vmcnt(15)\n\t" "ds_write_b128 v23, v[192:195] offset:0\n\t" \
   "s_mov_b32 s41, 3407872\n\t" "buffer_load_dwordx4 a[52:55], v16, %[ra], s41 offen\n\t" "s_waitcnt vmcnt(15)\n\t" \
   "ds_write_b128 v23, v[208:211] offset:4096\n\t" "s_mov_b32 s41, 3538944\n\t" "buffer_load_dwordx4 a[116:119], v16, %[ra], s41 offen\n\t" \
   "s_waitcnt vmcnt(15)\n\t" "ds_write_b128 v23, v[200:203] offset:8192\n\t" "s_mov_b32 s41, 3932160\n\t" \
   "buffer_load_dwordx4 a[60:63], v16, %[ra], s41 offen\n\t" "s_waitcnt vmcnt(15)\n\t" "ds_write_b128 v23, v[216:219] offset:12288\n\t" \
   "s_mov_b32 s41, 4063232\n\t" "buffer_load_dwordx4 a[124:127], v16, %[ra], s41 offen\n\t" "s_waitcnt lgkmcnt(0)\n\t" "s_barrier\n\t" LDSTAMP(2) \
   "s_lshl_b32 s40, %[p0], 13\n\t" "v_add_u32 v24, s40, v18\n\t" "v_add_u32 v25, s40, v19\n\t" "s_add_u32 s40, s40, 8192\n\t" \
   "s_cmp_eq_u32 s40, 81920\n\t" "s_cselect_b32 s40, 0, s40\n\t" "ds_read_b64_tr_b16 a[0:1], v24 offset:0\n\t" \
   "ds_read_b64_tr_b16 a[2:3], v24 offset:4096\n\t" "ds_read_b64_tr_b16 a[64:65], v25 offset:0\n\t" "ds_read_b64_tr_b16 a[66:67], v25 offset:4096\n\t" \
   "v_add_u32 v24, s40, v18\n\t" "v_add_u32 v25, s40, v19\n\t" "s_add_u32 s40, s40, 8192\n\t" "s_cmp_eq_u32 s40, 81920\n\t" \
   "s_cselect_b32 s40, 0, s40\n\t" "ds_read_b64_tr_b16 a[8:9], v24 offset:0\n\t" "ds_read_b64_tr_b16 a[10:11], v24 offset:4096\n\t" \
   "ds_read_b64_tr_b16 a[72:73], v25 offset:0\n\t" "ds_read_b64_tr_b16 a[74:75], v25 offset:4096\n\t" "v_add_u32 v24, s40, v18\n\t" \
   "v_add_u32 v25, s40, v19\n\t" "s_add_u32 s40, s40, 8192\n\t" "s_cmp_eq_u32 s40, 81920\n\t" "s_cselect_b32 s40, 0, s40\n\t" \
   "ds_read_b64_tr_b16 a[16:17], v24 offset:0\n\t" "ds_read_b64_tr_b16 a[18:19], v24 offset:4096\n\t" "ds_read_b64_tr_b16 a[80:81], v25 offset:0\n\t" \
   "ds_read_b64_tr_b16 a[82:83], v25 offset:4096\n\t" "v_add_u32 v24, s40, v18\n\t" "v_add_u32 v25, s40, v19\n\t" "s_add_u32 s40, s40, 8192\n\t" \
   "s_cmp_eq_u32 s40, 81920\n\t" "s_cselect_b32 s40, 0, s40\n\t" "ds_read_b64_tr_b16 a[24:25], v24 offset:0\n\t" \
   "ds_read_b64_tr_b16 a[26:27], v24 offset:4096\n\t" "ds_read_b64_tr_b16 a[88:89], v25 offset:0\n\t" \
   "ds_read_b64_tr_b16 a[90:91], v25 offset:4096\n\t" "v_add_u32 v24, s40, v18\n\t" "v_add_u32 v25, s40, v19\n\t" "s_add_u32 s40, s40, 8192\n\t" \
   "s_cmp_eq_u32 s40, 81920\n\t" "s_cselect_b32 s40, 0, s40\n\t" "ds_read_b64_tr_b16 a[32:33], v24 offset:0\n\t" \
   "ds_read_b64_tr_b16 a[34:35], v24 offset:4096\n\t" "ds_read_b64_tr_b16 a[96:97], v25 offset:0\n\t" \
   "ds_read_b64_tr_b16 a[98:99], v25 offset:4096\n\t" "v_add_u32 v24, s40, v18\n\t" "v_add_u32 v25, s40, v19\n\t" "s_add_u32 s40, s40, 8192\n\t" \
   "s_cmp_eq_u32 s40, 81920\n\t" "s_cselect_b32 s40, 0, s40\n\t" "ds_read_b64_tr_b16 a[40:41], v24 offset:0\n\t" \
   "ds_read_b64_tr_b16 a[42:43], v24 offset:4096\n\t" "ds_read_b64_tr_b16 a[104:105], v25 offset:0\n\t" \
   "ds_read_b64_tr_b16 a[106:107], v25 offset:4096\n\t" "v_add_u32 v24, s40, v18\n\t" "v_add_u32 v25, s40, v19\n\t" "s_add_u32 s40, s40, 8192\n\t" \
   "s_cmp_eq_u32 s40, 81920\n\t" "s_cselect_b32 s40, 0, s40\n\t" "ds_read_b64_tr_b16 a[48:49], v24 offset:0\n\t" \
   "ds_read_b64_tr_b16 a[50:51], v24 offset:4096\n\t" "ds_read_b64_tr_b16 a[112:113], v25 offset:0\n\t" \
   "ds_read_b64_tr_b16 a[114:115], v25 offset:4096\n\t" "v_add_u32 v24, s40, v18\n\t" "v_add_u32 v25, s40, v19\n\t" "s_add_u32 s40, s40, 8192\n\t" \
   "s_cmp_eq_u32 s40, 81920\n\t" "s_cselect_b32 s40, 0, s40\n\t" "ds_read_b64_tr_b16 a[56:57], v24 offset:0\n\t" \
   "ds_read_b64_tr_b16 a[58:59], v24 offset:4096\n\t" "ds_read_b64_tr_b16 a[120:121], v25 offset:0\n\t" \
   "ds_read_b64_tr_b16 a[122:123], v25 offset:4096\n\t" "v_add_u32 v24, s40, v18\n\t" "v_add_u32 v25, s40, v19\n\t" "s_add_u32 s40, s40, 8192\n\t" \
   "s_cmp_eq_u32 s40, 81920\n\t" "s_cselect_b32 s40, 0, s40\n\t" "ds_read_b64_tr_b16 v[192:193], v24 offset:0\n\t" \
   "ds_read_b64_tr_b16 v[194:195], v24 offset:4096\n\t" "ds_read_b64_tr_b16 v[208:209], v25 offset:0\n\t" \
   "ds_read_b64_tr_b16 v[210:211], v25 offset:4096\n\t" "v_add_u32 v24, s40, v18\n\t" "v_add_u32 v25, s40, v19\n\t" "s_add_u32 s40, s40, 8192\n\t" \
   "s_cmp_eq_u32 s40, 81920\n\t" "s_cselect_b32 s40, 0, s40\n\t" "ds_read_b64_tr_b16 v[200:201], v24 offset:0\n\t" \
   "ds_read_b64_tr_b16 v[202:203], v24 offset:4096\n\t" "ds_read_b64_tr_b16 v[216:217], v25 offset:0\n\t" \
   "ds_read_b64_tr_b16 v[218:219], v25 offset:4096\n\t" "s_waitcnt vmcnt(15)\n\t" "s_waitcnt lgkmcnt(0)\n\t" "s_barrier\n\t" \
   "ds_write_b128 v20, a[4:7] offset:0\n\t" "s_mov_b32 s41, 4456448\n\t" "buffer_load_dwordx4 v[196:199], v16, %[ra], s41 offen\n\t" \
   "s_waitcnt vmcnt(15)\n\t" "ds_write_b128 v20, a[68:71] offset:4096\n\t" "s_mov_b32 s41, 4587520\n\t" \
   "buffer_load_dwordx4 v[212:215], v16, %[ra], s41 offen\n\t" "s_waitcnt vmcnt(15)\n\t" "ds_write_b128 v20, a[12:15] offset:8192\n\t" \
   "s_mov_b32 s41, 4980736\n\t" "buffer_load_dwordx4 v[204:207], v16, %[ra], s41 offen\n\t" "s_waitcnt vmcnt(15)\n\t" \
   "ds_write_b128 v20, a[76:79] offset:12288\n\t" "s_mov_b32 s41, 5111808\n\t" "buffer_load_dwordx4 v[220:223], v16, %[ra], s41 offen\n\t" \
   "s_waitcnt vmcnt(15)\n\t" "ds_write_b128 v20, a[20:23] offset:16384\n\t" "s_mov_b32 s41, 0\n\t" \
   "buffer_load_dwordx4 a[128:131], v17, %[rb], s41 offen\n\t" "s_waitcnt vmcnt(15)\n\t" "ds_write_b128 v20, a[84:87] offset:20480\n\t" \
   "s_mov_b32 s41, 256\n\t" "buffer_load_dwordx4 a[132:135], v17, %[rb], s41 offen\n\t" "s_waitcnt vmcnt(15)\n\t" \
   "ds_write_b128 v20, a[28:31] offset:24576\n\t" "s_mov_b32 s41, 512\n\t" "buffer_load_dwordx4 a[160:163], v17, %[rb], s41 offen\n\t" \
   "s_waitcnt vmcnt(15)\n\t" "ds_write_b128 v20, a[92:95] offset:28672\n\t" "s_mov_b32 s41, 768\n\t" \
   "buffer_load_dwordx4 a[164:167], v17, %[rb], s41 offen\n\t" "s_waitcnt vmcnt(15)\n\t" "ds_write_b128 v20, a[36:39] offset:32768\n\t" \
   "s_mov_b32 s41, 1024\n\t" "buffer_load_dwordx4 a[192:195], v17, %[rb], s41 offen\n\t" "s_waitcnt vmcnt(15)\n\t" \
   "ds_write_b128 v20, a[100:103] offset:36864\n\t" "s_mov_b32 s41, 20480\n\t" "buffer_load_dwordx4 a[196:199], v17, %[rb], s41 offen\n\t" \
   "s_waitcnt vmcnt(15)\n\t" "ds_write_b128 v20, a[44:47] offset:40960\n\t" "s_mov_b32 s41, 20736\n\t" \
   "buffer_load_dwordx4 a[224:227], v17, %[rb], s41 offen\n\t" "s_waitcnt vmcnt(15)\n\t" "ds_write_b128 v20, a[108:111] offset:45056\n\t" \
   "s_mov_b32 s41, 20992\n\t" "buffer_load_dwordx4 a[228:231], v17, %[rb], s41 offen\n\t" "s_waitcnt vmcnt(15)\n\t" \
   "ds_write_b128 v20, a[52:55] offset:49152\n\t" "s_mov_b32 s41, 21248\n\t" "buffer_load_dwordx4 v[224:227], v17, %[rb], s41 offen\n\t" \
   "s_waitcnt vmcnt(15)\n\t" "ds_write_b128 v20, a[116:119] offset:53248\n\t" "s_mov_b32 s41, 21504\n\t" \
   "buffer_load_dwordx4 v[228:231], v17, %[rb], s41 offen\n\t" "s_waitcnt vmcnt(15)\n\t" "ds_write_b128 v20, a[60:63] offset:57344\n\t" \
   "s_mov_b32 s41, 40960\n\t" "buffer_load_dwordx4 a[136:139], v17, %[rb], s41 offen\n\t" "s_waitcnt vmcnt(15)\n\t" \
   "ds_write_b128 v20, a[124:127] offset:61440\n\t" "s_mov_b32 s41, 41216\n\t" "buffer_load_dwordx4 a[140:143], v17, %[rb], s41 offen\n\t" \
   "s_waitcnt vmcnt(15)\n\t" "ds_write_b128 v23, v[196:199] offset:0\n\t" "s_mov_b32 s41, 41472\n\t" \
   "buffer_load_dwordx4 a[168:171], v17, %[rb], s41 offen\n\t" "s_waitcnt vmcnt(15)\n\t" "ds_write_b128 v23, v[212:215] offset:4096\n\t" \
   "s_mov_b32 s41, 41728\n\t" "buffer_load_dwordx4 a[172:175], v17, %[rb], s41 offen\n\t" "s_waitcnt vmcnt(15)\n\t" \
   "ds_write_b128 v23, v[204:207] offset:8192\n\t" "s_mov_b32 s41, 41984\n\t" "buffer_load_dwordx4 a[200:203], v17, %[rb], s41 offen\n\t" \
   "s_waitcnt vmcnt(15)\n\t" "ds_write_b128 v23, v[220:223] offset:12288\n\t" "s_mov_b32 s41, 61440\n\t" \
   "buffer_load_dwordx4 a[204:207], v17, %[rb], s41 offen\n\t" "s_waitcnt lgkmcnt(0)\n\t" "s_barrier\n\t" LDSTAMP(3) "s_lshl_b32 s40, %[p0], 13\n\t" \
   "v_add_u32 v24, s40, v18\n\t" "v_add_u32 v25, s40, v19\n\t" "s_add_u32 s40, s40, 8192\n\t" "s_cmp_eq_u32 s40, 81920\n\t" \
   "s_cselect_b32 s40, 0, s40\n\t" "ds_read_b64_tr_b16 a[4:5], v24 offset:0\n\t" "ds_read_b64_tr_b16 a[6:7], v24 offset:4096\n\t" \
   "ds_read_b64_tr_b16 a[68:69], v25 offset:0\n\t" "ds_read_b64_tr_b16 a[70:71], v25 offset:4096\n\t" "v_add_u32 v24, s40, v18\n\t" \
   "v_add_u32 v25, s40, v19\n\t" "s_add_u32 s40, s40, 8192\n\t" "s_cmp_eq_u32 s40, 81920\n\t" "s_cselect_b32 s40, 0, s40\n\t" \
   "ds_read_b64_tr_b16 a[12:13], v24 offset:0\n\t" "ds_read_b64_tr_b16 a[14:15], v24 offset:4096\n\t" "ds_read_b64_tr_b16 a[76:77], v25 offset:0\n\t" \
   "ds_read_b64_tr_b16 a[78:79], v25 offset:4096\n\t" "v_add_u32 v24, s40, v18\n\t" "v_add_u32 v25, s40, v19\n\t" "s_add_u32 s40, s40, 8192\n\t" \
   "s_cmp_eq_u32 s40, 81920\n\t" "s_cselect_b32 s40, 0, s40\n\t" "ds_read_b64_tr_b16 a[20:21], v24 offset:0\n\t" \
   "ds_read_b64_tr_b16 a[22:23], v24 offset:4096\n\t" "ds_read_b64_tr_b16 a[84:85], v25 offset:0\n\t" \
   "ds_read_b64_tr_b16 a[86:87], v25 offset:4096\n\t" "v_add_u32 v24, s40, v18\n\t" "v_add_u32 v25, s40, v19\n\t" "s_add_u32 s40, s40, 8192\n\t" \
   "s_cmp_eq_u32 s40, 81920\n\t" "s_cselect_b32 s40, 0, s40\n\t" "ds_read_b64_tr_b16 a[28:29], v24 offset:0\n\t" \
   "ds_read_b64_tr_b16 a[30:31], v24 offset:4096\n\t" "ds_read_b64_tr_b16 a[92:93], v25 offset:0\n\t" \
   "ds_read_b64_tr_b16 a[94:95], v25 offset:4096\n\t" "v_add_u32 v24, s40, v18\n\t" "v_add_u32 v25, s40, v19\n\t" "s_add_u32 s40, s40, 8192\n\t" \
   "s_cmp_eq_u32 s40, 81920\n\t" "s_cselect_b32 s40, 0, s40\n\t" "ds_read_b64_tr_b16 a[36:37], v24 offset:0\n\t" \
   "ds_read_b64_tr_b16 a[38:39], v24 offset:4096\n\t" "ds_read_b64_tr_b16 a[100:101], v25 offset:0\n\t" \
   "ds_read_b64_tr_b16 a[102:103], v25 offset:4096\n\t" "v_add_u32 v24, s40, v18\n\t" "v_add_u32 v25, s40, v19\n\t" "s_add_u32 s40, s40, 8192\n\t" \
   "s_cmp_eq_u32 s40, 81920\n\t" "s_cselect_b32 s40, 0, s40\n\t" "ds_read_b64_tr_b16 a[44:45], v24 offset:0\n\t" \
   "ds_read_b64_tr_b16 a[46:47], v24 offset:4096\n\t" "ds_read_b64_tr_b16 a[108:109], v25 offset:0\n\t" \
   "ds_read_b64_tr_b16 a[110:111], v25 offset:4096\n\t" "v_add_u32 v24, s40, v18\n\t" "v_add_u32 v25, s40, v19\n\t" "s_add_u32 s40, s40, 8192\n\t" \
   "s_cmp_eq_u32 s40, 81920\n\t" "s_cselect_b32 s40, 0, s40\n\t" "ds_read_b64_tr_b16 a[52:53], v24 offset:0\n\t" \
   "ds_read_b64_tr_b16 a[54:55], v24 offset:4096\n\t" "ds_read_b64_tr_b16 a[116:117], v25 offset:0\n\t" \
   "ds_read_b64_tr_b16 a[118:119], v25 offset:4096\n\t" "v_add_u32 v24, s40, v18\n\t" "v_add_u32 v25, s40, v19\n\t" "s_add_u32 s40, s40, 8192\n\t" \
   "s_cmp_eq_u32 s40, 81920\n\t" "s_cselect_b32 s40, 0, s40\n\t" "ds_read_b64_tr_b16 a[60:61], v24 offset:0\n\t" \
   "ds_read_b64_tr_b16 a[62:63], v24 offset:4096\n\t" "ds_read_b64_tr_b16 a[124:125], v25 offset:0\n\t" \
   "ds_read_b64_tr_b16 a[126:127], v25 offset:4096\n\t" "v_add_u32 v24, s40, v18\n\t" "v_add_u32 v25, s40, v19\n\t" "s_add_u32 s40, s40, 8192\n\t" \
   "s_cmp_eq_u32 s40, 81920\n\t" "s_cselect_b32 s40, 0, s40\n\t" "ds_read_b64_tr_b16 v[196:197], v24 offset:0\n\t" \
   "ds_read_b64_tr_b16 v[198:199], v24 offset:4096\n\t" "ds_read_b64_tr_b16 v[212:213], v25 offset:0\n\t" \
   "ds_read_b64_tr_b16 v[214:215], v25 offset:4096\n\t" "v_add_u32 v24, s40, v18\n\t" "v_add_u32 v25, s40, v19\n\t" "s_add_u32 s40, s40, 8192\n\t" \
   "s_cmp_eq_u32 s40, 81920\n\t" "s_cselect_b32 s40, 0, s40\n\t" "ds_read_b64_tr_b16 v[204:205], v24 offset:0\n\t" \
   "ds_read_b64_tr_b16 v[206:207], v24 offset:4096\n\t" "ds_read_b64_tr_b16 v[220:221], v25 offset:0\n\t" \
   "ds_read_b64_tr_b16 v[222:223], v25 offset:4096\n\t" "s_waitcnt vmcnt(15)\n\t" "s_waitcnt lgkmcnt(0)\n\t" "s_barrier\n\t" \
   "ds_write_b32 v21, a128 offset:0\n\t" "ds_write_b32 v21, a129 offset:21120\n\t" "ds_write_b32 v21, a130 offset:10560\n\t" \
   "ds_write_b32 v21, a131 offset:31680\n\t" "s_mov_b32 s41, 61696\n\t" "buffer_load_dwordx4 a[232:235], v17, %[rb], s41 offen\n\t" \
   "s_waitcnt vmcnt(15)\n\t" "ds_write_b32 v21, a132 offset:64\n\t" "ds_write_b32 v21, a133 offset:21184\n\t" "ds_write_b32 v21, a134 offset:10624\n\t" \
   "ds_write_b32 v21, a135 offset:31744\n\t" "s_mov_b32 s41, 61952\n\t" "buffer_load_dwordx4 a[236:239], v17, %[rb], s41 offen\n\t" \
   "s_waitcnt vmcnt(15)\n\t" "ds_write_b32 v21, a160 offset:128\n\t" "ds_write_b32 v21, a161 offset:21248\n\t" \
   "ds_write_b32 v21, a162 offset:10688\n\t" "ds_write_b32 v21, a163 offset:31808\n\t" "s_mov_b32 s41, 62208\n\t" \
   "buffer_load_dwordx4 v[232:235], v17, %[rb], s41 offen\n\t" "s_waitcnt vmcnt(15)\n\t" "ds_write_b32 v21, a164 offset:192\n\t" \
   "ds_write_b32 v21, a165 offset:21312\n\t" "ds_write_b32 v21, a166 offset:10752\n\t" "ds_write_b32 v21, a167 offset:31872\n\t" \
   "s_mov_b32 s41, 62464\n\t" "buffer_load_dwordx4 v[236:239], v17, %[rb], s41 offen\n\t" "s_waitcnt vmcnt(15)\n\t" \
   "ds_write_b32 v21, a192 offset:256\n\t" "ds_write_b32 v21, a193 offset:21376\n\t" "ds_write_b32 v21, a194 offset:10816\n\t" \
   "ds_write_b32 v21, a195 offset:31936\n\t" "s_mov_b32 s41, 81920\n\t" "buffer_load_dwordx4 a[144:147], v17, %[rb], s41 offen\n\t" \
   "s_waitcnt vmcnt(15)\n\t" "ds_write_b32 v21, a196 offset:5248\n\t" "ds_write_b32 v21, a197 offset:26368\n\t" \
   "ds_write_b32 v21, a198 offset:15808\n\t" "ds_write_b32 v21, a199 offset:36928\n\t" "s_mov_b32 s41, 82176\n\t" \
   "buffer_load_dwordx4 a[148:151], v17, %[rb], s41 offen\n\t" "s_waitcnt vmcnt(15)\n\t" "ds_write_b32 v21, a224 offset:5312\n\t" \
   "ds_write_b32 v21, a225 offset:26432\n\t" "ds_write_b32 v21, a226 offset:15872\n\t" "ds_write_b32 v21, a227 offset:36992\n\t" \
   "s_mov_b32 s41, 82432\n\t" "buffer_load_dwordx4 a[176:179], v17, %[rb], s41 offen\n\t" "s_waitcnt vmcnt(15)\n\t" \
   "ds_write_b32 v21, a228 offset:5376\n\t" "ds_write_b32 v21, a229 offset:26496\n\t" "ds_write_b32 v21, a230 offset:15936\n\t" \
   "ds_write_b32 v21, a231 offset:37056\n\t" "s_mov_b32 s41, 82688\n\t" "buffer_load_dwordx4 a[180:183], v17, %[rb], s41 offen\n\t" \
   "s_waitcnt vmcnt(15)\n\t" "ds_write_b32 v21, v224 offset:5440\n\t" "ds_write_b32 v21, v225 offset:26560\n\t" \
   "ds_write_b32 v21, v226 offset:16000\n\t" "ds_write_b32 v21, v227 offset:37120\n\t" "s_mov_b32 s41, 82944\n\t" \
   "buffer_load_dwordx4 a[208:211], v17, %[rb], s41 offen\n\t" "s_waitcnt vmcnt(15)\n\t" "ds_write_b32 v21, v228 offset:5504\n\t" \
   "ds_write_b32 v21, v229 offset:26624\n\t" "ds_write_b32 v21, v230 offset:16064\n\t" "ds_write_b32 v21, v231 offset:37184\n\t" \
   "s_mov_b32 s41, 102400\n\t" "buffer_load_dwordx4 a[212:215], v17, %[rb], s41 offen\n\t" "s_waitcnt lgkmcnt(0)\n\t" "s_barrier\n\t" LDSTAMP(4) \
   "ds_read_b64_tr_b16 a[128:129], v22 offset:0\n\t" "ds_read_b64_tr_b16 a[130:131], v22 offset:656\n\t" \
   "ds_read_b64_tr_b16 a[132:133], v22 offset:2624\n\t" "ds_read_b64_tr_b16 a[134:135], v22 offset:3280\n\t" \
   "ds_read_b64_tr_b16 a[160:161], v22 offset:16\n\t" "ds_read_b64_tr_b16 a[162:163], v22 offset:672\n\t" \
   "ds_read_b64_tr_b16 a[164:165], v22 offset:2640\n\t" "ds_read_b64_tr_b16 a[166:167], v22 offset:3296\n\t" \
   "ds_read_b64_tr_b16 a[192:193], v22 offset:32\n\t" "ds_read_b64_tr_b16 a[194:195], v22 offset:688\n\t" \
   "ds_read_b64_tr_b16 a[196:197], v22 offset:2656\n\t" "ds_read_b64_tr_b16 a[198:199], v22 offset:3312\n\t" \
   "ds_read_b64_tr_b16 a[224:225], v22 offset:48\n\t" "ds_read_b64_tr_b16 a[226:227], v22 offset:704\n\t" \
   "ds_read_b64_tr_b16 a[228:229], v22 offset:2672\n\t" "ds_read_b64_tr_b16 a[230:231], v22 offset:3328\n\t" \
   "ds_read_b64_tr_b16 v[224:225], v22 offset:64\n\t" "ds_read_b64_tr_b16 v[226:227], v22 offset:720\n\t" \
   "ds_read_b64_tr_b16 v[228:229], v22 offset:2688\n\t" "ds_read_b64_tr_b16 v[230:231], v22 offset:3344\n\t" "s_waitcnt vmcnt(15)\n\t" \
   "s_waitcnt lgkmcnt(0)\n\t" "s_barrier\n\t" "ds_write_b32 v21, a136 offset:0\n\t" "ds_write_b32 v21, a137 offset:21120\n\t" \
   "ds_write_b32 v21, a138 offset:10560\n\t" "ds_write_b32 v21, a139 offset:31680\n\t" "s_mov_b32 s41, 102656\n\t" \
   "buffer_load_dwordx4 a[240:243], v17, %[rb], s41 offen\n\t" "s_waitcnt vmcnt(15)\n\t" "ds_write_b32 v21, a140 offset:64\n\t" \
   "ds_write_b32 v21, a141 offset:21184\n\t" "ds_write_b32 v21, a142 offset:10624\n\t" "ds_write_b32 v21, a143 offset:31744\n\t" \
   "s_mov_b32 s41, 102912\n\t" "buffer_load_dwordx4 a[244:247], v17, %[rb], s41 offen\n\t" "s_waitcnt vmcnt(15)\n\t" \
   "ds_write_b32 v21, a168 offset:128\n\t" "ds_write_b32 v21, a169 offset:21248\n\t" "ds_write_b32 v21, a170 offset:10688\n\t" \
   "ds_write_b32 v21, a171 offset:31808\n\t" "s_mov_b32 s41, 103168\n\t" "buffer_load_dwordx4 v[240:243], v17, %[rb], s41 offen\n\t" \
   "s_waitcnt vmcnt(15)\n\t" "ds_write_b32 v21, a172 offset:192\n\t" "ds_write_b32 v21, a173 offset:21312\n\t" \
   "ds_write_b32 v21, a174 offset:10752\n\t" "ds_write_b32 v21, a175 offset:31872\n\t" "s_mov_b32 s41, 103424\n\t" \
   "buffer_load_dwordx4 v[244:247], v17, %[rb], s41 offen\n\t" "s_waitcnt vmcnt(15)\n\t" "ds_write_b32 v21, a200 offset:256\n\t" \
   "ds_write_b32 v21, a201 offset:21376\n\t" "ds_write_b32 v21, a202 offset:10816\n\t" "ds_write_b32 v21, a203 offset:31936\n\t" \
   "s_mov_b32 s41, 122880\n\t" "buffer_load_dwordx4 a[152:155], v17, %[rb], s41 offen\n\t" "s_waitcnt vmcnt(15)\n\t" \
   "ds_write_b32 v21, a204 offset:5248\n\t" "ds_write_b32 v21, a205 offset:26368\n\t" "ds_write_b32 v21, a206 offset:15808\n\t" \
   "ds_write_b32 v21, a207 offset:36928\n\t" "s_mov_b32 s41, 123136\n\t" "buffer_load_dwordx4 a[156:159], v17, %[rb], s41 offen\n\t" \
   "s_waitcnt vmcnt(15)\n\t" "ds_write_b32 v21, a232 offset:5312\n\t" "ds_write_b32 v21, a233 offset:26432\n\t" \
   "ds_write_b32 v21, a234 offset:15872\n\t" "ds_write_b32 v21, a235 offset:36992\n\t" "s_mov_b32 s41, 123392\n\t" \
   "buffer_load_dwordx4 a[184:187], v17, %[rb], s41 offen\n\t" "s_waitcnt vmcnt(15)\n\t" "ds_write_b32 v21, a236 offset:5376\n\t" \
   "ds_write_b32 v21, a237 offset:26496\n\t" "ds_write_b32 v21, a238 offset:15936\n\t" "ds_write_b32 v21, a239 offset:37056\n\t" \
   "s_mov_b32 s41, 123648\n\t" "buffer_load_dwordx4 a[188:191], v17, %[rb], s41 offen\n\t" "s_waitcnt vmcnt(15)\n\t" \
   "ds_write_b32 v21, v232 offset:5440\n\t" "ds_write_b32 v21, v233 offset:26560\n\t" "ds_write_b32 v21, v234 offset:16000\n\t" \
   "ds_write_b32 v21, v235 offset:37120\n\t" "s_mov_b32 s41, 123904\n\t" "buffer_load_dwordx4 a[216:219], v17, %[rb], s41 offen\n\t" \
   "s_waitcnt vmcnt(15)\n\t" "ds_write_b32 v21, v236 offset:5504\n\t" "ds_write_b32 v21, v237 offset:26624\n\t" \
   "ds_write_b32 v21, v238 offset:16064\n\t" "ds_write_b32 v21, v239 offset:37184\n\t" "s_mov_b32 s41, 143360\n\t" \
   "buffer_load_dwordx4 a[220:223], v17, %[rb], s41 offen\n\t" "s_waitcnt lgkmcnt(0)\n\t" "s_barrier\n\t" LDSTAMP(5) \
   "ds_read_b64_tr_b16 a[136:137], v22 offset:0\n\t" "ds_read_b64_tr_b16 a[138:139], v22 offset:656\n\t" \
   "ds_read_b64_tr_b16 a[140:141], v22 offset:2624\n\t" "ds_read_b64_tr_b16 a[142:143], v22 offset:3280\n\t" \
   "ds_read_b64_tr_b16 a[168:169], v22 offset:16\n\t" "ds_read_b64_tr_b16 a[170:171], v22 offset:672\n\t" \
   "ds_read_b64_tr_b16 a[172:173], v22 offset:2640\n\t" "ds_read_b64_tr_b16 a[174:175], v22 offset:3296\n\t" \
   "ds_read_b64_tr_b16 a[200:201], v22 offset:32\n\t" "ds_read_b64_tr_b16 a[202:203], v22 offset:688\n\t" \
   "ds_read_b64_tr_b16 a[204:205], v22 offset:2656\n\t" "ds_read_b64_tr_b16 a[206:207], v22 offset:3312\n\t" \
   "ds_read_b64_tr_b16 a[232:233], v22 offset:48\n\t" "ds_read_b64_tr_b16 a[234:235], v22 offset:704\n\t" \
   "ds_read_b64_tr_b16 a[236:237], v22 offset:2672\n\t" "ds_read_b64_tr_b16 a[238:239], v22 offset:3328\n\t" \
   "ds_read_b64_tr_b16 v[232:233], v22 offset:64\n\t" "ds_read_b64_tr_b16 v[234:235], v22 offset:720\n\t" \
   "ds_read_b64_tr_b16 v[236:237], v22 offset:2688\n\t" "ds_read_b64_tr_b16 v[238:239], v22 offset:3344\n\t" "s_waitcnt vmcnt(15)\n\t" \
   "s_waitcnt lgkmcnt(0)\n\t" "s_barrier\n\t" "ds_write_b32 v21, a144 offset:0\n\t" "ds_write_b32 v21, a145 offset:21120\n\t" \
   "ds_write_b32 v21, a146 offset:10560\n\t" "ds_write_b32 v21, a147 offset:31680\n\t" "s_mov_b32 s41, 143616\n\t" \
   "buffer_load_dwordx4 a[248:251], v17, %[rb], s41 offen\n\t" "s_waitcnt vmcnt(15)\n\t" "ds_write_b32 v21, a148 offset:64\n\t" \
   "ds_write_b32 v21, a149 offset:21184\n\t" "ds_write_b32 v21, a150 offset:10624\n\t" "ds_write_b32 v21, a151 offset:31744\n\t" \
   "s_mov_b32 s41, 143872\n\t" "buffer_load_dwordx4 a[252:255], v17, %[rb], s41 offen\n\t" "s_waitcnt vmcnt(15)\n\t" \
   "ds_write_b32 v21, a176 offset:128\n\t" "ds_write_b32 v21, a177 offset:21248\n\t" "ds_write_b32 v21, a178 offset:10688\n\t" \
   "ds_write_b32 v21, a179 offset:31808\n\t" "s_mov_b32 s41, 144128\n\t" "buffer_load_dwordx4 v[248:251], v17, %[rb], s41 offen\n\t" \
   "s_waitcnt vmcnt(15)\n\t" "ds_write_b32 v21, a180 offset:192\n\t" "ds_write_b32 v21, a181 offset:21312\n\t" \
   "ds_write_b32 v21, a182 offset:10752\n\t" "ds_write_b32 v21, a183 offset:31872\n\t" "s_mov_b32 s41, 144384\n\t" \
   "buffer_load_dwordx4 v[252:255], v17, %[rb], s41 offen\n\t" "s_waitcnt vmcnt(15)\n\t" "ds_write_b32 v21, a208 offset:256\n\t" \
   "ds_write_b32 v21, a209 offset:21376\n\t" "ds_write_b32 v21, a210 offset:10816\n\t" "ds_write_b32 v21, a211 offset:31936\n\t" \
   "s_waitcnt vmcnt(14)\n\t" "ds_write_b32 v21, a212 offset:5248\n\t" "ds_write_b32 v21, a213 offset:26368\n\t" \
   "ds_write_b32 v21, a214 offset:15808\n\t" "ds_write_b32 v21, a215 offset:36928\n\t" "s_waitcnt vmcnt(13)\n\t" \
   "ds_write_b32 v21, a240 offset:5312\n\t" "ds_write_b32 v21, a241 offset:26432\n\t" "ds_write_b32 v21, a242 offset:15872\n\t" \
   "ds_write_b32 v21, a243 offset:36992\n\t" "s_waitcnt vmcnt(12)\n\t" "ds_write_b32 v21, a244 offset:5376\n\t" \
   "ds_write_b32 v21, a245 offset:26496\n\t" "ds_write_b32 v21, a246 offset:15936\n\t" "ds_write_b32 v21, a247 offset:37056\n\t" \
   "s_waitcnt vmcnt(11)\n\t" "ds_write_b32 v21, v240 offset:5440\n\t" "ds_write_b32 v21, v241 offset:26560\n\t" \
   "ds_write_b32 v21, v242 offset:16000\n\t" "ds_write_b32 v21, v243 offset:37120\n\t" "s_waitcnt vmcnt(10)\n\t" \
   "ds_write_b32 v21, v244 offset:5504\n\t" "ds_write_b32 v21, v245 offset:26624\n\t" "ds_write_b32 v21, v246 offset:16064\n\t" \
   "ds_write_b32 v21, v247 offset:37184\n\t" "buffer_load_dwordx4 v[28:31], v112, %[rw], 0 offen sc1\n\t" "s_waitcnt lgkmcnt(0)\n\t" "s_barrier\n\t" \
   LDSTAMP(6) "ds_read_b64_tr_b16 a[144:145], v22 offset:0\n\t" "ds_read_b64_tr_b16 a[146:147], v22 offset:656\n\t" \
   "ds_read_b64_tr_b16 a[148:149], v22 offset:2624\n\t" "ds_read_b64_tr_b16 a[150:151], v22 offset:3280\n\t" \
   "ds_read_b64_tr_b16 a[176:177], v22 offset:16\n\t" "ds_read_b64_tr_b16 a[178:179], v22 offset:672\n\t" \
   "ds_read_b64_tr_b16 a[180:181], v22 offset:2640\n\t" "ds_read_b64_tr_b16 a[182:183], v22 offset:3296\n\t" \
   "ds_read_b64_tr_b16 a[208:209], v22 offset:32\n\t" "ds_read_b64_tr_b16 a[210:211], v22 offset:688\n\t" \
   "ds_read_b64_tr_b16 a[212:213], v22 offset:2656\n\t" "ds_read_b64_tr_b16 a[214:215], v22 offset:3312\n\t" \
   "ds_read_b64_tr_b16 a[240:241], v22 offset:48\n\t" "ds_read_b64_tr_b16 a[242:243], v22 offset:704\n\t" \
   "ds_read_b64_tr_b16 a[244:245], v22 offset:2672\n\t" "ds_read_b64_tr_b16 a[246:247], v22 offset:3328\n\t" \
   "ds_read_b64_tr_b16 v[240:241], v22 offset:64\n\t" "ds_read_b64_tr_b16 v[242:243], v22 offset:720\n\t" \
   "ds_read_b64_tr_b16 v[244:245], v22 offset:2688\n\t" "ds_read_b64_tr_b16 v[246:247], v22 offset:3344\n\t" "s_waitcnt vmcnt(10)\n\t" \
   "s_waitcnt lgkmcnt(0)\n\t" "s_barrier\n\t" "ds_write_b32 v21, a152 offset:0\n\t" "ds_write_b32 v21, a153 offset:21120\n\t" \
   "ds_write_b32 v21, a154 offset:10560\n\t" "ds_write_b32 v21, a155 offset:31680\n\t" "s_waitcnt vmcnt(9)\n\t" "ds_write_b32 v21, a156 offset:64\n\t" \
   "ds_write_b32 v21, a157 offset:21184\n\t" "ds_write_b32 v21, a158 offset:10624\n\t" "ds_write_b32 v21, a159 offset:31744\n\t" \
   "s_waitcnt vmcnt(8)\n\t" "ds_write_b32 v21, a184 offset:128\n\t" "ds_write_b32 v21, a185 offset:21248\n\t" "ds_write_b32 v21, a186 offset:10688\n\t" \
   "ds_write_b32 v21, a187 offset:31808\n\t" "s_waitcnt vmcnt(7)\n\t" "ds_write_b32 v21, a188 offset:192\n\t" "ds_write_b32 v21, a189 offset:21312\n\t" \
   "ds_write_b32 v21, a190 offset:10752\n\t" "ds_write_b32 v21, a191 offset:31872\n\t" "s_waitcnt vmcnt(6)\n\t" "ds_write_b32 v21, a216 offset:256\n\t" \
   "ds_write_b32 v21, a217 offset:21376\n\t" "ds_write_b32 v21, a218 offset:10816\n\t" "ds_write_b32 v21, a219 offset:31936\n\t" \
   "s_waitcnt vmcnt(5)\n\t" "ds_write_b32 v21, a220 offset:5248\n\t" "ds_write_b32 v21, a221 offset:26368\n\t" \
   "ds_write_b32 v21, a222 offset:15808\n\t" "ds_write_b32 v21, a223 offset:36928\n\t" "s_waitcnt vmcnt(4)\n\t" \
   "ds_write_b32 v21, a248 offset:5312\n\t" "ds_write_b32 v21, a249 offset:26432\n\t" "ds_write_b32 v21, a250 offset:15872\n\t" \
   "ds_write_b32 v21, a251 offset:36992\n\t" "s_waitcnt vmcnt(3)\n\t" "ds_write_b32 v21, a252 offset:5376\n\t" \
   "ds_write_b32 v21, a253 offset:26496\n\t" "ds_write_b32 v21, a254 offset:15936\n\t" "ds_write_b32 v21, a255 offset:37056\n\t" \
   "s_waitcnt vmcnt(2)\n\t" "ds_write_b32 v21, v248 offset:5440\n\t" "ds_write_b32 v21, v249 offset:26560\n\t" \
   "ds_write_b32 v21, v250 offset:16000\n\t" "ds_write_b32 v21, v251 offset:37120\n\t" "s_waitcnt vmcnt(1)\n\t" \
   "ds_write_b32 v21, v252 offset:5504\n\t" "ds_write_b32 v21, v253 offset:26624\n\t" "ds_write_b32 v21, v254 offset:16064\n\t" \
   "ds_write_b32 v21, v255 offset:37184\n\t" "s_waitcnt lgkmcnt(0)\n\t" "s_barrier\n\t" LDSTAMP(7) "ds_read_b64_tr_b16 a[152:153], v22 offset:0\n\t" \
   "ds_read_b64_tr_b16 a[154:155], v22 offset:656\n\t" "ds_read_b64_tr_b16 a[156:157], v22 offset:2624\n\t" \
   "ds_read_b64_tr_b16 a[158:159], v22 offset:3280\n\t" "ds_read_b64_tr_b16 a[184:185], v22 offset:16\n\t" \
   "ds_read_b64_tr_b16 a[186:187], v22 offset:672\n\t" "ds_read_b64_tr_b16 a[188:189], v22 offset:2640\n\t" \
   "ds_read_b64_tr_b16 a[190:191], v22 offset:3296\n\t" "ds_read_b64_tr_b16 a[216:217], v22 offset:32\n\t" \
   "ds_read_b64_tr_b16 a[218:219], v22 offset:688\n\t" "ds_read_b64_tr_b16 a[220:221], v22 offset:2656\n\t" \
   "ds_read_b64_tr_b16 a[222:223], v22 offset:3312\n\t" "ds_read_b64_tr_b16 a[248:249], v22 offset:48\n\t" \
   "ds_read_b64_tr_b16 a[250:251], v22 offset:704\n\t" "ds_read_b64_tr_b16 a[252:253], v22 offset:2672\n\t" \
   "ds_read_b64_tr_b16 a[254:255], v22 offset:3328\n\t" "ds_read_b64_tr_b16 v[248:249], v22 offset:64\n\t" \
   "ds_read_b64_tr_b16 v[250:251], v22 offset:720\n\t" "ds_read_b64_tr_b16 v[252:253], v22 offset:2688\n\t" \
   "ds_read_b64_tr_b16 v[254:255], v22 offset:3344\n\t" "s_waitcnt vmcnt(0) lgkmcnt(0)\n\t" "s_barrier"
#define LDY_CLOB "v24", "v25", "v26", "v32", "v33", "v34", "v35", "v36", "v37", "v38", "v39", "v40", "v41", "v42", "v43", "v44", "v45", "v46", "v47", "v48", "v49", "v50", "v51", "v52", "v53", "v54", "v55", "v56", "v57", "v58", "v59", "v60", "v61", "v62", "v63", "v64", "v65", "v66", "v67", "v68", "v69", "v70", "v71", "v72", "v73", "v74", "v75", "v76", "v77", "v78", "v79", "v80", "v81", "v82", "v83", "v84", "v85", "v86", "v87", "v88", "v89", "v90", "v91", "v92", "v93", "v94", "v95", "v96", "v97", "v98", "v99", "v100", "v101", "v102", "v103", "v104", "v105", "v106", "v107", "v108", "v109", "v110", "v111", "v120", "v121", "v122", "v123", "s40", "s41"
#define W_OPS "{a[0:31]}"(w[0]), "{a[32:63]}"(w[1]), "{a[64:95]}"(w[2]), "{a[96:127]}"(w[3]), "{a[128:159]}"(w[4]), "{a[160:191]}"(w[5]), \
              "{a[192:223]}"(w[6]), "{a[224:255]}"(w[7]), "{v[192:223]}"(w[8]), "{v[224:255]}"(w[9])

__device__ __forceinline__ int mem_lane(int lane) { return lane * 16; }
__device__ __forceinline__ int pair_of(int g) { return (g & 1) * 2 + (g >> 1); }

__device__ __forceinline__ void rec_p1(int wv_, const v32u (&w)[10], unsigned char* ws, const Pool& pool, int layer, int h, int xs, RecTune tn, unsigned long long* stamps) {
  const Clk0 c0 = clk_sample();
  const unsigned lds0 = (unsigned)(uintptr_t)(__attribute__((address_space(3))) unsigned char*)dyn_lds;
  const int lane = lane_id(), wv = wv_, g = lane >> 4, r = lane & 15;
  const v4u rs = make_rs(ws, (unsigned)OFF_END);
  const int p0 = wv < 3 ? 3 * wv : 8;
  v16u ad, la = (unsigned)lane;
  const int kqo = (int)(wv < 2 ? OFF_KF : OFF_QF) + ((h * RT + (wv & 1)) * 4 * 64 + lane) * 16;
  const int progo = (wv == 0 && lane == 0) ? prog_off(layer, 0, h) : OOB;
  {
    const int p2 = wv < 3 ? p0 + 2 : 0;
    ad[0] = W_MEM + (p0 >> 1) * WIN + (p0 & 1) * 1024 + mem_lane(lane);
    ad[1] = W_MEM + ((p0 + 1) >> 1) * WIN + ((p0 + 1) & 1) * 1024 + mem_lane(lane);
    ad[2] = W_MEM + (p2 >> 1) * WIN + (p2 & 1) * 1024 + mem_lane(lane);
    ad[3] = lane == 0 ? W_MISC + h * 64 + wv * 4 : OOB;
    ad[4] = copy_off(wv) + (h >> 2) * WIN + (h & 3) * 512 + (g >> 1) * 256 + (2 * (r & 7) + (r >> 3)) * 16 + (g & 1) * 8;
    ad[5] = g < 2 ? copy_off(wv + 4 * (r >> 3)) + (8 + (h >> 4)) * WIN + ((h >> 3) & 1) * 1024 + (((h >> 1) & 3) * 16 + (h & 1) * 8 + (r & 7)) * 16 + g * 8 : OOB;
    ad[6] = (r & 1) ? 0xEEEEu : 0x4444u;
    ad[7] = lds0 + (32 * wv + r) * 16 + g * 4;
    ad[8] = lds0 + (4 * g + (r >> 2)) * 16 + (r & 1) * 8;
    ad[9] = lds0 + L_M2 + wv * 3072 + lane * 16;
    ad[10] = kqo;
    ad[11] = M_TM + xs * 128 + (lane & 1) * 12;
    ad[12] = kqf_off(layer, 0, h) + (lane & 3) * 4;
    ad[13] = wv > 1 ? (int)OFF_PXF + ((h * 2 + (wv & 1)) * 64 + lane) * 16 : OOB;
    ad[14] = (g < 2 && wv > 1) ? (int)OFF_PXM + (((h >> 2) * 2 + (wv & 1)) * 64 + (h & 3) * 16 + r) * 16 + g * 8 : OOB;
    ad[15] = progo;
#pragma unroll
    for (int i = 0; i < 10; ++i) { int p = p0 + i; p = p >= 10 ? p - 10 : p; la[i] = lds0 + p * 1024 + lane * 16; }
    la[10] = lds0 + L_KQ + lane * 16;
    la[12] = lds0 + REC_ST + wv * 8;
    asm volatile("v_mov_b32 %0, 0xceabfb8f" : "=v"(la[13]));
    la[14] = 16 * (wv & 1) + r - 4 * g;
    la[15] = lds0 + L_KQ + (wv & 1) * 4096 + lane * 16;
  }
  __builtin_amdgcn_s_waitcnt(0x0F70);
  if (layer) __builtin_amdgcn_s_sleep(64);
  v4u clk = clk_init(c0);
  unsigned long long stat;
  asm volatile(P1_ASM
               : "=&{s[76:77]}"(stat), "+{s[56:59]}"(clk)
               : W_OPS, "{v[128:143]}"(ad), "{v[112:127]}"(la), [rs] "s"(rs), [sel] "s"(pool.sel), [pool] "s"(pool.base), [xs] "s"(xs),
                 [lad0] "s"(ladder(xs, xs & 1)), [s3] "s"(pool.s3), [m1] "s"(tn.m1), [l2s] "s"(lds0 + L_M2 + wv * 3072),
                 [hi8] "s"(0xFF00FF00FF00FF00ull), [wv] "s"(wv), [kf0] "s"((int)OFF_KF + h * RT * 4096),
                 [esc] "s"(pool.base + (NSEG - 1) * REG)
               : P1_CLOB RST_CLOB, "vcc", "scc", "m0", "memory");
  LDSBAR();
  {
    const unsigned one = 1u;
    const int so = (NSEG - 1) * 4096;
    VM_ST32(one, progo, so);
    VM_WAIT(0);
  }
}

__device__ __forceinline__ void rec_pv(int wv_, const v32u (&w)[10], unsigned char* ws, const Pool& pool, int layer, int h, int xs, RecTune tn, unsigned long long* stamps) {
  const Clk0 c0 = clk_sample();
  const unsigned lds0 = (unsigned)(uintptr_t)(__attribute__((address_space(3))) unsigned char*)dyn_lds;
  const int lane = lane_id(), wv = wv_, g = lane >> 4, r = lane & 15;
  const v4u rs = make_rs(ws, (unsigned)OFF_END);
  const int p0 = wv < 3 ? 3 * wv : 8, p2 = wv < 3 ? p0 + 2 : 0;
  v16u ad = (unsigned)lane, la = (unsigned)lane;
  ad[0] = W_MEM + (p0 >> 1) * WIN + (p0 & 1) * 1024 + mem_lane(lane);
  ad[1] = W_MEM + ((p0 + 1) >> 1) * WIN + ((p0 + 1) & 1) * 1024 + mem_lane(lane);
  ad[2] = W_MEM + (p2 >> 1) * WIN + (p2 & 1) * 1024 + mem_lane(lane);
  ad[3] = (r & 8) ? 0xEEEEu : 0x4444u;
  ad[4] = (wv * 10 + (g >> 1)) * 16384 + h * 256 + r * 16 + (g & 1) * 8;
  ad[5] = h * 16384 + wv * 2048 + lane * 16;
  ad[6] = (r & 1) ? 0xEEEEu : 0x4444u;
  ad[7] = lds0 + (32 * wv + r) * 16 + pair_of(g) * 4;
  ad[8] = lds0 + (4 * g + (r >> 2)) * 16 + (r & 1) * 8;
  ad[11] = M_TM + (4 + xs) * 128 + (lane & 1) * 12;
  ad[15] = (wv == 0 && lane == 0) ? prog_off(layer, 0, 32 + h) : OOB;
#pragma unroll
  for (int i = 0; i < 10; ++i) { int p = p0 + i; p = p >= 10 ? p - 10 : p; la[i] = lds0 + p * 1024 + lane * 16; }
  la[12] = lds0 + REC_ST + wv * 8;
  __builtin_amdgcn_s_waitcnt(0x0F70);
  if (layer) __builtin_amdgcn_s_sleep(64);
  v4u clk = clk_init(c0);
  unsigned long long stat;
  asm volatile(PV_ASM
               : "=&{s[76:77]}"(stat), "+{s[56:59]}"(clk)
               : W_OPS, "{v[128:143]}"(ad), "{v[112:127]}"(la), [rs] "s"(rs), [sel] "s"(pool.sel), [pool] "s"(pool.base), [xs] "s"(xs),
                 [lad0] "s"(ladder(xs, xs & 1)), [s3] "s"(pool.s3), [m1c] "s"(tn.m1 + tn.c2), [esc] "s"(pool.base + (NSEG - 1) * REG),
                 [ysel] "s"(pool.ysel), [yb] "s"(pool.base + P_Y)
               : PV_CLOB RST_CLOB, "vcc", "scc", "memory");
}

#define TSTART 5000
#define TD1 900u
#define TR2 400u
#define TEMA 1
#define TEMB 2
#define TBUP 48
#define TBDN 6
#define TLATE 520
#define TSEC 50
__device__ __forceinline__ unsigned track_dur(unsigned e, int m, int rob, int s) {
  const int lo = (int)e - (s < 8 ? 64 : (rob & 255)), hi = (int)e + (rob >> 8);
  if (s >= 8) m = (int)e + ((m - (int)e + (m > (int)e ? (1 << TEMA) - 1 : 0)) >> (m > (int)e ? TEMA : TEMB));
  return (unsigned)min(max(m, lo), hi);
}
#define TTS 200
#define TTU 240
__device__ __forceinline__ void rec_sched(unsigned char* ws, const Pool& pool, int layer, int c16, int xs, RecTune tn, unsigned long long* stamps) {
  const Clk0 c0 = clk_sample();
  const unsigned lds0 = (unsigned)(uintptr_t)(__attribute__((address_space(3))) unsigned char*)dyn_lds;
  const int lane = lane_id();
  const v4u rs = make_rs(ws, (unsigned)OFF_END);
  unsigned long long stat = 0;
  const int sm = M_STAMP + min(lane, 39) * 64, sp = W_MISC + (lane & 31) * 64;
  const int yo = M_TM + xs * 128, oc = (c16 == 0 && lane < 16) ? M_TM + lane * 128 : OOB;
  const unsigned cv = flag_finish<2>(rs, kqf_off(layer, 0, 32 + (lane & 31)), 0xFFFFFFFFu);
  v4u clk = clk_init(c0);
  PH(3 + layer * 6)
  unsigned r2a = TR2, d1a = TD1, dd = TD1 + tn.m2, tp = 0, tm = 0, anchor = clk.x - dd;
  unsigned rr = (unsigned)min(max(wave_imax((int)(cv * 8u - clk.x)) + TSTART, 300), 4000);
  int bonus = 0;
  bool clean = false;
#pragma unroll 1
  for (int s = 0; s < NSEG - 1; ++s) {
    const int rgm = pool.region((s + NSEG - 1) & (NSEG - 1)), rg = pool.region(s);
    const unsigned slot = lds0 + L_T + (s & 1) * 32, ao = anchor;
    unsigned b2 = 1u;
    v4u b1 = {1u, 1u, 1u, 1u};
    if (s > 0) {
      asm volatile(WAIT_ASM : "+{s[56:59]}"(clk) : [ta] "s"(tm + TTS) : WAIT_CLOB, "scc", "memory");
      VM_LD32(b2, sm, rgm);
      VM_LD128(b1, sp, rgm, 0);
      asm volatile("s_waitcnt vmcnt(0)" : "+v"(b2), "+v"(b1)::"memory");
      anchor = tm;
      if (__builtin_expect(__any(b2 == 0xFFFFFFFFu || (int)(b2 - (tm + tn.m1 - tn.vis)) > 0), 0)) {
        STAT(3);
        for (unsigned spins = 0; __any(b2 == 0xFFFFFFFFu) && spins < SPIN_CAP; ++spins) {
          __builtin_amdgcn_s_sleep(2);
          VM_LD32(b2, sm, rgm);
          VM_WAIT1(0, b2);
        }
        const int d = wave_imax((int)(b2 - tm)) + tn.vis - tn.m1;
        anchor = tm + (d <= 0 ? 0 : (d <= tn.dlt ? tn.dlt + TSEC : d + TLATE));
      }
    }
    const unsigned tpo = tp, tmo = tm;
    tp = anchor + dd;
    tm = nosent(tp + rr);
    LDS_V4(slot) = v4u{tp, tm, (unsigned)(s + 1), anchor};
    if (anchor != tmo && s > 2) bonus = TBUP; else bonus = max(bonus - TBDN, 0);
    dd = d1a + tn.m2 + bonus;
    rr = r2a + bonus;
    {
      const v4u wq = v4u{tm, dd, rr, dd + rr};
      VM_ST128(wq, oc, rg);
    }
    if (s > 1 && !__any((b2 & 1u) != 0u)) r2a = track_dur(r2a, wave_imax((int)(b2 - tpo)), tn.rob, s);
    if (s > 1 && clean && !__any(((b1.x | b1.y | b1.z | b1.w) & 1u) != 0u))
      d1a = track_dur(d1a, wave_imax(max(max((int)(b1.x - ao), (int)(b1.y - ao)), max((int)(b1.z - ao), (int)(b1.w - ao)))), tn.rob, s);
    clean = anchor == tmo;
    while ((int)(tp + 40u - rtc() * 8u) > 0 && (int)(tp - rtc() * 8u) < 32000) __builtin_amdgcn_s_sleep(1);
    v4u lw;
    VM_LD128(lw, yo, rg, 0);
    VM_WAIT1(0, lw);
    const unsigned l0 = (unsigned)__builtin_amdgcn_readfirstlane(lw.x), l1 = (unsigned)__builtin_amdgcn_readfirstlane(lw.y),
                   l2 = (unsigned)__builtin_amdgcn_readfirstlane(lw.z);
    if (max(max(l0, l1), l2) != 0xFFFFFFFFu) { tm = l0; dd = l1; rr = l2; }
  }
}

__device__ __forceinline__ void rec_fin(unsigned char* ws, const Pool& pool, int layer, int c16, int xs, RecTune tn, unsigned long long* stamps) {
  const Clk0 c0 = clk_sample();
  const unsigned lds0 = (unsigned)(uintptr_t)(__attribute__((address_space(3))) unsigned char*)dyn_lds;
  const int lane = lane_id(), g = lane >> 4, r = lane & 15;
  const v4u rs = make_rs(ws, (unsigned)OFF_END);
  v8u ad;
  ad[0] = lane * 16;
  ad[1] = g * 256 + r * 8;
  ad[2] = (r & 1) ? 0x03020706u : 0x05040100u;
  ad[3] = (lane & 31) * 64;
  ad[4] = W_MEM + copy_off(((r >> 1) & 1) * 5) + (c16 >> 3) * WIN + (c16 & 7) * 256 + (2 * (2 * g + (r & 1)) + (r >> 3)) * 16 + ((r >> 2) & 1) * 8;
  ad[5] = lane == 0 ? M_STAMP + c16 * 64 : OOB;
  ad[6] = (r & 1) ? 0xEEEEu : 0x4444u;
  ad[7] = lds0 + L_T;
  __builtin_amdgcn_s_sleep(64);
  v4u clk = clk_init(c0);
  unsigned long long stat;
  asm volatile(FIN_ASM
               : "=&{s[76:77]}"(stat), "+{s[56:59]}"(clk), "+{v[16:23]}"(ad)
               : [rs] "s"(rs), [sel] "s"(pool.sel), [pool] "s"(pool.base), [xs] "s"(xs), [lad0] "s"(ladder(xs, xs & 1)), [s3] "s"(pool.s3),
                 [su] "s"((int)OFF_UF + c16 * 32768), [yb] "s"(pool.base + P_Y + c16 * 16384), [ysel] "s"(pool.ysel), [tu] "s"(TTU),
                 [stl] "s"(lds0 + REC_ST - 32), [uf] "s"(kqf_off(layer, 0, 32))
               : FIN_CLOB RST_CLOB, "vcc", "scc", "memory");
}

constexpr int PJ_BUF = 43 * 1024, PJ_X = 16384, PJ_LDS = PJ_X + 3 * PJ_BUF;
#define KQ_ASM \
   "s_mov_b32 s44, %[rdy]\n\t" "s_mov_b32 s45, 0\n\t" "s_mov_b32 s46, 0\n\t" "s_cmp_eq_u32 s44, -1\n\t" "s_cbranch_scc1 68f\n\t" \
   "s_mul_i32 s52, s45, 1536\n\t" "s_mul_i32 s51, s45, 40960\n\t" "s_add_u32 m0, s46, %[ldx]\n\t" "s_mov_b32 s53, s46\n\t" \
   "buffer_load_dwordx4 v16, %[rs], s51 offen offset:0 sc1 lds\n\t" "buffer_load_dwordx4 v16, %[rs], s51 offen offset:1024 sc1 lds\n\t" \
   "buffer_load_dwordx4 v16, %[rs], s51 offen offset:2048 sc1 lds\n\t" "buffer_load_dwordx4 v16, %[rs], s51 offen offset:3072 sc1 lds\n\t" \
   "s_add_u32 m0, m0, 4096\n\t" "s_add_u32 s51, s51, 4096\n\t" "buffer_load_dwordx4 v16, %[rs], s51 offen offset:0 sc1 lds\n\t" \
   "buffer_load_dwordx4 v16, %[rs], s51 offen offset:1024 sc1 lds\n\t" "buffer_load_dwordx4 v16, %[rs], s51 offen offset:2048 sc1 lds\n\t" \
   "buffer_load_dwordx4 v16, %[rs], s51 offen offset:3072 sc1 lds\n\t" "s_add_u32 m0, m0, 4096\n\t" "s_add_u32 s51, s51, 4096\n\t" \
   "buffer_load_dwordx4 v16, %[rs], s51 offen offset:0 sc1 lds\n\t" "buffer_load_dwordx4 v16, %[rs], s51 offen offset:1024 sc1 lds\n\t" \
   "s_add_u32 m0, s53, %[ld1]\n\t" "s_nop 0\n\t" "buffer_load_dwordx4 v17, %[rs], s52 offen sc1 lds\n\t" "s_mov_b32 s45, 1\n\t" \
   "s_mov_b32 s46, 44032\n\t" "s_mul_i32 s52, s45, 1536\n\t" "s_mul_i32 s51, s45, 40960\n\t" "s_add_u32 m0, s46, %[ldx]\n\t" "s_mov_b32 s53, s46\n\t" \
   "buffer_load_dwordx4 v16, %[rs], s51 offen offset:0 sc1 lds\n\t" "buffer_load_dwordx4 v16, %[rs], s51 offen offset:1024 sc1 lds\n\t" \
   "buffer_load_dwordx4 v16, %[rs], s51 offen offset:2048 sc1 lds\n\t" "buffer_load_dwordx4 v16, %[rs], s51 offen offset:3072 sc1 lds\n\t" \
   "s_add_u32 m0, m0, 4096\n\t" "s_add_u32 s51, s51, 4096\n\t" "buffer_load_dwordx4 v16, %[rs], s51 offen offset:0 sc1 lds\n\t" \
   "buffer_load_dwordx4 v16, %[rs], s51 offen offset:1024 sc1 lds\n\t" "buffer_load_dwordx4 v16, %[rs], s51 offen offset:2048 sc1 lds\n\t" \
   "buffer_load_dwordx4 v16, %[rs], s51 offen offset:3072 sc1 lds\n\t" "s_add_u32 m0, m0, 4096\n\t" "s_add_u32 s51, s51, 4096\n\t" \
   "buffer_load_dwordx4 v16, %[rs], s51 offen offset:0 sc1 lds\n\t" "buffer_load_dwordx4 v16, %[rs], s51 offen offset:1024 sc1 lds\n\t" \
   "s_add_u32 m0, s53, %[ld1]\n\t" "s_nop 0\n\t" "buffer_load_dwordx4 v17, %[rs], s52 offen sc1 lds\n\t" "68:\n\t" "v_mov_b32 v160, 0x3f803f80\n\t" \
   "v_mov_b32 v161, 0x3f803f80\n\t" "v_mov_b32 v162, 0x3f803f80\n\t" "v_mov_b32 v163, 0x3f803f80\n\t" "v_mov_b32 v31, 1\n\t" "v_mov_b32 v138, 0\n\t" \
   "v_mov_b32 v13, %[flw]\n\t" "v_and_b32 v12, -16, v13\n\t" "ds_write_b32 v13, v138\n\t" \
   "v_mfma_f32_32x32x16_bf16 v[32:47], a[0:3], v[160:163], 0\n\t" "v_mfma_f32_32x32x16_bf16 v[48:63], a[8:11], v[160:163], 0\n\t" \
   "v_mfma_f32_32x32x16_bf16 v[32:47], a[4:7], v[160:163], v[32:47]\n\t" "v_mfma_f32_32x32x16_bf16 v[48:63], a[12:15], v[160:163], v[48:63]\n\t" \
   "v_mfma_f32_32x32x16_bf16 v[32:47], a[16:19], v[160:163], v[32:47]\n\t" "v_mfma_f32_32x32x16_bf16 v[48:63], a[24:27], v[160:163], v[48:63]\n\t" \
   "v_mfma_f32_32x32x16_bf16 v[32:47], a[20:23], v[160:163], v[32:47]\n\t" "v_mfma_f32_32x32x16_bf16 v[48:63], a[28:31], v[160:163], v[48:63]\n\t" \
   "v_mfma_f32_32x32x16_bf16 v[32:47], a[32:35], v[160:163], v[32:47]\n\t" "v_mfma_f32_32x32x16_bf16 v[48:63], a[40:43], v[160:163], v[48:63]\n\t" \
   "v_mfma_f32_32x32x16_bf16 v[32:47], a[36:39], v[160:163], v[32:47]\n\t" "v_mfma_f32_32x32x16_bf16 v[48:63], a[44:47], v[160:163], v[48:63]\n\t" \
   "v_mfma_f32_32x32x16_bf16 v[32:47], a[48:51], v[160:163], v[32:47]\n\t" "v_mfma_f32_32x32x16_bf16 v[48:63], a[56:59], v[160:163], v[48:63]\n\t" \
   "v_mfma_f32_32x32x16_bf16 v[32:47], a[52:55], v[160:163], v[32:47]\n\t" "v_mfma_f32_32x32x16_bf16 v[48:63], a[60:63], v[160:163], v[48:63]\n\t" \
   "v_mfma_f32_32x32x16_bf16 v[32:47], a[64:67], v[160:163], v[32:47]\n\t" "v_mfma_f32_32x32x16_bf16 v[48:63], a[72:75], v[160:163], v[48:63]\n\t" \
   "v_mfma_f32_32x32x16_bf16 v[32:47], a[68:71], v[160:163], v[32:47]\n\t" "v_mfma_f32_32x32x16_bf16 v[48:63], a[76:79], v[160:163], v[48:63]\n\t" \
   "v_mfma_f32_32x32x16_bf16 v[32:47], a[80:83], v[160:163], v[32:47]\n\t" "v_mfma_f32_32x32x16_bf16 v[48:63], a[88:91], v[160:163], v[48:63]\n\t" \
   "v_mfma_f32_32x32x16_bf16 v[32:47], a[84:87], v[160:163], v[32:47]\n\t" "v_mfma_f32_32x32x16_bf16 v[48:63], a[92:95], v[160:163], v[48:63]\n\t" \
   "v_mfma_f32_32x32x16_bf16 v[32:47], a[96:99], v[160:163], v[32:47]\n\t" "v_mfma_f32_32x32x16_bf16 v[48:63], a[104:107], v[160:163], v[48:63]\n\t" \
   "v_mfma_f32_32x32x16_bf16 v[32:47], a[100:103], v[160:163], v[32:47]\n\t" "v_mfma_f32_32x32x16_bf16 v[48:63], a[108:111], v[160:163], v[48:63]\n\t" \
   "v_mfma_f32_32x32x16_bf16 v[32:47], a[112:115], v[160:163], v[32:47]\n\t" "v_mfma_f32_32x32x16_bf16 v[48:63], a[120:123], v[160:163], v[48:63]\n\t" \
   "v_mfma_f32_32x32x16_bf16 v[32:47], a[116:119], v[160:163], v[32:47]\n\t" "v_mfma_f32_32x32x16_bf16 v[48:63], a[124:127], v[160:163], v[48:63]\n\t" \
   "v_mfma_f32_32x32x16_bf16 v[32:47], a[128:131], v[160:163], v[32:47]\n\t" "v_mfma_f32_32x32x16_bf16 v[48:63], a[136:139], v[160:163], v[48:63]\n\t" \
   "v_mfma_f32_32x32x16_bf16 v[32:47], a[132:135], v[160:163], v[32:47]\n\t" "v_mfma_f32_32x32x16_bf16 v[48:63], a[140:143], v[160:163], v[48:63]\n\t" \
   "v_mfma_f32_32x32x16_bf16 v[32:47], a[144:147], v[160:163], v[32:47]\n\t" "v_mfma_f32_32x32x16_bf16 v[48:63], a[152:155], v[160:163], v[48:63]\n\t" \
   "v_mfma_f32_32x32x16_bf16 v[32:47], a[148:151], v[160:163], v[32:47]\n\t" "v_mfma_f32_32x32x16_bf16 v[48:63], a[156:159], v[160:163], v[48:63]\n\t" \
   "v_mfma_f32_32x32x16_bf16 v[32:47], a[160:163], v[160:163], v[32:47]\n\t" "v_mfma_f32_32x32x16_bf16 v[48:63], a[168:171], v[160:163], v[48:63]\n\t" \
   "v_mfma_f32_32x32x16_bf16 v[32:47], a[164:167], v[160:163], v[32:47]\n\t" "v_mfma_f32_32x32x16_bf16 v[48:63], a[172:175], v[160:163], v[48:63]\n\t" \
   "v_mfma_f32_32x32x16_bf16 v[32:47], a[176:179], v[160:163], v[32:47]\n\t" "v_mfma_f32_32x32x16_bf16 v[48:63], a[184:187], v[160:163], v[48:63]\n\t" \
   "v_mfma_f32_32x32x16_bf16 v[32:47], a[180:183], v[160:163], v[32:47]\n\t" "v_mfma_f32_32x32x16_bf16 v[48:63], a[188:191], v[160:163], v[48:63]\n\t" \
   "v_mfma_f32_32x32x16_bf16 v[32:47], a[192:195], v[160:163], v[32:47]\n\t" "v_mfma_f32_32x32x16_bf16 v[48:63], a[200:203], v[160:163], v[48:63]\n\t" \
   "v_mfma_f32_32x32x16_bf16 v[32:47], a[196:199], v[160:163], v[32:47]\n\t" "v_mfma_f32_32x32x16_bf16 v[48:63], a[204:207], v[160:163], v[48:63]\n\t" \
   "v_mfma_f32_32x32x16_bf16 v[32:47], a[208:211], v[160:163], v[32:47]\n\t" "v_mfma_f32_32x32x16_bf16 v[48:63], a[216:219], v[160:163], v[48:63]\n\t" \
   "v_mfma_f32_32x32x16_bf16 v[32:47], a[212:215], v[160:163], v[32:47]\n\t" "v_mfma_f32_32x32x16_bf16 v[48:63], a[220:223], v[160:163], v[48:63]\n\t" \
   "v_mfma_f32_32x32x16_bf16 v[32:47], a[224:227], v[160:163], v[32:47]\n\t" "v_mfma_f32_32x32x16_bf16 v[48:63], a[232:235], v[160:163], v[48:63]\n\t" \
   "v_mfma_f32_32x32x16_bf16 v[32:47], a[228:231], v[160:163], v[32:47]\n\t" "v_mfma_f32_32x32x16_bf16 v[48:63], a[236:239], v[160:163], v[48:63]\n\t" \
   "v_mfma_f32_32x32x16_bf16 v[32:47], a[240:243], v[160:163], v[32:47]\n\t" "v_mfma_f32_32x32x16_bf16 v[48:63], a[248:251], v[160:163], v[48:63]\n\t" \
   "v_mfma_f32_32x32x16_bf16 v[32:47], a[244:247], v[160:163], v[32:47]\n\t" "v_mfma_f32_32x32x16_bf16 v[48:63], a[252:255], v[160:163], v[48:63]\n\t" \
   "v_mfma_f32_32x32x16_bf16 v[32:47], v[192:195], v[160:163], v[32:47]\n\t" "v_mfma_f32_32x32x16_bf16 v[48:63], v[200:203], v[160:163], v[48:63]\n\t" \
   "v_mfma_f32_32x32x16_bf16 v[32:47], v[196:199], v[160:163], v[32:47]\n\t" "v_mfma_f32_32x32x16_bf16 v[48:63], v[204:207], v[160:163], v[48:63]\n\t" \
   "v_mfma_f32_32x32x16_bf16 v[32:47], v[208:211], v[160:163], v[32:47]\n\t" "v_mfma_f32_32x32x16_bf16 v[48:63], v[216:219], v[160:163], v[48:63]\n\t" \
   "v_mfma_f32_32x32x16_bf16 v[32:47], v[212:215], v[160:163], v[32:47]\n\t" "v_mfma_f32_32x32x16_bf16 v[48:63], v[220:223], v[160:163], v[48:63]\n\t" \
   "v_mfma_f32_32x32x16_bf16 v[32:47], v[224:227], v[160:163], v[32:47]\n\t" "v_mfma_f32_32x32x16_bf16 v[48:63], v[232:235], v[160:163], v[48:63]\n\t" \
   "v_mfma_f32_32x32x16_bf16 v[32:47], v[228:231], v[160:163], v[32:47]\n\t" "v_mfma_f32_32x32x16_bf16 v[48:63], v[236:239], v[160:163], v[48:63]\n\t" \
   "v_mfma_f32_32x32x16_bf16 v[32:47], v[240:243], v[160:163], v[32:47]\n\t" "v_mfma_f32_32x32x16_bf16 v[48:63], v[248:251], v[160:163], v[48:63]\n\t" \
   "v_mfma_f32_32x32x16_bf16 v[32:47], v[244:247], v[160:163], v[32:47]\n\t" "v_mfma_f32_32x32x16_bf16 v[48:63], v[252:255], v[160:163], v[48:63]\n\t" \
   "s_mov_b32 s40, 0\n\t" "s_mov_b32 s41, 0\n\t" "s_mov_b32 s42, 0\n\t" "s_mov_b32 s43, 44032\n\t" "v_mov_b32 v28, v22\n\t" "v_mov_b32 v29, v23\n\t" \
   "s_mov_b32 s45, 2\n\t" "s_mov_b32 s46, 88064\n\t" "s_waitcnt vmcnt(11) lgkmcnt(0)\n\t" "s_barrier\n\t" "s_lshr_b32 s48, s45, 1\n\t" \
   "s_bitcmp1_b32 s44, s48\n\t" "s_cbranch_scc0 172f\n\t" "72:\n\t" "ds_read_b128 v[160:163], v28 offset:0\n\t" \
   "ds_read_b128 v[164:167], v28 offset:512\n\t" "ds_read_b128 v[168:171], v28 offset:1024\n\t" "ds_read_b128 v[172:175], v28 offset:1536\n\t" "1:\n\t" \
   "v_mov_b32 v26, v28\n\t" "v_mov_b32 v27, v29\n\t" "s_waitcnt lgkmcnt(2)\n\t" "v_mfma_f32_32x32x16_bf16 v[64:79], a[0:3], v[160:163], 0\n\t" \
   "ds_read_b128 v[176:179], v26 offset:2048\n\t" "v_mfma_f32_32x32x16_bf16 v[80:95], a[8:11], v[160:163], 0\n\t" "v_mul_f32 v11, 0x3e0293ee, v10\n\t" \
   "s_lshl_b32 s47, s41, 13\n\t" "s_sub_i32 s47, s47, 8192\n\t" "s_max_i32 s47, s47, 0\n\t" "s_cmp_eq_u32 s40, 0\n\t" \
   "s_cselect_b32 s48, 0x7ffffff0, 0\n\t" "v_mfma_f32_32x32x16_bf16 v[64:79], a[4:7], v[164:167], v[64:79]\n\t" \
   "ds_read_b128 v[180:183], v26 offset:2560\n\t" "v_or_b32 v30, s48, v21\n\t" "v_fmac_f32 v96, v32, v8\n\t" "v_fmac_f32 v97, v33, v8\n\t" \
   "v_fmac_f32 v98, v34, v8\n\t" "v_mfma_f32_32x32x16_bf16 v[80:95], a[12:15], v[164:167], v[80:95]\n\t" "v_fmac_f32 v99, v35, v8\n\t" \
   "v_fmac_f32 v100, v36, v8\n\t" "v_fmac_f32 v101, v37, v8\n\t" "v_fmac_f32 v102, v38, v8\n\t" "v_fmac_f32 v103, v39, v8\n\t" \
   "s_waitcnt lgkmcnt(2)\n\t" "v_mfma_f32_32x32x16_bf16 v[64:79], a[16:19], v[168:171], v[64:79]\n\t" "ds_read_b128 v[184:187], v26 offset:3072\n\t" \
   "v_fmac_f32 v104, v40, v8\n\t" "v_fmac_f32 v105, v41, v8\n\t" "v_fmac_f32 v106, v42, v8\n\t" "v_fmac_f32 v107, v43, v8\n\t" \
   "v_mfma_f32_32x32x16_bf16 v[80:95], a[24:27], v[168:171], v[80:95]\n\t" "v_fmac_f32 v108, v44, v8\n\t" "v_fmac_f32 v109, v45, v8\n\t" \
   "v_fmac_f32 v110, v46, v8\n\t" "v_fmac_f32 v111, v47, v8\n\t" "v_mul_f32 v96, v96, v11\n\t" "v_mul_f32 v97, v97, v11\n\t" \
   "v_mfma_f32_32x32x16_bf16 v[64:79], a[20:23], v[172:175], v[64:79]\n\t" "ds_read_b128 v[188:191], v26 offset:3584\n\t" "v_mul_f32 v98, v98, v11\n\t" \
   "v_mul_f32 v99, v99, v11\n\t" "v_mul_f32 v100, v100, v11\n\t" "v_mul_f32 v101, v101, v11\n\t" \
   "v_mfma_f32_32x32x16_bf16 v[80:95], a[28:31], v[172:175], v[80:95]\n\t" "v_mul_f32 v102, v102, v11\n\t" "v_mul_f32 v103, v103, v11\n\t" \
   "v_mul_f32 v104, v104, v11\n\t" "v_mul_f32 v105, v105, v11\n\t" "v_mul_f32 v106, v106, v11\n\t" "s_waitcnt lgkmcnt(2)\n\t" \
   "v_mfma_f32_32x32x16_bf16 v[64:79], a[32:35], v[176:179], v[64:79]\n\t" "ds_read_b128 v[160:163], v26 offset:4096\n\t" \
   "v_mul_f32 v107, v107, v11\n\t" "v_mul_f32 v108, v108, v11\n\t" "v_mul_f32 v109, v109, v11\n\t" "v_mul_f32 v110, v110, v11\n\t" \
   "v_mfma_f32_32x32x16_bf16 v[80:95], a[40:43], v[176:179], v[80:95]\n\t" "v_mul_f32 v111, v111, v11\n\t" "s_add_u32 s48, s47, %[qf]\n\t" \
   "v_cvt_pk_bf16_f32 v96, v96, v97\n\t" "v_cvt_pk_bf16_f32 v97, v98, v99\n\t" "v_cvt_pk_bf16_f32 v98, v104, v105\n\t" \
   "v_cvt_pk_bf16_f32 v99, v106, v107\n\t" "v_mfma_f32_32x32x16_bf16 v[64:79], a[36:39], v[180:183], v[64:79]\n\t" \
   "ds_read_b128 v[164:167], v26 offset:4608\n\t" "v_mfma_f32_32x32x16_bf16 v[80:95], a[44:47], v[180:183], v[80:95]\n\t" \
   "buffer_store_dwordx4 v[96:99], v30, %[rs], s48 offen offset:0 sc1\n\t" "s_waitcnt lgkmcnt(2)\n\t" \
   "v_mfma_f32_32x32x16_bf16 v[64:79], a[48:51], v[184:187], v[64:79]\n\t" "ds_read_b128 v[168:171], v26 offset:5120\n\t" \
   "v_cvt_pk_bf16_f32 v100, v100, v101\n\t" "v_cvt_pk_bf16_f32 v101, v102, v103\n\t" "v_cvt_pk_bf16_f32 v102, v108, v109\n\t" \
   "v_cvt_pk_bf16_f32 v103, v110, v111\n\t" "v_mfma_f32_32x32x16_bf16 v[80:95], a[56:59], v[184:187], v[80:95]\n\t" \
   "buffer_store_dwordx4 v[100:103], v30, %[rs], s48 offen offset:512 sc1\n\t" "v_fmac_f32 v112, v48, v8\n\t" \
   "v_mfma_f32_32x32x16_bf16 v[64:79], a[52:55], v[188:191], v[64:79]\n\t" "ds_read_b128 v[172:175], v26 offset:5632\n\t" \
   "v_fmac_f32 v113, v49, v8\n\t" "v_fmac_f32 v114, v50, v8\n\t" "v_fmac_f32 v115, v51, v8\n\t" "v_fmac_f32 v116, v52, v8\n\t" \
   "v_mfma_f32_32x32x16_bf16 v[80:95], a[60:63], v[188:191], v[80:95]\n\t" "v_fmac_f32 v117, v53, v8\n\t" "v_fmac_f32 v118, v54, v8\n\t" \
   "v_fmac_f32 v119, v55, v8\n\t" "v_fmac_f32 v120, v56, v8\n\t" "v_fmac_f32 v121, v57, v8\n\t" "s_waitcnt lgkmcnt(2)\n\t" \
   "v_mfma_f32_32x32x16_bf16 v[64:79], a[64:67], v[160:163], v[64:79]\n\t" "ds_read_b128 v[176:179], v26 offset:6144\n\t" \
   "v_fmac_f32 v122, v58, v8\n\t" "v_fmac_f32 v123, v59, v8\n\t" "v_fmac_f32 v124, v60, v8\n\t" "v_fmac_f32 v125, v61, v8\n\t" \
   "v_mfma_f32_32x32x16_bf16 v[80:95], a[72:75], v[160:163], v[80:95]\n\t" "v_fmac_f32 v126, v62, v8\n\t" "v_fmac_f32 v127, v63, v8\n\t" \
   "v_mul_f32 v112, v112, v10\n\t" "v_mul_f32 v113, v113, v10\n\t" "v_mul_f32 v114, v114, v10\n\t" "v_mul_f32 v115, v115, v10\n\t" \
   "v_mfma_f32_32x32x16_bf16 v[64:79], a[68:71], v[164:167], v[64:79]\n\t" "ds_read_b128 v[180:183], v26 offset:6656\n\t" \
   "v_mul_f32 v116, v116, v10\n\t" "v_mul_f32 v117, v117, v10\n\t" "v_mul_f32 v118, v118, v10\n\t" "v_mul_f32 v119, v119, v10\n\t" \
   "v_mfma_f32_32x32x16_bf16 v[80:95], a[76:79], v[164:167], v[80:95]\n\t" "v_mul_f32 v120, v120, v10\n\t" "v_mul_f32 v121, v121, v10\n\t" \
   "v_mul_f32 v122, v122, v10\n\t" "v_mul_f32 v123, v123, v10\n\t" "v_mul_f32 v124, v124, v10\n\t" "s_waitcnt lgkmcnt(2)\n\t" \
   "v_mfma_f32_32x32x16_bf16 v[64:79], a[80:83], v[168:171], v[64:79]\n\t" "ds_read_b128 v[184:187], v26 offset:7168\n\t" \
   "v_mul_f32 v125, v125, v10\n\t" "v_mul_f32 v126, v126, v10\n\t" "v_mul_f32 v127, v127, v10\n\t" "s_add_u32 s48, s47, %[kf]\n\t" \
   "v_mfma_f32_32x32x16_bf16 v[80:95], a[88:91], v[168:171], v[80:95]\n\t" "v_cvt_pk_bf16_f32 v112, v112, v113\n\t" \
   "v_cvt_pk_bf16_f32 v113, v114, v115\n\t" "v_cvt_pk_bf16_f32 v114, v120, v121\n\t" "v_cvt_pk_bf16_f32 v115, v122, v123\n\t" \
   "ds_read_b128 v[128:131], v27 offset:0\n\t" "v_mfma_f32_32x32x16_bf16 v[64:79], a[84:87], v[172:175], v[64:79]\n\t" \
   "ds_read_b128 v[188:191], v26 offset:7680\n\t" "s_add_u32 s54, s41, 1\n\t" "s_lshr_b64 s[60:61], %[sel], s54\n\t" "s_lshl_b32 s55, s54, 19\n\t" \
   "s_and_b32 s60, s60, 1\n\t" "v_mfma_f32_32x32x16_bf16 v[80:95], a[92:95], v[172:175], v[80:95]\n\t" \
   "buffer_store_dwordx4 v[112:115], v30, %[rs], s48 offen offset:0 sc1\n\t" "s_waitcnt lgkmcnt(3)\n\t" \
   "v_mfma_f32_32x32x16_bf16 v[64:79], a[96:99], v[176:179], v[64:79]\n\t" "ds_read_b128 v[160:163], v26 offset:8192\n\t" \
   "v_cvt_pk_bf16_f32 v116, v116, v117\n\t" "v_cvt_pk_bf16_f32 v117, v118, v119\n\t" "v_cvt_pk_bf16_f32 v118, v124, v125\n\t" \
   "v_cvt_pk_bf16_f32 v119, v126, v127\n\t" "v_mfma_f32_32x32x16_bf16 v[80:95], a[104:107], v[176:179], v[80:95]\n\t" \
   "buffer_store_dwordx4 v[116:119], v30, %[rs], s48 offen offset:512 sc1\n\t" "s_add_u32 s55, s55, %[pool]\n\t" \
   "v_mfma_f32_32x32x16_bf16 v[64:79], a[100:103], v[180:183], v[64:79]\n\t" "ds_read_b128 v[164:167], v26 offset:8704\n\t" \
   "s_lshl_b32 s60, s60, 13\n\t" "s_add_u32 s55, s55, s60\n\t" "ds_read_b128 v[132:135], v27 offset:16\n\t" "v_add_u32 v28, s43, v22\n\t" \
   "v_mfma_f32_32x32x16_bf16 v[80:95], a[108:111], v[180:183], v[80:95]\n\t" "s_bitcmp1_b64 %[skip], s41\n\t" "s_cselect_b32 s58, 0, %[size]\n\t" \
   "ds_read_b64 v[136:137], v27 offset:32\n\t" "v_add_u32 v29, s43, v23\n\t" "s_waitcnt lgkmcnt(4)\n\t" \
   "v_mfma_f32_32x32x16_bf16 v[64:79], a[112:115], v[184:187], v[64:79]\n\t" "ds_read_b128 v[168:171], v26 offset:9216\n\t" \
   "v_mfma_f32_32x32x16_bf16 v[80:95], a[120:123], v[184:187], v[80:95]\n\t" \
   "buffer_store_dwordx4 v[156:159], v152, s[56:59], s55 offen offset:0 sc1\n\t" \
   "v_mfma_f32_32x32x16_bf16 v[64:79], a[116:119], v[188:191], v[64:79]\n\t" "ds_read_b128 v[172:175], v26 offset:9728\n\t" \
   "ds_read_b128 v[140:143], v12\n\t" "s_waitcnt lgkmcnt(3)\n\t" "v_add_f32 v128, v128, v130\n\t" "v_add_f32 v129, v129, v131\n\t" \
   "v_mfma_f32_32x32x16_bf16 v[80:95], a[124:127], v[188:191], v[80:95]\n\t" \
   "buffer_store_dwordx4 v[156:159], v152, s[56:59], s55 offen offset:1024 sc1\n\t" \
   "v_mfma_f32_32x32x16_bf16 v[64:79], a[128:131], v[160:163], v[64:79]\n\t" "ds_read_b128 v[176:179], v26 offset:10240\n\t" "s_waitcnt lgkmcnt(1)\n\t" \
   "v_min3_u32 v140, v140, v141, v142\n\t" "v_min_u32 v140, v140, v143\n\t" "v_cmp_ge_u32 vcc, v140, v138\n\t" \
   "v_mfma_f32_32x32x16_bf16 v[80:95], a[136:139], v[160:163], v[80:95]\n\t" "s_cbranch_vccz 150f\n\t" "50:\n\t" "s_mul_i32 s52, s45, 1536\n\t" \
   "s_mul_i32 s51, s45, 40960\n\t" "s_add_u32 m0, s46, %[ldx]\n\t" "s_mov_b32 s53, s46\n\t" \
   "v_mfma_f32_32x32x16_bf16 v[64:79], a[132:135], v[164:167], v[64:79]\n\t" "ds_read_b128 v[180:183], v26 offset:10752\n\t" \
   "buffer_load_dwordx4 v16, %[rs], s51 offen offset:0 sc1 lds\n\t" "s_lshr_b64 s[60:61], %[ysel], s54\n\t" \
   "v_mfma_f32_32x32x16_bf16 v[80:95], a[140:143], v[164:167], v[80:95]\n\t" "s_mul_i32 s61, s54, 43\n\t" "s_mul_i32 s55, s54, 80\n\t" \
   "v_add_f32 v132, v132, v134\n\t" "s_and_b32 s60, s60, 1\n\t" "v_add_f32 v133, v133, v135\n\t" \
   "v_mfma_f32_32x32x16_bf16 v[64:79], a[144:147], v[168:171], v[64:79]\n\t" "ds_read_b128 v[184:187], v26 offset:11264\n\t" \
   "v_add_f32 v128, v128, v136\n\t" "s_lshr_b32 s61, s61, 7\n\t" "v_add_f32 v129, v129, v137\n\t" "v_add_f32 v128, v128, v132\n\t" \
   "v_mfma_f32_32x32x16_bf16 v[80:95], a[152:155], v[168:171], v[80:95]\n\t" "buffer_load_dwordx4 v16, %[rs], s51 offen offset:1024 sc1 lds\n\t" \
   "s_add_u32 s55, s55, s60\n\t" "v_add_f32 v129, v129, v133\n\t" "v_mul_f32 v128, 0xbacccccd, v128\n\t" \
   "v_mfma_f32_32x32x16_bf16 v[64:79], a[148:151], v[172:175], v[64:79]\n\t" "ds_read_b128 v[188:191], v26 offset:11776\n\t" \
   "s_lshl4_add_u32 s55, s61, s55\n\t" "v_mul_f32 v129, 0x3acccccd, v129\n\t" "v_fma_f32 v129, -v128, v128, v129\n\t" "v_max_f32 v129, 0, v129\n\t" \
   "v_mfma_f32_32x32x16_bf16 v[80:95], a[156:159], v[172:175], v[80:95]\n\t" "s_lshl_b32 s55, s55, 13\n\t" "v_cvt_pk_bf16_f32 v129, v129, v128\n\t" \
   "v_and_b32 v8, 0xffff0000, v129\n\t" "v_lshlrev_b32 v129, 16, v129\n\t" "s_add_u32 s55, s55, %[yb]\n\t" "s_waitcnt lgkmcnt(2)\n\t" \
   "v_mfma_f32_32x32x16_bf16 v[64:79], a[160:163], v[176:179], v[64:79]\n\t" "ds_read_b128 v[160:163], v26 offset:12288\n\t" \
   "buffer_load_dwordx4 v16, %[rs], s51 offen offset:2048 sc1 lds\n\t" "v_add_f32 v129, 0x3727c5ac, v129\n\t" \
   "v_mfma_f32_32x32x16_bf16 v[80:95], a[168:171], v[176:179], v[80:95]\n\t" "v_cvt_pk_bf16_f32 v129, v129, v129\n\t" "s_cmp_eq_u32 s41, 63\n\t" \
   "s_cselect_b32 s58, 0, %[size]\n\t" "v_lshlrev_b32 v129, 16, v129\n\t" "v_sqrt_f32 v129, v129\n\t" \
   "v_mfma_f32_32x32x16_bf16 v[64:79], a[164:167], v[180:183], v[64:79]\n\t" "ds_read_b128 v[164:167], v26 offset:12800\n\t" "s_nop 0\n\t" \
   "v_cvt_pk_bf16_f32 v129, v129, v129\n\t" "v_lshlrev_b32 v129, 16, v129\n\t" \
   "v_mfma_f32_32x32x16_bf16 v[80:95], a[172:175], v[180:183], v[80:95]\n\t" "buffer_load_dwordx4 v16, %[rs], s51 offen offset:3072 sc1 lds\n\t" \
   "s_add_u32 m0, m0, 4096\n\t" "s_add_u32 s51, s51, 4096\n\t" "s_waitcnt lgkmcnt(2)\n\t" \
   "v_mfma_f32_32x32x16_bf16 v[64:79], a[176:179], v[184:187], v[64:79]\n\t" "ds_read_b128 v[168:171], v26 offset:13312\n\t" "s_waitcnt vmcnt(10)\n\t" \
   "v_add_u32 v138, 1, v138\n\t" "ds_write_b32 v13, v138\n\t" "v_mfma_f32_32x32x16_bf16 v[80:95], a[184:187], v[184:187], v[80:95]\n\t" \
   "v_rcp_f32 v10, v129\n\t" "v_mfma_f32_32x32x16_bf16 v[64:79], a[180:183], v[188:191], v[64:79]\n\t" "ds_read_b128 v[172:175], v26 offset:13824\n\t" \
   "buffer_load_dwordx4 v16, %[rs], s51 offen offset:0 sc1 lds\n\t" "v_mfma_f32_32x32x16_bf16 v[80:95], a[188:191], v[188:191], v[80:95]\n\t" \
   "s_waitcnt lgkmcnt(3)\n\t" "v_mfma_f32_32x32x16_bf16 v[64:79], a[192:195], v[160:163], v[64:79]\n\t" "ds_read_b128 v[176:179], v26 offset:14336\n\t" \
   "v_mfma_f32_32x32x16_bf16 v[80:95], a[200:203], v[160:163], v[80:95]\n\t" "buffer_store_dwordx4 v[156:159], v153, s[56:59], s55 offen sc1\n\t" \
   "v_mfma_f32_32x32x16_bf16 v[64:79], a[196:199], v[164:167], v[64:79]\n\t" "ds_read_b128 v[180:183], v26 offset:14848\n\t" \
   "buffer_load_dwordx4 v16, %[rs], s51 offen offset:1024 sc1 lds\n\t" "v_mfma_f32_32x32x16_bf16 v[80:95], a[204:207], v[164:167], v[80:95]\n\t" \
   "s_waitcnt lgkmcnt(2)\n\t" "v_mfma_f32_32x32x16_bf16 v[64:79], a[208:211], v[168:171], v[64:79]\n\t" "ds_read_b128 v[184:187], v26 offset:15360\n\t" \
   "buffer_load_dwordx4 v16, %[rs], s51 offen offset:2048 sc1 lds\n\t" "v_mfma_f32_32x32x16_bf16 v[80:95], a[216:219], v[168:171], v[80:95]\n\t" \
   "buffer_store_dwordx4 v[156:159], v154, s[56:59], s55 offen sc1\n\t" "v_mfma_f32_32x32x16_bf16 v[64:79], a[212:215], v[172:175], v[64:79]\n\t" \
   "ds_read_b128 v[188:191], v26 offset:15872\n\t" "v_mfma_f32_32x32x16_bf16 v[80:95], a[220:223], v[172:175], v[80:95]\n\t" \
   "buffer_load_dwordx4 v16, %[rs], s51 offen offset:3072 sc1 lds\n\t" "s_add_u32 m0, m0, 4096\n\t" "s_add_u32 s51, s51, 4096\n\t" \
   "s_waitcnt lgkmcnt(2)\n\t" "v_mfma_f32_32x32x16_bf16 v[64:79], a[224:227], v[176:179], v[64:79]\n\t" "ds_read_b128 v[160:163], v26 offset:16384\n\t" \
   "v_mfma_f32_32x32x16_bf16 v[80:95], a[232:235], v[176:179], v[80:95]\n\t" "buffer_store_dwordx4 v[156:159], v155, s[56:59], s55 offen sc1\n\t" \
   "v_mfma_f32_32x32x16_bf16 v[64:79], a[228:231], v[180:183], v[64:79]\n\t" "ds_read_b128 v[164:167], v26 offset:16896\n\t" \
   "buffer_load_dwordx4 v16, %[rs], s51 offen offset:0 sc1 lds\n\t" "v_mfma_f32_32x32x16_bf16 v[80:95], a[236:239], v[180:183], v[80:95]\n\t" \
   "s_waitcnt lgkmcnt(2)\n\t" "v_mfma_f32_32x32x16_bf16 v[64:79], a[240:243], v[184:187], v[64:79]\n\t" "ds_read_b128 v[168:171], v26 offset:17408\n\t" \
   "s_cmp_lt_i32 s40, 1\n\t" "s_cselect_b32 s49, 0x7ffffff0, 0\n\t" "v_or_b32 v9, s49, v20\n\t" "s_sub_i32 s49, s41, 1\n\t" \
   "v_mfma_f32_32x32x16_bf16 v[80:95], a[248:251], v[184:187], v[80:95]\n\t" "s_max_i32 s49, s49, 0\n\t" "s_lshl_b32 s49, s49, 12\n\t" \
   "s_waitcnt vmcnt(14)\n\t" "buffer_store_dword v31, v9, %[rs], s49 offen sc1\n\t" \
   "v_mfma_f32_32x32x16_bf16 v[64:79], a[244:247], v[188:191], v[64:79]\n\t" "ds_read_b128 v[172:175], v26 offset:17920\n\t" \
   "v_mfma_f32_32x32x16_bf16 v[80:95], a[252:255], v[188:191], v[80:95]\n\t" "buffer_load_dwordx4 v16, %[rs], s51 offen offset:1024 sc1 lds\n\t" \
   "s_add_u32 m0, s53, %[ld1]\n\t" "s_nop 0\n\t" "s_waitcnt lgkmcnt(2)\n\t" "v_mfma_f32_32x32x16_bf16 v[64:79], v[192:195], v[160:163], v[64:79]\n\t" \
   "ds_read_b128 v[176:179], v26 offset:18432\n\t" "buffer_load_dwordx4 v17, %[rs], s52 offen sc1 lds\n\t" "ds_read_b128 v[140:143], v12\n\t" \
   "v_mfma_f32_32x32x16_bf16 v[80:95], v[200:203], v[160:163], v[80:95]\n\t" "v_mfma_f32_32x32x16_bf16 v[64:79], v[196:199], v[164:167], v[64:79]\n\t" \
   "ds_read_b128 v[180:183], v26 offset:18944\n\t" "v_mfma_f32_32x32x16_bf16 v[80:95], v[204:207], v[164:167], v[80:95]\n\t" "s_waitcnt lgkmcnt(3)\n\t" \
   "v_mfma_f32_32x32x16_bf16 v[64:79], v[208:211], v[168:171], v[64:79]\n\t" "ds_read_b128 v[184:187], v26 offset:19456\n\t" "s_waitcnt lgkmcnt(2)\n\t" \
   "v_min3_u32 v140, v140, v141, v142\n\t" "v_min_u32 v140, v140, v143\n\t" "v_cmp_ge_u32 vcc, v140, v138\n\t" \
   "v_mfma_f32_32x32x16_bf16 v[80:95], v[216:219], v[168:171], v[80:95]\n\t" "s_cbranch_vccz 152f\n\t" "52:\n\t" \
   "v_mfma_f32_32x32x16_bf16 v[64:79], v[212:215], v[172:175], v[64:79]\n\t" "ds_read_b128 v[188:191], v26 offset:19968\n\t" \
   "v_mfma_f32_32x32x16_bf16 v[80:95], v[220:223], v[172:175], v[80:95]\n\t" "v_add_u32 v138, 1, v138\n\t" "ds_write_b32 v13, v138\n\t" \
   "s_waitcnt lgkmcnt(3)\n\t" "v_mfma_f32_32x32x16_bf16 v[64:79], v[224:227], v[176:179], v[64:79]\n\t" "ds_read_b128 v[160:163], v28 offset:0\n\t" \
   "s_add_u32 s45, s40, 3\n\t" "s_min_u32 s45, s45, 63\n\t" "s_mov_b32 s46, s42\n\t" \
   "v_mfma_f32_32x32x16_bf16 v[80:95], v[232:235], v[176:179], v[80:95]\n\t" "s_lshr_b32 s48, s45, 1\n\t" "s_bitcmp1_b32 s44, s48\n\t" \
   "s_cbranch_scc0 174f\n\t" "74:\n\t" "s_mov_b32 s42, s43\n\t" "s_add_u32 s43, s43, 44032\n\t" \
   "v_mfma_f32_32x32x16_bf16 v[64:79], v[228:231], v[180:183], v[64:79]\n\t" "ds_read_b128 v[164:167], v28 offset:512\n\t" \
   "s_cmp_eq_u32 s43, 132096\n\t" "s_cselect_b32 s43, 0, s43\n\t" "s_add_u32 s41, s41, 1\n\t" "s_add_u32 s40, s40, 1\n\t" \
   "v_mfma_f32_32x32x16_bf16 v[80:95], v[236:239], v[180:183], v[80:95]\n\t" "s_waitcnt lgkmcnt(3)\n\t" \
   "v_mfma_f32_32x32x16_bf16 v[64:79], v[240:243], v[184:187], v[64:79]\n\t" "ds_read_b128 v[168:171], v28 offset:1024\n\t" \
   "v_mfma_f32_32x32x16_bf16 v[80:95], v[248:251], v[184:187], v[80:95]\n\t" "v_mfma_f32_32x32x16_bf16 v[64:79], v[244:247], v[188:191], v[64:79]\n\t" \
   "ds_read_b128 v[172:175], v28 offset:1536\n\t" "v_mfma_f32_32x32x16_bf16 v[80:95], v[252:255], v[188:191], v[80:95]\n\t" "v_mov_b32 v26, v28\n\t" \
   "v_mov_b32 v27, v29\n\t" "s_waitcnt lgkmcnt(2)\n\t" "v_mfma_f32_32x32x16_bf16 v[96:111], a[0:3], v[160:163], 0\n\t" \
   "ds_read_b128 v[176:179], v26 offset:2048\n\t" "v_mfma_f32_32x32x16_bf16 v[112:127], a[8:11], v[160:163], 0\n\t" \
   "v_mul_f32 v11, 0x3e0293ee, v10\n\t" "s_lshl_b32 s47, s41, 13\n\t" "s_sub_i32 s47, s47, 8192\n\t" "v_fmac_f32 v64, v32, v8\n\t" \
   "v_fmac_f32 v65, v33, v8\n\t" "v_fmac_f32 v66, v34, v8\n\t" "v_mfma_f32_32x32x16_bf16 v[96:111], a[4:7], v[164:167], v[96:111]\n\t" \
   "ds_read_b128 v[180:183], v26 offset:2560\n\t" "v_fmac_f32 v67, v35, v8\n\t" "v_fmac_f32 v68, v36, v8\n\t" "v_fmac_f32 v69, v37, v8\n\t" \
   "v_fmac_f32 v70, v38, v8\n\t" "v_mfma_f32_32x32x16_bf16 v[112:127], a[12:15], v[164:167], v[112:127]\n\t" "v_fmac_f32 v71, v39, v8\n\t" \
   "v_fmac_f32 v72, v40, v8\n\t" "v_fmac_f32 v73, v41, v8\n\t" "v_fmac_f32 v74, v42, v8\n\t" "v_fmac_f32 v75, v43, v8\n\t" "s_waitcnt lgkmcnt(2)\n\t" \
   "v_mfma_f32_32x32x16_bf16 v[96:111], a[16:19], v[168:171], v[96:111]\n\t" "ds_read_b128 v[184:187], v26 offset:3072\n\t" \
   "v_fmac_f32 v76, v44, v8\n\t" "v_fmac_f32 v77, v45, v8\n\t" "v_fmac_f32 v78, v46, v8\n\t" "v_fmac_f32 v79, v47, v8\n\t" \
   "v_mfma_f32_32x32x16_bf16 v[112:127], a[24:27], v[168:171], v[112:127]\n\t" "v_mul_f32 v64, v64, v11\n\t" "v_mul_f32 v65, v65, v11\n\t" \
   "v_mul_f32 v66, v66, v11\n\t" "v_mul_f32 v67, v67, v11\n\t" "v_mul_f32 v68, v68, v11\n\t" "v_mul_f32 v69, v69, v11\n\t" \
   "v_mfma_f32_32x32x16_bf16 v[96:111], a[20:23], v[172:175], v[96:111]\n\t" "ds_read_b128 v[188:191], v26 offset:3584\n\t" \
   "v_mul_f32 v70, v70, v11\n\t" "v_mul_f32 v71, v71, v11\n\t" "v_mul_f32 v72, v72, v11\n\t" "v_mul_f32 v73, v73, v11\n\t" \
   "v_mfma_f32_32x32x16_bf16 v[112:127], a[28:31], v[172:175], v[112:127]\n\t" "v_mul_f32 v74, v74, v11\n\t" "v_mul_f32 v75, v75, v11\n\t" \
   "v_mul_f32 v76, v76, v11\n\t" "v_mul_f32 v77, v77, v11\n\t" "v_mul_f32 v78, v78, v11\n\t" "s_waitcnt lgkmcnt(2)\n\t" \
   "v_mfma_f32_32x32x16_bf16 v[96:111], a[32:35], v[176:179], v[96:111]\n\t" "ds_read_b128 v[160:163], v26 offset:4096\n\t" \
   "v_mul_f32 v79, v79, v11\n\t" "s_add_u32 s48, s47, %[qf]\n\t" "v_cvt_pk_bf16_f32 v64, v64, v65\n\t" "v_cvt_pk_bf16_f32 v65, v66, v67\n\t" \
   "v_mfma_f32_32x32x16_bf16 v[112:127], a[40:43], v[176:179], v[112:127]\n\t" "v_cvt_pk_bf16_f32 v66, v72, v73\n\t" \
   "v_cvt_pk_bf16_f32 v67, v74, v75\n\t" "v_mfma_f32_32x32x16_bf16 v[96:111], a[36:39], v[180:183], v[96:111]\n\t" \
   "ds_read_b128 v[164:167], v26 offset:4608\n\t" "v_mfma_f32_32x32x16_bf16 v[112:127], a[44:47], v[180:183], v[112:127]\n\t" \
   "buffer_store_dwordx4 v[64:67], v21, %[rs], s48 offen offset:0 sc1\n\t" "s_waitcnt lgkmcnt(2)\n\t" \
   "v_mfma_f32_32x32x16_bf16 v[96:111], a[48:51], v[184:187], v[96:111]\n\t" "ds_read_b128 v[168:171], v26 offset:5120\n\t" \
   "v_cvt_pk_bf16_f32 v68, v68, v69\n\t" "v_cvt_pk_bf16_f32 v69, v70, v71\n\t" "v_cvt_pk_bf16_f32 v70, v76, v77\n\t" \
   "v_cvt_pk_bf16_f32 v71, v78, v79\n\t" "v_mfma_f32_32x32x16_bf16 v[112:127], a[56:59], v[184:187], v[112:127]\n\t" \
   "buffer_store_dwordx4 v[68:71], v21, %[rs], s48 offen offset:512 sc1\n\t" "v_fmac_f32 v80, v48, v8\n\t" \
   "v_mfma_f32_32x32x16_bf16 v[96:111], a[52:55], v[188:191], v[96:111]\n\t" "ds_read_b128 v[172:175], v26 offset:5632\n\t" \
   "v_fmac_f32 v81, v49, v8\n\t" "v_fmac_f32 v82, v50, v8\n\t" "v_fmac_f32 v83, v51, v8\n\t" "v_fmac_f32 v84, v52, v8\n\t" \
   "v_mfma_f32_32x32x16_bf16 v[112:127], a[60:63], v[188:191], v[112:127]\n\t" "v_fmac_f32 v85, v53, v8\n\t" "v_fmac_f32 v86, v54, v8\n\t" \
   "v_fmac_f32 v87, v55, v8\n\t" "v_fmac_f32 v88, v56, v8\n\t" "v_fmac_f32 v89, v57, v8\n\t" "s_waitcnt lgkmcnt(2)\n\t" \
   "v_mfma_f32_32x32x16_bf16 v[96:111], a[64:67], v[160:163], v[96:111]\n\t" "ds_read_b128 v[176:179], v26 offset:6144\n\t" \
   "v_fmac_f32 v90, v58, v8\n\t" "v_fmac_f32 v91, v59, v8\n\t" "v_fmac_f32 v92, v60, v8\n\t" "v_fmac_f32 v93, v61, v8\n\t" \
   "v_mfma_f32_32x32x16_bf16 v[112:127], a[72:75], v[160:163], v[112:127]\n\t" "v_fmac_f32 v94, v62, v8\n\t" "v_fmac_f32 v95, v63, v8\n\t" \
   "v_mul_f32 v80, v80, v10\n\t" "v_mul_f32 v81, v81, v10\n\t" "v_mul_f32 v82, v82, v10\n\t" "v_mul_f32 v83, v83, v10\n\t" \
   "v_mfma_f32_32x32x16_bf16 v[96:111], a[68:71], v[164:167], v[96:111]\n\t" "ds_read_b128 v[180:183], v26 offset:6656\n\t" \
   "v_mul_f32 v84, v84, v10\n\t" "v_mul_f32 v85, v85, v10\n\t" "v_mul_f32 v86, v86, v10\n\t" "v_mul_f32 v87, v87, v10\n\t" \
   "v_mfma_f32_32x32x16_bf16 v[112:127], a[76:79], v[164:167], v[112:127]\n\t" "v_mul_f32 v88, v88, v10\n\t" "v_mul_f32 v89, v89, v10\n\t" \
   "v_mul_f32 v90, v90, v10\n\t" "v_mul_f32 v91, v91, v10\n\t" "v_mul_f32 v92, v92, v10\n\t" "s_waitcnt lgkmcnt(2)\n\t" \
   "v_mfma_f32_32x32x16_bf16 v[96:111], a[80:83], v[168:171], v[96:111]\n\t" "ds_read_b128 v[184:187], v26 offset:7168\n\t" \
   "v_mul_f32 v93, v93, v10\n\t" "v_mul_f32 v94, v94, v10\n\t" "v_mul_f32 v95, v95, v10\n\t" "s_add_u32 s48, s47, %[kf]\n\t" \
   "v_mfma_f32_32x32x16_bf16 v[112:127], a[88:91], v[168:171], v[112:127]\n\t" "v_cvt_pk_bf16_f32 v80, v80, v81\n\t" \
   "v_cvt_pk_bf16_f32 v81, v82, v83\n\t" "v_cvt_pk_bf16_f32 v82, v88, v89\n\t" "v_cvt_pk_bf16_f32 v83, v90, v91\n\t" \
   "ds_read_b128 v[128:131], v27 offset:0\n\t" "v_mfma_f32_32x32x16_bf16 v[96:111], a[84:87], v[172:175], v[96:111]\n\t" \
   "ds_read_b128 v[188:191], v26 offset:7680\n\t" "s_add_u32 s54, s41, 1\n\t" "s_lshr_b64 s[60:61], %[sel], s54\n\t" "s_lshl_b32 s55, s54, 19\n\t" \
   "s_and_b32 s60, s60, 1\n\t" "v_mfma_f32_32x32x16_bf16 v[112:127], a[92:95], v[172:175], v[112:127]\n\t" \
   "buffer_store_dwordx4 v[80:83], v21, %[rs], s48 offen offset:0 sc1\n\t" "s_waitcnt lgkmcnt(3)\n\t" \
   "v_mfma_f32_32x32x16_bf16 v[96:111], a[96:99], v[176:179], v[96:111]\n\t" "ds_read_b128 v[160:163], v26 offset:8192\n\t" \
   "v_cvt_pk_bf16_f32 v84, v84, v85\n\t" "v_cvt_pk_bf16_f32 v85, v86, v87\n\t" "v_cvt_pk_bf16_f32 v86, v92, v93\n\t" \
   "v_cvt_pk_bf16_f32 v87, v94, v95\n\t" "v_mfma_f32_32x32x16_bf16 v[112:127], a[104:107], v[176:179], v[112:127]\n\t" \
   "buffer_store_dwordx4 v[84:87], v21, %[rs], s48 offen offset:512 sc1\n\t" "s_add_u32 s55, s55, %[pool]\n\t" \
   "v_mfma_f32_32x32x16_bf16 v[96:111], a[100:103], v[180:183], v[96:111]\n\t" "ds_read_b128 v[164:167], v26 offset:8704\n\t" \
   "s_lshl_b32 s60, s60, 13\n\t" "s_add_u32 s55, s55, s60\n\t" "ds_read_b128 v[132:135], v27 offset:16\n\t" "v_add_u32 v28, s43, v22\n\t" \
   "v_mfma_f32_32x32x16_bf16 v[112:127], a[108:111], v[180:183], v[112:127]\n\t" "s_bitcmp1_b64 %[skip], s41\n\t" "s_cselect_b32 s58, 0, %[size]\n\t" \
   "ds_read_b64 v[136:137], v27 offset:32\n\t" "v_add_u32 v29, s43, v23\n\t" "s_waitcnt lgkmcnt(4)\n\t" \
   "v_mfma_f32_32x32x16_bf16 v[96:111], a[112:115], v[184:187], v[96:111]\n\t" "ds_read_b128 v[168:171], v26 offset:9216\n\t" \
   "v_mfma_f32_32x32x16_bf16 v[112:127], a[120:123], v[184:187], v[112:127]\n\t" \
   "buffer_store_dwordx4 v[156:159], v152, s[56:59], s55 offen offset:0 sc1\n\t" \
   "v_mfma_f32_32x32x16_bf16 v[96:111], a[116:119], v[188:191], v[96:111]\n\t" "ds_read_b128 v[172:175], v26 offset:9728\n\t" \
   "ds_read_b128 v[140:143], v12\n\t" "s_waitcnt lgkmcnt(3)\n\t" "v_add_f32 v128, v128, v130\n\t" "v_add_f32 v129, v129, v131\n\t" \
   "v_mfma_f32_32x32x16_bf16 v[112:127], a[124:127], v[188:191], v[112:127]\n\t" \
   "buffer_store_dwordx4 v[156:159], v152, s[56:59], s55 offen offset:1024 sc1\n\t" \
   "v_mfma_f32_32x32x16_bf16 v[96:111], a[128:131], v[160:163], v[96:111]\n\t" "ds_read_b128 v[176:179], v26 offset:10240\n\t" \
   "s_waitcnt lgkmcnt(1)\n\t" "v_min3_u32 v140, v140, v141, v142\n\t" "v_min_u32 v140, v140, v143\n\t" "v_cmp_ge_u32 vcc, v140, v138\n\t" \
   "v_mfma_f32_32x32x16_bf16 v[112:127], a[136:139], v[160:163], v[112:127]\n\t" "s_cbranch_vccz 151f\n\t" "51:\n\t" "s_mul_i32 s52, s45, 1536\n\t" \
   "s_mul_i32 s51, s45, 40960\n\t" "s_add_u32 m0, s46, %[ldx]\n\t" "s_mov_b32 s53, s46\n\t" \
   "v_mfma_f32_32x32x16_bf16 v[96:111], a[132:135], v[164:167], v[96:111]\n\t" "ds_read_b128 v[180:183], v26 offset:10752\n\t" \
   "buffer_load_dwordx4 v16, %[rs], s51 offen offset:0 sc1 lds\n\t" "s_lshr_b64 s[60:61], %[ysel], s54\n\t" \
   "v_mfma_f32_32x32x16_bf16 v[112:127], a[140:143], v[164:167], v[112:127]\n\t" "s_mul_i32 s61, s54, 43\n\t" "s_mul_i32 s55, s54, 80\n\t" \
   "v_add_f32 v132, v132, v134\n\t" "s_and_b32 s60, s60, 1\n\t" "v_add_f32 v133, v133, v135\n\t" \
   "v_mfma_f32_32x32x16_bf16 v[96:111], a[144:147], v[168:171], v[96:111]\n\t" "ds_read_b128 v[184:187], v26 offset:11264\n\t" \
   "v_add_f32 v128, v128, v136\n\t" "s_lshr_b32 s61, s61, 7\n\t" "v_add_f32 v129, v129, v137\n\t" "v_add_f32 v128, v128, v132\n\t" \
   "v_mfma_f32_32x32x16_bf16 v[112:127], a[152:155], v[168:171], v[112:127]\n\t" "buffer_load_dwordx4 v16, %[rs], s51 offen offset:1024 sc1 lds\n\t" \
   "s_add_u32 s55, s55, s60\n\t" "v_add_f32 v129, v129, v133\n\t" "v_mul_f32 v128, 0xbacccccd, v128\n\t" \
   "v_mfma_f32_32x32x16_bf16 v[96:111], a[148:151], v[172:175], v[96:111]\n\t" "ds_read_b128 v[188:191], v26 offset:11776\n\t" \
   "s_lshl4_add_u32 s55, s61, s55\n\t" "v_mul_f32 v129, 0x3acccccd, v129\n\t" "v_fma_f32 v129, -v128, v128, v129\n\t" "v_max_f32 v129, 0, v129\n\t" \
   "v_mfma_f32_32x32x16_bf16 v[112:127], a[156:159], v[172:175], v[112:127]\n\t" "s_lshl_b32 s55, s55, 13\n\t" "v_cvt_pk_bf16_f32 v129, v129, v128\n\t" \
   "v_and_b32 v8, 0xffff0000, v129\n\t" "v_lshlrev_b32 v129, 16, v129\n\t" "s_add_u32 s55, s55, %[yb]\n\t" "s_waitcnt lgkmcnt(2)\n\t" \
   "v_mfma_f32_32x32x16_bf16 v[96:111], a[160:163], v[176:179], v[96:111]\n\t" "ds_read_b128 v[160:163], v26 offset:12288\n\t" \
   "buffer_load_dwordx4 v16, %[rs], s51 offen offset:2048 sc1 lds\n\t" "v_add_f32 v129, 0x3727c5ac, v129\n\t" \
   "v_mfma_f32_32x32x16_bf16 v[112:127], a[168:171], v[176:179], v[112:127]\n\t" "v_cvt_pk_bf16_f32 v129, v129, v129\n\t" "s_cmp_eq_u32 s41, 63\n\t" \
   "s_cselect_b32 s58, 0, %[size]\n\t" "v_lshlrev_b32 v129, 16, v129\n\t" "v_sqrt_f32 v129, v129\n\t" \
   "v_mfma_f32_32x32x16_bf16 v[96:111], a[164:167], v[180:183], v[96:111]\n\t" "ds_read_b128 v[164:167], v26 offset:12800\n\t" "s_nop 0\n\t" \
   "v_cvt_pk_bf16_f32 v129, v129, v129\n\t" "v_lshlrev_b32 v129, 16, v129\n\t" \
   "v_mfma_f32_32x32x16_bf16 v[112:127], a[172:175], v[180:183], v[112:127]\n\t" "buffer_load_dwordx4 v16, %[rs], s51 offen offset:3072 sc1 lds\n\t" \
   "s_add_u32 m0, m0, 4096\n\t" "s_add_u32 s51, s51, 4096\n\t" "s_waitcnt lgkmcnt(2)\n\t" \
   "v_mfma_f32_32x32x16_bf16 v[96:111], a[176:179], v[184:187], v[96:111]\n\t" "ds_read_b128 v[168:171], v26 offset:13312\n\t" \
   "s_waitcnt vmcnt(10)\n\t" "v_add_u32 v138, 1, v138\n\t" "ds_write_b32 v13, v138\n\t" \
   "v_mfma_f32_32x32x16_bf16 v[112:127], a[184:187], v[184:187], v[112:127]\n\t" "v_rcp_f32 v10, v129\n\t" \
   "v_mfma_f32_32x32x16_bf16 v[96:111], a[180:183], v[188:191], v[96:111]\n\t" "ds_read_b128 v[172:175], v26 offset:13824\n\t" \
   "buffer_load_dwordx4 v16, %[rs], s51 offen offset:0 sc1 lds\n\t" "v_mfma_f32_32x32x16_bf16 v[112:127], a[188:191], v[188:191], v[112:127]\n\t" \
   "s_waitcnt lgkmcnt(3)\n\t" "v_mfma_f32_32x32x16_bf16 v[96:111], a[192:195], v[160:163], v[96:111]\n\t" \
   "ds_read_b128 v[176:179], v26 offset:14336\n\t" "v_mfma_f32_32x32x16_bf16 v[112:127], a[200:203], v[160:163], v[112:127]\n\t" \
   "buffer_store_dwordx4 v[156:159], v153, s[56:59], s55 offen sc1\n\t" "v_mfma_f32_32x32x16_bf16 v[96:111], a[196:199], v[164:167], v[96:111]\n\t" \
   "ds_read_b128 v[180:183], v26 offset:14848\n\t" "buffer_load_dwordx4 v16, %[rs], s51 offen offset:1024 sc1 lds\n\t" \
   "v_mfma_f32_32x32x16_bf16 v[112:127], a[204:207], v[164:167], v[112:127]\n\t" "s_waitcnt lgkmcnt(2)\n\t" \
   "v_mfma_f32_32x32x16_bf16 v[96:111], a[208:211], v[168:171], v[96:111]\n\t" "ds_read_b128 v[184:187], v26 offset:15360\n\t" \
   "buffer_load_dwordx4 v16, %[rs], s51 offen offset:2048 sc1 lds\n\t" "v_mfma_f32_32x32x16_bf16 v[112:127], a[216:219], v[168:171], v[112:127]\n\t" \
   "buffer_store_dwordx4 v[156:159], v154, s[56:59], s55 offen sc1\n\t" "v_mfma_f32_32x32x16_bf16 v[96:111], a[212:215], v[172:175], v[96:111]\n\t" \
   "ds_read_b128 v[188:191], v26 offset:15872\n\t" "v_mfma_f32_32x32x16_bf16 v[112:127], a[220:223], v[172:175], v[112:127]\n\t" \
   "buffer_load_dwordx4 v16, %[rs], s51 offen offset:3072 sc1 lds\n\t" "s_add_u32 m0, m0, 4096\n\t" "s_add_u32 s51, s51, 4096\n\t" \
   "s_waitcnt lgkmcnt(2)\n\t" "v_mfma_f32_32x32x16_bf16 v[96:111], a[224:227], v[176:179], v[96:111]\n\t" \
   "ds_read_b128 v[160:163], v26 offset:16384\n\t" "v_mfma_f32_32x32x16_bf16 v[112:127], a[232:235], v[176:179], v[112:127]\n\t" \
   "buffer_store_dwordx4 v[156:159], v155, s[56:59], s55 offen sc1\n\t" "v_mfma_f32_32x32x16_bf16 v[96:111], a[228:231], v[180:183], v[96:111]\n\t" \
   "ds_read_b128 v[164:167], v26 offset:16896\n\t" "buffer_load_dwordx4 v16, %[rs], s51 offen offset:0 sc1 lds\n\t" \
   "v_mfma_f32_32x32x16_bf16 v[112:127], a[236:239], v[180:183], v[112:127]\n\t" "s_waitcnt lgkmcnt(2)\n\t" \
   "v_mfma_f32_32x32x16_bf16 v[96:111], a[240:243], v[184:187], v[96:111]\n\t" "ds_read_b128 v[168:171], v26 offset:17408\n\t" \
   "s_cmp_lt_i32 s40, 1\n\t" "s_cselect_b32 s49, 0x7ffffff0, 0\n\t" "v_or_b32 v9, s49, v20\n\t" "s_sub_i32 s49, s41, 1\n\t" \
   "v_mfma_f32_32x32x16_bf16 v[112:127], a[248:251], v[184:187], v[112:127]\n\t" "s_max_i32 s49, s49, 0\n\t" "s_lshl_b32 s49, s49, 12\n\t" \
   "s_waitcnt vmcnt(14)\n\t" "buffer_store_dword v31, v9, %[rs], s49 offen sc1\n\t" \
   "v_mfma_f32_32x32x16_bf16 v[96:111], a[244:247], v[188:191], v[96:111]\n\t" "ds_read_b128 v[172:175], v26 offset:17920\n\t" \
   "v_mfma_f32_32x32x16_bf16 v[112:127], a[252:255], v[188:191], v[112:127]\n\t" "buffer_load_dwordx4 v16, %[rs], s51 offen offset:1024 sc1 lds\n\t" \
   "s_add_u32 m0, s53, %[ld1]\n\t" "s_nop 0\n\t" "s_waitcnt lgkmcnt(2)\n\t" "v_mfma_f32_32x32x16_bf16 v[96:111], v[192:195], v[160:163], v[96:111]\n\t" \
   "ds_read_b128 v[176:179], v26 offset:18432\n\t" "buffer_load_dwordx4 v17, %[rs], s52 offen sc1 lds\n\t" "ds_read_b128 v[140:143], v12\n\t" \
   "v_mfma_f32_32x32x16_bf16 v[112:127], v[200:203], v[160:163], v[112:127]\n\t" \
   "v_mfma_f32_32x32x16_bf16 v[96:111], v[196:199], v[164:167], v[96:111]\n\t" "ds_read_b128 v[180:183], v26 offset:18944\n\t" \
   "v_mfma_f32_32x32x16_bf16 v[112:127], v[204:207], v[164:167], v[112:127]\n\t" "s_waitcnt lgkmcnt(3)\n\t" \
   "v_mfma_f32_32x32x16_bf16 v[96:111], v[208:211], v[168:171], v[96:111]\n\t" "ds_read_b128 v[184:187], v26 offset:19456\n\t" \
   "s_waitcnt lgkmcnt(2)\n\t" "v_min3_u32 v140, v140, v141, v142\n\t" "v_min_u32 v140, v140, v143\n\t" "v_cmp_ge_u32 vcc, v140, v138\n\t" \
   "v_mfma_f32_32x32x16_bf16 v[112:127], v[216:219], v[168:171], v[112:127]\n\t" "s_cbranch_vccz 153f\n\t" "53:\n\t" \
   "v_mfma_f32_32x32x16_bf16 v[96:111], v[212:215], v[172:175], v[96:111]\n\t" "ds_read_b128 v[188:191], v26 offset:19968\n\t" \
   "v_mfma_f32_32x32x16_bf16 v[112:127], v[220:223], v[172:175], v[112:127]\n\t" "v_add_u32 v138, 1, v138\n\t" "ds_write_b32 v13, v138\n\t" \
   "s_waitcnt lgkmcnt(3)\n\t" "v_mfma_f32_32x32x16_bf16 v[96:111], v[224:227], v[176:179], v[96:111]\n\t" "ds_read_b128 v[160:163], v28 offset:0\n\t" \
   "s_add_u32 s45, s40, 3\n\t" "s_min_u32 s45, s45, 63\n\t" "s_mov_b32 s46, s42\n\t" \
   "v_mfma_f32_32x32x16_bf16 v[112:127], v[232:235], v[176:179], v[112:127]\n\t" "s_lshr_b32 s48, s45, 1\n\t" "s_bitcmp1_b32 s44, s48\n\t" \
   "s_cbranch_scc0 178f\n\t" "78:\n\t" "s_mov_b32 s42, s43\n\t" "s_add_u32 s43, s43, 44032\n\t" \
   "v_mfma_f32_32x32x16_bf16 v[96:111], v[228:231], v[180:183], v[96:111]\n\t" "ds_read_b128 v[164:167], v28 offset:512\n\t" \
   "s_cmp_eq_u32 s43, 132096\n\t" "s_cselect_b32 s43, 0, s43\n\t" "s_add_u32 s41, s41, 1\n\t" "s_add_u32 s40, s40, 1\n\t" \
   "v_mfma_f32_32x32x16_bf16 v[112:127], v[236:239], v[180:183], v[112:127]\n\t" "s_waitcnt lgkmcnt(3)\n\t" \
   "v_mfma_f32_32x32x16_bf16 v[96:111], v[240:243], v[184:187], v[96:111]\n\t" "ds_read_b128 v[168:171], v28 offset:1024\n\t" \
   "v_mfma_f32_32x32x16_bf16 v[112:127], v[248:251], v[184:187], v[112:127]\n\t" \
   "v_mfma_f32_32x32x16_bf16 v[96:111], v[244:247], v[188:191], v[96:111]\n\t" "ds_read_b128 v[172:175], v28 offset:1536\n\t" \
   "v_mfma_f32_32x32x16_bf16 v[112:127], v[252:255], v[188:191], v[112:127]\n\t" "s_cmp_lt_u32 s40, 64\n\t" "s_cbranch_scc1 1b\n\t" "s_nop 7\n\t" \
   "v_mul_f32 v11, 0x3e0293ee, v10\n\t" "s_lshl_b32 s47, s41, 13\n\t" "s_sub_i32 s47, s47, 8192\n\t" "v_fmac_f32 v96, v32, v8\n\t" \
   "v_fmac_f32 v97, v33, v8\n\t" "v_fmac_f32 v98, v34, v8\n\t" "v_fmac_f32 v99, v35, v8\n\t" "v_fmac_f32 v100, v36, v8\n\t" \
   "v_fmac_f32 v101, v37, v8\n\t" "v_fmac_f32 v102, v38, v8\n\t" "v_fmac_f32 v103, v39, v8\n\t" "v_fmac_f32 v104, v40, v8\n\t" \
   "v_fmac_f32 v105, v41, v8\n\t" "v_fmac_f32 v106, v42, v8\n\t" "v_fmac_f32 v107, v43, v8\n\t" "v_fmac_f32 v108, v44, v8\n\t" \
   "v_fmac_f32 v109, v45, v8\n\t" "v_fmac_f32 v110, v46, v8\n\t" "v_fmac_f32 v111, v47, v8\n\t" "v_mul_f32 v96, v96, v11\n\t" \
   "v_mul_f32 v97, v97, v11\n\t" "v_mul_f32 v98, v98, v11\n\t" "v_mul_f32 v99, v99, v11\n\t" "v_mul_f32 v100, v100, v11\n\t" \
   "v_mul_f32 v101, v101, v11\n\t" "v_mul_f32 v102, v102, v11\n\t" "v_mul_f32 v103, v103, v11\n\t" "v_mul_f32 v104, v104, v11\n\t" \
   "v_mul_f32 v105, v105, v11\n\t" "v_mul_f32 v106, v106, v11\n\t" "v_mul_f32 v107, v107, v11\n\t" "v_mul_f32 v108, v108, v11\n\t" \
   "v_mul_f32 v109, v109, v11\n\t" "v_mul_f32 v110, v110, v11\n\t" "v_mul_f32 v111, v111, v11\n\t" "s_add_u32 s48, s47, %[qf]\n\t" \
   "v_cvt_pk_bf16_f32 v96, v96, v97\n\t" "v_cvt_pk_bf16_f32 v97, v98, v99\n\t" "v_cvt_pk_bf16_f32 v98, v104, v105\n\t" \
   "v_cvt_pk_bf16_f32 v99, v106, v107\n\t" "buffer_store_dwordx4 v[96:99], v21, %[rs], s48 offen offset:0 sc1\n\t" \
   "v_cvt_pk_bf16_f32 v100, v100, v101\n\t" "v_cvt_pk_bf16_f32 v101, v102, v103\n\t" "v_cvt_pk_bf16_f32 v102, v108, v109\n\t" \
   "v_cvt_pk_bf16_f32 v103, v110, v111\n\t" "buffer_store_dwordx4 v[100:103], v21, %[rs], s48 offen offset:512 sc1\n\t" "v_fmac_f32 v112, v48, v8\n\t" \
   "v_fmac_f32 v113, v49, v8\n\t" "v_fmac_f32 v114, v50, v8\n\t" "v_fmac_f32 v115, v51, v8\n\t" "v_fmac_f32 v116, v52, v8\n\t" \
   "v_fmac_f32 v117, v53, v8\n\t" "v_fmac_f32 v118, v54, v8\n\t" "v_fmac_f32 v119, v55, v8\n\t" "v_fmac_f32 v120, v56, v8\n\t" \
   "v_fmac_f32 v121, v57, v8\n\t" "v_fmac_f32 v122, v58, v8\n\t" "v_fmac_f32 v123, v59, v8\n\t" "v_fmac_f32 v124, v60, v8\n\t" \
   "v_fmac_f32 v125, v61, v8\n\t" "v_fmac_f32 v126, v62, v8\n\t" "v_fmac_f32 v127, v63, v8\n\t" "v_mul_f32 v112, v112, v10\n\t" \
   "v_mul_f32 v113, v113, v10\n\t" "v_mul_f32 v114, v114, v10\n\t" "v_mul_f32 v115, v115, v10\n\t" "v_mul_f32 v116, v116, v10\n\t" \
   "v_mul_f32 v117, v117, v10\n\t" "v_mul_f32 v118, v118, v10\n\t" "v_mul_f32 v119, v119, v10\n\t" "v_mul_f32 v120, v120, v10\n\t" \
   "v_mul_f32 v121, v121, v10\n\t" "v_mul_f32 v122, v122, v10\n\t" "v_mul_f32 v123, v123, v10\n\t" "v_mul_f32 v124, v124, v10\n\t" \
   "v_mul_f32 v125, v125, v10\n\t" "v_mul_f32 v126, v126, v10\n\t" "v_mul_f32 v127, v127, v10\n\t" "s_add_u32 s48, s47, %[kf]\n\t" \
   "v_cvt_pk_bf16_f32 v112, v112, v113\n\t" "v_cvt_pk_bf16_f32 v113, v114, v115\n\t" "v_cvt_pk_bf16_f32 v114, v120, v121\n\t" \
   "v_cvt_pk_bf16_f32 v115, v122, v123\n\t" "buffer_store_dwordx4 v[112:115], v21, %[rs], s48 offen offset:0 sc1\n\t" \
   "v_cvt_pk_bf16_f32 v116, v116, v117\n\t" "v_cvt_pk_bf16_f32 v117, v118, v119\n\t" "v_cvt_pk_bf16_f32 v118, v124, v125\n\t" \
   "v_cvt_pk_bf16_f32 v119, v126, v127\n\t" "buffer_store_dwordx4 v[116:119], v21, %[rs], s48 offen offset:512 sc1\n\t" \
   "s_waitcnt vmcnt(0) lgkmcnt(0)\n\t" "s_cmp_lt_i32 s40, 1\n\t" "s_cselect_b32 s47, 0x7ffffff0, 0\n\t" "v_or_b32 v30, s47, v20\n\t" \
   "s_sub_i32 s47, s41, 1\n\t" "s_max_i32 s47, s47, 0\n\t" "s_lshl_b32 s47, s47, 12\n\t" "buffer_store_dword v31, v30, %[rs], s47 offen sc1\n\t" \
   "s_branch 9f\n\t" "172:\n\t" "s_mov_b32 s47, 0\n\t" "buffer_load_dword v30, v19, %[rs], 0 offen sc1\n\t" "s_waitcnt vmcnt(0)\n\t" \
   "v_add_u32 v30, 1, v30\n\t" "v_cmp_lt_u32 vcc, 7, v30\n\t" "s_mov_b32 s44, vcc_lo\n\t" "s_bitcmp1_b32 s44, s48\n\t" "s_cbranch_scc1 72b\n\t" \
   "s_bitset1_b32 s44, s48\n\t" "s_lshl_b32 s48, s48, 7\n\t" "s_add_u32 s48, s48, %[cnt]\n\t" "73:\n\t" "s_sleep 10\n\t" \
   "buffer_load_dword v30, off, %[rs], s48 sc1\n\t" "s_add_u32 s47, s47, 1\n\t" "s_waitcnt vmcnt(0)\n\t" "v_add_u32 v30, 1, v30\n\t" \
   "v_cmp_lt_u32 vcc, 7, v30\n\t" "s_cmp_gt_u32 s47, 0x20000\n\t" "s_cbranch_scc1 72b\n\t" "s_cbranch_vccz 73b\n\t" "s_branch 72b\n\t" "174:\n\t" \
   "s_mov_b32 s47, 0\n\t" "buffer_load_dword v30, v19, %[rs], 0 offen sc1\n\t" "s_waitcnt vmcnt(0)\n\t" "v_add_u32 v30, 1, v30\n\t" \
   "v_cmp_lt_u32 vcc, 7, v30\n\t" "s_mov_b32 s44, vcc_lo\n\t" "s_bitcmp1_b32 s44, s48\n\t" "s_cbranch_scc1 74b\n\t" "s_bitset1_b32 s44, s48\n\t" \
   "s_lshl_b32 s48, s48, 7\n\t" "s_add_u32 s48, s48, %[cnt]\n\t" "75:\n\t" "s_sleep 10\n\t" "buffer_load_dword v30, off, %[rs], s48 sc1\n\t" \
   "s_add_u32 s47, s47, 1\n\t" "s_waitcnt vmcnt(0)\n\t" "v_add_u32 v30, 1, v30\n\t" "v_cmp_lt_u32 vcc, 7, v30\n\t" "s_cmp_gt_u32 s47, 0x20000\n\t" \
   "s_cbranch_scc1 74b\n\t" "s_cbranch_vccz 75b\n\t" "s_branch 74b\n\t" "178:\n\t" "s_mov_b32 s47, 0\n\t" \
   "buffer_load_dword v30, v19, %[rs], 0 offen sc1\n\t" "s_waitcnt vmcnt(0)\n\t" "v_add_u32 v30, 1, v30\n\t" "v_cmp_lt_u32 vcc, 7, v30\n\t" \
   "s_mov_b32 s44, vcc_lo\n\t" "s_bitcmp1_b32 s44, s48\n\t" "s_cbranch_scc1 78b\n\t" "s_bitset1_b32 s44, s48\n\t" "s_lshl_b32 s48, s48, 7\n\t" \
   "s_add_u32 s48, s48, %[cnt]\n\t" "79:\n\t" "s_sleep 10\n\t" "buffer_load_dword v30, off, %[rs], s48 sc1\n\t" "s_add_u32 s47, s47, 1\n\t" \
   "s_waitcnt vmcnt(0)\n\t" "v_add_u32 v30, 1, v30\n\t" "v_cmp_lt_u32 vcc, 7, v30\n\t" "s_cmp_gt_u32 s47, 0x20000\n\t" "s_cbranch_scc1 78b\n\t" \
   "s_cbranch_vccz 79b\n\t" "s_branch 78b\n\t" "150:\n\t" "ds_read_b128 v[140:143], v12\n\t" "s_waitcnt lgkmcnt(0)\n\t" \
   "v_min3_u32 v140, v140, v141, v142\n\t" "v_min_u32 v140, v140, v143\n\t" "v_cmp_ge_u32 vcc, v140, v138\n\t" "s_cbranch_vccz 150b\n\t" \
   "s_branch 50b\n\t" "152:\n\t" "ds_read_b128 v[140:143], v12\n\t" "s_waitcnt lgkmcnt(0)\n\t" "v_min3_u32 v140, v140, v141, v142\n\t" \
   "v_min_u32 v140, v140, v143\n\t" "v_cmp_ge_u32 vcc, v140, v138\n\t" "s_cbranch_vccz 152b\n\t" "s_branch 52b\n\t" "151:\n\t" \
   "ds_read_b128 v[140:143], v12\n\t" "s_waitcnt lgkmcnt(0)\n\t" "v_min3_u32 v140, v140, v141, v142\n\t" "v_min_u32 v140, v140, v143\n\t" \
   "v_cmp_ge_u32 vcc, v140, v138\n\t" "s_cbranch_vccz 151b\n\t" "s_branch 51b\n\t" "153:\n\t" "ds_read_b128 v[140:143], v12\n\t" \
   "s_waitcnt lgkmcnt(0)\n\t" "v_min3_u32 v140, v140, v141, v142\n\t" "v_min_u32 v140, v140, v143\n\t" "v_cmp_ge_u32 vcc, v140, v138\n\t" \
   "s_cbranch_vccz 153b\n\t" "s_branch 53b\n\t" "9:\n\t" "s_waitcnt vmcnt(0)"
#define KQ_CLOB "v8", "v9", "v10", "v11", "v12", "v13", "v14", "v15", "v32", "v33", "v34", "v35", "v36", "v37", "v38", "v39", "v40", "v41", "v42", "v43", "v44", "v45", "v46", "v47", "v48", "v49", "v50", "v51", "v52", "v53", "v54", "v55", "v56", "v57", "v58", "v59", "v60", "v61", "v62", "v63", "v64", "v65", "v66", "v67", "v68", "v69", "v70", "v71", "v72", "v73", "v74", "v75", "v76", "v77", "v78", "v79", "v80", "v81", "v82", "v83", "v84", "v85", "v86", "v87", "v88", "v89", "v90", "v91", "v92", "v93", "v94", "v95", "v96", "v97", "v98", "v99", "v100", "v101", "v102", "v103", "v104", "v105", "v106", "v107", "v108", "v109", "v110", "v111", "v112", "v113", "v114", "v115", "v116", "v117", "v118", "v119", "v120", "v121", "v122", "v123", "v124", "v125", "v126", "v127", "v128", "v129", "v130", "v131", "v132", "v133", "v134", "v135", "v136", "v137", "v138", "v139", "v140", "v141", "v142", "v143", "v144", "v145", "v146", "v147", "v148", "v149", "v150", "v151", "v160", "v161", "v162", "v163", "v164", "v165", "v166", "v167", "v168", "v169", "v170", "v171", "v172", "v173", "v174", "v175", "v176", "v177", "v178", "v179", "v180", "v181", "v182", "v183", "v184", "v185", "v186", "v187", "v188", "v189", "v190", "v191", "s40", "s41", "s42", "s43", "s44", "s45", "s46", "s47", "s48", "s49", "s50", "s51", "s52", "s53", "s54", "s55", "s60", "s61"
#define VU_ASM \
   "s_mov_b32 s44, %[rdy]\n\t" "s_mov_b32 s45, 0\n\t" "s_mov_b32 s46, 0\n\t" "s_cmp_eq_u32 s44, -1\n\t" "s_cbranch_scc1 68f\n\t" \
   "s_mul_i32 s52, s45, 1536\n\t" "s_mul_i32 s51, s45, 40960\n\t" "s_add_u32 m0, s46, %[ldx]\n\t" "s_mov_b32 s53, s46\n\t" \
   "buffer_load_dwordx4 v16, %[rs], s51 offen offset:0 sc1 lds\n\t" "buffer_load_dwordx4 v16, %[rs], s51 offen offset:1024 sc1 lds\n\t" \
   "buffer_load_dwordx4 v16, %[rs], s51 offen offset:2048 sc1 lds\n\t" "buffer_load_dwordx4 v16, %[rs], s51 offen offset:3072 sc1 lds\n\t" \
   "s_add_u32 m0, m0, 4096\n\t" "s_add_u32 s51, s51, 4096\n\t" "buffer_load_dwordx4 v16, %[rs], s51 offen offset:0 sc1 lds\n\t" \
   "buffer_load_dwordx4 v16, %[rs], s51 offen offset:1024 sc1 lds\n\t" "buffer_load_dwordx4 v16, %[rs], s51 offen offset:2048 sc1 lds\n\t" \
   "buffer_load_dwordx4 v16, %[rs], s51 offen offset:3072 sc1 lds\n\t" "s_add_u32 m0, m0, 4096\n\t" "s_add_u32 s51, s51, 4096\n\t" \
   "buffer_load_dwordx4 v16, %[rs], s51 offen offset:0 sc1 lds\n\t" "buffer_load_dwordx4 v16, %[rs], s51 offen offset:1024 sc1 lds\n\t" \
   "s_add_u32 m0, s53, %[ld1]\n\t" "s_nop 0\n\t" "buffer_load_dwordx4 v17, %[rs], s52 offen sc1 lds\n\t" "s_mov_b32 s45, 1\n\t" \
   "s_mov_b32 s46, 44032\n\t" "s_mul_i32 s52, s45, 1536\n\t" "s_mul_i32 s51, s45, 40960\n\t" "s_add_u32 m0, s46, %[ldx]\n\t" "s_mov_b32 s53, s46\n\t" \
   "buffer_load_dwordx4 v16, %[rs], s51 offen offset:0 sc1 lds\n\t" "buffer_load_dwordx4 v16, %[rs], s51 offen offset:1024 sc1 lds\n\t" \
   "buffer_load_dwordx4 v16, %[rs], s51 offen offset:2048 sc1 lds\n\t" "buffer_load_dwordx4 v16, %[rs], s51 offen offset:3072 sc1 lds\n\t" \
   "s_add_u32 m0, m0, 4096\n\t" "s_add_u32 s51, s51, 4096\n\t" "buffer_load_dwordx4 v16, %[rs], s51 offen offset:0 sc1 lds\n\t" \
   "buffer_load_dwordx4 v16, %[rs], s51 offen offset:1024 sc1 lds\n\t" "buffer_load_dwordx4 v16, %[rs], s51 offen offset:2048 sc1 lds\n\t" \
   "buffer_load_dwordx4 v16, %[rs], s51 offen offset:3072 sc1 lds\n\t" "s_add_u32 m0, m0, 4096\n\t" "s_add_u32 s51, s51, 4096\n\t" \
   "buffer_load_dwordx4 v16, %[rs], s51 offen offset:0 sc1 lds\n\t" "buffer_load_dwordx4 v16, %[rs], s51 offen offset:1024 sc1 lds\n\t" \
   "s_add_u32 m0, s53, %[ld1]\n\t" "s_nop 0\n\t" "buffer_load_dwordx4 v17, %[rs], s52 offen sc1 lds\n\t" "68:\n\t" "v_mov_b32 v160, 0x3f803f80\n\t" \
   "v_mov_b32 v161, 0x3f803f80\n\t" "v_mov_b32 v162, 0x3f803f80\n\t" "v_mov_b32 v163, 0x3f803f80\n\t" "v_mov_b32 v31, 1\n\t" "v_mov_b32 v138, 0\n\t" \
   "v_mov_b32 v13, %[flw]\n\t" "v_and_b32 v12, -16, v13\n\t" "ds_write_b32 v13, v138\n\t" \
   "v_mfma_f32_32x32x16_bf16 v[32:47], a[0:3], v[160:163], 0\n\t" "v_mfma_f32_32x32x16_bf16 v[32:47], a[4:7], v[160:163], v[32:47]\n\t" \
   "v_mfma_f32_32x32x16_bf16 v[32:47], a[8:11], v[160:163], v[32:47]\n\t" "v_mfma_f32_32x32x16_bf16 v[32:47], a[12:15], v[160:163], v[32:47]\n\t" \
   "v_mfma_f32_32x32x16_bf16 v[32:47], a[16:19], v[160:163], v[32:47]\n\t" "v_mfma_f32_32x32x16_bf16 v[32:47], a[20:23], v[160:163], v[32:47]\n\t" \
   "v_mfma_f32_32x32x16_bf16 v[32:47], a[24:27], v[160:163], v[32:47]\n\t" "v_mfma_f32_32x32x16_bf16 v[32:47], a[28:31], v[160:163], v[32:47]\n\t" \
   "v_mfma_f32_32x32x16_bf16 v[32:47], a[32:35], v[160:163], v[32:47]\n\t" "v_mfma_f32_32x32x16_bf16 v[32:47], a[36:39], v[160:163], v[32:47]\n\t" \
   "v_mfma_f32_32x32x16_bf16 v[32:47], a[40:43], v[160:163], v[32:47]\n\t" "v_mfma_f32_32x32x16_bf16 v[32:47], a[44:47], v[160:163], v[32:47]\n\t" \
   "v_mfma_f32_32x32x16_bf16 v[32:47], a[48:51], v[160:163], v[32:47]\n\t" "v_mfma_f32_32x32x16_bf16 v[32:47], a[52:55], v[160:163], v[32:47]\n\t" \
   "v_mfma_f32_32x32x16_bf16 v[32:47], a[56:59], v[160:163], v[32:47]\n\t" "v_mfma_f32_32x32x16_bf16 v[32:47], a[60:63], v[160:163], v[32:47]\n\t" \
   "v_mfma_f32_32x32x16_bf16 v[32:47], a[64:67], v[160:163], v[32:47]\n\t" "v_mfma_f32_32x32x16_bf16 v[32:47], a[68:71], v[160:163], v[32:47]\n\t" \
   "v_mfma_f32_32x32x16_bf16 v[32:47], a[72:75], v[160:163], v[32:47]\n\t" "v_mfma_f32_32x32x16_bf16 v[32:47], a[76:79], v[160:163], v[32:47]\n\t" \
   "v_mfma_f32_32x32x16_bf16 v[32:47], a[80:83], v[160:163], v[32:47]\n\t" "v_mfma_f32_32x32x16_bf16 v[32:47], a[84:87], v[160:163], v[32:47]\n\t" \
   "v_mfma_f32_32x32x16_bf16 v[32:47], a[88:91], v[160:163], v[32:47]\n\t" "v_mfma_f32_32x32x16_bf16 v[32:47], a[92:95], v[160:163], v[32:47]\n\t" \
   "v_mfma_f32_32x32x16_bf16 v[32:47], a[96:99], v[160:163], v[32:47]\n\t" "v_mfma_f32_32x32x16_bf16 v[32:47], a[100:103], v[160:163], v[32:47]\n\t" \
   "v_mfma_f32_32x32x16_bf16 v[32:47], a[104:107], v[160:163], v[32:47]\n\t" "v_mfma_f32_32x32x16_bf16 v[32:47], a[108:111], v[160:163], v[32:47]\n\t" \
   "v_mfma_f32_32x32x16_bf16 v[32:47], a[112:115], v[160:163], v[32:47]\n\t" "v_mfma_f32_32x32x16_bf16 v[32:47], a[116:119], v[160:163], v[32:47]\n\t" \
   "v_mfma_f32_32x32x16_bf16 v[32:47], a[120:123], v[160:163], v[32:47]\n\t" "v_mfma_f32_32x32x16_bf16 v[32:47], a[124:127], v[160:163], v[32:47]\n\t" \
   "v_mfma_f32_32x32x16_bf16 v[32:47], a[128:131], v[160:163], v[32:47]\n\t" "v_mfma_f32_32x32x16_bf16 v[32:47], a[132:135], v[160:163], v[32:47]\n\t" \
   "v_mfma_f32_32x32x16_bf16 v[32:47], a[136:139], v[160:163], v[32:47]\n\t" "v_mfma_f32_32x32x16_bf16 v[32:47], a[140:143], v[160:163], v[32:47]\n\t" \
   "v_mfma_f32_32x32x16_bf16 v[32:47], a[144:147], v[160:163], v[32:47]\n\t" "v_mfma_f32_32x32x16_bf16 v[32:47], a[148:151], v[160:163], v[32:47]\n\t" \
   "v_mfma_f32_32x32x16_bf16 v[32:47], a[152:155], v[160:163], v[32:47]\n\t" "v_mfma_f32_32x32x16_bf16 v[32:47], a[156:159], v[160:163], v[32:47]\n\t" \
   "s_mov_b32 s40, 0\n\t" "s_mov_b32 s41, 0\n\t" "s_mov_b32 s42, 0\n\t" "s_mov_b32 s43, 44032\n\t" "v_mov_b32 v28, v22\n\t" "v_mov_b32 v29, v23\n\t" \
   "s_mov_b32 s45, 2\n\t" "s_mov_b32 s46, 88064\n\t" "s_waitcnt vmcnt(11) lgkmcnt(0)\n\t" "s_barrier\n\t" "s_lshr_b32 s48, s45, 1\n\t" \
   "s_bitcmp1_b32 s44, s48\n\t" "s_cbranch_scc0 172f\n\t" "72:\n\t" "ds_read_b128 v[160:163], v28 offset:0\n\t" \
   "ds_read_b128 v[164:167], v28 offset:512\n\t" "ds_read_b128 v[168:171], v28 offset:1024\n\t" "ds_read_b128 v[172:175], v28 offset:1536\n\t" \
   "ds_read_b128 v[176:179], v28 offset:2048\n\t" "ds_read_b128 v[180:183], v28 offset:2560\n\t" "ds_read_b128 v[184:187], v28 offset:3072\n\t" \
   "v_mov_b32 v14, v24\n\t" "v_mov_b32 v15, v25\n\t" "v_mov_b32 v18, 0x7ffffff0\n\t" "s_mov_b32 s49, 0\n\t" "v_mov_b32 v26, v28\n\t" \
   "v_mov_b32 v27, v29\n\t" "s_waitcnt lgkmcnt(5)\n\t" "v_mfma_f32_32x32x16_bf16 v[48:63], a[0:3], v[160:163], 0\n\t" \
   "ds_read_b128 v[188:191], v26 offset:3584\n\t" "s_mul_i32 s52, s45, 1536\n\t" "s_mul_i32 s51, s45, 40960\n\t" "s_add_u32 m0, s46, %[ldx]\n\t" \
   "s_mov_b32 s53, s46\n\t" "v_mfma_f32_32x32x16_bf16 v[48:63], a[4:7], v[164:167], v[48:63]\n\t" "ds_read_b128 v[160:163], v26 offset:4096\n\t" \
   "ds_read_b128 v[128:131], v27 offset:0\n\t" "ds_read_b128 v[132:135], v27 offset:16\n\t" "s_waitcnt lgkmcnt(7)\n\t" \
   "v_mfma_f32_32x32x16_bf16 v[48:63], a[8:11], v[168:171], v[48:63]\n\t" "ds_read_b128 v[164:167], v26 offset:4608\n\t" \
   "ds_read_b64 v[136:137], v27 offset:32\n\t" "v_add_u32 v28, s43, v22\n\t" "v_add_u32 v29, s43, v23\n\t" \
   "v_mfma_f32_32x32x16_bf16 v[48:63], a[12:15], v[172:175], v[48:63]\n\t" "ds_read_b128 v[168:171], v26 offset:5120\n\t" \
   "buffer_load_dwordx4 v16, %[rs], s51 offen offset:0 sc1 lds\n\t" "s_waitcnt lgkmcnt(8)\n\t" \
   "v_mfma_f32_32x32x16_bf16 v[48:63], a[16:19], v[176:179], v[48:63]\n\t" "ds_read_b128 v[172:175], v26 offset:5632\n\t" \
   "v_mfma_f32_32x32x16_bf16 v[48:63], a[20:23], v[180:183], v[48:63]\n\t" "ds_read_b128 v[176:179], v26 offset:6144\n\t" "s_waitcnt lgkmcnt(3)\n\t" \
   "v_add_f32 v128, v128, v130\n\t" "v_add_f32 v129, v129, v131\n\t" "v_add_f32 v132, v132, v134\n\t" \
   "v_mfma_f32_32x32x16_bf16 v[48:63], a[24:27], v[184:187], v[48:63]\n\t" "ds_read_b128 v[180:183], v26 offset:6656\n\t" \
   "buffer_load_dwordx4 v16, %[rs], s51 offen offset:1024 sc1 lds\n\t" "v_add_f32 v133, v133, v135\n\t" \
   "v_mfma_f32_32x32x16_bf16 v[48:63], a[28:31], v[188:191], v[48:63]\n\t" "ds_read_b128 v[184:187], v26 offset:7168\n\t" \
   "v_add_f32 v128, v128, v136\n\t" "v_add_f32 v129, v129, v137\n\t" "v_add_f32 v128, v128, v132\n\t" \
   "v_mfma_f32_32x32x16_bf16 v[48:63], a[32:35], v[160:163], v[48:63]\n\t" "ds_read_b128 v[188:191], v26 offset:7680\n\t" \
   "v_add_f32 v129, v129, v133\n\t" "v_mul_f32 v128, 0xbacccccd, v128\n\t" "v_mul_f32 v129, 0x3acccccd, v129\n\t" \
   "v_fma_f32 v129, -v128, v128, v129\n\t" "v_mfma_f32_32x32x16_bf16 v[48:63], a[36:39], v[164:167], v[48:63]\n\t" \
   "ds_read_b128 v[160:163], v26 offset:8192\n\t" "buffer_load_dwordx4 v16, %[rs], s51 offen offset:2048 sc1 lds\n\t" "s_waitcnt lgkmcnt(5)\n\t" \
   "v_mfma_f32_32x32x16_bf16 v[48:63], a[40:43], v[168:171], v[48:63]\n\t" "ds_read_b128 v[164:167], v26 offset:8704\n\t" "v_max_f32 v129, 0, v129\n\t" \
   "v_cvt_pk_bf16_f32 v129, v129, v128\n\t" "v_and_b32 v8, 0xffff0000, v129\n\t" "v_lshlrev_b32 v129, 16, v129\n\t" \
   "v_mfma_f32_32x32x16_bf16 v[48:63], a[44:47], v[172:175], v[48:63]\n\t" "ds_read_b128 v[168:171], v26 offset:9216\n\t" \
   "v_add_f32 v129, 0x3727c5ac, v129\n\t" "v_cvt_pk_bf16_f32 v129, v129, v129\n\t" "v_lshlrev_b32 v129, 16, v129\n\t" "s_waitcnt lgkmcnt(5)\n\t" \
   "v_mfma_f32_32x32x16_bf16 v[48:63], a[48:51], v[176:179], v[48:63]\n\t" "ds_read_b128 v[172:175], v26 offset:9728\n\t" "v_sqrt_f32 v129, v129\n\t" \
   "s_nop 0\n\t" "v_cvt_pk_bf16_f32 v129, v129, v129\n\t" "v_mfma_f32_32x32x16_bf16 v[48:63], a[52:55], v[180:183], v[48:63]\n\t" \
   "ds_read_b128 v[176:179], v26 offset:10240\n\t" "v_lshlrev_b32 v129, 16, v129\n\t" "v_rcp_f32 v10, v129\n\t" "s_waitcnt lgkmcnt(5)\n\t" \
   "v_mfma_f32_32x32x16_bf16 v[48:63], a[56:59], v[184:187], v[48:63]\n\t" "ds_read_b128 v[180:183], v26 offset:10752\n\t" \
   "buffer_load_dwordx4 v16, %[rs], s51 offen offset:3072 sc1 lds\n\t" "s_add_u32 m0, m0, 4096\n\t" "s_add_u32 s51, s51, 4096\n\t" \
   "v_mfma_f32_32x32x16_bf16 v[48:63], a[60:63], v[188:191], v[48:63]\n\t" "ds_read_b128 v[184:187], v26 offset:11264\n\t" \
   "buffer_load_dwordx4 v16, %[rs], s51 offen offset:0 sc1 lds\n\t" "s_waitcnt lgkmcnt(5)\n\t" \
   "v_mfma_f32_32x32x16_bf16 v[48:63], a[64:67], v[160:163], v[48:63]\n\t" "ds_read_b128 v[188:191], v26 offset:11776\n\t" \
   "v_mfma_f32_32x32x16_bf16 v[48:63], a[68:71], v[164:167], v[48:63]\n\t" "ds_read_b128 v[160:163], v26 offset:12288\n\t" "s_waitcnt lgkmcnt(5)\n\t" \
   "v_mfma_f32_32x32x16_bf16 v[48:63], a[72:75], v[168:171], v[48:63]\n\t" "ds_read_b128 v[164:167], v26 offset:12800\n\t" \
   "buffer_load_dwordx4 v16, %[rs], s51 offen offset:1024 sc1 lds\n\t" "v_mfma_f32_32x32x16_bf16 v[48:63], a[76:79], v[172:175], v[48:63]\n\t" \
   "ds_read_b128 v[168:171], v26 offset:13312\n\t" "s_waitcnt lgkmcnt(5)\n\t" "v_mfma_f32_32x32x16_bf16 v[48:63], a[80:83], v[176:179], v[48:63]\n\t" \
   "ds_read_b128 v[172:175], v26 offset:13824\n\t" "v_mfma_f32_32x32x16_bf16 v[48:63], a[84:87], v[180:183], v[48:63]\n\t" \
   "ds_read_b128 v[176:179], v26 offset:14336\n\t" "buffer_load_dwordx4 v16, %[rs], s51 offen offset:2048 sc1 lds\n\t" "s_waitcnt lgkmcnt(5)\n\t" \
   "v_mfma_f32_32x32x16_bf16 v[48:63], a[88:91], v[184:187], v[48:63]\n\t" "ds_read_b128 v[180:183], v26 offset:14848\n\t" \
   "v_mfma_f32_32x32x16_bf16 v[48:63], a[92:95], v[188:191], v[48:63]\n\t" "ds_read_b128 v[184:187], v26 offset:15360\n\t" "s_waitcnt lgkmcnt(5)\n\t" \
   "v_mfma_f32_32x32x16_bf16 v[48:63], a[96:99], v[160:163], v[48:63]\n\t" "ds_read_b128 v[188:191], v26 offset:15872\n\t" \
   "v_mfma_f32_32x32x16_bf16 v[48:63], a[100:103], v[164:167], v[48:63]\n\t" "ds_read_b128 v[160:163], v26 offset:16384\n\t" "s_waitcnt lgkmcnt(5)\n\t" \
   "v_mfma_f32_32x32x16_bf16 v[48:63], a[104:107], v[168:171], v[48:63]\n\t" "ds_read_b128 v[164:167], v26 offset:16896\n\t" \
   "buffer_load_dwordx4 v16, %[rs], s51 offen offset:3072 sc1 lds\n\t" "s_add_u32 m0, m0, 4096\n\t" "s_add_u32 s51, s51, 4096\n\t" \
   "v_mfma_f32_32x32x16_bf16 v[48:63], a[108:111], v[172:175], v[48:63]\n\t" "ds_read_b128 v[168:171], v26 offset:17408\n\t" \
   "buffer_load_dwordx4 v16, %[rs], s51 offen offset:0 sc1 lds\n\t" "s_waitcnt lgkmcnt(5)\n\t" \
   "v_mfma_f32_32x32x16_bf16 v[48:63], a[112:115], v[176:179], v[48:63]\n\t" "ds_read_b128 v[172:175], v26 offset:17920\n\t" \
   "v_mfma_f32_32x32x16_bf16 v[48:63], a[116:119], v[180:183], v[48:63]\n\t" "ds_read_b128 v[176:179], v26 offset:18432\n\t" "s_waitcnt lgkmcnt(5)\n\t" \
   "v_mfma_f32_32x32x16_bf16 v[48:63], a[120:123], v[184:187], v[48:63]\n\t" "ds_read_b128 v[180:183], v26 offset:18944\n\t" \
   "v_mfma_f32_32x32x16_bf16 v[48:63], a[124:127], v[188:191], v[48:63]\n\t" "ds_read_b128 v[184:187], v26 offset:19456\n\t" "s_waitcnt lgkmcnt(5)\n\t" \
   "v_mfma_f32_32x32x16_bf16 v[48:63], a[128:131], v[160:163], v[48:63]\n\t" "ds_read_b128 v[188:191], v26 offset:19968\n\t" \
   "buffer_load_dwordx4 v16, %[rs], s51 offen offset:1024 sc1 lds\n\t" "s_add_u32 m0, s53, %[ld1]\n\t" "s_nop 0\n\t" \
   "v_mfma_f32_32x32x16_bf16 v[48:63], a[132:135], v[164:167], v[48:63]\n\t" "buffer_load_dwordx4 v17, %[rs], s52 offen sc1 lds\n\t" \
   "s_waitcnt lgkmcnt(4)\n\t" "v_mfma_f32_32x32x16_bf16 v[48:63], a[136:139], v[168:171], v[48:63]\n\t" \
   "v_mfma_f32_32x32x16_bf16 v[48:63], a[140:143], v[172:175], v[48:63]\n\t" "s_waitcnt lgkmcnt(2)\n\t" \
   "v_mfma_f32_32x32x16_bf16 v[48:63], a[144:147], v[176:179], v[48:63]\n\t" "v_mfma_f32_32x32x16_bf16 v[48:63], a[148:151], v[180:183], v[48:63]\n\t" \
   "s_waitcnt lgkmcnt(0)\n\t" "v_mfma_f32_32x32x16_bf16 v[48:63], a[152:155], v[184:187], v[48:63]\n\t" \
   "v_mfma_f32_32x32x16_bf16 v[48:63], a[156:159], v[188:191], v[48:63]\n\t" "s_memrealtime s[54:55]\n\t" "s_nop 7\n\t" "s_nop 2\n\t" \
   "v_fmac_f32 v48, v32, v8\n\t" "v_fmac_f32 v49, v33, v8\n\t" "v_fmac_f32 v50, v34, v8\n\t" "v_fmac_f32 v51, v35, v8\n\t" \
   "v_fmac_f32 v52, v36, v8\n\t" "v_fmac_f32 v53, v37, v8\n\t" "v_fmac_f32 v54, v38, v8\n\t" "v_fmac_f32 v55, v39, v8\n\t" \
   "v_fmac_f32 v56, v40, v8\n\t" "v_fmac_f32 v57, v41, v8\n\t" "v_fmac_f32 v58, v42, v8\n\t" "v_fmac_f32 v59, v43, v8\n\t" \
   "v_fmac_f32 v60, v44, v8\n\t" "v_fmac_f32 v61, v45, v8\n\t" "v_fmac_f32 v62, v46, v8\n\t" "v_fmac_f32 v63, v47, v8\n\t" \
   "v_mul_f32 v48, v48, v10\n\t" "v_mul_f32 v49, v49, v10\n\t" "v_mul_f32 v50, v50, v10\n\t" "v_mul_f32 v51, v51, v10\n\t" \
   "v_mul_f32 v52, v52, v10\n\t" "v_mul_f32 v53, v53, v10\n\t" "v_mul_f32 v54, v54, v10\n\t" "v_mul_f32 v55, v55, v10\n\t" \
   "v_mul_f32 v56, v56, v10\n\t" "v_mul_f32 v57, v57, v10\n\t" "v_mul_f32 v58, v58, v10\n\t" "v_mul_f32 v59, v59, v10\n\t" \
   "v_mul_f32 v60, v60, v10\n\t" "v_mul_f32 v61, v61, v10\n\t" "v_mul_f32 v62, v62, v10\n\t" "v_mul_f32 v63, v63, v10\n\t" \
   "v_cvt_pk_bf16_f32 v48, v48, v49\n\t" "v_cvt_pk_bf16_f32 v49, v50, v51\n\t" "v_cvt_pk_bf16_f32 v50, v52, v53\n\t" \
   "v_cvt_pk_bf16_f32 v51, v54, v55\n\t" "ds_write_b128 v14, v[48:51] offset:0\n\t" "v_cvt_pk_bf16_f32 v56, v56, v57\n\t" \
   "v_cvt_pk_bf16_f32 v57, v58, v59\n\t" "v_cvt_pk_bf16_f32 v58, v60, v61\n\t" "v_cvt_pk_bf16_f32 v59, v62, v63\n\t" \
   "ds_write_b128 v14, v[56:59] offset:1024\n\t" "s_waitcnt vmcnt(11) lgkmcnt(0)\n\t" "s_barrier\n\t" "ds_read_b128 v[160:163], v28 offset:0\n\t" \
   "ds_read_b128 v[164:167], v28 offset:512\n\t" "ds_read_b128 v[168:171], v28 offset:1024\n\t" "ds_read_b128 v[172:175], v28 offset:1536\n\t" \
   "ds_read_b128 v[176:179], v28 offset:2048\n\t" "ds_read_b128 v[180:183], v28 offset:2560\n\t" "ds_read_b128 v[184:187], v28 offset:3072\n\t" \
   "s_add_u32 s45, s40, 3\n\t" "s_min_u32 s45, s45, 63\n\t" "s_mov_b32 s46, s42\n\t" "s_lshr_b32 s48, s45, 1\n\t" "s_bitcmp1_b32 s44, s48\n\t" \
   "s_cbranch_scc0 176f\n\t" "76:\n\t" "s_mov_b32 s42, s43\n\t" "s_add_u32 s43, s43, 44032\n\t" "s_cmp_eq_u32 s43, 132096\n\t" \
   "s_cselect_b32 s43, 0, s43\n\t" "s_add_u32 s41, s41, 1\n\t" "s_add_u32 s40, s40, 1\n\t" "v_xor_b32 v14, 0x2000, v14\n\t" \
   "v_xor_b32 v15, 0x2000, v15\n\t" "v_mov_b32 v31, s54\n\t" "v_min_u32 v31, -2, v31\n\t" "1:\n\t" "v_mov_b32 v26, v28\n\t" "v_mov_b32 v27, v29\n\t" \
   "s_waitcnt lgkmcnt(5)\n\t" "v_mfma_f32_32x32x16_bf16 v[48:63], a[0:3], v[160:163], 0\n\t" "ds_read_b128 v[188:191], v26 offset:3584\n\t" \
   "ds_read_b128 v[156:159], v12\n\t" "v_mfma_f32_32x32x16_bf16 v[48:63], a[4:7], v[164:167], v[48:63]\n\t" \
   "ds_read_b128 v[160:163], v26 offset:4096\n\t" "s_add_u32 s50, s49, 262144\n\t" "v_cvt_pk_bf16_f32 v140, v140, v141\n\t" \
   "v_cvt_pk_bf16_f32 v141, v148, v149\n\t" "s_waitcnt lgkmcnt(6)\n\t" "v_mfma_f32_32x32x16_bf16 v[48:63], a[8:11], v[168:171], v[48:63]\n\t" \
   "ds_read_b128 v[164:167], v26 offset:4608\n\t" "s_waitcnt lgkmcnt(2)\n\t" "v_min3_u32 v156, v156, v157, v158\n\t" "v_min_u32 v156, v156, v159\n\t" \
   "v_cmp_ge_u32 vcc, v156, v138\n\t" "v_mfma_f32_32x32x16_bf16 v[48:63], a[12:15], v[172:175], v[48:63]\n\t" \
   "ds_read_b128 v[168:171], v26 offset:5120\n\t" "s_cbranch_vccz 150f\n\t" "50:\n\t" "ds_read_b128 v[96:99], v15 offset:0\n\t" \
   "v_mfma_f32_32x32x16_bf16 v[48:63], a[16:19], v[176:179], v[48:63]\n\t" "ds_read_b128 v[172:175], v26 offset:5632\n\t" \
   "ds_read_b128 v[100:103], v15 offset:1024\n\t" "ds_read_b128 v[104:107], v15 offset:2048\n\t" "ds_read_b128 v[108:111], v15 offset:3072\n\t" \
   "v_mfma_f32_32x32x16_bf16 v[48:63], a[20:23], v[180:183], v[48:63]\n\t" "ds_read_b128 v[176:179], v26 offset:6144\n\t" \
   "ds_read_b128 v[112:115], v15 offset:4096\n\t" "ds_read_b128 v[116:119], v15 offset:5120\n\t" \
   "v_mfma_f32_32x32x16_bf16 v[48:63], a[24:27], v[184:187], v[48:63]\n\t" "ds_read_b128 v[180:183], v26 offset:6656\n\t" \
   "ds_read_b128 v[120:123], v15 offset:6144\n\t" "ds_read_b128 v[124:127], v15 offset:7168\n\t" "s_mul_i32 s52, s45, 1536\n\t" \
   "s_mul_i32 s51, s45, 40960\n\t" "s_add_u32 m0, s46, %[ldx]\n\t" "s_mov_b32 s53, s46\n\t" \
   "v_mfma_f32_32x32x16_bf16 v[48:63], a[28:31], v[188:191], v[48:63]\n\t" "ds_read_b128 v[184:187], v26 offset:7168\n\t" \
   "v_cvt_pk_bf16_f32 v142, v142, v143\n\t" "v_cvt_pk_bf16_f32 v143, v150, v151\n\t" "s_waitcnt lgkmcnt(9)\n\t" \
   "v_mfma_f32_32x32x16_bf16 v[64:79], v[96:99], a[160:163], 0\n\t" "buffer_store_dwordx4 v[140:143], v18, %[rs], s50 offen offset:0 sc1\n\t" \
   "v_mfma_f32_32x32x16_bf16 v[48:63], a[32:35], v[160:163], v[48:63]\n\t" "ds_read_b128 v[188:191], v26 offset:7680\n\t" \
   "v_cvt_pk_bf16_f32 v144, v144, v145\n\t" "v_cvt_pk_bf16_f32 v145, v152, v153\n\t" "v_cvt_pk_bf16_f32 v146, v146, v147\n\t" \
   "v_cvt_pk_bf16_f32 v147, v154, v155\n\t" "v_mfma_f32_32x32x16_bf16 v[48:63], a[36:39], v[164:167], v[48:63]\n\t" \
   "ds_read_b128 v[160:163], v26 offset:8192\n\t" "v_add_u32 v28, s43, v22\n\t" "v_add_u32 v29, s43, v23\n\t" \
   "v_mfma_f32_32x32x16_bf16 v[64:79], v[100:103], a[176:179], v[64:79]\n\t" \
   "buffer_store_dwordx4 v[144:147], v18, %[rs], s50 offen offset:512 sc1\n\t" "s_sub_i32 s49, s41, 1\n\t" "s_mul_i32 s49, s49, 1310720\n\t" \
   "v_mfma_f32_32x32x16_bf16 v[48:63], a[40:43], v[168:171], v[48:63]\n\t" "ds_read_b128 v[164:167], v26 offset:8704\n\t" \
   "ds_read_b128 v[128:131], v27 offset:0\n\t" "buffer_load_dwordx4 v16, %[rs], s51 offen offset:0 sc1 lds\n\t" \
   "v_mfma_f32_32x32x16_bf16 v[48:63], a[44:47], v[172:175], v[48:63]\n\t" "ds_read_b128 v[168:171], v26 offset:9216\n\t" \
   "ds_read_b128 v[132:135], v27 offset:16\n\t" "ds_read_b64 v[136:137], v27 offset:32\n\t" "s_waitcnt lgkmcnt(14)\n\t" \
   "v_mfma_f32_32x32x16_bf16 v[64:79], v[104:107], a[164:167], v[64:79]\n\t" "s_waitcnt lgkmcnt(10)\n\t" \
   "v_mfma_f32_32x32x16_bf16 v[48:63], a[48:51], v[176:179], v[48:63]\n\t" "ds_read_b128 v[172:175], v26 offset:9728\n\t" \
   "v_mfma_f32_32x32x16_bf16 v[48:63], a[52:55], v[180:183], v[48:63]\n\t" "ds_read_b128 v[176:179], v26 offset:10240\n\t" \
   "buffer_load_dwordx4 v16, %[rs], s51 offen offset:1024 sc1 lds\n\t" "v_mfma_f32_32x32x16_bf16 v[64:79], v[108:111], a[180:183], v[64:79]\n\t" \
   "s_waitcnt lgkmcnt(2)\n\t" "v_add_f32 v128, v128, v130\n\t" "v_add_f32 v129, v129, v131\n\t" "v_add_f32 v132, v132, v134\n\t" \
   "v_add_f32 v133, v133, v135\n\t" "v_mfma_f32_32x32x16_bf16 v[48:63], a[56:59], v[184:187], v[48:63]\n\t" \
   "ds_read_b128 v[180:183], v26 offset:10752\n\t" "v_add_f32 v128, v128, v136\n\t" "v_add_f32 v129, v129, v137\n\t" "v_add_f32 v128, v128, v132\n\t" \
   "v_add_f32 v129, v129, v133\n\t" "v_mfma_f32_32x32x16_bf16 v[48:63], a[60:63], v[188:191], v[48:63]\n\t" \
   "ds_read_b128 v[184:187], v26 offset:11264\n\t" "v_mul_f32 v128, 0xbacccccd, v128\n\t" "v_mul_f32 v129, 0x3acccccd, v129\n\t" \
   "v_fma_f32 v129, -v128, v128, v129\n\t" "v_max_f32 v129, 0, v129\n\t" "v_mfma_f32_32x32x16_bf16 v[64:79], v[112:115], a[168:171], v[64:79]\n\t" \
   "buffer_load_dwordx4 v16, %[rs], s51 offen offset:2048 sc1 lds\n\t" "v_cvt_pk_bf16_f32 v129, v129, v128\n\t" "v_and_b32 v8, 0xffff0000, v129\n\t" \
   "v_mfma_f32_32x32x16_bf16 v[48:63], a[64:67], v[160:163], v[48:63]\n\t" "ds_read_b128 v[188:191], v26 offset:11776\n\t" \
   "v_lshlrev_b32 v129, 16, v129\n\t" "v_add_f32 v129, 0x3727c5ac, v129\n\t" "v_cvt_pk_bf16_f32 v129, v129, v129\n\t" \
   "v_lshlrev_b32 v129, 16, v129\n\t" "v_mfma_f32_32x32x16_bf16 v[48:63], a[68:71], v[164:167], v[48:63]\n\t" \
   "ds_read_b128 v[160:163], v26 offset:12288\n\t" "v_sqrt_f32 v129, v129\n\t" "s_nop 0\n\t" "v_cvt_pk_bf16_f32 v129, v129, v129\n\t" \
   "v_mfma_f32_32x32x16_bf16 v[64:79], v[116:119], a[184:187], v[64:79]\n\t" "v_lshlrev_b32 v129, 16, v129\n\t" "v_rcp_f32 v10, v129\n\t" \
   "s_waitcnt lgkmcnt(5)\n\t" "v_mfma_f32_32x32x16_bf16 v[48:63], a[72:75], v[168:171], v[48:63]\n\t" "ds_read_b128 v[164:167], v26 offset:12800\n\t" \
   "v_mfma_f32_32x32x16_bf16 v[48:63], a[76:79], v[172:175], v[48:63]\n\t" "ds_read_b128 v[168:171], v26 offset:13312\n\t" \
   "v_mfma_f32_32x32x16_bf16 v[64:79], v[120:123], a[172:175], v[64:79]\n\t" "buffer_load_dwordx4 v16, %[rs], s51 offen offset:3072 sc1 lds\n\t" \
   "s_add_u32 m0, m0, 4096\n\t" "s_add_u32 s51, s51, 4096\n\t" "s_waitcnt lgkmcnt(5)\n\t" \
   "v_mfma_f32_32x32x16_bf16 v[48:63], a[80:83], v[176:179], v[48:63]\n\t" "ds_read_b128 v[172:175], v26 offset:13824\n\t" \
   "v_mfma_f32_32x32x16_bf16 v[48:63], a[84:87], v[180:183], v[48:63]\n\t" "ds_read_b128 v[176:179], v26 offset:14336\n\t" \
   "buffer_load_dwordx4 v16, %[rs], s51 offen offset:0 sc1 lds\n\t" "v_mfma_f32_32x32x16_bf16 v[64:79], v[124:127], a[188:191], v[64:79]\n\t" \
   "s_waitcnt lgkmcnt(5)\n\t" "v_mfma_f32_32x32x16_bf16 v[48:63], a[88:91], v[184:187], v[48:63]\n\t" "ds_read_b128 v[180:183], v26 offset:14848\n\t" \
   "v_mfma_f32_32x32x16_bf16 v[48:63], a[92:95], v[188:191], v[48:63]\n\t" "ds_read_b128 v[184:187], v26 offset:15360\n\t" "s_add_u32 s50, s49, 0\n\t" \
   "v_cvt_pk_bf16_f32 v64, v64, v65\n\t" "v_cvt_pk_bf16_f32 v65, v72, v73\n\t" "v_cvt_pk_bf16_f32 v66, v66, v67\n\t" \
   "v_mfma_f32_32x32x16_bf16 v[80:95], v[96:99], a[192:195], 0\n\t" "v_cvt_pk_bf16_f32 v67, v74, v75\n\t" \
   "buffer_load_dwordx4 v16, %[rs], s51 offen offset:1024 sc1 lds\n\t" "s_waitcnt lgkmcnt(5)\n\t" \
   "v_mfma_f32_32x32x16_bf16 v[48:63], a[96:99], v[160:163], v[48:63]\n\t" "ds_read_b128 v[188:191], v26 offset:15872\n\t" \
   "v_mfma_f32_32x32x16_bf16 v[48:63], a[100:103], v[164:167], v[48:63]\n\t" "ds_read_b128 v[160:163], v26 offset:16384\n\t" \
   "v_mfma_f32_32x32x16_bf16 v[80:95], v[100:103], a[208:211], v[80:95]\n\t" "buffer_store_dwordx4 v[64:67], v21, %[rs], s50 offen offset:0 sc1\n\t" \
   "s_waitcnt lgkmcnt(5)\n\t" "v_mfma_f32_32x32x16_bf16 v[48:63], a[104:107], v[168:171], v[48:63]\n\t" "ds_read_b128 v[164:167], v26 offset:16896\n\t" \
   "v_cvt_pk_bf16_f32 v68, v68, v69\n\t" "v_cvt_pk_bf16_f32 v69, v76, v77\n\t" "v_cvt_pk_bf16_f32 v70, v70, v71\n\t" \
   "v_cvt_pk_bf16_f32 v71, v78, v79\n\t" "v_mfma_f32_32x32x16_bf16 v[48:63], a[108:111], v[172:175], v[48:63]\n\t" \
   "ds_read_b128 v[168:171], v26 offset:17408\n\t" "s_waitcnt vmcnt(9)\n\t" "v_add_u32 v138, 1, v138\n\t" "ds_write_b32 v13, v138\n\t" \
   "v_mfma_f32_32x32x16_bf16 v[80:95], v[104:107], a[196:199], v[80:95]\n\t" "buffer_store_dwordx4 v[68:71], v21, %[rs], s50 offen offset:512 sc1\n\t" \
   "s_waitcnt lgkmcnt(6)\n\t" "v_mfma_f32_32x32x16_bf16 v[48:63], a[112:115], v[176:179], v[48:63]\n\t" "ds_read_b128 v[172:175], v26 offset:17920\n\t" \
   "buffer_load_dwordx4 v16, %[rs], s51 offen offset:2048 sc1 lds\n\t" "v_mfma_f32_32x32x16_bf16 v[48:63], a[116:119], v[180:183], v[48:63]\n\t" \
   "ds_read_b128 v[176:179], v26 offset:18432\n\t" "v_mfma_f32_32x32x16_bf16 v[80:95], v[108:111], a[212:215], v[80:95]\n\t" \
   "buffer_load_dwordx4 v16, %[rs], s51 offen offset:3072 sc1 lds\n\t" "s_add_u32 m0, m0, 4096\n\t" "s_add_u32 s51, s51, 4096\n\t" \
   "s_waitcnt lgkmcnt(6)\n\t" "v_mfma_f32_32x32x16_bf16 v[48:63], a[120:123], v[184:187], v[48:63]\n\t" "ds_read_b128 v[180:183], v26 offset:18944\n\t" \
   "v_mfma_f32_32x32x16_bf16 v[48:63], a[124:127], v[188:191], v[48:63]\n\t" "ds_read_b128 v[184:187], v26 offset:19456\n\t" \
   "v_mfma_f32_32x32x16_bf16 v[80:95], v[112:115], a[200:203], v[80:95]\n\t" "buffer_load_dwordx4 v16, %[rs], s51 offen offset:0 sc1 lds\n\t" \
   "s_waitcnt lgkmcnt(6)\n\t" "v_mfma_f32_32x32x16_bf16 v[48:63], a[128:131], v[160:163], v[48:63]\n\t" "ds_read_b128 v[188:191], v26 offset:19968\n\t" \
   "v_mfma_f32_32x32x16_bf16 v[48:63], a[132:135], v[164:167], v[48:63]\n\t" "v_mfma_f32_32x32x16_bf16 v[80:95], v[116:119], a[216:219], v[80:95]\n\t" \
   "s_waitcnt lgkmcnt(4)\n\t" "v_mfma_f32_32x32x16_bf16 v[48:63], a[136:139], v[168:171], v[48:63]\n\t" "s_cmp_lt_i32 s40, 2\n\t" \
   "s_cselect_b32 s47, 0x7ffffff0, 0\n\t" "v_or_b32 v30, s47, v20\n\t" "s_sub_i32 s47, s41, 2\n\t" "s_max_i32 s47, s47, 0\n\t" \
   "s_lshl_b32 s47, s47, 12\n\t" "v_mfma_f32_32x32x16_bf16 v[48:63], a[140:143], v[172:175], v[48:63]\n\t" "s_waitcnt vmcnt(11)\n\t" \
   "buffer_load_dwordx4 v16, %[rs], s51 offen offset:1024 sc1 lds\n\t" "s_add_u32 m0, s53, %[ld1]\n\t" "s_nop 0\n\t" \
   "v_mfma_f32_32x32x16_bf16 v[80:95], v[120:123], a[204:207], v[80:95]\n\t" "buffer_store_dword v31, v30, %[rs], s47 offen sc1\n\t" \
   "s_waitcnt lgkmcnt(2)\n\t" "v_mfma_f32_32x32x16_bf16 v[48:63], a[144:147], v[176:179], v[48:63]\n\t" "ds_read_b128 v[156:159], v12\n\t" \
   "v_mfma_f32_32x32x16_bf16 v[48:63], a[148:151], v[180:183], v[48:63]\n\t" "buffer_load_dwordx4 v17, %[rs], s52 offen sc1 lds\n\t" \
   "v_mfma_f32_32x32x16_bf16 v[80:95], v[124:127], a[220:223], v[80:95]\n\t" "s_waitcnt lgkmcnt(1)\n\t" \
   "v_mfma_f32_32x32x16_bf16 v[48:63], a[152:155], v[184:187], v[48:63]\n\t" "s_waitcnt lgkmcnt(0)\n\t" "v_min3_u32 v156, v156, v157, v158\n\t" \
   "v_min_u32 v156, v156, v159\n\t" "v_cmp_ge_u32 vcc, v156, v138\n\t" "s_cbranch_vccz 152f\n\t" "52:\n\t" \
   "v_mfma_f32_32x32x16_bf16 v[48:63], a[156:159], v[188:191], v[48:63]\n\t" "s_add_u32 s50, s49, 65536\n\t" "v_cvt_pk_bf16_f32 v80, v80, v81\n\t" \
   "v_cvt_pk_bf16_f32 v81, v88, v89\n\t" "v_cvt_pk_bf16_f32 v82, v82, v83\n\t" "v_cvt_pk_bf16_f32 v83, v90, v91\n\t" \
   "v_mfma_f32_32x32x16_bf16 v[64:79], v[96:99], a[224:227], 0\n\t" "buffer_store_dwordx4 v[80:83], v21, %[rs], s50 offen offset:0 sc1\n\t" \
   "v_cvt_pk_bf16_f32 v84, v84, v85\n\t" "v_mfma_f32_32x32x16_bf16 v[64:79], v[100:103], a[240:243], v[64:79]\n\t" "v_fmac_f32 v48, v32, v8\n\t" \
   "v_fmac_f32 v49, v33, v8\n\t" "v_fmac_f32 v50, v34, v8\n\t" "v_fmac_f32 v51, v35, v8\n\t" "v_fmac_f32 v52, v36, v8\n\t" \
   "v_fmac_f32 v53, v37, v8\n\t" "v_mfma_f32_32x32x16_bf16 v[64:79], v[104:107], a[228:231], v[64:79]\n\t" "v_fmac_f32 v54, v38, v8\n\t" \
   "v_fmac_f32 v55, v39, v8\n\t" "v_fmac_f32 v56, v40, v8\n\t" "v_fmac_f32 v57, v41, v8\n\t" "v_fmac_f32 v58, v42, v8\n\t" \
   "v_fmac_f32 v59, v43, v8\n\t" "v_mfma_f32_32x32x16_bf16 v[64:79], v[108:111], a[244:247], v[64:79]\n\t" "v_fmac_f32 v60, v44, v8\n\t" \
   "v_fmac_f32 v61, v45, v8\n\t" "v_fmac_f32 v62, v46, v8\n\t" "v_fmac_f32 v63, v47, v8\n\t" "v_mul_f32 v48, v48, v10\n\t" \
   "v_mul_f32 v49, v49, v10\n\t" "v_mfma_f32_32x32x16_bf16 v[64:79], v[112:115], a[232:235], v[64:79]\n\t" "v_mul_f32 v50, v50, v10\n\t" \
   "v_mul_f32 v51, v51, v10\n\t" "v_mul_f32 v52, v52, v10\n\t" "v_mul_f32 v53, v53, v10\n\t" "v_mul_f32 v54, v54, v10\n\t" \
   "v_mul_f32 v55, v55, v10\n\t" "v_mfma_f32_32x32x16_bf16 v[64:79], v[116:119], a[248:251], v[64:79]\n\t" "v_mul_f32 v56, v56, v10\n\t" \
   "v_mul_f32 v57, v57, v10\n\t" "v_mul_f32 v58, v58, v10\n\t" "v_mul_f32 v59, v59, v10\n\t" "v_mul_f32 v60, v60, v10\n\t" \
   "v_mul_f32 v61, v61, v10\n\t" "v_mfma_f32_32x32x16_bf16 v[64:79], v[120:123], a[236:239], v[64:79]\n\t" "v_cvt_pk_bf16_f32 v85, v92, v93\n\t" \
   "v_cvt_pk_bf16_f32 v86, v86, v87\n\t" "v_cvt_pk_bf16_f32 v87, v94, v95\n\t" \
   "buffer_store_dwordx4 v[84:87], v21, %[rs], s50 offen offset:512 sc1\n\t" "v_mfma_f32_32x32x16_bf16 v[64:79], v[124:127], a[252:255], v[64:79]\n\t" \
   "v_mul_f32 v62, v62, v10\n\t" "v_mul_f32 v63, v63, v10\n\t" "v_cvt_pk_bf16_f32 v48, v48, v49\n\t" "v_cvt_pk_bf16_f32 v49, v50, v51\n\t" \
   "v_cvt_pk_bf16_f32 v50, v52, v53\n\t" "v_cvt_pk_bf16_f32 v51, v54, v55\n\t" "v_mfma_f32_32x32x16_bf16 v[80:95], v[96:99], v[192:195], 0\n\t" \
   "ds_write_b128 v14, v[48:51] offset:0\n\t" "v_cvt_pk_bf16_f32 v56, v56, v57\n\t" "v_cvt_pk_bf16_f32 v57, v58, v59\n\t" \
   "v_mfma_f32_32x32x16_bf16 v[80:95], v[100:103], v[208:211], v[80:95]\n\t" "v_cvt_pk_bf16_f32 v58, v60, v61\n\t" \
   "v_cvt_pk_bf16_f32 v59, v62, v63\n\t" "ds_write_b128 v14, v[56:59] offset:1024\n\t" "v_add_u32 v138, 1, v138\n\t" "ds_write_b32 v13, v138\n\t" \
   "v_mfma_f32_32x32x16_bf16 v[80:95], v[104:107], v[196:199], v[80:95]\n\t" "s_add_u32 s50, s49, 131072\n\t" "v_cvt_pk_bf16_f32 v64, v64, v65\n\t" \
   "v_cvt_pk_bf16_f32 v65, v72, v73\n\t" "v_cvt_pk_bf16_f32 v66, v66, v67\n\t" "v_cvt_pk_bf16_f32 v67, v74, v75\n\t" "s_add_u32 s45, s40, 3\n\t" \
   "v_mfma_f32_32x32x16_bf16 v[80:95], v[108:111], v[212:215], v[80:95]\n\t" "buffer_store_dwordx4 v[64:67], v21, %[rs], s50 offen offset:0 sc1\n\t" \
   "v_cvt_pk_bf16_f32 v68, v68, v69\n\t" "v_mfma_f32_32x32x16_bf16 v[80:95], v[112:115], v[200:203], v[80:95]\n\t" \
   "v_cvt_pk_bf16_f32 v69, v76, v77\n\t" "v_cvt_pk_bf16_f32 v70, v70, v71\n\t" "v_cvt_pk_bf16_f32 v71, v78, v79\n\t" "s_min_u32 s45, s45, 63\n\t" \
   "s_mov_b32 s46, s42\n\t" "v_mfma_f32_32x32x16_bf16 v[80:95], v[116:119], v[216:219], v[80:95]\n\t" \
   "buffer_store_dwordx4 v[68:71], v21, %[rs], s50 offen offset:512 sc1\n\t" "v_mfma_f32_32x32x16_bf16 v[80:95], v[120:123], v[204:207], v[80:95]\n\t" \
   "s_lshr_b32 s48, s45, 1\n\t" "s_bitcmp1_b32 s44, s48\n\t" "s_cbranch_scc0 174f\n\t" "74:\n\t" "ds_read_b128 v[160:163], v28 offset:0\n\t" \
   "ds_read_b128 v[164:167], v28 offset:512\n\t" "v_mfma_f32_32x32x16_bf16 v[80:95], v[124:127], v[220:223], v[80:95]\n\t" "s_mov_b32 s42, s43\n\t" \
   "ds_read_b128 v[168:171], v28 offset:1024\n\t" "s_add_u32 s43, s43, 44032\n\t" "s_cmp_eq_u32 s43, 132096\n\t" "s_cselect_b32 s43, 0, s43\n\t" \
   "v_mfma_f32_32x32x16_bf16 v[140:155], v[96:99], v[224:227], 0\n\t" "ds_read_b128 v[172:175], v28 offset:1536\n\t" \
   "ds_read_b128 v[176:179], v28 offset:2048\n\t" "s_add_u32 s41, s41, 1\n\t" "s_add_u32 s40, s40, 1\n\t" "v_xor_b32 v14, 0x2000, v14\n\t" \
   "v_mfma_f32_32x32x16_bf16 v[140:155], v[100:103], v[240:243], v[140:155]\n\t" "s_add_u32 s50, s49, 196608\n\t" "v_cvt_pk_bf16_f32 v80, v80, v81\n\t" \
   "v_cvt_pk_bf16_f32 v81, v88, v89\n\t" "v_cvt_pk_bf16_f32 v82, v82, v83\n\t" "v_cvt_pk_bf16_f32 v83, v90, v91\n\t" "v_xor_b32 v15, 0x2000, v15\n\t" \
   "v_mfma_f32_32x32x16_bf16 v[140:155], v[104:107], v[228:231], v[140:155]\n\t" \
   "buffer_store_dwordx4 v[80:83], v21, %[rs], s50 offen offset:0 sc1\n\t" "v_cvt_pk_bf16_f32 v84, v84, v85\n\t" \
   "v_mfma_f32_32x32x16_bf16 v[140:155], v[108:111], v[244:247], v[140:155]\n\t" "v_cvt_pk_bf16_f32 v85, v92, v93\n\t" \
   "v_cvt_pk_bf16_f32 v86, v86, v87\n\t" "v_cvt_pk_bf16_f32 v87, v94, v95\n\t" "ds_read_b128 v[180:183], v28 offset:2560\n\t" \
   "ds_read_b128 v[184:187], v28 offset:3072\n\t" "v_mfma_f32_32x32x16_bf16 v[140:155], v[112:115], v[232:235], v[140:155]\n\t" \
   "buffer_store_dwordx4 v[84:87], v21, %[rs], s50 offen offset:512 sc1\n\t" "v_mov_b32 v18, v21\n\t" \
   "v_mfma_f32_32x32x16_bf16 v[140:155], v[116:119], v[248:251], v[140:155]\n\t" \
   "v_mfma_f32_32x32x16_bf16 v[140:155], v[120:123], v[236:239], v[140:155]\n\t" \
   "v_mfma_f32_32x32x16_bf16 v[140:155], v[124:127], v[252:255], v[140:155]\n\t" "s_cmp_lt_u32 s40, 64\n\t" "s_cbranch_scc1 1b\n\t" "s_nop 7\n\t" \
   "s_nop 2\n\t" "s_waitcnt lgkmcnt(0)\n\t" "s_barrier\n\t" "s_add_u32 s50, s49, 262144\n\t" "v_cvt_pk_bf16_f32 v140, v140, v141\n\t" \
   "v_cvt_pk_bf16_f32 v141, v148, v149\n\t" "v_cvt_pk_bf16_f32 v142, v142, v143\n\t" "v_cvt_pk_bf16_f32 v143, v150, v151\n\t" \
   "buffer_store_dwordx4 v[140:143], v21, %[rs], s50 offen offset:0 sc1\n\t" "v_cvt_pk_bf16_f32 v144, v144, v145\n\t" \
   "v_cvt_pk_bf16_f32 v145, v152, v153\n\t" "v_cvt_pk_bf16_f32 v146, v146, v147\n\t" "v_cvt_pk_bf16_f32 v147, v154, v155\n\t" \
   "buffer_store_dwordx4 v[144:147], v21, %[rs], s50 offen offset:512 sc1\n\t" "s_sub_i32 s49, s41, 1\n\t" "s_mul_i32 s49, s49, 1310720\n\t" \
   "ds_read_b128 v[96:99], v15 offset:0\n\t" "ds_read_b128 v[100:103], v15 offset:1024\n\t" "ds_read_b128 v[104:107], v15 offset:2048\n\t" \
   "ds_read_b128 v[108:111], v15 offset:3072\n\t" "ds_read_b128 v[112:115], v15 offset:4096\n\t" "ds_read_b128 v[116:119], v15 offset:5120\n\t" \
   "ds_read_b128 v[120:123], v15 offset:6144\n\t" "ds_read_b128 v[124:127], v15 offset:7168\n\t" "s_waitcnt lgkmcnt(6)\n\t" \
   "v_mfma_f32_32x32x16_bf16 v[64:79], v[96:99], a[160:163], 0\n\t" "v_mfma_f32_32x32x16_bf16 v[64:79], v[100:103], a[176:179], v[64:79]\n\t" \
   "s_waitcnt lgkmcnt(4)\n\t" "v_mfma_f32_32x32x16_bf16 v[64:79], v[104:107], a[164:167], v[64:79]\n\t" \
   "v_mfma_f32_32x32x16_bf16 v[64:79], v[108:111], a[180:183], v[64:79]\n\t" "s_waitcnt lgkmcnt(2)\n\t" \
   "v_mfma_f32_32x32x16_bf16 v[64:79], v[112:115], a[168:171], v[64:79]\n\t" "v_mfma_f32_32x32x16_bf16 v[64:79], v[116:119], a[184:187], v[64:79]\n\t" \
   "s_waitcnt lgkmcnt(0)\n\t" "v_mfma_f32_32x32x16_bf16 v[64:79], v[120:123], a[172:175], v[64:79]\n\t" \
   "v_mfma_f32_32x32x16_bf16 v[64:79], v[124:127], a[188:191], v[64:79]\n\t" "v_mfma_f32_32x32x16_bf16 v[80:95], v[96:99], a[192:195], 0\n\t" \
   "v_mfma_f32_32x32x16_bf16 v[80:95], v[100:103], a[208:211], v[80:95]\n\t" "s_add_u32 s50, s49, 0\n\t" "v_cvt_pk_bf16_f32 v64, v64, v65\n\t" \
   "v_cvt_pk_bf16_f32 v65, v72, v73\n\t" "v_cvt_pk_bf16_f32 v66, v66, v67\n\t" "v_cvt_pk_bf16_f32 v67, v74, v75\n\t" \
   "v_mfma_f32_32x32x16_bf16 v[80:95], v[104:107], a[196:199], v[80:95]\n\t" "buffer_store_dwordx4 v[64:67], v21, %[rs], s50 offen offset:0 sc1\n\t" \
   "v_cvt_pk_bf16_f32 v68, v68, v69\n\t" "v_mfma_f32_32x32x16_bf16 v[80:95], v[108:111], a[212:215], v[80:95]\n\t" \
   "v_cvt_pk_bf16_f32 v69, v76, v77\n\t" "v_cvt_pk_bf16_f32 v70, v70, v71\n\t" "v_cvt_pk_bf16_f32 v71, v78, v79\n\t" \
   "v_mfma_f32_32x32x16_bf16 v[80:95], v[112:115], a[200:203], v[80:95]\n\t" "buffer_store_dwordx4 v[68:71], v21, %[rs], s50 offen offset:512 sc1\n\t" \
   "v_mfma_f32_32x32x16_bf16 v[80:95], v[116:119], a[216:219], v[80:95]\n\t" "v_mfma_f32_32x32x16_bf16 v[80:95], v[120:123], a[204:207], v[80:95]\n\t" \
   "v_mfma_f32_32x32x16_bf16 v[80:95], v[124:127], a[220:223], v[80:95]\n\t" "v_mfma_f32_32x32x16_bf16 v[64:79], v[96:99], a[224:227], 0\n\t" \
   "v_mfma_f32_32x32x16_bf16 v[64:79], v[100:103], a[240:243], v[64:79]\n\t" "s_add_u32 s50, s49, 65536\n\t" "v_cvt_pk_bf16_f32 v80, v80, v81\n\t" \
   "v_cvt_pk_bf16_f32 v81, v88, v89\n\t" "v_cvt_pk_bf16_f32 v82, v82, v83\n\t" "v_cvt_pk_bf16_f32 v83, v90, v91\n\t" \
   "v_mfma_f32_32x32x16_bf16 v[64:79], v[104:107], a[228:231], v[64:79]\n\t" "buffer_store_dwordx4 v[80:83], v21, %[rs], s50 offen offset:0 sc1\n\t" \
   "v_cvt_pk_bf16_f32 v84, v84, v85\n\t" "v_mfma_f32_32x32x16_bf16 v[64:79], v[108:111], a[244:247], v[64:79]\n\t" \
   "v_cvt_pk_bf16_f32 v85, v92, v93\n\t" "v_cvt_pk_bf16_f32 v86, v86, v87\n\t" "v_cvt_pk_bf16_f32 v87, v94, v95\n\t" \
   "v_mfma_f32_32x32x16_bf16 v[64:79], v[112:115], a[232:235], v[64:79]\n\t" "buffer_store_dwordx4 v[84:87], v21, %[rs], s50 offen offset:512 sc1\n\t" \
   "v_mfma_f32_32x32x16_bf16 v[64:79], v[116:119], a[248:251], v[64:79]\n\t" "v_mfma_f32_32x32x16_bf16 v[64:79], v[120:123], a[236:239], v[64:79]\n\t" \
   "v_mfma_f32_32x32x16_bf16 v[64:79], v[124:127], a[252:255], v[64:79]\n\t" "v_mfma_f32_32x32x16_bf16 v[80:95], v[96:99], v[192:195], 0\n\t" \
   "v_mfma_f32_32x32x16_bf16 v[80:95], v[100:103], v[208:211], v[80:95]\n\t" "s_add_u32 s50, s49, 131072\n\t" "v_cvt_pk_bf16_f32 v64, v64, v65\n\t" \
   "v_cvt_pk_bf16_f32 v65, v72, v73\n\t" "v_cvt_pk_bf16_f32 v66, v66, v67\n\t" "v_cvt_pk_bf16_f32 v67, v74, v75\n\t" \
   "v_mfma_f32_32x32x16_bf16 v[80:95], v[104:107], v[196:199], v[80:95]\n\t" "buffer_store_dwordx4 v[64:67], v21, %[rs], s50 offen offset:0 sc1\n\t" \
   "v_cvt_pk_bf16_f32 v68, v68, v69\n\t" "v_mfma_f32_32x32x16_bf16 v[80:95], v[108:111], v[212:215], v[80:95]\n\t" \
   "v_cvt_pk_bf16_f32 v69, v76, v77\n\t" "v_cvt_pk_bf16_f32 v70, v70, v71\n\t" "v_cvt_pk_bf16_f32 v71, v78, v79\n\t" \
   "v_mfma_f32_32x32x16_bf16 v[80:95], v[112:115], v[200:203], v[80:95]\n\t" "buffer_store_dwordx4 v[68:71], v21, %[rs], s50 offen offset:512 sc1\n\t" \
   "v_mfma_f32_32x32x16_bf16 v[80:95], v[116:119], v[216:219], v[80:95]\n\t" "v_mfma_f32_32x32x16_bf16 v[80:95], v[120:123], v[204:207], v[80:95]\n\t" \
   "v_mfma_f32_32x32x16_bf16 v[80:95], v[124:127], v[220:223], v[80:95]\n\t" "v_mfma_f32_32x32x16_bf16 v[140:155], v[96:99], v[224:227], 0\n\t" \
   "v_mfma_f32_32x32x16_bf16 v[140:155], v[100:103], v[240:243], v[140:155]\n\t" "s_add_u32 s50, s49, 196608\n\t" "v_cvt_pk_bf16_f32 v80, v80, v81\n\t" \
   "v_cvt_pk_bf16_f32 v81, v88, v89\n\t" "v_cvt_pk_bf16_f32 v82, v82, v83\n\t" "v_cvt_pk_bf16_f32 v83, v90, v91\n\t" \
   "v_mfma_f32_32x32x16_bf16 v[140:155], v[104:107], v[228:231], v[140:155]\n\t" \
   "buffer_store_dwordx4 v[80:83], v21, %[rs], s50 offen offset:0 sc1\n\t" "v_cvt_pk_bf16_f32 v84, v84, v85\n\t" \
   "v_mfma_f32_32x32x16_bf16 v[140:155], v[108:111], v[244:247], v[140:155]\n\t" "v_cvt_pk_bf16_f32 v85, v92, v93\n\t" \
   "v_cvt_pk_bf16_f32 v86, v86, v87\n\t" "v_cvt_pk_bf16_f32 v87, v94, v95\n\t" \
   "v_mfma_f32_32x32x16_bf16 v[140:155], v[112:115], v[232:235], v[140:155]\n\t" \
   "buffer_store_dwordx4 v[84:87], v21, %[rs], s50 offen offset:512 sc1\n\t" \
   "v_mfma_f32_32x32x16_bf16 v[140:155], v[116:119], v[248:251], v[140:155]\n\t" \
   "v_mfma_f32_32x32x16_bf16 v[140:155], v[120:123], v[236:239], v[140:155]\n\t" \
   "v_mfma_f32_32x32x16_bf16 v[140:155], v[124:127], v[252:255], v[140:155]\n\t" "s_nop 7\n\t" "s_nop 2\n\t" "s_add_u32 s50, s49, 262144\n\t" \
   "v_cvt_pk_bf16_f32 v140, v140, v141\n\t" "v_cvt_pk_bf16_f32 v141, v148, v149\n\t" "v_cvt_pk_bf16_f32 v142, v142, v143\n\t" \
   "v_cvt_pk_bf16_f32 v143, v150, v151\n\t" "buffer_store_dwordx4 v[140:143], v21, %[rs], s50 offen offset:0 sc1\n\t" \
   "v_cvt_pk_bf16_f32 v144, v144, v145\n\t" "v_cvt_pk_bf16_f32 v145, v152, v153\n\t" "v_cvt_pk_bf16_f32 v146, v146, v147\n\t" \
   "v_cvt_pk_bf16_f32 v147, v154, v155\n\t" "buffer_store_dwordx4 v[144:147], v21, %[rs], s50 offen offset:512 sc1\n\t" "s_waitcnt vmcnt(0)\n\t" \
   "s_cmp_lt_i32 s40, 2\n\t" "s_cselect_b32 s47, 0x7ffffff0, 0\n\t" "v_or_b32 v30, s47, v20\n\t" "s_sub_i32 s47, s41, 2\n\t" \
   "s_max_i32 s47, s47, 0\n\t" "s_lshl_b32 s47, s47, 12\n\t" "buffer_store_dword v31, v30, %[rs], s47 offen sc1\n\t" "s_cmp_lt_i32 s40, 1\n\t" \
   "s_cselect_b32 s47, 0x7ffffff0, 0\n\t" "v_or_b32 v30, s47, v20\n\t" "s_sub_i32 s47, s41, 1\n\t" "s_max_i32 s47, s47, 0\n\t" \
   "s_lshl_b32 s47, s47, 12\n\t" "buffer_store_dword v31, v30, %[rs], s47 offen sc1\n\t" "s_branch 9f\n\t" "172:\n\t" "s_mov_b32 s47, 0\n\t" \
   "buffer_load_dword v30, v19, %[rs], 0 offen sc1\n\t" "s_waitcnt vmcnt(0)\n\t" "v_add_u32 v30, 1, v30\n\t" "v_cmp_lt_u32 vcc, 7, v30\n\t" \
   "s_mov_b32 s44, vcc_lo\n\t" "s_bitcmp1_b32 s44, s48\n\t" "s_cbranch_scc1 72b\n\t" "s_bitset1_b32 s44, s48\n\t" "s_lshl_b32 s48, s48, 7\n\t" \
   "s_add_u32 s48, s48, %[cnt]\n\t" "73:\n\t" "s_sleep 10\n\t" "buffer_load_dword v30, off, %[rs], s48 sc1\n\t" "s_add_u32 s47, s47, 1\n\t" \
   "s_waitcnt vmcnt(0)\n\t" "v_add_u32 v30, 1, v30\n\t" "v_cmp_lt_u32 vcc, 7, v30\n\t" "s_cmp_gt_u32 s47, 0x20000\n\t" "s_cbranch_scc1 72b\n\t" \
   "s_cbranch_vccz 73b\n\t" "s_branch 72b\n\t" "176:\n\t" "s_mov_b32 s47, 0\n\t" "buffer_load_dword v30, v19, %[rs], 0 offen sc1\n\t" \
   "s_waitcnt vmcnt(0)\n\t" "v_add_u32 v30, 1, v30\n\t" "v_cmp_lt_u32 vcc, 7, v30\n\t" "s_mov_b32 s44, vcc_lo\n\t" "s_bitcmp1_b32 s44, s48\n\t" \
   "s_cbranch_scc1 76b\n\t" "s_bitset1_b32 s44, s48\n\t" "s_lshl_b32 s48, s48, 7\n\t" "s_add_u32 s48, s48, %[cnt]\n\t" "77:\n\t" "s_sleep 10\n\t" \
   "buffer_load_dword v30, off, %[rs], s48 sc1\n\t" "s_add_u32 s47, s47, 1\n\t" "s_waitcnt vmcnt(0)\n\t" "v_add_u32 v30, 1, v30\n\t" \
   "v_cmp_lt_u32 vcc, 7, v30\n\t" "s_cmp_gt_u32 s47, 0x20000\n\t" "s_cbranch_scc1 76b\n\t" "s_cbranch_vccz 77b\n\t" "s_branch 76b\n\t" "174:\n\t" \
   "s_mov_b32 s47, 0\n\t" "buffer_load_dword v30, v19, %[rs], 0 offen sc1\n\t" "s_waitcnt vmcnt(0)\n\t" "v_add_u32 v30, 1, v30\n\t" \
   "v_cmp_lt_u32 vcc, 7, v30\n\t" "s_mov_b32 s44, vcc_lo\n\t" "s_bitcmp1_b32 s44, s48\n\t" "s_cbranch_scc1 74b\n\t" "s_bitset1_b32 s44, s48\n\t" \
   "s_lshl_b32 s48, s48, 7\n\t" "s_add_u32 s48, s48, %[cnt]\n\t" "75:\n\t" "s_sleep 10\n\t" "buffer_load_dword v30, off, %[rs], s48 sc1\n\t" \
   "s_add_u32 s47, s47, 1\n\t" "s_waitcnt vmcnt(0)\n\t" "v_add_u32 v30, 1, v30\n\t" "v_cmp_lt_u32 vcc, 7, v30\n\t" "s_cmp_gt_u32 s47, 0x20000\n\t" \
   "s_cbranch_scc1 74b\n\t" "s_cbranch_vccz 75b\n\t" "s_branch 74b\n\t" "150:\n\t" "ds_read_b128 v[156:159], v12\n\t" "s_waitcnt lgkmcnt(0)\n\t" \
   "v_min3_u32 v156, v156, v157, v158\n\t" "v_min_u32 v156, v156, v159\n\t" "v_cmp_ge_u32 vcc, v156, v138\n\t" "s_cbranch_vccz 150b\n\t" \
   "s_branch 50b\n\t" "152:\n\t" "ds_read_b128 v[156:159], v12\n\t" "s_waitcnt lgkmcnt(0)\n\t" "v_min3_u32 v156, v156, v157, v158\n\t" \
   "v_min_u32 v156, v156, v159\n\t" "v_cmp_ge_u32 vcc, v156, v138\n\t" "s_cbranch_vccz 152b\n\t" "s_branch 52b\n\t" "9:\n\t" "s_waitcnt vmcnt(0)"
#define VU_CLOB "v8", "v9", "v10", "v11", "v12", "v13", "v14", "v15", "v32", "v33", "v34", "v35", "v36", "v37", "v38", "v39", "v40", "v41", "v42", "v43", "v44", "v45", "v46", "v47", "v48", "v49", "v50", "v51", "v52", "v53", "v54", "v55", "v56", "v57", "v58", "v59", "v60", "v61", "v62", "v63", "v64", "v65", "v66", "v67", "v68", "v69", "v70", "v71", "v72", "v73", "v74", "v75", "v76", "v77", "v78", "v79", "v80", "v81", "v82", "v83", "v84", "v85", "v86", "v87", "v88", "v89", "v90", "v91", "v92", "v93", "v94", "v95", "v96", "v97", "v98", "v99", "v100", "v101", "v102", "v103", "v104", "v105", "v106", "v107", "v108", "v109", "v110", "v111", "v112", "v113", "v114", "v115", "v116", "v117", "v118", "v119", "v120", "v121", "v122", "v123", "v124", "v125", "v126", "v127", "v128", "v129", "v130", "v131", "v132", "v133", "v134", "v135", "v136", "v137", "v138", "v139", "v140", "v141", "v142", "v143", "v144", "v145", "v146", "v147", "v148", "v149", "v150", "v151", "v152", "v153", "v154", "v155", "v156", "v157", "v158", "v159", "v160", "v161", "v162", "v163", "v164", "v165", "v166", "v167", "v168", "v169", "v170", "v171", "v172", "v173", "v174", "v175", "v176", "v177", "v178", "v179", "v180", "v181", "v182", "v183", "v184", "v185", "v186", "v187", "v188", "v189", "v190", "v191", "s40", "s41", "s42", "s43", "s44", "s45", "s46", "s47", "s48", "s49", "s50", "s51", "s52", "s53", "s54", "s55"
#define DF_ASM \
   "v_mov_b32 v80, 0x3f803f80\n\t" "v_mov_b32 v81, 0x3f803f80\n\t" "v_mov_b32 v82, 0x3f803f80\n\t" "v_mov_b32 v83, 0x3f803f80\n\t" \
   "s_mov_b32 s45, %[set]\n\t" "s_mov_b32 s46, 0\n\t" "v_mfma_f32_16x16x32_bf16 v[32:35], a[0:3], v[80:83], 0\n\t" \
   "v_mfma_f32_16x16x32_bf16 v[32:35], a[4:7], v[80:83], v[32:35]\n\t" "v_mfma_f32_16x16x32_bf16 v[32:35], a[8:11], v[80:83], v[32:35]\n\t" \
   "v_mfma_f32_16x16x32_bf16 v[32:35], a[12:15], v[80:83], v[32:35]\n\t" "v_mfma_f32_16x16x32_bf16 v[32:35], a[16:19], v[80:83], v[32:35]\n\t" \
   "v_mfma_f32_16x16x32_bf16 v[32:35], a[20:23], v[80:83], v[32:35]\n\t" "v_mfma_f32_16x16x32_bf16 v[32:35], a[24:27], v[80:83], v[32:35]\n\t" \
   "v_mfma_f32_16x16x32_bf16 v[32:35], a[28:31], v[80:83], v[32:35]\n\t" "v_mfma_f32_16x16x32_bf16 v[32:35], a[32:35], v[80:83], v[32:35]\n\t" \
   "v_mfma_f32_16x16x32_bf16 v[32:35], a[36:39], v[80:83], v[32:35]\n\t" "v_mfma_f32_16x16x32_bf16 v[32:35], a[40:43], v[80:83], v[32:35]\n\t" \
   "v_mfma_f32_16x16x32_bf16 v[32:35], a[44:47], v[80:83], v[32:35]\n\t" "v_mfma_f32_16x16x32_bf16 v[32:35], a[48:51], v[80:83], v[32:35]\n\t" \
   "v_mfma_f32_16x16x32_bf16 v[32:35], a[52:55], v[80:83], v[32:35]\n\t" "v_mfma_f32_16x16x32_bf16 v[32:35], a[56:59], v[80:83], v[32:35]\n\t" \
   "v_mfma_f32_16x16x32_bf16 v[32:35], a[60:63], v[80:83], v[32:35]\n\t" "v_mfma_f32_16x16x32_bf16 v[32:35], a[64:67], v[80:83], v[32:35]\n\t" \
   "v_mfma_f32_16x16x32_bf16 v[32:35], a[68:71], v[80:83], v[32:35]\n\t" "v_mfma_f32_16x16x32_bf16 v[32:35], a[72:75], v[80:83], v[32:35]\n\t" \
   "v_mfma_f32_16x16x32_bf16 v[32:35], a[76:79], v[80:83], v[32:35]\n\t" "v_mfma_f32_16x16x32_bf16 v[36:39], a[80:83], v[80:83], 0\n\t" \
   "v_mfma_f32_16x16x32_bf16 v[36:39], a[84:87], v[80:83], v[36:39]\n\t" "v_mfma_f32_16x16x32_bf16 v[36:39], a[88:91], v[80:83], v[36:39]\n\t" \
   "v_mfma_f32_16x16x32_bf16 v[36:39], a[92:95], v[80:83], v[36:39]\n\t" "v_mfma_f32_16x16x32_bf16 v[36:39], a[96:99], v[80:83], v[36:39]\n\t" \
   "v_mfma_f32_16x16x32_bf16 v[36:39], a[100:103], v[80:83], v[36:39]\n\t" "v_mfma_f32_16x16x32_bf16 v[36:39], a[104:107], v[80:83], v[36:39]\n\t" \
   "v_mfma_f32_16x16x32_bf16 v[36:39], a[108:111], v[80:83], v[36:39]\n\t" "v_mfma_f32_16x16x32_bf16 v[36:39], a[112:115], v[80:83], v[36:39]\n\t" \
   "v_mfma_f32_16x16x32_bf16 v[36:39], a[116:119], v[80:83], v[36:39]\n\t" "v_mfma_f32_16x16x32_bf16 v[36:39], a[120:123], v[80:83], v[36:39]\n\t" \
   "v_mfma_f32_16x16x32_bf16 v[36:39], a[124:127], v[80:83], v[36:39]\n\t" "v_mfma_f32_16x16x32_bf16 v[36:39], a[128:131], v[80:83], v[36:39]\n\t" \
   "v_mfma_f32_16x16x32_bf16 v[36:39], a[132:135], v[80:83], v[36:39]\n\t" "v_mfma_f32_16x16x32_bf16 v[36:39], a[136:139], v[80:83], v[36:39]\n\t" \
   "v_mfma_f32_16x16x32_bf16 v[36:39], a[140:143], v[80:83], v[36:39]\n\t" "v_mfma_f32_16x16x32_bf16 v[36:39], a[144:147], v[80:83], v[36:39]\n\t" \
   "v_mfma_f32_16x16x32_bf16 v[36:39], a[148:151], v[80:83], v[36:39]\n\t" "v_mfma_f32_16x16x32_bf16 v[36:39], a[152:155], v[80:83], v[36:39]\n\t" \
   "v_mfma_f32_16x16x32_bf16 v[36:39], a[156:159], v[80:83], v[36:39]\n\t" "1:\n\t" "s_lshl_b32 s48, s45, 7\n\t" "s_mov_b32 s47, 0\n\t" \
   "s_cmp_lg_u32 %[ldx], %[ld0]\n\t" "s_cbranch_scc1 3f\n\t" "2:\n\t" "buffer_load_dword v30, v19, %[rs], s48 offen sc1\n\t" \
   "s_add_u32 s47, s47, 1\n\t" "s_waitcnt vmcnt(0)\n\t" "v_readfirstlane_b32 s44, v30\n\t" "s_add_u32 s44, s44, 1\n\t" "s_cmp_ge_u32 s44, 10\n\t" \
   "s_cbranch_scc1 3f\n\t" "s_cmp_gt_u32 s47, 0x20000\n\t" "s_cbranch_scc1 3f\n\t" "s_sleep 3\n\t" "s_cmp_lg_u32 s44, 0\n\t" "s_cbranch_scc1 2b\n\t" \
   "s_sleep 8\n\t" "s_branch 2b\n\t" "3:\n\t" "s_barrier\n\t" "s_mul_i32 s52, s45, 1536\n\t" "s_mul_i32 s51, s45, 40960\n\t" \
   "s_add_u32 m0, s46, %[ldx]\n\t" "s_mov_b32 s53, s46\n\t" "buffer_load_dwordx4 v16, %[rs], s51 offen offset:0 sc1 lds\n\t" \
   "buffer_load_dwordx4 v16, %[rs], s51 offen offset:1024 sc1 lds\n\t" "buffer_load_dwordx4 v16, %[rs], s51 offen offset:2048 sc1 lds\n\t" \
   "buffer_load_dwordx4 v16, %[rs], s51 offen offset:3072 sc1 lds\n\t" "s_add_u32 m0, m0, 4096\n\t" "s_add_u32 s51, s51, 4096\n\t" \
   "buffer_load_dwordx4 v16, %[rs], s51 offen offset:0 sc1 lds\n\t" "buffer_load_dwordx4 v16, %[rs], s51 offen offset:1024 sc1 lds\n\t" \
   "buffer_load_dwordx4 v16, %[rs], s51 offen offset:2048 sc1 lds\n\t" "buffer_load_dwordx4 v16, %[rs], s51 offen offset:3072 sc1 lds\n\t" \
   "s_add_u32 m0, m0, 4096\n\t" "s_add_u32 s51, s51, 4096\n\t" "buffer_load_dwordx4 v16, %[rs], s51 offen offset:0 sc1 lds\n\t" \
   "buffer_load_dwordx4 v16, %[rs], s51 offen offset:1024 sc1 lds\n\t" "s_add_u32 m0, s53, %[ld1]\n\t" "s_nop 0\n\t" \
   "buffer_load_dwordx4 v17, %[rs], s52 offen sc1 lds\n\t" "v_add_u32 v26, s46, v22\n\t" "v_add_u32 v27, s46, v23\n\t" "s_waitcnt vmcnt(0)\n\t" \
   "s_barrier\n\t" "ds_read_b128 v[80:83], v26 offset:0\n\t" "ds_read_b128 v[84:87], v26 offset:1024\n\t" "ds_read_b128 v[88:91], v26 offset:2048\n\t" \
   "ds_read_b128 v[92:95], v26 offset:3072\n\t" "ds_read_b128 v[96:99], v26 offset:4096\n\t" "ds_read_b128 v[100:103], v26 offset:5120\n\t" \
   "ds_read_b128 v[104:107], v26 offset:6144\n\t" "s_waitcnt lgkmcnt(5)\n\t" "v_mfma_f32_16x16x32_bf16 v[52:55], a[80:83], v[80:83], 0\n\t" \
   "ds_read_b128 v[112:115], v27 offset:0\n\t" "v_mfma_f32_16x16x32_bf16 v[48:51], a[0:3], v[80:83], 0\n\t" \
   "ds_read_b128 v[108:111], v26 offset:7168\n\t" "ds_read_b128 v[116:119], v27 offset:16\n\t" \
   "v_mfma_f32_16x16x32_bf16 v[52:55], a[84:87], v[84:87], v[52:55]\n\t" "ds_read_b64 v[120:121], v27 offset:32\n\t" \
   "v_mfma_f32_16x16x32_bf16 v[48:51], a[4:7], v[84:87], v[48:51]\n\t" "ds_read_b128 v[80:83], v26 offset:8192\n\t" "s_waitcnt lgkmcnt(8)\n\t" \
   "v_mfma_f32_16x16x32_bf16 v[52:55], a[88:91], v[88:91], v[52:55]\n\t" "ds_read_b128 v[122:125], v27 offset:768\n\t" \
   "v_mfma_f32_16x16x32_bf16 v[48:51], a[8:11], v[88:91], v[48:51]\n\t" "ds_read_b128 v[84:87], v26 offset:9216\n\t" \
   "ds_read_b128 v[126:129], v27 offset:784\n\t" "v_mfma_f32_16x16x32_bf16 v[52:55], a[92:95], v[92:95], v[52:55]\n\t" \
   "v_mfma_f32_16x16x32_bf16 v[48:51], a[12:15], v[92:95], v[48:51]\n\t" "ds_read_b128 v[88:91], v26 offset:10240\n\t" \
   "ds_read_b64 v[130:131], v27 offset:800\n\t" "s_waitcnt lgkmcnt(11)\n\t" "v_mfma_f32_16x16x32_bf16 v[52:55], a[96:99], v[96:99], v[52:55]\n\t" \
   "s_lshl_b32 s48, s45, 8\n\t" "v_mfma_f32_16x16x32_bf16 v[48:51], a[16:19], v[96:99], v[48:51]\n\t" "ds_read_b128 v[92:95], v26 offset:11264\n\t" \
   "s_add_u32 s47, s48, %[xs4]\n\t" "v_mfma_f32_16x16x32_bf16 v[52:55], a[100:103], v[100:103], v[52:55]\n\t" \
   "v_mfma_f32_16x16x32_bf16 v[48:51], a[20:23], v[100:103], v[48:51]\n\t" "ds_read_b128 v[96:99], v26 offset:12288\n\t" "s_waitcnt lgkmcnt(8)\n\t" \
   "v_add_f32 v112, v112, v114\n\t" "v_mfma_f32_16x16x32_bf16 v[52:55], a[104:107], v[104:107], v[52:55]\n\t" "v_add_f32 v113, v113, v115\n\t" \
   "v_mfma_f32_16x16x32_bf16 v[48:51], a[24:27], v[104:107], v[48:51]\n\t" "ds_read_b128 v[100:103], v26 offset:13312\n\t" \
   "v_mfma_f32_16x16x32_bf16 v[52:55], a[108:111], v[108:111], v[52:55]\n\t" "v_add_f32 v116, v116, v118\n\t" \
   "v_mfma_f32_16x16x32_bf16 v[48:51], a[28:31], v[108:111], v[48:51]\n\t" "ds_read_b128 v[104:107], v26 offset:14336\n\t" \
   "v_add_f32 v117, v117, v119\n\t" "s_waitcnt lgkmcnt(7)\n\t" "v_mfma_f32_16x16x32_bf16 v[52:55], a[112:115], v[80:83], v[52:55]\n\t" \
   "v_mfma_f32_16x16x32_bf16 v[48:51], a[32:35], v[80:83], v[48:51]\n\t" "ds_read_b128 v[108:111], v26 offset:15360\n\t" \
   "v_add_f32 v112, v112, v120\n\t" "v_mfma_f32_16x16x32_bf16 v[52:55], a[116:119], v[84:87], v[52:55]\n\t" "v_add_f32 v113, v113, v121\n\t" \
   "v_mfma_f32_16x16x32_bf16 v[48:51], a[36:39], v[84:87], v[48:51]\n\t" "ds_read_b128 v[80:83], v26 offset:16384\n\t" "v_add_f32 v112, v112, v116\n\t" \
   "s_waitcnt lgkmcnt(5)\n\t" "v_mfma_f32_16x16x32_bf16 v[52:55], a[120:123], v[88:91], v[52:55]\n\t" \
   "v_mfma_f32_16x16x32_bf16 v[48:51], a[40:43], v[88:91], v[48:51]\n\t" "ds_read_b128 v[84:87], v26 offset:17408\n\t" "v_add_f32 v113, v113, v117\n\t" \
   "v_mfma_f32_16x16x32_bf16 v[52:55], a[124:127], v[92:95], v[52:55]\n\t" "v_mul_f32 v112, 0xbacccccd, v112\n\t" \
   "v_mfma_f32_16x16x32_bf16 v[48:51], a[44:47], v[92:95], v[48:51]\n\t" "ds_read_b128 v[88:91], v26 offset:18432\n\t" "s_waitcnt lgkmcnt(5)\n\t" \
   "v_mfma_f32_16x16x32_bf16 v[52:55], a[128:131], v[96:99], v[52:55]\n\t" "v_mul_f32 v113, 0x3acccccd, v113\n\t" \
   "v_mfma_f32_16x16x32_bf16 v[48:51], a[48:51], v[96:99], v[48:51]\n\t" "ds_read_b128 v[92:95], v26 offset:19456\n\t" \
   "v_fma_f32 v113, -v112, v112, v113\n\t" "v_mfma_f32_16x16x32_bf16 v[52:55], a[132:135], v[100:103], v[52:55]\n\t" "v_max_f32 v113, 0, v113\n\t" \
   "v_mfma_f32_16x16x32_bf16 v[48:51], a[52:55], v[100:103], v[48:51]\n\t" "ds_read_b128 v[96:99], v26 offset:20480\n\t" "s_waitcnt lgkmcnt(5)\n\t" \
   "v_mfma_f32_16x16x32_bf16 v[52:55], a[136:139], v[104:107], v[52:55]\n\t" "v_cvt_pk_bf16_f32 v113, v113, v112\n\t" \
   "v_mfma_f32_16x16x32_bf16 v[48:51], a[56:59], v[104:107], v[48:51]\n\t" "ds_read_b128 v[100:103], v26 offset:21504\n\t" \
   "v_and_b32 v8, 0xffff0000, v113\n\t" "v_mfma_f32_16x16x32_bf16 v[52:55], a[140:143], v[108:111], v[52:55]\n\t" \
   "v_mfma_f32_16x16x32_bf16 v[48:51], a[60:63], v[108:111], v[48:51]\n\t" "ds_read_b128 v[104:107], v26 offset:22528\n\t" \
   "v_lshlrev_b32 v113, 16, v113\n\t" "s_waitcnt lgkmcnt(5)\n\t" "v_mfma_f32_16x16x32_bf16 v[52:55], a[144:147], v[80:83], v[52:55]\n\t" \
   "v_add_f32 v113, 0x3727c5ac, v113\n\t" "v_mfma_f32_16x16x32_bf16 v[48:51], a[64:67], v[80:83], v[48:51]\n\t" \
   "ds_read_b128 v[108:111], v26 offset:23552\n\t" "v_mfma_f32_16x16x32_bf16 v[52:55], a[148:151], v[84:87], v[52:55]\n\t" \
   "v_cvt_pk_bf16_f32 v113, v113, v113\n\t" "v_mfma_f32_16x16x32_bf16 v[48:51], a[68:71], v[84:87], v[48:51]\n\t" \
   "ds_read_b128 v[80:83], v26 offset:24576\n\t" "v_lshlrev_b32 v113, 16, v113\n\t" "s_waitcnt lgkmcnt(5)\n\t" \
   "v_mfma_f32_16x16x32_bf16 v[52:55], a[152:155], v[88:91], v[52:55]\n\t" "v_sqrt_f32 v113, v113\n\t" \
   "v_mfma_f32_16x16x32_bf16 v[48:51], a[72:75], v[88:91], v[48:51]\n\t" "ds_read_b128 v[84:87], v26 offset:25600\n\t" \
   "v_mfma_f32_16x16x32_bf16 v[52:55], a[156:159], v[92:95], v[52:55]\n\t" "s_nop 0\n\t" \
   "v_mfma_f32_16x16x32_bf16 v[48:51], a[76:79], v[92:95], v[48:51]\n\t" "ds_read_b128 v[88:91], v26 offset:26624\n\t" \
   "v_cvt_pk_bf16_f32 v113, v113, v113\n\t" "s_waitcnt lgkmcnt(5)\n\t" "v_mfma_f32_16x16x32_bf16 v[60:63], a[80:83], v[96:99], 0\n\t" \
   "v_mfma_f32_16x16x32_bf16 v[56:59], a[0:3], v[96:99], 0\n\t" "ds_read_b128 v[92:95], v26 offset:27648\n\t" "v_lshlrev_b32 v113, 16, v113\n\t" \
   "v_mfma_f32_16x16x32_bf16 v[60:63], a[84:87], v[100:103], v[60:63]\n\t" "v_rcp_f32 v10, v113\n\t" \
   "v_mfma_f32_16x16x32_bf16 v[56:59], a[4:7], v[100:103], v[56:59]\n\t" "ds_read_b128 v[96:99], v26 offset:28672\n\t" "s_waitcnt lgkmcnt(5)\n\t" \
   "v_mfma_f32_16x16x32_bf16 v[60:63], a[88:91], v[104:107], v[60:63]\n\t" "v_fmac_f32 v48, v32, v8\n\t" "v_fmac_f32 v49, v33, v8\n\t" \
   "v_mfma_f32_16x16x32_bf16 v[56:59], a[8:11], v[104:107], v[56:59]\n\t" "ds_read_b128 v[100:103], v26 offset:29696\n\t" "v_fmac_f32 v50, v34, v8\n\t" \
   "v_mfma_f32_16x16x32_bf16 v[60:63], a[92:95], v[108:111], v[60:63]\n\t" "v_fmac_f32 v51, v35, v8\n\t" \
   "v_mfma_f32_16x16x32_bf16 v[56:59], a[12:15], v[108:111], v[56:59]\n\t" "ds_read_b128 v[104:107], v26 offset:30720\n\t" \
   "v_fmac_f32 v52, v36, v8\n\t" "s_waitcnt lgkmcnt(5)\n\t" "v_mfma_f32_16x16x32_bf16 v[60:63], a[96:99], v[80:83], v[60:63]\n\t" \
   "v_fmac_f32 v53, v37, v8\n\t" "v_fmac_f32 v54, v38, v8\n\t" "v_mfma_f32_16x16x32_bf16 v[56:59], a[16:19], v[80:83], v[56:59]\n\t" \
   "ds_read_b128 v[108:111], v26 offset:31744\n\t" "v_fmac_f32 v55, v39, v8\n\t" \
   "v_mfma_f32_16x16x32_bf16 v[60:63], a[100:103], v[84:87], v[60:63]\n\t" "v_mul_f32 v48, v48, v10\n\t" \
   "v_mfma_f32_16x16x32_bf16 v[56:59], a[20:23], v[84:87], v[56:59]\n\t" "ds_read_b128 v[80:83], v26 offset:32768\n\t" "v_mul_f32 v49, v49, v10\n\t" \
   "s_waitcnt lgkmcnt(5)\n\t" "v_mfma_f32_16x16x32_bf16 v[60:63], a[104:107], v[88:91], v[60:63]\n\t" "v_mul_f32 v50, v50, v10\n\t" \
   "v_mul_f32 v51, v51, v10\n\t" "v_mfma_f32_16x16x32_bf16 v[56:59], a[24:27], v[88:91], v[56:59]\n\t" "ds_read_b128 v[84:87], v26 offset:33792\n\t" \
   "v_mul_f32 v52, v52, v10\n\t" "v_mfma_f32_16x16x32_bf16 v[60:63], a[108:111], v[92:95], v[60:63]\n\t" "v_mul_f32 v53, v53, v10\n\t" \
   "v_mfma_f32_16x16x32_bf16 v[56:59], a[28:31], v[92:95], v[56:59]\n\t" "ds_read_b128 v[88:91], v26 offset:34816\n\t" "v_mul_f32 v54, v54, v10\n\t" \
   "s_waitcnt lgkmcnt(5)\n\t" "v_mfma_f32_16x16x32_bf16 v[60:63], a[112:115], v[96:99], v[60:63]\n\t" "v_mul_f32 v55, v55, v10\n\t" \
   "v_cvt_pk_bf16_f32 v64, v48, v49\n\t" "v_mfma_f32_16x16x32_bf16 v[56:59], a[32:35], v[96:99], v[56:59]\n\t" \
   "ds_read_b128 v[92:95], v26 offset:35840\n\t" "v_cvt_pk_bf16_f32 v65, v50, v51\n\t" \
   "v_mfma_f32_16x16x32_bf16 v[60:63], a[116:119], v[100:103], v[60:63]\n\t" "v_cvt_pk_bf16_f32 v66, v52, v53\n\t" \
   "v_mfma_f32_16x16x32_bf16 v[56:59], a[36:39], v[100:103], v[56:59]\n\t" "ds_read_b128 v[96:99], v26 offset:36864\n\t" \
   "v_cvt_pk_bf16_f32 v67, v54, v55\n\t" "s_waitcnt lgkmcnt(5)\n\t" "v_mfma_f32_16x16x32_bf16 v[60:63], a[120:123], v[104:107], v[60:63]\n\t" \
   "buffer_store_dwordx2 v[64:65], v21, %[rs], s48 offen offset:0 sc1\n\t" "buffer_store_dwordx2 v[66:67], v21, %[rs], s47 offen offset:0 sc1\n\t" \
   "v_mfma_f32_16x16x32_bf16 v[56:59], a[40:43], v[104:107], v[56:59]\n\t" "ds_read_b128 v[100:103], v26 offset:37888\n\t" \
   "v_add_f32 v122, v122, v124\n\t" "v_mfma_f32_16x16x32_bf16 v[60:63], a[124:127], v[108:111], v[60:63]\n\t" "v_add_f32 v123, v123, v125\n\t" \
   "v_mfma_f32_16x16x32_bf16 v[56:59], a[44:47], v[108:111], v[56:59]\n\t" "ds_read_b128 v[104:107], v26 offset:38912\n\t" \
   "v_add_f32 v126, v126, v128\n\t" "s_waitcnt lgkmcnt(5)\n\t" "v_mfma_f32_16x16x32_bf16 v[60:63], a[128:131], v[80:83], v[60:63]\n\t" \
   "v_add_f32 v127, v127, v129\n\t" "v_add_f32 v122, v122, v130\n\t" "v_mfma_f32_16x16x32_bf16 v[56:59], a[48:51], v[80:83], v[56:59]\n\t" \
   "ds_read_b128 v[108:111], v26 offset:39936\n\t" "v_add_f32 v123, v123, v131\n\t" \
   "v_mfma_f32_16x16x32_bf16 v[60:63], a[132:135], v[84:87], v[60:63]\n\t" "v_add_f32 v122, v122, v126\n\t" \
   "v_mfma_f32_16x16x32_bf16 v[56:59], a[52:55], v[84:87], v[56:59]\n\t" "v_add_f32 v123, v123, v127\n\t" "s_waitcnt lgkmcnt(4)\n\t" \
   "v_mfma_f32_16x16x32_bf16 v[60:63], a[136:139], v[88:91], v[60:63]\n\t" "v_mul_f32 v122, 0xbacccccd, v122\n\t" \
   "v_mul_f32 v123, 0x3acccccd, v123\n\t" "v_mfma_f32_16x16x32_bf16 v[56:59], a[56:59], v[88:91], v[56:59]\n\t" "v_fma_f32 v123, -v122, v122, v123\n\t" \
   "v_mfma_f32_16x16x32_bf16 v[60:63], a[140:143], v[92:95], v[60:63]\n\t" "v_max_f32 v123, 0, v123\n\t" \
   "v_mfma_f32_16x16x32_bf16 v[56:59], a[60:63], v[92:95], v[56:59]\n\t" "v_cvt_pk_bf16_f32 v123, v123, v122\n\t" "s_waitcnt lgkmcnt(2)\n\t" \
   "v_mfma_f32_16x16x32_bf16 v[60:63], a[144:147], v[96:99], v[60:63]\n\t" "v_and_b32 v12, 0xffff0000, v123\n\t" "v_lshlrev_b32 v123, 16, v123\n\t" \
   "v_mfma_f32_16x16x32_bf16 v[56:59], a[64:67], v[96:99], v[56:59]\n\t" "v_add_f32 v123, 0x3727c5ac, v123\n\t" \
   "v_mfma_f32_16x16x32_bf16 v[60:63], a[148:151], v[100:103], v[60:63]\n\t" "v_cvt_pk_bf16_f32 v123, v123, v123\n\t" \
   "v_mfma_f32_16x16x32_bf16 v[56:59], a[68:71], v[100:103], v[56:59]\n\t" "v_lshlrev_b32 v123, 16, v123\n\t" "s_waitcnt lgkmcnt(0)\n\t" \
   "v_mfma_f32_16x16x32_bf16 v[60:63], a[152:155], v[104:107], v[60:63]\n\t" "v_sqrt_f32 v123, v123\n\t" "s_nop 0\n\t" \
   "v_mfma_f32_16x16x32_bf16 v[56:59], a[72:75], v[104:107], v[56:59]\n\t" "v_cvt_pk_bf16_f32 v123, v123, v123\n\t" \
   "v_mfma_f32_16x16x32_bf16 v[60:63], a[156:159], v[108:111], v[60:63]\n\t" "v_lshlrev_b32 v123, 16, v123\n\t" \
   "v_mfma_f32_16x16x32_bf16 v[56:59], a[76:79], v[108:111], v[56:59]\n\t" "v_rcp_f32 v132, v123\n\t" "s_xor_b32 s46, s46, 44032\n\t" \
   "s_add_u32 s45, s45, 4\n\t" "s_nop 5\n\t" "v_fmac_f32 v56, v32, v12\n\t" "v_fmac_f32 v57, v33, v12\n\t" "v_fmac_f32 v58, v34, v12\n\t" \
   "v_fmac_f32 v59, v35, v12\n\t" "v_fmac_f32 v60, v36, v12\n\t" "v_fmac_f32 v61, v37, v12\n\t" "v_fmac_f32 v62, v38, v12\n\t" \
   "v_fmac_f32 v63, v39, v12\n\t" "v_mul_f32 v56, v56, v132\n\t" "v_mul_f32 v57, v57, v132\n\t" "v_mul_f32 v58, v58, v132\n\t" \
   "v_mul_f32 v59, v59, v132\n\t" "v_mul_f32 v60, v60, v132\n\t" "v_mul_f32 v61, v61, v132\n\t" "v_mul_f32 v62, v62, v132\n\t" \
   "v_mul_f32 v63, v63, v132\n\t" "v_cvt_pk_bf16_f32 v68, v56, v57\n\t" "v_cvt_pk_bf16_f32 v69, v58, v59\n\t" "v_cvt_pk_bf16_f32 v70, v60, v61\n\t" \
   "v_cvt_pk_bf16_f32 v71, v62, v63\n\t" "buffer_store_dwordx2 v[68:69], v21, %[rs], s48 offen offset:128 sc1\n\t" \
   "buffer_store_dwordx2 v[70:71], v21, %[rs], s47 offen offset:128 sc1\n\t" "s_cmp_lt_u32 s45, 64\n\t" "s_cbranch_scc1 1b\n\t" \
   "s_waitcnt vmcnt(0) lgkmcnt(0)"
#define DF_CLOB "v8", "v9", "v10", "v11", "v12", "v13", "v14", "v15", "v32", "v33", "v34", "v35", "v36", "v37", "v38", "v39", "v40", "v41", "v42", "v43", "v44", "v45", "v46", "v47", "v48", "v49", "v50", "v51", "v52", "v53", "v54", "v55", "v56", "v57", "v58", "v59", "v60", "v61", "v62", "v63", "v64", "v65", "v66", "v67", "v68", "v69", "v70", "v71", "v72", "v73", "v74", "v75", "v76", "v77", "v78", "v79", "v80", "v81", "v82", "v83", "v84", "v85", "v86", "v87", "v88", "v89", "v90", "v91", "v92", "v93", "v94", "v95", "v96", "v97", "v98", "v99", "v100", "v101", "v102", "v103", "v104", "v105", "v106", "v107", "v108", "v109", "v110", "v111", "v112", "v113", "v114", "v115", "v116", "v117", "v118", "v119", "v120", "v121", "v122", "v123", "v124", "v125", "v126", "v127", "v128", "v129", "v130", "v131", "v132", "v133", "v134", "v135", "s40", "s41", "s42", "s43", "s44", "s45", "s46", "s47", "s48", "s49", "s50", "s51", "s52", "s53"
template <bool VU>
__device__ __forceinline__ void job_proj(int wv_, const v32u (&w)[10], unsigned char* ws, const Pool& pool, int layer, int h) {
  const unsigned lds0 = (unsigned)(uintptr_t)(__attribute__((address_space(3))) unsigned char*)dyn_lds;
  const int lane = lane_id(), wv = wv_, g = lane >> 4, r = lane & 15;
  const v4u rs = make_rs(ws, (unsigned)OFF_END);
  v16u ad = (unsigned)lane;
  ad[0] = (int)OFF_XR + wv * 10240 + lane * 16;
  ad[1] = wv * 64 + lane < 96 ? (int)OFF_ST + wv * 1024 + lane * 16 : OOB;
  ad[3] = cnt_off(layer, CNT_IDFT, lane & 31);
  ad[4] = lane == 0 ? kqf_off(layer, 0, (VU ? 32 : 0) + h) + wv * 4 : OOB;
  const int q = (g >> 1) * 256 + r * 16;
  ad[5] = (VU ? (int)OFF_UF + (wv * 320 + h) * 1024 + (g & 1) * 32768 : (h * RT * 4 + wv) * 1024 + (g & 1) * 4096) + q;
  ad[6] = lds0 + PJ_X + (g & 1) * 20480 + q;
  ad[7] = lds0 + PJ_X + 40960 + (lane & 31) * 48;
  ad[8] = lds0 + wv * 2048 + lane * 16;
  ad[9] = lds0 + 8192 + lane * 16;
  v4u rf = rs;
  v8u fo;
  if constexpr (!VU) {
    fo[0] = h * 16384 + wv * 2048 + lane * 16;
#pragma unroll
    for (int k = 0; k < 3; ++k) { const int u = h * 10 + wv + 4 * k; fo[1 + k] = wv + 4 * k < 10 ? (u >> 3) * 16384 + (u & 7) * 1024 + lane * 16 : OOB; }
    asm volatile("v_mov_b32 %0, -1" : "=v"(fo[4]));
    fo[5] = fo[6] = fo[7] = fo[4];
  }
  if (layer > 0) fan_wait<64, NFIRST>(wv_, ws, fan_off(1, 1, 0), h);
  PH(2 + layer * 6)
#define PJ_DBG
#define PJ_OPS                                                                                                                      \
  : W_OPS, [rs] "s"(rs), [rdy] "s"((layer - 1) | ((1 << NFIRST / 8) - 1)), [ldx] "s"(lds0 + PJ_X + wv * 10240),                        \
    [ld1] "s"(lds0 + PJ_X + 40960 + (wv < 2 ? wv : 2) * 1024), [qf] "s"((int)OFF_QF), [kf] "s"((int)OFF_KF), [cnt] "s"(cnt_off(layer, CNT_IDFT, 0)), \
    [flw] "s"(lds0 + PJ_LDS + 512 + wv * 4) PJ_DBG
  if constexpr (VU) asm volatile(VU_ASM : "+{v[16:31]}"(ad) PJ_OPS : VU_CLOB, "vcc", "scc", "m0", "memory");
  else asm volatile(KQ_ASM : "+{v[16:31]}"(ad), "+&{s[56:59]}"(rf) PJ_OPS, "{v[152:159]}"(fo), [sel] "s"(pool.sel), [ysel] "s"(pool.ysel), [pool] "s"(pool.base),
                    [yb] "s"(pool.base + P_Y), [size] "s"((unsigned)OFF_END), [skip] "s"(h >= 20 && h < 30 ? 3ull << 62 : 2ull << 62)
                    : KQ_CLOB, "vcc", "scc", "m0", "memory");
  LDSBAR();
}

__device__ __forceinline__ unsigned dft_idx(int f, int d) { return (unsigned)((f == 1 ? 320 : f >> 1) * d + (((f & 1) && f != 1) ? 160 : 0)) % 640u; }
__device__ __forceinline__ float cos640(unsigned i) { return __builtin_amdgcn_cosf((float)i * (1.f / 640)); }
__device__ __forceinline__ void job_dft(int wv_, unsigned char* ws, int layer, int fg, int set) {
  const unsigned lds0 = (unsigned)(uintptr_t)(__attribute__((address_space(3))) unsigned char*)dyn_lds;
  const int lane = lane_id(), wv = wv_, g = lane >> 4, r = lane & 15;
  const v4u rs = make_rs(ws, (unsigned)OFF_END);
  v32u w[5];
#pragma unroll
  for (int t = 0; t < 2; ++t) {
    const int f = 16 * (fg * 8 + wv * 2 + t) + r;
    const unsigned step = dft_idx(f & ~(f != 1), 32);
#pragma unroll
    for (int q = 0; q < 4; ++q) {
      unsigned m0 = dft_idx(f, sigma(g, 2 * q)), m1 = dft_idx(f, sigma(g, 2 * q + 1));
#pragma unroll
      for (int kt = 0; kt < KT; ++kt) {
        const int i = t * KT + kt;
        w[i >> 3][(i & 7) * 4 + q] = pk(cos640(m0), cos640(m1));
        m0 += step; m0 = min(m0, m0 - 640u); m1 += step; m1 = min(m1, m1 - 640u);
      }
    }
  }
  v16u ad = (unsigned)lane;
  ad[0] = (int)OFF_XR + wv * 10240 + lane * 16;
  ad[1] = wv * 64 + lane < 96 ? (int)OFF_ST + wv * 1024 + lane * 16 : OOB;
  ad[3] = cnt_off(layer, CNT_OUT, 0);
  ad[5] = (int)OFF_X1T + (((fg * 8 + wv * 2) * 4 + g) * XS + r) * 8;
  ad[6] = lds0 + lane * 16;
  ad[7] = lds0 + 40960 + r * 48;
  asm volatile(DF_ASM
               : "+{v[16:31]}"(ad)
               : "{a[0:31]}"(w[0]), "{a[32:63]}"(w[1]), "{a[64:95]}"(w[2]), "{a[96:127]}"(w[3]), "{a[128:159]}"(w[4]), [rs] "s"(rs), [set] "s"(set), [xs4] "s"(4 * XS * 8),
                 [ldx] "s"(lds0 + wv * 10240), [ld0] "s"(lds0), [ld1] "s"(lds0 + 40960 + (wv < 2 ? wv : 2) * 1024)
               : DF_CLOB, "vcc", "scc", "m0", "memory");
}

constexpr int NOUT = 44, NLATE = 108;
constexpr int L_HS = PJ_LDS, MAIN_LDS = L_HS + 1024;
static_assert(MAIN_LDS >= JOB_LDS && MAIN_LDS >= REC_LDS && MAIN_LDS <= 160 * 1024, "LDS");
struct Go { Pool pool; int rot; };
__device__ __forceinline__ void st32(rsrc_t rs, int off, unsigned v) { __builtin_amdgcn_raw_buffer_store_b32(v, rs, off, 0, AUX_SC1); }
__device__ __forceinline__ unsigned hello_load(rsrc_t rs, int b) { return __builtin_amdgcn_raw_buffer_load_b32(rs, HS_GO + b * 16, 0, AUX_SC1); }
__device__ __forceinline__ void hello(int wv, rsrc_t rs, int b, unsigned t) {
  if (wv == 0) {
    st32(rs, lane_id() == 0 ? HS_HI + b * 8 : OOB, t + 1u);
    LDS_U32(lds_base() + L_HS) = t + 1u;
  }
}
__device__ __forceinline__ int go_off(int b) {
  const int l = lane_id() & 3;
  return l == 0 ? HS_GO + b * 16 : (l == 3 ? HS_ASK + (b & 127) * 16 : HS_PAY + (b & 31) * 32 + l * 16 - 16);
}
__device__ __forceinline__ Go wait_go(int wv, unsigned char* ws, int b, v4u rep, v4u m) {
  const v4u rs = make_rs(ws, (unsigned)OFF_END);
  const int l = lane_id();
  const unsigned a = __builtin_amdgcn_readfirstlane(LDS_U32(lds_base() + L_HS));
  for (unsigned spins = 0; spins < (1u << 18); ++spins) {
    if (spins || b >= 128) {
      VM_LD128(m, go_off(b), 0, 0);
      VM_WAIT1(0, m);
    }
    const unsigned n = __builtin_amdgcn_readlane(m.y, 0);
    if (__builtin_amdgcn_readlane(m.x, 0) == a && __builtin_amdgcn_readlane(m.x, 1) == n && __builtin_amdgcn_readlane(m.x, 2) == n) break;
    const unsigned t = (wv & 2) ? ((wv & 1) ? m.w : m.z) : ((wv & 1) ? m.y : m.x);
    const bool again = b >= 128 && l == 3 && t != rep.x;
    rep.x = t;
    VM_ST128(rep, again ? HS_CAL - 128 * 64 + b * 64 + wv * 16 : OOB, 0);
    if (spins) __builtin_amdgcn_s_sleep(6);
  }
  VM_ST32(m.z, (wv == 0 && l == 0) ? HS_HI + b * 8 + 4 : OOB, 0);
  if (b >= 128) { PH(1) }
  const auto u64 = [](unsigned lo, int i, unsigned hi, int k) {
    return (unsigned)__builtin_amdgcn_readlane(lo, i) | (unsigned long long)(unsigned)__builtin_amdgcn_readlane(hi, k) << 32;
  };
  Go g;
  g.pool.sel = u64(m.y, 1, m.z, 1); g.pool.s3 = u64(m.w, 1, m.w, 2); g.pool.ysel = u64(m.y, 2, m.z, 2);
  g.pool.base = __builtin_amdgcn_readfirstlane(pool_base(ws));
  g.rot = __builtin_amdgcn_readlane(m.w, 0) & 7;
  return g;
}
#define LDSTAMP(n)
#define LDX
#define LDC
__device__ __forceinline__ v4u load_rows(v32u (&w)[10], int kind, const u16* A, const u16* B, const u16* x, int h, int wv, unsigned slot, unsigned char* ws, int b, bool first) {
  unsigned tr;
  const int l = lane_id(), i = l & 15, g = l >> 4;
  const v4u ra = make_rs(A, 640u * 8192u), rb = make_rs(B, 640u * 8192u), rs = make_rs(ws, (unsigned)OFF_END), rx = make_rs(x, S_ * D_ * 2);
  v8u ad, ex;
  const bool pj = kind == 1 || kind == 2;
  const int wr = 4 * wv + g, rr = 4 * (pj ? g >> 1 : g) + (i >> 2), gt = pj ? g & 1 : 0;
  ad[0] = wr * 8192 + h * 256 + i * 16;
  ad[1] = (h * 128 + wr) * 1280 + i * 16;
  ad[2] = slot + rr * 256 + ((2 * wv + gt) ^ (rr & 7)) * 32 + (i & 3) * 8;
  ad[3] = pj ? ad[2] + 2048u : ad[2] ^ 32u;
  ad[4] = slot + wr * 256 + ((i >> 1) ^ (wr & 7)) * 32 + (i & 1) * 16;
  ad[5] = slot + wr * 328 + ((i >> 2) * 2 + (i & 1)) * 8 + ((i >> 1) & 1) * 4;
  ad[6] = slot + (kind == 3 ? (16 * (g & 1) + 4 * (g >> 1) + ((i >> 2) & 1)) * 328 + (i >> 3) * 8 : rr * 328 + gt * 8) + (i & 3) * 10560 + wv * 80;
  ad[7] = ad[4] + 65536u;
  const unsigned im = lds_base() + PJ_X + (wv >> 1) * PJ_BUF;
  const bool hi = first && wv == 0;
  ex[0] = go_off(b);
  ex[1] = hi ? HS_GO + b * 16 : OOB;
  ex[2] = hi && l == 0 ? HS_HI + b * 8 : OOB;
  ex[3] = lds_base() + L_HS + (hi ? 0 : 4);
  ex[4] = (wv * 16 + (l >> 3)) * 1280 + (l & 7) * 16;
  ex[5] = im + ((wv & 1) * KT + ((l >> 2) & 1)) * 1024 + ((l & 1) * 32 + (l >> 3)) * 16 + (l & 2) * 4;
  ex[6] = im + 40960 + ((wv & 1) * 16 + (l >> 3)) * 48 + min(l & 7, 5) * 8;
  ex[7] = (l & 7) ? 0u : ~0u;
  v4u m;
#define LD_OPS                                                                                                                                       \
  : "={a[0:31]}"(w[0]), "={a[32:63]}"(w[1]), "={a[64:95]}"(w[2]), "={a[96:127]}"(w[3]), "={a[128:159]}"(w[4]), "={a[160:191]}"(w[5]),                \
    "={a[192:223]}"(w[6]), "={a[224:255]}"(w[7]), "={v[192:223]}"(w[8]), "={v[224:255]}"(w[9]), "={v[28:31]}"(m) LDX                                 \
  : "{v[16:23]}"(ad), "{v[112:119]}"(ex), [ra] "s"(ra), [rb] "s"(rb), [rw] "s"(rs), [rx] "s"(rx), [p0] "s"(wv < 3 ? 3 * wv : 8)                      \
  : LDP_CLOB LDC, "scc", "memory"
  if (kind == 0) asm volatile(LDP_ASM LD_OPS);
  else if (kind == 1) asm volatile(LDK_ASM LD_OPS);
  else if (kind == 2) asm volatile(LDV_ASM LD_OPS);
  else asm volatile(LDY_ASM LD_OPS);
  return m;
}
__device__ __forceinline__ v4u prep_jobs(int wv, unsigned char* ws, rsrc_t rs, const u16* __restrict__ x, const u16* __restrict__ mem0, int j) {
  const int lane = lane_id(), g = lane >> 4, r = lane & 15, tid = wv * 64 + lane, pb = pool_base(ws);
  const int xc = (int)xcc_id(), u = (j >> 3) * 4 + (xc & 3), page = xc < 4 ? u >> 2 : 16 + u % NYP;
  const int cb = pb + page * PAGE + M_CAL + (xc < 4 ? wv * REG + xc * 512 : 3 * REG - u / NYP * WIN + wv * 512);
  stc(rs, lane < 32 ? cb + (lane >> 3) * 8192 + (lane & 7) * 16 : OOB, v4u{1u, 2u, 3u, 4u});
  const unsigned char* p = ws + __builtin_amdgcn_readfirstlane(cb);
  unsigned t0, t1, t2, t3, t4;
  asm volatile("s_load_dword %0, %4, 0x80\n\ts_load_dword %1, %4, 0x2080\n\ts_load_dword %2, %4, 0x4080\n\ts_load_dword %3, %4, 0x6080"
               : "={s40}"(t0), "={s41}"(t1), "={s42}"(t2), "={s43}"(t3) : "s"(p) : "memory");
  const unsigned tg = hello_load(rs, 128 + j);
  v2u c[5][2];
  unsigned t[4];
#pragma unroll
  for (int q = 0; q < 5; ++q) {
    const u16* xr = x + (size_t)(j * 16 + r) * D_ + 32 * (5 * wv + q) + 4 * g;
    c[q][0] = *(const v2u*)xr; c[q][1] = *(const v2u*)(xr + 16);
  }
#pragma unroll
  for (int k = 0; k < 4; ++k) t[k] = *(const unsigned*)(mem0 + (size_t)((tid >> 1) & 7) * D_ + colmap((min(j >> 3, 4) * 8 + ((tid >> 4) & 7)) * 2 + (tid & 1), 2 * k));
  __builtin_amdgcn_sched_barrier(0);
  const v4u ones = {0xFFFFFFFFu, 0xFFFFFFFFu, 0xFFFFFFFFu, 0xFFFFFFFFu};
  const int o = (j * 256 + tid) * 16;
  const bool cal = (o & 0x79800) == 0x79800;
  stc(rs, (int)OFF_HO + o, ones);
  stc(rs, o + REG < (int)HO_FIRST ? (int)OFF_HO + REG + o : OOB, ones);
  stc(rs, (cal || (o >= W_MEM && o < W_MISC)) ? OOB : pb + (NSEG - 1) * REG + o, ones);
  stc(rs, cal ? OOB : pb + o, ones);
  stc(rs, pb + P_Y + o, ones);
  stc(rs, o < 8 * 16384 ? pb + P_Y + REG + o : OOB, ones);
  hello(wv, rs, 128 + j, tg);
  asm volatile("" ::"v"(tg));
  __builtin_amdgcn_sched_barrier(0);
#define CAL_T(r, o) "s_load_dword " r ", %5, " o "\n\ts_waitcnt lgkmcnt(0)\n\ts_memtime s[46:47]\n\ts_waitcnt lgkmcnt(0)\n\ts_mov_b32 " r ", s46\n\t"
  asm volatile(CAL_T("%0", "0x80") CAL_T("%1", "0") CAL_T("%2", "0x2000") CAL_T("%3", "0x4000") CAL_T("%4", "0x6000")
               : "+{s40}"(t0), "+{s41}"(t1), "+{s42}"(t2), "+{s43}"(t3), "={s44}"(t4) : "s"(p) : "s46", "s47", "memory");
  PH(16)
  __builtin_amdgcn_sched_barrier(0);
  f32x2 st = {0.f, 0.f};
#pragma unroll
  for (int q = 0; q < 5; ++q) {
    const v4u f = v4u{c[q][0].x, c[q][0].y, c[q][1].x, c[q][1].y};
    stc(rs, (int)OFF_XR + ((j * KT + 5 * wv + q) * 64 + lane) * 16, f);
    st += frag_stat(f);
  }
  stc(rs, j < 40 ? pb + (NSEG - 1) * REG + W_MEM + o : OOB, v4u{nosent(t[0]), nosent(t[1]), nosent(t[2]), nosent(t[3])});
  st = rows_sum(st);
  __builtin_amdgcn_raw_buffer_store_b64(__builtin_bit_cast(v2u, g ? f32x2{0.f, 0.f} : st), rs,
                                        (g == 0 || (g == 1 && wv == 0)) ? (int)OFF_ST + ((j * 16 + r) * 6 + (g ? 4 : wv)) * 8 : OOB, 0, AUX_SC1);
  const unsigned ask = __builtin_amdgcn_raw_buffer_load_b32(rs, HS_ASK + j * 16 + wv * 4, 0, AUX_SC1);
  const unsigned b0[4] = {min(t1 - t0, 9999u), min(t2 - t1, 9999u), min(t3 - t2, 9999u), min(t4 - t3, 9999u)}, sh = ((xc ^ wv) & 1) * 16;
  const v4u rep = {ask, (unsigned)page, b0[0] << sh | b0[2] << (16 - sh), b0[1] << sh | b0[3] << (16 - sh)};
  asm volatile("s_waitcnt vmcnt(0)" ::: "memory");
  stc(rs, lane == 0 ? HS_CAL + j * 64 + wv * 16 : OOB, rep);
  LDSBAR();
  PH(18)
  return rep;
}
__device__ __forceinline__ int wave_isum(int v) {
  asm volatile("s_nop 1\n\tv_add_u32_dpp %0, %0, %0 quad_perm:[1,0,3,2] row_mask:0xf bank_mask:0xf\n\t"
               "s_nop 1\n\tv_add_u32_dpp %0, %0, %0 quad_perm:[2,3,0,1] row_mask:0xf bank_mask:0xf\n\t"
               "s_nop 1\n\tv_add_u32_dpp %0, %0, %0 row_half_mirror row_mask:0xf bank_mask:0xf\n\t"
               "s_nop 1\n\tv_add_u32_dpp %0, %0, %0 row_mirror row_mask:0xf bank_mask:0xf\n\ts_nop 0"
               : "+v"(v));
  return (__builtin_amdgcn_readlane(v, 0) + __builtin_amdgcn_readlane(v, 16)) + (__builtin_amdgcn_readlane(v, 32) + __builtin_amdgcn_readlane(v, 48));
}
__device__ __forceinline__ Go lead(int wv, unsigned char* ws, const u16* x, const u16* mem0, int rot) {
  const v4u rs = make_rs(ws, (unsigned)OFF_END);
  const int t = wv * 64 + lane_id();
  int* acc = (int*)(dyn_lds + L_HS + 128);
#define VM_LD64(dst, off) asm volatile("buffer_load_dwordx2 %0, %1, %2, 0 offen sc1" : "=v"(dst) : "v"(off), "s"(rs) : "memory")
  v4u c0, c1;
  v2u hi;
  unsigned old, n = 0;
  VM_LD128(c0, HS_CAL + t * 32, 0, 0);
  VM_LD128(c1, HS_CAL + t * 32, 0, 16);
  VM_LD64(hi, HS_HI + t * 8);
  VM_LD32(old, HS_PAY + (t & 63) * 16, 0);
  asm volatile("s_waitcnt vmcnt(0)" : "+v"(c0), "+v"(c1), "+v"(hi), "+v"(old)::"memory");
  const unsigned r0 = c0.x + 1u, r1 = c1.x + 1u, q = hi.y + 1u;
  __builtin_amdgcn_raw_buffer_store_b64(v2u{r0, r1}, ws_rsrc(ws), HS_ASK + t * 8, 0, AUX_SC1);
  while (__any(old == n)) ++n;
  if (t < 192) acc[t] = 9999;
  prep_jobs(wv, ws, ws_rsrc(ws), x, mem0, 127);
  for (unsigned spins = 0; spins < (1u << 18); ++spins) {
    VM_LD128(c0, HS_CAL + t * 32, 0, 0);
    VM_LD128(c1, HS_CAL + t * 32, 0, 16);
    VM_LD64(hi, HS_HI + t * 8);
    asm volatile("s_waitcnt vmcnt(0)" : "+v"(c0), "+v"(c1), "+v"(hi)::"memory");
    if (!__any(t < 254 && (c0.x != r0 || c1.x != r1))) break;
    __builtin_amdgcn_s_sleep(2);
  }
  if (t < 254) {
    int *a0 = acc + min(c0.y, 47u) * 4, *a1 = acc + min(c1.y, 47u) * 4;
    atomicMin(a0, c0.z & 0xFFFF); atomicMin(a0 + 1, c0.z >> 16); atomicMin(a0 + 2, c0.w & 0xFFFF); atomicMin(a0 + 3, c0.w >> 16);
    atomicMin(a1, c1.z & 0xFFFF); atomicMin(a1 + 1, c1.z >> 16); atomicMin(a1 + 2, c1.w & 0xFFFF); atomicMin(a1 + 3, c1.w >> 16);
  }
  __syncthreads();
  PH(19)
  const int s = lane_id(), fx = (s & 4) ? -1 : 1, fy = ((s * 43 >> 7) & 1) ? -1 : 1;
  const v4i u = ((const v4i*)acc)[s >> 2], v = ((const v4i*)acc)[16 + (s * 43 >> 7)];
  int ax = min(u.x, u.y) - min(u.z, u.w), yx = min(v.z, v.w) - min(v.x, v.y);
  const int n0 = ax > 0 ? u.z : u.x, n1 = ax > 0 ? u.w : u.y, lu = min(n0, n1), lv = min(min(v.x, v.y), min(v.z, v.w)), near = 110 - wave_imax(-min(lu, lv));
  const int sa = (lu < near && abs(ax) > 60) ? (ax > 0 ? fx : -fx) : 0, sy = (lv < near && abs(yx) > 60) ? (yx > 0 ? fy : -fy) : 0;
  const int g = wave_isum(sa + sy), m = wave_isum(abs(sa) + abs(sy));
  const bool with = sa * g > 0;
  const int bs = wave_imax(with ? -n1 : -9999) - wave_imax(with ? -n0 : -9999);
  int b = n0 - n1;
  if (abs(g) * 8 > m * 7) {
    if (sa * g >= 0) { ax = g * fx; b = bs; }
    if (sy == 0) yx = g * fy;
  }
  Go go;
  go.pool.sel = __ballot(ax > 0);
  go.pool.s3 = __ballot(b > 0) ^ 0xAAAAAAAAAAAAAAAAull;
  go.pool.ysel = __ballot(yx > 0);
  go.pool.base = __builtin_amdgcn_readfirstlane(pool_base(ws));
  go.rot = rot;
  PH(20)
  {
    const unsigned long long v = (s & 1) ? go.pool.ysel : go.pool.sel;
    VM_ST128((v4u{n, (unsigned)v, (unsigned)(v >> 32), (unsigned)(go.pool.s3 >> (s & 1) * 32)}), wv == 0 ? HS_PAY + s * 16 : OOB, 0);
  }
  for (unsigned spins = 0; spins < (1u << 18); ++spins) {
    VM_ST128((v4u{hi.x, n, q, (unsigned)rot}), HS_GO + t * 16, 0);
    const unsigned sent = hi.x;
    do {
      __builtin_amdgcn_s_sleep(8);
      VM_LD64(hi, HS_HI + t * 8);
      VM_WAIT1(0, hi);
    } while (!__any(hi.x != sent) && __any(t < 255 && hi.y != q) && ++spins < (1u << 18));
    if (!__any(t < 255 && hi.y != q)) break;
  }
  return go;
}
__device__ __forceinline__ void late_jobs(int wv_, unsigned char* ws, rsrc_t rs, const u16* __restrict__ wr, const u16* __restrict__ wi, int j) {
  const int lane = lane_id(), wv = wv_, g = lane >> 4, r = lane & 15;
  unsigned* t32 = (unsigned*)dyn_lds;
  for (int o = (int)HO_FIRST + (j * 256 + wv * 64 + lane) * 16; o < 2 * (int)HO_LAYER; o += NLATE * 4096)
    stc(rs, (int)OFF_HO + o, v4u{0xFFFFFFFFu, 0xFFFFFFFFu, 0xFFFFFFFFu, 0xFFFFFFFFu});
#pragma unroll 1
  for (int fi = j * 4 + wv; fi < 20 * NFK; fi += NLATE * 4) {
    v4u v;
#pragma unroll
    for (int q = 0; q < 4; ++q) {
      const int e = fi % NFK, d = 16 * (fi / NFK) + r, f = 4 * (32 * (e >> 2) + 8 * q + 2 * (e & 2) + g) + 2 * (e & 1);
      const float sc = f ? 2.f : 1.f;
      v[q] = pk(cos640(dft_idx(f, d)) * sc, cos640(dft_idx(f + 1, d)) * sc);
    }
    stc(rs, (int)OFF_INV + (fi * 64 + lane) * 16, v);
  }
#pragma unroll 1
  for (int b = j; b < 32 * 6; b += NLATE) {
    const int m0 = (b & 31) * 64, k0 = (b >> 5) * 64, n0 = (b & 24) * 2 + (b & 7) * 256;
#pragma unroll
    for (int q = 0; q < 16; ++q) {
      const size_t i = (size_t)(n0 + q + 64 * wv) * 321 + min(k0 + lane, 320);
      t32[(q * 4 + wv) * 65 + lane] = (unsigned)wr[i] | ((unsigned)wi[i] << 16);
    }
    __syncthreads();
#pragma unroll
    for (int q = 0; q < 16; ++q) {
      const int k = q * 4 + wv;
      st32(rs, k0 + k < 321 ? (int)OFF_WT + ((k0 + k) * S_ + m0 + lane) * 4 : OOB, t32[lane * 65 + k]);
    }
    __syncthreads();
  }
  dep_signal(wv_, ws, cnt_off(0, CNT_IDFT, 0));
}
__device__ __forceinline__ void late_wait(int wv_, unsigned char* ws) {
  if (TIDX == 0) {
    for (unsigned spins = 0; spins < (1u << 17); ++spins) {
      if (__hip_atomic_load((gu32*)(ws + cnt_off(0, CNT_IDFT, 0)), __ATOMIC_RELAXED, __HIP_MEMORY_SCOPE_AGENT) == NLATE - 1) break;
      __builtin_amdgcn_s_sleep(16);
    }
  }
  __syncthreads();
}
#define TROB (24 * 256 + 16)
#define TC2 30
#define TVIS 32
#define TDLT 170
#define TM1 88
#define SPARE_US 200
#define SPARE_NAP 127
#define TM2 60
__global__ void __launch_bounds__(256, 1) k_main(unsigned char* ws, const u16* x, const u16* mem0, const u16* Wq, const u16* Wk, const u16* Wv, const u16* Wo,
                                                const u16* wr, const u16* wi, u16* out, long nlayer
                                                ) {
  v4u* smem = (v4u*)dyn_lds;
  const int wv_ = __builtin_amdgcn_readfirstlane(threadIdx.x >> 6);
  const RecTune tn = {TM1, TM2, 4, TROB, TC2, TVIS, TDLT, 1};
  unsigned long long* dbg = nullptr;
#define RSTAMP_(i)
  const rsrc_t rs = ws_rsrc(ws);
  PH(0)
  const int b = blockIdx.x, tt = b >> 3;
  const unsigned t00 = rtc();
  int rot = (int)(xcc_id() - b) & 7, lpl, ljb = -1;
  if (tt < 16) {
    v32u w[10];
    Go go;
    const unsigned lds0 = lds_base();
#pragma unroll 1
    for (int it = 0;; ++it) {
      const int p = (b + rot) & 7, h = (p & 3) * 8 + (tt & 7);
      const bool v = (tt & 8) != 0;
      const v4u m = load_rows(w, p < 4 ? (v ? 3 : 0) : (v ? 2 : 1), v ? Wv : Wq, v ? Wo : Wk, x, h, wv_, lds0 + (p < 4 ? 0 : PJ_X + 2 * PJ_BUF), ws, b, it == 0);
      PH(21 + (p >> 2))
      go = wait_go(wv_, ws, b, m, m);
      if (go.rot == rot || it) break;
      rot = go.rot;
    }
    Pool pool = go.pool;
    const int pl = (b + rot) & 7, role = pl < 4 ? (tt & 8) * 4 + pl * 8 + (tt & 7) : pl * 32 + tt;
    const int hd = (pl - 4) * 8 + (tt & 7);
#pragma unroll 1
    for (int layer = 0; layer < nlayer; ++layer) {
      int ro = role, xs = pl, wq = wv_, h2 = hd | (tt & 8) << 8;
      asm volatile("" : "+s"(ro), "+s"(xs), "+s"(wq), "+s"(h2), "+s"(ws), "+s"(pool.base));
      if (layer > 0) {
        const int jb = ro < 64 ? ro + 57 : (xs - 4) * 16 + (ro & 31);
        late_wait(wq, ws);
        if (ro < 64 || jb < 57) {
          job_col(wq, ws, ws_rsrc(ws), lds_base(), jb, fan_off(0, 1, 0), fan_off(1, 0, 0), false);
          __syncthreads();
          RSTAMP_(5)
          PH(7)
        } else fan_wait<64, 20>(wq, ws, fan_off(0, 1, 0), jb);
      }
      if (ro < 32) { rec_p1(wq, w, ws, pool, layer, ro, xs, tn, dbg); PH(4 + layer * 6) }
      else if (ro < 64) rec_pv(wq, w, ws, pool, layer, ro - 32, xs, tn, dbg);
      else {
        if (h2 >> 8) job_proj<true>(wq, w, ws, pool, layer, h2 & 31);
        else job_proj<false>(wq, w, ws, pool, layer, h2 & 31);
        RSTAMP_(2)
      }
    }
    lpl = pl;
    ljb = role < 64 ? role + 57 : (pl - 4) * 16 + (role & 31);
    if (role >= 64 && ljb >= 57) ljb = -1;
  } else {
  const Go go = b == 255 ? lead(wv_, ws, x, mem0, rot) : wait_go(wv_, ws, b, prep_jobs(wv_, ws, rs, x, mem0, b - 128), v4u{0u, 0u, 0u, 0u});
  Pool pool = go.pool;
  const int pl = (b + go.rot) & 7;
  const int role = pl < 4 ? (tt < 26 ? 64 + pl * 10 + tt - 16 : 104 + pl * 6 + tt - 26) : pl * 32 + tt;
  if (pl < 4 || tt < 27) late_jobs(wv_, ws, rs, wr, wi, pl < 4 ? 44 + pl * 16 + tt - 16 : (pl - 4) * 11 + tt - 16);
  PH(23)
  lpl = pl;
  if (role >= 104 && role < 128) {
    while (rtc() - t00 < 100u * SPARE_US) __builtin_amdgcn_s_sleep(127);
    late_wait(wv_, ws);
    prog_prewait(wv_, ws, kqf_off((int)nlayer - 1, NSEG - 1, ((pl + 1) & 3) * 8 + tt - 26));
    __syncthreads();
    __builtin_amdgcn_s_sleep(SPARE_NAP);
  } else if (role >= 64 && role < 104) {
#pragma unroll 1
    for (int layer = 0; layer < nlayer; ++layer) {
      if (layer > 0) {
        late_wait(wv_, ws);
        job_col(wv_, ws, rs, lds_base(), role + 57, fan_off(0, 1, 0), fan_off(1, 0, 0), false);
        __syncthreads();
        PH(7)
      }
      ((unsigned*)(dyn_lds + L_T))[lane_id() & 15] = 0u;
      __syncthreads();
      if (wv_ == 3) rec_fin(ws, pool, layer, role - 64, pl, tn, dbg);
      else if (wv_ == 0) rec_sched(ws, pool, layer, role - 64, pl, tn, dbg);
      __syncthreads();
    }
    ljb = role + 57;
  } else {
#pragma unroll 1
  for (int layer = 0; layer < nlayer; ++layer) {
    int wq = wv_;
    asm volatile("" : "+s"(wq));
    if (layer > 0) late_wait(wq, ws);
    if (layer > 0) job_idft(wq, ws, rs, smem, ((tt - 16) >> 1) * 4 + pl - 4, tt & 1, dbg + 60000 + role * 8);
    RSTAMP_(2)
    __syncthreads();
    if (tt >= 27) {
      job_dft(wq, ws, layer, tt - 27, pl - 4);
      fan_signal<64>(wq, ws, fan_off(0, 1, 0), layer * 20 + (pl - 4) * 5 + tt - 27);
      if (layer) { PH(14) }
    } else {
#pragma unroll 1
      for (int j = (tt - 16) * 4 + pl - 4; j < 640 - NFAST * 5; j += NOUT) {
        const int u = j / 5 - NFAST, s = u < 0 ? j / 5 : NFAST + (u >> 1);
        flags32_wait(wq, ws, kqf_off(layer, s, 32));
        job_out(wq, ws, rs, pool.ysel, layer, s, j % 5, u < 0 ? -1 : u & 1);
      }
    }
    RSTAMP_(3)
    PH((tt >= 27 ? 6 : 5) + layer * 6)
  }
  }
  }
  int wq = wv_, tl = tt;
  asm volatile("" : "+s"(wq), "+s"(ljb), "+s"(lpl), "+s"(tl), "+s"(ws));
  job_last(wq, ws, out, ljb, lpl * 16 + (tl >> 1), tl & 1);
}

extern "C" void kernel_launch(void* const* d_in, const int* in_sizes, int n_in, void* d_out, int out_size, void* d_ws, size_t ws_size,
                              hipStream_t stream) {
  const u16* x = (const u16*)d_in[0];
  const u16* mem0 = (const u16*)d_in[1];
  const u16* Wq = (const u16*)d_in[2];
  const u16* Wk = (const u16*)d_in[3];
  const u16* Wv = (const u16*)d_in[4];
  const u16* Wo = (const u16*)d_in[5];
  const u16* wr = (const u16*)d_in[8];
  const u16* wi = (const u16*)d_in[9];
  unsigned char* ws = (unsigned char*)d_ws;
  u16* out = (u16*)d_out;
  (void)hipFuncSetAttribute((const void*)k_main, hipFuncAttributeMaxDynamicSharedMemorySize, MAIN_LDS);
  k_main<<<256, 256, MAIN_LDS, stream>>>(ws, x, mem0, Wq, Wk, Wv, Wo, wr, wi, out, 2);
}
```

```cpp
#include <hip/hip_runtime.h>
#include <hip/hip_bf16.h>
#include <stdint.h>

typedef unsigned v4u __attribute__((ext_vector_type(4)));
typedef unsigned v2u __attribute__((ext_vector_type(2)));
typedef float f32x4 __attribute__((ext_vector_type(4)));
typedef float f32x2 __attribute__((ext_vector_type(2)));
typedef __bf16 bf16x8 __attribute__((ext_vector_type(8)));
typedef __bf16 bf16x2 __attribute__((ext_vector_type(2)));
typedef unsigned short u16;

constexpr int S_ = 2048, D_ = 640, NSEG = 64, KT = 20, RT = 128;
constexpr int NFT = 40;
constexpr int NFK = 20;
constexpr int NCOL = 161;
constexpr int WIN = 32768, REG = 16 * WIN;
constexpr int W_MEM = 10 * WIN, W_MISC = 15 * WIN;
__host__ __device__ constexpr int copy_off(int c) { return (c & 3) * 2048 + (c >> 2) * 16384; }
constexpr int M_STAMP = W_MISC + copy_off(4), M_TM = M_STAMP + 40 * 64, M_CAL = W_MISC + copy_off(3);
constexpr int PAGE = 4 * REG, NYP = 22, P_Y = NSEG * REG;
constexpr size_t SZ_POOL = (size_t)P_Y + (size_t)(NYP + 1) * PAGE;

constexpr size_t OFF_CNT = 0;
constexpr int CNT_IDFT = 0, CNT_OUT = 1;
constexpr size_t OFF_PROG = OFF_CNT + (size_t)3 * 32 * 128;
constexpr size_t OFF_KQF = OFF_PROG + (size_t)NSEG * 4096;
#define FNAP 6
#define FSP 16
#define FNG 32
constexpr int FAN_G = (161 * FSP + 127) / 128 * 128 > 3840 ? (161 * FSP + 127) / 128 * 128 : 3840;
constexpr size_t OFF_FAN = OFF_KQF + (size_t)NSEG * 4096;
constexpr size_t HO_LAYER = OFF_FAN + (size_t)2 * FNG * FAN_G;
constexpr size_t OFF_HO = SZ_POOL;
constexpr size_t HO_FIRST = OFF_FAN + (size_t)FNG * FAN_G;
constexpr size_t OFF_HS = OFF_HO + 2 * HO_LAYER;
constexpr int HS_GO = (int)OFF_HS, HS_PAY = HS_GO + 4096, HS_ASK = HS_PAY + 1024, HS_HI = HS_ASK + 2048, HS_CAL = HS_HI + 2048;
__host__ __device__ constexpr int prog_off(int layer, int rb, int slot) { return (int)(OFF_HO + layer * HO_LAYER + OFF_PROG) + (rb * 64 + slot) * 64; }
__host__ __device__ constexpr int kqf_off(int layer, int sg, int h) { return (int)(OFF_HO + layer * HO_LAYER + OFF_KQF) + (sg * 64 + h) * 64; }
__host__ __device__ constexpr int cnt_off(int layer, int kind, int rb) { return (int)(OFF_HO + layer * HO_LAYER + OFF_CNT) + (kind * 32 + rb) * 128; }
__host__ __device__ constexpr int fan_off(int layer, int set, int i) { return (int)(OFF_HO + layer * HO_LAYER + OFF_FAN) + ((set ^ layer ^ 1) * FNG + i) * FAN_G; }
constexpr size_t OFF_XR = (OFF_HS + 65536 + 4095) / 4096 * 4096;
constexpr size_t OFF_ST = OFF_XR + (size_t)RT * KT * 1024;
constexpr size_t OFF_QF = OFF_ST + (size_t)S_ * 48;
constexpr size_t OFF_KF = OFF_QF + (size_t)32 * RT * 4 * 1024;
constexpr size_t OFF_UF = OFF_KF + (size_t)32 * RT * 4 * 1024;
constexpr size_t OFF_PXF = OFF_UF + (size_t)NSEG * 80 * 16 * 1024;
constexpr size_t OFF_PXM = OFF_PXF + (size_t)NSEG * 32 * 2 * 1024;
constexpr size_t OFF_INV = OFF_PXM + (size_t)NSEG * 8 * 2 * 1024;
constexpr size_t OFF_X1T = OFF_INV + (size_t)20 * NFK * 1024;
#define ZPAD 32
#define XPAD 32
constexpr int XS = S_ + XPAD, ZS = S_ + ZPAD;
constexpr size_t OFF_ZT = OFF_X1T + (size_t)160 * XS * 8;
constexpr size_t OFF_WT = OFF_ZT + (size_t)161 * ZS * 8;
constexpr size_t OFF_DBG = OFF_WT + (size_t)321 * S_ * 4;
constexpr size_t OFF_END = OFF_DBG + (size_t)1048576;
static_assert(OFF_END <= ((size_t)256 << 20), "workspace too large");

constexpr int AUX_SC1 = 16;
#define PH(k)
#define PHV(k, v)
#define PHT(n)

__device__ __forceinline__ f32x4 mfma(v4u a, v4u b, f32x4 c) {
  return __builtin_amdgcn_mfma_f32_16x16x32_bf16(__builtin_bit_cast(bf16x8, a), __builtin_bit_cast(bf16x8, b), c, 0, 0, 0);
}
__device__ __forceinline__ unsigned pk(float a, float b) {
  f32x2 v = {a, b};
  return __builtin_bit_cast(unsigned, __builtin_convertvector(v, bf16x2));
}
__device__ __forceinline__ float lo_f(unsigned u) { return __builtin_bit_cast(float, u << 16); }
__device__ __forceinline__ float hi_f(unsigned u) { return __builtin_bit_cast(float, u & 0xFFFF0000u); }
__device__ __forceinline__ float bfr(float x) { return lo_f(pk(x, x)); }
__device__ __forceinline__ v4u pk8(f32x4 a, f32x4 b) { return v4u{pk(a.x, a.y), pk(a.z, a.w), pk(b.x, b.y), pk(b.z, b.w)}; }
__device__ __forceinline__ int sigma(int g, int jj) { return 16 * (jj >> 2) + 4 * g + (jj & 3); }
__device__ __forceinline__ int colmap(int c8, int jj) {
  const int p = c8 >> 3, ga = (c8 >> 1) & 3, half = c8 & 1;
  return 32 * (2 * p + (ga >> 1)) + 16 * ((jj >> 1) & 1) + 4 * (2 * (ga & 1) + (jj >> 2)) + 2 * half + (jj & 1);
}
__device__ __forceinline__ int pool_base(const void* ws) { return (int)((0u - (unsigned)(uintptr_t)ws) & (unsigned)(PAGE - 1)); }
__device__ __forceinline__ unsigned xcc_id() { return (unsigned)__builtin_amdgcn_s_getreg(20 | (3 << 11)) & 7u; }
__device__ __forceinline__ float shx(float v, int m) { return __shfl_xor(v, m); }
__device__ __forceinline__ unsigned nosent(unsigned x) { return x < 0xFFFFFFFEu ? x : 0xFFFFFFFEu; }

__device__ __forceinline__ f32x2 frag_stat(v4u c) {
  const float v[8] = {lo_f(c.x), hi_f(c.x), lo_f(c.y), hi_f(c.y), lo_f(c.z), hi_f(c.z), lo_f(c.w), hi_f(c.w)};
  float q = 0.f;
#pragma unroll
  for (int j = 0; j < 8; ++j) q = __builtin_fmaf(v[j], v[j], q);
  return f32x2{((v[0] + v[1]) + (v[2] + v[3])) + ((v[4] + v[5]) + (v[6] + v[7])), q};
}
__device__ __forceinline__ f32x2 rows_sum(f32x2 a) {
  a.x += shx(a.x, 16); a.y += shx(a.y, 16); a.x += shx(a.x, 32); a.y += shx(a.y, 32);
  return a;
}

typedef int v4i __attribute__((ext_vector_type(4)));
__device__ __forceinline__ int y_region(unsigned long long sel, int s) { return (s * 80 + (s * 43 >> 7) * 16 + (int)((sel >> s) & 1ull)) * 8192; }

__device__ __forceinline__ int lane_id() {
  int l;
  asm volatile("v_mbcnt_lo_u32_b32 %0, -1, 0\n\tv_mbcnt_hi_u32_b32 %0, -1, %0" : "=v"(l));
  return l;
}
#define TIDX (wv_ * 64 + lane_id())
typedef __amdgpu_buffer_rsrc_t rsrc_t;
__device__ __forceinline__ rsrc_t ws_rsrc(unsigned char* ws) { return __builtin_amdgcn_make_buffer_rsrc(ws, 0, (int)OFF_END, 0x00020000); }
__device__ __forceinline__ v4u ldc(rsrc_t rs, int off) { return __builtin_amdgcn_raw_buffer_load_b128(rs, off, 0, AUX_SC1); }
__device__ __forceinline__ void stc(rsrc_t rs, int off, v4u v) { __builtin_amdgcn_raw_buffer_store_b128(v, rs, off, 0, AUX_SC1); }

extern __shared__ __attribute__((aligned(16))) unsigned char dyn_lds[];
__device__ __forceinline__ unsigned lds_base() { return (unsigned)(uintptr_t)(__attribute__((address_space(3))) unsigned char*)dyn_lds; }
__device__ __forceinline__ unsigned rtc() { return (unsigned)__builtin_amdgcn_s_memrealtime(); }
__device__ __forceinline__ int wave_imax(int v) {
  asm volatile("s_nop 1\n\tv_max_i32_dpp %0, %0, %0 quad_perm:[1,0,3,2] row_mask:0xf bank_mask:0xf\n\t"
               "s_nop 1\n\tv_max_i32_dpp %0, %0, %0 quad_perm:[2,3,0,1] row_mask:0xf bank_mask:0xf\n\t"
               "s_nop 1\n\tv_max_i32_dpp %0, %0, %0 row_half_mirror row_mask:0xf bank_mask:0xf\n\t"
               "s_nop 1\n\tv_max_i32_dpp %0, %0, %0 row_mirror row_mask:0xf bank_mask:0xf\n\ts_nop 0"
               : "+v"(v));
  const int a = __builtin_amdgcn_readlane(v, 0), b = __builtin_amdgcn_readlane(v, 16), c = __builtin_amdgcn_readlane(v, 32),
            d = __builtin_amdgcn_readlane(v, 48);
  return max(max(a, b), max(c, d));
}

#define PWN 40
#define PWL 52
typedef __attribute__((address_space(1))) unsigned gu32;
__device__ __forceinline__ void prog_prewait(int wv_, unsigned char* ws, int off) {
  if (TIDX == 0) {
    gu32* p = (gu32*)(ws + off);
    for (unsigned spins = 0; spins < (1u << 16); ++spins) {
      if (__hip_atomic_load(p, __ATOMIC_RELAXED, __HIP_MEMORY_SCOPE_AGENT) != 0xFFFFFFFFu) break;
      __builtin_amdgcn_s_sleep(48);
    }
  }
}
__device__ __forceinline__ void prog_wait(int wv_, unsigned char* ws, int off, bool lazy) {
  if (wv_ == 0) {
    const int l = lane_id();
    gu32* p0 = (gu32*)(ws + off + l * 64);
    for (unsigned spins = 0; spins < (1u << 17); ++spins) {
      const unsigned a = __hip_atomic_load(p0, __ATOMIC_RELAXED, __HIP_MEMORY_SCOPE_AGENT);
      if (!__any(a == 0xFFFFFFFFu)) break;
      __builtin_amdgcn_s_sleep(8);
      if (lazy) __builtin_amdgcn_s_sleep(PWN);
    }
  }
  __syncthreads();
}
__device__ __forceinline__ void flags32_wait(int wv_, unsigned char* ws, int off) {
  if (wv_ == 0) {
    const int l = lane_id();
    __attribute__((address_space(1))) unsigned long long* p0 = (__attribute__((address_space(1))) unsigned long long*)(ws + off + (l & 31) * 64 + (l >> 5) * 8);
    for (unsigned spins = 0; spins < (1u << 17); ++spins) {
      const unsigned long long v = __hip_atomic_load(p0, __ATOMIC_RELAXED, __HIP_MEMORY_SCOPE_AGENT);
      if (!__any(max((unsigned)v, (unsigned)(v >> 32)) == 0xFFFFFFFFu)) break;
      __builtin_amdgcn_s_sleep(16);
    }
  }
  __syncthreads();
}
__device__ __forceinline__ void dep_signal(int wv_, unsigned char* ws, int off, unsigned inc = 1u) {
  asm volatile("s_waitcnt vmcnt(0)" ::: "memory");
  __syncthreads();
  if (TIDX == 0) __hip_atomic_fetch_add((gu32*)(ws + off), inc, __ATOMIC_RELAXED, __HIP_MEMORY_SCOPE_AGENT);
}

template <int SP> __device__ __forceinline__ void fan_signal(int wv_, unsigned char* ws, int fan, int i) {
  asm volatile("s_waitcnt vmcnt(0)" ::: "memory");
  __syncthreads();
  const int l = lane_id();
  if (wv_ == 0 && l < FNG) __hip_atomic_store((gu32*)(ws + fan + l * FAN_G + i * SP), nosent(rtc()), __ATOMIC_RELAXED, __HIP_MEMORY_SCOPE_AGENT);
}
constexpr int L_WHEN = 90112;
template <int SP, int N> __device__ __forceinline__ void fan_wait(int wv_, unsigned char* ws, int fan, int salt) {
  if (wv_ * 64 < N) {
    gu32* p = (gu32*)(ws + fan + (((int)xcc_id() * (FNG / 8) + (salt & (FNG / 8 - 1))) * FAN_G + min(TIDX, N - 1) * SP));
    unsigned v;
    for (unsigned spins = 0; spins < (1u << 17); ++spins) {
      v = __hip_atomic_load(p, __ATOMIC_RELAXED, __HIP_MEMORY_SCOPE_AGENT);
      const unsigned long long m = __ballot(v == 0xFFFFFFFFu);
      if (m == 0) break;
      __builtin_amdgcn_s_sleep(N == NCOL ? 24 : FNAP);
      if (N == 20 && (m & 0x3FF)) __builtin_amdgcn_s_sleep(40);
    }
    if (N == 20) {
      const unsigned t = rtc();
      *(unsigned*)(dyn_lds + L_WHEN) = t + (unsigned)wave_imax((int)(v - t));
    }
  }
  __syncthreads();
}

typedef unsigned v8u __attribute__((ext_vector_type(8)));
typedef unsigned v16u __attribute__((ext_vector_type(16)));
typedef unsigned v32u __attribute__((ext_vector_type(32)));
__device__ __forceinline__ float cosi(int i) { return __builtin_amdgcn_cosf((float)i * (1.f / 2048)); }
__device__ __forceinline__ float sini(int i) { return __builtin_amdgcn_sinf((float)i * (1.f / 2048)); }
__device__ __forceinline__ void cis8(v32u& d, int x0, int dx, int d4, unsigned m) {
#pragma unroll
  for (int q = 0; q < 4; ++q) {
    const int x = x0 + dx * 2 * (q & 1) + d4 * (q >> 1);
    d[q] = pk(cosi(x), cosi(x + dx)); d[4 + q] = d[q] ^ m;
    d[8 + q] = pk(sini(x), sini(x + dx)); d[12 + q] = d[8 + q] ^ m;
  }
}
#define LDS_V4(a) (*(__attribute__((address_space(3))) v4u*)(uintptr_t)(a))
#define LDS_U32(a) (*(__attribute__((address_space(3))) unsigned*)(uintptr_t)(a))
#define LDS_V2(a) (*(__attribute__((address_space(3))) v2u*)(uintptr_t)(a))
#define COL_ASM \
   "s_waitcnt lgkmcnt(0)\n\t" "s_barrier\n\t" "ds_read_b64_tr_b16 v[136:137], v16 offset:0\n\t" "ds_read_b64_tr_b16 v[138:139], v16 offset:1056\n\t" \
   "ds_read_b64_tr_b16 v[140:141], v16 offset:8448\n\t" "ds_read_b64_tr_b16 v[142:143], v16 offset:9504\n\t" \
   "ds_read_b64_tr_b16 v[144:145], v16 offset:8\n\t" "ds_read_b64_tr_b16 v[146:147], v16 offset:1064\n\t" \
   "ds_read_b64_tr_b16 v[148:149], v16 offset:8456\n\t" "ds_read_b64_tr_b16 v[150:151], v16 offset:9512\n\t" "s_waitcnt lgkmcnt(4)\n\t" \
   "v_mfma_f32_16x16x32_bf16 v[160:163], v[136:139], v[32:35], 0\n\t" "v_mfma_f32_16x16x32_bf16 v[164:167], v[136:139], v[40:43], 0\n\t" \
   "v_mfma_f32_16x16x32_bf16 v[160:163], v[140:143], v[36:39], v[160:163]\n\t" \
   "v_mfma_f32_16x16x32_bf16 v[164:167], v[140:143], v[44:47], v[164:167]\n\t" "ds_read_b64_tr_b16 v[152:153], v16 offset:16\n\t" \
   "ds_read_b64_tr_b16 v[154:155], v16 offset:1072\n\t" "ds_read_b64_tr_b16 v[156:157], v16 offset:8464\n\t" \
   "ds_read_b64_tr_b16 v[158:159], v16 offset:9520\n\t" "s_nop 3\n\t" "s_waitcnt lgkmcnt(4)\n\t" \
   "v_mfma_f32_16x16x32_bf16 v[168:171], v[144:147], v[32:35], 0\n\t" "v_add_f32 v160, v160, v165\n\t" "v_sub_f32 v161, v161, v164\n\t" \
   "v_mfma_f32_16x16x32_bf16 v[172:175], v[144:147], v[40:43], 0\n\t" "v_add_f32 v162, v162, v167\n\t" "v_sub_f32 v163, v163, v166\n\t" \
   "v_mfma_f32_16x16x32_bf16 v[168:171], v[148:151], v[36:39], v[168:171]\n\t" "v_mul_f32 v24, v161, v49\n\t" "v_mul_f32 v25, v161, v48\n\t" \
   "v_mfma_f32_16x16x32_bf16 v[172:175], v[148:151], v[44:47], v[172:175]\n\t" "v_mul_f32 v26, v163, v49\n\t" "v_mul_f32 v27, v163, v48\n\t" \
   "v_fma_f32 v177, v160, v49, v25\n\t" "v_fma_f32 v176, v160, v48, -v24\n\t" "v_fma_f32 v179, v162, v49, v27\n\t" \
   "v_fma_f32 v178, v162, v48, -v26\n\t" "ds_read_b64_tr_b16 v[136:137], v16 offset:24\n\t" "ds_read_b64_tr_b16 v[138:139], v16 offset:1080\n\t" \
   "ds_read_b64_tr_b16 v[140:141], v16 offset:8472\n\t" "ds_read_b64_tr_b16 v[142:143], v16 offset:9528\n\t" "s_waitcnt lgkmcnt(4)\n\t" \
   "v_mfma_f32_16x16x32_bf16 v[160:163], v[152:155], v[32:35], 0\n\t" "v_add_f32 v168, v168, v173\n\t" "v_sub_f32 v169, v169, v172\n\t" \
   "v_mfma_f32_16x16x32_bf16 v[164:167], v[152:155], v[40:43], 0\n\t" "v_add_f32 v170, v170, v175\n\t" "v_sub_f32 v171, v171, v174\n\t" \
   "v_mfma_f32_16x16x32_bf16 v[160:163], v[156:159], v[36:39], v[160:163]\n\t" "v_mul_f32 v24, v169, v51\n\t" "v_mul_f32 v25, v169, v50\n\t" \
   "v_mfma_f32_16x16x32_bf16 v[164:167], v[156:159], v[44:47], v[164:167]\n\t" "v_mul_f32 v26, v171, v51\n\t" "v_mul_f32 v27, v171, v50\n\t" \
   "v_fma_f32 v181, v168, v51, v25\n\t" "v_fma_f32 v180, v168, v50, -v24\n\t" "v_fma_f32 v183, v170, v51, v27\n\t" \
   "v_fma_f32 v182, v170, v50, -v26\n\t" "v_cvt_pk_bf16_f32 v12, v177, v181\n\t" "v_cvt_pk_bf16_f32 v8, v176, v180\n\t" \
   "v_cvt_pk_bf16_f32 v188, v179, v183\n\t" "v_cvt_pk_bf16_f32 v184, v178, v182\n\t" "ds_read_b64_tr_b16 v[144:145], v16 offset:128\n\t" \
   "ds_read_b64_tr_b16 v[146:147], v16 offset:1184\n\t" "ds_read_b64_tr_b16 v[148:149], v16 offset:8576\n\t" \
   "ds_read_b64_tr_b16 v[150:151], v16 offset:9632\n\t" "s_waitcnt lgkmcnt(4)\n\t" "v_mfma_f32_16x16x32_bf16 v[168:171], v[136:139], v[32:35], 0\n\t" \
   "v_add_f32 v160, v160, v165\n\t" "v_sub_f32 v161, v161, v164\n\t" "v_mfma_f32_16x16x32_bf16 v[172:175], v[136:139], v[40:43], 0\n\t" \
   "v_add_f32 v162, v162, v167\n\t" "v_sub_f32 v163, v163, v166\n\t" "v_mfma_f32_16x16x32_bf16 v[168:171], v[140:143], v[36:39], v[168:171]\n\t" \
   "v_mul_f32 v24, v161, v53\n\t" "v_mul_f32 v25, v161, v52\n\t" "v_mfma_f32_16x16x32_bf16 v[172:175], v[140:143], v[44:47], v[172:175]\n\t" \
   "v_mul_f32 v26, v163, v53\n\t" "v_mul_f32 v27, v163, v52\n\t" "v_fma_f32 v177, v160, v53, v25\n\t" "v_fma_f32 v176, v160, v52, -v24\n\t" \
   "v_fma_f32 v179, v162, v53, v27\n\t" "v_fma_f32 v178, v162, v52, -v26\n\t" "ds_read_b64_tr_b16 v[152:153], v16 offset:136\n\t" \
   "ds_read_b64_tr_b16 v[154:155], v16 offset:1192\n\t" "ds_read_b64_tr_b16 v[156:157], v16 offset:8584\n\t" \
   "ds_read_b64_tr_b16 v[158:159], v16 offset:9640\n\t" "s_waitcnt lgkmcnt(4)\n\t" "v_mfma_f32_16x16x32_bf16 v[160:163], v[144:147], v[32:35], 0\n\t" \
   "v_add_f32 v168, v168, v173\n\t" "v_sub_f32 v169, v169, v172\n\t" "v_mfma_f32_16x16x32_bf16 v[164:167], v[144:147], v[40:43], 0\n\t" \
   "v_add_f32 v170, v170, v175\n\t" "v_sub_f32 v171, v171, v174\n\t" "v_mfma_f32_16x16x32_bf16 v[160:163], v[148:151], v[36:39], v[160:163]\n\t" \
   "v_mul_f32 v24, v169, v55\n\t" "v_mul_f32 v25, v169, v54\n\t" "v_mfma_f32_16x16x32_bf16 v[164:167], v[148:151], v[44:47], v[164:167]\n\t" \
   "v_mul_f32 v26, v171, v55\n\t" "v_mul_f32 v27, v171, v54\n\t" "v_fma_f32 v181, v168, v55, v25\n\t" "v_fma_f32 v180, v168, v54, -v24\n\t" \
   "v_fma_f32 v183, v170, v55, v27\n\t" "v_fma_f32 v182, v170, v54, -v26\n\t" "v_cvt_pk_bf16_f32 v13, v177, v181\n\t" \
   "v_cvt_pk_bf16_f32 v9, v176, v180\n\t" "v_cvt_pk_bf16_f32 v189, v179, v183\n\t" "v_cvt_pk_bf16_f32 v185, v178, v182\n\t" \
   "ds_read_b64_tr_b16 v[136:137], v16 offset:144\n\t" "ds_read_b64_tr_b16 v[138:139], v16 offset:1200\n\t" \
   "ds_read_b64_tr_b16 v[140:141], v16 offset:8592\n\t" "ds_read_b64_tr_b16 v[142:143], v16 offset:9648\n\t" "s_waitcnt lgkmcnt(4)\n\t" \
   "v_mfma_f32_16x16x32_bf16 v[168:171], v[152:155], v[32:35], 0\n\t" "v_add_f32 v160, v160, v165\n\t" "v_sub_f32 v161, v161, v164\n\t" \
   "v_mfma_f32_16x16x32_bf16 v[172:175], v[152:155], v[40:43], 0\n\t" "v_add_f32 v162, v162, v167\n\t" "v_sub_f32 v163, v163, v166\n\t" \
   "v_mfma_f32_16x16x32_bf16 v[168:171], v[156:159], v[36:39], v[168:171]\n\t" "v_mul_f32 v24, v161, v57\n\t" "v_mul_f32 v25, v161, v56\n\t" \
   "v_mfma_f32_16x16x32_bf16 v[172:175], v[156:159], v[44:47], v[172:175]\n\t" "v_mul_f32 v26, v163, v57\n\t" "v_mul_f32 v27, v163, v56\n\t" \
   "v_fma_f32 v177, v160, v57, v25\n\t" "v_fma_f32 v176, v160, v56, -v24\n\t" "v_fma_f32 v179, v162, v57, v27\n\t" \
   "v_fma_f32 v178, v162, v56, -v26\n\t" "ds_read_b64_tr_b16 v[144:145], v16 offset:152\n\t" "ds_read_b64_tr_b16 v[146:147], v16 offset:1208\n\t" \
   "ds_read_b64_tr_b16 v[148:149], v16 offset:8600\n\t" "ds_read_b64_tr_b16 v[150:151], v16 offset:9656\n\t" "s_waitcnt lgkmcnt(4)\n\t" \
   "v_mfma_f32_16x16x32_bf16 v[160:163], v[136:139], v[32:35], 0\n\t" "v_add_f32 v168, v168, v173\n\t" "v_sub_f32 v169, v169, v172\n\t" \
   "v_mfma_f32_16x16x32_bf16 v[164:167], v[136:139], v[40:43], 0\n\t" "v_add_f32 v170, v170, v175\n\t" "v_sub_f32 v171, v171, v174\n\t" \
   "v_mfma_f32_16x16x32_bf16 v[160:163], v[140:143], v[36:39], v[160:163]\n\t" "v_mul_f32 v24, v169, v59\n\t" "v_mul_f32 v25, v169, v58\n\t" \
   "v_mfma_f32_16x16x32_bf16 v[164:167], v[140:143], v[44:47], v[164:167]\n\t" "v_mul_f32 v26, v171, v59\n\t" "v_mul_f32 v27, v171, v58\n\t" \
   "v_fma_f32 v181, v168, v59, v25\n\t" "v_fma_f32 v180, v168, v58, -v24\n\t" "v_fma_f32 v183, v170, v59, v27\n\t" \
   "v_fma_f32 v182, v170, v58, -v26\n\t" "v_cvt_pk_bf16_f32 v14, v177, v181\n\t" "v_cvt_pk_bf16_f32 v10, v176, v180\n\t" \
   "v_cvt_pk_bf16_f32 v190, v179, v183\n\t" "v_cvt_pk_bf16_f32 v186, v178, v182\n\t" "s_nop 1\n\t" "s_waitcnt lgkmcnt(0)\n\t" \
   "v_mfma_f32_16x16x32_bf16 v[168:171], v[144:147], v[32:35], 0\n\t" "v_add_f32 v160, v160, v165\n\t" "v_sub_f32 v161, v161, v164\n\t" \
   "v_mfma_f32_16x16x32_bf16 v[172:175], v[144:147], v[40:43], 0\n\t" "v_add_f32 v162, v162, v167\n\t" "v_sub_f32 v163, v163, v166\n\t" \
   "v_mfma_f32_16x16x32_bf16 v[168:171], v[148:151], v[36:39], v[168:171]\n\t" "v_mul_f32 v24, v161, v61\n\t" "v_mul_f32 v25, v161, v60\n\t" \
   "v_mfma_f32_16x16x32_bf16 v[172:175], v[148:151], v[44:47], v[172:175]\n\t" "v_mul_f32 v26, v163, v61\n\t" "v_mul_f32 v27, v163, v60\n\t" \
   "v_fma_f32 v177, v160, v61, v25\n\t" "v_fma_f32 v176, v160, v60, -v24\n\t" "v_fma_f32 v179, v162, v61, v27\n\t" \
   "v_fma_f32 v178, v162, v60, -v26\n\t" "s_nop 1\n\t" "v_add_f32 v168, v168, v173\n\t" "v_sub_f32 v169, v169, v172\n\t" "v_mul_f32 v24, v169, v63\n\t" \
   "v_mul_f32 v25, v169, v62\n\t" "v_fma_f32 v181, v168, v63, v25\n\t" "v_fma_f32 v180, v168, v62, -v24\n\t" "v_cvt_pk_bf16_f32 v15, v177, v181\n\t" \
   "v_cvt_pk_bf16_f32 v11, v176, v180\n\t" "s_nop 1\n\t" "v_mfma_f32_16x16x32_bf16 v[160:163], v[64:67], v[8:11], 0\n\t" \
   "v_add_f32 v170, v170, v175\n\t" "v_sub_f32 v171, v171, v174\n\t" "v_mfma_f32_16x16x32_bf16 v[164:167], v[64:67], v[12:15], 0\n\t" \
   "v_mul_f32 v24, v171, v63\n\t" "v_mfma_f32_16x16x32_bf16 v[160:163], v[72:75], v[12:15], v[160:163]\n\t" "v_mul_f32 v25, v171, v62\n\t" \
   "v_mfma_f32_16x16x32_bf16 v[164:167], v[128:131], v[8:11], v[164:167]\n\t" "v_fma_f32 v183, v170, v63, v25\n\t" \
   "v_fma_f32 v182, v170, v62, -v24\n\t" "v_cvt_pk_bf16_f32 v191, v179, v183\n\t" "v_cvt_pk_bf16_f32 v187, v178, v182\n\t" "s_nop 4\n\t" \
   "v_mfma_f32_16x16x32_bf16 v[168:171], v[68:71], v[8:11], 0\n\t" "v_mul_f32 v24, v164, v97\n\t" "v_mul_f32 v25, v164, v96\n\t" \
   "v_mul_f32 v26, v165, v99\n\t" "v_mfma_f32_16x16x32_bf16 v[172:175], v[68:71], v[12:15], 0\n\t" "v_mul_f32 v27, v165, v98\n\t" \
   "v_mul_f32 v28, v166, v101\n\t" "v_mul_f32 v29, v166, v100\n\t" "v_mfma_f32_16x16x32_bf16 v[168:171], v[76:79], v[12:15], v[168:171]\n\t" \
   "v_mul_f32 v30, v167, v103\n\t" "v_mul_f32 v31, v167, v102\n\t" "v_fma_f32 v164, v160, v97, v25\n\t" \
   "v_mfma_f32_16x16x32_bf16 v[172:175], v[132:135], v[8:11], v[172:175]\n\t" "v_fma_f32 v160, v160, v96, -v24\n\t" \
   "v_fma_f32 v165, v161, v99, v27\n\t" "v_fma_f32 v161, v161, v98, -v26\n\t" "v_fma_f32 v166, v162, v101, v29\n\t" \
   "v_fma_f32 v162, v162, v100, -v28\n\t" "v_fma_f32 v167, v163, v103, v31\n\t" "v_fma_f32 v163, v163, v102, -v30\n\t" \
   "v_cvt_pk_bf16_f32 v140, v164, v165\n\t" "v_cvt_pk_bf16_f32 v136, v160, v161\n\t" "v_cvt_pk_bf16_f32 v141, v166, v167\n\t" \
   "v_cvt_pk_bf16_f32 v137, v162, v163\n\t" "v_mfma_f32_16x16x32_bf16 v[160:163], v[64:67], v[184:187], 0\n\t" "v_mul_f32 v24, v172, v105\n\t" \
   "v_mul_f32 v25, v172, v104\n\t" "v_mul_f32 v26, v173, v107\n\t" "v_mfma_f32_16x16x32_bf16 v[164:167], v[64:67], v[188:191], 0\n\t" \
   "v_mul_f32 v27, v173, v106\n\t" "v_mul_f32 v28, v174, v109\n\t" "v_mul_f32 v29, v174, v108\n\t" \
   "v_mfma_f32_16x16x32_bf16 v[160:163], v[72:75], v[188:191], v[160:163]\n\t" "v_mul_f32 v30, v175, v111\n\t" "v_mul_f32 v31, v175, v110\n\t" \
   "v_fma_f32 v172, v168, v105, v25\n\t" "v_mfma_f32_16x16x32_bf16 v[164:167], v[128:131], v[184:187], v[164:167]\n\t" \
   "v_fma_f32 v168, v168, v104, -v24\n\t" "v_fma_f32 v173, v169, v107, v27\n\t" "v_fma_f32 v169, v169, v106, -v26\n\t" \
   "v_fma_f32 v174, v170, v109, v29\n\t" "v_fma_f32 v170, v170, v108, -v28\n\t" "v_fma_f32 v175, v171, v111, v31\n\t" \
   "v_fma_f32 v171, v171, v110, -v30\n\t" "v_cvt_pk_bf16_f32 v142, v172, v173\n\t" "v_cvt_pk_bf16_f32 v138, v168, v169\n\t" \
   "v_cvt_pk_bf16_f32 v143, v174, v175\n\t" "v_cvt_pk_bf16_f32 v139, v170, v171\n\t" "v_mfma_f32_16x16x32_bf16 v[168:171], v[68:71], v[184:187], 0\n\t" \
   "v_mul_f32 v24, v164, v113\n\t" "v_mul_f32 v25, v164, v112\n\t" "v_mul_f32 v26, v165, v115\n\t" \
   "v_mfma_f32_16x16x32_bf16 v[172:175], v[68:71], v[188:191], 0\n\t" "v_mul_f32 v27, v165, v114\n\t" "v_mul_f32 v28, v166, v117\n\t" \
   "v_mul_f32 v29, v166, v116\n\t" "v_mfma_f32_16x16x32_bf16 v[168:171], v[76:79], v[188:191], v[168:171]\n\t" "v_mul_f32 v30, v167, v119\n\t" \
   "v_mul_f32 v31, v167, v118\n\t" "v_fma_f32 v164, v160, v113, v25\n\t" "v_mfma_f32_16x16x32_bf16 v[172:175], v[132:135], v[184:187], v[172:175]\n\t" \
   "v_fma_f32 v160, v160, v112, -v24\n\t" "v_fma_f32 v165, v161, v115, v27\n\t" "v_fma_f32 v161, v161, v114, -v26\n\t" \
   "v_fma_f32 v166, v162, v117, v29\n\t" "v_fma_f32 v162, v162, v116, -v28\n\t" "v_fma_f32 v167, v163, v119, v31\n\t" \
   "v_fma_f32 v163, v163, v118, -v30\n\t" "v_cvt_pk_bf16_f32 v148, v164, v165\n\t" "v_cvt_pk_bf16_f32 v144, v160, v161\n\t" \
   "v_cvt_pk_bf16_f32 v149, v166, v167\n\t" "v_cvt_pk_bf16_f32 v145, v162, v163\n\t" "v_mfma_f32_16x16x32_bf16 v[160:163], v[136:139], v[64:67], 0\n\t" \
   "v_mul_f32 v24, v172, v121\n\t" "v_mul_f32 v25, v172, v120\n\t" "v_mul_f32 v26, v173, v123\n\t" \
   "v_mfma_f32_16x16x32_bf16 v[164:167], v[140:143], v[64:67], 0\n\t" "v_mul_f32 v27, v173, v122\n\t" "v_mul_f32 v28, v174, v125\n\t" \
   "v_mul_f32 v29, v174, v124\n\t" "v_mfma_f32_16x16x32_bf16 v[160:163], v[140:143], v[128:131], v[160:163]\n\t" "v_mul_f32 v30, v175, v127\n\t" \
   "v_mul_f32 v31, v175, v126\n\t" "v_fma_f32 v172, v168, v121, v25\n\t" "v_mfma_f32_16x16x32_bf16 v[164:167], v[136:139], v[72:75], v[164:167]\n\t" \
   "v_fma_f32 v168, v168, v120, -v24\n\t" "v_fma_f32 v173, v169, v123, v27\n\t" "v_fma_f32 v169, v169, v122, -v26\n\t" \
   "v_fma_f32 v174, v170, v125, v29\n\t" "v_fma_f32 v170, v170, v124, -v28\n\t" "v_fma_f32 v175, v171, v127, v31\n\t" \
   "v_fma_f32 v171, v171, v126, -v30\n\t" "v_cvt_pk_bf16_f32 v150, v172, v173\n\t" "v_cvt_pk_bf16_f32 v146, v168, v169\n\t" \
   "v_cvt_pk_bf16_f32 v151, v174, v175\n\t" "v_cvt_pk_bf16_f32 v147, v170, v171\n\t" "v_mfma_f32_16x16x32_bf16 v[168:171], v[136:139], v[68:71], 0\n\t" \
   "v_mul_f32 v24, v164, v81\n\t" "v_mul_f32 v25, v164, v80\n\t" "v_mul_f32 v26, v165, v83\n\t" \
   "v_mfma_f32_16x16x32_bf16 v[172:175], v[140:143], v[68:71], 0\n\t" "v_mul_f32 v27, v165, v82\n\t" "v_mul_f32 v28, v166, v85\n\t" \
   "v_mul_f32 v29, v166, v84\n\t" "v_mfma_f32_16x16x32_bf16 v[168:171], v[140:143], v[132:135], v[168:171]\n\t" "v_mul_f32 v30, v167, v87\n\t" \
   "v_mul_f32 v31, v167, v86\n\t" "v_fma_f32 v164, v160, v81, v25\n\t" "v_mfma_f32_16x16x32_bf16 v[172:175], v[136:139], v[76:79], v[172:175]\n\t" \
   "v_fma_f32 v160, v160, v80, -v24\n\t" "v_fma_f32 v165, v161, v83, v27\n\t" "v_fma_f32 v161, v161, v82, -v26\n\t" \
   "v_fma_f32 v166, v162, v85, v29\n\t" "v_fma_f32 v162, v162, v84, -v28\n\t" "v_fma_f32 v167, v163, v87, v31\n\t" \
   "v_fma_f32 v163, v163, v86, -v30\n\t" "v_cvt_pk_bf16_f32 v178, v164, v165\n\t" "v_cvt_pk_bf16_f32 v176, v160, v161\n\t" \
   "v_cvt_pk_bf16_f32 v179, v166, v167\n\t" "v_cvt_pk_bf16_f32 v177, v162, v163\n\t" "ds_write_b64 v17, v[178:179] offset:128\n\t" \
   "ds_write_b64 v17, v[176:177] offset:0\n\t" "v_mfma_f32_16x16x32_bf16 v[160:163], v[144:147], v[64:67], 0\n\t" "v_mul_f32 v24, v172, v89\n\t" \
   "v_mul_f32 v25, v172, v88\n\t" "v_mul_f32 v26, v173, v91\n\t" "v_mfma_f32_16x16x32_bf16 v[164:167], v[148:151], v[64:67], 0\n\t" \
   "v_mul_f32 v27, v173, v90\n\t" "v_mul_f32 v28, v174, v93\n\t" "v_mul_f32 v29, v174, v92\n\t" \
   "v_mfma_f32_16x16x32_bf16 v[160:163], v[148:151], v[128:131], v[160:163]\n\t" "v_mul_f32 v30, v175, v95\n\t" "v_mul_f32 v31, v175, v94\n\t" \
   "v_fma_f32 v172, v168, v89, v25\n\t" "v_mfma_f32_16x16x32_bf16 v[164:167], v[144:147], v[72:75], v[164:167]\n\t" \
   "v_fma_f32 v168, v168, v88, -v24\n\t" "v_fma_f32 v173, v169, v91, v27\n\t" "v_fma_f32 v169, v169, v90, -v26\n\t" \
   "v_fma_f32 v174, v170, v93, v29\n\t" "v_fma_f32 v170, v170, v92, -v28\n\t" "v_fma_f32 v175, v171, v95, v31\n\t" \
   "v_fma_f32 v171, v171, v94, -v30\n\t" "v_cvt_pk_bf16_f32 v182, v172, v173\n\t" "v_cvt_pk_bf16_f32 v180, v168, v169\n\t" \
   "v_cvt_pk_bf16_f32 v183, v174, v175\n\t" "v_cvt_pk_bf16_f32 v181, v170, v171\n\t" "ds_write_b64 v17, v[182:183] offset:8384\n\t" \
   "ds_write_b64 v17, v[180:181] offset:8256\n\t" "v_mfma_f32_16x16x32_bf16 v[168:171], v[144:147], v[68:71], 0\n\t" "v_mul_f32 v24, v164, v81\n\t" \
   "v_mul_f32 v25, v164, v80\n\t" "v_mul_f32 v26, v165, v83\n\t" "v_mfma_f32_16x16x32_bf16 v[172:175], v[148:151], v[68:71], 0\n\t" \
   "v_mul_f32 v27, v165, v82\n\t" "v_mul_f32 v28, v166, v85\n\t" "v_mul_f32 v29, v166, v84\n\t" \
   "v_mfma_f32_16x16x32_bf16 v[168:171], v[148:151], v[132:135], v[168:171]\n\t" "v_mul_f32 v30, v167, v87\n\t" "v_mul_f32 v31, v167, v86\n\t" \
   "v_fma_f32 v164, v160, v81, v25\n\t" "v_mfma_f32_16x16x32_bf16 v[172:175], v[144:147], v[76:79], v[172:175]\n\t" \
   "v_fma_f32 v160, v160, v80, -v24\n\t" "v_fma_f32 v165, v161, v83, v27\n\t" "v_fma_f32 v161, v161, v82, -v26\n\t" \
   "v_fma_f32 v166, v162, v85, v29\n\t" "v_fma_f32 v162, v162, v84, -v28\n\t" "v_fma_f32 v167, v163, v87, v31\n\t" \
   "v_fma_f32 v163, v163, v86, -v30\n\t" "v_cvt_pk_bf16_f32 v178, v164, v165\n\t" "v_cvt_pk_bf16_f32 v176, v160, v161\n\t" \
   "v_cvt_pk_bf16_f32 v179, v166, v167\n\t" "v_cvt_pk_bf16_f32 v177, v162, v163\n\t" "ds_write_b64 v18, v[178:179] offset:128\n\t" \
   "ds_write_b64 v18, v[176:177] offset:0\n\t" "v_mul_f32 v24, v172, v89\n\t" "v_mul_f32 v25, v172, v88\n\t" "v_mul_f32 v26, v173, v91\n\t" \
   "v_mul_f32 v27, v173, v90\n\t" "v_mul_f32 v28, v174, v93\n\t" "v_mul_f32 v29, v174, v92\n\t" "v_mul_f32 v30, v175, v95\n\t" \
   "v_mul_f32 v31, v175, v94\n\t" "v_fma_f32 v172, v168, v89, v25\n\t" "v_fma_f32 v168, v168, v88, -v24\n\t" "v_fma_f32 v173, v169, v91, v27\n\t" \
   "v_fma_f32 v169, v169, v90, -v26\n\t" "v_fma_f32 v174, v170, v93, v29\n\t" "v_fma_f32 v170, v170, v92, -v28\n\t" \
   "v_fma_f32 v175, v171, v95, v31\n\t" "v_fma_f32 v171, v171, v94, -v30\n\t" "v_cvt_pk_bf16_f32 v182, v172, v173\n\t" \
   "v_cvt_pk_bf16_f32 v180, v168, v169\n\t" "v_cvt_pk_bf16_f32 v183, v174, v175\n\t" "v_cvt_pk_bf16_f32 v181, v170, v171\n\t" \
   "ds_write_b64 v18, v[182:183] offset:8384\n\t" "ds_write_b64 v18, v[180:181] offset:8256\n\t" "s_waitcnt lgkmcnt(0)\n\t" "s_barrier\n\t" \
   "ds_read_b128 v[136:139], v19 offset:0\n\t" "ds_read_b128 v[140:143], v20 offset:0\n\t" "ds_read_b128 v[144:147], v19 offset:2064\n\t" \
   "ds_read_b128 v[148:151], v20 offset:2064\n\t" "s_waitcnt lgkmcnt(2)\n\t" "v_mfma_f32_16x16x32_bf16 v[160:163], v[136:139], v[32:35], 0\n\t" \
   "ds_read_b128 v[152:155], v19 offset:4128\n\t" "ds_read_b128 v[156:159], v20 offset:4128\n\t" \
   "v_mfma_f32_16x16x32_bf16 v[164:167], v[136:139], v[40:43], 0\n\t" "v_mfma_f32_16x16x32_bf16 v[160:163], v[140:143], v[36:39], v[160:163]\n\t" \
   "v_mfma_f32_16x16x32_bf16 v[164:167], v[140:143], v[44:47], v[164:167]\n\t" "s_waitcnt lgkmcnt(2)\n\t" \
   "v_mfma_f32_16x16x32_bf16 v[168:171], v[144:147], v[32:35], 0\n\t" "ds_read_b128 v[136:139], v19 offset:6192\n\t" \
   "ds_read_b128 v[140:143], v20 offset:6192\n\t" "v_mfma_f32_16x16x32_bf16 v[172:175], v[144:147], v[40:43], 0\n\t" "v_sub_f32 v160, v160, v165\n\t" \
   "v_add_f32 v161, v161, v164\n\t" "v_sub_f32 v162, v162, v167\n\t" "v_mfma_f32_16x16x32_bf16 v[168:171], v[148:151], v[36:39], v[168:171]\n\t" \
   "v_add_f32 v163, v163, v166\n\t" "v_cvt_pk_bf16_f32 v8, v160, v161\n\t" "v_cvt_pk_bf16_f32 v9, v162, v163\n\t" \
   "v_mfma_f32_16x16x32_bf16 v[172:175], v[148:151], v[44:47], v[172:175]\n\t" "s_waitcnt lgkmcnt(2)\n\t" \
   "v_mfma_f32_16x16x32_bf16 v[160:163], v[152:155], v[32:35], 0\n\t" "ds_read_b128 v[144:147], v19 offset:8256\n\t" \
   "ds_read_b128 v[148:151], v20 offset:8256\n\t" "v_mfma_f32_16x16x32_bf16 v[164:167], v[152:155], v[40:43], 0\n\t" "v_sub_f32 v168, v168, v173\n\t" \
   "v_add_f32 v169, v169, v172\n\t" "v_sub_f32 v170, v170, v175\n\t" "v_mfma_f32_16x16x32_bf16 v[160:163], v[156:159], v[36:39], v[160:163]\n\t" \
   "v_add_f32 v171, v171, v174\n\t" "v_cvt_pk_bf16_f32 v10, v168, v169\n\t" "v_cvt_pk_bf16_f32 v11, v170, v171\n\t" \
   "v_mfma_f32_16x16x32_bf16 v[164:167], v[156:159], v[44:47], v[164:167]\n\t" "buffer_store_dwordx4 v[8:11], v21, %[rs], 0 offen offset:0 sc1\n\t" \
   "s_waitcnt lgkmcnt(2)\n\t" "v_mfma_f32_16x16x32_bf16 v[168:171], v[136:139], v[32:35], 0\n\t" "ds_read_b128 v[152:155], v19 offset:10320\n\t" \
   "ds_read_b128 v[156:159], v20 offset:10320\n\t" "v_mfma_f32_16x16x32_bf16 v[172:175], v[136:139], v[40:43], 0\n\t" "v_sub_f32 v160, v160, v165\n\t" \
   "v_add_f32 v161, v161, v164\n\t" "v_sub_f32 v162, v162, v167\n\t" "v_mfma_f32_16x16x32_bf16 v[168:171], v[140:143], v[36:39], v[168:171]\n\t" \
   "v_add_f32 v163, v163, v166\n\t" "v_cvt_pk_bf16_f32 v12, v160, v161\n\t" "v_cvt_pk_bf16_f32 v13, v162, v163\n\t" \
   "v_mfma_f32_16x16x32_bf16 v[172:175], v[140:143], v[44:47], v[172:175]\n\t" "s_waitcnt lgkmcnt(2)\n\t" \
   "v_mfma_f32_16x16x32_bf16 v[160:163], v[144:147], v[32:35], 0\n\t" "ds_read_b128 v[136:139], v19 offset:12384\n\t" \
   "ds_read_b128 v[140:143], v20 offset:12384\n\t" "v_mfma_f32_16x16x32_bf16 v[164:167], v[144:147], v[40:43], 0\n\t" "v_sub_f32 v168, v168, v173\n\t" \
   "v_add_f32 v169, v169, v172\n\t" "v_sub_f32 v170, v170, v175\n\t" "v_mfma_f32_16x16x32_bf16 v[160:163], v[148:151], v[36:39], v[160:163]\n\t" \
   "v_add_f32 v171, v171, v174\n\t" "v_cvt_pk_bf16_f32 v14, v168, v169\n\t" "v_cvt_pk_bf16_f32 v15, v170, v171\n\t" \
   "v_mfma_f32_16x16x32_bf16 v[164:167], v[148:151], v[44:47], v[164:167]\n\t" "buffer_store_dwordx4 v[12:15], v21, %[rs], 0 offen offset:64 sc1\n\t" \
   "s_waitcnt lgkmcnt(2)\n\t" "v_mfma_f32_16x16x32_bf16 v[168:171], v[152:155], v[32:35], 0\n\t" "ds_read_b128 v[144:147], v19 offset:14448\n\t" \
   "ds_read_b128 v[148:151], v20 offset:14448\n\t" "v_mfma_f32_16x16x32_bf16 v[172:175], v[152:155], v[40:43], 0\n\t" "v_sub_f32 v160, v160, v165\n\t" \
   "v_add_f32 v161, v161, v164\n\t" "v_sub_f32 v162, v162, v167\n\t" "v_mfma_f32_16x16x32_bf16 v[168:171], v[156:159], v[36:39], v[168:171]\n\t" \
   "v_add_f32 v163, v163, v166\n\t" "v_cvt_pk_bf16_f32 v8, v160, v161\n\t" "v_cvt_pk_bf16_f32 v9, v162, v163\n\t" \
   "v_mfma_f32_16x16x32_bf16 v[172:175], v[156:159], v[44:47], v[172:175]\n\t" "s_waitcnt lgkmcnt(2)\n\t" \
   "v_mfma_f32_16x16x32_bf16 v[160:163], v[136:139], v[32:35], 0\n\t" "s_nop 0\n\t" "s_nop 0\n\t" \
   "v_mfma_f32_16x16x32_bf16 v[164:167], v[136:139], v[40:43], 0\n\t" "v_sub_f32 v168, v168, v173\n\t" "v_add_f32 v169, v169, v172\n\t" \
   "v_sub_f32 v170, v170, v175\n\t" "v_mfma_f32_16x16x32_bf16 v[160:163], v[140:143], v[36:39], v[160:163]\n\t" "v_add_f32 v171, v171, v174\n\t" \
   "v_cvt_pk_bf16_f32 v10, v168, v169\n\t" "v_cvt_pk_bf16_f32 v11, v170, v171\n\t" \
   "v_mfma_f32_16x16x32_bf16 v[164:167], v[140:143], v[44:47], v[164:167]\n\t" "buffer_store_dwordx4 v[8:11], v21, %[rs], 0 offen offset:128 sc1\n\t" \
   "s_waitcnt lgkmcnt(0)\n\t" "v_mfma_f32_16x16x32_bf16 v[168:171], v[144:147], v[32:35], 0\n\t" "s_nop 0\n\t" "s_nop 0\n\t" \
   "v_mfma_f32_16x16x32_bf16 v[172:175], v[144:147], v[40:43], 0\n\t" "v_sub_f32 v160, v160, v165\n\t" "v_add_f32 v161, v161, v164\n\t" \
   "v_sub_f32 v162, v162, v167\n\t" "v_mfma_f32_16x16x32_bf16 v[168:171], v[148:151], v[36:39], v[168:171]\n\t" "v_add_f32 v163, v163, v166\n\t" \
   "v_cvt_pk_bf16_f32 v12, v160, v161\n\t" "v_cvt_pk_bf16_f32 v13, v162, v163\n\t" \
   "v_mfma_f32_16x16x32_bf16 v[172:175], v[148:151], v[44:47], v[172:175]\n\t" "s_nop 7\n\t" "v_sub_f32 v168, v168, v173\n\t" \
   "v_add_f32 v169, v169, v172\n\t" "v_sub_f32 v170, v170, v175\n\t" "v_add_f32 v171, v171, v174\n\t" "v_cvt_pk_bf16_f32 v14, v168, v169\n\t" \
   "v_cvt_pk_bf16_f32 v15, v170, v171\n\t" "buffer_store_dwordx4 v[12:15], v21, %[rs], 0 offen offset:192 sc1\n\t" "s_nop 1"
#define COL_CLOB "v8", "v9", "v10", "v11", "v12", "v13", "v14", "v15", "v24", "v25", "v26", "v27", "v28", "v29", "v30", "v31", "v136", "v137", "v138", "v139", "v140", "v141", "v142", "v143", "v144", "v145", "v146", "v147", "v148", "v149", "v150", "v151", "v152", "v153", "v154", "v155", "v156", "v157", "v158", "v159", "v160", "v161", "v162", "v163", "v164", "v165", "v166", "v167", "v168", "v169", "v170", "v171", "v172", "v173", "v174", "v175", "v176", "v177", "v178", "v179", "v180", "v181", "v182", "v183", "v184", "v185", "v186", "v187", "v188", "v189", "v190", "v191"
#define IM_ASM \
   "s_mov_b32 s40, 0\n\t" "buffer_load_dwordx4 a[0:3], %[o0], %[rs], s40 offen offset:0 sc1\n\t" "s_mov_b32 s41, 20480\n\t" \
   "buffer_load_dwordx4 a[80:83], %[o0], %[rs], s41 offen offset:0 sc1\n\t" "s_mov_b32 s42, 40960\n\t" \
   "buffer_load_dwordx4 a[160:163], %[o1], %[rs], s42 offen offset:0 sc1\n\t" "buffer_load_dwordx4 a[4:7], %[o0], %[rs], s40 offen offset:1024 sc1\n\t" \
   "buffer_load_dwordx4 a[84:87], %[o0], %[rs], s41 offen offset:1024 sc1\n\t" \
   "buffer_load_dwordx4 a[164:167], %[o1], %[rs], s42 offen offset:1024 sc1\n\t" \
   "buffer_load_dwordx4 a[8:11], %[o0], %[rs], s40 offen offset:2048 sc1\n\t" \
   "buffer_load_dwordx4 a[88:91], %[o0], %[rs], s41 offen offset:2048 sc1\n\t" \
   "buffer_load_dwordx4 a[168:171], %[o1], %[rs], s42 offen offset:2048 sc1\n\t" \
   "buffer_load_dwordx4 a[12:15], %[o0], %[rs], s40 offen offset:3072 sc1\n\t" \
   "buffer_load_dwordx4 a[92:95], %[o0], %[rs], s41 offen offset:3072 sc1\n\t" \
   "buffer_load_dwordx4 a[172:175], %[o1], %[rs], s42 offen offset:3072 sc1\n\t" "s_mov_b32 s40, 4096\n\t" \
   "buffer_load_dwordx4 a[16:19], %[o0], %[rs], s40 offen offset:0 sc1\n\t" "s_mov_b32 s41, 24576\n\t" \
   "buffer_load_dwordx4 a[96:99], %[o0], %[rs], s41 offen offset:0 sc1\n\t" "s_mov_b32 s42, 45056\n\t" \
   "buffer_load_dwordx4 a[176:179], %[o1], %[rs], s42 offen offset:0 sc1\n\t" \
   "buffer_load_dwordx4 a[20:23], %[o0], %[rs], s40 offen offset:1024 sc1\n\t" \
   "buffer_load_dwordx4 a[100:103], %[o0], %[rs], s41 offen offset:1024 sc1\n\t" \
   "buffer_load_dwordx4 a[180:183], %[o1], %[rs], s42 offen offset:1024 sc1\n\t" \
   "buffer_load_dwordx4 a[24:27], %[o0], %[rs], s40 offen offset:2048 sc1\n\t" \
   "buffer_load_dwordx4 a[104:107], %[o0], %[rs], s41 offen offset:2048 sc1\n\t" \
   "buffer_load_dwordx4 a[184:187], %[o1], %[rs], s42 offen offset:2048 sc1\n\t" \
   "buffer_load_dwordx4 a[28:31], %[o0], %[rs], s40 offen offset:3072 sc1\n\t" \
   "buffer_load_dwordx4 a[108:111], %[o0], %[rs], s41 offen offset:3072 sc1\n\t" \
   "buffer_load_dwordx4 a[188:191], %[o1], %[rs], s42 offen offset:3072 sc1\n\t" "s_mov_b32 s40, 8192\n\t" \
   "buffer_load_dwordx4 a[32:35], %[o0], %[rs], s40 offen offset:0 sc1\n\t" "s_mov_b32 s41, 28672\n\t" \
   "buffer_load_dwordx4 a[112:115], %[o0], %[rs], s41 offen offset:0 sc1\n\t" "s_mov_b32 s42, 49152\n\t" \
   "buffer_load_dwordx4 a[192:195], %[o1], %[rs], s42 offen offset:0 sc1\n\t" \
   "buffer_load_dwordx4 a[36:39], %[o0], %[rs], s40 offen offset:1024 sc1\n\t" \
   "buffer_load_dwordx4 a[116:119], %[o0], %[rs], s41 offen offset:1024 sc1\n\t" \
   "buffer_load_dwordx4 a[196:199], %[o1], %[rs], s42 offen offset:1024 sc1\n\t" \
   "buffer_load_dwordx4 a[40:43], %[o0], %[rs], s40 offen offset:2048 sc1\n\t" \
   "buffer_load_dwordx4 a[120:123], %[o0], %[rs], s41 offen offset:2048 sc1\n\t" \
   "buffer_load_dwordx4 a[200:203], %[o1], %[rs], s42 offen offset:2048 sc1\n\t" \
   "buffer_load_dwordx4 a[44:47], %[o0], %[rs], s40 offen offset:3072 sc1\n\t" \
   "buffer_load_dwordx4 a[124:127], %[o0], %[rs], s41 offen offset:3072 sc1\n\t" \
   "buffer_load_dwordx4 a[204:207], %[o1], %[rs], s42 offen offset:3072 sc1\n\t" "s_mov_b32 s40, 12288\n\t" \
   "buffer_load_dwordx4 a[48:51], %[o0], %[rs], s40 offen offset:0 sc1\n\t" "s_mov_b32 s41, 32768\n\t" \
   "buffer_load_dwordx4 a[128:131], %[o0], %[rs], s41 offen offset:0 sc1\n\t" "s_mov_b32 s42, 53248\n\t" \
   "buffer_load_dwordx4 a[208:211], %[o1], %[rs], s42 offen offset:0 sc1\n\t" \
   "buffer_load_dwordx4 a[52:55], %[o0], %[rs], s40 offen offset:1024 sc1\n\t" \
   "buffer_load_dwordx4 a[132:135], %[o0], %[rs], s41 offen offset:1024 sc1\n\t" \
   "buffer_load_dwordx4 a[212:215], %[o1], %[rs], s42 offen offset:1024 sc1\n\t" \
   "buffer_load_dwordx4 a[56:59], %[o0], %[rs], s40 offen offset:2048 sc1\n\t" \
   "buffer_load_dwordx4 a[136:139], %[o0], %[rs], s41 offen offset:2048 sc1\n\t" \
   "buffer_load_dwordx4 a[216:219], %[o1], %[rs], s42 offen offset:2048 sc1\n\t" \
   "buffer_load_dwordx4 a[60:63], %[o0], %[rs], s40 offen offset:3072 sc1\n\t" \
   "buffer_load_dwordx4 a[140:143], %[o0], %[rs], s41 offen offset:3072 sc1\n\t" \
   "buffer_load_dwordx4 a[220:223], %[o1], %[rs], s42 offen offset:3072 sc1\n\t" "s_mov_b32 s40, 16384\n\t" \
   "buffer_load_dwordx4 a[64:67], %[o0], %[rs], s40 offen offset:0 sc1\n\t" "s_mov_b32 s41, 36864\n\t" \
   "buffer_load_dwordx4 a[144:147], %[o0], %[rs], s41 offen offset:0 sc1\n\t" "s_mov_b32 s42, 57344\n\t" \
   "buffer_load_dwordx4 a[224:227], %[o1], %[rs], s42 offen offset:0 sc1\n\t" \
   "buffer_load_dwordx4 a[68:71], %[o0], %[rs], s40 offen offset:1024 sc1\n\t" \
   "buffer_load_dwordx4 a[148:151], %[o0], %[rs], s41 offen offset:1024 sc1\n\t" \
   "buffer_load_dwordx4 a[228:231], %[o1], %[rs], s42 offen offset:1024 sc1\n\t" \
   "buffer_load_dwordx4 a[72:75], %[o0], %[rs], s40 offen offset:2048 sc1\n\t" \
   "buffer_load_dwordx4 a[152:155], %[o0], %[rs], s41 offen offset:2048 sc1\n\t" \
   "buffer_load_dwordx4 a[232:235], %[o1], %[rs], s42 offen offset:2048 sc1\n\t" \
   "buffer_load_dwordx4 a[76:79], %[o0], %[rs], s40 offen offset:3072 sc1\n\t" \
   "buffer_load_dwordx4 a[156:159], %[o0], %[rs], s41 offen offset:3072 sc1\n\t" \
   "buffer_load_dwordx4 a[236:239], %[o1], %[rs], s42 offen offset:3072 sc1\n\t" "s_waitcnt vmcnt(0)"
#define IM_CLOB "s40", "s41", "s42"
#define ID_ASM \
   "s_waitcnt lgkmcnt(0)\n\t" "s_barrier\n\t" "ds_read_b128 v[32:35], %[la] offset:0\n\t" "ds_read_b128 v[36:39], %[la] offset:10240\n\t" \
   "ds_read_b128 v[40:43], %[la] offset:1024\n\t" "ds_read_b128 v[44:47], %[la] offset:11264\n\t" "ds_read_b128 v[48:51], %[la] offset:2048\n\t" \
   "ds_read_b128 v[52:55], %[la] offset:12288\n\t" "ds_read_b128 v[56:59], %[la] offset:3072\n\t" "ds_read_b128 v[60:63], %[la] offset:13312\n\t" \
   "ds_read_b128 v[64:67], %[la] offset:4096\n\t" "ds_read_b128 v[68:71], %[la] offset:14336\n\t" "ds_read_b128 v[72:75], %[la] offset:5120\n\t" \
   "ds_read_b128 v[76:79], %[la] offset:15360\n\t" "ds_read_b128 v[80:83], %[la] offset:6144\n\t" "ds_read_b128 v[84:87], %[la] offset:16384\n\t" \
   "ds_read_b128 v[88:91], %[la] offset:7168\n\t" "ds_read_b128 v[92:95], %[la] offset:17408\n\t" "ds_read_b128 v[96:99], %[la] offset:8192\n\t" \
   "ds_read_b128 v[100:103], %[la] offset:18432\n\t" "ds_read_b128 v[104:107], %[la] offset:9216\n\t" "ds_read_b128 v[108:111], %[la] offset:19456\n\t" \
   "v_lshlrev_b32 v152, 16, v136\n\t" "v_and_b32 v153, 0xffff0000, v136\n\t" "v_lshlrev_b32 v154, 16, v137\n\t" "v_and_b32 v155, 0xffff0000, v137\n\t" \
   "v_lshlrev_b32 v156, 16, v140\n\t" "v_and_b32 v157, 0xffff0000, v140\n\t" "v_lshlrev_b32 v158, 16, v141\n\t" "v_and_b32 v159, 0xffff0000, v141\n\t" \
   "v_lshlrev_b32 v160, 16, v138\n\t" "v_and_b32 v161, 0xffff0000, v138\n\t" "v_lshlrev_b32 v162, 16, v139\n\t" "v_and_b32 v163, 0xffff0000, v139\n\t" \
   "s_waitcnt lgkmcnt(15)\n\t" "v_mfma_f32_16x16x32_bf16 v[112:115], a[0:3], v[32:35], 0\n\t" "v_lshlrev_b32 v164, 16, v142\n\t" \
   "v_and_b32 v165, 0xffff0000, v142\n\t" "v_mfma_f32_16x16x32_bf16 v[116:119], a[4:7], v[36:39], 0\n\t" "v_lshlrev_b32 v166, 16, v143\n\t" \
   "v_and_b32 v167, 0xffff0000, v143\n\t" "v_mfma_f32_16x16x32_bf16 v[112:115], a[8:11], v[40:43], v[112:115]\n\t" "v_lshlrev_b32 v168, 16, v144\n\t" \
   "v_and_b32 v169, 0xffff0000, v144\n\t" "v_mfma_f32_16x16x32_bf16 v[116:119], a[12:15], v[44:47], v[116:119]\n\t" "v_lshlrev_b32 v170, 16, v145\n\t" \
   "v_and_b32 v171, 0xffff0000, v145\n\t" "v_mfma_f32_16x16x32_bf16 v[112:115], a[16:19], v[48:51], v[112:115]\n\t" "v_lshlrev_b32 v172, 16, v146\n\t" \
   "v_and_b32 v173, 0xffff0000, v146\n\t" "s_waitcnt lgkmcnt(14)\n\t" "v_mfma_f32_16x16x32_bf16 v[116:119], a[20:23], v[52:55], v[116:119]\n\t" \
   "v_lshlrev_b32 v174, 16, v147\n\t" "v_and_b32 v175, 0xffff0000, v147\n\t" "s_waitcnt lgkmcnt(13)\n\t" \
   "v_mfma_f32_16x16x32_bf16 v[112:115], a[24:27], v[56:59], v[112:115]\n\t" "s_waitcnt lgkmcnt(12)\n\t" \
   "v_mfma_f32_16x16x32_bf16 v[116:119], a[28:31], v[60:63], v[116:119]\n\t" "s_waitcnt lgkmcnt(11)\n\t" \
   "v_mfma_f32_16x16x32_bf16 v[112:115], a[32:35], v[64:67], v[112:115]\n\t" "s_waitcnt lgkmcnt(10)\n\t" \
   "v_mfma_f32_16x16x32_bf16 v[116:119], a[36:39], v[68:71], v[116:119]\n\t" "s_waitcnt lgkmcnt(9)\n\t" \
   "v_mfma_f32_16x16x32_bf16 v[112:115], a[40:43], v[72:75], v[112:115]\n\t" "s_waitcnt lgkmcnt(8)\n\t" \
   "v_mfma_f32_16x16x32_bf16 v[116:119], a[44:47], v[76:79], v[116:119]\n\t" "s_waitcnt lgkmcnt(7)\n\t" \
   "v_mfma_f32_16x16x32_bf16 v[112:115], a[48:51], v[80:83], v[112:115]\n\t" "s_waitcnt lgkmcnt(6)\n\t" \
   "v_mfma_f32_16x16x32_bf16 v[116:119], a[52:55], v[84:87], v[116:119]\n\t" "s_waitcnt lgkmcnt(5)\n\t" \
   "v_mfma_f32_16x16x32_bf16 v[112:115], a[56:59], v[88:91], v[112:115]\n\t" "s_waitcnt lgkmcnt(4)\n\t" \
   "v_mfma_f32_16x16x32_bf16 v[116:119], a[60:63], v[92:95], v[116:119]\n\t" "s_waitcnt lgkmcnt(3)\n\t" \
   "v_mfma_f32_16x16x32_bf16 v[112:115], a[64:67], v[96:99], v[112:115]\n\t" "s_waitcnt lgkmcnt(2)\n\t" \
   "v_mfma_f32_16x16x32_bf16 v[116:119], a[68:71], v[100:103], v[116:119]\n\t" "s_waitcnt lgkmcnt(1)\n\t" \
   "v_mfma_f32_16x16x32_bf16 v[112:115], a[72:75], v[104:107], v[112:115]\n\t" "s_waitcnt lgkmcnt(0)\n\t" \
   "v_mfma_f32_16x16x32_bf16 v[116:119], a[76:79], v[108:111], v[116:119]\n\t" "v_mfma_f32_16x16x32_bf16 v[120:123], a[80:83], v[32:35], 0\n\t" \
   "v_mfma_f32_16x16x32_bf16 v[124:127], a[84:87], v[36:39], 0\n\t" "v_mfma_f32_16x16x32_bf16 v[120:123], a[88:91], v[40:43], v[120:123]\n\t" \
   "s_nop 1\n\t" "v_add_f32 v176, v112, v116\n\t" "v_sub_f32 v180, v112, v116\n\t" \
   "v_mfma_f32_16x16x32_bf16 v[124:127], a[92:95], v[44:47], v[124:127]\n\t" "v_add_f32 v177, v113, v117\n\t" "v_sub_f32 v181, v113, v117\n\t" \
   "v_mfma_f32_16x16x32_bf16 v[120:123], a[96:99], v[48:51], v[120:123]\n\t" "v_add_f32 v178, v114, v118\n\t" "v_sub_f32 v182, v114, v118\n\t" \
   "v_mfma_f32_16x16x32_bf16 v[124:127], a[100:103], v[52:55], v[124:127]\n\t" "v_add_f32 v179, v115, v119\n\t" "v_sub_f32 v183, v115, v119\n\t" \
   "v_mfma_f32_16x16x32_bf16 v[120:123], a[104:107], v[56:59], v[120:123]\n\t" "v_cvt_pk_bf16_f32 v184, v176, v177\n\t" \
   "v_cvt_pk_bf16_f32 v185, v178, v179\n\t" "v_mfma_f32_16x16x32_bf16 v[124:127], a[108:111], v[60:63], v[124:127]\n\t" \
   "v_cvt_pk_bf16_f32 v186, v180, v181\n\t" "v_cvt_pk_bf16_f32 v187, v182, v183\n\t" \
   "v_mfma_f32_16x16x32_bf16 v[120:123], a[112:115], v[64:67], v[120:123]\n\t" "v_lshlrev_b32 v176, 16, v184\n\t" \
   "v_and_b32 v177, 0xffff0000, v184\n\t" "v_mfma_f32_16x16x32_bf16 v[124:127], a[116:119], v[68:71], v[124:127]\n\t" \
   "v_lshlrev_b32 v178, 16, v185\n\t" "v_and_b32 v179, 0xffff0000, v185\n\t" \
   "v_mfma_f32_16x16x32_bf16 v[120:123], a[120:123], v[72:75], v[120:123]\n\t" "v_lshlrev_b32 v180, 16, v186\n\t" \
   "v_and_b32 v181, 0xffff0000, v186\n\t" "v_mfma_f32_16x16x32_bf16 v[124:127], a[124:127], v[76:79], v[124:127]\n\t" \
   "v_lshlrev_b32 v182, 16, v187\n\t" "v_and_b32 v183, 0xffff0000, v187\n\t" \
   "v_mfma_f32_16x16x32_bf16 v[120:123], a[128:131], v[80:83], v[120:123]\n\t" "v_add_f32 v176, v176, v152\n\t" "v_add_f32 v177, v177, v153\n\t" \
   "v_mfma_f32_16x16x32_bf16 v[124:127], a[132:135], v[84:87], v[124:127]\n\t" "v_add_f32 v178, v178, v154\n\t" "v_add_f32 v179, v179, v155\n\t" \
   "v_mfma_f32_16x16x32_bf16 v[120:123], a[136:139], v[88:91], v[120:123]\n\t" "v_add_f32 v180, v180, v156\n\t" "v_add_f32 v181, v181, v157\n\t" \
   "v_mfma_f32_16x16x32_bf16 v[124:127], a[140:143], v[92:95], v[124:127]\n\t" "v_add_f32 v182, v182, v158\n\t" "v_add_f32 v183, v183, v159\n\t" \
   "v_mfma_f32_16x16x32_bf16 v[120:123], a[144:147], v[96:99], v[120:123]\n\t" "v_cvt_pk_bf16_f32 v136, v176, v177\n\t" \
   "v_cvt_pk_bf16_f32 v137, v178, v179\n\t" "v_mfma_f32_16x16x32_bf16 v[124:127], a[148:151], v[100:103], v[124:127]\n\t" \
   "v_cvt_pk_bf16_f32 v140, v180, v181\n\t" "v_cvt_pk_bf16_f32 v141, v182, v183\n\t" \
   "v_mfma_f32_16x16x32_bf16 v[120:123], a[152:155], v[104:107], v[120:123]\n\t" \
   "v_mfma_f32_16x16x32_bf16 v[124:127], a[156:159], v[108:111], v[124:127]\n\t" "v_mfma_f32_16x16x32_bf16 v[128:131], a[160:163], v[32:35], 0\n\t" \
   "v_mfma_f32_16x16x32_bf16 v[132:135], a[164:167], v[36:39], 0\n\t" "v_mfma_f32_16x16x32_bf16 v[128:131], a[168:171], v[40:43], v[128:131]\n\t" \
   "s_nop 1\n\t" "v_add_f32 v176, v120, v124\n\t" "v_sub_f32 v180, v120, v124\n\t" \
   "v_mfma_f32_16x16x32_bf16 v[132:135], a[172:175], v[44:47], v[132:135]\n\t" "v_add_f32 v177, v121, v125\n\t" "v_sub_f32 v181, v121, v125\n\t" \
   "v_mfma_f32_16x16x32_bf16 v[128:131], a[176:179], v[48:51], v[128:131]\n\t" "v_add_f32 v178, v122, v126\n\t" "v_sub_f32 v182, v122, v126\n\t" \
   "v_mfma_f32_16x16x32_bf16 v[132:135], a[180:183], v[52:55], v[132:135]\n\t" "v_add_f32 v179, v123, v127\n\t" "v_sub_f32 v183, v123, v127\n\t" \
   "v_mfma_f32_16x16x32_bf16 v[128:131], a[184:187], v[56:59], v[128:131]\n\t" "v_cvt_pk_bf16_f32 v184, v176, v177\n\t" \
   "v_cvt_pk_bf16_f32 v185, v178, v179\n\t" "v_mfma_f32_16x16x32_bf16 v[132:135], a[188:191], v[60:63], v[132:135]\n\t" \
   "v_cvt_pk_bf16_f32 v186, v180, v181\n\t" "v_cvt_pk_bf16_f32 v187, v182, v183\n\t" \
   "v_mfma_f32_16x16x32_bf16 v[128:131], a[192:195], v[64:67], v[128:131]\n\t" "v_lshlrev_b32 v176, 16, v184\n\t" \
   "v_and_b32 v177, 0xffff0000, v184\n\t" "v_mfma_f32_16x16x32_bf16 v[132:135], a[196:199], v[68:71], v[132:135]\n\t" \
   "v_lshlrev_b32 v178, 16, v185\n\t" "v_and_b32 v179, 0xffff0000, v185\n\t" \
   "v_mfma_f32_16x16x32_bf16 v[128:131], a[200:203], v[72:75], v[128:131]\n\t" "v_lshlrev_b32 v180, 16, v186\n\t" \
   "v_and_b32 v181, 0xffff0000, v186\n\t" "v_mfma_f32_16x16x32_bf16 v[132:135], a[204:207], v[76:79], v[132:135]\n\t" \
   "v_lshlrev_b32 v182, 16, v187\n\t" "v_and_b32 v183, 0xffff0000, v187\n\t" \
   "v_mfma_f32_16x16x32_bf16 v[128:131], a[208:211], v[80:83], v[128:131]\n\t" "v_add_f32 v176, v176, v160\n\t" "v_add_f32 v177, v177, v161\n\t" \
   "v_mfma_f32_16x16x32_bf16 v[132:135], a[212:215], v[84:87], v[132:135]\n\t" "v_add_f32 v178, v178, v162\n\t" "v_add_f32 v179, v179, v163\n\t" \
   "v_mfma_f32_16x16x32_bf16 v[128:131], a[216:219], v[88:91], v[128:131]\n\t" "v_add_f32 v180, v180, v164\n\t" "v_add_f32 v181, v181, v165\n\t" \
   "v_mfma_f32_16x16x32_bf16 v[132:135], a[220:223], v[92:95], v[132:135]\n\t" "v_add_f32 v182, v182, v166\n\t" "v_add_f32 v183, v183, v167\n\t" \
   "v_mfma_f32_16x16x32_bf16 v[128:131], a[224:227], v[96:99], v[128:131]\n\t" "v_cvt_pk_bf16_f32 v138, v176, v177\n\t" \
   "v_cvt_pk_bf16_f32 v139, v178, v179\n\t" "v_mfma_f32_16x16x32_bf16 v[132:135], a[228:231], v[100:103], v[132:135]\n\t" \
   "v_cvt_pk_bf16_f32 v142, v180, v181\n\t" "v_cvt_pk_bf16_f32 v143, v182, v183\n\t" \
   "v_mfma_f32_16x16x32_bf16 v[128:131], a[232:235], v[104:107], v[128:131]\n\t" \
   "v_mfma_f32_16x16x32_bf16 v[132:135], a[236:239], v[108:111], v[132:135]\n\t" "s_nop 7\n\t" "v_add_f32 v176, v128, v132\n\t" \
   "v_sub_f32 v180, v128, v132\n\t" "v_add_f32 v177, v129, v133\n\t" "v_sub_f32 v181, v129, v133\n\t" "v_add_f32 v178, v130, v134\n\t" \
   "v_sub_f32 v182, v130, v134\n\t" "v_add_f32 v179, v131, v135\n\t" "v_sub_f32 v183, v131, v135\n\t" "v_cvt_pk_bf16_f32 v184, v176, v177\n\t" \
   "v_cvt_pk_bf16_f32 v185, v178, v179\n\t" "v_cvt_pk_bf16_f32 v186, v180, v181\n\t" "v_cvt_pk_bf16_f32 v187, v182, v183\n\t" \
   "v_lshlrev_b32 v176, 16, v184\n\t" "v_and_b32 v177, 0xffff0000, v184\n\t" "v_lshlrev_b32 v178, 16, v185\n\t" "v_and_b32 v179, 0xffff0000, v185\n\t" \
   "v_lshlrev_b32 v180, 16, v186\n\t" "v_and_b32 v181, 0xffff0000, v186\n\t" "v_lshlrev_b32 v182, 16, v187\n\t" "v_and_b32 v183, 0xffff0000, v187\n\t" \
   "v_add_f32 v176, v176, v168\n\t" "v_add_f32 v177, v177, v169\n\t" "v_add_f32 v178, v178, v170\n\t" "v_add_f32 v179, v179, v171\n\t" \
   "v_add_f32 v180, v180, v172\n\t" "v_add_f32 v181, v181, v173\n\t" "v_add_f32 v182, v182, v174\n\t" "v_add_f32 v183, v183, v175\n\t" \
   "v_cvt_pk_bf16_f32 v144, v176, v177\n\t" "v_cvt_pk_bf16_f32 v145, v178, v179\n\t" "v_cvt_pk_bf16_f32 v146, v180, v181\n\t" \
   "v_cvt_pk_bf16_f32 v147, v182, v183\n\t" "s_nop 1"
#define ID_CLOB "v32", "v33", "v34", "v35", "v36", "v37", "v38", "v39", "v40", "v41", "v42", "v43", "v44", "v45", "v46", "v47", "v48", "v49", "v50", "v51", "v52", "v53", "v54", "v55", "v56", "v57", "v58", "v59", "v60", "v61", "v62", "v63", "v64", "v65", "v66", "v67", "v68", "v69", "v70", "v71", "v72", "v73", "v74", "v75", "v76", "v77", "v78", "v79", "v80", "v81", "v82", "v83", "v84", "v85", "v86", "v87", "v88", "v89", "v90", "v91", "v92", "v93", "v94", "v95", "v96", "v97", "v98", "v99", "v100", "v101", "v102", "v103", "v104", "v105", "v106", "v107", "v108", "v109", "v110", "v111", "v112", "v113", "v114", "v115", "v116", "v117", "v118", "v119", "v120", "v121", "v122", "v123", "v124", "v125", "v126", "v127", "v128", "v129", "v130", "v131", "v132", "v133", "v134", "v135", "v152", "v153", "v154", "v155", "v156", "v157", "v158", "v159", "v160", "v161", "v162", "v163", "v164", "v165", "v166", "v167", "v168", "v169", "v170", "v171", "v172", "v173", "v174", "v175", "v176", "v177", "v178", "v179", "v180", "v181", "v182", "v183", "v184", "v185", "v186", "v187"
#define OU_ASM \
   "s_add_u32 s40, %[so], 0\n\t" "buffer_load_dwordx4 a[0:3], %[o], %[rs], s40 offen offset:0 sc1\n\t" \
   "buffer_load_dwordx4 a[32:35], %[o], %[rs], s40 offen offset:1024 sc1\n\t" \
   "buffer_load_dwordx4 a[64:67], %[o], %[rs], s40 offen offset:2048 sc1\n\t" \
   "buffer_load_dwordx4 a[96:99], %[o], %[rs], s40 offen offset:3072 sc1\n\t" "s_add_u32 s40, %[so], 4096\n\t" \
   "buffer_load_dwordx4 a[128:131], %[o], %[rs], s40 offen offset:0 sc1\n\t" \
   "buffer_load_dwordx4 a[160:163], %[o], %[rs], s40 offen offset:1024 sc1\n\t" \
   "buffer_load_dwordx4 a[192:195], %[o], %[rs], s40 offen offset:2048 sc1\n\t" \
   "buffer_load_dwordx4 a[224:227], %[o], %[rs], s40 offen offset:3072 sc1\n\t" "s_add_u32 s40, %[so], 32768\n\t" \
   "buffer_load_dwordx4 a[4:7], %[o], %[rs], s40 offen offset:0 sc1\n\t" "buffer_load_dwordx4 a[36:39], %[o], %[rs], s40 offen offset:1024 sc1\n\t" \
   "buffer_load_dwordx4 a[68:71], %[o], %[rs], s40 offen offset:2048 sc1\n\t" \
   "buffer_load_dwordx4 a[100:103], %[o], %[rs], s40 offen offset:3072 sc1\n\t" "s_add_u32 s40, %[so], 36864\n\t" \
   "buffer_load_dwordx4 a[132:135], %[o], %[rs], s40 offen offset:0 sc1\n\t" \
   "buffer_load_dwordx4 a[164:167], %[o], %[rs], s40 offen offset:1024 sc1\n\t" \
   "buffer_load_dwordx4 a[196:199], %[o], %[rs], s40 offen offset:2048 sc1\n\t" \
   "buffer_load_dwordx4 a[228:231], %[o], %[rs], s40 offen offset:3072 sc1\n\t" "s_add_u32 s40, %[so], 65536\n\t" \
   "buffer_load_dwordx4 a[8:11], %[o], %[rs], s40 offen offset:0 sc1\n\t" "buffer_load_dwordx4 a[40:43], %[o], %[rs], s40 offen offset:1024 sc1\n\t" \
   "buffer_load_dwordx4 a[72:75], %[o], %[rs], s40 offen offset:2048 sc1\n\t" \
   "buffer_load_dwordx4 a[104:107], %[o], %[rs], s40 offen offset:3072 sc1\n\t" "s_add_u32 s40, %[so], 69632\n\t" \
   "buffer_load_dwordx4 a[136:139], %[o], %[rs], s40 offen offset:0 sc1\n\t" \
   "buffer_load_dwordx4 a[168:171], %[o], %[rs], s40 offen offset:1024 sc1\n\t" \
   "buffer_load_dwordx4 a[200:203], %[o], %[rs], s40 offen offset:2048 sc1\n\t" \
   "buffer_load_dwordx4 a[232:235], %[o], %[rs], s40 offen offset:3072 sc1\n\t" "s_add_u32 s40, %[so], 98304\n\t" \
   "buffer_load_dwordx4 a[12:15], %[o], %[rs], s40 offen offset:0 sc1\n\t" "buffer_load_dwordx4 a[44:47], %[o], %[rs], s40 offen offset:1024 sc1\n\t" \
   "buffer_load_dwordx4 a[76:79], %[o], %[rs], s40 offen offset:2048 sc1\n\t" \
   "buffer_load_dwordx4 a[108:111], %[o], %[rs], s40 offen offset:3072 sc1\n\t" "s_add_u32 s40, %[so], 102400\n\t" \
   "buffer_load_dwordx4 a[140:143], %[o], %[rs], s40 offen offset:0 sc1\n\t" \
   "buffer_load_dwordx4 a[172:175], %[o], %[rs], s40 offen offset:1024 sc1\n\t" \
   "buffer_load_dwordx4 a[204:207], %[o], %[rs], s40 offen offset:2048 sc1\n\t" \
   "buffer_load_dwordx4 a[236:239], %[o], %[rs], s40 offen offset:3072 sc1\n\t" "s_add_u32 s40, %[so], 131072\n\t" \
   "buffer_load_dwordx4 a[16:19], %[o], %[rs], s40 offen offset:0 sc1\n\t" "buffer_load_dwordx4 a[48:51], %[o], %[rs], s40 offen offset:1024 sc1\n\t" \
   "buffer_load_dwordx4 a[80:83], %[o], %[rs], s40 offen offset:2048 sc1\n\t" \
   "buffer_load_dwordx4 a[112:115], %[o], %[rs], s40 offen offset:3072 sc1\n\t" "s_add_u32 s40, %[so], 135168\n\t" \
   "buffer_load_dwordx4 a[144:147], %[o], %[rs], s40 offen offset:0 sc1\n\t" \
   "buffer_load_dwordx4 a[176:179], %[o], %[rs], s40 offen offset:1024 sc1\n\t" \
   "buffer_load_dwordx4 a[208:211], %[o], %[rs], s40 offen offset:2048 sc1\n\t" \
   "buffer_load_dwordx4 a[240:243], %[o], %[rs], s40 offen offset:3072 sc1\n\t" "s_add_u32 s40, %[so], 163840\n\t" \
   "buffer_load_dwordx4 a[20:23], %[o], %[rs], s40 offen offset:0 sc1\n\t" "buffer_load_dwordx4 a[52:55], %[o], %[rs], s40 offen offset:1024 sc1\n\t" \
   "buffer_load_dwordx4 a[84:87], %[o], %[rs], s40 offen offset:2048 sc1\n\t" \
   "buffer_load_dwordx4 a[116:119], %[o], %[rs], s40 offen offset:3072 sc1\n\t" "s_add_u32 s40, %[so], 167936\n\t" \
   "buffer_load_dwordx4 a[148:151], %[o], %[rs], s40 offen offset:0 sc1\n\t" \
   "buffer_load_dwordx4 a[180:183], %[o], %[rs], s40 offen offset:1024 sc1\n\t" \
   "buffer_load_dwordx4 a[212:215], %[o], %[rs], s40 offen offset:2048 sc1\n\t" \
   "buffer_load_dwordx4 a[244:247], %[o], %[rs], s40 offen offset:3072 sc1\n\t" "s_add_u32 s40, %[so], 196608\n\t" \
   "buffer_load_dwordx4 a[24:27], %[o], %[rs], s40 offen offset:0 sc1\n\t" "buffer_load_dwordx4 a[56:59], %[o], %[rs], s40 offen offset:1024 sc1\n\t" \
   "buffer_load_dwordx4 a[88:91], %[o], %[rs], s40 offen offset:2048 sc1\n\t" \
   "buffer_load_dwordx4 a[120:123], %[o], %[rs], s40 offen offset:3072 sc1\n\t" "s_add_u32 s40, %[so], 200704\n\t" \
   "buffer_load_dwordx4 a[152:155], %[o], %[rs], s40 offen offset:0 sc1\n\t" \
   "buffer_load_dwordx4 a[184:187], %[o], %[rs], s40 offen offset:1024 sc1\n\t" \
   "buffer_load_dwordx4 a[216:219], %[o], %[rs], s40 offen offset:2048 sc1\n\t" \
   "buffer_load_dwordx4 a[248:251], %[o], %[rs], s40 offen offset:3072 sc1\n\t" "s_add_u32 s40, %[so], 229376\n\t" \
   "buffer_load_dwordx4 a[28:31], %[o], %[rs], s40 offen offset:0 sc1\n\t" "buffer_load_dwordx4 a[60:63], %[o], %[rs], s40 offen offset:1024 sc1\n\t" \
   "buffer_load_dwordx4 a[92:95], %[o], %[rs], s40 offen offset:2048 sc1\n\t" \
   "buffer_load_dwordx4 a[124:127], %[o], %[rs], s40 offen offset:3072 sc1\n\t" "s_add_u32 s40, %[so], 233472\n\t" \
   "buffer_load_dwordx4 a[156:159], %[o], %[rs], s40 offen offset:0 sc1\n\t" \
   "buffer_load_dwordx4 a[188:191], %[o], %[rs], s40 offen offset:1024 sc1\n\t" \
   "buffer_load_dwordx4 a[220:223], %[o], %[rs], s40 offen offset:2048 sc1\n\t" \
   "buffer_load_dwordx4 a[252:255], %[o], %[rs], s40 offen offset:3072 sc1\n\t" "s_waitcnt vmcnt(0)"
#define OU_CLOB "s40"
#define OY_ASM \
   "s_add_u32 s40, %[sy], 0\n\t" "buffer_load_dwordx4 v[192:195], %[yo], %[rs], s40 offen offset:0 sc1\n\t" "s_add_u32 s40, %[sy], 16384\n\t" \
   "buffer_load_dwordx4 v[196:199], %[yo], %[rs], s40 offen offset:0 sc1\n\t" "s_add_u32 s40, %[sy], 32768\n\t" \
   "buffer_load_dwordx4 v[200:203], %[yo], %[rs], s40 offen offset:0 sc1\n\t" "s_add_u32 s40, %[sy], 49152\n\t" \
   "buffer_load_dwordx4 v[204:207], %[yo], %[rs], s40 offen offset:0 sc1\n\t" "s_add_u32 s40, %[sy], 65536\n\t" \
   "buffer_load_dwordx4 v[208:211], %[yo], %[rs], s40 offen offset:0 sc1\n\t" "s_add_u32 s40, %[sy], 81920\n\t" \
   "buffer_load_dwordx4 v[212:215], %[yo], %[rs], s40 offen offset:0 sc1\n\t" "s_add_u32 s40, %[sy], 98304\n\t" \
   "buffer_load_dwordx4 v[216:219], %[yo], %[rs], s40 offen offset:0 sc1\n\t" "s_add_u32 s40, %[sy], 114688\n\t" \
   "buffer_load_dwordx4 v[220:223], %[yo], %[rs], s40 offen offset:0 sc1\n\t" "s_add_u32 s40, %[sy], 0\n\t" \
   "buffer_load_dwordx4 v[224:227], %[yo], %[rs], s40 offen offset:1024 sc1\n\t" "s_add_u32 s40, %[sy], 16384\n\t" \
   "buffer_load_dwordx4 v[228:231], %[yo], %[rs], s40 offen offset:1024 sc1\n\t" "s_add_u32 s40, %[sy], 32768\n\t" \
   "buffer_load_dwordx4 v[232:235], %[yo], %[rs], s40 offen offset:1024 sc1\n\t" "s_add_u32 s40, %[sy], 49152\n\t" \
   "buffer_load_dwordx4 v[236:239], %[yo], %[rs], s40 offen offset:1024 sc1\n\t" "s_add_u32 s40, %[sy], 65536\n\t" \
   "buffer_load_dwordx4 v[240:243], %[yo], %[rs], s40 offen offset:1024 sc1\n\t" "s_add_u32 s40, %[sy], 81920\n\t" \
   "buffer_load_dwordx4 v[244:247], %[yo], %[rs], s40 offen offset:1024 sc1\n\t" "s_add_u32 s40, %[sy], 98304\n\t" \
   "buffer_load_dwordx4 v[248:251], %[yo], %[rs], s40 offen offset:1024 sc1\n\t" "s_add_u32 s40, %[sy], 114688\n\t" \
   "buffer_load_dwordx4 v[252:255], %[yo], %[rs], s40 offen offset:1024 sc1\n\t" "v_mov_b32 %[m], 0\n\t" "s_waitcnt vmcnt(15)\n\t" \
   "v_max3_u32 %[m], %[m], v192, v194\n\t" "s_waitcnt vmcnt(14)\n\t" "v_max3_u32 %[m], %[m], v196, v198\n\t" "s_waitcnt vmcnt(13)\n\t" \
   "v_max3_u32 %[m], %[m], v200, v202\n\t" "s_waitcnt vmcnt(12)\n\t" "v_max3_u32 %[m], %[m], v204, v206\n\t" "s_waitcnt vmcnt(11)\n\t" \
   "v_max3_u32 %[m], %[m], v208, v210\n\t" "s_waitcnt vmcnt(10)\n\t" "v_max3_u32 %[m], %[m], v212, v214\n\t" "s_waitcnt vmcnt(9)\n\t" \
   "v_max3_u32 %[m], %[m], v216, v218\n\t" "s_waitcnt vmcnt(8)\n\t" "v_max3_u32 %[m], %[m], v220, v222\n\t" "s_waitcnt vmcnt(7)\n\t" \
   "v_max3_u32 %[m], %[m], v224, v226\n\t" "s_waitcnt vmcnt(6)\n\t" "v_max3_u32 %[m], %[m], v228, v230\n\t" "s_waitcnt vmcnt(5)\n\t" \
   "v_max3_u32 %[m], %[m], v232, v234\n\t" "s_waitcnt vmcnt(4)\n\t" "v_max3_u32 %[m], %[m], v236, v238\n\t" "s_waitcnt vmcnt(3)\n\t" \
   "v_max3_u32 %[m], %[m], v240, v242\n\t" "s_waitcnt vmcnt(2)\n\t" "v_max3_u32 %[m], %[m], v244, v246\n\t" "s_waitcnt vmcnt(1)\n\t" \
   "v_max3_u32 %[m], %[m], v248, v250\n\t" "s_waitcnt vmcnt(0)\n\t" "v_max3_u32 %[m], %[m], v252, v254\n\t" "s_nop 0"
#define OY_CLOB "s40"
#define O1_ASM \
   "s_add_u32 s40, %[sp], 0\n\t" "buffer_load_dwordx4 v[112:115], %[po], %[rs], s40 offen offset:0 sc1\n\t" \
   "buffer_load_dwordx4 v[116:119], %[po], %[rs], s40 offen offset:2048 sc1\n\t" "s_add_u32 s40, %[sp], 4096\n\t" \
   "buffer_load_dwordx4 v[120:123], %[po], %[rs], s40 offen offset:0 sc1\n\t" \
   "buffer_load_dwordx4 v[124:127], %[po], %[rs], s40 offen offset:2048 sc1\n\t" "s_add_u32 s40, %[sp], 8192\n\t" \
   "buffer_load_dwordx4 v[128:131], %[po], %[rs], s40 offen offset:0 sc1\n\t" \
   "buffer_load_dwordx4 v[132:135], %[po], %[rs], s40 offen offset:2048 sc1\n\t" "s_add_u32 s40, %[sp], 12288\n\t" \
   "buffer_load_dwordx4 v[136:139], %[po], %[rs], s40 offen offset:0 sc1\n\t" \
   "buffer_load_dwordx4 v[140:143], %[po], %[rs], s40 offen offset:2048 sc1\n\t" \
   "buffer_load_dwordx4 v[144:147], %[mo], %[rs], %[sm] offen offset:0 sc1\n\t" \
   "buffer_load_dwordx4 v[148:151], %[mo], %[rs], %[sm] offen offset:2048 sc1\n\t" "s_waitcnt vmcnt(9)\n\t" \
   "v_mfma_f32_16x16x32_bf16 v[32:35], a[0:3], v[112:115], 0\n\t" "v_mfma_f32_16x16x32_bf16 v[36:39], a[4:7], v[112:115], 0\n\t" \
   "v_mfma_f32_16x16x32_bf16 v[40:43], a[8:11], v[112:115], 0\n\t" "v_mfma_f32_16x16x32_bf16 v[44:47], a[12:15], v[112:115], 0\n\t" \
   "v_mfma_f32_16x16x32_bf16 v[48:51], a[16:19], v[112:115], 0\n\t" "v_mfma_f32_16x16x32_bf16 v[52:55], a[20:23], v[112:115], 0\n\t" \
   "v_mfma_f32_16x16x32_bf16 v[56:59], a[24:27], v[112:115], 0\n\t" "v_mfma_f32_16x16x32_bf16 v[60:63], a[28:31], v[112:115], 0\n\t" \
   "s_waitcnt vmcnt(8)\n\t" "v_mfma_f32_16x16x32_bf16 v[32:35], a[32:35], v[116:119], v[32:35]\n\t" \
   "v_mfma_f32_16x16x32_bf16 v[36:39], a[36:39], v[116:119], v[36:39]\n\t" "v_mfma_f32_16x16x32_bf16 v[40:43], a[40:43], v[116:119], v[40:43]\n\t" \
   "v_mfma_f32_16x16x32_bf16 v[44:47], a[44:47], v[116:119], v[44:47]\n\t" "v_mfma_f32_16x16x32_bf16 v[48:51], a[48:51], v[116:119], v[48:51]\n\t" \
   "v_mfma_f32_16x16x32_bf16 v[52:55], a[52:55], v[116:119], v[52:55]\n\t" "v_mfma_f32_16x16x32_bf16 v[56:59], a[56:59], v[116:119], v[56:59]\n\t" \
   "v_mfma_f32_16x16x32_bf16 v[60:63], a[60:63], v[116:119], v[60:63]\n\t" "s_waitcnt vmcnt(7)\n\t" \
   "v_mfma_f32_16x16x32_bf16 v[32:35], a[64:67], v[120:123], v[32:35]\n\t" "v_mfma_f32_16x16x32_bf16 v[36:39], a[68:71], v[120:123], v[36:39]\n\t" \
   "v_mfma_f32_16x16x32_bf16 v[40:43], a[72:75], v[120:123], v[40:43]\n\t" "v_mfma_f32_16x16x32_bf16 v[44:47], a[76:79], v[120:123], v[44:47]\n\t" \
   "v_mfma_f32_16x16x32_bf16 v[48:51], a[80:83], v[120:123], v[48:51]\n\t" "v_mfma_f32_16x16x32_bf16 v[52:55], a[84:87], v[120:123], v[52:55]\n\t" \
   "v_mfma_f32_16x16x32_bf16 v[56:59], a[88:91], v[120:123], v[56:59]\n\t" "v_mfma_f32_16x16x32_bf16 v[60:63], a[92:95], v[120:123], v[60:63]\n\t" \
   "s_waitcnt vmcnt(6)\n\t" "v_mfma_f32_16x16x32_bf16 v[32:35], a[96:99], v[124:127], v[32:35]\n\t" \
   "v_mfma_f32_16x16x32_bf16 v[36:39], a[100:103], v[124:127], v[36:39]\n\t" "v_mfma_f32_16x16x32_bf16 v[40:43], a[104:107], v[124:127], v[40:43]\n\t" \
   "v_mfma_f32_16x16x32_bf16 v[44:47], a[108:111], v[124:127], v[44:47]\n\t" "v_mfma_f32_16x16x32_bf16 v[48:51], a[112:115], v[124:127], v[48:51]\n\t" \
   "v_mfma_f32_16x16x32_bf16 v[52:55], a[116:119], v[124:127], v[52:55]\n\t" "v_mfma_f32_16x16x32_bf16 v[56:59], a[120:123], v[124:127], v[56:59]\n\t" \
   "v_mfma_f32_16x16x32_bf16 v[60:63], a[124:127], v[124:127], v[60:63]\n\t" "s_waitcnt vmcnt(5)\n\t" \
   "v_mfma_f32_16x16x32_bf16 v[32:35], a[128:131], v[128:131], v[32:35]\n\t" "v_mfma_f32_16x16x32_bf16 v[36:39], a[132:135], v[128:131], v[36:39]\n\t" \
   "v_mfma_f32_16x16x32_bf16 v[40:43], a[136:139], v[128:131], v[40:43]\n\t" "v_mfma_f32_16x16x32_bf16 v[44:47], a[140:143], v[128:131], v[44:47]\n\t" \
   "v_mfma_f32_16x16x32_bf16 v[48:51], a[144:147], v[128:131], v[48:51]\n\t" "v_mfma_f32_16x16x32_bf16 v[52:55], a[148:151], v[128:131], v[52:55]\n\t" \
   "v_mfma_f32_16x16x32_bf16 v[56:59], a[152:155], v[128:131], v[56:59]\n\t" "v_mfma_f32_16x16x32_bf16 v[60:63], a[156:159], v[128:131], v[60:63]\n\t" \
   "s_waitcnt vmcnt(4)\n\t" "v_mfma_f32_16x16x32_bf16 v[32:35], a[160:163], v[132:135], v[32:35]\n\t" \
   "v_mfma_f32_16x16x32_bf16 v[36:39], a[164:167], v[132:135], v[36:39]\n\t" "v_mfma_f32_16x16x32_bf16 v[40:43], a[168:171], v[132:135], v[40:43]\n\t" \
   "v_mfma_f32_16x16x32_bf16 v[44:47], a[172:175], v[132:135], v[44:47]\n\t" "v_mfma_f32_16x16x32_bf16 v[48:51], a[176:179], v[132:135], v[48:51]\n\t" \
   "v_mfma_f32_16x16x32_bf16 v[52:55], a[180:183], v[132:135], v[52:55]\n\t" "v_mfma_f32_16x16x32_bf16 v[56:59], a[184:187], v[132:135], v[56:59]\n\t" \
   "v_mfma_f32_16x16x32_bf16 v[60:63], a[188:191], v[132:135], v[60:63]\n\t" "s_waitcnt vmcnt(3)\n\t" \
   "v_mfma_f32_16x16x32_bf16 v[32:35], a[192:195], v[136:139], v[32:35]\n\t" "v_mfma_f32_16x16x32_bf16 v[36:39], a[196:199], v[136:139], v[36:39]\n\t" \
   "v_mfma_f32_16x16x32_bf16 v[40:43], a[200:203], v[136:139], v[40:43]\n\t" "v_mfma_f32_16x16x32_bf16 v[44:47], a[204:207], v[136:139], v[44:47]\n\t" \
   "v_mfma_f32_16x16x32_bf16 v[48:51], a[208:211], v[136:139], v[48:51]\n\t" "v_mfma_f32_16x16x32_bf16 v[52:55], a[212:215], v[136:139], v[52:55]\n\t" \
   "v_mfma_f32_16x16x32_bf16 v[56:59], a[216:219], v[136:139], v[56:59]\n\t" "v_mfma_f32_16x16x32_bf16 v[60:63], a[220:223], v[136:139], v[60:63]\n\t" \
   "s_waitcnt vmcnt(2)\n\t" "s_waitcnt vmcnt(0)\n\t" "v_mfma_f32_16x16x32_bf16 v[32:35], a[224:227], v[140:143], v[32:35]\n\t" \
   "v_swap_b32 v145, v146\n\t" "v_mfma_f32_16x16x32_bf16 v[36:39], a[228:231], v[140:143], v[36:39]\n\t" "v_swap_b32 v149, v150\n\t" \
   "v_mfma_f32_16x16x32_bf16 v[40:43], a[232:235], v[140:143], v[40:43]\n\t" "v_mfma_f32_16x16x32_bf16 v[44:47], a[236:239], v[140:143], v[44:47]\n\t" \
   "v_mfma_f32_16x16x32_bf16 v[48:51], a[240:243], v[140:143], v[48:51]\n\t" "v_mfma_f32_16x16x32_bf16 v[52:55], a[244:247], v[140:143], v[52:55]\n\t" \
   "v_mfma_f32_16x16x32_bf16 v[56:59], a[248:251], v[140:143], v[56:59]\n\t" "v_mfma_f32_16x16x32_bf16 v[60:63], a[252:255], v[140:143], v[60:63]\n\t" \
   "v_mfma_f32_16x16x32_bf16 v[32:35], v[192:195], v[144:147], v[32:35]\n\t" "v_mfma_f32_16x16x32_bf16 v[36:39], v[196:199], v[144:147], v[36:39]\n\t" \
   "v_mfma_f32_16x16x32_bf16 v[40:43], v[200:203], v[144:147], v[40:43]\n\t" "v_mfma_f32_16x16x32_bf16 v[44:47], v[204:207], v[144:147], v[44:47]\n\t" \
   "v_mfma_f32_16x16x32_bf16 v[48:51], v[208:211], v[144:147], v[48:51]\n\t" "v_mfma_f32_16x16x32_bf16 v[52:55], v[212:215], v[144:147], v[52:55]\n\t" \
   "v_mfma_f32_16x16x32_bf16 v[56:59], v[216:219], v[144:147], v[56:59]\n\t" "v_mfma_f32_16x16x32_bf16 v[60:63], v[220:223], v[144:147], v[60:63]\n\t" \
   "v_mfma_f32_16x16x32_bf16 v[32:35], v[224:227], v[148:151], v[32:35]\n\t" "v_mfma_f32_16x16x32_bf16 v[36:39], v[228:231], v[148:151], v[36:39]\n\t" \
   "v_mfma_f32_16x16x32_bf16 v[40:43], v[232:235], v[148:151], v[40:43]\n\t" "v_mfma_f32_16x16x32_bf16 v[44:47], v[236:239], v[148:151], v[44:47]\n\t" \
   "ds_write_b128 %[lw], v[32:35] offset:0\n\t" "v_mfma_f32_16x16x32_bf16 v[48:51], v[240:243], v[148:151], v[48:51]\n\t" \
   "ds_write_b128 %[lw], v[36:39] offset:1024\n\t" "v_mfma_f32_16x16x32_bf16 v[52:55], v[244:247], v[148:151], v[52:55]\n\t" \
   "ds_write_b128 %[lw], v[40:43] offset:2048\n\t" "v_mfma_f32_16x16x32_bf16 v[56:59], v[248:251], v[148:151], v[56:59]\n\t" \
   "ds_write_b128 %[lw], v[44:47] offset:3072\n\t" "v_mfma_f32_16x16x32_bf16 v[60:63], v[252:255], v[148:151], v[60:63]\n\t" \
   "ds_write_b128 %[lw], v[48:51] offset:4096\n\t" "s_nop 1\n\t" "s_nop 3\n\t" "ds_write_b128 %[lw], v[52:55] offset:5120\n\t" "s_nop 3\n\t" \
   "ds_write_b128 %[lw], v[56:59] offset:6144\n\t" "s_nop 3\n\t" "ds_write_b128 %[lw], v[60:63] offset:7168\n\t" "s_waitcnt lgkmcnt(0)\n\t" \
   "s_barrier\n\t" "ds_read_b128 v[112:115], %[lr] offset:0\n\t" "ds_read_b128 v[116:119], %[lr] offset:256\n\t" \
   "ds_read_b128 v[120:123], %[lr] offset:8192\n\t" "ds_read_b128 v[124:127], %[lr] offset:8448\n\t" "ds_read_b128 v[128:131], %[lr] offset:16384\n\t" \
   "ds_read_b128 v[132:135], %[lr] offset:16640\n\t" "ds_read_b128 v[136:139], %[lr] offset:24576\n\t" \
   "ds_read_b128 v[140:143], %[lr] offset:24832\n\t" "s_waitcnt lgkmcnt(4)\n\t" "v_add_f32 v112, v112, v120\n\t" "v_add_f32 v113, v113, v121\n\t" \
   "v_add_f32 v114, v114, v122\n\t" "v_add_f32 v115, v115, v123\n\t" "v_add_f32 v116, v116, v124\n\t" "v_add_f32 v117, v117, v125\n\t" \
   "v_add_f32 v118, v118, v126\n\t" "v_add_f32 v119, v119, v127\n\t" "s_waitcnt lgkmcnt(0)\n\t" "v_add_f32 v128, v128, v136\n\t" \
   "v_add_f32 v129, v129, v137\n\t" "v_add_f32 v130, v130, v138\n\t" "v_add_f32 v131, v131, v139\n\t" "v_add_f32 v132, v132, v140\n\t" \
   "v_add_f32 v133, v133, v141\n\t" "v_add_f32 v134, v134, v142\n\t" "v_add_f32 v135, v135, v143\n\t" "v_add_f32 v112, v112, v128\n\t" \
   "v_add_f32 v113, v113, v129\n\t" "v_add_f32 v114, v114, v130\n\t" "v_add_f32 v115, v115, v131\n\t" "v_add_f32 v116, v116, v132\n\t" \
   "v_add_f32 v117, v117, v133\n\t" "v_add_f32 v118, v118, v134\n\t" "v_add_f32 v119, v119, v135\n\t" "v_cvt_pk_bf16_f32 v96, v112, v113\n\t" \
   "v_cvt_pk_bf16_f32 v97, v114, v115\n\t" "v_cvt_pk_bf16_f32 v98, v116, v117\n\t" "v_cvt_pk_bf16_f32 v99, v118, v119\n\t" "s_nop 1"
#define O1_CLOB "v32", "v33", "v34", "v35", "v36", "v37", "v38", "v39", "v40", "v41", "v42", "v43", "v44", "v45", "v46", "v47", "v48", "v49", "v50", "v51", "v52", "v53", "v54", "v55", "v56", "v57", "v58", "v59", "v60", "v61", "v62", "v63", "v64", "v65", "v66", "v67", "v68", "v69", "v70", "v71", "v72", "v73", "v74", "v75", "v76", "v77", "v78", "v79", "v80", "v81", "v82", "v83", "v84", "v85", "v86", "v87", "v88", "v89", "v90", "v91", "v92", "v93", "v94", "v95", "v112", "v113", "v114", "v115", "v116", "v117", "v118", "v119", "v120", "v121", "v122", "v123", "v124", "v125", "v126", "v127", "v128", "v129", "v130", "v131", "v132", "v133", "v134", "v135", "v136", "v137", "v138", "v139", "v140", "v141", "v142", "v143", "v144", "v145", "v146", "v147", "v148", "v149", "v150", "v151", "v152", "v153", "v154", "v155", "v156", "v157", "v158", "v159", "v160", "v161", "v162", "v163", "v164", "v165", "v166", "v167", "v168", "v169", "v170", "v171", "v172", "v173", "v174", "v175", "v176", "v177", "v178", "v179", "v180", "v181", "v182", "v183", "v184", "v185", "v186", "v187", "v188", "v189", "v190", "v191", "s40"
#define O2_ASM \
   "s_add_u32 s40, %[sp], 0\n\t" "buffer_load_dwordx4 v[112:115], %[po], %[rs], s40 offen offset:0 sc1\n\t" \
   "buffer_load_dwordx4 v[116:119], %[po], %[rs], s40 offen offset:2048 sc1\n\t" "s_add_u32 s40, %[sp], 4096\n\t" \
   "buffer_load_dwordx4 v[120:123], %[po], %[rs], s40 offen offset:0 sc1\n\t" \
   "buffer_load_dwordx4 v[124:127], %[po], %[rs], s40 offen offset:2048 sc1\n\t" "s_add_u32 s40, %[sp], 8192\n\t" \
   "buffer_load_dwordx4 v[128:131], %[po], %[rs], s40 offen offset:0 sc1\n\t" \
   "buffer_load_dwordx4 v[132:135], %[po], %[rs], s40 offen offset:2048 sc1\n\t" "s_add_u32 s40, %[sp], 12288\n\t" \
   "buffer_load_dwordx4 v[136:139], %[po], %[rs], s40 offen offset:0 sc1\n\t" \
   "buffer_load_dwordx4 v[140:143], %[po], %[rs], s40 offen offset:2048 sc1\n\t" "s_add_u32 s40, %[sp], 0\n\t" \
   "buffer_load_dwordx4 v[152:155], %[po], %[rs], s40 offen offset:1024 sc1\n\t" \
   "buffer_load_dwordx4 v[156:159], %[po], %[rs], s40 offen offset:3072 sc1\n\t" "s_add_u32 s40, %[sp], 4096\n\t" \
   "buffer_load_dwordx4 v[160:163], %[po], %[rs], s40 offen offset:1024 sc1\n\t" \
   "buffer_load_dwordx4 v[164:167], %[po], %[rs], s40 offen offset:3072 sc1\n\t" "s_add_u32 s40, %[sp], 8192\n\t" \
   "buffer_load_dwordx4 v[168:171], %[po], %[rs], s40 offen offset:1024 sc1\n\t" \
   "buffer_load_dwordx4 v[172:175], %[po], %[rs], s40 offen offset:3072 sc1\n\t" "s_add_u32 s40, %[sp], 12288\n\t" \
   "buffer_load_dwordx4 v[176:179], %[po], %[rs], s40 offen offset:1024 sc1\n\t" \
   "buffer_load_dwordx4 v[180:183], %[po], %[rs], s40 offen offset:3072 sc1\n\t" \
   "buffer_load_dwordx4 v[144:147], %[mo], %[rs], %[sm] offen offset:0 sc1\n\t" \
   "buffer_load_dwordx4 v[148:151], %[mo], %[rs], %[sm] offen offset:2048 sc1\n\t" \
   "buffer_load_dwordx4 v[184:187], %[mo], %[rs], %[sm] offen offset:1024 sc1\n\t" \
   "buffer_load_dwordx4 v[188:191], %[mo], %[rs], %[sm] offen offset:3072 sc1\n\t" "s_add_u32 s40, %[sy], 0\n\t" \
   "buffer_load_dwordx4 v[192:195], %[yo], %[rs], s40 offen offset:0 sc1\n\t" "s_add_u32 s40, %[sy], 16384\n\t" \
   "buffer_load_dwordx4 v[196:199], %[yo], %[rs], s40 offen offset:0 sc1\n\t" "s_add_u32 s40, %[sy], 32768\n\t" \
   "buffer_load_dwordx4 v[200:203], %[yo], %[rs], s40 offen offset:0 sc1\n\t" "s_add_u32 s40, %[sy], 49152\n\t" \
   "buffer_load_dwordx4 v[204:207], %[yo], %[rs], s40 offen offset:0 sc1\n\t" "s_add_u32 s40, %[sy], 65536\n\t" \
   "buffer_load_dwordx4 v[208:211], %[yo], %[rs], s40 offen offset:0 sc1\n\t" "s_add_u32 s40, %[sy], 81920\n\t" \
   "buffer_load_dwordx4 v[212:215], %[yo], %[rs], s40 offen offset:0 sc1\n\t" "s_add_u32 s40, %[sy], 98304\n\t" \
   "buffer_load_dwordx4 v[216:219], %[yo], %[rs], s40 offen offset:0 sc1\n\t" "s_add_u32 s40, %[sy], 114688\n\t" \
   "buffer_load_dwordx4 v[220:223], %[yo], %[rs], s40 offen offset:0 sc1\n\t" "s_add_u32 s40, %[sy], 0\n\t" \
   "buffer_load_dwordx4 v[224:227], %[yo], %[rs], s40 offen offset:1024 sc1\n\t" "s_add_u32 s40, %[sy], 16384\n\t" \
   "buffer_load_dwordx4 v[228:231], %[yo], %[rs], s40 offen offset:1024 sc1\n\t" "s_add_u32 s40, %[sy], 32768\n\t" \
   "buffer_load_dwordx4 v[232:235], %[yo], %[rs], s40 offen offset:1024 sc1\n\t" "s_add_u32 s40, %[sy], 49152\n\t" \
   "buffer_load_dwordx4 v[236:239], %[yo], %[rs], s40 offen offset:1024 sc1\n\t" "s_add_u32 s40, %[sy], 65536\n\t" \
   "buffer_load_dwordx4 v[240:243], %[yo], %[rs], s40 offen offset:1024 sc1\n\t" "s_add_u32 s40, %[sy], 81920\n\t" \
   "buffer_load_dwordx4 v[244:247], %[yo], %[rs], s40 offen offset:1024 sc1\n\t" "s_add_u32 s40, %[sy], 98304\n\t" \
   "buffer_load_dwordx4 v[248:251], %[yo], %[rs], s40 offen offset:1024 sc1\n\t" "s_add_u32 s40, %[sy], 114688\n\t" \
   "buffer_load_dwordx4 v[252:255], %[yo], %[rs], s40 offen offset:1024 sc1\n\t" "s_waitcnt vmcnt(35)\n\t" \
   "v_mfma_f32_16x16x32_bf16 v[32:35], a[0:3], v[112:115], 0\n\t" "v_mfma_f32_16x16x32_bf16 v[36:39], a[4:7], v[112:115], 0\n\t" \
   "v_mfma_f32_16x16x32_bf16 v[40:43], a[8:11], v[112:115], 0\n\t" "v_mfma_f32_16x16x32_bf16 v[44:47], a[12:15], v[112:115], 0\n\t" \
   "v_mfma_f32_16x16x32_bf16 v[48:51], a[16:19], v[112:115], 0\n\t" "v_mfma_f32_16x16x32_bf16 v[52:55], a[20:23], v[112:115], 0\n\t" \
   "v_mfma_f32_16x16x32_bf16 v[56:59], a[24:27], v[112:115], 0\n\t" "v_mfma_f32_16x16x32_bf16 v[60:63], a[28:31], v[112:115], 0\n\t" \
   "s_waitcnt vmcnt(34)\n\t" "v_mfma_f32_16x16x32_bf16 v[32:35], a[32:35], v[116:119], v[32:35]\n\t" \
   "v_mfma_f32_16x16x32_bf16 v[36:39], a[36:39], v[116:119], v[36:39]\n\t" "v_mfma_f32_16x16x32_bf16 v[40:43], a[40:43], v[116:119], v[40:43]\n\t" \
   "v_mfma_f32_16x16x32_bf16 v[44:47], a[44:47], v[116:119], v[44:47]\n\t" "v_mfma_f32_16x16x32_bf16 v[48:51], a[48:51], v[116:119], v[48:51]\n\t" \
   "v_mfma_f32_16x16x32_bf16 v[52:55], a[52:55], v[116:119], v[52:55]\n\t" "v_mfma_f32_16x16x32_bf16 v[56:59], a[56:59], v[116:119], v[56:59]\n\t" \
   "v_mfma_f32_16x16x32_bf16 v[60:63], a[60:63], v[116:119], v[60:63]\n\t" "s_waitcnt vmcnt(33)\n\t" \
   "v_mfma_f32_16x16x32_bf16 v[32:35], a[64:67], v[120:123], v[32:35]\n\t" "v_mfma_f32_16x16x32_bf16 v[36:39], a[68:71], v[120:123], v[36:39]\n\t" \
   "v_mfma_f32_16x16x32_bf16 v[40:43], a[72:75], v[120:123], v[40:43]\n\t" "v_mfma_f32_16x16x32_bf16 v[44:47], a[76:79], v[120:123], v[44:47]\n\t" \
   "v_mfma_f32_16x16x32_bf16 v[48:51], a[80:83], v[120:123], v[48:51]\n\t" "v_mfma_f32_16x16x32_bf16 v[52:55], a[84:87], v[120:123], v[52:55]\n\t" \
   "v_mfma_f32_16x16x32_bf16 v[56:59], a[88:91], v[120:123], v[56:59]\n\t" "v_mfma_f32_16x16x32_bf16 v[60:63], a[92:95], v[120:123], v[60:63]\n\t" \
   "s_waitcnt vmcnt(32)\n\t" "v_mfma_f32_16x16x32_bf16 v[32:35], a[96:99], v[124:127], v[32:35]\n\t" \
   "v_mfma_f32_16x16x32_bf16 v[36:39], a[100:103], v[124:127], v[36:39]\n\t" "v_mfma_f32_16x16x32_bf16 v[40:43], a[104:107], v[124:127], v[40:43]\n\t" \
   "v_mfma_f32_16x16x32_bf16 v[44:47], a[108:111], v[124:127], v[44:47]\n\t" "v_mfma_f32_16x16x32_bf16 v[48:51], a[112:115], v[124:127], v[48:51]\n\t" \
   "v_mfma_f32_16x16x32_bf16 v[52:55], a[116:119], v[124:127], v[52:55]\n\t" "v_mfma_f32_16x16x32_bf16 v[56:59], a[120:123], v[124:127], v[56:59]\n\t" \
   "v_mfma_f32_16x16x32_bf16 v[60:63], a[124:127], v[124:127], v[60:63]\n\t" "s_waitcnt vmcnt(31)\n\t" \
   "v_mfma_f32_16x16x32_bf16 v[32:35], a[128:131], v[128:131], v[32:35]\n\t" "v_mfma_f32_16x16x32_bf16 v[36:39], a[132:135], v[128:131], v[36:39]\n\t" \
   "v_mfma_f32_16x16x32_bf16 v[40:43], a[136:139], v[128:131], v[40:43]\n\t" "v_mfma_f32_16x16x32_bf16 v[44:47], a[140:143], v[128:131], v[44:47]\n\t" \
   "v_mfma_f32_16x16x32_bf16 v[48:51], a[144:147], v[128:131], v[48:51]\n\t" "v_mfma_f32_16x16x32_bf16 v[52:55], a[148:151], v[128:131], v[52:55]\n\t" \
   "v_mfma_f32_16x16x32_bf16 v[56:59], a[152:155], v[128:131], v[56:59]\n\t" "v_mfma_f32_16x16x32_bf16 v[60:63], a[156:159], v[128:131], v[60:63]\n\t" \
   "s_waitcnt vmcnt(30)\n\t" "v_mfma_f32_16x16x32_bf16 v[32:35], a[160:163], v[132:135], v[32:35]\n\t" \
   "v_mfma_f32_16x16x32_bf16 v[36:39], a[164:167], v[132:135], v[36:39]\n\t" "v_mfma_f32_16x16x32_bf16 v[40:43], a[168:171], v[132:135], v[40:43]\n\t" \
   "v_mfma_f32_16x16x32_bf16 v[44:47], a[172:175], v[132:135], v[44:47]\n\t" "v_mfma_f32_16x16x32_bf16 v[48:51], a[176:179], v[132:135], v[48:51]\n\t" \
   "v_mfma_f32_16x16x32_bf16 v[52:55], a[180:183], v[132:135], v[52:55]\n\t" "v_mfma_f32_16x16x32_bf16 v[56:59], a[184:187], v[132:135], v[56:59]\n\t" \
   "v_mfma_f32_16x16x32_bf16 v[60:63], a[188:191], v[132:135], v[60:63]\n\t" "s_waitcnt vmcnt(29)\n\t" \
   "v_mfma_f32_16x16x32_bf16 v[32:35], a[192:195], v[136:139], v[32:35]\n\t" "v_mfma_f32_16x16x32_bf16 v[36:39], a[196:199], v[136:139], v[36:39]\n\t" \
   "v_mfma_f32_16x16x32_bf16 v[40:43], a[200:203], v[136:139], v[40:43]\n\t" "v_mfma_f32_16x16x32_bf16 v[44:47], a[204:207], v[136:139], v[44:47]\n\t" \
   "v_mfma_f32_16x16x32_bf16 v[48:51], a[208:211], v[136:139], v[48:51]\n\t" "v_mfma_f32_16x16x32_bf16 v[52:55], a[212:215], v[136:139], v[52:55]\n\t" \
   "v_mfma_f32_16x16x32_bf16 v[56:59], a[216:219], v[136:139], v[56:59]\n\t" "v_mfma_f32_16x16x32_bf16 v[60:63], a[220:223], v[136:139], v[60:63]\n\t" \
   "s_waitcnt vmcnt(28)\n\t" "v_mfma_f32_16x16x32_bf16 v[32:35], a[224:227], v[140:143], v[32:35]\n\t" \
   "v_mfma_f32_16x16x32_bf16 v[36:39], a[228:231], v[140:143], v[36:39]\n\t" "v_mfma_f32_16x16x32_bf16 v[40:43], a[232:235], v[140:143], v[40:43]\n\t" \
   "v_mfma_f32_16x16x32_bf16 v[44:47], a[236:239], v[140:143], v[44:47]\n\t" "v_mfma_f32_16x16x32_bf16 v[48:51], a[240:243], v[140:143], v[48:51]\n\t" \
   "v_mfma_f32_16x16x32_bf16 v[52:55], a[244:247], v[140:143], v[52:55]\n\t" "v_mfma_f32_16x16x32_bf16 v[56:59], a[248:251], v[140:143], v[56:59]\n\t" \
   "v_mfma_f32_16x16x32_bf16 v[60:63], a[252:255], v[140:143], v[60:63]\n\t" "s_waitcnt vmcnt(27)\n\t" \
   "v_mfma_f32_16x16x32_bf16 v[64:67], a[0:3], v[152:155], 0\n\t" "v_mfma_f32_16x16x32_bf16 v[68:71], a[4:7], v[152:155], 0\n\t" \
   "v_mfma_f32_16x16x32_bf16 v[72:75], a[8:11], v[152:155], 0\n\t" "v_mfma_f32_16x16x32_bf16 v[76:79], a[12:15], v[152:155], 0\n\t" \
   "v_mfma_f32_16x16x32_bf16 v[80:83], a[16:19], v[152:155], 0\n\t" "v_mfma_f32_16x16x32_bf16 v[84:87], a[20:23], v[152:155], 0\n\t" \
   "v_mfma_f32_16x16x32_bf16 v[88:91], a[24:27], v[152:155], 0\n\t" "v_mfma_f32_16x16x32_bf16 v[92:95], a[28:31], v[152:155], 0\n\t" \
   "s_waitcnt vmcnt(26)\n\t" "v_mfma_f32_16x16x32_bf16 v[64:67], a[32:35], v[156:159], v[64:67]\n\t" \
   "v_mfma_f32_16x16x32_bf16 v[68:71], a[36:39], v[156:159], v[68:71]\n\t" "v_mfma_f32_16x16x32_bf16 v[72:75], a[40:43], v[156:159], v[72:75]\n\t" \
   "v_mfma_f32_16x16x32_bf16 v[76:79], a[44:47], v[156:159], v[76:79]\n\t" "v_mfma_f32_16x16x32_bf16 v[80:83], a[48:51], v[156:159], v[80:83]\n\t" \
   "v_mfma_f32_16x16x32_bf16 v[84:87], a[52:55], v[156:159], v[84:87]\n\t" "v_mfma_f32_16x16x32_bf16 v[88:91], a[56:59], v[156:159], v[88:91]\n\t" \
   "v_mfma_f32_16x16x32_bf16 v[92:95], a[60:63], v[156:159], v[92:95]\n\t" "s_waitcnt vmcnt(25)\n\t" \
   "v_mfma_f32_16x16x32_bf16 v[64:67], a[64:67], v[160:163], v[64:67]\n\t" "v_mfma_f32_16x16x32_bf16 v[68:71], a[68:71], v[160:163], v[68:71]\n\t" \
   "v_mfma_f32_16x16x32_bf16 v[72:75], a[72:75], v[160:163], v[72:75]\n\t" "v_mfma_f32_16x16x32_bf16 v[76:79], a[76:79], v[160:163], v[76:79]\n\t" \
   "v_mfma_f32_16x16x32_bf16 v[80:83], a[80:83], v[160:163], v[80:83]\n\t" "v_mfma_f32_16x16x32_bf16 v[84:87], a[84:87], v[160:163], v[84:87]\n\t" \
   "v_mfma_f32_16x16x32_bf16 v[88:91], a[88:91], v[160:163], v[88:91]\n\t" "v_mfma_f32_16x16x32_bf16 v[92:95], a[92:95], v[160:163], v[92:95]\n\t" \
   "s_waitcnt vmcnt(24)\n\t" "v_mfma_f32_16x16x32_bf16 v[64:67], a[96:99], v[164:167], v[64:67]\n\t" \
   "v_mfma_f32_16x16x32_bf16 v[68:71], a[100:103], v[164:167], v[68:71]\n\t" "v_mfma_f32_16x16x32_bf16 v[72:75], a[104:107], v[164:167], v[72:75]\n\t" \
   "v_mfma_f32_16x16x32_bf16 v[76:79], a[108:111], v[164:167], v[76:79]\n\t" "v_mfma_f32_16x16x32_bf16 v[80:83], a[112:115], v[164:167], v[80:83]\n\t" \
   "v_mfma_f32_16x16x32_bf16 v[84:87], a[116:119], v[164:167], v[84:87]\n\t" "v_mfma_f32_16x16x32_bf16 v[88:91], a[120:123], v[164:167], v[88:91]\n\t" \
   "v_mfma_f32_16x16x32_bf16 v[92:95], a[124:127], v[164:167], v[92:95]\n\t" "s_waitcnt vmcnt(23)\n\t" \
   "v_mfma_f32_16x16x32_bf16 v[64:67], a[128:131], v[168:171], v[64:67]\n\t" "v_mfma_f32_16x16x32_bf16 v[68:71], a[132:135], v[168:171], v[68:71]\n\t" \
   "v_mfma_f32_16x16x32_bf16 v[72:75], a[136:139], v[168:171], v[72:75]\n\t" "v_mfma_f32_16x16x32_bf16 v[76:79], a[140:143], v[168:171], v[76:79]\n\t" \
   "v_mfma_f32_16x16x32_bf16 v[80:83], a[144:147], v[168:171], v[80:83]\n\t" "v_mfma_f32_16x16x32_bf16 v[84:87], a[148:151], v[168:171], v[84:87]\n\t" \
   "v_mfma_f32_16x16x32_bf16 v[88:91], a[152:155], v[168:171], v[88:91]\n\t" "v_mfma_f32_16x16x32_bf16 v[92:95], a[156:159], v[168:171], v[92:95]\n\t" \
   "s_waitcnt vmcnt(22)\n\t" "v_mfma_f32_16x16x32_bf16 v[64:67], a[160:163], v[172:175], v[64:67]\n\t" \
   "v_mfma_f32_16x16x32_bf16 v[68:71], a[164:167], v[172:175], v[68:71]\n\t" "v_mfma_f32_16x16x32_bf16 v[72:75], a[168:171], v[172:175], v[72:75]\n\t" \
   "v_mfma_f32_16x16x32_bf16 v[76:79], a[172:175], v[172:175], v[76:79]\n\t" "v_mfma_f32_16x16x32_bf16 v[80:83], a[176:179], v[172:175], v[80:83]\n\t" \
   "v_mfma_f32_16x16x32_bf16 v[84:87], a[180:183], v[172:175], v[84:87]\n\t" "v_mfma_f32_16x16x32_bf16 v[88:91], a[184:187], v[172:175], v[88:91]\n\t" \
   "v_mfma_f32_16x16x32_bf16 v[92:95], a[188:191], v[172:175], v[92:95]\n\t" "s_waitcnt vmcnt(21)\n\t" \
   "v_mfma_f32_16x16x32_bf16 v[64:67], a[192:195], v[176:179], v[64:67]\n\t" "v_mfma_f32_16x16x32_bf16 v[68:71], a[196:199], v[176:179], v[68:71]\n\t" \
   "v_mfma_f32_16x16x32_bf16 v[72:75], a[200:203], v[176:179], v[72:75]\n\t" "v_mfma_f32_16x16x32_bf16 v[76:79], a[204:207], v[176:179], v[76:79]\n\t" \
   "v_mfma_f32_16x16x32_bf16 v[80:83], a[208:211], v[176:179], v[80:83]\n\t" "v_mfma_f32_16x16x32_bf16 v[84:87], a[212:215], v[176:179], v[84:87]\n\t" \
   "v_mfma_f32_16x16x32_bf16 v[88:91], a[216:219], v[176:179], v[88:91]\n\t" "v_mfma_f32_16x16x32_bf16 v[92:95], a[220:223], v[176:179], v[92:95]\n\t" \
   "s_waitcnt vmcnt(20)\n\t" "s_waitcnt vmcnt(16)\n\t" "v_mfma_f32_16x16x32_bf16 v[64:67], a[224:227], v[180:183], v[64:67]\n\t" \
   "v_swap_b32 v145, v146\n\t" "v_mfma_f32_16x16x32_bf16 v[68:71], a[228:231], v[180:183], v[68:71]\n\t" "v_swap_b32 v149, v150\n\t" \
   "v_mfma_f32_16x16x32_bf16 v[72:75], a[232:235], v[180:183], v[72:75]\n\t" "v_swap_b32 v185, v186\n\t" \
   "v_mfma_f32_16x16x32_bf16 v[76:79], a[236:239], v[180:183], v[76:79]\n\t" "v_swap_b32 v189, v190\n\t" \
   "v_mfma_f32_16x16x32_bf16 v[80:83], a[240:243], v[180:183], v[80:83]\n\t" "v_mfma_f32_16x16x32_bf16 v[84:87], a[244:247], v[180:183], v[84:87]\n\t" \
   "v_mfma_f32_16x16x32_bf16 v[88:91], a[248:251], v[180:183], v[88:91]\n\t" "v_mfma_f32_16x16x32_bf16 v[92:95], a[252:255], v[180:183], v[92:95]\n\t" \
   "s_waitcnt vmcnt(15)\n\t" "v_mfma_f32_16x16x32_bf16 v[32:35], v[192:195], v[144:147], v[32:35]\n\t" "s_waitcnt vmcnt(14)\n\t" \
   "v_mfma_f32_16x16x32_bf16 v[36:39], v[196:199], v[144:147], v[36:39]\n\t" "s_waitcnt vmcnt(13)\n\t" \
   "v_mfma_f32_16x16x32_bf16 v[40:43], v[200:203], v[144:147], v[40:43]\n\t" "s_waitcnt vmcnt(12)\n\t" \
   "v_mfma_f32_16x16x32_bf16 v[44:47], v[204:207], v[144:147], v[44:47]\n\t" "s_waitcnt vmcnt(11)\n\t" \
   "v_mfma_f32_16x16x32_bf16 v[48:51], v[208:211], v[144:147], v[48:51]\n\t" "s_waitcnt vmcnt(10)\n\t" \
   "v_mfma_f32_16x16x32_bf16 v[52:55], v[212:215], v[144:147], v[52:55]\n\t" "s_waitcnt vmcnt(9)\n\t" \
   "v_mfma_f32_16x16x32_bf16 v[56:59], v[216:219], v[144:147], v[56:59]\n\t" "s_waitcnt vmcnt(8)\n\t" \
   "v_mfma_f32_16x16x32_bf16 v[60:63], v[220:223], v[144:147], v[60:63]\n\t" "s_waitcnt vmcnt(7)\n\t" \
   "v_mfma_f32_16x16x32_bf16 v[32:35], v[224:227], v[148:151], v[32:35]\n\t" "s_waitcnt vmcnt(6)\n\t" \
   "v_mfma_f32_16x16x32_bf16 v[36:39], v[228:231], v[148:151], v[36:39]\n\t" "s_waitcnt vmcnt(5)\n\t" \
   "v_mfma_f32_16x16x32_bf16 v[40:43], v[232:235], v[148:151], v[40:43]\n\t" "s_waitcnt vmcnt(4)\n\t" \
   "v_mfma_f32_16x16x32_bf16 v[44:47], v[236:239], v[148:151], v[44:47]\n\t" "s_waitcnt vmcnt(3)\n\t" \
   "v_mfma_f32_16x16x32_bf16 v[48:51], v[240:243], v[148:151], v[48:51]\n\t" "s_waitcnt vmcnt(2)\n\t" \
   "v_mfma_f32_16x16x32_bf16 v[52:55], v[244:247], v[148:151], v[52:55]\n\t" "s_waitcnt vmcnt(1)\n\t" \
   "v_mfma_f32_16x16x32_bf16 v[56:59], v[248:251], v[148:151], v[56:59]\n\t" "s_waitcnt vmcnt(0)\n\t" \
   "v_mfma_f32_16x16x32_bf16 v[60:63], v[252:255], v[148:151], v[60:63]\n\t" "v_mfma_f32_16x16x32_bf16 v[64:67], v[192:195], v[184:187], v[64:67]\n\t" \
   "v_mfma_f32_16x16x32_bf16 v[68:71], v[196:199], v[184:187], v[68:71]\n\t" "v_mfma_f32_16x16x32_bf16 v[72:75], v[200:203], v[184:187], v[72:75]\n\t" \
   "v_mfma_f32_16x16x32_bf16 v[76:79], v[204:207], v[184:187], v[76:79]\n\t" "v_mfma_f32_16x16x32_bf16 v[80:83], v[208:211], v[184:187], v[80:83]\n\t" \
   "v_mfma_f32_16x16x32_bf16 v[84:87], v[212:215], v[184:187], v[84:87]\n\t" "v_mfma_f32_16x16x32_bf16 v[88:91], v[216:219], v[184:187], v[88:91]\n\t" \
   "v_mfma_f32_16x16x32_bf16 v[92:95], v[220:223], v[184:187], v[92:95]\n\t" "v_mfma_f32_16x16x32_bf16 v[64:67], v[224:227], v[188:191], v[64:67]\n\t" \
   "ds_write_b128 %[lw], v[32:35] offset:0\n\t" "v_mfma_f32_16x16x32_bf16 v[68:71], v[228:231], v[188:191], v[68:71]\n\t" \
   "ds_write_b128 %[lw], v[36:39] offset:1024\n\t" "v_mfma_f32_16x16x32_bf16 v[72:75], v[232:235], v[188:191], v[72:75]\n\t" \
   "ds_write_b128 %[lw], v[40:43] offset:2048\n\t" "v_mfma_f32_16x16x32_bf16 v[76:79], v[236:239], v[188:191], v[76:79]\n\t" \
   "ds_write_b128 %[lw], v[44:47] offset:3072\n\t" "ds_write_b128 %[lw], v[64:67] offset:32768\n\t" \
   "v_mfma_f32_16x16x32_bf16 v[80:83], v[240:243], v[188:191], v[80:83]\n\t" "ds_write_b128 %[lw], v[48:51] offset:4096\n\t" \
   "ds_write_b128 %[lw], v[68:71] offset:33792\n\t" "v_mfma_f32_16x16x32_bf16 v[84:87], v[244:247], v[188:191], v[84:87]\n\t" \
   "ds_write_b128 %[lw], v[52:55] offset:5120\n\t" "ds_write_b128 %[lw], v[72:75] offset:34816\n\t" \
   "v_mfma_f32_16x16x32_bf16 v[88:91], v[248:251], v[188:191], v[88:91]\n\t" "ds_write_b128 %[lw], v[56:59] offset:6144\n\t" \
   "ds_write_b128 %[lw], v[76:79] offset:35840\n\t" "v_mfma_f32_16x16x32_bf16 v[92:95], v[252:255], v[188:191], v[92:95]\n\t" \
   "ds_write_b128 %[lw], v[60:63] offset:7168\n\t" "ds_write_b128 %[lw], v[80:83] offset:36864\n\t" "s_nop 1\n\t" "s_nop 3\n\t" \
   "ds_write_b128 %[lw], v[84:87] offset:37888\n\t" "s_nop 3\n\t" "ds_write_b128 %[lw], v[88:91] offset:38912\n\t" "s_nop 3\n\t" \
   "ds_write_b128 %[lw], v[92:95] offset:39936\n\t" "s_waitcnt lgkmcnt(0)\n\t" "s_barrier\n\t" "ds_read_b128 v[112:115], %[lr] offset:0\n\t" \
   "ds_read_b128 v[116:119], %[lr] offset:256\n\t" "ds_read_b128 v[120:123], %[lr] offset:8192\n\t" "ds_read_b128 v[124:127], %[lr] offset:8448\n\t" \
   "ds_read_b128 v[128:131], %[lr] offset:16384\n\t" "ds_read_b128 v[132:135], %[lr] offset:16640\n\t" \
   "ds_read_b128 v[136:139], %[lr] offset:24576\n\t" "ds_read_b128 v[140:143], %[lr] offset:24832\n\t" \
   "ds_read_b128 v[144:147], %[lr] offset:32768\n\t" "ds_read_b128 v[148:151], %[lr] offset:33024\n\t" \
   "ds_read_b128 v[152:155], %[lr] offset:40960\n\t" "ds_read_b128 v[156:159], %[lr] offset:41216\n\t" \
   "ds_read_b128 v[160:163], %[lr] offset:49152\n\t" "ds_read_b128 v[164:167], %[lr] offset:49408\n\t" \
   "ds_read_b128 v[168:171], %[lr] offset:57344\n\t" "ds_read_b128 v[172:175], %[lr] offset:57600\n\t" "s_waitcnt lgkmcnt(12)\n\t" \
   "v_add_f32 v112, v112, v120\n\t" "v_add_f32 v113, v113, v121\n\t" "v_add_f32 v114, v114, v122\n\t" "v_add_f32 v115, v115, v123\n\t" \
   "v_add_f32 v116, v116, v124\n\t" "v_add_f32 v117, v117, v125\n\t" "v_add_f32 v118, v118, v126\n\t" "v_add_f32 v119, v119, v127\n\t" \
   "s_waitcnt lgkmcnt(8)\n\t" "v_add_f32 v128, v128, v136\n\t" "v_add_f32 v129, v129, v137\n\t" "v_add_f32 v130, v130, v138\n\t" \
   "v_add_f32 v131, v131, v139\n\t" "v_add_f32 v132, v132, v140\n\t" "v_add_f32 v133, v133, v141\n\t" "v_add_f32 v134, v134, v142\n\t" \
   "v_add_f32 v135, v135, v143\n\t" "v_add_f32 v112, v112, v128\n\t" "v_add_f32 v113, v113, v129\n\t" "v_add_f32 v114, v114, v130\n\t" \
   "v_add_f32 v115, v115, v131\n\t" "v_add_f32 v116, v116, v132\n\t" "v_add_f32 v117, v117, v133\n\t" "v_add_f32 v118, v118, v134\n\t" \
   "v_add_f32 v119, v119, v135\n\t" "v_cvt_pk_bf16_f32 v96, v112, v113\n\t" "v_cvt_pk_bf16_f32 v97, v114, v115\n\t" \
   "v_cvt_pk_bf16_f32 v98, v116, v117\n\t" "v_cvt_pk_bf16_f32 v99, v118, v119\n\t" "s_waitcnt lgkmcnt(4)\n\t" "v_add_f32 v144, v144, v152\n\t" \
   "v_add_f32 v145, v145, v153\n\t" "v_add_f32 v146, v146, v154\n\t" "v_add_f32 v147, v147, v155\n\t" "v_add_f32 v148, v148, v156\n\t" \
   "v_add_f32 v149, v149, v157\n\t" "v_add_f32 v150, v150, v158\n\t" "v_add_f32 v151, v151, v159\n\t" "s_waitcnt lgkmcnt(0)\n\t" \
   "v_add_f32 v160, v160, v168\n\t" "v_add_f32 v161, v161, v169\n\t" "v_add_f32 v162, v162, v170\n\t" "v_add_f32 v163, v163, v171\n\t" \
   "v_add_f32 v164, v164, v172\n\t" "v_add_f32 v165, v165, v173\n\t" "v_add_f32 v166, v166, v174\n\t" "v_add_f32 v167, v167, v175\n\t" \
   "v_add_f32 v144, v144, v160\n\t" "v_add_f32 v145, v145, v161\n\t" "v_add_f32 v146, v146, v162\n\t" "v_add_f32 v147, v147, v163\n\t" \
   "v_add_f32 v148, v148, v164\n\t" "v_add_f32 v149, v149, v165\n\t" "v_add_f32 v150, v150, v166\n\t" "v_add_f32 v151, v151, v167\n\t" \
   "v_cvt_pk_bf16_f32 v100, v144, v145\n\t" "v_cvt_pk_bf16_f32 v101, v146, v147\n\t" "v_cvt_pk_bf16_f32 v102, v148, v149\n\t" \
   "v_cvt_pk_bf16_f32 v103, v150, v151\n\t" "s_nop 1"
#define O2_CLOB "v32", "v33", "v34", "v35", "v36", "v37", "v38", "v39", "v40", "v41", "v42", "v43", "v44", "v45", "v46", "v47", "v48", "v49", "v50", "v51", "v52", "v53", "v54", "v55", "v56", "v57", "v58", "v59", "v60", "v61", "v62", "v63", "v64", "v65", "v66", "v67", "v68", "v69", "v70", "v71", "v72", "v73", "v74", "v75", "v76", "v77", "v78", "v79", "v80", "v81", "v82", "v83", "v84", "v85", "v86", "v87", "v88", "v89", "v90", "v91", "v92", "v93", "v94", "v95", "v112", "v113", "v114", "v115", "v116", "v117", "v118", "v119", "v120", "v121", "v122", "v123", "v124", "v125", "v126", "v127", "v128", "v129", "v130", "v131", "v132", "v133", "v134", "v135", "v136", "v137", "v138", "v139", "v140", "v141", "v142", "v143", "v144", "v145", "v146", "v147", "v148", "v149", "v150", "v151", "v152", "v153", "v154", "v155", "v156", "v157", "v158", "v159", "v160", "v161", "v162", "v163", "v164", "v165", "v166", "v167", "v168", "v169", "v170", "v171", "v172", "v173", "v174", "v175", "v176", "v177", "v178", "v179", "v180", "v181", "v182", "v183", "v184", "v185", "v186", "v187", "v188", "v189", "v190", "v191", "s40"
#define TZ 336
#define TZM 368
constexpr int COL_I2 = 64 * 264, COL_LDS = COL_I2 + 8 * 2064;
__device__ __forceinline__ void job_col(int wv, unsigned char* ws, rsrc_t rs, unsigned lds, int jb, int go, int clr, bool all) {
  const int lane = lane_id(), g = lane >> 4, r = lane & 15, j = wv * 64 + lane;
  const int xin = (int)OFF_X1T + (jb == 160 ? 0 : jb) * XS * 8 + j * 16;
  v4u x[4], wf[4];
#pragma unroll
  for (int q = 0; q < 4; ++q) stc(rs, (int)OFF_ZT + jb * ZS * 8 + q * 4096 + j * 16, v4u{0xFFFFFFFFu, 0xFFFFFFFFu, 0xFFFFFFFFu, 0xFFFFFFFFu});
#pragma unroll
  for (int q = 0; q < 4; ++q) {
    const int o = (int)OFF_WT + (jb == 160 ? 320 : 2 * jb + (q >> 1)) * S_ * 4 + ((wv * 2 + (q & 1)) * 64 + lane) * 16;
    wf[q] = ldc(rs, o);
  }
  v32u k0, k1, k2;
  v8u k3, ad = (unsigned)lane;
  {
    const int n = 16 * wv + r;
    cis8(k0, n * (512 * (g & 1) + 256 * (g >> 1)), n * 32, n * 128, (r & 1) ? 0x80008000u : 0u);
    cis8(k1, r * g * 256, r * 64, r * 1024, 0x80000000u);
#pragma unroll
    for (int q = 0; q < 8; ++q) {
      const int i = -n * sigma(g, q), k = (r + 16 * (q >> 2)) * (16 * wv + 4 * g + (q & 3));
      const float sc = 1.0f / ((float)S_ * (float)D_);
      k0[16 + 2 * q] = __builtin_bit_cast(unsigned, cosi(i)); k0[17 + 2 * q] = __builtin_bit_cast(unsigned, sini(i));
      k1[16 + 2 * q] = __builtin_bit_cast(unsigned, cosi(k) * sc); k1[17 + 2 * q] = __builtin_bit_cast(unsigned, sini(k) * sc);
      k3[q] = k1[8 + q] ^ 0x80008000u;
    }
    ad[0] = lds + 264 * (16 * (g & 1) + 8 * (g >> 1) + (r >> 2)) + 32 * (r & 3);
    ad[1] = lds + COL_I2 + (2 * (r >> 3) + (r & 1)) * 2064 + ((r >> 1) & 3) * 512 + (g & 1) * 8;
    ad[2] = ad[1] + 256 + (((4 * (wv >> 1) + 2 * (g >> 1) + (wv & 1)) ^ ((r & 6) + 1)) << 4);
    ad[1] += ((4 * (wv >> 1) + 2 * (g >> 1) + (wv & 1)) ^ (r & 6)) << 4;
    ad[3] = lds + COL_I2 + r * 128 + ((g ^ (r >> 1)) << 4);
    ad[4] = lds + COL_I2 + r * 128 + (((4 + g) ^ (r >> 1)) << 4);
    ad[5] = (int)OFF_ZT + (jb * ZS + 32 * (16 * wv + r) + 2 * g) * 8;
    asm volatile("" : "+{v[32:63]}"(k0), "+{v[64:95]}"(k1), "+{v[128:135]}"(k3), "+{v[16:23]}"(ad));
  }
  const unsigned sh = jb == 160 ? 16u : 0u, mk = jb == 0 ? 0xFFFFu : ~0u, d = lds + (j >> 4) * 264 + r * 16;
#pragma unroll
  for (int q = 0; q < 16; ++q) { k2[2 * q] = wf[q >> 2][q & 3] << 16; k2[2 * q + 1] = wf[q >> 2][q & 3] & 0xFFFF0000u; }
  asm volatile("" : "+{v[96:127]}"(k2));
  fan_signal<FSP>(wv, ws, clr, jb);
  if (all) fan_wait<FSP, NCOL>(wv, ws, clr, jb);
  fan_wait<64, 20>(wv, ws, go, jb);
  PH(27)
#pragma unroll
  for (int q = 0; q < 4; ++q) x[q] = ldc(rs, xin + q * 4096);
#define CSTAMP(i)
  CSTAMP(0)
#pragma unroll
  for (int q = 0; q < 4; ++q) {
    LDS_V2(d + q * 16 * 264) = v2u{(x[q].x >> sh) & mk, x[q].y};
    LDS_V2(d + q * 16 * 264 + 8) = v2u{(x[q].z >> sh) & mk, x[q].w};
  }
  CSTAMP(1)
  PH(28)
  asm volatile(COL_ASM ::"{v[32:63]}"(k0), "{v[64:95]}"(k1), "{v[96:127]}"(k2), "{v[128:135]}"(k3), "{v[16:23]}"(ad), [rs] "s"(rs) : COL_CLOB, "memory");
  CSTAMP(2)
  PH(29)
  CSTAMP(3)
}
constexpr int OOB = 0x7FFFFFF0;
struct XRows { v4u p[2]; v2u q[2]; };
__device__ __forceinline__ int xr_off(int rt, int hf, int wv, int lane, bool third) {
  const int o = (int)OFF_XR + ((rt * KT + hf * 5) * 64 + (lane & 48) + 2 * (lane & 7) + ((lane >> 3) & 1)) * 16;
  return third ? (wv < 2 ? o + 4096 + wv * 8 : OOB) : o + wv * 1024;
}
__device__ __forceinline__ void idft_x(XRows& x, rsrc_t rs, int rt, int hf, int wv, int lane) {
#pragma unroll
  for (int m = 0; m < 2; ++m) {
    x.p[m] = __builtin_amdgcn_raw_buffer_load_b128(rs, xr_off(rt, hf, wv, lane, false), m * 10240, AUX_SC1);
    x.q[m] = __builtin_amdgcn_raw_buffer_load_b64(rs, xr_off(rt, hf, wv, lane, true), m * 10240, AUX_SC1);
  }
}
__device__ __forceinline__ void idft_stage(rsrc_t rs, v4u* lds, int rt, int wv, int lane) {
  const int o = (int)OFF_ZT + ((lane >> 3) * ZS + rt * 16 + 2 * (lane & 7)) * 8;
  const bool more = lane >> 4 == wv;
  int off[9];
  v4u t[9];
#pragma unroll
  for (int i = 0; i < 9; ++i) {
    off[i] = i < 4 ? o + (wv * 32 + i * 8) * ZS * 8 : (more && (i < 8 || lane < 8) ? o + (96 + i * 8) * ZS * 8 : OOB);
    t[i] = ldc(rs, off[i]);
  }
  for (unsigned spins = 0; spins < (1u << 16); ++spins) {
    unsigned m = 0;
#pragma unroll
    for (int i = 0; i < 9; ++i) m = max(m, max(t[i].x, t[i].z));
    if (__builtin_expect(!__any(m == 0xFFFFFFFFu), 1)) break;
    __builtin_amdgcn_s_sleep(2);
#pragma unroll
    for (int i = 0; i < 9; ++i) {
      const bool again = max(t[i].x, t[i].z) == 0xFFFFFFFFu;
      const v4u u = ldc(rs, again ? off[i] : OOB);
      if (again) t[i] = u;
    }
    asm volatile("" ::: "memory");
  }
  if (more && lane < 8) {
    t[0].x = (t[0].x & 0xFFFFu) | (t[8].x << 16);
    t[0].z = (t[0].z & 0xFFFFu) | (t[8].z << 16);
  }
  v4u* dst = lds + (lane >> 3) * 16 + (lane & 7);
#pragma unroll
  for (int c = 0; c < 4; ++c) dst[wv * 128 + (c & 1) * 640 + (c >> 1) * 8] = v4u{t[0][c], t[1][c], t[2][c], t[3][c]};
  if (more) {
#pragma unroll
    for (int c = 0; c < 4; ++c) dst[512 + (c & 1) * 640 + (c >> 1) * 8] = v4u{t[4][c], t[5][c], t[6][c], t[7][c]};
  }
}
#define A_IN(a) "{a[0:31]}"(a[0]), "{a[32:63]}"(a[1]), "{a[64:95]}"(a[2]), "{a[96:127]}"(a[3]), "{a[128:159]}"(a[4]), "{a[160:191]}"(a[5]), "{a[192:223]}"(a[6]), "{a[224:255]}"(a[7])
#define A_OUT(a) "={a[0:31]}"(a[0]), "={a[32:63]}"(a[1]), "={a[64:95]}"(a[2]), "={a[96:127]}"(a[3]), "={a[128:159]}"(a[4]), "={a[160:191]}"(a[5]), \
                 "={a[192:223]}"(a[6]), "={a[224:255]}"(a[7])
__device__ __forceinline__ void idft_mul(XRows& x, const v32u (&a)[8], unsigned la) {
  asm volatile(ID_ASM : "+{v[136:139]}"(x.p[0]), "+{v[140:143]}"(x.p[1]), "+{v[144:145]}"(x.q[0]), "+{v[146:147]}"(x.q[1])
               : A_IN(a), [la] "v"(la) : ID_CLOB, "memory");
}
__device__ __forceinline__ void idft_matrix(v32u (&a)[8], rsrc_t rs, int hf, int wv, int lane) {
  const int o = (int)OFF_INV + hf * 10 * NFK * 1024 + lane * 16;
  asm volatile(IM_ASM : A_OUT(a)
               : [o0] "v"(o + wv * 2 * NFK * 1024), [o1] "v"(wv < 2 ? o + (wv + 6) * NFK * 1024 : OOB), [rs] "s"(rs) : IM_CLOB, "memory");
}
__device__ __forceinline__ void idft_when(unsigned tz) {
  const unsigned t = __builtin_amdgcn_readfirstlane(*(unsigned*)(dyn_lds + L_WHEN)) + tz;
  while ((int)(t - rtc()) > 0 && (int)(t - rtc()) < 1000) __builtin_amdgcn_s_sleep(1);
}
constexpr int NFIRST = 40;
__device__ __forceinline__ void job_idft(int wv_, unsigned char* ws, rsrc_t rs, v4u* lds, int rt0, int hf, unsigned long long* dbg) {
  const int lane = lane_id(), wv = wv_;
  v32u a[8];
  idft_matrix(a, rs, hf, wv, lane);
  fan_wait<FSP, NCOL>(wv_, ws, fan_off(1, 0, 0), rt0 >> 2);
  fan_wait<64, 20>(wv_, ws, fan_off(0, 1, 0), rt0 >> 2);
#pragma unroll 1
  for (int rt = rt0; rt < RT; rt += 32) {
    XRows x;
    idft_x(x, rs, rt, hf, wv, lane);
    if (rt == rt0) { idft_when(TZM); PH(30) }
    idft_stage(rs, lds, rt, wv, lane);
    idft_mul(x, a, lds_base() + lane * 16);
#pragma unroll
    for (int m = 0; m < 2; ++m) {
      __builtin_amdgcn_raw_buffer_store_b128(x.p[m], rs, xr_off(rt, hf, wv, lane, false), m * 10240, AUX_SC1);
      __builtin_amdgcn_raw_buffer_store_b64(x.q[m], rs, xr_off(rt, hf, wv, lane, true), m * 10240, AUX_SC1);
    }
    const f32x2 st = rows_sum(frag_stat(x.p[0]) + frag_stat(x.p[1]) + frag_stat(v4u{x.q[0].x, x.q[0].y, x.q[1].x, x.q[1].y}));
    f32x2* sp = (f32x2*)(lds + 5120) + (lane & 15);
    if (lane < 16) sp[wv * 16] = st;
    asm volatile("s_waitcnt lgkmcnt(0)\n\ts_barrier" ::: "memory");
    if (wv == 0) {
      const f32x2 s0 = sp[0] + sp[16], s1 = sp[32] + sp[48];
      const f32x4 sv = lane < 16 ? f32x4{s0.x, s0.y, s1.x, s1.y} : f32x4{0, 0, 0, 0};
      stc(rs, lane < 16 + hf * 16 ? (int)OFF_ST + (rt * 16 + 2 * (lane & 7) + ((lane >> 3) & 1)) * 48 + (hf + (lane >> 4)) * 16 : OOB, __builtin_bit_cast(v4u, sv));
    }
    if (rt == rt0) { PH(31) }
    if (rt < NFIRST / 2) fan_signal<64>(wv_, ws, fan_off(1, 1, 0), rt * 2 + hf);
    else dep_signal(wv_, ws, cnt_off(1, CNT_IDFT, rt >> 2));
  }
}

constexpr int NFAST = 62;
#define TYL 40
__device__ __forceinline__ void job_out(int wv_, unsigned char* ws, rsrc_t rs, unsigned long long ysel, int layer, int s, int cg, int qt) {
  const int lane = lane_id(), wv = wv_, g = lane >> 4, r = lane & 15, q0 = max(qt, 0);
  const int yo = wv * 2048 + lane * 16;
  const int sy = __builtin_amdgcn_readfirstlane(pool_base(ws) + P_Y + y_region(ysel, s) + cg * 131072);
  const int xo = (int)OFF_XR + (((2 * s + q0) * KT + cg * 4 + wv) * 64 + lane) * 16;
  const v4u x0 = ldc(rs, xo), x1 = ldc(rs, qt < 0 ? xo + KT * 1024 : OOB);
  v32u u[8], y0, y1;
  asm volatile(OU_ASM : A_OUT(u) : [o] "v"(wv * 8192 + lane * 16), [so] "s"((int)OFF_UF + (s * 40 + cg * 8) * 32768), [rs] "s"(rs) : OU_CLOB, "scc", "memory");
  const int sw = min(s, NFAST - 1);
  if (s > 1) prog_prewait(wv_, ws, prog_off(layer, sw - 2, (s + cg * 7) & 31));
  prog_wait(wv_, ws, prog_off(layer, sw, 0), s < PWL);
  v8u acc;
#define OM_OPS A_IN(u), [po] "v"(wv * 16384 + lane * 16), [mo] "v"(wv * 4096 + lane * 16), [sp] "s"((int)OFF_PXF + s * 65536 + q0 * 1024),         \
               [sm] "s"((int)OFF_PXM + s * 16384 + q0 * 1024), [lw] "v"(lds_base() + wv * 8192 + lane * 16), [lr] "v"(lds_base() + wv * 2048 + g * 512 + r * 16), [rs] "s"(rs)
  PHT(1)
  if (qt >= 0) {
    if (s > NFAST) __builtin_amdgcn_s_sleep(TYL);
    for (int i = 0; i < 2; ++i) {
      unsigned m;
      asm volatile(OY_ASM : "={v[192:223]}"(y0), "={v[224:255]}"(y1), [m] "=&v"(m) : [yo] "v"(yo), [sy] "s"(sy), [rs] "s"(rs) : OY_CLOB, "scc", "memory");
      if (i == 0) prog_wait(wv_, ws, prog_off(layer, s, 0), false);
      if (!__any(m == 0xFFFFFFFFu)) break;
    }
    PHT(0)
    asm volatile(O1_ASM : "=&{v[96:103]}"(acc) : OM_OPS, "{v[192:223]}"(y0), "{v[224:255]}"(y1) : O1_CLOB, "scc", "memory");
    PHV(32 + (s - 61) * 4, pht0)
  } else {
    asm volatile(O2_ASM : "=&{v[96:103]}"(acc), "=&{v[192:223]}"(y0), "=&{v[224:255]}"(y1) : OM_OPS, [yo] "v"(yo), [sy] "s"(sy) : O2_CLOB, "scc", "memory");
  }
  f32x2* sp = (f32x2*)(dyn_lds + 81920);
#pragma unroll
  for (int q = 0; q < 2; ++q) {
    if (q && qt >= 0) break;
    const v4u x = q ? x1 : x0;
    const auto s0 = __builtin_amdgcn_permlane16_swap(acc[4 * q], acc[4 * q + 2], false, false);
    const auto s1 = __builtin_amdgcn_permlane16_swap(acc[4 * q + 1], acc[4 * q + 3], false, false);
    v4u y;
    y.x = pk(lo_f(x.x) + lo_f(s0[0]), hi_f(x.x) + hi_f(s0[0]));
    y.y = pk(lo_f(x.y) + lo_f(s0[1]), hi_f(x.y) + hi_f(s0[1]));
    y.z = pk(lo_f(x.z) + lo_f(s1[0]), hi_f(x.z) + hi_f(s1[0]));
    y.w = pk(lo_f(x.w) + lo_f(s1[1]), hi_f(x.w) + hi_f(s1[1]));
    stc(rs, xo + q * KT * 1024, y);
    const f32x2 st = rows_sum(frag_stat(y));
    if (lane < 16) sp[(wv * 2 + q) * 16 + lane] = st;
  }
  PHT(2)
  asm volatile("s_waitcnt lgkmcnt(0)\n\ts_barrier" ::: "memory");
  if (wv == 0) {
    sp += lane & 31;
    __builtin_amdgcn_raw_buffer_store_b64(__builtin_bit_cast(v2u, (sp[0] + sp[32]) + (sp[64] + sp[96])), rs,
                                          lane < (qt < 0 ? 32 : 16) ? (int)OFF_ST + ((s * 32 + q0 * 16 + lane) * 6 + cg) * 8 : OOB, 0, AUX_SC1);
  }
  dep_signal(wv_, ws, cnt_off(layer, CNT_OUT, s), qt < 0 ? 2u : 1u);
  PHT(3)
  if (s > 60) { PHV(33 + (s - 61) * 4, pht1) PHV(34 + (s - 61) * 4, pht2) PHV(35 + (s - 61) * 4, pht3) }
}
constexpr int JOB_LDS = 4 * NFK * 1024;
constexpr int FAN_LAST = fan_off(0, 1, 0) + 20 * 64;
#define OAUX 0
__device__ __forceinline__ void job_last(int wv_, unsigned char* ws, u16* out, int jb, int rt, int hf) {
  const rsrc_t rs = ws_rsrc(ws), ro = __builtin_amdgcn_make_buffer_rsrc(out, 0, S_ * D_ * 2, 0x00020000);
  const int wv = wv_;
  v32u a[8];
  idft_matrix(a, rs, hf, wv, lane_id());
  PH(26)
  if (jb >= 0) {
    job_col(wv_, ws, rs, lds_base(), jb, FAN_LAST, fan_off(0, 0, 0), true);
    PH(13)
  } else {
    fan_wait<FSP, NCOL>(wv_, ws, fan_off(0, 0, 0), rt);
    fan_wait<64, 20>(wv_, ws, FAN_LAST, rt);
  }
  const int lane = lane_id();
  v4u* lds = (v4u*)dyn_lds + 3072;
  XRows x;
  idft_x(x, rs, rt, hf, wv, lane);
  idft_when(TZ);
  PHT(0)
  idft_stage(rs, lds, rt, wv, lane);
  PHT(1)
  idft_mul(x, a, lds_base() + 49152 + lane * 16);
  const int o = ((rt * 16 + 2 * (lane & 7) + ((lane >> 3) & 1)) * D_ + hf * 160 + (lane >> 5) * 8) * 2;
#pragma unroll
  for (int m = 0; m < 3; ++m) {
    const v4u y = m < 2 ? x.p[m] : v4u{x.q[0].x, x.q[0].y, x.q[1].x, x.q[1].y};
    const auto s0 = __builtin_amdgcn_permlane16_swap(y.x, y.z, false, false), s1 = __builtin_amdgcn_permlane16_swap(y.y, y.w, false, false);
    __builtin_amdgcn_raw_buffer_store_b128(v4u{s0[0], s1[0], s0[1], s1[1]}, ro,
                                           m < 2 ? o + m * 640 + wv * 64 + (lane & 16) * 2 : (wv < 2 ? o + 256 + wv * 32 + (lane & 16) * 40 : OOB), 0, OAUX);
  }
  PH(15)
  PHV(24, pht0) PHV(25, pht1)
}

constexpr unsigned SPIN_CAP = 1u << 16;
#define NFILL 24
#define FSPARE
#define FLAG 4

#define VM_LD128(dst, off, so, IMM) asm volatile("buffer_load_dwordx4 %0, %1, %2, %3 offen offset:" #IMM " sc1" : "=v"(dst) : "v"(off), "s"(rs), "s"(so) : "memory")
#define VM_LD32(dst, off, so) asm volatile("buffer_load_dword %0, %1, %2, %3 offen sc1" : "=v"(dst) : "v"(off), "s"(rs), "s"(so) : "memory")
#define VM_ST32(val, off, so) asm volatile("buffer_store_dword %0, %1, %2, %3 offen sc1" ::"v"(val), "v"(off), "s"(rs), "s"(so) : "memory")
#define VM_ST128(val, off, so) asm volatile("buffer_store_dwordx4 %0, %1, %2, %3 offen sc1\n\ts_nop 1" ::"v"(val), "v"(off), "s"(rs), "s"(so) : "memory")
#define VM_WAIT(N) asm volatile("s_waitcnt vmcnt(" #N ")" ::: "memory")
#define VM_WAIT1(N, a) asm volatile("s_waitcnt vmcnt(" #N ")" : "+v"(a)::"memory")
__device__ __forceinline__ v4u make_rs(const void* p, unsigned bytes) {
  const unsigned long long a = (unsigned long long)p;
  v4u r;
  r.x = (unsigned)__builtin_amdgcn_readfirstlane((unsigned)a);
  r.y = (unsigned)__builtin_amdgcn_readfirstlane((unsigned)(a >> 32) & 0xFFFFu);
  r.z = bytes;
  r.w = 0x00020000u;
  return r;
}
struct Clk0 { unsigned r, c; };
__device__ __forceinline__ Clk0 clk_sample() {
  unsigned long long r, c;
  asm volatile("s_memrealtime %0\n\ts_memtime %1\n\ts_waitcnt lgkmcnt(0)" : "=s"(r), "=s"(c)::"memory");
  return Clk0{(unsigned)r, (unsigned)c};
}
__device__ __forceinline__ v4u clk_init(Clk0 a) {
  const Clk0 b = clk_sample();
  unsigned k = 1400u;
  if (b.r - a.r >= 100u) k = (unsigned)((float)(b.r - a.r) * 32768.f / (float)(b.c - a.c));
  k = (unsigned)__builtin_amdgcn_readfirstlane((int)min(max(k, 1200u), 3000u));
  asm volatile("" : "+s"(k));
  return v4u{b.r * 8u + 16u, b.c, k, 0u};
}
#define LDSBAR() asm volatile("s_waitcnt lgkmcnt(0)\n\ts_barrier" ::: "memory")
#define MEMPIN() asm volatile("" ::: "memory")

__device__ __forceinline__ int ladder(int x, int i) { return copy_off(((x + (i >> 1)) & 3) | ((i & 1) << 2)); }

struct RecTune { int m1, m2, dl1, rob, c2, vis, dlt, du; };

#define STAT(r)
__device__ __forceinline__ unsigned max4(v4u v) { return max(max(v.x, v.y), max(v.z, v.w)); }
template <int NAP = 6>
__device__ __forceinline__ unsigned flag_finish(const v4u rs, int off, unsigned cv) {
  for (unsigned spins = 0; __any(cv == 0xFFFFFFFFu) && spins < SPIN_CAP; ++spins) {
    __builtin_amdgcn_s_sleep(NAP);
    v4u t;
    VM_LD128(t, off, 0, 0);
    VM_WAIT1(0, t);
    cv = max4(t);
  }
  return cv;
}
struct Pool {
  unsigned long long sel, s3, ysel; int base;
  __device__ __forceinline__ int near(int s) const { return (int)((s3 >> s) & 1ull) * 16384; }
  __device__ __forceinline__ int region(int s) const { return base + s * REG + (int)((sel >> s) & 1ull) * 8192; }
};
constexpr int L_QT = 10240, L_KT = 12288, L_CL = 14336, L_KQ = 16384;
constexpr int L_T = 14336;
constexpr int L_M2 = L_KQ + 65536;
constexpr int REC_ST = L_M2 + 12288;
constexpr int REC_LDS = REC_ST + 4096 + 64;

#define RSTAMP(i)
#define YSTAMP(n)
#define RST_CLOB
#define P1TRACE
#define PVTRACE
#define FINTRACE
#define P1_ASM \
   "s_mov_b32 s64, -1\n\t" "s_mov_b32 s76, 0\n\t" "s_mov_b32 s77, 0\n\t" "s_mov_b32 s71, 63\n\t" "s_mov_b32 s47, 1300\n\t" "s_mov_b32 s49, 0\n\t" \
   "s_mov_b32 s51, 0\n\t" "s_mov_b32 s52, 0\n\t" "s_mov_b32 s70, 0\n\t" "s_mov_b32 s69, 0\n\t" "v_cmp_gt_i32 s[20:21], 0, v126\n\t" \
   "v_cmp_gt_i32 s[22:23], 1, v126\n\t" "v_cmp_gt_i32 s[24:25], 2, v126\n\t" "v_cmp_gt_i32 s[26:27], 3, v126\n\t" "v_cmp_gt_i32 s[28:29], 16, v126\n\t" \
   "v_cmp_gt_i32 s[30:31], 17, v126\n\t" "v_cmp_gt_i32 s[32:33], 18, v126\n\t" "v_cmp_gt_i32 s[34:35], 19, v126\n\t" "s_waitcnt vmcnt(0)\n\t" \
   "s_lshr_b64 s[72:73], %[sel], s71\n\t" "s_lshl_b32 s65, s71, 19\n\t" "s_and_b32 s72, s72, 1\n\t" "s_add_u32 s65, s65, %[pool]\n\t" \
   "s_lshl_b32 s72, s72, 13\n\t" "s_add_u32 s65, s65, s72\n\t" "s_lshr_b64 s[72:73], %[s3], s71\n\t" "s_and_b32 s54, s72, 1\n\t" \
   "s_lshl_b32 s54, s54, 14\n\t" "s_memtime s[62:63]\n\t" "s_memrealtime s[60:61]\n\t" "40:\n\t" "s_cmp_eq_u32 s64, 0\n\t" "s_cbranch_scc1 95f\n\t" \
   "96:\n\t" "s_add_i32 s72, s64, 2\n\t" "s_min_i32 s72, s72, 63\n\t" "s_lshl_b32 s59, s72, 12\n\t" "s_lshl_b32 s79, s72, 13\n\t" \
   "buffer_load_dword v108, v139, %[rs], s65 offen sc1\n\t" "s_cmp_gt_u32 %[wv], 1\n\t" "s_waitcnt vmcnt(4)\n\t" "s_cbranch_scc1 44f\n\t" \
   "buffer_load_dword v15, v140, %[rs], s59 offen sc1\n\t" "ds_write_b128 v127, v[16:19] offset:0\n\t" "ds_write_b128 v127, v[20:23] offset:1024\n\t" \
   "ds_write_b128 v127, v[24:27] offset:2048\n\t" "ds_write_b128 v127, v[28:31] offset:3072\n\t" "s_waitcnt lgkmcnt(0)\n\t" \
   "s_sub_u32 s74, s62, s57\n\t" "s_mul_i32 s74, s74, s58\n\t" "s_lshr_b32 s74, s74, 12\n\t" "s_add_u32 s74, s74, s56\n\t" "s_lshl_b32 s60, s60, 3\n\t" \
   "s_sub_u32 s75, s74, s60\n\t" "s_cmp_lt_u32 s75, 32\n\t" "s_cbranch_scc1 38f\n\t" "s_cmp_lt_i32 s75, 0\n\t" "s_cselect_b32 s61, 0, 31\n\t" \
   "s_add_u32 s74, s60, s61\n\t" "s_sub_i32 s75, s61, s75\n\t" "s_add_i32 s75, s75, 1\n\t" "s_ashr_i32 s75, s75, 1\n\t" "s_min_i32 s75, s75, 8\n\t" \
   "s_max_i32 s75, s75, -8\n\t" "s_mov_b32 s56, s74\n\t" "s_mov_b32 s57, s62\n\t" "s_add_u32 s58, s58, s75\n\t" "38:\n\t" "s_min_u32 s72, s70, 1\n\t" \
   "s_and_b32 s71, s74, -4\n\t" "s_or_b32 s71, s71, s72\n\t" "v_mov_b32 v111, s71\n\t" "buffer_store_dword v111, v131, %[rs], s65 offen sc1\n\t" \
   P1TRACE "s_barrier\n\t" "ds_read_b128 v[160:163], v122 offset:0\n\t" "ds_read_b128 v[164:167], v122 offset:1024\n\t" \
   "ds_read_b128 v[168:171], v122 offset:2048\n\t" "ds_read_b128 v[172:175], v122 offset:3072\n\t" "ds_read_b128 v[176:179], v122 offset:4096\n\t" \
   "ds_read_b128 v[180:183], v122 offset:5120\n\t" "ds_read_b128 v[184:187], v122 offset:6144\n\t" "ds_read_b128 v[188:191], v122 offset:7168\n\t" \
   "s_cmp_lt_i32 s64, 1\n\t" "s_cselect_b32 s72, 0x7ffffff0, 0\n\t" "s_max_i32 s71, s64, 1\n\t" "v_or_b32 v109, s72, v143\n\t" \
   "s_lshl_b32 s71, s71, 12\n\t" "v_mov_b32 v110, 1\n\t" "s_sub_u32 s71, s71, 4096\n\t" "buffer_store_dword v110, v109, %[rs], s71 offen sc1\n\t" \
   "s_add_u32 s48, s69, s47\n\t" "s_cmp_eq_u32 s70, 0\n\t" "s_cselect_b32 s48, s48, -1\n\t" "s_mov_b32 s66, s65\n\t" "s_add_i32 s64, s64, 1\n\t" \
   "s_and_b32 s71, s64, 63\n\t" "s_lshr_b64 s[72:73], %[sel], s71\n\t" "s_lshl_b32 s65, s71, 19\n\t" "s_and_b32 s72, s72, 1\n\t" \
   "s_add_u32 s65, s65, %[pool]\n\t" "s_lshl_b32 s72, s72, 13\n\t" "s_add_u32 s65, s65, s72\n\t" "s_xor_b32 s67, s54, %[lad0]\n\t" \
   "s_xor_b32 s68, s67, 0x4000\n\t" "s_add_u32 s67, s67, s66\n\t" "s_add_u32 s68, s68, s66\n\t" "s_lshr_b64 s[72:73], %[s3], s71\n\t" \
   "s_and_b32 s54, s72, 1\n\t" "s_lshl_b32 s54, s54, 14\n\t" "s_cmp_eq_u32 s64, 0\n\t" "s_cselect_b32 s70, 1, 0\n\t" "s_and_b32 s55, %[wv], 1\n\t" \
   "s_lshl_b32 s55, s55, 14\n\t" "s_xor_b32 s55, s55, s54\n\t" "s_xor_b32 s50, s55, 0x4000\n\t" "s_add_u32 s55, s55, s65\n\t" \
   "s_add_u32 s50, s50, s65\n\t" "v_mov_b32 v48, 0\n\t" "v_mov_b32 v49, 0\n\t" "v_mov_b32 v50, 0\n\t" "v_mov_b32 v51, 0\n\t" "v_mov_b32 v52, 0\n\t" \
   "v_mov_b32 v53, 0\n\t" "v_mov_b32 v54, 0\n\t" "v_mov_b32 v55, 0\n\t" "v_mov_b32 v56, 0\n\t" "v_mov_b32 v57, 0\n\t" "v_mov_b32 v58, 0\n\t" \
   "v_mov_b32 v59, 0\n\t" "v_mov_b32 v60, 0\n\t" "v_mov_b32 v61, 0\n\t" "v_mov_b32 v62, 0\n\t" "v_mov_b32 v63, 0\n\t" "s_waitcnt vmcnt(2)\n\t" \
   "v_cmp_eq_u32 vcc, -1, v15\n\t" "s_nop 0\n\t" "s_cbranch_vccnz 141f\n\t" "41:\n\t" "s_branch 48f\n\t" "44:\n\t" \
   "v_mfma_f32_16x16x32_bf16 v[32:35], v[160:163], v[16:19], 0\n\t" "s_max_i32 s51, s64, 0\n\t" "s_lshl_b32 s52, s51, 16\n\t" \
   "s_lshl_b32 s51, s51, 14\n\t" "v_mfma_f32_16x16x32_bf16 v[36:39], v[176:179], v[16:19], 0\n\t" "s_cmp_lt_i32 s64, 0\n\t" \
   "s_cselect_b32 s49, 0x7ffffff0, 0\n\t" "s_add_i32 s72, s64, 1\n\t" "v_mfma_f32_16x16x32_bf16 v[32:35], v[164:167], v[20:23], v[32:35]\n\t" \
   "s_min_i32 s72, s72, 63\n\t" "s_lshl_b32 s79, s72, 13\n\t" "v_mfma_f32_16x16x32_bf16 v[36:39], v[180:183], v[20:23], v[36:39]\n\t" \
   "v_mfma_f32_16x16x32_bf16 v[32:35], v[168:171], v[24:27], v[32:35]\n\t" "s_waitcnt lgkmcnt(0)\n\t" \
   "v_mfma_f32_16x16x32_bf16 v[36:39], v[184:187], v[24:27], v[36:39]\n\t" "s_sub_u32 s74, s62, s57\n\t" "s_mul_i32 s74, s74, s58\n\t" \
   "s_lshr_b32 s74, s74, 12\n\t" "v_mfma_f32_16x16x32_bf16 v[32:35], v[172:175], v[28:31], v[32:35]\n\t" "s_add_u32 s74, s74, s56\n\t" \
   "s_lshl_b32 s60, s60, 3\n\t" "s_sub_u32 s75, s74, s60\n\t" "v_mfma_f32_16x16x32_bf16 v[36:39], v[188:191], v[28:31], v[36:39]\n\t" \
   "s_cmp_lt_u32 s75, 32\n\t" "s_cbranch_scc1 39f\n\t" "s_cmp_lt_i32 s75, 0\n\t" "s_cselect_b32 s61, 0, 31\n\t" "s_add_u32 s74, s60, s61\n\t" \
   "s_sub_i32 s75, s61, s75\n\t" "s_add_i32 s75, s75, 1\n\t" "s_ashr_i32 s75, s75, 1\n\t" "s_min_i32 s75, s75, 8\n\t" "s_max_i32 s75, s75, -8\n\t" \
   "s_mov_b32 s56, s74\n\t" "s_mov_b32 s57, s62\n\t" "s_add_u32 s58, s58, s75\n\t" "39:\n\t" "s_barrier\n\t" \
   "v_mfma_f32_16x16x32_bf16 v[40:43], v[144:147], v[16:19], 0\n\t" "s_min_u32 s72, s70, 1\n\t" "s_and_b32 s71, s74, -4\n\t" \
   "s_or_b32 s71, s71, s72\n\t" "v_mov_b32 v111, s71\n\t" "v_mfma_f32_16x16x32_bf16 v[40:43], v[148:151], v[20:23], v[40:43]\n\t" \
   "buffer_store_dword v111, v131, %[rs], s65 offen sc1\n\t" P1TRACE "v_mov_b32 v48, 0\n\t" "v_mov_b32 v49, 0\n\t" \
   "v_mfma_f32_16x16x32_bf16 v[40:43], v[152:155], v[24:27], v[40:43]\n\t" "v_mov_b32 v50, 0\n\t" "v_mov_b32 v51, 0\n\t" "v_mov_b32 v52, 0\n\t" \
   "v_mov_b32 v53, 0\n\t" "v_mfma_f32_16x16x32_bf16 v[40:43], v[156:159], v[28:31], v[40:43]\n\t" "v_mov_b32 v54, 0\n\t" "v_mov_b32 v55, 0\n\t" \
   "v_cndmask_b32 v32, v32, v125, s[20:21]\n\t" "v_cndmask_b32 v33, v33, v125, s[22:23]\n\t" "v_cndmask_b32 v34, v34, v125, s[24:25]\n\t" \
   "v_cndmask_b32 v35, v35, v125, s[26:27]\n\t" "v_cndmask_b32 v36, v36, v125, s[28:29]\n\t" "v_cndmask_b32 v37, v37, v125, s[30:31]\n\t" \
   "v_cndmask_b32 v38, v38, v125, s[32:33]\n\t" "v_cndmask_b32 v39, v39, v125, s[34:35]\n\t" "v_exp_f32 v64, v32\n\t" "s_add_u32 s48, s69, s47\n\t" \
   "s_cmp_eq_u32 s70, 0\n\t" "s_cselect_b32 s48, s48, -1\n\t" "v_exp_f32 v65, v33\n\t" "s_mov_b32 s66, s65\n\t" "s_add_i32 s64, s64, 1\n\t" \
   "s_and_b32 s71, s64, 63\n\t" "v_exp_f32 v66, v34\n\t" "s_lshr_b64 s[72:73], %[sel], s71\n\t" "s_lshl_b32 s65, s71, 19\n\t" \
   "s_and_b32 s72, s72, 1\n\t" "v_exp_f32 v67, v35\n\t" "s_add_u32 s65, s65, %[pool]\n\t" "s_lshl_b32 s72, s72, 13\n\t" "s_add_u32 s65, s65, s72\n\t" \
   "v_exp_f32 v68, v36\n\t" "s_xor_b32 s67, s54, %[lad0]\n\t" "s_xor_b32 s68, s67, 0x4000\n\t" "s_add_u32 s67, s67, s66\n\t" "v_exp_f32 v69, v37\n\t" \
   "s_add_u32 s68, s68, s66\n\t" "s_lshr_b64 s[72:73], %[s3], s71\n\t" "s_and_b32 s54, s72, 1\n\t" "v_exp_f32 v70, v38\n\t" \
   "s_lshl_b32 s54, s54, 14\n\t" "s_cmp_eq_u32 s64, 0\n\t" "s_cselect_b32 s70, 1, 0\n\t" "v_exp_f32 v71, v39\n\t" "s_and_b32 s55, %[wv], 1\n\t" \
   "s_lshl_b32 s55, s55, 14\n\t" "s_xor_b32 s55, s55, s54\n\t" "v_exp_f32 v72, v40\n\t" "s_xor_b32 s50, s55, 0x4000\n\t" "s_add_u32 s55, s55, s65\n\t" \
   "s_add_u32 s50, s50, s65\n\t" "v_exp_f32 v73, v41\n\t" "v_mov_b32 v56, 0\n\t" "v_mov_b32 v57, 0\n\t" "v_mov_b32 v58, 0\n\t" "v_exp_f32 v74, v42\n\t" \
   "v_mov_b32 v59, 0\n\t" "v_mov_b32 v60, 0\n\t" "v_mov_b32 v61, 0\n\t" "v_exp_f32 v75, v43\n\t" "v_mov_b32 v62, 0\n\t" "v_mov_b32 v63, 0\n\t" \
   "v_add_f32 v80, v64, v65\n\t" "v_add_f32 v81, v66, v67\n\t" "v_add_f32 v82, v68, v69\n\t" "v_add_f32 v83, v70, v71\n\t" \
   "v_add_f32 v84, v72, v73\n\t" "v_add_f32 v85, v74, v75\n\t" "v_add_f32 v80, v80, v81\n\t" "v_add_f32 v82, v82, v83\n\t" \
   "v_add_f32 v84, v84, v85\n\t" "v_add_f32 v80, v80, v82\n\t" "v_fma_f32 v82, v80, 2.0, v84\n\t" "v_fma_f32 v80, v80, 2.0, v84\n\t" \
   "ds_read_b128 v[160:163], v122 offset:0\n\t" "ds_read_b128 v[164:167], v122 offset:1024\n\t" "v_permlane16_swap_b32 v80, v82\n\t" \
   "v_add_f32 v81, v80, v82\n\t" "v_add_f32 v80, v80, v82\n\t" "ds_read_b128 v[168:171], v122 offset:2048\n\t" \
   "ds_read_b128 v[172:175], v122 offset:3072\n\t" "v_permlane32_swap_b32 v80, v81\n\t" "v_add_f32 v80, v80, v81\n\t" "v_rcp_f32 v90, v80\n\t" \
   "v_add_u32 v81, 0xe0800000, v80\n\t" "v_add_f32 v90, v90, v90\n\t" "v_cmp_lt_u32 vcc, 0x40000000, v81\n\t" \
   "ds_read_b128 v[176:179], v122 offset:4096\n\t" "ds_read_b128 v[180:183], v122 offset:5120\n\t" "ds_read_b128 v[184:187], v122 offset:6144\n\t" \
   "ds_read_b128 v[188:191], v122 offset:7168\n\t" "s_cbranch_vccnz 74f\n\t" "75:\n\t" "v_pk_mul_f32 v[64:65], v[64:65], v[90:91] op_sel_hi:[1,0]\n\t" \
   "v_pk_mul_f32 v[66:67], v[66:67], v[90:91] op_sel_hi:[1,0]\n\t" "v_pk_mul_f32 v[68:69], v[68:69], v[90:91] op_sel_hi:[1,0]\n\t" \
   "v_pk_mul_f32 v[70:71], v[70:71], v[90:91] op_sel_hi:[1,0]\n\t" "v_pk_mul_f32 v[72:73], v[72:73], v[90:91] op_sel_hi:[1,0]\n\t" \
   "v_pk_mul_f32 v[74:75], v[74:75], v[90:91] op_sel_hi:[1,0]\n\t" "v_cvt_pk_bf16_f32 v44, v64, v65\n\t" "v_cvt_pk_bf16_f32 v46, v66, v67\n\t" \
   "v_cvt_pk_bf16_f32 v45, v68, v69\n\t" "v_cvt_pk_bf16_f32 v47, v70, v71\n\t" "v_cvt_pk_bf16_f32 v32, v72, v73\n\t" \
   "v_cvt_pk_bf16_f32 v33, v74, v75\n\t" "s_waitcnt vmcnt(1)\n\t" "48:\n\t" "v_readlane_b32 s69, v108, 0\n\t" "v_readlane_b32 s71, v108, 1\n\t" \
   "s_cmp_eq_u32 s64, 64\n\t" "s_cbranch_scc1 90f\n\t" "s_cmp_eq_u32 s69, -1\n\t" "s_cbranch_scc1 63f\n\t" "s_cmp_eq_u32 s71, -1\n\t" \
   "s_cselect_b32 s47, s47, s71\n\t" "50:\n\t" "s_add_u32 s73, s69, %[m1]\n\t" RSTAMP(5) "51:\n\t" "s_memrealtime s[60:61]\n\t" \
   "s_memtime s[62:63]\n\t" "s_waitcnt lgkmcnt(0)\n\t" "s_sub_u32 s74, s62, s57\n\t" "s_mul_i32 s74, s74, s58\n\t" "s_lshr_b32 s74, s74, 12\n\t" \
   "s_add_u32 s74, s74, s56\n\t" "s_lshl_b32 s60, s60, 3\n\t" "s_sub_u32 s75, s74, s60\n\t" "s_cmp_lt_u32 s75, 32\n\t" "s_cbranch_scc1 52f\n\t" \
   "s_cmp_lt_i32 s75, 0\n\t" "s_cselect_b32 s61, 0, 31\n\t" "s_add_u32 s74, s60, s61\n\t" "s_sub_i32 s75, s61, s75\n\t" "s_add_i32 s75, s75, 1\n\t" \
   "s_ashr_i32 s75, s75, 1\n\t" "s_min_i32 s75, s75, 8\n\t" "s_max_i32 s75, s75, -8\n\t" "s_mov_b32 s56, s74\n\t" "s_mov_b32 s57, s62\n\t" \
   "s_add_u32 s58, s58, s75\n\t" "52:\n\t" "s_sub_i32 s74, s73, s74\n\t" "s_cmp_le_i32 s74, 150\n\t" "s_cbranch_scc1 53f\n\t" \
   "s_cmp_lt_i32 s74, 260\n\t" "s_cbranch_scc1 51b\n\t" "s_sleep 2\n\t" "s_cmp_lt_i32 s74, 32000\n\t" "s_cbranch_scc1 51b\n\t" "s_mov_b32 s74, 0\n\t" \
   "53:\n\t" "s_sub_i32 s74, s74, 56\n\t" "s_max_i32 s74, s74, 0\n\t" "s_mul_i32 s74, s74, 23\n\t" "s_lshr_b32 s74, s74, 5\n\t" \
   "s_and_b32 s74, s74, -4\n\t" "s_getpc_b64 s[60:61]\n\t" "54:\n\t" "s_sub_u32 s74, 55f-54b, s74\n\t" "s_add_u32 s60, s60, s74\n\t" \
   "s_addc_u32 s61, s61, 0\n\t" "s_setpc_b64 s[60:61]\n\t" "s_nop 3\n\t" "s_nop 3\n\t" "s_nop 3\n\t" "s_nop 3\n\t" "s_nop 3\n\t" "s_nop 3\n\t" \
   "s_nop 3\n\t" "s_nop 3\n\t" "s_nop 3\n\t" "s_nop 3\n\t" "s_nop 3\n\t" "s_nop 3\n\t" "s_nop 3\n\t" "s_nop 3\n\t" "s_nop 3\n\t" "s_nop 3\n\t" \
   "s_nop 3\n\t" "s_nop 3\n\t" "s_nop 3\n\t" "s_nop 3\n\t" "s_nop 3\n\t" "s_nop 3\n\t" "s_nop 3\n\t" "s_nop 3\n\t" "55:\n\t" "20:\n\t" RSTAMP(4) \
   "buffer_load_dwordx4 v[64:67], v128, %[rs], s67 offen sc1\n\t" "buffer_load_dwordx4 v[68:71], v129, %[rs], s67 offen sc1\n\t" \
   "buffer_load_dwordx4 v[72:75], v130, %[rs], s67 offen sc1\n\t" "s_nop 7\n\t" "s_nop 7\n\t" "s_nop 7\n\t" "s_cmp_lg_u32 s70, 0\n\t" \
   "s_cbranch_scc1 27f\n\t" "buffer_load_dwordx4 v[16:19], v138, %[rs], s79 offen offset:0 sc1\n\t" \
   "buffer_load_dwordx4 v[20:23], v138, %[rs], s79 offen offset:1024 sc1\n\t" \
   "buffer_load_dwordx4 v[24:27], v138, %[rs], s79 offen offset:2048 sc1\n\t" \
   "buffer_load_dwordx4 v[28:31], v138, %[rs], s79 offen offset:3072 sc1\n\t" "v_or_b32 v108, s49, v141\n\t" "v_or_b32 v109, s49, v142\n\t" \
   "buffer_store_dwordx4 v[44:47], v108, %[rs], s52 offen sc1\n\t" "buffer_store_dwordx2 v[32:33], v109, %[rs], s51 offen sc1\n\t" "28:\n\t" RSTAMP(3) \
   "s_waitcnt vmcnt(8)\n\t" "ds_write_b128 v112, v[64:67]\n\t" "v_smfmac_f32_16x16x64_bf16 v[48:51], v[64:67], a[0:7], v134\n\t" \
   "s_waitcnt vmcnt(7)\n\t" "ds_write_b128 v113, v[68:71]\n\t" "s_waitcnt vmcnt(6)\n\t" "ds_write_b128 v114, v[72:75]\n\t" RSTAMP(7) "2:\n\t" \
   "v_smfmac_f32_16x16x64_bf16 v[52:55], v[64:67], a[64:71], v134\n\t" "v_max3_u32 v110, v64, v66, v68\n\t" "v_max3_u32 v111, v70, v72, v74\n\t" \
   "v_smfmac_f32_16x16x64_bf16 v[48:51], v[68:71], a[8:15], v134\n\t" "v_max_u32 v110, v110, v111\n\t" "v_cmp_eq_u32 vcc, -1, v110\n\t" \
   "v_smfmac_f32_16x16x64_bf16 v[52:55], v[68:71], a[72:79], v134\n\t" "s_cbranch_vccnz 8f\n\t" \
   "v_smfmac_f32_16x16x64_bf16 v[48:51], v[72:75], a[16:23], v134\n\t" "s_waitcnt lgkmcnt(0)\n\t" "s_barrier\n\t" RSTAMP(0) \
   "ds_read_b128 v[76:79], v115\n\t" "ds_read_b128 v[80:83], v116\n\t" "v_smfmac_f32_16x16x64_bf16 v[52:55], v[72:75], a[80:87], v134\n\t" \
   "ds_read_b128 v[84:87], v117\n\t" "ds_read_b128 v[88:91], v118\n\t" "v_smfmac_f32_16x16x64_bf16 v[56:59], v[64:67], a[128:135], v134\n\t" \
   "ds_read_b128 v[92:95], v119\n\t" "ds_read_b128 v[96:99], v120\n\t" "v_smfmac_f32_16x16x64_bf16 v[60:63], v[64:67], a[192:199], v134\n\t" \
   "ds_read_b128 v[100:103], v121\n\t" "v_smfmac_f32_16x16x64_bf16 v[56:59], v[68:71], a[136:143], v134\n\t" \
   "v_smfmac_f32_16x16x64_bf16 v[60:63], v[68:71], a[200:207], v134\n\t" "s_waitcnt lgkmcnt(6)\n\t" \
   "v_smfmac_f32_16x16x64_bf16 v[48:51], v[76:79], a[24:31], v134\n\t" "v_smfmac_f32_16x16x64_bf16 v[52:55], v[76:79], a[88:95], v134\n\t" \
   "s_waitcnt lgkmcnt(5)\n\t" "v_smfmac_f32_16x16x64_bf16 v[48:51], v[80:83], a[32:39], v134\n\t" \
   "v_smfmac_f32_16x16x64_bf16 v[52:55], v[80:83], a[96:103], v134\n\t" "s_waitcnt lgkmcnt(4)\n\t" \
   "v_smfmac_f32_16x16x64_bf16 v[48:51], v[84:87], a[40:47], v134\n\t" "v_smfmac_f32_16x16x64_bf16 v[52:55], v[84:87], a[104:111], v134\n\t" \
   "s_waitcnt lgkmcnt(3)\n\t" "v_smfmac_f32_16x16x64_bf16 v[48:51], v[88:91], a[48:55], v134\n\t" \
   "v_smfmac_f32_16x16x64_bf16 v[52:55], v[88:91], a[112:119], v134\n\t" "s_waitcnt lgkmcnt(2)\n\t" \
   "v_smfmac_f32_16x16x64_bf16 v[48:51], v[92:95], a[56:63], v134\n\t" "v_smfmac_f32_16x16x64_bf16 v[52:55], v[92:95], a[120:127], v134\n\t" \
   "s_waitcnt lgkmcnt(0)\n\t" "v_smfmac_f32_16x16x64_bf16 v[48:51], v[96:99], v[192:199], v134\n\t" \
   "v_smfmac_f32_16x16x64_bf16 v[48:51], v[100:103], v[200:207], v134\n\t" "v_smfmac_f32_16x16x64_bf16 v[52:55], v[96:99], v[208:215], v134\n\t" \
   "v_smfmac_f32_16x16x64_bf16 v[52:55], v[100:103], v[216:223], v134\n\t" "v_smfmac_f32_16x16x64_bf16 v[56:59], v[72:75], a[144:151], v134\n\t" \
   "v_add_f32 v48, v48, v49\n\t" "v_add_f32 v49, v50, v51\n\t" "v_smfmac_f32_16x16x64_bf16 v[60:63], v[72:75], a[208:215], v134\n\t" \
   "v_mul_f32 v48, 0x3e0293ee, v48\n\t" "v_mul_f32 v49, 0x3e0293ee, v49\n\t" "v_smfmac_f32_16x16x64_bf16 v[56:59], v[76:79], a[152:159], v134\n\t" \
   "v_add_f32 v52, v52, v53\n\t" "v_add_f32 v53, v54, v55\n\t" "v_smfmac_f32_16x16x64_bf16 v[56:59], v[80:83], a[160:167], v134\n\t" \
   "v_mul_f32 v52, 0x3e0293ee, v52\n\t" "v_mul_f32 v53, 0x3e0293ee, v53\n\t" "v_smfmac_f32_16x16x64_bf16 v[56:59], v[84:87], a[168:175], v134\n\t" \
   "v_cvt_pk_bf16_f32 v48, v48, v49\n\t" "ds_write_b32 v135, v48 offset:10240\n\t" \
   "v_smfmac_f32_16x16x64_bf16 v[56:59], v[88:91], a[176:183], v134\n\t" "v_cvt_pk_bf16_f32 v52, v52, v53\n\t" \
   "ds_write_b32 v135, v52 offset:10496\n\t" "v_smfmac_f32_16x16x64_bf16 v[56:59], v[92:95], a[184:191], v134\n\t" \
   "v_smfmac_f32_16x16x64_bf16 v[56:59], v[96:99], v[224:231], v134\n\t" "v_smfmac_f32_16x16x64_bf16 v[56:59], v[100:103], v[232:239], v134\n\t" \
   "s_waitcnt lgkmcnt(0)\n\t" "s_barrier\n\t" "ds_read_b64_tr_b16 v[64:65], v136 offset:10240\n\t" "ds_read_b64_tr_b16 v[66:67], v136 offset:10496\n\t" \
   "v_smfmac_f32_16x16x64_bf16 v[60:63], v[76:79], a[216:223], v134\n\t" "ds_read_b64_tr_b16 v[68:69], v136 offset:10752\n\t" \
   "v_smfmac_f32_16x16x64_bf16 v[60:63], v[80:83], a[224:231], v134\n\t" "ds_read_b64_tr_b16 v[70:71], v136 offset:11008\n\t" \
   "v_smfmac_f32_16x16x64_bf16 v[60:63], v[84:87], a[232:239], v134\n\t" "ds_read_b64_tr_b16 v[72:73], v136 offset:11264\n\t" \
   "v_smfmac_f32_16x16x64_bf16 v[60:63], v[88:91], a[240:247], v134\n\t" "ds_read_b64_tr_b16 v[74:75], v136 offset:11520\n\t" \
   "v_add_f32 v56, v56, v57\n\t" "v_add_f32 v57, v58, v59\n\t" "v_smfmac_f32_16x16x64_bf16 v[60:63], v[92:95], a[248:255], v134\n\t" \
   "ds_read_b64_tr_b16 v[104:105], v136 offset:11776\n\t" "ds_read_b64_tr_b16 v[106:107], v136 offset:12032\n\t" \
   "v_smfmac_f32_16x16x64_bf16 v[60:63], v[96:99], v[240:247], v134\n\t" "v_cvt_pk_bf16_f32 v56, v56, v57\n\t" \
   "ds_write_b32 v135, v56 offset:12288\n\t" "v_smfmac_f32_16x16x64_bf16 v[60:63], v[100:103], v[248:255], v134\n\t" RSTAMP(6) \
   "s_waitcnt lgkmcnt(7)\n\t" "v_mfma_f32_16x16x32_bf16 v[48:51], v[160:163], v[64:67], 0\n\t" \
   "v_mfma_f32_16x16x32_bf16 v[52:55], v[176:179], v[64:67], 0\n\t" "s_waitcnt lgkmcnt(3)\n\t" "s_nop 0\n\t" "v_add_f32 v60, v60, v61\n\t" \
   "v_add_f32 v61, v62, v63\n\t" "v_cvt_pk_bf16_f32 v60, v60, v61\n\t" "ds_write_b32 v135, v60 offset:12544\n\t" \
   "v_mfma_f32_16x16x32_bf16 v[48:51], v[164:167], v[68:71], v[48:51]\n\t" "v_mfma_f32_16x16x32_bf16 v[52:55], v[180:183], v[68:71], v[52:55]\n\t" \
   "v_mfma_f32_16x16x32_bf16 v[48:51], v[168:171], v[72:75], v[48:51]\n\t" "v_mfma_f32_16x16x32_bf16 v[52:55], v[184:187], v[72:75], v[52:55]\n\t" \
   "s_waitcnt lgkmcnt(2)\n\t" "v_mfma_f32_16x16x32_bf16 v[48:51], v[172:175], v[104:107], v[48:51]\n\t" \
   "v_mfma_f32_16x16x32_bf16 v[52:55], v[188:191], v[104:107], v[52:55]\n\t" "s_waitcnt lgkmcnt(0)\n\t" "s_barrier\n\t" \
   "ds_read_b64_tr_b16 v[144:145], v136 offset:12288\n\t" "ds_read_b64_tr_b16 v[146:147], v136 offset:12544\n\t" \
   "ds_read_b64_tr_b16 v[148:149], v136 offset:12800\n\t" "ds_read_b64_tr_b16 v[150:151], v136 offset:13056\n\t" \
   "ds_read_b64_tr_b16 v[152:153], v136 offset:13312\n\t" "ds_read_b64_tr_b16 v[154:155], v136 offset:13568\n\t" \
   "ds_read_b64_tr_b16 v[156:157], v136 offset:13824\n\t" "ds_read_b64_tr_b16 v[158:159], v136 offset:14080\n\t" "s_cmp_eq_u32 s64, 0\n\t" \
   "s_cbranch_scc1 92f\n\t" "93:\n\t" RSTAMP(1) "v_cndmask_b32 v48, v48, v52, %[hi8]\n\t" "v_cndmask_b32 v49, v49, v53, %[hi8]\n\t" \
   "v_cndmask_b32 v50, v50, v54, %[hi8]\n\t" "v_cndmask_b32 v51, v51, v55, %[hi8]\n\t" "v_exp_f32 v80, v48\n\t" "v_exp_f32 v81, v49\n\t" \
   "s_waitcnt lgkmcnt(6)\n\t" "v_mfma_f32_16x16x32_bf16 v[76:79], v[144:147], v[64:67], 0\n\t" "v_exp_f32 v82, v50\n\t" "v_exp_f32 v83, v51\n\t" \
   "s_waitcnt lgkmcnt(4)\n\t" "v_mfma_f32_16x16x32_bf16 v[76:79], v[148:151], v[68:71], v[76:79]\n\t" "v_add_f32 v96, v80, v81\n\t" \
   "v_add_f32 v103, v82, v83\n\t" "s_waitcnt lgkmcnt(2)\n\t" "v_mfma_f32_16x16x32_bf16 v[76:79], v[152:155], v[72:75], v[76:79]\n\t" \
   "v_add_f32 v94, v96, v103\n\t" "v_add_f32 v96, v96, v103\n\t" "s_waitcnt lgkmcnt(0)\n\t" \
   "v_mfma_f32_16x16x32_bf16 v[76:79], v[156:159], v[104:107], v[76:79]\n\t" "v_permlane32_swap_b32 v96, v94\n\t" "v_add_f32 v96, v96, v94\n\t" \
   "s_nop 1\n\t" "v_add_f32_dpp v96, v96, v96 row_ror:8 row_mask:0xf bank_mask:0xf\n\t" "s_nop 2\n\t" "v_exp_f32 v88, v76\n\t" "v_exp_f32 v89, v77\n\t" \
   "v_add_f32 v103, v96, v88\n\t" "v_exp_f32 v90, v78\n\t" "v_add_f32 v103, v103, v89\n\t" "v_exp_f32 v91, v79\n\t" "v_add_f32 v103, v103, v90\n\t" \
   "v_add_f32 v97, v103, v91\n\t" "v_add_f32 v103, v103, v91\n\t" "s_nop 1\n\t" "v_permlane16_swap_b32 v103, v97\n\t" "v_add_f32 v103, v103, v97\n\t" \
   "v_rcp_f32 v100, v103\n\t" "v_add_u32 v94, 0xe0800000, v103\n\t" "v_pk_mul_f32 v[80:81], v[80:81], v[100:101] op_sel_hi:[1,0]\n\t" \
   "v_cmp_lt_u32 vcc, 0x40000000, v94\n\t" "v_pk_mul_f32 v[82:83], v[82:83], v[100:101] op_sel_hi:[1,0]\n\t" "s_cbranch_vccnz 70f\n\t" \
   "v_cvt_pk_bf16_f32 v48, v80, v81\n\t" "v_cvt_pk_bf16_f32 v49, v82, v83\n\t" "buffer_store_dwordx2 v[48:49], v132, %[rs], s55 offen sc1\n\t" \
   "v_pk_mul_f32 v[88:89], v[88:89], v[100:101] op_sel_hi:[1,0]\n\t" "v_pk_mul_f32 v[90:91], v[90:91], v[100:101] op_sel_hi:[1,0]\n\t" \
   "v_cvt_pk_bf16_f32 v52, v88, v89\n\t" "v_cvt_pk_bf16_f32 v53, v90, v91\n\t" "71:\n\t" \
   "buffer_store_dwordx2 v[52:53], v133, %[rs], s65 offen sc1\n\t" "s_memtime s[62:63]\n\t" "s_memrealtime s[60:61]\n\t" \
   "buffer_store_dwordx2 v[48:49], v132, %[rs], s50 offen sc1\n\t" RSTAMP(2) "s_branch 40b\n\t" "70:\n\t" "v_max3_f32 v94, v48, v49, v50\n\t" \
   "v_max3_f32 v102, v76, v77, v78\n\t" "v_max3_f32 v94, v94, v51, v79\n\t" "v_max_f32 v92, v94, v102\n\t" "s_nop 1\n\t" \
   "v_max_f32_dpp v102, v92, v92 row_ror:8 row_mask:0xf bank_mask:0xf\n\t" "v_max_f32_dpp v94, v92, v92 row_ror:8 row_mask:0xf bank_mask:0xf\n\t" \
   "s_nop 1\n\t" "v_permlane32_swap_b32 v102, v94\n\t" "v_max_f32 v95, v102, v94\n\t" "v_max_f32 v102, v102, v94\n\t" "s_nop 1\n\t" \
   "v_permlane16_swap_b32 v102, v95\n\t" "v_max_f32 v102, v102, v95\n\t" "v_sub_f32 v80, v48, v102\n\t" "v_sub_f32 v88, v76, v102\n\t" \
   "v_sub_f32 v81, v49, v102\n\t" "v_sub_f32 v89, v77, v102\n\t" "v_sub_f32 v82, v50, v102\n\t" "v_sub_f32 v90, v78, v102\n\t" \
   "v_sub_f32 v83, v51, v102\n\t" "v_sub_f32 v91, v79, v102\n\t" "v_exp_f32 v80, v80\n\t" "v_exp_f32 v88, v88\n\t" "v_exp_f32 v81, v81\n\t" \
   "v_exp_f32 v89, v89\n\t" "v_exp_f32 v82, v82\n\t" "v_exp_f32 v90, v90\n\t" "v_exp_f32 v83, v83\n\t" "v_exp_f32 v91, v91\n\t" "s_nop 0\n\t" \
   "v_add_f32 v96, v80, v81\n\t" "v_add_f32 v103, v82, v83\n\t" "v_add_f32 v94, v96, v103\n\t" "v_add_f32 v96, v96, v103\n\t" "s_nop 1\n\t" \
   "v_permlane32_swap_b32 v96, v94\n\t" "v_add_f32 v96, v96, v94\n\t" "s_nop 1\n\t" \
   "v_add_f32_dpp v96, v96, v96 row_ror:8 row_mask:0xf bank_mask:0xf\n\t" "v_add_f32 v103, v96, v88\n\t" "v_add_f32 v103, v103, v89\n\t" \
   "v_add_f32 v103, v103, v90\n\t" "v_add_f32 v97, v103, v91\n\t" "v_add_f32 v103, v103, v91\n\t" "s_nop 1\n\t" "v_permlane16_swap_b32 v103, v97\n\t" \
   "v_add_f32 v103, v103, v97\n\t" "v_rcp_f32 v100, v103\n\t" "s_nop 0\n\t" "v_pk_mul_f32 v[80:81], v[80:81], v[100:101] op_sel_hi:[1,0]\n\t" \
   "v_pk_mul_f32 v[82:83], v[82:83], v[100:101] op_sel_hi:[1,0]\n\t" "v_pk_mul_f32 v[88:89], v[88:89], v[100:101] op_sel_hi:[1,0]\n\t" \
   "v_pk_mul_f32 v[90:91], v[90:91], v[100:101] op_sel_hi:[1,0]\n\t" "v_cvt_pk_bf16_f32 v48, v80, v81\n\t" "v_cvt_pk_bf16_f32 v49, v82, v83\n\t" \
   "v_cvt_pk_bf16_f32 v52, v88, v89\n\t" "v_cvt_pk_bf16_f32 v53, v90, v91\n\t" "v_min_u32 v48, -2, v48\n\t" "v_min_u32 v52, -2, v52\n\t" \
   "buffer_store_dwordx2 v[48:49], v132, %[rs], s55 offen sc1\n\t" "s_branch 71b\n\t" "74:\n\t" "v_max3_f32 v80, v32, v33, v34\n\t" \
   "v_max3_f32 v81, v35, v36, v37\n\t" "v_max3_f32 v82, v38, v39, v40\n\t" "v_max3_f32 v83, v41, v42, v43\n\t" "v_max3_f32 v80, v80, v81, v82\n\t" \
   "v_max_f32 v81, v80, v83\n\t" "v_max_f32 v80, v80, v83\n\t" "s_nop 1\n\t" "v_permlane16_swap_b32 v80, v81\n\t" "v_max_f32 v82, v80, v81\n\t" \
   "v_max_f32 v80, v80, v81\n\t" "s_nop 1\n\t" "v_permlane32_swap_b32 v80, v82\n\t" "v_max_f32 v80, v80, v82\n\t" "v_sub_f32 v64, v32, v80\n\t" \
   "v_sub_f32 v65, v33, v80\n\t" "v_sub_f32 v66, v34, v80\n\t" "v_sub_f32 v67, v35, v80\n\t" "v_sub_f32 v68, v36, v80\n\t" \
   "v_sub_f32 v69, v37, v80\n\t" "v_sub_f32 v70, v38, v80\n\t" "v_sub_f32 v71, v39, v80\n\t" "v_sub_f32 v72, v40, v80\n\t" \
   "v_sub_f32 v73, v41, v80\n\t" "v_sub_f32 v74, v42, v80\n\t" "v_sub_f32 v75, v43, v80\n\t" "v_exp_f32 v64, v64\n\t" "v_exp_f32 v65, v65\n\t" \
   "v_exp_f32 v66, v66\n\t" "v_exp_f32 v67, v67\n\t" "v_exp_f32 v68, v68\n\t" "v_exp_f32 v69, v69\n\t" "v_exp_f32 v70, v70\n\t" \
   "v_exp_f32 v71, v71\n\t" "v_exp_f32 v72, v72\n\t" "v_exp_f32 v73, v73\n\t" "v_exp_f32 v74, v74\n\t" "v_exp_f32 v75, v75\n\t" "s_nop 0\n\t" \
   "v_add_f32 v80, v64, v65\n\t" "v_add_f32 v81, v66, v67\n\t" "v_add_f32 v82, v68, v69\n\t" "v_add_f32 v83, v70, v71\n\t" \
   "v_add_f32 v84, v72, v73\n\t" "v_add_f32 v85, v74, v75\n\t" "v_add_f32 v80, v80, v81\n\t" "v_add_f32 v82, v82, v83\n\t" \
   "v_add_f32 v84, v84, v85\n\t" "v_add_f32 v80, v80, v82\n\t" "v_fma_f32 v82, v80, 2.0, v84\n\t" "v_fma_f32 v80, v80, 2.0, v84\n\t" "s_nop 0\n\t" \
   "s_nop 0\n\t" "v_permlane16_swap_b32 v80, v82\n\t" "v_add_f32 v81, v80, v82\n\t" "v_add_f32 v80, v80, v82\n\t" "s_nop 0\n\t" "s_nop 0\n\t" \
   "v_permlane32_swap_b32 v80, v81\n\t" "v_add_f32 v80, v80, v81\n\t" "v_rcp_f32 v90, v80\n\t" "s_nop 0\n\t" "v_add_f32 v90, v90, v90\n\t" \
   "s_branch 75b\n\t" "63:\n\t" "s_cmp_lg_u32 s64, 0\n\t" "s_cbranch_scc1 64f\n\t" "s_sub_u32 s69, s56, 0x10000\n\t" "s_and_b32 s69, s69, -2\n\t" \
   "s_branch 50b\n\t" "64:\n\t" "s_mov_b32 s69, s48\n\t" "s_cmp_lt_u32 s64, 3\n\t" "s_cselect_b32 s69, -1, s69\n\t" "s_cmp_lg_u32 s69, -1\n\t" \
   "s_cbranch_scc1 50b\n\t" "s_add_u32 s76, s76, 0x1000000\n\t" "s_mov_b32 s73, 0\n\t" "65:\n\t" "s_add_u32 s73, s73, 1\n\t" \
   "s_min_u32 s72, s73, 7\n\t" "s_mul_i32 s72, s72, 5\n\t" "s_add_u32 s72, s72, %[xs]\n\t" "s_and_b32 s72, s72, 7\n\t" "s_lshl_b32 s71, %[xs], 7\n\t" \
   "v_subrev_u32 v109, s71, v139\n\t" "s_lshl_b32 s72, s72, 7\n\t" "s_add_u32 s72, s72, s66\n\t" "s_cmp_lt_u32 s73, 7\n\t" "s_cbranch_scc1 66f\n\t" \
   "s_sleep 4\n\t" "66:\n\t" "buffer_load_dword v108, v109, %[rs], s72 offen sc1\n\t" "s_waitcnt vmcnt(0)\n\t" "v_readfirstlane_b32 s69, v108\n\t" \
   "s_cmp_gt_u32 s73, 0x10000\n\t" "s_cbranch_scc1 50b\n\t" "s_cmp_eq_u32 s69, -1\n\t" "s_cbranch_scc1 65b\n\t" "s_branch 50b\n\t" "8:\n\t" \
   "s_nop 7\n\t" "s_waitcnt vmcnt(6) lgkmcnt(0)\n\t" "s_add_u32 s70, s70, 1\n\t" "s_cmp_gt_u32 s70, 2\n\t" "s_cbranch_scc1 81f\n\t" \
   "s_lshl_b32 s71, s70, 3\n\t" "s_lshl_b32 s71, 1, s71\n\t" "s_add_u32 s76, s76, s71\n\t" "81:\n\t" "v_mov_b32 v48, 0\n\t" "v_mov_b32 v49, 0\n\t" \
   "v_mov_b32 v50, 0\n\t" "v_mov_b32 v51, 0\n\t" "v_mov_b32 v52, 0\n\t" "v_mov_b32 v53, 0\n\t" "v_mov_b32 v54, 0\n\t" "v_mov_b32 v55, 0\n\t" \
   "v_mov_b32 v56, 0\n\t" "v_mov_b32 v57, 0\n\t" "v_mov_b32 v58, 0\n\t" "v_mov_b32 v59, 0\n\t" "v_mov_b32 v60, 0\n\t" "v_mov_b32 v61, 0\n\t" \
   "v_mov_b32 v62, 0\n\t" "v_mov_b32 v63, 0\n\t" "s_min_u32 s71, s70, 7\n\t" "s_lshr_b32 s67, s71, 1\n\t" "s_add_u32 s67, s67, %[xs]\n\t" \
   "s_and_b32 s67, s67, 3\n\t" "s_lshl_b32 s67, s67, 11\n\t" "s_and_b32 s72, s71, 1\n\t" "s_lshl_b32 s72, s72, 14\n\t" "s_add_u32 s67, s67, s72\n\t" \
   "s_add_u32 s67, s67, s66\n\t" "s_cmp_eq_u32 s70, 1\n\t" "s_cselect_b32 s67, s68, s67\n\t" "s_cmp_lt_u32 s70, 7\n\t" "s_cbranch_scc1 20b\n\t" \
   "s_sleep 3\n\t" "s_cmp_lt_u32 s70, 0x20000\n\t" "s_cbranch_scc1 20b\n\t" "s_mov_b32 s67, %[esc]\n\t" "s_mov_b32 s68, %[esc]\n\t" "s_branch 20b\n\t" \
   "27:\n\t" "v_mov_b32 v98, 0x7ffffff0\n\t" "buffer_store_dword v98, v98, %[rs], 0 offen\n\t" "buffer_store_dword v98, v98, %[rs], 0 offen\n\t" \
   "buffer_store_dword v98, v98, %[rs], 0 offen\n\t" "buffer_store_dword v98, v98, %[rs], 0 offen\n\t" \
   "buffer_store_dword v98, v98, %[rs], 0 offen\n\t" "buffer_store_dword v98, v98, %[rs], 0 offen\n\t" "s_branch 28b\n\t" "141:\n\t" \
   "s_mov_b32 s74, 0\n\t" "s_cmp_eq_u32 s64, 0\n\t" "s_cbranch_scc1 41b\n\t" "42:\n\t" "buffer_load_dword v15, v140, %[rs], s59 offen sc1\n\t" \
   "s_add_u32 s74, s74, 1\n\t" "s_waitcnt vmcnt(0)\n\t" "v_cmp_eq_u32 vcc, -1, v15\n\t" "s_cmp_gt_u32 s74, 0x10000\n\t" "s_cbranch_scc1 41b\n\t" \
   "s_cbranch_vccz 41b\n\t" "s_sleep 2\n\t" "s_branch 42b\n\t" "95:\n\t" "s_movk_i32 s59, 0x1000\n\t" "s_branch 197f\n\t" "97:\n\t" \
   "buffer_load_dwordx4 v[16:19], v138, %[rs], s79 offen offset:0 sc1\n\t" "buffer_load_dwordx4 v[20:23], v138, %[rs], s79 offen offset:1024 sc1\n\t" \
   "buffer_load_dwordx4 v[24:27], v138, %[rs], s79 offen offset:2048 sc1\n\t" \
   "buffer_load_dwordx4 v[28:31], v138, %[rs], s79 offen offset:3072 sc1\n\t" "v_or_b32 v108, s49, v141\n\t" "v_or_b32 v109, s49, v142\n\t" \
   "buffer_store_dwordx4 v[44:47], v108, %[rs], s52 offen sc1\n\t" "buffer_store_dwordx2 v[32:33], v109, %[rs], s51 offen sc1\n\t" \
   "s_waitcnt vmcnt(0)\n\t" "s_branch 96b\n\t" "197:\n\t" "s_mov_b32 s74, 0\n\t" "98:\n\t" "buffer_load_dword v15, v140, %[rs], s59 offen sc1\n\t" \
   "s_add_u32 s74, s74, 1\n\t" "s_waitcnt vmcnt(0)\n\t" "v_cmp_eq_u32 vcc, -1, v15\n\t" "s_cmp_gt_u32 s74, 0x10000\n\t" "s_cbranch_scc1 97b\n\t" \
   "s_cbranch_vccz 97b\n\t" "s_sleep 2\n\t" "s_branch 98b\n\t" "92:\n\t" "s_mov_b32 s59, 0\n\t" "s_branch 145f\n\t" "45:\n\t" \
   "v_mbcnt_lo_u32_b32 v98, -1, 0\n\t" "v_mbcnt_hi_u32_b32 v98, -1, v98\n\t" "v_lshlrev_b32 v98, 4, v98\n\t" "s_add_u32 s72, %[kf0], 4096\n\t" \
   "buffer_load_dwordx4 v[160:163], v98, %[rs], %[kf0] offen offset:0 sc1\n\t" \
   "buffer_load_dwordx4 v[164:167], v98, %[rs], %[kf0] offen offset:1024 sc1\n\t" \
   "buffer_load_dwordx4 v[168:171], v98, %[rs], %[kf0] offen offset:2048 sc1\n\t" \
   "buffer_load_dwordx4 v[172:175], v98, %[rs], %[kf0] offen offset:3072 sc1\n\t" \
   "buffer_load_dwordx4 v[176:179], v98, %[rs], s72 offen offset:0 sc1\n\t" "buffer_load_dwordx4 v[180:183], v98, %[rs], s72 offen offset:1024 sc1\n\t" \
   "buffer_load_dwordx4 v[184:187], v98, %[rs], s72 offen offset:2048 sc1\n\t" \
   "buffer_load_dwordx4 v[188:191], v98, %[rs], s72 offen offset:3072 sc1\n\t" "s_waitcnt vmcnt(0)\n\t" \
   "v_mfma_f32_16x16x32_bf16 v[48:51], v[160:163], v[64:67], 0\n\t" "v_mfma_f32_16x16x32_bf16 v[52:55], v[176:179], v[64:67], 0\n\t" \
   "v_mfma_f32_16x16x32_bf16 v[48:51], v[164:167], v[68:71], v[48:51]\n\t" "v_mfma_f32_16x16x32_bf16 v[52:55], v[180:183], v[68:71], v[52:55]\n\t" \
   "v_mfma_f32_16x16x32_bf16 v[48:51], v[168:171], v[72:75], v[48:51]\n\t" "v_mfma_f32_16x16x32_bf16 v[52:55], v[184:187], v[72:75], v[52:55]\n\t" \
   "v_mfma_f32_16x16x32_bf16 v[48:51], v[172:175], v[104:107], v[48:51]\n\t" "v_mfma_f32_16x16x32_bf16 v[52:55], v[188:191], v[104:107], v[52:55]\n\t" \
   "s_nop 7\n\t" "s_branch 93b\n\t" "145:\n\t" "s_mov_b32 s74, 0\n\t" "46:\n\t" "buffer_load_dword v15, v140, %[rs], s59 offen sc1\n\t" \
   "s_add_u32 s74, s74, 1\n\t" "s_waitcnt vmcnt(0)\n\t" "v_cmp_eq_u32 vcc, -1, v15\n\t" "s_cmp_gt_u32 s74, 0x10000\n\t" "s_cbranch_scc1 45b\n\t" \
   "s_cbranch_vccz 45b\n\t" "s_sleep 2\n\t" "s_branch 46b\n\t" "90:\n\t" "v_or_b32 v108, s49, v141\n\t" "v_or_b32 v109, s49, v142\n\t" \
   "buffer_store_dwordx4 v[44:47], v108, %[rs], s52 offen sc1\n\t" "buffer_store_dwordx2 v[32:33], v109, %[rs], s51 offen sc1\n\t" \
   "s_waitcnt vmcnt(0) lgkmcnt(0)"
#define P1_CLOB "v15", "v16", "v17", "v18", "v19", "v20", "v21", "v22", "v23", "v24", "v25", "v26", "v27", "v28", "v29", "v30", "v31", "v32", "v33", "v34", "v35", "v36", "v37", "v38", "v39", "v40", "v41", "v42", "v43", "v44", "v45", "v46", "v47", "v48", "v49", "v50", "v51", "v52", "v53", "v54", "v55", "v56", "v57", "v58", "v59", "v60", "v61", "v62", "v63", "v64", "v65", "v66", "v67", "v68", "v69", "v70", "v71", "v72", "v73", "v74", "v75", "v76", "v77", "v78", "v79", "v80", "v81", "v82", "v83", "v84", "v85", "v86", "v87", "v88", "v89", "v90", "v91", "v92", "v93", "v94", "v95", "v96", "v97", "v98", "v99", "v100", "v101", "v102", "v103", "v104", "v105", "v106", "v107", "v108", "v109", "v110", "v111", "v144", "v145", "v146", "v147", "v148", "v149", "v150", "v151", "v152", "v153", "v154", "v155", "v156", "v157", "v158", "v159", "v160", "v161", "v162", "v163", "v164", "v165", "v166", "v167", "v168", "v169", "v170", "v171", "v172", "v173", "v174", "v175", "v176", "v177", "v178", "v179", "v180", "v181", "v182", "v183", "v184", "v185", "v186", "v187", "v188", "v189", "v190", "v191", "s60", "s61", "s62", "s63", "s64", "s65", "s66", "s67", "s68", "s69", "s70", "s71", "s72", "s73", "s74", "s75", "s47", "s48", "s49", "s50", "s51", "s52", "s54", "s55", "s79", "s20", "s21", "s22", "s23", "s24", "s25", "s26", "s27", "s28", "s29", "s30", "s31", "s32", "s33", "s34", "s35"
#define PV_ASM \
   "s_mov_b32 s64, -1\n\t" "s_mov_b32 s76, 0\n\t" "s_mov_b32 s77, 0\n\t" "s_mov_b32 s71, 63\n\t" "s_mov_b32 s47, 1300\n\t" "s_mov_b32 s70, 0\n\t" \
   "s_mov_b32 s69, 0\n\t" "s_lshr_b64 s[72:73], %[sel], s71\n\t" "s_lshl_b32 s65, s71, 19\n\t" "s_and_b32 s72, s72, 1\n\t" \
   "s_add_u32 s65, s65, %[pool]\n\t" "s_lshl_b32 s72, s72, 13\n\t" "s_add_u32 s65, s65, s72\n\t" "s_lshr_b64 s[72:73], %[s3], s71\n\t" \
   "s_and_b32 s54, s72, 1\n\t" "s_lshl_b32 s54, s54, 14\n\t" "40:\n\t" "s_cmp_lt_i32 s64, 0\n\t" "s_cselect_b32 s72, 0x7ffffff0, 0\n\t" \
   "s_max_i32 s71, s64, 0\n\t" "v_or_b32 v109, s72, v143\n\t" "s_lshl_b32 s71, s71, 12\n\t" "v_mov_b32 v110, 1\n\t" "s_cmp_eq_u32 s64, 63\n\t" \
   "s_cbranch_scc1 70f\n\t" "71:\n\t" "s_waitcnt vmcnt(0)\n\t" RSTAMP(3) "s_barrier\n\t" "buffer_load_dword v108, v139, %[rs], s65 offen sc1\n\t" \
   "buffer_store_dword v110, v109, %[rs], s71 offen sc1\n\t" PVTRACE "s_add_u32 s48, s69, s47\n\t" "s_cmp_eq_u32 s70, 0\n\t" \
   "s_cselect_b32 s48, s48, -1\n\t" "s_mov_b32 s66, s65\n\t" "s_add_i32 s64, s64, 1\n\t" "s_and_b32 s71, s64, 63\n\t" \
   "s_lshr_b64 s[72:73], %[sel], s71\n\t" "s_lshl_b32 s65, s71, 19\n\t" "s_and_b32 s72, s72, 1\n\t" "s_add_u32 s65, s65, %[pool]\n\t" \
   "s_lshl_b32 s72, s72, 13\n\t" "s_add_u32 s65, s65, s72\n\t" "s_xor_b32 s67, s54, %[lad0]\n\t" "s_xor_b32 s68, s67, 0x4000\n\t" \
   "s_add_u32 s67, s67, s66\n\t" "s_add_u32 s68, s68, s66\n\t" "s_lshr_b64 s[72:73], %[s3], s71\n\t" "s_and_b32 s54, s72, 1\n\t" \
   "s_lshl_b32 s54, s54, 14\n\t" "s_mov_b32 s70, 0\n\t" "s_lshr_b64 s[72:73], %[ysel], s71\n\t" "s_mul_i32 s73, s71, 43\n\t" \
   "s_mul_i32 s49, s71, 80\n\t" "s_and_b32 s72, s72, 1\n\t" "s_lshr_b32 s73, s73, 7\n\t" "s_add_u32 s49, s49, s72\n\t" \
   "s_lshl4_add_u32 s49, s73, s49\n\t" "s_lshl_b32 s49, s49, 13\n\t" "s_add_u32 s49, s49, %[yb]\n\t" "s_add_u32 s50, s49, 32768\n\t" \
   "s_add_u32 s51, s49, 65536\n\t" "s_add_u32 s52, s49, 98304\n\t" "s_add_u32 s53, s49, 131072\n\t" "v_mov_b32 v48, 0\n\t" "v_mov_b32 v49, 0\n\t" \
   "v_mov_b32 v50, 0\n\t" "v_mov_b32 v51, 0\n\t" "v_mov_b32 v52, 0\n\t" "v_mov_b32 v53, 0\n\t" "v_mov_b32 v54, 0\n\t" "v_mov_b32 v55, 0\n\t" \
   "v_mov_b32 v144, 0\n\t" "v_mov_b32 v145, 0\n\t" "v_mov_b32 v146, 0\n\t" "v_mov_b32 v147, 0\n\t" "v_mov_b32 v148, 0\n\t" "v_mov_b32 v149, 0\n\t" \
   "v_mov_b32 v150, 0\n\t" "v_mov_b32 v151, 0\n\t" "v_mov_b32 v152, 0\n\t" "v_mov_b32 v153, 0\n\t" "v_mov_b32 v154, 0\n\t" "v_mov_b32 v155, 0\n\t" \
   "v_mov_b32 v156, 0\n\t" "v_mov_b32 v157, 0\n\t" "v_mov_b32 v158, 0\n\t" "v_mov_b32 v159, 0\n\t" "v_mov_b32 v160, 0\n\t" "v_mov_b32 v161, 0\n\t" \
   "v_mov_b32 v162, 0\n\t" "v_mov_b32 v163, 0\n\t" "s_cmp_eq_u32 s64, 64\n\t" "s_cbranch_scc1 90f\n\t" "s_waitcnt vmcnt(1)\n\t" \
   "v_readlane_b32 s69, v108, 0\n\t" "v_readlane_b32 s71, v108, 1\n\t" "s_cmp_eq_u32 s69, -1\n\t" "s_cbranch_scc1 63f\n\t" "s_cmp_eq_u32 s71, -1\n\t" \
   "s_cselect_b32 s47, s47, s71\n\t" "50:\n\t" "s_add_u32 s73, s69, %[m1c]\n\t" RSTAMP(5) "51:\n\t" "s_memrealtime s[60:61]\n\t" \
   "s_memtime s[62:63]\n\t" "s_waitcnt lgkmcnt(0)\n\t" "s_sub_u32 s74, s62, s57\n\t" "s_mul_i32 s74, s74, s58\n\t" "s_lshr_b32 s74, s74, 12\n\t" \
   "s_add_u32 s74, s74, s56\n\t" "s_lshl_b32 s60, s60, 3\n\t" "s_sub_u32 s75, s74, s60\n\t" "s_cmp_lt_u32 s75, 32\n\t" "s_cbranch_scc1 52f\n\t" \
   "s_cmp_lt_i32 s75, 0\n\t" "s_cselect_b32 s61, 0, 31\n\t" "s_add_u32 s74, s60, s61\n\t" "s_sub_i32 s75, s61, s75\n\t" "s_add_i32 s75, s75, 1\n\t" \
   "s_ashr_i32 s75, s75, 1\n\t" "s_min_i32 s75, s75, 8\n\t" "s_max_i32 s75, s75, -8\n\t" "s_mov_b32 s56, s74\n\t" "s_mov_b32 s57, s62\n\t" \
   "s_add_u32 s58, s58, s75\n\t" "52:\n\t" "s_sub_i32 s74, s73, s74\n\t" "s_cmp_le_i32 s74, 150\n\t" "s_cbranch_scc1 53f\n\t" \
   "s_cmp_lt_i32 s74, 260\n\t" "s_cbranch_scc1 51b\n\t" "s_sleep 2\n\t" "s_cmp_lt_i32 s74, 32000\n\t" "s_cbranch_scc1 51b\n\t" "s_mov_b32 s74, 0\n\t" \
   "53:\n\t" "s_sub_i32 s74, s74, 56\n\t" "s_max_i32 s74, s74, 0\n\t" "s_mul_i32 s74, s74, 23\n\t" "s_lshr_b32 s74, s74, 5\n\t" \
   "s_and_b32 s74, s74, -4\n\t" "s_getpc_b64 s[60:61]\n\t" "54:\n\t" "s_sub_u32 s74, 55f-54b, s74\n\t" "s_add_u32 s60, s60, s74\n\t" \
   "s_addc_u32 s61, s61, 0\n\t" "s_setpc_b64 s[60:61]\n\t" "s_nop 3\n\t" "s_nop 3\n\t" "s_nop 3\n\t" "s_nop 3\n\t" "s_nop 3\n\t" "s_nop 3\n\t" \
   "s_nop 3\n\t" "s_nop 3\n\t" "s_nop 3\n\t" "s_nop 3\n\t" "s_nop 3\n\t" "s_nop 3\n\t" "s_nop 3\n\t" "s_nop 3\n\t" "s_nop 3\n\t" "s_nop 3\n\t" \
   "s_nop 3\n\t" "s_nop 3\n\t" "s_nop 3\n\t" "s_nop 3\n\t" "s_nop 3\n\t" "s_nop 3\n\t" "s_nop 3\n\t" "s_nop 3\n\t" "55:\n\t" "20:\n\t" RSTAMP(4) \
   "buffer_load_dwordx4 v[64:67], v128, %[rs], s67 offen sc1\n\t" "buffer_load_dwordx4 v[68:71], v129, %[rs], s67 offen sc1\n\t" \
   "buffer_load_dwordx4 v[72:75], v130, %[rs], s67 offen sc1\n\t" "s_waitcnt vmcnt(2)\n\t" "ds_write_b128 v112, v[64:67]\n\t" \
   "v_smfmac_f32_16x16x64_bf16 v[48:51], v[64:67], a[0:7], v134\n\t" "s_waitcnt vmcnt(1)\n\t" "ds_write_b128 v113, v[68:71]\n\t" \
   "s_waitcnt vmcnt(0)\n\t" "ds_write_b128 v114, v[72:75]\n\t" RSTAMP(7) "v_smfmac_f32_16x16x64_bf16 v[52:55], v[64:67], a[64:71], v134\n\t" \
   "v_max3_u32 v110, v64, v66, v68\n\t" "v_max3_u32 v111, v70, v72, v74\n\t" "v_smfmac_f32_16x16x64_bf16 v[48:51], v[68:71], a[8:15], v134\n\t" \
   "v_max_u32 v110, v110, v111\n\t" "v_cmp_eq_u32 vcc, -1, v110\n\t" "v_smfmac_f32_16x16x64_bf16 v[52:55], v[68:71], a[72:79], v134\n\t" \
   "s_cbranch_vccnz 8f\n\t" "s_waitcnt lgkmcnt(0)\n\t" "s_barrier\n\t" RSTAMP(0) "ds_read_b128 v[76:79], v115\n\t" "ds_read_b128 v[80:83], v116\n\t" \
   "ds_read_b128 v[84:87], v117\n\t" "v_smfmac_f32_16x16x64_bf16 v[48:51], v[72:75], a[16:23], v134\n\t" "ds_read_b128 v[88:91], v118\n\t" \
   "ds_read_b128 v[92:95], v119\n\t" "v_smfmac_f32_16x16x64_bf16 v[52:55], v[72:75], a[80:87], v134\n\t" "ds_read_b128 v[96:99], v120\n\t" \
   "ds_read_b128 v[100:103], v121\n\t" "s_waitcnt lgkmcnt(6)\n\t" "v_smfmac_f32_16x16x64_bf16 v[48:51], v[76:79], a[24:31], v134\n\t" \
   "v_smfmac_f32_16x16x64_bf16 v[52:55], v[76:79], a[88:95], v134\n\t" "s_waitcnt lgkmcnt(5)\n\t" \
   "v_smfmac_f32_16x16x64_bf16 v[48:51], v[80:83], a[32:39], v134\n\t" "v_smfmac_f32_16x16x64_bf16 v[52:55], v[80:83], a[96:103], v134\n\t" \
   "s_waitcnt lgkmcnt(4)\n\t" "v_smfmac_f32_16x16x64_bf16 v[48:51], v[84:87], a[40:47], v134\n\t" \
   "v_smfmac_f32_16x16x64_bf16 v[52:55], v[84:87], a[104:111], v134\n\t" "s_waitcnt lgkmcnt(3)\n\t" \
   "v_smfmac_f32_16x16x64_bf16 v[48:51], v[88:91], a[48:55], v134\n\t" "v_smfmac_f32_16x16x64_bf16 v[52:55], v[88:91], a[112:119], v134\n\t" \
   "s_waitcnt lgkmcnt(0)\n\t" "v_smfmac_f32_16x16x64_bf16 v[48:51], v[92:95], a[56:63], v134\n\t" \
   "v_smfmac_f32_16x16x64_bf16 v[48:51], v[96:99], v[192:199], v134\n\t" "v_smfmac_f32_16x16x64_bf16 v[48:51], v[100:103], v[200:207], v134\n\t" \
   "v_smfmac_f32_16x16x64_bf16 v[52:55], v[92:95], a[120:127], v134\n\t" "v_smfmac_f32_16x16x64_bf16 v[52:55], v[96:99], v[208:215], v134\n\t" \
   "v_smfmac_f32_16x16x64_bf16 v[52:55], v[100:103], v[216:223], v134\n\t" "v_add_f32 v48, v48, v49\n\t" "v_add_f32 v49, v50, v51\n\t" \
   "v_cvt_pk_bf16_f32 v48, v48, v49\n\t" "ds_write_b32 v135, v48 offset:10240\n\t" "s_nop 3\n\t" "v_add_f32 v52, v52, v53\n\t" \
   "v_add_f32 v53, v54, v55\n\t" "v_cvt_pk_bf16_f32 v52, v52, v53\n\t" "ds_write_b32 v135, v52 offset:10496\n\t" "s_waitcnt lgkmcnt(0)\n\t" \
   "s_barrier\n\t" RSTAMP(6) "ds_read_b64_tr_b16 v[16:17], v136 offset:10240\n\t" "ds_read_b64_tr_b16 v[18:19], v136 offset:10496\n\t" \
   "ds_read_b64_tr_b16 v[20:21], v136 offset:10752\n\t" "ds_read_b64_tr_b16 v[22:23], v136 offset:11008\n\t" \
   "ds_read_b64_tr_b16 v[24:25], v136 offset:11264\n\t" "ds_read_b64_tr_b16 v[26:27], v136 offset:11520\n\t" \
   "ds_read_b64_tr_b16 v[28:29], v136 offset:11776\n\t" "ds_read_b64_tr_b16 v[30:31], v136 offset:12032\n\t" "s_waitcnt lgkmcnt(6)\n\t" \
   "v_smfmac_f32_16x16x64_bf16 v[144:147], v[16:19], a[128:135], v131\n\t" "v_smfmac_f32_16x16x64_bf16 v[148:151], v[16:19], a[160:167], v131\n\t" \
   "v_smfmac_f32_16x16x64_bf16 v[152:155], v[16:19], a[192:199], v131\n\t" "v_smfmac_f32_16x16x64_bf16 v[156:159], v[16:19], a[224:231], v131\n\t" \
   "v_smfmac_f32_16x16x64_bf16 v[160:163], v[16:19], v[224:231], v131\n\t" "s_waitcnt lgkmcnt(0)\n\t" \
   "v_smfmac_f32_16x16x64_bf16 v[144:147], v[20:23], a[136:143], v131\n\t" "v_smfmac_f32_16x16x64_bf16 v[144:147], v[24:27], a[144:151], v131\n\t" \
   "v_smfmac_f32_16x16x64_bf16 v[144:147], v[28:31], a[152:159], v131\n\t" "v_smfmac_f32_16x16x64_bf16 v[148:151], v[20:23], a[168:175], v131\n\t" \
   "v_smfmac_f32_16x16x64_bf16 v[148:151], v[24:27], a[176:183], v131\n\t" "v_smfmac_f32_16x16x64_bf16 v[148:151], v[28:31], a[184:191], v131\n\t" \
   "v_smfmac_f32_16x16x64_bf16 v[152:155], v[20:23], a[200:207], v131\n\t" "v_cvt_pk_bf16_f32 v164, v144, v145\n\t" \
   "v_cvt_pk_bf16_f32 v165, v146, v147\n\t" "v_smfmac_f32_16x16x64_bf16 v[152:155], v[24:27], a[208:215], v131\n\t" "v_min_u32 v164, -2, v164\n\t" \
   "buffer_store_dwordx2 v[164:165], v132, %[rs], s49 offen sc1\n\t" "v_smfmac_f32_16x16x64_bf16 v[152:155], v[28:31], a[216:223], v131\n\t" \
   "v_smfmac_f32_16x16x64_bf16 v[156:159], v[20:23], a[232:239], v131\n\t" "v_cvt_pk_bf16_f32 v166, v148, v149\n\t" \
   "v_cvt_pk_bf16_f32 v167, v150, v151\n\t" "v_smfmac_f32_16x16x64_bf16 v[156:159], v[24:27], a[240:247], v131\n\t" "v_min_u32 v166, -2, v166\n\t" \
   "buffer_store_dwordx2 v[166:167], v132, %[rs], s50 offen sc1\n\t" "v_smfmac_f32_16x16x64_bf16 v[156:159], v[28:31], a[248:255], v131\n\t" \
   "v_smfmac_f32_16x16x64_bf16 v[160:163], v[20:23], v[232:239], v131\n\t" "v_cvt_pk_bf16_f32 v168, v152, v153\n\t" \
   "v_cvt_pk_bf16_f32 v169, v154, v155\n\t" "v_smfmac_f32_16x16x64_bf16 v[160:163], v[24:27], v[240:247], v131\n\t" "v_min_u32 v168, -2, v168\n\t" \
   "buffer_store_dwordx2 v[168:169], v132, %[rs], s51 offen sc1\n\t" "v_smfmac_f32_16x16x64_bf16 v[160:163], v[28:31], v[248:255], v131\n\t" \
   "v_cvt_pk_bf16_f32 v170, v156, v157\n\t" "v_cvt_pk_bf16_f32 v171, v158, v159\n\t" "v_min_u32 v170, -2, v170\n\t" \
   "buffer_store_dwordx2 v[170:171], v132, %[rs], s52 offen sc1\n\t" "s_nop 3\n\t" "v_cvt_pk_bf16_f32 v172, v160, v161\n\t" \
   "v_cvt_pk_bf16_f32 v173, v162, v163\n\t" "v_min_u32 v172, -2, v172\n\t" "buffer_store_dwordx2 v[172:173], v132, %[rs], s53 offen sc1\n\t" RSTAMP(2) \
   "s_branch 40b\n\t" "70:\n\t" "s_mov_b32 s46, 0\n\t" "s_lshr_b64 s[72:73], %[sel], s46\n\t" "s_lshl_b32 s45, s46, 19\n\t" "s_and_b32 s72, s72, 1\n\t" \
   "s_add_u32 s45, s45, %[pool]\n\t" "s_lshl_b32 s72, s72, 13\n\t" "s_add_u32 s45, s45, s72\n\t" "v_mov_b32 v76, -1\n\t" "v_mov_b32 v77, -1\n\t" \
   "v_mov_b32 v78, -1\n\t" "v_mov_b32 v79, -1\n\t" "buffer_store_dwordx4 v[76:79], v133, %[rs], s45 offen sc1\n\t" \
   "buffer_store_dwordx4 v[76:79], v133, %[rs], s45 offen offset:1024 sc1\n\t" "s_branch 71b\n\t" "63:\n\t" "s_cmp_lg_u32 s64, 0\n\t" \
   "s_cbranch_scc1 64f\n\t" "s_sub_u32 s69, s56, 0x10000\n\t" "s_and_b32 s69, s69, -2\n\t" "s_branch 50b\n\t" "64:\n\t" "s_mov_b32 s69, s48\n\t" \
   "s_cmp_lt_u32 s64, 3\n\t" "s_cselect_b32 s69, -1, s69\n\t" "s_cmp_lg_u32 s69, -1\n\t" "s_cbranch_scc1 50b\n\t" "s_add_u32 s76, s76, 0x1000000\n\t" \
   "s_mov_b32 s73, 0\n\t" "65:\n\t" "s_add_u32 s73, s73, 1\n\t" "s_sleep 6\n\t" "s_and_b32 s72, s73, 3\n\t" "s_add_u32 s72, s72, 8\n\t" \
   "s_sub_u32 s72, s72, %[xs]\n\t" "s_lshl_b32 s72, s72, 7\n\t" "s_add_u32 s72, s72, s66\n\t" "buffer_load_dword v108, v139, %[rs], s72 offen sc1\n\t" \
   "s_waitcnt vmcnt(0)\n\t" "v_readfirstlane_b32 s69, v108\n\t" "s_cmp_gt_u32 s73, 0x10000\n\t" "s_cbranch_scc1 50b\n\t" "s_cmp_eq_u32 s69, -1\n\t" \
   "s_cbranch_scc1 65b\n\t" "s_branch 50b\n\t" "8:\n\t" "s_nop 7\n\t" "s_waitcnt lgkmcnt(0)\n\t" "s_add_u32 s70, s70, 1\n\t" "s_cmp_gt_u32 s70, 2\n\t" \
   "s_cbranch_scc1 81f\n\t" "s_lshl_b32 s71, s70, 3\n\t" "s_lshl_b32 s71, 1, s71\n\t" "s_add_u32 s76, s76, s71\n\t" "81:\n\t" "v_mov_b32 v48, 0\n\t" \
   "v_mov_b32 v49, 0\n\t" "v_mov_b32 v50, 0\n\t" "v_mov_b32 v51, 0\n\t" "v_mov_b32 v52, 0\n\t" "v_mov_b32 v53, 0\n\t" "v_mov_b32 v54, 0\n\t" \
   "v_mov_b32 v55, 0\n\t" "s_add_u32 s71, s70, 1\n\t" "s_min_u32 s71, s71, 7\n\t" "s_lshr_b32 s67, s71, 1\n\t" "s_add_u32 s67, s67, %[xs]\n\t" \
   "s_and_b32 s67, s67, 3\n\t" "s_lshl_b32 s67, s67, 11\n\t" "s_and_b32 s72, s71, 1\n\t" "s_lshl_b32 s72, s72, 14\n\t" "s_add_u32 s67, s67, s72\n\t" \
   "s_add_u32 s67, s67, s66\n\t" "s_cmp_lt_u32 s70, 6\n\t" "s_cbranch_scc1 20b\n\t" "s_sleep 3\n\t" "s_cmp_lt_u32 s70, 0x20000\n\t" \
   "s_cbranch_scc1 20b\n\t" "s_mov_b32 s67, %[esc]\n\t" "s_branch 20b\n\t" "90:\n\t" "s_waitcnt vmcnt(0) lgkmcnt(0)"
#define PV_CLOB "v16", "v17", "v18", "v19", "v20", "v21", "v22", "v23", "v24", "v25", "v26", "v27", "v28", "v29", "v30", "v31", "v48", "v49", "v50", "v51", "v52", "v53", "v54", "v55", "v56", "v57", "v58", "v59", "v60", "v61", "v62", "v63", "v64", "v65", "v66", "v67", "v68", "v69", "v70", "v71", "v72", "v73", "v74", "v75", "v76", "v77", "v78", "v79", "v80", "v81", "v82", "v83", "v84", "v85", "v86", "v87", "v88", "v89", "v90", "v91", "v92", "v93", "v94", "v95", "v96", "v97", "v98", "v99", "v100", "v101", "v102", "v103", "v104", "v105", "v106", "v107", "v108", "v109", "v110", "v111", "v144", "v145", "v146", "v147", "v148", "v149", "v150", "v151", "v152", "v153", "v154", "v155", "v156", "v157", "v158", "v159", "v160", "v161", "v162", "v163", "v164", "v165", "v166", "v167", "v168", "v169", "v170", "v171", "v172", "v173", "v174", "v175", "s60", "s61", "s62", "s63", "s64", "s65", "s66", "s67", "s68", "s69", "s70", "s71", "s72", "s73", "s74", "s75", "s45", "s46", "s47", "s48", "s49", "s50", "s51", "s52", "s53", "s54"
#define WAIT_ASM \
   "11:\n\t" "s_memrealtime s[60:61]\n\t" "s_memtime s[62:63]\n\t" "s_waitcnt lgkmcnt(0)\n\t" "s_sub_u32 s74, s62, s57\n\t" \
   "s_mul_i32 s74, s74, s58\n\t" "s_lshr_b32 s74, s74, 12\n\t" "s_add_u32 s74, s74, s56\n\t" "s_lshl_b32 s60, s60, 3\n\t" "s_sub_u32 s75, s74, s60\n\t" \
   "s_cmp_lt_u32 s75, 32\n\t" "s_cbranch_scc1 12f\n\t" "s_cmp_lt_i32 s75, 0\n\t" "s_cselect_b32 s61, 0, 31\n\t" "s_add_u32 s74, s60, s61\n\t" \
   "s_sub_i32 s75, s61, s75\n\t" "s_add_i32 s75, s75, 1\n\t" "s_ashr_i32 s75, s75, 1\n\t" "s_min_i32 s75, s75, 8\n\t" "s_max_i32 s75, s75, -8\n\t" \
   "s_mov_b32 s56, s74\n\t" "s_mov_b32 s57, s62\n\t" "s_add_u32 s58, s58, s75\n\t" "12:\n\t" "s_sub_i32 s74, %[ta], s74\n\t" \
   "s_cmp_le_i32 s74, 150\n\t" "s_cbranch_scc1 13f\n\t" "s_cmp_lt_i32 s74, 260\n\t" "s_cbranch_scc1 11b\n\t" "s_sleep 2\n\t" \
   "s_cmp_lt_i32 s74, 32000\n\t" "s_cbranch_scc1 11b\n\t" "s_mov_b32 s74, 0\n\t" "13:\n\t" "s_sub_i32 s74, s74, 56\n\t" "s_max_i32 s74, s74, 0\n\t" \
   "s_mul_i32 s74, s74, 23\n\t" "s_lshr_b32 s74, s74, 5\n\t" "s_and_b32 s74, s74, -4\n\t" "s_getpc_b64 s[60:61]\n\t" "14:\n\t" \
   "s_sub_u32 s74, 15f-14b, s74\n\t" "s_add_u32 s60, s60, s74\n\t" "s_addc_u32 s61, s61, 0\n\t" "s_setpc_b64 s[60:61]\n\t" "s_nop 3\n\t" "s_nop 3\n\t" \
   "s_nop 3\n\t" "s_nop 3\n\t" "s_nop 3\n\t" "s_nop 3\n\t" "s_nop 3\n\t" "s_nop 3\n\t" "s_nop 3\n\t" "s_nop 3\n\t" "s_nop 3\n\t" "s_nop 3\n\t" \
   "s_nop 3\n\t" "s_nop 3\n\t" "s_nop 3\n\t" "s_nop 3\n\t" "s_nop 3\n\t" "s_nop 3\n\t" "s_nop 3\n\t" "s_nop 3\n\t" "s_nop 3\n\t" "s_nop 3\n\t" \
   "s_nop 3\n\t" "s_nop 3\n\t" "15:\n\t" ""
#define WAIT_CLOB "s60", "s61", "s62", "s63", "s74", "s75"
#define FIN_ASM \
   "s_mov_b32 s40, 0\n\t" "s_mov_b32 s76, 0\n\t" "s_mov_b32 s77, 0\n\t" "s_sub_u32 s45, s56, 0x10000\n\t" "v_mov_b32 v13, -2\n\t" \
   "v_mov_b32 v11, -1\n\t" "30:\n\t" "s_lshr_b64 s[72:73], %[sel], s40\n\t" "s_lshl_b32 s41, s40, 19\n\t" "s_and_b32 s72, s72, 1\n\t" \
   "s_add_u32 s41, s41, %[pool]\n\t" "s_lshl_b32 s72, s72, 13\n\t" "s_add_u32 s41, s41, s72\n\t" "s_lshr_b64 s[72:73], %[s3], s40\n\t" \
   "s_and_b32 s47, s72, 1\n\t" "s_lshl_b32 s47, s47, 14\n\t" "s_mul_i32 s49, s40, 1310720\n\t" "s_add_u32 s49, s49, %[su]\n\t" \
   "s_lshr_b64 s[72:73], %[ysel], s40\n\t" "s_mul_i32 s73, s40, 43\n\t" "s_mul_i32 s50, s40, 80\n\t" "s_and_b32 s72, s72, 1\n\t" \
   "s_lshr_b32 s73, s73, 7\n\t" "s_add_u32 s50, s50, s72\n\t" "s_lshl4_add_u32 s50, s73, s50\n\t" "s_lshl_b32 s50, s50, 13\n\t" \
   "s_add_u32 s50, s50, %[yb]\n\t" "s_add_u32 s51, s50, 4096\n\t" "s_lshl_b32 s52, s40, 12\n\t" "s_add_u32 s52, s52, %[uf]\n\t" \
   "s_add_u32 s69, s45, %[tu]\n\t" "v_max3_u32 v15, v8, v9, v10\n\t" "v_max_u32 v15, v15, v11\n\t" "v_cmp_eq_u32 vcc, -1, v15\n\t" \
   "s_add_u32 s40, s40, 1\n\t" "s_cbranch_vccnz 95f\n\t" "96:\n\t" "11:\n\t" "s_memrealtime s[60:61]\n\t" "s_memtime s[62:63]\n\t" \
   "s_waitcnt lgkmcnt(0)\n\t" "s_sub_u32 s74, s62, s57\n\t" "s_mul_i32 s74, s74, s58\n\t" "s_lshr_b32 s74, s74, 12\n\t" "s_add_u32 s74, s74, s56\n\t" \
   "s_lshl_b32 s60, s60, 3\n\t" "s_sub_u32 s75, s74, s60\n\t" "s_cmp_lt_u32 s75, 32\n\t" "s_cbranch_scc1 12f\n\t" "s_cmp_lt_i32 s75, 0\n\t" \
   "s_cselect_b32 s61, 0, 31\n\t" "s_add_u32 s74, s60, s61\n\t" "s_sub_i32 s75, s61, s75\n\t" "s_add_i32 s75, s75, 1\n\t" "s_ashr_i32 s75, s75, 1\n\t" \
   "s_min_i32 s75, s75, 8\n\t" "s_max_i32 s75, s75, -8\n\t" "s_mov_b32 s56, s74\n\t" "s_mov_b32 s57, s62\n\t" "s_add_u32 s58, s58, s75\n\t" "12:\n\t" \
   "s_sub_i32 s74, s69, s74\n\t" "s_cmp_le_i32 s74, 150\n\t" "s_cbranch_scc1 13f\n\t" "s_cmp_lt_i32 s74, 260\n\t" "s_cbranch_scc1 11b\n\t" \
   "s_sleep 2\n\t" "s_cmp_lt_i32 s74, 32000\n\t" "s_cbranch_scc1 11b\n\t" "s_mov_b32 s74, 0\n\t" "13:\n\t" "s_sub_i32 s74, s74, 56\n\t" \
   "s_max_i32 s74, s74, 0\n\t" "s_mul_i32 s74, s74, 23\n\t" "s_lshr_b32 s74, s74, 5\n\t" "s_and_b32 s74, s74, -4\n\t" "s_getpc_b64 s[60:61]\n\t" \
   "14:\n\t" "s_sub_u32 s74, 15f-14b, s74\n\t" "s_add_u32 s60, s60, s74\n\t" "s_addc_u32 s61, s61, 0\n\t" "s_setpc_b64 s[60:61]\n\t" "s_nop 3\n\t" \
   "s_nop 3\n\t" "s_nop 3\n\t" "s_nop 3\n\t" "s_nop 3\n\t" "s_nop 3\n\t" "s_nop 3\n\t" "s_nop 3\n\t" "s_nop 3\n\t" "s_nop 3\n\t" "s_nop 3\n\t" \
   "s_nop 3\n\t" "s_nop 3\n\t" "s_nop 3\n\t" "s_nop 3\n\t" "s_nop 3\n\t" "s_nop 3\n\t" "s_nop 3\n\t" "s_nop 3\n\t" "s_nop 3\n\t" "s_nop 3\n\t" \
   "s_nop 3\n\t" "s_nop 3\n\t" "s_nop 3\n\t" "15:\n\t" RSTAMP(4) "buffer_load_dwordx4 a[0:3], v16, %[rs], s49 offen offset:0 sc1\n\t" "s_nop 0\n\t" \
   "buffer_load_dwordx4 a[4:7], v16, %[rs], s49 offen offset:1024 sc1\n\t" "s_nop 0\n\t" \
   "buffer_load_dwordx4 a[8:11], v16, %[rs], s49 offen offset:2048 sc1\n\t" "s_nop 0\n\t" \
   "buffer_load_dwordx4 a[12:15], v16, %[rs], s49 offen offset:3072 sc1\n\t" "s_nop 0\n\t" "s_add_u32 s49, s49, 4096\n\t" \
   "buffer_load_dwordx4 a[16:19], v16, %[rs], s49 offen offset:0 sc1\n\t" "s_nop 0\n\t" \
   "buffer_load_dwordx4 a[20:23], v16, %[rs], s49 offen offset:1024 sc1\n\t" "s_nop 0\n\t" \
   "buffer_load_dwordx4 a[24:27], v16, %[rs], s49 offen offset:2048 sc1\n\t" "s_nop 0\n\t" \
   "buffer_load_dwordx4 a[28:31], v16, %[rs], s49 offen offset:3072 sc1\n\t" "s_nop 0\n\t" "s_add_u32 s49, s49, 4096\n\t" \
   "buffer_load_dwordx4 a[32:35], v16, %[rs], s49 offen offset:0 sc1\n\t" "s_nop 0\n\t" \
   "buffer_load_dwordx4 a[36:39], v16, %[rs], s49 offen offset:1024 sc1\n\t" "s_nop 0\n\t" \
   "buffer_load_dwordx4 a[40:43], v16, %[rs], s49 offen offset:2048 sc1\n\t" "s_nop 0\n\t" \
   "buffer_load_dwordx4 a[44:47], v16, %[rs], s49 offen offset:3072 sc1\n\t" "s_nop 0\n\t" "s_add_u32 s49, s49, 4096\n\t" \
   "buffer_load_dwordx4 a[48:51], v16, %[rs], s49 offen offset:0 sc1\n\t" "s_nop 0\n\t" \
   "buffer_load_dwordx4 a[52:55], v16, %[rs], s49 offen offset:1024 sc1\n\t" "s_nop 0\n\t" \
   "buffer_load_dwordx4 a[56:59], v16, %[rs], s49 offen offset:2048 sc1\n\t" "s_nop 0\n\t" \
   "buffer_load_dwordx4 a[60:63], v16, %[rs], s49 offen offset:3072 sc1\n\t" "s_nop 0\n\t" "s_add_u32 s49, s49, 4096\n\t" \
   "buffer_load_dwordx4 a[64:67], v16, %[rs], s49 offen offset:0 sc1\n\t" "s_nop 0\n\t" \
   "buffer_load_dwordx4 a[68:71], v16, %[rs], s49 offen offset:1024 sc1\n\t" "s_nop 0\n\t" \
   "buffer_load_dwordx4 a[72:75], v16, %[rs], s49 offen offset:2048 sc1\n\t" "s_nop 0\n\t" \
   "buffer_load_dwordx4 a[76:79], v16, %[rs], s49 offen offset:3072 sc1\n\t" "s_nop 0\n\t" "s_add_u32 s49, s49, 4096\n\t" \
   "buffer_load_dwordx4 a[80:83], v16, %[rs], s49 offen offset:0 sc1\n\t" "s_nop 0\n\t" \
   "buffer_load_dwordx4 a[84:87], v16, %[rs], s49 offen offset:1024 sc1\n\t" "s_nop 0\n\t" \
   "buffer_load_dwordx4 a[88:91], v16, %[rs], s49 offen offset:2048 sc1\n\t" "s_nop 0\n\t" \
   "buffer_load_dwordx4 a[92:95], v16, %[rs], s49 offen offset:3072 sc1\n\t" "s_nop 0\n\t" "s_add_u32 s49, s49, 4096\n\t" \
   "buffer_load_dwordx4 a[96:99], v16, %[rs], s49 offen offset:0 sc1\n\t" "s_nop 0\n\t" \
   "buffer_load_dwordx4 a[100:103], v16, %[rs], s49 offen offset:1024 sc1\n\t" "s_nop 0\n\t" \
   "buffer_load_dwordx4 a[104:107], v16, %[rs], s49 offen offset:2048 sc1\n\t" "s_nop 0\n\t" \
   "buffer_load_dwordx4 a[108:111], v16, %[rs], s49 offen offset:3072 sc1\n\t" "s_nop 0\n\t" "s_add_u32 s49, s49, 4096\n\t" \
   "buffer_load_dwordx4 a[112:115], v16, %[rs], s49 offen offset:0 sc1\n\t" "s_nop 0\n\t" \
   "buffer_load_dwordx4 a[116:119], v16, %[rs], s49 offen offset:1024 sc1\n\t" "s_nop 0\n\t" \
   "buffer_load_dwordx4 a[120:123], v16, %[rs], s49 offen offset:2048 sc1\n\t" "s_nop 0\n\t" \
   "buffer_load_dwordx4 a[124:127], v16, %[rs], s49 offen offset:3072 sc1\n\t" "s_nop 0\n\t" "s_add_u32 s52, s52, 4096\n\t" \
   "buffer_load_dwordx4 v[8:11], v19, %[rs], s52 offen sc1\n\t" RSTAMP(0) "s_mov_b32 s46, 0\n\t" "s_xor_b32 s64, s47, %[lad0]\n\t" \
   "s_and_b32 s65, s64, 0x4000\n\t" "s_xor_b32 s64, s64, s65\n\t" "v_or_b32 v224, s65, v16\n\t" "v_or_b32 v225, s65, v17\n\t" \
   "v_xor_b32 v226, 0x4000, v224\n\t" "v_xor_b32 v227, 0x4000, v225\n\t" "s_add_u32 s64, s64, s41\n\t" "v_xor_b32 v14, s47, v20\n\t" \
   "v_xor_b32 v15, 0x4000, v14\n\t" "s_add_u32 s65, s64, 32768\n\t" "s_add_u32 s66, s64, 65536\n\t" "s_add_u32 s67, s64, 98304\n\t" \
   "s_add_u32 s68, s64, 131072\n\t" "s_add_u32 s69, s64, 163840\n\t" "s_add_u32 s70, s64, 196608\n\t" "s_add_u32 s71, s64, 229376\n\t" \
   "s_add_u32 s72, s64, 262144\n\t" "s_add_u32 s73, s64, 294912\n\t" "s_add_u32 s53, s41, 4096\n\t" "v_mov_b32 v28, 0\n\t" "v_mov_b32 v29, 0\n\t" \
   "v_mov_b32 v30, 0\n\t" "v_mov_b32 v31, 0\n\t" "19:\n\t" "ds_read_b128 v[24:27], v23\n\t" "s_waitcnt lgkmcnt(0)\n\t" \
   "v_readfirstlane_b32 s74, v26\n\t" "v_readfirstlane_b32 s44, v24\n\t" "v_readfirstlane_b32 s45, v25\n\t" "s_sub_u32 s48, s44, 110\n\t" \
   "s_cmp_lt_u32 s74, s40\n\t" "s_cbranch_scc1 18f\n\t" RSTAMP(7) "21:\n\t" "s_memrealtime s[60:61]\n\t" "s_memtime s[62:63]\n\t" \
   "s_waitcnt lgkmcnt(0)\n\t" "s_sub_u32 s74, s62, s57\n\t" "s_mul_i32 s74, s74, s58\n\t" "s_lshr_b32 s74, s74, 12\n\t" "s_add_u32 s74, s74, s56\n\t" \
   "s_lshl_b32 s60, s60, 3\n\t" "s_sub_u32 s75, s74, s60\n\t" "s_cmp_lt_u32 s75, 32\n\t" "s_cbranch_scc1 22f\n\t" "s_cmp_lt_i32 s75, 0\n\t" \
   "s_cselect_b32 s61, 0, 31\n\t" "s_add_u32 s74, s60, s61\n\t" "s_sub_i32 s75, s61, s75\n\t" "s_add_i32 s75, s75, 1\n\t" "s_ashr_i32 s75, s75, 1\n\t" \
   "s_min_i32 s75, s75, 8\n\t" "s_max_i32 s75, s75, -8\n\t" "s_mov_b32 s56, s74\n\t" "s_mov_b32 s57, s62\n\t" "s_add_u32 s58, s58, s75\n\t" "22:\n\t" \
   "s_sub_i32 s74, s48, s74\n\t" "s_cmp_le_i32 s74, 150\n\t" "s_cbranch_scc1 23f\n\t" "s_cmp_lt_i32 s74, 260\n\t" "s_cbranch_scc1 21b\n\t" \
   "s_sleep 2\n\t" "s_cmp_lt_i32 s74, 32000\n\t" "s_cbranch_scc1 21b\n\t" "s_mov_b32 s74, 0\n\t" "23:\n\t" "s_sub_i32 s74, s74, 56\n\t" \
   "s_max_i32 s74, s74, 0\n\t" "s_mul_i32 s74, s74, 23\n\t" "s_lshr_b32 s74, s74, 5\n\t" "s_and_b32 s74, s74, -4\n\t" "s_getpc_b64 s[60:61]\n\t" \
   "24:\n\t" "s_sub_u32 s74, 25f-24b, s74\n\t" "s_add_u32 s60, s60, s74\n\t" "s_addc_u32 s61, s61, 0\n\t" "s_setpc_b64 s[60:61]\n\t" "s_nop 3\n\t" \
   "s_nop 3\n\t" "s_nop 3\n\t" "s_nop 3\n\t" "s_nop 3\n\t" "s_nop 3\n\t" "s_nop 3\n\t" "s_nop 3\n\t" "s_nop 3\n\t" "s_nop 3\n\t" "s_nop 3\n\t" \
   "s_nop 3\n\t" "s_nop 3\n\t" "s_nop 3\n\t" "s_nop 3\n\t" "s_nop 3\n\t" "s_nop 3\n\t" "s_nop 3\n\t" "s_nop 3\n\t" "s_nop 3\n\t" "s_nop 3\n\t" \
   "s_nop 3\n\t" "s_nop 3\n\t" "s_nop 3\n\t" "25:\n\t" "buffer_load_dwordx4 v[32:35], v16, %[rs], s50 offen offset:0 sc1\n\t" \
   "buffer_load_dwordx4 v[36:39], v16, %[rs], s50 offen offset:1024 sc1\n\t" "buffer_load_dwordx4 v[40:43], v16, %[rs], s50 offen offset:2048 sc1\n\t" \
   "buffer_load_dwordx4 v[44:47], v16, %[rs], s50 offen offset:3072 sc1\n\t" "buffer_load_dwordx4 v[48:51], v16, %[rs], s51 offen offset:0 sc1\n\t" \
   "buffer_load_dwordx4 v[52:55], v16, %[rs], s51 offen offset:1024 sc1\n\t" "buffer_load_dwordx4 v[56:59], v16, %[rs], s51 offen offset:2048 sc1\n\t" \
   "buffer_load_dwordx4 v[60:63], v16, %[rs], s51 offen offset:3072 sc1\n\t" "s_nop 3\n\t" "s_nop 3\n\t" "s_nop 3\n\t" "s_nop 3\n\t" "s_nop 3\n\t" \
   "s_nop 3\n\t" "s_nop 3\n\t" "s_nop 3\n\t" "s_nop 3\n\t" "s_nop 3\n\t" "s_nop 3\n\t" "s_nop 3\n\t" "s_nop 3\n\t" "s_nop 3\n\t" "s_nop 3\n\t" \
   "s_nop 3\n\t" RSTAMP(6) "buffer_load_dwordx4 v[64:67], v224, %[rs], s64 offen offset:0 sc1\n\t" \
   "buffer_load_dwordx4 v[68:71], v224, %[rs], s64 offen offset:1024 sc1\n\t" "buffer_load_dwordx4 v[72:75], v224, %[rs], s65 offen offset:0 sc1\n\t" \
   "buffer_load_dwordx4 v[76:79], v224, %[rs], s65 offen offset:1024 sc1\n\t" "buffer_load_dwordx4 v[80:83], v224, %[rs], s66 offen offset:0 sc1\n\t" \
   "buffer_load_dwordx4 v[84:87], v224, %[rs], s66 offen offset:1024 sc1\n\t" "buffer_load_dwordx4 v[88:91], v224, %[rs], s67 offen offset:0 sc1\n\t" \
   "buffer_load_dwordx4 v[92:95], v224, %[rs], s67 offen offset:1024 sc1\n\t" "buffer_load_dwordx4 v[96:99], v224, %[rs], s68 offen offset:0 sc1\n\t" \
   "buffer_load_dwordx4 v[100:103], v224, %[rs], s68 offen offset:1024 sc1\n\t" \
   "buffer_load_dwordx4 v[104:107], v224, %[rs], s69 offen offset:0 sc1\n\t" \
   "buffer_load_dwordx4 v[108:111], v224, %[rs], s69 offen offset:1024 sc1\n\t" \
   "buffer_load_dwordx4 v[112:115], v224, %[rs], s70 offen offset:0 sc1\n\t" \
   "buffer_load_dwordx4 v[116:119], v224, %[rs], s70 offen offset:1024 sc1\n\t" \
   "buffer_load_dwordx4 v[120:123], v224, %[rs], s71 offen offset:0 sc1\n\t" \
   "buffer_load_dwordx4 v[124:127], v224, %[rs], s71 offen offset:1024 sc1\n\t" \
   "buffer_load_dwordx2 v[128:129], v225, %[rs], s72 offen offset:0 sc1\n\t" \
   "buffer_load_dwordx2 v[130:131], v225, %[rs], s72 offen offset:128 sc1\n\t" \
   "buffer_load_dwordx2 v[132:133], v225, %[rs], s72 offen offset:1024 sc1\n\t" \
   "buffer_load_dwordx2 v[134:135], v225, %[rs], s72 offen offset:1152 sc1\n\t" \
   "buffer_load_dwordx2 v[136:137], v225, %[rs], s73 offen offset:0 sc1\n\t" \
   "buffer_load_dwordx2 v[138:139], v225, %[rs], s73 offen offset:128 sc1\n\t" \
   "buffer_load_dwordx2 v[140:141], v225, %[rs], s73 offen offset:1024 sc1\n\t" \
   "buffer_load_dwordx2 v[142:143], v225, %[rs], s73 offen offset:1152 sc1\n\t" YSTAMP(24) "s_waitcnt vmcnt(23)\n\t" RSTAMP(1) \
   "v_smfmac_f32_16x16x64_bf16 v[28:31], v[64:67], a[0:7], v22\n\t" "s_waitcnt vmcnt(22)\n\t" \
   "v_smfmac_f32_16x16x64_bf16 v[28:31], v[68:71], a[8:15], v22\n\t" "s_waitcnt vmcnt(21)\n\t" \
   "v_smfmac_f32_16x16x64_bf16 v[28:31], v[72:75], a[16:23], v22\n\t" "s_waitcnt vmcnt(20)\n\t" \
   "v_smfmac_f32_16x16x64_bf16 v[28:31], v[76:79], a[24:31], v22\n\t" "s_waitcnt vmcnt(19)\n\t" \
   "v_smfmac_f32_16x16x64_bf16 v[28:31], v[80:83], a[32:39], v22\n\t" "s_waitcnt vmcnt(18)\n\t" \
   "v_smfmac_f32_16x16x64_bf16 v[28:31], v[84:87], a[40:47], v22\n\t" "s_waitcnt vmcnt(17)\n\t" \
   "v_smfmac_f32_16x16x64_bf16 v[28:31], v[88:91], a[48:55], v22\n\t" "s_waitcnt vmcnt(16)\n\t" \
   "v_smfmac_f32_16x16x64_bf16 v[28:31], v[92:95], a[56:63], v22\n\t" "s_waitcnt vmcnt(15)\n\t" \
   "v_smfmac_f32_16x16x64_bf16 v[28:31], v[96:99], a[64:71], v22\n\t" "s_waitcnt vmcnt(14)\n\t" \
   "v_smfmac_f32_16x16x64_bf16 v[28:31], v[100:103], a[72:79], v22\n\t" "s_waitcnt vmcnt(13)\n\t" \
   "v_smfmac_f32_16x16x64_bf16 v[28:31], v[104:107], a[80:87], v22\n\t" "s_waitcnt vmcnt(12)\n\t" \
   "v_smfmac_f32_16x16x64_bf16 v[28:31], v[108:111], a[88:95], v22\n\t" "s_waitcnt vmcnt(11)\n\t" \
   "v_smfmac_f32_16x16x64_bf16 v[28:31], v[112:115], a[96:103], v22\n\t" "s_waitcnt vmcnt(10)\n\t" \
   "v_smfmac_f32_16x16x64_bf16 v[28:31], v[116:119], a[104:111], v22\n\t" "s_waitcnt vmcnt(9)\n\t" \
   "v_smfmac_f32_16x16x64_bf16 v[28:31], v[120:123], a[112:119], v22\n\t" "s_waitcnt vmcnt(8)\n\t" \
   "v_smfmac_f32_16x16x64_bf16 v[28:31], v[124:127], a[120:127], v22\n\t" "s_waitcnt vmcnt(6)\n\t" \
   "v_smfmac_f32_16x16x64_bf16 v[28:31], v[128:131], v[32:39], v22\n\t" "s_waitcnt vmcnt(4)\n\t" \
   "v_smfmac_f32_16x16x64_bf16 v[28:31], v[132:135], v[40:47], v22\n\t" "s_waitcnt vmcnt(2)\n\t" \
   "v_smfmac_f32_16x16x64_bf16 v[28:31], v[136:139], v[48:55], v22\n\t" "s_waitcnt vmcnt(0)\n\t" \
   "v_smfmac_f32_16x16x64_bf16 v[28:31], v[140:143], v[56:63], v22\n\t" "s_nop 7\n\t" "v_add_f32 v28, v28, v29\n\t" "v_add_f32 v30, v30, v31\n\t" \
   "v_cvt_pk_bf16_f32 v24, v28, v30\n\t" "v_cmp_u_f32 vcc, v28, v30\n\t" "s_nop 0\n\t" \
   "v_mov_b32_dpp v25, v24 quad_perm:[1,0,3,2] row_mask:0xf bank_mask:0xf\n\t" "s_cbranch_vccnz 8f\n\t" "2:\n\t" RSTAMP(2) \
   "v_perm_b32 v26, v25, v24, v18\n\t" "s_nop 1\n\t" "v_min_u32_dpp v24, v26, v13 quad_perm:[0,1,0,1] row_mask:0xf bank_mask:0xf\n\t" \
   "v_mov_b32_dpp v25, v26 quad_perm:[2,3,2,3] row_mask:0xf bank_mask:0xf\n\t" "buffer_store_dwordx2 v[24:25], v14, %[rs], s41 offen sc1\n\t" \
   "buffer_store_dwordx2 v[24:25], v14, %[rs], s53 offen sc1\n\t" "s_memtime s[62:63]\n\t" "s_memrealtime s[60:61]\n\t" \
   "buffer_store_dwordx2 v[24:25], v15, %[rs], s41 offen sc1\n\t" "buffer_store_dwordx2 v[24:25], v15, %[rs], s53 offen sc1\n\t" RSTAMP(3) \
   "s_waitcnt lgkmcnt(0)\n\t" "s_sub_u32 s74, s62, s57\n\t" "s_mul_i32 s74, s74, s58\n\t" "s_lshr_b32 s74, s74, 12\n\t" "s_add_u32 s74, s74, s56\n\t" \
   "s_lshl_b32 s60, s60, 3\n\t" "s_sub_u32 s75, s74, s60\n\t" "s_cmp_lt_u32 s75, 32\n\t" "s_cbranch_scc1 38f\n\t" "s_cmp_lt_i32 s75, 0\n\t" \
   "s_cselect_b32 s61, 0, 31\n\t" "s_add_u32 s74, s60, s61\n\t" "s_sub_i32 s75, s61, s75\n\t" "s_add_i32 s75, s75, 1\n\t" "s_ashr_i32 s75, s75, 1\n\t" \
   "s_min_i32 s75, s75, 8\n\t" "s_max_i32 s75, s75, -8\n\t" "s_mov_b32 s56, s74\n\t" "s_mov_b32 s57, s62\n\t" "s_add_u32 s58, s58, s75\n\t" "38:\n\t" \
   "s_min_u32 s75, s46, 1\n\t" "s_and_b32 s74, s74, -4\n\t" "s_or_b32 s74, s74, s75\n\t" "v_mov_b32 v26, s74\n\t" \
   "buffer_store_dword v26, v21, %[rs], s41 offen sc1\n\t" FINTRACE "v_xor_b32 v23, 32, v23\n\t" "s_cmp_lt_u32 s40, 63\n\t" "s_cbranch_scc1 30b\n\t" \
   "s_branch 90f\n\t" "18:\n\t" "s_sleep 2\n\t" "s_branch 19b\n\t" "8:\n\t" "s_mov_b32 s74, 0\n\t" "87:\n\t" "v_max3_u32 v12, v32, v34, v36\n\t" \
   "v_max3_u32 v12, v12, v38, v40\n\t" "v_max3_u32 v12, v12, v42, v44\n\t" "v_max3_u32 v12, v12, v46, v48\n\t" "v_max3_u32 v12, v12, v50, v52\n\t" \
   "v_max3_u32 v12, v12, v54, v56\n\t" "v_max3_u32 v12, v12, v58, v60\n\t" "v_max_u32 v12, v12, v62\n\t" "v_cmp_eq_u32 vcc, -1, v12\n\t" \
   "s_cmp_gt_u32 s74, 0x10000\n\t" "s_cbranch_scc1 86f\n\t" "s_cbranch_vccz 86f\n\t" "s_cmp_eq_u32 s74, 0\n\t" "s_cselect_b32 s75, 0x100, 0\n\t" \
   "s_cbranch_scc1 88f\n\t" "s_sleep 3\n\t" "88:\n\t" "buffer_load_dwordx4 v[32:35], v16, %[rs], s50 offen offset:0 sc1\n\t" \
   "buffer_load_dwordx4 v[36:39], v16, %[rs], s50 offen offset:1024 sc1\n\t" "buffer_load_dwordx4 v[40:43], v16, %[rs], s50 offen offset:2048 sc1\n\t" \
   "buffer_load_dwordx4 v[44:47], v16, %[rs], s50 offen offset:3072 sc1\n\t" "buffer_load_dwordx4 v[48:51], v16, %[rs], s51 offen offset:0 sc1\n\t" \
   "buffer_load_dwordx4 v[52:55], v16, %[rs], s51 offen offset:1024 sc1\n\t" "buffer_load_dwordx4 v[56:59], v16, %[rs], s51 offen offset:2048 sc1\n\t" \
   "buffer_load_dwordx4 v[60:63], v16, %[rs], s51 offen offset:3072 sc1\n\t" "s_add_u32 s76, s76, s75\n\t" "s_add_u32 s74, s74, 1\n\t" \
   "s_waitcnt vmcnt(0)\n\t" "s_branch 87b\n\t" "86:\n\t" "v_mov_b32 v27, 0\n\t" "v_mov_b32 v28, 0\n\t" "v_mov_b32 v29, 0\n\t" "v_mov_b32 v30, 0\n\t" \
   "v_mov_b32 v31, 0\n\t" "v_max3_u32 v27, v27, v64, v66\n\t" "v_max3_u32 v27, v27, v68, v70\n\t" "v_max3_u32 v27, v27, v72, v74\n\t" \
   "v_max3_u32 v27, v27, v76, v78\n\t" "v_max3_u32 v27, v27, v80, v82\n\t" "v_max3_u32 v27, v27, v84, v86\n\t" "v_max3_u32 v27, v27, v88, v90\n\t" \
   "v_max3_u32 v27, v27, v92, v94\n\t" "v_max3_u32 v27, v27, v96, v98\n\t" "v_max3_u32 v27, v27, v100, v102\n\t" "v_max3_u32 v27, v27, v104, v106\n\t" \
   "v_max3_u32 v27, v27, v108, v110\n\t" "v_max3_u32 v27, v27, v112, v114\n\t" "v_max3_u32 v27, v27, v116, v118\n\t" \
   "v_max3_u32 v27, v27, v120, v122\n\t" "v_max3_u32 v27, v27, v124, v126\n\t" "v_max3_u32 v27, v27, v128, v130\n\t" \
   "v_max3_u32 v27, v27, v132, v134\n\t" "v_max3_u32 v27, v27, v136, v138\n\t" "v_max3_u32 v27, v27, v140, v142\n\t" "v_cmp_eq_u32 vcc, -1, v27\n\t" \
   "s_nop 0\n\t" "s_cbranch_vccnz 85f\n\t" "v_smfmac_f32_16x16x64_bf16 v[28:31], v[64:67], a[0:7], v22\n\t" \
   "v_smfmac_f32_16x16x64_bf16 v[28:31], v[68:71], a[8:15], v22\n\t" "v_smfmac_f32_16x16x64_bf16 v[28:31], v[72:75], a[16:23], v22\n\t" \
   "v_smfmac_f32_16x16x64_bf16 v[28:31], v[76:79], a[24:31], v22\n\t" "v_smfmac_f32_16x16x64_bf16 v[28:31], v[80:83], a[32:39], v22\n\t" \
   "v_smfmac_f32_16x16x64_bf16 v[28:31], v[84:87], a[40:47], v22\n\t" "v_smfmac_f32_16x16x64_bf16 v[28:31], v[88:91], a[48:55], v22\n\t" \
   "v_smfmac_f32_16x16x64_bf16 v[28:31], v[92:95], a[56:63], v22\n\t" "v_smfmac_f32_16x16x64_bf16 v[28:31], v[96:99], a[64:71], v22\n\t" \
   "v_smfmac_f32_16x16x64_bf16 v[28:31], v[100:103], a[72:79], v22\n\t" "v_smfmac_f32_16x16x64_bf16 v[28:31], v[104:107], a[80:87], v22\n\t" \
   "v_smfmac_f32_16x16x64_bf16 v[28:31], v[108:111], a[88:95], v22\n\t" "v_smfmac_f32_16x16x64_bf16 v[28:31], v[112:115], a[96:103], v22\n\t" \
   "v_smfmac_f32_16x16x64_bf16 v[28:31], v[116:119], a[104:111], v22\n\t" "v_smfmac_f32_16x16x64_bf16 v[28:31], v[120:123], a[112:119], v22\n\t" \
   "v_smfmac_f32_16x16x64_bf16 v[28:31], v[124:127], a[120:127], v22\n\t" "v_smfmac_f32_16x16x64_bf16 v[28:31], v[128:131], v[32:39], v22\n\t" \
   "v_smfmac_f32_16x16x64_bf16 v[28:31], v[132:135], v[40:47], v22\n\t" "v_smfmac_f32_16x16x64_bf16 v[28:31], v[136:139], v[48:55], v22\n\t" \
   "v_smfmac_f32_16x16x64_bf16 v[28:31], v[140:143], v[56:63], v22\n\t" "84:\n\t" "v_cmp_eq_u32 vcc, -1, v27\n\t" "s_cmp_gt_u32 s46, 0x10000\n\t" \
   "s_cbranch_scc1 83f\n\t" "s_cbranch_vccz 83f\n\t" "85:\n\t" "s_add_u32 s46, s46, 1\n\t" "s_cmp_gt_u32 s46, 2\n\t" "s_cbranch_scc1 81f\n\t" \
   "s_lshl_b32 s74, s46, 3\n\t" "s_lshl_b32 s74, 1, s74\n\t" "s_add_u32 s77, s77, s74\n\t" "81:\n\t" "s_min_u32 s74, s46, 7\n\t" \
   "s_lshr_b32 s64, s74, 1\n\t" "s_add_u32 s64, s64, %[xs]\n\t" "s_and_b32 s64, s64, 3\n\t" "s_lshl_b32 s64, s64, 11\n\t" "s_and_b32 s75, s74, 1\n\t" \
   "s_lshl_b32 s75, s75, 14\n\t" "s_xor_b32 s75, s75, s47\n\t" "s_xor_b32 s75, s75, %[lad0]\n\t" "s_and_b32 s75, s75, 0x4000\n\t" \
   "s_add_u32 s64, s64, s75\n\t" "s_add_u32 s64, s64, s41\n\t" "s_add_u32 s65, s64, 32768\n\t" "s_add_u32 s66, s64, 65536\n\t" \
   "s_add_u32 s67, s64, 98304\n\t" "s_add_u32 s68, s64, 131072\n\t" "s_add_u32 s69, s64, 163840\n\t" "s_add_u32 s70, s64, 196608\n\t" \
   "s_add_u32 s71, s64, 229376\n\t" "s_add_u32 s72, s64, 262144\n\t" "s_add_u32 s73, s64, 294912\n\t" "v_mov_b32 v28, 0\n\t" "v_mov_b32 v29, 0\n\t" \
   "v_mov_b32 v30, 0\n\t" "v_mov_b32 v31, 0\n\t" "v_mov_b32 v27, 0\n\t" "s_cmp_lt_u32 s46, 7\n\t" "s_cbranch_scc1 82f\n\t" "s_sleep 3\n\t" "82:\n\t" \
   RSTAMP(6) "buffer_load_dwordx4 v[64:67], v16, %[rs], s64 offen offset:0 sc1\n\t" \
   "buffer_load_dwordx4 v[68:71], v16, %[rs], s64 offen offset:1024 sc1\n\t" "buffer_load_dwordx4 v[72:75], v16, %[rs], s65 offen offset:0 sc1\n\t" \
   "buffer_load_dwordx4 v[76:79], v16, %[rs], s65 offen offset:1024 sc1\n\t" "buffer_load_dwordx4 v[80:83], v16, %[rs], s66 offen offset:0 sc1\n\t" \
   "buffer_load_dwordx4 v[84:87], v16, %[rs], s66 offen offset:1024 sc1\n\t" "buffer_load_dwordx4 v[88:91], v16, %[rs], s67 offen offset:0 sc1\n\t" \
   "buffer_load_dwordx4 v[92:95], v16, %[rs], s67 offen offset:1024 sc1\n\t" "buffer_load_dwordx4 v[96:99], v16, %[rs], s68 offen offset:0 sc1\n\t" \
   "buffer_load_dwordx4 v[100:103], v16, %[rs], s68 offen offset:1024 sc1\n\t" "buffer_load_dwordx4 v[104:107], v16, %[rs], s69 offen offset:0 sc1\n\t" \
   "buffer_load_dwordx4 v[108:111], v16, %[rs], s69 offen offset:1024 sc1\n\t" "buffer_load_dwordx4 v[112:115], v16, %[rs], s70 offen offset:0 sc1\n\t" \
   "buffer_load_dwordx4 v[116:119], v16, %[rs], s70 offen offset:1024 sc1\n\t" "buffer_load_dwordx4 v[120:123], v16, %[rs], s71 offen offset:0 sc1\n\t" \
   "buffer_load_dwordx4 v[124:127], v16, %[rs], s71 offen offset:1024 sc1\n\t" "buffer_load_dwordx2 v[128:129], v17, %[rs], s72 offen offset:0 sc1\n\t" \
   "buffer_load_dwordx2 v[130:131], v17, %[rs], s72 offen offset:128 sc1\n\t" \
   "buffer_load_dwordx2 v[132:133], v17, %[rs], s72 offen offset:1024 sc1\n\t" \
   "buffer_load_dwordx2 v[134:135], v17, %[rs], s72 offen offset:1152 sc1\n\t" "buffer_load_dwordx2 v[136:137], v17, %[rs], s73 offen offset:0 sc1\n\t" \
   "buffer_load_dwordx2 v[138:139], v17, %[rs], s73 offen offset:128 sc1\n\t" \
   "buffer_load_dwordx2 v[140:141], v17, %[rs], s73 offen offset:1024 sc1\n\t" \
   "buffer_load_dwordx2 v[142:143], v17, %[rs], s73 offen offset:1152 sc1\n\t" "s_waitcnt vmcnt(23)\n\t" \
   "v_smfmac_f32_16x16x64_bf16 v[28:31], v[64:67], a[0:7], v22\n\t" "v_max3_u32 v27, v27, v64, v66\n\t" "s_waitcnt vmcnt(22)\n\t" \
   "v_smfmac_f32_16x16x64_bf16 v[28:31], v[68:71], a[8:15], v22\n\t" "v_max3_u32 v27, v27, v68, v70\n\t" "s_waitcnt vmcnt(21)\n\t" \
   "v_smfmac_f32_16x16x64_bf16 v[28:31], v[72:75], a[16:23], v22\n\t" "v_max3_u32 v27, v27, v72, v74\n\t" "s_waitcnt vmcnt(20)\n\t" \
   "v_smfmac_f32_16x16x64_bf16 v[28:31], v[76:79], a[24:31], v22\n\t" "v_max3_u32 v27, v27, v76, v78\n\t" "s_waitcnt vmcnt(19)\n\t" \
   "v_smfmac_f32_16x16x64_bf16 v[28:31], v[80:83], a[32:39], v22\n\t" "v_max3_u32 v27, v27, v80, v82\n\t" "s_waitcnt vmcnt(18)\n\t" \
   "v_smfmac_f32_16x16x64_bf16 v[28:31], v[84:87], a[40:47], v22\n\t" "v_max3_u32 v27, v27, v84, v86\n\t" "s_waitcnt vmcnt(17)\n\t" \
   "v_smfmac_f32_16x16x64_bf16 v[28:31], v[88:91], a[48:55], v22\n\t" "v_max3_u32 v27, v27, v88, v90\n\t" "s_waitcnt vmcnt(16)\n\t" \
   "v_smfmac_f32_16x16x64_bf16 v[28:31], v[92:95], a[56:63], v22\n\t" "v_max3_u32 v27, v27, v92, v94\n\t" "s_waitcnt vmcnt(15)\n\t" \
   "v_smfmac_f32_16x16x64_bf16 v[28:31], v[96:99], a[64:71], v22\n\t" "v_max3_u32 v27, v27, v96, v98\n\t" "s_waitcnt vmcnt(14)\n\t" \
   "v_smfmac_f32_16x16x64_bf16 v[28:31], v[100:103], a[72:79], v22\n\t" "v_max3_u32 v27, v27, v100, v102\n\t" "s_waitcnt vmcnt(13)\n\t" \
   "v_smfmac_f32_16x16x64_bf16 v[28:31], v[104:107], a[80:87], v22\n\t" "v_max3_u32 v27, v27, v104, v106\n\t" "s_waitcnt vmcnt(12)\n\t" \
   "v_smfmac_f32_16x16x64_bf16 v[28:31], v[108:111], a[88:95], v22\n\t" "v_max3_u32 v27, v27, v108, v110\n\t" "s_waitcnt vmcnt(11)\n\t" \
   "v_smfmac_f32_16x16x64_bf16 v[28:31], v[112:115], a[96:103], v22\n\t" "v_max3_u32 v27, v27, v112, v114\n\t" "s_waitcnt vmcnt(10)\n\t" \
   "v_smfmac_f32_16x16x64_bf16 v[28:31], v[116:119], a[104:111], v22\n\t" "v_max3_u32 v27, v27, v116, v118\n\t" "s_waitcnt vmcnt(9)\n\t" \
   "v_smfmac_f32_16x16x64_bf16 v[28:31], v[120:123], a[112:119], v22\n\t" "v_max3_u32 v27, v27, v120, v122\n\t" "s_waitcnt vmcnt(8)\n\t" \
   "v_smfmac_f32_16x16x64_bf16 v[28:31], v[124:127], a[120:127], v22\n\t" "v_max3_u32 v27, v27, v124, v126\n\t" "s_waitcnt vmcnt(6)\n\t" \
   "v_smfmac_f32_16x16x64_bf16 v[28:31], v[128:131], v[32:39], v22\n\t" "v_max3_u32 v27, v27, v128, v130\n\t" "s_waitcnt vmcnt(4)\n\t" \
   "v_smfmac_f32_16x16x64_bf16 v[28:31], v[132:135], v[40:47], v22\n\t" "v_max3_u32 v27, v27, v132, v134\n\t" "s_waitcnt vmcnt(2)\n\t" \
   "v_smfmac_f32_16x16x64_bf16 v[28:31], v[136:139], v[48:55], v22\n\t" "v_max3_u32 v27, v27, v136, v138\n\t" "s_waitcnt vmcnt(0)\n\t" \
   "v_smfmac_f32_16x16x64_bf16 v[28:31], v[140:143], v[56:63], v22\n\t" "v_max3_u32 v27, v27, v140, v142\n\t" "s_branch 84b\n\t" "83:\n\t" \
   "s_or_b32 s46, s46, 0x10000000\n\t" "s_nop 4\n\t" "v_add_f32 v28, v28, v29\n\t" "v_add_f32 v30, v30, v31\n\t" "v_cvt_pk_bf16_f32 v24, v28, v30\n\t" \
   "s_nop 1\n\t" "v_mov_b32_dpp v25, v24 quad_perm:[1,0,3,2] row_mask:0xf bank_mask:0xf\n\t" "s_branch 2b\n\t" "95:\n\t" "s_mov_b32 s74, 0\n\t" \
   "97:\n\t" "buffer_load_dwordx4 v[8:11], v19, %[rs], s52 offen sc1\n\t" "s_add_u32 s74, s74, 1\n\t" "s_waitcnt vmcnt(0)\n\t" \
   "v_max3_u32 v15, v8, v9, v10\n\t" "v_max_u32 v15, v15, v11\n\t" "v_cmp_eq_u32 vcc, -1, v15\n\t" "s_cmp_gt_u32 s74, 0x10000\n\t" \
   "s_cbranch_scc1 96b\n\t" "s_cbranch_vccz 96b\n\t" "s_sleep 2\n\t" "s_branch 97b\n\t" "90:\n\t" "s_waitcnt vmcnt(0) lgkmcnt(0)"
#define FIN_CLOB "v8", "v9", "v10", "v11", "v12", "v13", "v14", "v15", "v24", "v25", "v26", "v27", "v28", "v29", "v30", "v31", "v32", "v33", "v34", "v35", "v36", "v37", "v38", "v39", "v40", "v41", "v42", "v43", "v44", "v45", "v46", "v47", "v48", "v49", "v50", "v51", "v52", "v53", "v54", "v55", "v56", "v57", "v58", "v59", "v60", "v61", "v62", "v63", "v64", "v65", "v66", "v67", "v68", "v69", "v70", "v71", "v72", "v73", "v74", "v75", "v76", "v77", "v78", "v79", "v80", "v81", "v82", "v83", "v84", "v85", "v86", "v87", "v88", "v89", "v90", "v91", "v92", "v93", "v94", "v95", "v96", "v97", "v98", "v99", "v100", "v101", "v102", "v103", "v104", "v105", "v106", "v107", "v108", "v109", "v110", "v111", "v112", "v113", "v114", "v115", "v116", "v117", "v118", "v119", "v120", "v121", "v122", "v123", "v124", "v125", "v126", "v127", "v128", "v129", "v130", "v131", "v132", "v133", "v134", "v135", "v136", "v137", "v138", "v139", "v140", "v141", "v142", "v143", "v144", "v145", "v146", "v147", "v148", "v149", "v150", "v151", "v152", "v153", "v154", "v155", "v156", "v157", "v158", "v159", "v160", "v161", "v162", "v163", "v164", "v165", "v166", "v167", "v168", "v169", "v170", "v171", "v172", "v173", "v174", "v175", "v176", "v177", "v178", "v179", "v180", "v181", "v182", "v183", "v184", "v185", "v186", "v187", "v188", "v189", "v190", "v191", "v192", "v193", "v194", "v195", "v196", "v197", "v198", "v199", "v200", "v201", "v202", "v203", "v204", "v205", "v206", "v207", "v208", "v209", "v210", "v211", "v212", "v213", "v214", "v215", "v216", "v217", "v218", "v219", "v220", "v221", "v222", "v223", "v224", "v225", "v226", "v227", "s40", "s41", "s42", "s43", "s44", "s45", "s46", "s47", "s48", "s49", "s50", "s51", "s52", "s53", "s60", "s61", "s62", "s63", "s64", "s65", "s66", "s67", "s68", "s69", "s70", "s71", "s72", "s73", "s74", "s75", "a0", "a1", "a2", "a3", "a4", "a5", "a6", "a7", "a8", "a9", "a10", "a11", "a12", "a13", "a14", "a15", "a16", "a17", "a18", "a19", "a20", "a21", "a22", "a23", "a24", "a25", "a26", "a27", "a28", "a29", "a30", "a31", "a32", "a33", "a34", "a35", "a36", "a37", "a38", "a39", "a40", "a41", "a42", "a43", "a44", "a45", "a46", "a47", "a48", "a49", "a50", "a51", "a52", "a53", "a54", "a55", "a56", "a57", "a58", "a59", "a60", "a61", "a62", "a63", "a64", "a65", "a66", "a67", "a68", "a69", "a70", "a71", "a72", "a73", "a74", "a75", "a76", "a77", "a78", "a79", "a80", "a81", "a82", "a83", "a84", "a85", "a86", "a87", "a88", "a89", "a90", "a91", "a92", "a93", "a94", "a95", "a96", "a97", "a98", "a99", "a100", "a101", "a102", "a103", "a104", "a105", "a106", "a107", "a108", "a109", "a110", "a111", "a112", "a113", "a114", "a115", "a116", "a117", "a118", "a119", "a120", "a121", "a122", "a123", "a124", "a125", "a126", "a127"
__device__ __forceinline__ void put4(v32u& d, int i, v4u v) { d[4 * i] = v.x; d[4 * i + 1] = v.y; d[4 * i + 2] = v.z; d[4 * i + 3] = v.w; }
#define LDP_ASM \
   "buffer_load_dword v26, v113, %[rw], 0 offen sc1\n\t" "s_mov_b32 s41, 0\n\t" "buffer_load_dwordx4 a[0:3], v16, %[ra], s41 offen\n\t" \
   "s_mov_b32 s41, 131072\n\t" "buffer_load_dwordx4 a[64:67], v16, %[ra], s41 offen\n\t" "s_mov_b32 s41, 524288\n\t" \
   "buffer_load_dwordx4 a[8:11], v16, %[ra], s41 offen\n\t" "s_mov_b32 s41, 655360\n\t" "buffer_load_dwordx4 a[72:75], v16, %[ra], s41 offen\n\t" \
   "s_mov_b32 s41, 1048576\n\t" "buffer_load_dwordx4 a[16:19], v16, %[ra], s41 offen\n\t" "s_mov_b32 s41, 1179648\n\t" \
   "buffer_load_dwordx4 a[80:83], v16, %[ra], s41 offen\n\t" "s_mov_b32 s41, 1572864\n\t" "buffer_load_dwordx4 a[24:27], v16, %[ra], s41 offen\n\t" \
   "s_mov_b32 s41, 1703936\n\t" "buffer_load_dwordx4 a[88:91], v16, %[ra], s41 offen\n\t" "s_mov_b32 s41, 2097152\n\t" \
   "buffer_load_dwordx4 a[32:35], v16, %[ra], s41 offen\n\t" "s_mov_b32 s41, 2228224\n\t" "buffer_load_dwordx4 a[96:99], v16, %[ra], s41 offen\n\t" \
   "s_mov_b32 s41, 2621440\n\t" "buffer_load_dwordx4 a[40:43], v16, %[ra], s41 offen\n\t" "s_mov_b32 s41, 2752512\n\t" \
   "buffer_load_dwordx4 a[104:107], v16, %[ra], s41 offen\n\t" "s_mov_b32 s41, 3145728\n\t" "buffer_load_dwordx4 a[48:51], v16, %[ra], s41 offen\n\t" \
   "s_mov_b32 s41, 3276800\n\t" "buffer_load_dwordx4 a[112:115], v16, %[ra], s41 offen\n\t" "s_mov_b32 s41, 3670016\n\t" \
   "buffer_load_dwordx4 a[56:59], v16, %[ra], s41 offen\n\t" "s_mov_b32 s41, 3801088\n\t" "buffer_load_dwordx4 a[120:123], v16, %[ra], s41 offen\n\t" \
   "s_waitcnt vmcnt(16)\n\t" "v_add_u32 v26, 1, v26\n\t" "buffer_store_dword v26, v114, %[rw], 0 offen sc1\n\t" "ds_write_b32 v115, v26\n\t" LDSTAMP(0) \
   "s_waitcnt vmcnt(15)\n\t" LDSTAMP(1) "ds_write_b128 v20, a[0:3] offset:0\n\t" "s_mov_b32 s41, 4194304\n\t" \
   "buffer_load_dwordx4 v[192:195], v16, %[ra], s41 offen\n\t" "s_waitcnt vmcnt(15)\n\t" "ds_write_b128 v20, a[64:67] offset:4096\n\t" \
   "s_mov_b32 s41, 4325376\n\t" "buffer_load_dwordx4 v[208:211], v16, %[ra], s41 offen\n\t" "s_waitcnt vmcnt(15)\n\t" \
   "ds_write_b128 v20, a[8:11] offset:8192\n\t" "s_mov_b32 s41, 4718592\n\t" "buffer_load_dwordx4 v[200:203], v16, %[ra], s41 offen\n\t" \
   "s_waitcnt vmcnt(15)\n\t" "ds_write_b128 v20, a[72:75] offset:12288\n\t" "s_mov_b32 s41, 4849664\n\t" \
   "buffer_load_dwordx4 v[216:219], v16, %[ra], s41 offen\n\t" "s_waitcnt vmcnt(15)\n\t" "ds_write_b128 v20, a[16:19] offset:16384\n\t" \
   "s_mov_b32 s41, 0\n\t" "buffer_load_dwordx4 a[128:131], v16, %[rb], s41 offen\n\t" "s_waitcnt vmcnt(15)\n\t" \
   "ds_write_b128 v20, a[80:83] offset:20480\n\t" "s_mov_b32 s41, 131072\n\t" "buffer_load_dwordx4 a[192:195], v16, %[rb], s41 offen\n\t" \
   "s_waitcnt vmcnt(15)\n\t" "ds_write_b128 v20, a[24:27] offset:24576\n\t" "s_mov_b32 s41, 524288\n\t" \
   "buffer_load_dwordx4 a[136:139], v16, %[rb], s41 offen\n\t" "s_waitcnt vmcnt(15)\n\t" "ds_write_b128 v20, a[88:91] offset:28672\n\t" \
   "s_mov_b32 s41, 655360\n\t" "buffer_load_dwordx4 a[200:203], v16, %[rb], s41 offen\n\t" "s_waitcnt vmcnt(15)\n\t" \
   "ds_write_b128 v20, a[32:35] offset:32768\n\t" "s_mov_b32 s41, 1048576\n\t" "buffer_load_dwordx4 a[144:147], v16, %[rb], s41 offen\n\t" \
   "s_waitcnt vmcnt(15)\n\t" "ds_write_b128 v20, a[96:99] offset:36864\n\t" "s_mov_b32 s41, 1179648\n\t" \
   "buffer_load_dwordx4 a[208:211], v16, %[rb], s41 offen\n\t" "s_waitcnt vmcnt(15)\n\t" "ds_write_b128 v20, a[40:43] offset:40960\n\t" \
   "s_mov_b32 s41, 1572864\n\t" "buffer_load_dwordx4 a[152:155], v16, %[rb], s41 offen\n\t" "s_waitcnt vmcnt(15)\n\t" \
   "ds_write_b128 v20, a[104:107] offset:45056\n\t" "s_mov_b32 s41, 1703936\n\t" "buffer_load_dwordx4 a[216:219], v16, %[rb], s41 offen\n\t" \
   "s_waitcnt vmcnt(15)\n\t" "ds_write_b128 v20, a[48:51] offset:49152\n\t" "s_mov_b32 s41, 2097152\n\t" \
   "buffer_load_dwordx4 a[160:163], v16, %[rb], s41 offen\n\t" "s_waitcnt vmcnt(15)\n\t" "ds_write_b128 v20, a[112:115] offset:53248\n\t" \
   "s_mov_b32 s41, 2228224\n\t" "buffer_load_dwordx4 a[224:227], v16, %[rb], s41 offen\n\t" "s_waitcnt vmcnt(15)\n\t" \
   "ds_write_b128 v20, a[56:59] offset:57344\n\t" "s_mov_b32 s41, 2621440\n\t" "buffer_load_dwordx4 a[168:171], v16, %[rb], s41 offen\n\t" \
   "s_waitcnt vmcnt(15)\n\t" "ds_write_b128 v20, a[120:123] offset:61440\n\t" "s_mov_b32 s41, 2752512\n\t" \
   "buffer_load_dwordx4 a[232:235], v16, %[rb], s41 offen\n\t" "s_waitcnt vmcnt(15)\n\t" "ds_write_b128 v23, v[192:195] offset:0\n\t" \
   "s_mov_b32 s41, 3145728\n\t" "buffer_load_dwordx4 a[176:179], v16, %[rb], s41 offen\n\t" "s_waitcnt vmcnt(15)\n\t" \
   "ds_write_b128 v23, v[208:211] offset:4096\n\t" "s_mov_b32 s41, 3276800\n\t" "buffer_load_dwordx4 a[240:243], v16, %[rb], s41 offen\n\t" \
   "s_waitcnt vmcnt(15)\n\t" "ds_write_b128 v23, v[200:203] offset:8192\n\t" "s_mov_b32 s41, 3670016\n\t" \
   "buffer_load_dwordx4 a[184:187], v16, %[rb], s41 offen\n\t" "s_waitcnt vmcnt(15)\n\t" "ds_write_b128 v23, v[216:219] offset:12288\n\t" \
   "s_mov_b32 s41, 3801088\n\t" "buffer_load_dwordx4 a[248:251], v16, %[rb], s41 offen\n\t" "s_waitcnt lgkmcnt(0)\n\t" "s_barrier\n\t" LDSTAMP(2) \
   "s_lshl_b32 s40, %[p0], 13\n\t" "v_add_u32 v24, s40, v18\n\t" "v_add_u32 v25, s40, v19\n\t" "s_add_u32 s40, s40, 8192\n\t" \
   "s_cmp_eq_u32 s40, 81920\n\t" "s_cselect_b32 s40, 0, s40\n\t" "ds_read_b64_tr_b16 a[0:1], v24 offset:0\n\t" \
   "ds_read_b64_tr_b16 a[2:3], v24 offset:4096\n\t" "ds_read_b64_tr_b16 a[64:65], v25 offset:0\n\t" "ds_read_b64_tr_b16 a[66:67], v25 offset:4096\n\t" \
   "v_add_u32 v24, s40, v18\n\t" "v_add_u32 v25, s40, v19\n\t" "s_add_u32 s40, s40, 8192\n\t" "s_cmp_eq_u32 s40, 81920\n\t" \
   "s_cselect_b32 s40, 0, s40\n\t" "ds_read_b64_tr_b16 a[8:9], v24 offset:0\n\t" "ds_read_b64_tr_b16 a[10:11], v24 offset:4096\n\t" \
   "ds_read_b64_tr_b16 a[72:73], v25 offset:0\n\t" "ds_read_b64_tr_b16 a[74:75], v25 offset:4096\n\t" "v_add_u32 v24, s40, v18\n\t" \
   "v_add_u32 v25, s40, v19\n\t" "s_add_u32 s40, s40, 8192\n\t" "s_cmp_eq_u32 s40, 81920\n\t" "s_cselect_b32 s40, 0, s40\n\t" \
   "ds_read_b64_tr_b16 a[16:17], v24 offset:0\n\t" "ds_read_b64_tr_b16 a[18:19], v24 offset:4096\n\t" "ds_read_b64_tr_b16 a[80:81], v25 offset:0\n\t" \
   "ds_read_b64_tr_b16 a[82:83], v25 offset:4096\n\t" "v_add_u32 v24, s40, v18\n\t" "v_add_u32 v25, s40, v19\n\t" "s_add_u32 s40, s40, 8192\n\t" \
   "s_cmp_eq_u32 s40, 81920\n\t" "s_cselect_b32 s40, 0, s40\n\t" "ds_read_b64_tr_b16 a[24:25], v24 offset:0\n\t" \
   "ds_read_b64_tr_b16 a[26:27], v24 offset:4096\n\t" "ds_read_b64_tr_b16 a[88:89], v25 offset:0\n\t" \
   "ds_read_b64_tr_b16 a[90:91], v25 offset:4096\n\t" "v_add_u32 v24, s40, v18\n\t" "v_add_u32 v25, s40, v19\n\t" "s_add_u32 s40, s40, 8192\n\t" \
   "s_cmp_eq_u32 s40, 81920\n\t" "s_cselect_b32 s40, 0, s40\n\t" "ds_read_b64_tr_b16 a[32:33], v24 offset:0\n\t" \
   "ds_read_b64_tr_b16 a[34:35], v24 offset:4096\n\t" "ds_read_b64_tr_b16 a[96:97], v25 offset:0\n\t" \
   "ds_read_b64_tr_b16 a[98:99], v25 offset:4096\n\t" "v_add_u32 v24, s40, v18\n\t" "v_add_u32 v25, s40, v19\n\t" "s_add_u32 s40, s40, 8192\n\t" \
   "s_cmp_eq_u32 s40, 81920\n\t" "s_cselect_b32 s40, 0, s40\n\t" "ds_read_b64_tr_b16 a[40:41], v24 offset:0\n\t" \
   "ds_read_b64_tr_b16 a[42:43], v24 offset:4096\n\t" "ds_read_b64_tr_b16 a[104:105], v25 offset:0\n\t" \
   "ds_read_b64_tr_b16 a[106:107], v25 offset:4096\n\t" "v_add_u32 v24, s40, v18\n\t" "v_add_u32 v25, s40, v19\n\t" "s_add_u32 s40, s40, 8192\n\t" \
   "s_cmp_eq_u32 s40, 81920\n\t" "s_cselect_b32 s40, 0, s40\n\t" "ds_read_b64_tr_b16 a[48:49], v24 offset:0\n\t" \
   "ds_read_b64_tr_b16 a[50:51], v24 offset:4096\n\t" "ds_read_b64_tr_b16 a[112:113], v25 offset:0\n\t" \
   "ds_read_b64_tr_b16 a[114:115], v25 offset:4096\n\t" "v_add_u32 v24, s40, v18\n\t" "v_add_u32 v25, s40, v19\n\t" "s_add_u32 s40, s40, 8192\n\t" \
   "s_cmp_eq_u32 s40, 81920\n\t" "s_cselect_b32 s40, 0, s40\n\t" "ds_read_b64_tr_b16 a[56:57], v24 offset:0\n\t" \
   "ds_read_b64_tr_b16 a[58:59], v24 offset:4096\n\t" "ds_read_b64_tr_b16 a[120:121], v25 offset:0\n\t" \
   "ds_read_b64_tr_b16 a[122:123], v25 offset:4096\n\t" "v_add_u32 v24, s40, v18\n\t" "v_add_u32 v25, s40, v19\n\t" "s_add_u32 s40, s40, 8192\n\t" \
   "s_cmp_eq_u32 s40, 81920\n\t" "s_cselect_b32 s40, 0, s40\n\t" "ds_read_b64_tr_b16 v[192:193], v24 offset:0\n\t" \
   "ds_read_b64_tr_b16 v[194:195], v24 offset:4096\n\t" "ds_read_b64_tr_b16 v[208:209], v25 offset:0\n\t" \
   "ds_read_b64_tr_b16 v[210:211], v25 offset:4096\n\t" "v_add_u32 v24, s40, v18\n\t" "v_add_u32 v25, s40, v19\n\t" "s_add_u32 s40, s40, 8192\n\t" \
   "s_cmp_eq_u32 s40, 81920\n\t" "s_cselect_b32 s40, 0, s40\n\t" "ds_read_b64_tr_b16 v[200:201], v24 offset:0\n\t" \
   "ds_read_b64_tr_b16 v[202:203], v24 offset:4096\n\t" "ds_read_b64_tr_b16 v[216:217], v25 offset:0\n\t" \
   "ds_read_b64_tr_b16 v[218:219], v25 offset:4096\n\t" "s_waitcnt vmcnt(15)\n\t" "s_waitcnt lgkmcnt(0)\n\t" "s_barrier\n\t" \
   "ds_write_b128 v20, a[128:131] offset:0\n\t" "s_mov_b32 s41, 4194304\n\t" "buffer_load_dwordx4 v[224:227], v16, %[rb], s41 offen\n\t" \
   "s_waitcnt vmcnt(15)\n\t" "ds_write_b128 v20, a[192:195] offset:4096\n\t" "s_mov_b32 s41, 4325376\n\t" \
   "buffer_load_dwordx4 v[240:243], v16, %[rb], s41 offen\n\t" "s_waitcnt vmcnt(15)\n\t" "ds_write_b128 v20, a[136:139] offset:8192\n\t" \
   "s_mov_b32 s41, 4718592\n\t" "buffer_load_dwordx4 v[232:235], v16, %[rb], s41 offen\n\t" "s_waitcnt vmcnt(15)\n\t" \
   "ds_write_b128 v20, a[200:203] offset:12288\n\t" "s_mov_b32 s41, 4849664\n\t" "buffer_load_dwordx4 v[248:251], v16, %[rb], s41 offen\n\t" \
   "s_waitcnt vmcnt(15)\n\t" "ds_write_b128 v20, a[144:147] offset:16384\n\t" "s_mov_b32 s41, 262144\n\t" \
   "buffer_load_dwordx4 a[4:7], v16, %[ra], s41 offen\n\t" "s_waitcnt vmcnt(15)\n\t" "ds_write_b128 v20, a[208:211] offset:20480\n\t" \
   "s_mov_b32 s41, 393216\n\t" "buffer_load_dwordx4 a[68:71], v16, %[ra], s41 offen\n\t" "s_waitcnt vmcnt(15)\n\t" \
   "ds_write_b128 v20, a[152:155] offset:24576\n\t" "s_mov_b32 s41, 786432\n\t" "buffer_load_dwordx4 a[12:15], v16, %[ra], s41 offen\n\t" \
   "s_waitcnt vmcnt(15)\n\t" "ds_write_b128 v20, a[216:219] offset:28672\n\t" "s_mov_b32 s41, 917504\n\t" \
   "buffer_load_dwordx4 a[76:79], v16, %[ra], s41 offen\n\t" "s_waitcnt vmcnt(15)\n\t" "ds_write_b128 v20, a[160:163] offset:32768\n\t" \
   "s_mov_b32 s41, 1310720\n\t" "buffer_load_dwordx4 a[20:23], v16, %[ra], s41 offen\n\t" "s_waitcnt vmcnt(15)\n\t" \
   "ds_write_b128 v20, a[224:227] offset:36864\n\t" "s_mov_b32 s41, 1441792\n\t" "buffer_load_dwordx4 a[84:87], v16, %[ra], s41 offen\n\t" \
   "s_waitcnt vmcnt(15)\n\t" "ds_write_b128 v20, a[168:171] offset:40960\n\t" "s_mov_b32 s41, 1835008\n\t" \
   "buffer_load_dwordx4 a[28:31], v16, %[ra], s41 offen\n\t" "s_waitcnt vmcnt(15)\n\t" "ds_write_b128 v20, a[232:235] offset:45056\n\t" \
   "s_mov_b32 s41, 1966080\n\t" "buffer_load_dwordx4 a[92:95], v16, %[ra], s41 offen\n\t" "s_waitcnt vmcnt(15)\n\t" \
   "ds_write_b128 v20, a[176:179] offset:49152\n\t" "s_mov_b32 s41, 2359296\n\t" "buffer_load_dwordx4 a[36:39], v16, %[ra], s41 offen\n\t" \
   "s_waitcnt vmcnt(15)\n\t" "ds_write_b128 v20, a[240:243] offset:53248\n\t" "s_mov_b32 s41, 2490368\n\t" \
   "buffer_load_dwordx4 a[100:103], v16, %[ra], s41 offen\n\t" "s_waitcnt vmcnt(15)\n\t" "ds_write_b128 v20, a[184:187] offset:57344\n\t" \
   "s_mov_b32 s41, 2883584\n\t" "buffer_load_dwordx4 a[44:47], v16, %[ra], s41 offen\n\t" "s_waitcnt vmcnt(15)\n\t" \
   "ds_write_b128 v20, a[248:251] offset:61440\n\t" "s_mov_b32 s41, 3014656\n\t" "buffer_load_dwordx4 a[108:111], v16, %[ra], s41 offen\n\t" \
   "s_waitcnt vmcnt(15)\n\t" "ds_write_b128 v23, v[224:227] offset:0\n\t" "s_mov_b32 s41, 3407872\n\t" \
   "buffer_load_dwordx4 a[52:55], v16, %[ra], s41 offen\n\t" "s_waitcnt vmcnt(15)\n\t" "ds_write_b128 v23, v[240:243] offset:4096\n\t" \
   "s_mov_b32 s41, 3538944\n\t" "buffer_load_dwordx4 a[116:119], v16, %[ra], s41 offen\n\t" "s_waitcnt vmcnt(15)\n\t" \
   "ds_write_b128 v23, v[232:235] offset:8192\n\t" "s_mov_b32 s41, 3932160\n\t" "buffer_load_dwordx4 a[60:63], v16, %[ra], s41 offen\n\t" \
   "s_waitcnt vmcnt(15)\n\t" "ds_write_b128 v23, v[248:251] offset:12288\n\t" "s_mov_b32 s41, 4063232\n\t" \
   "buffer_load_dwordx4 a[124:127], v16, %[ra], s41 offen\n\t" "s_waitcnt lgkmcnt(0)\n\t" "s_barrier\n\t" LDSTAMP(3) "s_lshl_b32 s40, %[p0], 13\n\t" \
   "v_add_u32 v24, s40, v18\n\t" "v_add_u32 v25, s40, v19\n\t" "s_add_u32 s40, s40, 8192\n\t" "s_cmp_eq_u32 s40, 81920\n\t" \
   "s_cselect_b32 s40, 0, s40\n\t" "ds_read_b64_tr_b16 a[128:129], v24 offset:0\n\t" "ds_read_b64_tr_b16 a[130:131], v24 offset:4096\n\t" \
   "ds_read_b64_tr_b16 a[192:193], v25 offset:0\n\t" "ds_read_b64_tr_b16 a[194:195], v25 offset:4096\n\t" "v_add_u32 v24, s40, v18\n\t" \
   "v_add_u32 v25, s40, v19\n\t" "s_add_u32 s40, s40, 8192\n\t" "s_cmp_eq_u32 s40, 81920\n\t" "s_cselect_b32 s40, 0, s40\n\t" \
   "ds_read_b64_tr_b16 a[136:137], v24 offset:0\n\t" "ds_read_b64_tr_b16 a[138:139], v24 offset:4096\n\t" \
   "ds_read_b64_tr_b16 a[200:201], v25 offset:0\n\t" "ds_read_b64_tr_b16 a[202:203], v25 offset:4096\n\t" "v_add_u32 v24, s40, v18\n\t" \
   "v_add_u32 v25, s40, v19\n\t" "s_add_u32 s40, s40, 8192\n\t" "s_cmp_eq_u32 s40, 81920\n\t" "s_cselect_b32 s40, 0, s40\n\t" \
   "ds_read_b64_tr_b16 a[144:145], v24 offset:0\n\t" "ds_read_b64_tr_b16 a[146:147], v24 offset:4096\n\t" \
   "ds_read_b64_tr_b16 a[208:209], v25 offset:0\n\t" "ds_read_b64_tr_b16 a[210:211], v25 offset:4096\n\t" "v_add_u32 v24, s40, v18\n\t" \
   "v_add_u32 v25, s40, v19\n\t" "s_add_u32 s40, s40, 8192\n\t" "s_cmp_eq_u32 s40, 81920\n\t" "s_cselect_b32 s40, 0, s40\n\t" \
   "ds_read_b64_tr_b16 a[152:153], v24 offset:0\n\t" "ds_read_b64_tr_b16 a[154:155], v24 offset:4096\n\t" \
   "ds_read_b64_tr_b16 a[216:217], v25 offset:0\n\t" "ds_read_b64_tr_b16 a[218:219], v25 offset:4096\n\t" "v_add_u32 v24, s40, v18\n\t" \
   "v_add_u32 v25, s40, v19\n\t" "s_add_u32 s40, s40, 8192\n\t" "s_cmp_eq_u32 s40, 81920\n\t" "s_cselect_b32 s40, 0, s40\n\t" \
   "ds_read_b64_tr_b16 a[160:161], v24 offset:0\n\t" "ds_read_b64_tr_b16 a[162:163], v24 offset:4096\n\t" \
   "ds_read_b64_tr_b16 a[224:225], v25 offset:0\n\t" "ds_read_b64_tr_b16 a[226:227], v25 offset:4096\n\t" "v_add_u32 v24, s40, v18\n\t" \
   "v_add_u32 v25, s40, v19\n\t" "s_add_u32 s40, s40, 8192\n\t" "s_cmp_eq_u32 s40, 81920\n\t" "s_cselect_b32 s40, 0, s40\n\t" \
   "ds_read_b64_tr_b16 a[168:169], v24 offset:0\n\t" "ds_read_b64_tr_b16 a[170:171], v24 offset:4096\n\t" \
   "ds_read_b64_tr_b16 a[232:233], v25 offset:0\n\t" "ds_read_b64_tr_b16 a[234:235], v25 offset:4096\n\t" "v_add_u32 v24, s40, v18\n\t" \
   "v_add_u32 v25, s40, v19\n\t" "s_add_u32 s40, s40, 8192\n\t" "s_cmp_eq_u32 s40, 81920\n\t" "s_cselect_b32 s40, 0, s40\n\t" \
   "ds_read_b64_tr_b16 a[176:177], v24 offset:0\n\t" "ds_read_b64_tr_b16 a[178:179], v24 offset:4096\n\t" \
   "ds_read_b64_tr_b16 a[240:241], v25 offset:0\n\t" "ds_read_b64_tr_b16 a[242:243], v25 offset:4096\n\t" "v_add_u32 v24, s40, v18\n\t" \
   "v_add_u32 v25, s40, v19\n\t" "s_add_u32 s40, s40, 8192\n\t" "s_cmp_eq_u32 s40, 81920\n\t" "s_cselect_b32 s40, 0, s40\n\t" \
   "ds_read_b64_tr_b16 a[184:185], v24 offset:0\n\t" "ds_read_b64_tr_b16 a[186:187], v24 offset:4096\n\t" \
   "ds_read_b64_tr_b16 a[248:249], v25 offset:0\n\t" "ds_read_b64_tr_b16 a[250:251], v25 offset:4096\n\t" "v_add_u32 v24, s40, v18\n\t" \
   "v_add_u32 v25, s40, v19\n\t" "s_add_u32 s40, s40, 8192\n\t" "s_cmp_eq_u32 s40, 81920\n\t" "s_cselect_b32 s40, 0, s40\n\t" \
   "ds_read_b64_tr_b16 v[224:225], v24 offset:0\n\t" "ds_read_b64_tr_b16 v[226:227], v24 offset:4096\n\t" \
   "ds_read_b64_tr_b16 v[240:241], v25 offset:0\n\t" "ds_read_b64_tr_b16 v[242:243], v25 offset:4096\n\t" "v_add_u32 v24, s40, v18\n\t" \
   "v_add_u32 v25, s40, v19\n\t" "s_add_u32 s40, s40, 8192\n\t" "s_cmp_eq_u32 s40, 81920\n\t" "s_cselect_b32 s40, 0, s40\n\t" \
   "ds_read_b64_tr_b16 v[232:233], v24 offset:0\n\t" "ds_read_b64_tr_b16 v[234:235], v24 offset:4096\n\t" \
   "ds_read_b64_tr_b16 v[248:249], v25 offset:0\n\t" "ds_read_b64_tr_b16 v[250:251], v25 offset:4096\n\t" "s_waitcnt vmcnt(15)\n\t" \
   "s_waitcnt lgkmcnt(0)\n\t" "s_barrier\n\t" "ds_write_b128 v20, a[4:7] offset:0\n\t" "s_mov_b32 s41, 4456448\n\t" \
   "buffer_load_dwordx4 v[196:199], v16, %[ra], s41 offen\n\t" "s_waitcnt vmcnt(15)\n\t" "ds_write_b128 v20, a[68:71] offset:4096\n\t" \
   "s_mov_b32 s41, 4587520\n\t" "buffer_load_dwordx4 v[212:215], v16, %[ra], s41 offen\n\t" "s_waitcnt vmcnt(15)\n\t" \
   "ds_write_b128 v20, a[12:15] offset:8192\n\t" "s_mov_b32 s41, 4980736\n\t" "buffer_load_dwordx4 v[204:207], v16, %[ra], s41 offen\n\t" \
   "s_waitcnt vmcnt(15)\n\t" "ds_write_b128 v20, a[76:79] offset:12288\n\t" "s_mov_b32 s41, 5111808\n\t" \
   "buffer_load_dwordx4 v[220:223], v16, %[ra], s41 offen\n\t" "s_waitcnt vmcnt(15)\n\t" "ds_write_b128 v20, a[20:23] offset:16384\n\t" \
   "s_mov_b32 s41, 262144\n\t" "buffer_load_dwordx4 a[132:135], v16, %[rb], s41 offen\n\t" "s_waitcnt vmcnt(15)\n\t" \
   "ds_write_b128 v20, a[84:87] offset:20480\n\t" "s_mov_b32 s41, 393216\n\t" "buffer_load_dwordx4 a[196:199], v16, %[rb], s41 offen\n\t" \
   "s_waitcnt vmcnt(15)\n\t" "ds_write_b128 v20, a[28:31] offset:24576\n\t" "s_mov_b32 s41, 786432\n\t" \
   "buffer_load_dwordx4 a[140:143], v16, %[rb], s41 offen\n\t" "s_waitcnt vmcnt(15)\n\t" "ds_write_b128 v20, a[92:95] offset:28672\n\t" \
   "s_mov_b32 s41, 917504\n\t" "buffer_load_dwordx4 a[204:207], v16, %[rb], s41 offen\n\t" "s_waitcnt vmcnt(15)\n\t" \
   "ds_write_b128 v20, a[36:39] offset:32768\n\t" "s_mov_b32 s41, 1310720\n\t" "buffer_load_dwordx4 a[148:151], v16, %[rb], s41 offen\n\t" \
   "s_waitcnt vmcnt(15)\n\t" "ds_write_b128 v20, a[100:103] offset:36864\n\t" "s_mov_b32 s41, 1441792\n\t" \
   "buffer_load_dwordx4 a[212:215], v16, %[rb], s41 offen\n\t" "s_waitcnt vmcnt(15)\n\t" "ds_write_b128 v20, a[44:47] offset:40960\n\t" \
   "s_mov_b32 s41, 1835008\n\t" "buffer_load_dwordx4 a[156:159], v16, %[rb], s41 offen\n\t" "s_waitcnt vmcnt(15)\n\t" \
   "ds_write_b128 v20, a[108:111] offset:45056\n\t" "s_mov_b32 s41, 1966080\n\t" "buffer_load_dwordx4 a[220:223], v16, %[rb], s41 offen\n\t" \
   "s_waitcnt vmcnt(15)\n\t" "ds_write_b128 v20, a[52:55] offset:49152\n\t" "s_mov_b32 s41, 2359296\n\t" \
   "buffer_load_dwordx4 a[164:167], v16, %[rb], s41 offen\n\t" "s_waitcnt vmcnt(15)\n\t" "ds_write_b128 v20, a[116:119] offset:53248\n\t" \
   "s_mov_b32 s41, 2490368\n\t" "buffer_load_dwordx4 a[228:231], v16, %[rb], s41 offen\n\t" "s_waitcnt vmcnt(15)\n\t" \
   "ds_write_b128 v20, a[60:63] offset:57344\n\t" "s_mov_b32 s41, 2883584\n\t" "buffer_load_dwordx4 a[172:175], v16, %[rb], s41 offen\n\t" \
   "s_waitcnt vmcnt(15)\n\t" "ds_write_b128 v20, a[124:127] offset:61440\n\t" "s_mov_b32 s41, 3014656\n\t" \
   "buffer_load_dwordx4 a[236:239], v16, %[rb], s41 offen\n\t" "s_waitcnt vmcnt(15)\n\t" "ds_write_b128 v23, v[196:199] offset:0\n\t" \
   "s_mov_b32 s41, 3407872\n\t" "buffer_load_dwordx4 a[180:183], v16, %[rb], s41 offen\n\t" "s_waitcnt vmcnt(15)\n\t" \
   "ds_write_b128 v23, v[212:215] offset:4096\n\t" "s_mov_b32 s41, 3538944\n\t" "buffer_load_dwordx4 a[244:247], v16, %[rb], s41 offen\n\t" \
   "s_waitcnt vmcnt(15)\n\t" "ds_write_b128 v23, v[204:207] offset:8192\n\t" "s_mov_b32 s41, 3932160\n\t" \
   "buffer_load_dwordx4 a[188:191], v16, %[rb], s41 offen\n\t" "s_waitcnt vmcnt(15)\n\t" "ds_write_b128 v23, v[220:223] offset:12288\n\t" \
   "s_mov_b32 s41, 4063232\n\t" "buffer_load_dwordx4 a[252:255], v16, %[rb], s41 offen\n\t" "s_waitcnt lgkmcnt(0)\n\t" "s_barrier\n\t" LDSTAMP(4) \
   "s_lshl_b32 s40, %[p0], 13\n\t" "v_add_u32 v24, s40, v18\n\t" "v_add_u32 v25, s40, v19\n\t" "s_add_u32 s40, s40, 8192\n\t" \
   "s_cmp_eq_u32 s40, 81920\n\t" "s_cselect_b32 s40, 0, s40\n\t" "ds_read_b64_tr_b16 a[4:5], v24 offset:0\n\t" \
   "ds_read_b64_tr_b16 a[6:7], v24 offset:4096\n\t" "ds_read_b64_tr_b16 a[68:69], v25 offset:0\n\t" "ds_read_b64_tr_b16 a[70:71], v25 offset:4096\n\t" \
   "v_add_u32 v24, s40, v18\n\t" "v_add_u32 v25, s40, v19\n\t" "s_add_u32 s40, s40, 8192\n\t" "s_cmp_eq_u32 s40, 81920\n\t" \
   "s_cselect_b32 s40, 0, s40\n\t" "ds_read_b64_tr_b16 a[12:13], v24 offset:0\n\t" "ds_read_b64_tr_b16 a[14:15], v24 offset:4096\n\t" \
   "ds_read_b64_tr_b16 a[76:77], v25 offset:0\n\t" "ds_read_b64_tr_b16 a[78:79], v25 offset:4096\n\t" "v_add_u32 v24, s40, v18\n\t" \
   "v_add_u32 v25, s40, v19\n\t" "s_add_u32 s40, s40, 8192\n\t" "s_cmp_eq_u32 s40, 81920\n\t" "s_cselect_b32 s40, 0, s40\n\t" \
   "ds_read_b64_tr_b16 a[20:21], v24 offset:0\n\t" "ds_read_b64_tr_b16 a[22:23], v24 offset:4096\n\t" "ds_read_b64_tr_b16 a[84:85], v25 offset:0\n\t" \
   "ds_read_b64_tr_b16 a[86:87], v25 offset:4096\n\t" "v_add_u32 v24, s40, v18\n\t" "v_add_u32 v25, s40, v19\n\t" "s_add_u32 s40, s40, 8192\n\t" \
   "s_cmp_eq_u32 s40, 81920\n\t" "s_cselect_b32 s40, 0, s40\n\t" "ds_read_b64_tr_b16 a[28:29], v24 offset:0\n\t" \
   "ds_read_b64_tr_b16 a[30:31], v24 offset:4096\n\t" "ds_read_b64_tr_b16 a[92:93], v25 offset:0\n\t" \
   "ds_read_b64_tr_b16 a[94:95], v25 offset:4096\n\t" "v_add_u32 v24, s40, v18\n\t" "v_add_u32 v25, s40, v19\n\t" "s_add_u32 s40, s40, 8192\n\t" \
   "s_cmp_eq_u32 s40, 81920\n\t" "s_cselect_b32 s40, 0, s40\n\t" "ds_read_b64_tr_b16 a[36:37], v24 offset:0\n\t" \
   "ds_read_b64_tr_b16 a[38:39], v24 offset:4096\n\t" "ds_read_b64_tr_b16 a[100:101], v25 offset:0\n\t" \
   "ds_read_b64_tr_b16 a[102:103], v25 offset:4096\n\t" "v_add_u32 v24, s40, v18\n\t" "v_add_u32 v25, s40, v19\n\t" "s_add_u32 s40, s40, 8192\n\t" \
   "s_cmp_eq_u32 s40, 81920\n\t" "s_cselect_b32 s40, 0, s40\n\t" "ds_read_b64_tr_b16 a[44:45], v24 offset:0\n\t" \
   "ds_read_b64_tr_b16 a[46:47], v24 offset:4096\n\t" "ds_read_b64_tr_b16 a[108:109], v25 offset:0\n\t" \
   "ds_read_b64_tr_b16 a[110:111], v25 offset:4096\n\t" "v_add_u32 v24, s40, v18\n\t" "v_add_u32 v25, s40, v19\n\t" "s_add_u32 s40, s40, 8192\n\t" \
   "s_cmp_eq_u32 s40, 81920\n\t" "s_cselect_b32 s40, 0, s40\n\t" "ds_read_b64_tr_b16 a[52:53], v24 offset:0\n\t" \
   "ds_read_b64_tr_b16 a[54:55], v24 offset:4096\n\t" "ds_read_b64_tr_b16 a[116:117], v25 offset:0\n\t" \
   "ds_read_b64_tr_b16 a[118:119], v25 offset:4096\n\t" "v_add_u32 v24, s40, v18\n\t" "v_add_u32 v25, s40, v19\n\t" "s_add_u32 s40, s40, 8192\n\t" \
   "s_cmp_eq_u32 s40, 81920\n\t" "s_cselect_b32 s40, 0, s40\n\t" "ds_read_b64_tr_b16 a[60:61], v24 offset:0\n\t" \
   "ds_read_b64_tr_b16 a[62:63], v24 offset:4096\n\t" "ds_read_b64_tr_b16 a[124:125], v25 offset:0\n\t" \
   "ds_read_b64_tr_b16 a[126:127], v25 offset:4096\n\t" "v_add_u32 v24, s40, v18\n\t" "v_add_u32 v25, s40, v19\n\t" "s_add_u32 s40, s40, 8192\n\t" \
   "s_cmp_eq_u32 s40, 81920\n\t" "s_cselect_b32 s40, 0, s40\n\t" "ds_read_b64_tr_b16 v[196:197], v24 offset:0\n\t" \
   "ds_read_b64_tr_b16 v[198:199], v24 offset:4096\n\t" "ds_read_b64_tr_b16 v[212:213], v25 offset:0\n\t" \
   "ds_read_b64_tr_b16 v[214:215], v25 offset:4096\n\t" "v_add_u32 v24, s40, v18\n\t" "v_add_u32 v25, s40, v19\n\t" "s_add_u32 s40, s40, 8192\n\t" \
   "s_cmp_eq_u32 s40, 81920\n\t" "s_cselect_b32 s40, 0, s40\n\t" "ds_read_b64_tr_b16 v[204:205], v24 offset:0\n\t" \
   "ds_read_b64_tr_b16 v[206:207], v24 offset:4096\n\t" "ds_read_b64_tr_b16 v[220:221], v25 offset:0\n\t" \
   "ds_read_b64_tr_b16 v[222:223], v25 offset:4096\n\t" "s_waitcnt vmcnt(15)\n\t" "s_waitcnt lgkmcnt(0)\n\t" "s_barrier\n\t" \
   "ds_write_b128 v20, a[132:135] offset:0\n\t" "s_mov_b32 s41, 4456448\n\t" "buffer_load_dwordx4 v[228:231], v16, %[rb], s41 offen\n\t" \
   "s_waitcnt vmcnt(15)\n\t" "ds_write_b128 v20, a[196:199] offset:4096\n\t" "s_mov_b32 s41, 4587520\n\t" \
   "buffer_load_dwordx4 v[244:247], v16, %[rb], s41 offen\n\t" "s_waitcnt vmcnt(15)\n\t" "ds_write_b128 v20, a[140:143] offset:8192\n\t" \
   "s_mov_b32 s41, 4980736\n\t" "buffer_load_dwordx4 v[236:239], v16, %[rb], s41 offen\n\t" "s_waitcnt vmcnt(15)\n\t" \
   "ds_write_b128 v20, a[204:207] offset:12288\n\t" "s_mov_b32 s41, 5111808\n\t" "buffer_load_dwordx4 v[252:255], v16, %[rb], s41 offen\n\t" \
   "s_waitcnt vmcnt(15)\n\t" "ds_write_b128 v20, a[148:151] offset:16384\n\t" "s_waitcnt vmcnt(14)\n\t" \
   "ds_write_b128 v20, a[212:215] offset:20480\n\t" "s_waitcnt vmcnt(13)\n\t" "ds_write_b128 v20, a[156:159] offset:24576\n\t" \
   "s_waitcnt vmcnt(12)\n\t" "ds_write_b128 v20, a[220:223] offset:28672\n\t" "s_waitcnt vmcnt(11)\n\t" \
   "ds_write_b128 v20, a[164:167] offset:32768\n\t" "s_waitcnt vmcnt(10)\n\t" "ds_write_b128 v20, a[228:231] offset:36864\n\t" \
   "buffer_load_dwordx4 v[28:31], v112, %[rw], 0 offen sc1\n\t" "s_waitcnt vmcnt(10)\n\t" "ds_write_b128 v20, a[172:175] offset:40960\n\t" \
   "s_waitcnt vmcnt(9)\n\t" "ds_write_b128 v20, a[236:239] offset:45056\n\t" "s_waitcnt vmcnt(8)\n\t" "ds_write_b128 v20, a[180:183] offset:49152\n\t" \
   "s_waitcnt vmcnt(7)\n\t" "ds_write_b128 v20, a[244:247] offset:53248\n\t" "s_waitcnt vmcnt(6)\n\t" "ds_write_b128 v20, a[188:191] offset:57344\n\t" \
   "s_waitcnt vmcnt(5)\n\t" "ds_write_b128 v20, a[252:255] offset:61440\n\t" "s_waitcnt vmcnt(4)\n\t" "ds_write_b128 v23, v[228:231] offset:0\n\t" \
   "s_waitcnt vmcnt(3)\n\t" "ds_write_b128 v23, v[244:247] offset:4096\n\t" "s_waitcnt vmcnt(2)\n\t" "ds_write_b128 v23, v[236:239] offset:8192\n\t" \
   "s_waitcnt vmcnt(1)\n\t" "ds_write_b128 v23, v[252:255] offset:12288\n\t" "s_waitcnt lgkmcnt(0)\n\t" "s_barrier\n\t" LDSTAMP(5) \
   "s_lshl_b32 s40, %[p0], 13\n\t" "v_add_u32 v24, s40, v18\n\t" "v_add_u32 v25, s40, v19\n\t" "s_add_u32 s40, s40, 8192\n\t" \
   "s_cmp_eq_u32 s40, 81920\n\t" "s_cselect_b32 s40, 0, s40\n\t" "ds_read_b64_tr_b16 a[132:133], v24 offset:0\n\t" \
   "ds_read_b64_tr_b16 a[134:135], v24 offset:4096\n\t" "ds_read_b64_tr_b16 a[196:197], v25 offset:0\n\t" \
   "ds_read_b64_tr_b16 a[198:199], v25 offset:4096\n\t" "v_add_u32 v24, s40, v18\n\t" "v_add_u32 v25, s40, v19\n\t" "s_add_u32 s40, s40, 8192\n\t" \
   "s_cmp_eq_u32 s40, 81920\n\t" "s_cselect_b32 s40, 0, s40\n\t" "ds_read_b64_tr_b16 a[140:141], v24 offset:0\n\t" \
   "ds_read_b64_tr_b16 a[142:143], v24 offset:4096\n\t" "ds_read_b64_tr_b16 a[204:205], v25 offset:0\n\t" \
   "ds_read_b64_tr_b16 a[206:207], v25 offset:4096\n\t" "v_add_u32 v24, s40, v18\n\t" "v_add_u32 v25, s40, v19\n\t" "s_add_u32 s40, s40, 8192\n\t" \
   "s_cmp_eq_u32 s40, 81920\n\t" "s_cselect_b32 s40, 0, s40\n\t" "ds_read_b64_tr_b16 a[148:149], v24 offset:0\n\t" \
   "ds_read_b64_tr_b16 a[150:151], v24 offset:4096\n\t" "ds_read_b64_tr_b16 a[212:213], v25 offset:0\n\t" \
   "ds_read_b64_tr_b16 a[214:215], v25 offset:4096\n\t" "v_add_u32 v24, s40, v18\n\t" "v_add_u32 v25, s40, v19\n\t" "s_add_u32 s40, s40, 8192\n\t" \
   "s_cmp_eq_u32 s40, 81920\n\t" "s_cselect_b32 s40, 0, s40\n\t" "ds_read_b64_tr_b16 a[156:157], v24 offset:0\n\t" \
   "ds_read_b64_tr_b16 a[158:159], v24 offset:4096\n\t" "ds_read_b64_tr_b16 a[220:221], v25 offset:0\n\t" \
   "ds_read_b64_tr_b16 a[222:223], v25 offset:4096\n\t" "v_add_u32 v24, s40, v18\n\t" "v_add_u32 v25, s40, v19\n\t" "s_add_u32 s40, s40, 8192\n\t" \
   "s_cmp_eq_u32 s40, 81920\n\t" "s_cselect_b32 s40, 0, s40\n\t" "ds_read_b64_tr_b16 a[164:165], v24 offset:0\n\t" \
   "ds_read_b64_tr_b16 a[166:167], v24 offset:4096\n\t" "ds_read_b64_tr_b16 a[228:229], v25 offset:0\n\t" \
   "ds_read_b64_tr_b16 a[230:231], v25 offset:4096\n\t" "v_add_u32 v24, s40, v18\n\t" "v_add_u32 v25, s40, v19\n\t" "s_add_u32 s40, s40, 8192\n\t" \
   "s_cmp_eq_u32 s40, 81920\n\t" "s_cselect_b32 s40, 0, s40\n\t" "ds_read_b64_tr_b16 a[172:173], v24 offset:0\n\t" \
   "ds_read_b64_tr_b16 a[174:175], v24 offset:4096\n\t" "ds_read_b64_tr_b16 a[236:237], v25 offset:0\n\t" \
   "ds_read_b64_tr_b16 a[238:239], v25 offset:4096\n\t" "v_add_u32 v24, s40, v18\n\t" "v_add_u32 v25, s40, v19\n\t" "s_add_u32 s40, s40, 8192\n\t" \
   "s_cmp_eq_u32 s40, 81920\n\t" "s_cselect_b32 s40, 0, s40\n\t" "ds_read_b64_tr_b16 a[180:181], v24 offset:0\n\t" \
   "ds_read_b64_tr_b16 a[182:183], v24 offset:4096\n\t" "ds_read_b64_tr_b16 a[244:245], v25 offset:0\n\t" \
   "ds_read_b64_tr_b16 a[246:247], v25 offset:4096\n\t" "v_add_u32 v24, s40, v18\n\t" "v_add_u32 v25, s40, v19\n\t" "s_add_u32 s40, s40, 8192\n\t" \
   "s_cmp_eq_u32 s40, 81920\n\t" "s_cselect_b32 s40, 0, s40\n\t" "ds_read_b64_tr_b16 a[188:189], v24 offset:0\n\t" \
   "ds_read_b64_tr_b16 a[190:191], v24 offset:4096\n\t" "ds_read_b64_tr_b16 a[252:253], v25 offset:0\n\t" \
   "ds_read_b64_tr_b16 a[254:255], v25 offset:4096\n\t" "v_add_u32 v24, s40, v18\n\t" "v_add_u32 v25, s40, v19\n\t" "s_add_u32 s40, s40, 8192\n\t" \
   "s_cmp_eq_u32 s40, 81920\n\t" "s_cselect_b32 s40, 0, s40\n\t" "ds_read_b64_tr_b16 v[228:229], v24 offset:0\n\t" \
   "ds_read_b64_tr_b16 v[230:231], v24 offset:4096\n\t" "ds_read_b64_tr_b16 v[244:245], v25 offset:0\n\t" \
   "ds_read_b64_tr_b16 v[246:247], v25 offset:4096\n\t" "v_add_u32 v24, s40, v18\n\t" "v_add_u32 v25, s40, v19\n\t" "s_add_u32 s40, s40, 8192\n\t" \
   "s_cmp_eq_u32 s40, 81920\n\t" "s_cselect_b32 s40, 0, s40\n\t" "ds_read_b64_tr_b16 v[236:237], v24 offset:0\n\t" \
   "ds_read_b64_tr_b16 v[238:239], v24 offset:4096\n\t" "ds_read_b64_tr_b16 v[252:253], v25 offset:0\n\t" \
   "ds_read_b64_tr_b16 v[254:255], v25 offset:4096\n\t" "s_waitcnt vmcnt(0) lgkmcnt(0)\n\t" "s_barrier"
#define LDP_CLOB "v24", "v25", "v26", "v32", "v33", "v34", "v35", "v36", "v37", "v38", "v39", "v40", "v41", "v42", "v43", "v44", "v45", "v46", "v47", "v48", "v49", "v50", "v51", "v52", "v53", "v54", "v55", "v56", "v57", "v58", "v59", "v60", "v61", "v62", "v63", "v64", "v65", "v66", "v67", "v68", "v69", "v70", "v71", "v72", "v73", "v74", "v75", "v76", "v77", "v78", "v79", "v80", "v81", "v82", "v83", "v84", "v85", "v86", "v87", "v88", "v89", "v90", "v91", "v92", "v93", "v94", "v95", "v96", "v97", "v98", "v99", "v100", "v101", "v102", "v103", "v104", "v105", "v106", "v107", "v108", "v109", "v110", "v111", "v120", "v121", "v122", "v123", "s40", "s41"
#define LDK_ASM \
   "buffer_load_dword v26, v113, %[rw], 0 offen sc1\n\t" "s_mov_b32 s41, 0\n\t" "buffer_load_dwordx4 v[32:35], v116, %[rx], s41 offen\n\t" \
   "s_mov_b32 s41, 10240\n\t" "buffer_load_dwordx4 v[36:39], v116, %[rx], s41 offen\n\t" "s_mov_b32 s41, 128\n\t" \
   "buffer_load_dwordx4 v[40:43], v116, %[rx], s41 offen\n\t" "s_mov_b32 s41, 10368\n\t" "buffer_load_dwordx4 v[44:47], v116, %[rx], s41 offen\n\t" \
   "s_mov_b32 s41, 256\n\t" "buffer_load_dwordx4 v[48:51], v116, %[rx], s41 offen\n\t" "s_mov_b32 s41, 10496\n\t" \
   "buffer_load_dwordx4 v[52:55], v116, %[rx], s41 offen\n\t" "s_mov_b32 s41, 384\n\t" "buffer_load_dwordx4 v[56:59], v116, %[rx], s41 offen\n\t" \
   "s_mov_b32 s41, 10624\n\t" "buffer_load_dwordx4 v[60:63], v116, %[rx], s41 offen\n\t" "s_mov_b32 s41, 512\n\t" \
   "buffer_load_dwordx4 v[64:67], v116, %[rx], s41 offen\n\t" "s_mov_b32 s41, 10752\n\t" "buffer_load_dwordx4 v[68:71], v116, %[rx], s41 offen\n\t" \
   "s_mov_b32 s41, 640\n\t" "buffer_load_dwordx4 v[72:75], v116, %[rx], s41 offen\n\t" "s_mov_b32 s41, 10880\n\t" \
   "buffer_load_dwordx4 v[76:79], v116, %[rx], s41 offen\n\t" "s_mov_b32 s41, 768\n\t" "buffer_load_dwordx4 v[80:83], v116, %[rx], s41 offen\n\t" \
   "s_mov_b32 s41, 11008\n\t" "buffer_load_dwordx4 v[84:87], v116, %[rx], s41 offen\n\t" "s_mov_b32 s41, 896\n\t" \
   "buffer_load_dwordx4 v[88:91], v116, %[rx], s41 offen\n\t" "s_mov_b32 s41, 11136\n\t" "buffer_load_dwordx4 v[92:95], v116, %[rx], s41 offen\n\t" \
   "s_mov_b32 s40, 0x3f803f80\n\t" "v_mov_b32 v120, 0\n\t" "v_mov_b32 v121, 0\n\t" "v_mov_b32 v122, 0\n\t" "v_mov_b32 v123, 0\n\t" \
   "s_waitcnt vmcnt(16)\n\t" "v_add_u32 v26, 1, v26\n\t" "buffer_store_dword v26, v114, %[rw], 0 offen sc1\n\t" "ds_write_b32 v115, v26\n\t" LDSTAMP(0) \
   "s_waitcnt vmcnt(15)\n\t" LDSTAMP(1) "ds_write_b64 v117, v[32:33] offset:0\n\t" "ds_write_b64 v117, v[34:35] offset:256\n\t" \
   "s_mov_b32 s41, 1024\n\t" "buffer_load_dwordx4 v[96:99], v116, %[rx], s41 offen\n\t" "v_dot2c_f32_bf16 v120, s40, v32\n\t" \
   "v_dot2c_f32_bf16 v121, v32, v32\n\t" "v_dot2c_f32_bf16 v120, s40, v33\n\t" "v_dot2c_f32_bf16 v121, v33, v33\n\t" \
   "v_dot2c_f32_bf16 v120, s40, v34\n\t" "v_dot2c_f32_bf16 v121, v34, v34\n\t" "v_dot2c_f32_bf16 v120, s40, v35\n\t" \
   "v_dot2c_f32_bf16 v121, v35, v35\n\t" "s_waitcnt vmcnt(15)\n\t" "ds_write_b64 v117, v[36:37] offset:128\n\t" \
   "ds_write_b64 v117, v[38:39] offset:384\n\t" "s_mov_b32 s41, 11264\n\t" "buffer_load_dwordx4 v[100:103], v116, %[rx], s41 offen\n\t" \
   "v_dot2c_f32_bf16 v122, s40, v36\n\t" "v_dot2c_f32_bf16 v123, v36, v36\n\t" "v_dot2c_f32_bf16 v122, s40, v37\n\t" \
   "v_dot2c_f32_bf16 v123, v37, v37\n\t" "v_dot2c_f32_bf16 v122, s40, v38\n\t" "v_dot2c_f32_bf16 v123, v38, v38\n\t" \
   "v_dot2c_f32_bf16 v122, s40, v39\n\t" "v_dot2c_f32_bf16 v123, v39, v39\n\t" "s_waitcnt vmcnt(15)\n\t" "ds_write_b64 v117, v[40:41] offset:2048\n\t" \
   "ds_write_b64 v117, v[42:43] offset:2304\n\t" "s_mov_b32 s41, 1152\n\t" "buffer_load_dwordx4 v[104:107], v116, %[rx], s41 offen\n\t" \
   "v_dot2c_f32_bf16 v120, s40, v40\n\t" "v_dot2c_f32_bf16 v121, v40, v40\n\t" "v_dot2c_f32_bf16 v120, s40, v41\n\t" \
   "v_dot2c_f32_bf16 v121, v41, v41\n\t" "v_dot2c_f32_bf16 v120, s40, v42\n\t" "v_dot2c_f32_bf16 v121, v42, v42\n\t" \
   "v_dot2c_f32_bf16 v120, s40, v43\n\t" "v_dot2c_f32_bf16 v121, v43, v43\n\t" "s_waitcnt vmcnt(15)\n\t" "ds_write_b64 v117, v[44:45] offset:2176\n\t" \
   "ds_write_b64 v117, v[46:47] offset:2432\n\t" "s_mov_b32 s41, 11392\n\t" "buffer_load_dwordx4 v[108:111], v116, %[rx], s41 offen\n\t" \
   "v_dot2c_f32_bf16 v122, s40, v44\n\t" "v_dot2c_f32_bf16 v123, v44, v44\n\t" "v_dot2c_f32_bf16 v122, s40, v45\n\t" \
   "v_dot2c_f32_bf16 v123, v45, v45\n\t" "v_dot2c_f32_bf16 v122, s40, v46\n\t" "v_dot2c_f32_bf16 v123, v46, v46\n\t" \
   "v_dot2c_f32_bf16 v122, s40, v47\n\t" "v_dot2c_f32_bf16 v123, v47, v47\n\t" "s_waitcnt vmcnt(15)\n\t" "ds_write_b64 v117, v[48:49] offset:4096\n\t" \
   "ds_write_b64 v117, v[50:51] offset:4352\n\t" "s_mov_b32 s41, 0\n\t" "buffer_load_dwordx4 a[0:3], v16, %[ra], s41 offen\n\t" \
   "v_dot2c_f32_bf16 v120, s40, v48\n\t" "v_dot2c_f32_bf16 v121, v48, v48\n\t" "v_dot2c_f32_bf16 v120, s40, v49\n\t" \
   "v_dot2c_f32_bf16 v121, v49, v49\n\t" "v_dot2c_f32_bf16 v120, s40, v50\n\t" "v_dot2c_f32_bf16 v121, v50, v50\n\t" \
   "v_dot2c_f32_bf16 v120, s40, v51\n\t" "v_dot2c_f32_bf16 v121, v51, v51\n\t" "s_waitcnt vmcnt(15)\n\t" "ds_write_b64 v117, v[52:53] offset:4224\n\t" \
   "ds_write_b64 v117, v[54:55] offset:4480\n\t" "s_mov_b32 s41, 131072\n\t" "buffer_load_dwordx4 a[4:7], v16, %[ra], s41 offen\n\t" \
   "v_dot2c_f32_bf16 v122, s40, v52\n\t" "v_dot2c_f32_bf16 v123, v52, v52\n\t" "v_dot2c_f32_bf16 v122, s40, v53\n\t" \
   "v_dot2c_f32_bf16 v123, v53, v53\n\t" "v_dot2c_f32_bf16 v122, s40, v54\n\t" "v_dot2c_f32_bf16 v123, v54, v54\n\t" \
   "v_dot2c_f32_bf16 v122, s40, v55\n\t" "v_dot2c_f32_bf16 v123, v55, v55\n\t" "s_waitcnt vmcnt(15)\n\t" "ds_write_b64 v117, v[56:57] offset:6144\n\t" \
   "ds_write_b64 v117, v[58:59] offset:6400\n\t" "s_mov_b32 s41, 262144\n\t" "buffer_load_dwordx4 a[16:19], v16, %[ra], s41 offen\n\t" \
   "v_dot2c_f32_bf16 v120, s40, v56\n\t" "v_dot2c_f32_bf16 v121, v56, v56\n\t" "v_dot2c_f32_bf16 v120, s40, v57\n\t" \
   "v_dot2c_f32_bf16 v121, v57, v57\n\t" "v_dot2c_f32_bf16 v120, s40, v58\n\t" "v_dot2c_f32_bf16 v121, v58, v58\n\t" \
   "v_dot2c_f32_bf16 v120, s40, v59\n\t" "v_dot2c_f32_bf16 v121, v59, v59\n\t" "s_waitcnt vmcnt(15)\n\t" "ds_write_b64 v117, v[60:61] offset:6272\n\t" \
   "ds_write_b64 v117, v[62:63] offset:6528\n\t" "s_mov_b32 s41, 393216\n\t" "buffer_load_dwordx4 a[20:23], v16, %[ra], s41 offen\n\t" \
   "v_dot2c_f32_bf16 v122, s40, v60\n\t" "v_dot2c_f32_bf16 v123, v60, v60\n\t" "v_dot2c_f32_bf16 v122, s40, v61\n\t" \
   "v_dot2c_f32_bf16 v123, v61, v61\n\t" "v_dot2c_f32_bf16 v122, s40, v62\n\t" "v_dot2c_f32_bf16 v123, v62, v62\n\t" \
   "v_dot2c_f32_bf16 v122, s40, v63\n\t" "v_dot2c_f32_bf16 v123, v63, v63\n\t" "s_waitcnt vmcnt(15)\n\t" "ds_write_b64 v117, v[64:65] offset:8192\n\t" \
   "ds_write_b64 v117, v[66:67] offset:8448\n\t" "s_mov_b32 s41, 524288\n\t" "buffer_load_dwordx4 a[32:35], v16, %[ra], s41 offen\n\t" \
   "v_dot2c_f32_bf16 v120, s40, v64\n\t" "v_dot2c_f32_bf16 v121, v64, v64\n\t" "v_dot2c_f32_bf16 v120, s40, v65\n\t" \
   "v_dot2c_f32_bf16 v121, v65, v65\n\t" "v_dot2c_f32_bf16 v120, s40, v66\n\t" "v_dot2c_f32_bf16 v121, v66, v66\n\t" \
   "v_dot2c_f32_bf16 v120, s40, v67\n\t" "v_dot2c_f32_bf16 v121, v67, v67\n\t" "s_waitcnt vmcnt(15)\n\t" "ds_write_b64 v117, v[68:69] offset:8320\n\t" \
   "ds_write_b64 v117, v[70:71] offset:8576\n\t" "s_mov_b32 s41, 655360\n\t" "buffer_load_dwordx4 a[36:39], v16, %[ra], s41 offen\n\t" \
   "v_dot2c_f32_bf16 v122, s40, v68\n\t" "v_dot2c_f32_bf16 v123, v68, v68\n\t" "v_dot2c_f32_bf16 v122, s40, v69\n\t" \
   "v_dot2c_f32_bf16 v123, v69, v69\n\t" "v_dot2c_f32_bf16 v122, s40, v70\n\t" "v_dot2c_f32_bf16 v123, v70, v70\n\t" \
   "v_dot2c_f32_bf16 v122, s40, v71\n\t" "v_dot2c_f32_bf16 v123, v71, v71\n\t" "s_waitcnt vmcnt(15)\n\t" "ds_write_b64 v117, v[72:73] offset:10240\n\t" \
   "ds_write_b64 v117, v[74:75] offset:10496\n\t" "s_mov_b32 s41, 786432\n\t" "buffer_load_dwordx4 a[48:51], v16, %[ra], s41 offen\n\t" \
   "v_dot2c_f32_bf16 v120, s40, v72\n\t" "v_dot2c_f32_bf16 v121, v72, v72\n\t" "v_dot2c_f32_bf16 v120, s40, v73\n\t" \
   "v_dot2c_f32_bf16 v121, v73, v73\n\t" "v_dot2c_f32_bf16 v120, s40, v74\n\t" "v_dot2c_f32_bf16 v121, v74, v74\n\t" \
   "v_dot2c_f32_bf16 v120, s40, v75\n\t" "v_dot2c_f32_bf16 v121, v75, v75\n\t" "s_waitcnt vmcnt(15)\n\t" "ds_write_b64 v117, v[76:77] offset:10368\n\t" \
   "ds_write_b64 v117, v[78:79] offset:10624\n\t" "s_mov_b32 s41, 917504\n\t" "buffer_load_dwordx4 a[52:55], v16, %[ra], s41 offen\n\t" \
   "v_dot2c_f32_bf16 v122, s40, v76\n\t" "v_dot2c_f32_bf16 v123, v76, v76\n\t" "v_dot2c_f32_bf16 v122, s40, v77\n\t" \
   "v_dot2c_f32_bf16 v123, v77, v77\n\t" "v_dot2c_f32_bf16 v122, s40, v78\n\t" "v_dot2c_f32_bf16 v123, v78, v78\n\t" \
   "v_dot2c_f32_bf16 v122, s40, v79\n\t" "v_dot2c_f32_bf16 v123, v79, v79\n\t" "s_waitcnt vmcnt(15)\n\t" "ds_write_b64 v117, v[80:81] offset:12288\n\t" \
   "ds_write_b64 v117, v[82:83] offset:12544\n\t" "s_mov_b32 s41, 1048576\n\t" "buffer_load_dwordx4 a[64:67], v16, %[ra], s41 offen\n\t" \
   "v_dot2c_f32_bf16 v120, s40, v80\n\t" "v_dot2c_f32_bf16 v121, v80, v80\n\t" "v_dot2c_f32_bf16 v120, s40, v81\n\t" \
   "v_dot2c_f32_bf16 v121, v81, v81\n\t" "v_dot2c_f32_bf16 v120, s40, v82\n\t" "v_dot2c_f32_bf16 v121, v82, v82\n\t" \
   "v_dot2c_f32_bf16 v120, s40, v83\n\t" "v_dot2c_f32_bf16 v121, v83, v83\n\t" "s_waitcnt vmcnt(15)\n\t" "ds_write_b64 v117, v[84:85] offset:12416\n\t" \
   "ds_write_b64 v117, v[86:87] offset:12672\n\t" "s_mov_b32 s41, 1179648\n\t" "buffer_load_dwordx4 a[68:71], v16, %[ra], s41 offen\n\t" \
   "v_dot2c_f32_bf16 v122, s40, v84\n\t" "v_dot2c_f32_bf16 v123, v84, v84\n\t" "v_dot2c_f32_bf16 v122, s40, v85\n\t" \
   "v_dot2c_f32_bf16 v123, v85, v85\n\t" "v_dot2c_f32_bf16 v122, s40, v86\n\t" "v_dot2c_f32_bf16 v123, v86, v86\n\t" \
   "v_dot2c_f32_bf16 v122, s40, v87\n\t" "v_dot2c_f32_bf16 v123, v87, v87\n\t" "s_waitcnt vmcnt(15)\n\t" "ds_write_b64 v117, v[88:89] offset:14336\n\t" \
   "ds_write_b64 v117, v[90:91] offset:14592\n\t" "s_mov_b32 s41, 0\n\t" "buffer_load_dwordx4 a[8:11], v16, %[rb], s41 offen\n\t" \
   "v_dot2c_f32_bf16 v120, s40, v88\n\t" "v_dot2c_f32_bf16 v121, v88, v88\n\t" "v_dot2c_f32_bf16 v120, s40, v89\n\t" \
   "v_dot2c_f32_bf16 v121, v89, v89\n\t" "v_dot2c_f32_bf16 v120, s40, v90\n\t" "v_dot2c_f32_bf16 v121, v90, v90\n\t" \
   "v_dot2c_f32_bf16 v120, s40, v91\n\t" "v_dot2c_f32_bf16 v121, v91, v91\n\t" "s_waitcnt vmcnt(15)\n\t" "ds_write_b64 v117, v[92:93] offset:14464\n\t" \
   "ds_write_b64 v117, v[94:95] offset:14720\n\t" "s_mov_b32 s41, 131072\n\t" "buffer_load_dwordx4 a[12:15], v16, %[rb], s41 offen\n\t" \
   "v_dot2c_f32_bf16 v122, s40, v92\n\t" "v_dot2c_f32_bf16 v123, v92, v92\n\t" "v_dot2c_f32_bf16 v122, s40, v93\n\t" \
   "v_dot2c_f32_bf16 v123, v93, v93\n\t" "v_dot2c_f32_bf16 v122, s40, v94\n\t" "v_dot2c_f32_bf16 v123, v94, v94\n\t" \
   "v_dot2c_f32_bf16 v122, s40, v95\n\t" "v_dot2c_f32_bf16 v123, v95, v95\n\t" "s_waitcnt vmcnt(15)\n\t" "ds_write_b64 v117, v[96:97] offset:16384\n\t" \
   "ds_write_b64 v117, v[98:99] offset:16640\n\t" "s_mov_b32 s41, 262144\n\t" "buffer_load_dwordx4 a[24:27], v16, %[rb], s41 offen\n\t" \
   "v_dot2c_f32_bf16 v120, s40, v96\n\t" "v_dot2c_f32_bf16 v121, v96, v96\n\t" "v_dot2c_f32_bf16 v120, s40, v97\n\t" \
   "v_dot2c_f32_bf16 v121, v97, v97\n\t" "v_dot2c_f32_bf16 v120, s40, v98\n\t" "v_dot2c_f32_bf16 v121, v98, v98\n\t" \
   "v_dot2c_f32_bf16 v120, s40, v99\n\t" "v_dot2c_f32_bf16 v121, v99, v99\n\t" "s_waitcnt vmcnt(15)\n\t" \
   "ds_write_b64 v117, v[100:101] offset:16512\n\t" "ds_write_b64 v117, v[102:103] offset:16768\n\t" "s_mov_b32 s41, 393216\n\t" \
   "buffer_load_dwordx4 a[28:31], v16, %[rb], s41 offen\n\t" "v_dot2c_f32_bf16 v122, s40, v100\n\t" "v_dot2c_f32_bf16 v123, v100, v100\n\t" \
   "v_dot2c_f32_bf16 v122, s40, v101\n\t" "v_dot2c_f32_bf16 v123, v101, v101\n\t" "v_dot2c_f32_bf16 v122, s40, v102\n\t" \
   "v_dot2c_f32_bf16 v123, v102, v102\n\t" "v_dot2c_f32_bf16 v122, s40, v103\n\t" "v_dot2c_f32_bf16 v123, v103, v103\n\t" "s_waitcnt vmcnt(15)\n\t" \
   "ds_write_b64 v117, v[104:105] offset:18432\n\t" "ds_write_b64 v117, v[106:107] offset:18688\n\t" "s_mov_b32 s41, 524288\n\t" \
   "buffer_load_dwordx4 a[40:43], v16, %[rb], s41 offen\n\t" "v_dot2c_f32_bf16 v120, s40, v104\n\t" "v_dot2c_f32_bf16 v121, v104, v104\n\t" \
   "v_dot2c_f32_bf16 v120, s40, v105\n\t" "v_dot2c_f32_bf16 v121, v105, v105\n\t" "v_dot2c_f32_bf16 v120, s40, v106\n\t" \
   "v_dot2c_f32_bf16 v121, v106, v106\n\t" "v_dot2c_f32_bf16 v120, s40, v107\n\t" "v_dot2c_f32_bf16 v121, v107, v107\n\t" "s_waitcnt vmcnt(15)\n\t" \
   "ds_write_b64 v117, v[108:109] offset:18560\n\t" "ds_write_b64 v117, v[110:111] offset:18816\n\t" "s_mov_b32 s41, 655360\n\t" \
   "buffer_load_dwordx4 a[44:47], v16, %[rb], s41 offen\n\t" "v_dot2c_f32_bf16 v122, s40, v108\n\t" "v_dot2c_f32_bf16 v123, v108, v108\n\t" \
   "v_dot2c_f32_bf16 v122, s40, v109\n\t" "v_dot2c_f32_bf16 v123, v109, v109\n\t" "v_dot2c_f32_bf16 v122, s40, v110\n\t" \
   "v_dot2c_f32_bf16 v123, v110, v110\n\t" "v_dot2c_f32_bf16 v122, s40, v111\n\t" "v_dot2c_f32_bf16 v123, v111, v111\n\t" "s_nop 3\n\t" \
   "v_add_f32_dpp v120, v120, v120 quad_perm:[1,0,3,2] row_mask:0xf bank_mask:0xf\n\t" \
   "v_add_f32_dpp v121, v121, v121 quad_perm:[1,0,3,2] row_mask:0xf bank_mask:0xf\n\t" \
   "v_add_f32_dpp v122, v122, v122 quad_perm:[1,0,3,2] row_mask:0xf bank_mask:0xf\n\t" \
   "v_add_f32_dpp v123, v123, v123 quad_perm:[1,0,3,2] row_mask:0xf bank_mask:0xf\n\t" \
   "v_add_f32_dpp v120, v120, v120 quad_perm:[2,3,0,1] row_mask:0xf bank_mask:0xf\n\t" \
   "v_add_f32_dpp v121, v121, v121 quad_perm:[2,3,0,1] row_mask:0xf bank_mask:0xf\n\t" \
   "v_add_f32_dpp v122, v122, v122 quad_perm:[2,3,0,1] row_mask:0xf bank_mask:0xf\n\t" \
   "v_add_f32_dpp v123, v123, v123 quad_perm:[2,3,0,1] row_mask:0xf bank_mask:0xf\n\t" \
   "v_add_f32_dpp v120, v120, v120 row_half_mirror row_mask:0xf bank_mask:0xf\n\t" \
   "v_add_f32_dpp v121, v121, v121 row_half_mirror row_mask:0xf bank_mask:0xf\n\t" \
   "v_add_f32_dpp v122, v122, v122 row_half_mirror row_mask:0xf bank_mask:0xf\n\t" \
   "v_add_f32_dpp v123, v123, v123 row_half_mirror row_mask:0xf bank_mask:0xf\n\t" "v_and_b32 v120, v119, v120\n\t" "v_and_b32 v121, v119, v121\n\t" \
   "v_and_b32 v122, v119, v122\n\t" "v_and_b32 v123, v119, v123\n\t" "ds_write_b64 v118, v[120:121]\n\t" "ds_write_b64 v118, v[122:123] offset:384\n\t" \
   "s_waitcnt vmcnt(15)\n\t" "ds_write_b128 v20, a[0:3] offset:0\n\t" "s_mov_b32 s41, 786432\n\t" \
   "buffer_load_dwordx4 a[56:59], v16, %[rb], s41 offen\n\t" "s_waitcnt vmcnt(15)\n\t" "ds_write_b128 v20, a[4:7] offset:4096\n\t" \
   "s_mov_b32 s41, 917504\n\t" "buffer_load_dwordx4 a[60:63], v16, %[rb], s41 offen\n\t" "s_waitcnt vmcnt(15)\n\t" \
   "ds_write_b128 v20, a[16:19] offset:8192\n\t" "s_mov_b32 s41, 1048576\n\t" "buffer_load_dwordx4 a[72:75], v16, %[rb], s41 offen\n\t" \
   "s_waitcnt vmcnt(15)\n\t" "ds_write_b128 v20, a[20:23] offset:12288\n\t" "s_mov_b32 s41, 1179648\n\t" \
   "buffer_load_dwordx4 a[76:79], v16, %[rb], s41 offen\n\t" "s_waitcnt vmcnt(15)\n\t" "ds_write_b128 v20, a[32:35] offset:16384\n\t" \
   "s_mov_b32 s41, 1310720\n\t" "buffer_load_dwordx4 a[80:83], v16, %[ra], s41 offen\n\t" "s_waitcnt vmcnt(15)\n\t" \
   "ds_write_b128 v20, a[36:39] offset:20480\n\t" "s_mov_b32 s41, 1441792\n\t" "buffer_load_dwordx4 a[84:87], v16, %[ra], s41 offen\n\t" \
   "s_waitcnt vmcnt(15)\n\t" "ds_write_b128 v20, a[48:51] offset:24576\n\t" "s_mov_b32 s41, 1572864\n\t" \
   "buffer_load_dwordx4 a[96:99], v16, %[ra], s41 offen\n\t" "s_waitcnt vmcnt(15)\n\t" "ds_write_b128 v20, a[52:55] offset:28672\n\t" \
   "s_mov_b32 s41, 1703936\n\t" "buffer_load_dwordx4 a[100:103], v16, %[ra], s41 offen\n\t" "s_waitcnt vmcnt(15)\n\t" \
   "ds_write_b128 v20, a[64:67] offset:32768\n\t" "s_mov_b32 s41, 1835008\n\t" "buffer_load_dwordx4 a[112:115], v16, %[ra], s41 offen\n\t" \
   "s_waitcnt vmcnt(15)\n\t" "ds_write_b128 v20, a[68:71] offset:36864\n\t" "s_mov_b32 s41, 1966080\n\t" \
   "buffer_load_dwordx4 a[116:119], v16, %[ra], s41 offen\n\t" "s_waitcnt lgkmcnt(0)\n\t" "s_barrier\n\t" LDSTAMP(2) \
   "ds_read_b64_tr_b16 a[0:1], v18 offset:0\n\t" "ds_read_b64_tr_b16 a[2:3], v18 offset:4096\n\t" "ds_read_b64_tr_b16 a[4:5], v19 offset:0\n\t" \
   "ds_read_b64_tr_b16 a[6:7], v19 offset:4096\n\t" "ds_read_b64_tr_b16 a[16:17], v18 offset:8192\n\t" \
   "ds_read_b64_tr_b16 a[18:19], v18 offset:12288\n\t" "ds_read_b64_tr_b16 a[20:21], v19 offset:8192\n\t" \
   "ds_read_b64_tr_b16 a[22:23], v19 offset:12288\n\t" "ds_read_b64_tr_b16 a[32:33], v18 offset:16384\n\t" \
   "ds_read_b64_tr_b16 a[34:35], v18 offset:20480\n\t" "ds_read_b64_tr_b16 a[36:37], v19 offset:16384\n\t" \
   "ds_read_b64_tr_b16 a[38:39], v19 offset:20480\n\t" "ds_read_b64_tr_b16 a[48:49], v18 offset:24576\n\t" \
   "ds_read_b64_tr_b16 a[50:51], v18 offset:28672\n\t" "ds_read_b64_tr_b16 a[52:53], v19 offset:24576\n\t" \
   "ds_read_b64_tr_b16 a[54:55], v19 offset:28672\n\t" "ds_read_b64_tr_b16 a[64:65], v18 offset:32768\n\t" \
   "ds_read_b64_tr_b16 a[66:67], v18 offset:36864\n\t" "ds_read_b64_tr_b16 a[68:69], v19 offset:32768\n\t" \
   "ds_read_b64_tr_b16 a[70:71], v19 offset:36864\n\t" "s_waitcnt vmcnt(15)\n\t" "s_waitcnt lgkmcnt(0)\n\t" "s_barrier\n\t" \
   "v_mov_b32 v64, 0x3f803f80\n\t" "v_mov_b32 v65, 0x3f803f80\n\t" "v_mov_b32 v66, 0x3f803f80\n\t" "v_mov_b32 v67, 0x3f803f80\n\t" \
   "ds_write_b128 v20, a[8:11] offset:0\n\t" "s_mov_b32 s41, 2097152\n\t" "buffer_load_dwordx4 a[128:131], v16, %[ra], s41 offen\n\t" \
   "v_mfma_f32_32x32x16_bf16 v[32:47], a[0:3], v[64:67], 0\n\t" "s_waitcnt vmcnt(15)\n\t" "ds_write_b128 v20, a[12:15] offset:4096\n\t" \
   "s_mov_b32 s41, 2228224\n\t" "buffer_load_dwordx4 a[132:135], v16, %[ra], s41 offen\n\t" \
   "v_mfma_f32_32x32x16_bf16 v[32:47], a[4:7], v[64:67], v[32:47]\n\t" "s_waitcnt vmcnt(15)\n\t" "ds_write_b128 v20, a[24:27] offset:8192\n\t" \
   "s_mov_b32 s41, 2359296\n\t" "buffer_load_dwordx4 a[144:147], v16, %[ra], s41 offen\n\t" \
   "v_mfma_f32_32x32x16_bf16 v[32:47], a[16:19], v[64:67], v[32:47]\n\t" "s_waitcnt vmcnt(15)\n\t" "ds_write_b128 v20, a[28:31] offset:12288\n\t" \
   "s_mov_b32 s41, 2490368\n\t" "buffer_load_dwordx4 a[148:151], v16, %[ra], s41 offen\n\t" \
   "v_mfma_f32_32x32x16_bf16 v[32:47], a[20:23], v[64:67], v[32:47]\n\t" "s_waitcnt vmcnt(15)\n\t" "ds_write_b128 v20, a[40:43] offset:16384\n\t" \
   "s_mov_b32 s41, 1310720\n\t" "buffer_load_dwordx4 a[88:91], v16, %[rb], s41 offen\n\t" \
   "v_mfma_f32_32x32x16_bf16 v[32:47], a[32:35], v[64:67], v[32:47]\n\t" "s_waitcnt vmcnt(15)\n\t" "ds_write_b128 v20, a[44:47] offset:20480\n\t" \
   "s_mov_b32 s41, 1441792\n\t" "buffer_load_dwordx4 a[92:95], v16, %[rb], s41 offen\n\t" \
   "v_mfma_f32_32x32x16_bf16 v[32:47], a[36:39], v[64:67], v[32:47]\n\t" "s_waitcnt vmcnt(15)\n\t" "ds_write_b128 v20, a[56:59] offset:24576\n\t" \
   "s_mov_b32 s41, 1572864\n\t" "buffer_load_dwordx4 a[104:107], v16, %[rb], s41 offen\n\t" \
   "v_mfma_f32_32x32x16_bf16 v[32:47], a[48:51], v[64:67], v[32:47]\n\t" "s_waitcnt vmcnt(15)\n\t" "ds_write_b128 v20, a[60:63] offset:28672\n\t" \
   "s_mov_b32 s41, 1703936\n\t" "buffer_load_dwordx4 a[108:111], v16, %[rb], s41 offen\n\t" \
   "v_mfma_f32_32x32x16_bf16 v[32:47], a[52:55], v[64:67], v[32:47]\n\t" "s_waitcnt vmcnt(15)\n\t" "ds_write_b128 v20, a[72:75] offset:32768\n\t" \
   "s_mov_b32 s41, 1835008\n\t" "buffer_load_dwordx4 a[120:123], v16, %[rb], s41 offen\n\t" \
   "v_mfma_f32_32x32x16_bf16 v[32:47], a[64:67], v[64:67], v[32:47]\n\t" "s_waitcnt vmcnt(15)\n\t" "ds_write_b128 v20, a[76:79] offset:36864\n\t" \
   "s_mov_b32 s41, 1966080\n\t" "buffer_load_dwordx4 a[124:127], v16, %[rb], s41 offen\n\t" \
   "v_mfma_f32_32x32x16_bf16 v[32:47], a[68:71], v[64:67], v[32:47]\n\t" "s_waitcnt lgkmcnt(0)\n\t" "s_barrier\n\t" LDSTAMP(3) \
   "ds_read_b64_tr_b16 a[8:9], v18 offset:0\n\t" "ds_read_b64_tr_b16 a[10:11], v18 offset:4096\n\t" "ds_read_b64_tr_b16 a[12:13], v19 offset:0\n\t" \
   "ds_read_b64_tr_b16 a[14:15], v19 offset:4096\n\t" "ds_read_b64_tr_b16 a[24:25], v18 offset:8192\n\t" \
   "ds_read_b64_tr_b16 a[26:27], v18 offset:12288\n\t" "ds_read_b64_tr_b16 a[28:29], v19 offset:8192\n\t" \
   "ds_read_b64_tr_b16 a[30:31], v19 offset:12288\n\t" "ds_read_b64_tr_b16 a[40:41], v18 offset:16384\n\t" \
   "ds_read_b64_tr_b16 a[42:43], v18 offset:20480\n\t" "ds_read_b64_tr_b16 a[44:45], v19 offset:16384\n\t" \
   "ds_read_b64_tr_b16 a[46:47], v19 offset:20480\n\t" "ds_read_b64_tr_b16 a[56:57], v18 offset:24576\n\t" \
   "ds_read_b64_tr_b16 a[58:59], v18 offset:28672\n\t" "ds_read_b64_tr_b16 a[60:61], v19 offset:24576\n\t" \
   "ds_read_b64_tr_b16 a[62:63], v19 offset:28672\n\t" "ds_read_b64_tr_b16 a[72:73], v18 offset:32768\n\t" \
   "ds_read_b64_tr_b16 a[74:75], v18 offset:36864\n\t" "ds_read_b64_tr_b16 a[76:77], v19 offset:32768\n\t" \
   "ds_read_b64_tr_b16 a[78:79], v19 offset:36864\n\t" "s_waitcnt vmcnt(15)\n\t" "s_waitcnt lgkmcnt(0)\n\t" "s_barrier\n\t" \
   "ds_write_b128 v20, a[80:83] offset:0\n\t" "s_mov_b32 s41, 2097152\n\t" "buffer_load_dwordx4 a[136:139], v16, %[rb], s41 offen\n\t" \
   "v_mfma_f32_32x32x16_bf16 v[48:63], a[8:11], v[64:67], 0\n\t" "s_waitcnt vmcnt(15)\n\t" "ds_write_b128 v20, a[84:87] offset:4096\n\t" \
   "s_mov_b32 s41, 2228224\n\t" "buffer_load_dwordx4 a[140:143], v16, %[rb], s41 offen\n\t" \
   "v_mfma_f32_32x32x16_bf16 v[48:63], a[12:15], v[64:67], v[48:63]\n\t" "s_waitcnt vmcnt(15)\n\t" "ds_write_b128 v20, a[96:99] offset:8192\n\t" \
   "s_mov_b32 s41, 2359296\n\t" "buffer_load_dwordx4 a[152:155], v16, %[rb], s41 offen\n\t" \
   "v_mfma_f32_32x32x16_bf16 v[48:63], a[24:27], v[64:67], v[48:63]\n\t" "s_waitcnt vmcnt(15)\n\t" "ds_write_b128 v20, a[100:103] offset:12288\n\t" \
   "s_mov_b32 s41, 2490368\n\t" "buffer_load_dwordx4 a[156:159], v16, %[rb], s41 offen\n\t" \
   "v_mfma_f32_32x32x16_bf16 v[48:63], a[28:31], v[64:67], v[48:63]\n\t" "s_waitcnt vmcnt(15)\n\t" "ds_write_b128 v20, a[112:115] offset:16384\n\t" \
   "s_mov_b32 s41, 2621440\n\t" "buffer_load_dwordx4 a[160:163], v16, %[ra], s41 offen\n\t" \
   "v_mfma_f32_32x32x16_bf16 v[48:63], a[40:43], v[64:67], v[48:63]\n\t" "s_waitcnt vmcnt(15)\n\t" "ds_write_b128 v20, a[116:119] offset:20480\n\t" \
   "s_mov_b32 s41, 2752512\n\t" "buffer_load_dwordx4 a[164:167], v16, %[ra], s41 offen\n\t" \
   "v_mfma_f32_32x32x16_bf16 v[48:63], a[44:47], v[64:67], v[48:63]\n\t" "s_waitcnt vmcnt(15)\n\t" "ds_write_b128 v20, a[128:131] offset:24576\n\t" \
   "s_mov_b32 s41, 2883584\n\t" "buffer_load_dwordx4 a[176:179], v16, %[ra], s41 offen\n\t" \
   "v_mfma_f32_32x32x16_bf16 v[48:63], a[56:59], v[64:67], v[48:63]\n\t" "s_waitcnt vmcnt(15)\n\t" "ds_write_b128 v20, a[132:135] offset:28672\n\t" \
   "s_mov_b32 s41, 3014656\n\t" "buffer_load_dwordx4 a[180:183], v16, %[ra], s41 offen\n\t" \
   "v_mfma_f32_32x32x16_bf16 v[48:63], a[60:63], v[64:67], v[48:63]\n\t" "s_waitcnt vmcnt(15)\n\t" "ds_write_b128 v20, a[144:147] offset:32768\n\t" \
   "s_mov_b32 s41, 3145728\n\t" "buffer_load_dwordx4 a[192:195], v16, %[ra], s41 offen\n\t" \
   "v_mfma_f32_32x32x16_bf16 v[48:63], a[72:75], v[64:67], v[48:63]\n\t" "s_waitcnt vmcnt(15)\n\t" "ds_write_b128 v20, a[148:151] offset:36864\n\t" \
   "s_mov_b32 s41, 3276800\n\t" "buffer_load_dwordx4 a[196:199], v16, %[ra], s41 offen\n\t" \
   "v_mfma_f32_32x32x16_bf16 v[48:63], a[76:79], v[64:67], v[48:63]\n\t" "s_waitcnt lgkmcnt(0)\n\t" "s_barrier\n\t" LDSTAMP(4) \
   "ds_read_b64_tr_b16 a[80:81], v18 offset:0\n\t" "ds_read_b64_tr_b16 a[82:83], v18 offset:4096\n\t" "ds_read_b64_tr_b16 a[84:85], v19 offset:0\n\t" \
   "ds_read_b64_tr_b16 a[86:87], v19 offset:4096\n\t" "ds_read_b64_tr_b16 a[96:97], v18 offset:8192\n\t" \
   "ds_read_b64_tr_b16 a[98:99], v18 offset:12288\n\t" "ds_read_b64_tr_b16 a[100:101], v19 offset:8192\n\t" \
   "ds_read_b64_tr_b16 a[102:103], v19 offset:12288\n\t" "ds_read_b64_tr_b16 a[112:113], v18 offset:16384\n\t" \
   "ds_read_b64_tr_b16 a[114:115], v18 offset:20480\n\t" "ds_read_b64_tr_b16 a[116:117], v19 offset:16384\n\t" \
   "ds_read_b64_tr_b16 a[118:119], v19 offset:20480\n\t" "ds_read_b64_tr_b16 a[128:129], v18 offset:24576\n\t" \
   "ds_read_b64_tr_b16 a[130:131], v18 offset:28672\n\t" "ds_read_b64_tr_b16 a[132:133], v19 offset:24576\n\t" \
   "ds_read_b64_tr_b16 a[134:135], v19 offset:28672\n\t" "ds_read_b64_tr_b16 a[144:145], v18 offset:32768\n\t" \
   "ds_read_b64_tr_b16 a[146:147], v18 offset:36864\n\t" "ds_read_b64_tr_b16 a[148:149], v19 offset:32768\n\t" \
   "ds_read_b64_tr_b16 a[150:151], v19 offset:36864\n\t" "s_waitcnt vmcnt(15)\n\t" "s_waitcnt lgkmcnt(0)\n\t" "s_barrier\n\t" \
   "ds_write_b128 v20, a[88:91] offset:0\n\t" "s_mov_b32 s41, 3407872\n\t" "buffer_load_dwordx4 a[208:211], v16, %[ra], s41 offen\n\t" \
   "v_mfma_f32_32x32x16_bf16 v[32:47], a[80:83], v[64:67], v[32:47]\n\t" "s_waitcnt vmcnt(15)\n\t" "ds_write_b128 v20, a[92:95] offset:4096\n\t" \
   "s_mov_b32 s41, 3538944\n\t" "buffer_load_dwordx4 a[212:215], v16, %[ra], s41 offen\n\t" \
   "v_mfma_f32_32x32x16_bf16 v[32:47], a[84:87], v[64:67], v[32:47]\n\t" "s_waitcnt vmcnt(15)\n\t" "ds_write_b128 v20, a[104:107] offset:8192\n\t" \
   "s_mov_b32 s41, 3670016\n\t" "buffer_load_dwordx4 a[224:227], v16, %[ra], s41 offen\n\t" \
   "v_mfma_f32_32x32x16_bf16 v[32:47], a[96:99], v[64:67], v[32:47]\n\t" "s_waitcnt vmcnt(15)\n\t" "ds_write_b128 v20, a[108:111] offset:12288\n\t" \
   "s_mov_b32 s41, 3801088\n\t" "buffer_load_dwordx4 a[228:231], v16, %[ra], s41 offen\n\t" \
   "v_mfma_f32_32x32x16_bf16 v[32:47], a[100:103], v[64:67], v[32:47]\n\t" "s_waitcnt vmcnt(15)\n\t" "ds_write_b128 v20, a[120:123] offset:16384\n\t" \
   "s_mov_b32 s41, 2621440\n\t" "buffer_load_dwordx4 a[168:171], v16, %[rb], s41 offen\n\t" \
   "v_mfma_f32_32x32x16_bf16 v[32:47], a[112:115], v[64:67], v[32:47]\n\t" "s_waitcnt vmcnt(15)\n\t" "ds_write_b128 v20, a[124:127] offset:20480\n\t" \
   "s_mov_b32 s41, 2752512\n\t" "buffer_load_dwordx4 a[172:175], v16, %[rb], s41 offen\n\t" \
   "v_mfma_f32_32x32x16_bf16 v[32:47], a[116:119], v[64:67], v[32:47]\n\t" "s_waitcnt vmcnt(15)\n\t" "ds_write_b128 v20, a[136:139] offset:24576\n\t" \
   "s_mov_b32 s41, 2883584\n\t" "buffer_load_dwordx4 a[184:187], v16, %[rb], s41 offen\n\t" \
   "v_mfma_f32_32x32x16_bf16 v[32:47], a[128:131], v[64:67], v[32:47]\n\t" "s_waitcnt vmcnt(15)\n\t" "ds_write_b128 v20, a[140:143] offset:28672\n\t" \
   "s_mov_b32 s41, 3014656\n\t" "buffer_load_dwordx4 a[188:191], v16, %[rb], s41 offen\n\t" \
   "v_mfma_f32_32x32x16_bf16 v[32:47], a[132:135], v[64:67], v[32:47]\n\t" "s_waitcnt vmcnt(15)\n\t" "ds_write_b128 v20, a[152:155] offset:32768\n\t" \
   "s_mov_b32 s41, 3145728\n\t" "buffer_load_dwordx4 a[200:203], v16, %[rb], s41 offen\n\t" \
   "v_mfma_f32_32x32x16_bf16 v[32:47], a[144:147], v[64:67], v[32:47]\n\t" "s_waitcnt vmcnt(15)\n\t" "ds_write_b128 v20, a[156:159] offset:36864\n\t" \
   "s_mov_b32 s41, 3276800\n\t" "buffer_load_dwordx4 a[204:207], v16, %[rb], s41 offen\n\t" \
   "v_mfma_f32_32x32x16_bf16 v[32:47], a[148:151], v[64:67], v[32:47]\n\t" "s_waitcnt lgkmcnt(0)\n\t" "s_barrier\n\t" LDSTAMP(5) \
   "ds_read_b64_tr_b16 a[88:89], v18 offset:0\n\t" "ds_read_b64_tr_b16 a[90:91], v18 offset:4096\n\t" "ds_read_b64_tr_b16 a[92:93], v19 offset:0\n\t" \
   "ds_read_b64_tr_b16 a[94:95], v19 offset:4096\n\t" "ds_read_b64_tr_b16 a[104:105], v18 offset:8192\n\t" \
   "ds_read_b64_tr_b16 a[106:107], v18 offset:12288\n\t" "ds_read_b64_tr_b16 a[108:109], v19 offset:8192\n\t" \
   "ds_read_b64_tr_b16 a[110:111], v19 offset:12288\n\t" "ds_read_b64_tr_b16 a[120:121], v18 offset:16384\n\t" \
   "ds_read_b64_tr_b16 a[122:123], v18 offset:20480\n\t" "ds_read_b64_tr_b16 a[124:125], v19 offset:16384\n\t" \
   "ds_read_b64_tr_b16 a[126:127], v19 offset:20480\n\t" "ds_read_b64_tr_b16 a[136:137], v18 offset:24576\n\t" \
   "ds_read_b64_tr_b16 a[138:139], v18 offset:28672\n\t" "ds_read_b64_tr_b16 a[140:141], v19 offset:24576\n\t" \
   "ds_read_b64_tr_b16 a[142:143], v19 offset:28672\n\t" "ds_read_b64_tr_b16 a[152:153], v18 offset:32768\n\t" \
   "ds_read_b64_tr_b16 a[154:155], v18 offset:36864\n\t" "ds_read_b64_tr_b16 a[156:157], v19 offset:32768\n\t" \
   "ds_read_b64_tr_b16 a[158:159], v19 offset:36864\n\t" "s_waitcnt vmcnt(15)\n\t" "s_waitcnt lgkmcnt(0)\n\t" "s_barrier\n\t" \
   "ds_write_b128 v20, a[160:163] offset:0\n\t" "s_mov_b32 s41, 3407872\n\t" "buffer_load_dwordx4 a[216:219], v16, %[rb], s41 offen\n\t" \
   "v_mfma_f32_32x32x16_bf16 v[48:63], a[88:91], v[64:67], v[48:63]\n\t" "s_waitcnt vmcnt(15)\n\t" "ds_write_b128 v20, a[164:167] offset:4096\n\t" \
   "s_mov_b32 s41, 3538944\n\t" "buffer_load_dwordx4 a[220:223], v16, %[rb], s41 offen\n\t" \
   "v_mfma_f32_32x32x16_bf16 v[48:63], a[92:95], v[64:67], v[48:63]\n\t" "s_waitcnt vmcnt(15)\n\t" "ds_write_b128 v20, a[176:179] offset:8192\n\t" \
   "s_mov_b32 s41, 3670016\n\t" "buffer_load_dwordx4 a[232:235], v16, %[rb], s41 offen\n\t" \
   "v_mfma_f32_32x32x16_bf16 v[48:63], a[104:107], v[64:67], v[48:63]\n\t" "s_waitcnt vmcnt(15)\n\t" "ds_write_b128 v20, a[180:183] offset:12288\n\t" \
   "s_mov_b32 s41, 3801088\n\t" "buffer_load_dwordx4 a[236:239], v16, %[rb], s41 offen\n\t" \
   "v_mfma_f32_32x32x16_bf16 v[48:63], a[108:111], v[64:67], v[48:63]\n\t" "s_waitcnt vmcnt(15)\n\t" "ds_write_b128 v20, a[192:195] offset:16384\n\t" \
   "s_mov_b32 s41, 3932160\n\t" "buffer_load_dwordx4 a[240:243], v16, %[ra], s41 offen\n\t" \
   "v_mfma_f32_32x32x16_bf16 v[48:63], a[120:123], v[64:67], v[48:63]\n\t" "s_waitcnt vmcnt(15)\n\t" "ds_write_b128 v20, a[196:199] offset:20480\n\t" \
   "s_mov_b32 s41, 4063232\n\t" "buffer_load_dwordx4 a[244:247], v16, %[ra], s41 offen\n\t" \
   "v_mfma_f32_32x32x16_bf16 v[48:63], a[124:127], v[64:67], v[48:63]\n\t" "s_waitcnt vmcnt(15)\n\t" "ds_write_b128 v20, a[208:211] offset:24576\n\t" \
   "s_mov_b32 s41, 4194304\n\t" "buffer_load_dwordx4 v[192:195], v16, %[ra], s41 offen\n\t" \
   "v_mfma_f32_32x32x16_bf16 v[48:63], a[136:139], v[64:67], v[48:63]\n\t" "s_waitcnt vmcnt(15)\n\t" "ds_write_b128 v20, a[212:215] offset:28672\n\t" \
   "s_mov_b32 s41, 4325376\n\t" "buffer_load_dwordx4 v[196:199], v16, %[ra], s41 offen\n\t" \
   "v_mfma_f32_32x32x16_bf16 v[48:63], a[140:143], v[64:67], v[48:63]\n\t" "s_waitcnt vmcnt(15)\n\t" "ds_write_b128 v20, a[224:227] offset:32768\n\t" \
   "s_mov_b32 s41, 4456448\n\t" "buffer_load_dwordx4 v[208:211], v16, %[ra], s41 offen\n\t" \
   "v_mfma_f32_32x32x16_bf16 v[48:63], a[152:155], v[64:67], v[48:63]\n\t" "s_waitcnt vmcnt(15)\n\t" "ds_write_b128 v20, a[228:231] offset:36864\n\t" \
   "s_mov_b32 s41, 4587520\n\t" "buffer_load_dwordx4 v[212:215], v16, %[ra], s41 offen\n\t" \
   "v_mfma_f32_32x32x16_bf16 v[48:63], a[156:159], v[64:67], v[48:63]\n\t" "s_waitcnt lgkmcnt(0)\n\t" "s_barrier\n\t" LDSTAMP(6) \
   "ds_read_b64_tr_b16 a[160:161], v18 offset:0\n\t" "ds_read_b64_tr_b16 a[162:163], v18 offset:4096\n\t" \
   "ds_read_b64_tr_b16 a[164:165], v19 offset:0\n\t" "ds_read_b64_tr_b16 a[166:167], v19 offset:4096\n\t" \
   "ds_read_b64_tr_b16 a[176:177], v18 offset:8192\n\t" "ds_read_b64_tr_b16 a[178:179], v18 offset:12288\n\t" \
   "ds_read_b64_tr_b16 a[180:181], v19 offset:8192\n\t" "ds_read_b64_tr_b16 a[182:183], v19 offset:12288\n\t" \
   "ds_read_b64_tr_b16 a[192:193], v18 offset:16384\n\t" "ds_read_b64_tr_b16 a[194:195], v18 offset:20480\n\t" \
   "ds_read_b64_tr_b16 a[196:197], v19 offset:16384\n\t" "ds_read_b64_tr_b16 a[198:199], v19 offset:20480\n\t" \
   "ds_read_b64_tr_b16 a[208:209], v18 offset:24576\n\t" "ds_read_b64_tr_b16 a[210:211], v18 offset:28672\n\t" \
   "ds_read_b64_tr_b16 a[212:213], v19 offset:24576\n\t" "ds_read_b64_tr_b16 a[214:215], v19 offset:28672\n\t" \
   "ds_read_b64_tr_b16 a[224:225], v18 offset:32768\n\t" "ds_read_b64_tr_b16 a[226:227], v18 offset:36864\n\t" \
   "ds_read_b64_tr_b16 a[228:229], v19 offset:32768\n\t" "ds_read_b64_tr_b16 a[230:231], v19 offset:36864\n\t" "s_waitcnt vmcnt(15)\n\t" \
   "s_waitcnt lgkmcnt(0)\n\t" "s_barrier\n\t" "ds_write_b128 v20, a[168:171] offset:0\n\t" "s_mov_b32 s41, 4718592\n\t" \
   "buffer_load_dwordx4 v[224:227], v16, %[ra], s41 offen\n\t" "v_mfma_f32_32x32x16_bf16 v[32:47], a[160:163], v[64:67], v[32:47]\n\t" \
   "s_waitcnt vmcnt(15)\n\t" "ds_write_b128 v20, a[172:175] offset:4096\n\t" "s_mov_b32 s41, 4849664\n\t" \
   "buffer_load_dwordx4 v[228:231], v16, %[ra], s41 offen\n\t" "v_mfma_f32_32x32x16_bf16 v[32:47], a[164:167], v[64:67], v[32:47]\n\t" \
   "s_waitcnt vmcnt(15)\n\t" "ds_write_b128 v20, a[184:187] offset:8192\n\t" "s_mov_b32 s41, 4980736\n\t" \
   "buffer_load_dwordx4 v[240:243], v16, %[ra], s41 offen\n\t" "v_mfma_f32_32x32x16_bf16 v[32:47], a[176:179], v[64:67], v[32:47]\n\t" \
   "s_waitcnt vmcnt(15)\n\t" "ds_write_b128 v20, a[188:191] offset:12288\n\t" "s_mov_b32 s41, 5111808\n\t" \
   "buffer_load_dwordx4 v[244:247], v16, %[ra], s41 offen\n\t" "v_mfma_f32_32x32x16_bf16 v[32:47], a[180:183], v[64:67], v[32:47]\n\t" \
   "s_waitcnt vmcnt(15)\n\t" "ds_write_b128 v20, a[200:203] offset:16384\n\t" "s_mov_b32 s41, 3932160\n\t" \
   "buffer_load_dwordx4 a[248:251], v16, %[rb], s41 offen\n\t" "v_mfma_f32_32x32x16_bf16 v[32:47], a[192:195], v[64:67], v[32:47]\n\t" \
   "s_waitcnt vmcnt(15)\n\t" "ds_write_b128 v20, a[204:207] offset:20480\n\t" "s_mov_b32 s41, 4063232\n\t" \
   "buffer_load_dwordx4 a[252:255], v16, %[rb], s41 offen\n\t" "v_mfma_f32_32x32x16_bf16 v[32:47], a[196:199], v[64:67], v[32:47]\n\t" \
   "s_waitcnt vmcnt(15)\n\t" "ds_write_b128 v20, a[216:219] offset:24576\n\t" "s_mov_b32 s41, 4194304\n\t" \
   "buffer_load_dwordx4 v[200:203], v16, %[rb], s41 offen\n\t" "v_mfma_f32_32x32x16_bf16 v[32:47], a[208:211], v[64:67], v[32:47]\n\t" \
   "s_waitcnt vmcnt(15)\n\t" "ds_write_b128 v20, a[220:223] offset:28672\n\t" "s_mov_b32 s41, 4325376\n\t" \
   "buffer_load_dwordx4 v[204:207], v16, %[rb], s41 offen\n\t" "v_mfma_f32_32x32x16_bf16 v[32:47], a[212:215], v[64:67], v[32:47]\n\t" \
   "s_waitcnt vmcnt(15)\n\t" "ds_write_b128 v20, a[232:235] offset:32768\n\t" "s_mov_b32 s41, 4456448\n\t" \
   "buffer_load_dwordx4 v[216:219], v16, %[rb], s41 offen\n\t" "v_mfma_f32_32x32x16_bf16 v[32:47], a[224:227], v[64:67], v[32:47]\n\t" \
   "s_waitcnt vmcnt(15)\n\t" "ds_write_b128 v20, a[236:239] offset:36864\n\t" "s_mov_b32 s41, 4587520\n\t" \
   "buffer_load_dwordx4 v[220:223], v16, %[rb], s41 offen\n\t" "v_mfma_f32_32x32x16_bf16 v[32:47], a[228:231], v[64:67], v[32:47]\n\t" \
   "s_waitcnt lgkmcnt(0)\n\t" "s_barrier\n\t" LDSTAMP(7) "ds_read_b64_tr_b16 a[168:169], v18 offset:0\n\t" \
   "ds_read_b64_tr_b16 a[170:171], v18 offset:4096\n\t" "ds_read_b64_tr_b16 a[172:173], v19 offset:0\n\t" \
   "ds_read_b64_tr_b16 a[174:175], v19 offset:4096\n\t" "ds_read_b64_tr_b16 a[184:185], v18 offset:8192\n\t" \
   "ds_read_b64_tr_b16 a[186:187], v18 offset:12288\n\t" "ds_read_b64_tr_b16 a[188:189], v19 offset:8192\n\t" \
   "ds_read_b64_tr_b16 a[190:191], v19 offset:12288\n\t" "ds_read_b64_tr_b16 a[200:201], v18 offset:16384\n\t" \
   "ds_read_b64_tr_b16 a[202:203], v18 offset:20480\n\t" "ds_read_b64_tr_b16 a[204:205], v19 offset:16384\n\t" \
   "ds_read_b64_tr_b16 a[206:207], v19 offset:20480\n\t" "ds_read_b64_tr_b16 a[216:217], v18 offset:24576\n\t" \
   "ds_read_b64_tr_b16 a[218:219], v18 offset:28672\n\t" "ds_read_b64_tr_b16 a[220:221], v19 offset:24576\n\t" \
   "ds_read_b64_tr_b16 a[222:223], v19 offset:28672\n\t" "ds_read_b64_tr_b16 a[232:233], v18 offset:32768\n\t" \
   "ds_read_b64_tr_b16 a[234:235], v18 offset:36864\n\t" "ds_read_b64_tr_b16 a[236:237], v19 offset:32768\n\t" \
   "ds_read_b64_tr_b16 a[238:239], v19 offset:36864\n\t" "s_waitcnt vmcnt(15)\n\t" "s_waitcnt lgkmcnt(0)\n\t" "s_barrier\n\t" \
   "ds_write_b128 v20, a[240:243] offset:0\n\t" "s_mov_b32 s41, 4718592\n\t" "buffer_load_dwordx4 v[232:235], v16, %[rb], s41 offen\n\t" \
   "v_mfma_f32_32x32x16_bf16 v[48:63], a[168:171], v[64:67], v[48:63]\n\t" "s_waitcnt vmcnt(15)\n\t" "ds_write_b128 v20, a[244:247] offset:4096\n\t" \
   "s_mov_b32 s41, 4849664\n\t" "buffer_load_dwordx4 v[236:239], v16, %[rb], s41 offen\n\t" \
   "v_mfma_f32_32x32x16_bf16 v[48:63], a[172:175], v[64:67], v[48:63]\n\t" "s_waitcnt vmcnt(15)\n\t" "ds_write_b128 v20, v[192:195] offset:8192\n\t" \
   "s_mov_b32 s41, 4980736\n\t" "buffer_load_dwordx4 v[248:251], v16, %[rb], s41 offen\n\t" \
   "v_mfma_f32_32x32x16_bf16 v[48:63], a[184:187], v[64:67], v[48:63]\n\t" "s_waitcnt vmcnt(15)\n\t" "ds_write_b128 v20, v[196:199] offset:12288\n\t" \
   "s_mov_b32 s41, 5111808\n\t" "buffer_load_dwordx4 v[252:255], v16, %[rb], s41 offen\n\t" \
   "v_mfma_f32_32x32x16_bf16 v[48:63], a[188:191], v[64:67], v[48:63]\n\t" "s_waitcnt vmcnt(15)\n\t" "ds_write_b128 v20, v[208:211] offset:16384\n\t" \
   "v_mfma_f32_32x32x16_bf16 v[48:63], a[200:203], v[64:67], v[48:63]\n\t" "s_waitcnt vmcnt(14)\n\t" "ds_write_b128 v20, v[212:215] offset:20480\n\t" \
   "v_mfma_f32_32x32x16_bf16 v[48:63], a[204:207], v[64:67], v[48:63]\n\t" "s_waitcnt vmcnt(13)\n\t" "ds_write_b128 v20, v[224:227] offset:24576\n\t" \
   "v_mfma_f32_32x32x16_bf16 v[48:63], a[216:219], v[64:67], v[48:63]\n\t" "s_waitcnt vmcnt(12)\n\t" "ds_write_b128 v20, v[228:231] offset:28672\n\t" \
   "v_mfma_f32_32x32x16_bf16 v[48:63], a[220:223], v[64:67], v[48:63]\n\t" "s_waitcnt vmcnt(11)\n\t" "ds_write_b128 v20, v[240:243] offset:32768\n\t" \
   "v_mfma_f32_32x32x16_bf16 v[48:63], a[232:235], v[64:67], v[48:63]\n\t" "s_waitcnt vmcnt(10)\n\t" "ds_write_b128 v20, v[244:247] offset:36864\n\t" \
   "v_mfma_f32_32x32x16_bf16 v[48:63], a[236:239], v[64:67], v[48:63]\n\t" "buffer_load_dwordx4 v[28:31], v112, %[rw], 0 offen sc1\n\t" \
   "s_waitcnt lgkmcnt(0)\n\t" "s_barrier\n\t" LDSTAMP(8) "ds_read_b64_tr_b16 a[240:241], v18 offset:0\n\t" \
   "ds_read_b64_tr_b16 a[242:243], v18 offset:4096\n\t" "ds_read_b64_tr_b16 a[244:245], v19 offset:0\n\t" \
   "ds_read_b64_tr_b16 a[246:247], v19 offset:4096\n\t" "ds_read_b64_tr_b16 v[192:193], v18 offset:8192\n\t" \
   "ds_read_b64_tr_b16 v[194:195], v18 offset:12288\n\t" "ds_read_b64_tr_b16 v[196:197], v19 offset:8192\n\t" \
   "ds_read_b64_tr_b16 v[198:199], v19 offset:12288\n\t" "ds_read_b64_tr_b16 v[208:209], v18 offset:16384\n\t" \
   "ds_read_b64_tr_b16 v[210:211], v18 offset:20480\n\t" "ds_read_b64_tr_b16 v[212:213], v19 offset:16384\n\t" \
   "ds_read_b64_tr_b16 v[214:215], v19 offset:20480\n\t" "ds_read_b64_tr_b16 v[224:225], v18 offset:24576\n\t" \
   "ds_read_b64_tr_b16 v[226:227], v18 offset:28672\n\t" "ds_read_b64_tr_b16 v[228:229], v19 offset:24576\n\t" \
   "ds_read_b64_tr_b16 v[230:231], v19 offset:28672\n\t" "ds_read_b64_tr_b16 v[240:241], v18 offset:32768\n\t" \
   "ds_read_b64_tr_b16 v[242:243], v18 offset:36864\n\t" "ds_read_b64_tr_b16 v[244:245], v19 offset:32768\n\t" \
   "ds_read_b64_tr_b16 v[246:247], v19 offset:36864\n\t" "s_waitcnt vmcnt(10)\n\t" "s_waitcnt lgkmcnt(0)\n\t" "s_barrier\n\t" \
   "ds_write_b128 v20, a[248:251] offset:0\n\t" "v_mfma_f32_32x32x16_bf16 v[32:47], a[240:243], v[64:67], v[32:47]\n\t" "s_waitcnt vmcnt(9)\n\t" \
   "ds_write_b128 v20, a[252:255] offset:4096\n\t" "v_mfma_f32_32x32x16_bf16 v[32:47], a[244:247], v[64:67], v[32:47]\n\t" "s_waitcnt vmcnt(8)\n\t" \
   "ds_write_b128 v20, v[200:203] offset:8192\n\t" "v_mfma_f32_32x32x16_bf16 v[32:47], v[192:195], v[64:67], v[32:47]\n\t" "s_waitcnt vmcnt(7)\n\t" \
   "ds_write_b128 v20, v[204:207] offset:12288\n\t" "v_mfma_f32_32x32x16_bf16 v[32:47], v[196:199], v[64:67], v[32:47]\n\t" "s_waitcnt vmcnt(6)\n\t" \
   "ds_write_b128 v20, v[216:219] offset:16384\n\t" "v_mfma_f32_32x32x16_bf16 v[32:47], v[208:211], v[64:67], v[32:47]\n\t" "s_waitcnt vmcnt(5)\n\t" \
   "ds_write_b128 v20, v[220:223] offset:20480\n\t" "v_mfma_f32_32x32x16_bf16 v[32:47], v[212:215], v[64:67], v[32:47]\n\t" "s_waitcnt vmcnt(4)\n\t" \
   "ds_write_b128 v20, v[232:235] offset:24576\n\t" "v_mfma_f32_32x32x16_bf16 v[32:47], v[224:227], v[64:67], v[32:47]\n\t" "s_waitcnt vmcnt(3)\n\t" \
   "ds_write_b128 v20, v[236:239] offset:28672\n\t" "v_mfma_f32_32x32x16_bf16 v[32:47], v[228:231], v[64:67], v[32:47]\n\t" "s_waitcnt vmcnt(2)\n\t" \
   "ds_write_b128 v20, v[248:251] offset:32768\n\t" "v_mfma_f32_32x32x16_bf16 v[32:47], v[240:243], v[64:67], v[32:47]\n\t" "s_waitcnt vmcnt(1)\n\t" \
   "ds_write_b128 v20, v[252:255] offset:36864\n\t" "v_mfma_f32_32x32x16_bf16 v[32:47], v[244:247], v[64:67], v[32:47]\n\t" "s_waitcnt lgkmcnt(0)\n\t" \
   "s_barrier\n\t" LDSTAMP(9) "ds_read_b64_tr_b16 a[248:249], v18 offset:0\n\t" "ds_read_b64_tr_b16 a[250:251], v18 offset:4096\n\t" \
   "ds_read_b64_tr_b16 a[252:253], v19 offset:0\n\t" "ds_read_b64_tr_b16 a[254:255], v19 offset:4096\n\t" \
   "ds_read_b64_tr_b16 v[200:201], v18 offset:8192\n\t" "ds_read_b64_tr_b16 v[202:203], v18 offset:12288\n\t" \
   "ds_read_b64_tr_b16 v[204:205], v19 offset:8192\n\t" "ds_read_b64_tr_b16 v[206:207], v19 offset:12288\n\t" \
   "ds_read_b64_tr_b16 v[216:217], v18 offset:16384\n\t" "ds_read_b64_tr_b16 v[218:219], v18 offset:20480\n\t" \
   "ds_read_b64_tr_b16 v[220:221], v19 offset:16384\n\t" "ds_read_b64_tr_b16 v[222:223], v19 offset:20480\n\t" \
   "ds_read_b64_tr_b16 v[232:233], v18 offset:24576\n\t" "ds_read_b64_tr_b16 v[234:235], v18 offset:28672\n\t" \
   "ds_read_b64_tr_b16 v[236:237], v19 offset:24576\n\t" "ds_read_b64_tr_b16 v[238:239], v19 offset:28672\n\t" \
   "ds_read_b64_tr_b16 v[248:249], v18 offset:32768\n\t" "ds_read_b64_tr_b16 v[250:251], v18 offset:36864\n\t" \
   "ds_read_b64_tr_b16 v[252:253], v19 offset:32768\n\t" "ds_read_b64_tr_b16 v[254:255], v19 offset:36864\n\t" "s_waitcnt vmcnt(0) lgkmcnt(0)\n\t" \
   "v_mfma_f32_32x32x16_bf16 v[48:63], a[248:251], v[64:67], v[48:63]\n\t" "v_mfma_f32_32x32x16_bf16 v[48:63], a[252:255], v[64:67], v[48:63]\n\t" \
   "v_mfma_f32_32x32x16_bf16 v[48:63], v[200:203], v[64:67], v[48:63]\n\t" "v_mfma_f32_32x32x16_bf16 v[48:63], v[204:207], v[64:67], v[48:63]\n\t" \
   "v_mfma_f32_32x32x16_bf16 v[48:63], v[216:219], v[64:67], v[48:63]\n\t" "v_mfma_f32_32x32x16_bf16 v[48:63], v[220:223], v[64:67], v[48:63]\n\t" \
   "v_mfma_f32_32x32x16_bf16 v[48:63], v[232:235], v[64:67], v[48:63]\n\t" "v_mfma_f32_32x32x16_bf16 v[48:63], v[236:239], v[64:67], v[48:63]\n\t" \
   "v_mfma_f32_32x32x16_bf16 v[48:63], v[248:251], v[64:67], v[48:63]\n\t" "v_mfma_f32_32x32x16_bf16 v[48:63], v[252:255], v[64:67], v[48:63]\n\t" \
   "s_mov_b32 exec_lo, 1\n\t" "s_mov_b32 exec_hi, 1\n\t" "s_nop 7\n\t" "s_nop 3\n\t" "ds_write_b128 v27, v[32:35] offset:0\n\t" \
   "ds_write_b128 v27, v[36:39] offset:16\n\t" "ds_write_b128 v27, v[40:43] offset:32\n\t" "ds_write_b128 v27, v[44:47] offset:48\n\t" \
   "ds_write_b128 v27, v[48:51] offset:64\n\t" "ds_write_b128 v27, v[52:55] offset:80\n\t" "ds_write_b128 v27, v[56:59] offset:96\n\t" \
   "ds_write_b128 v27, v[60:63] offset:112\n\t" "s_mov_b64 exec, -1\n\t" "s_waitcnt lgkmcnt(0)\n\t" "s_barrier"
#define LDK_CLOB "v24", "v25", "v26", "v32", "v33", "v34", "v35", "v36", "v37", "v38", "v39", "v40", "v41", "v42", "v43", "v44", "v45", "v46", "v47", "v48", "v49", "v50", "v51", "v52", "v53", "v54", "v55", "v56", "v57", "v58", "v59", "v60", "v61", "v62", "v63", "v64", "v65", "v66", "v67", "v68", "v69", "v70", "v71", "v72", "v73", "v74", "v75", "v76", "v77", "v78", "v79", "v80", "v81", "v82", "v83", "v84", "v85", "v86", "v87", "v88", "v89", "v90", "v91", "v92", "v93", "v94", "v95", "v96", "v97", "v98", "v99", "v100", "v101", "v102", "v103", "v104", "v105", "v106", "v107", "v108", "v109", "v110", "v111", "v120", "v121", "v122", "v123", "s40", "s41"
#define LDV_ASM \
   "buffer_load_dword v26, v113, %[rw], 0 offen sc1\n\t" "s_mov_b32 s41, 0\n\t" "buffer_load_dwordx4 v[32:35], v116, %[rx], s41 offen\n\t" \
   "s_mov_b32 s41, 10240\n\t" "buffer_load_dwordx4 v[36:39], v116, %[rx], s41 offen\n\t" "s_mov_b32 s41, 128\n\t" \
   "buffer_load_dwordx4 v[40:43], v116, %[rx], s41 offen\n\t" "s_mov_b32 s41, 10368\n\t" "buffer_load_dwordx4 v[44:47], v116, %[rx], s41 offen\n\t" \
   "s_mov_b32 s41, 256\n\t" "buffer_load_dwordx4 v[48:51], v116, %[rx], s41 offen\n\t" "s_mov_b32 s41, 10496\n\t" \
   "buffer_load_dwordx4 v[52:55], v116, %[rx], s41 offen\n\t" "s_mov_b32 s41, 384\n\t" "buffer_load_dwordx4 v[56:59], v116, %[rx], s41 offen\n\t" \
   "s_mov_b32 s41, 10624\n\t" "buffer_load_dwordx4 v[60:63], v116, %[rx], s41 offen\n\t" "s_mov_b32 s41, 512\n\t" \
   "buffer_load_dwordx4 v[64:67], v116, %[rx], s41 offen\n\t" "s_mov_b32 s41, 10752\n\t" "buffer_load_dwordx4 v[68:71], v116, %[rx], s41 offen\n\t" \
   "s_mov_b32 s41, 640\n\t" "buffer_load_dwordx4 v[72:75], v116, %[rx], s41 offen\n\t" "s_mov_b32 s41, 10880\n\t" \
   "buffer_load_dwordx4 v[76:79], v116, %[rx], s41 offen\n\t" "s_mov_b32 s41, 768\n\t" "buffer_load_dwordx4 v[80:83], v116, %[rx], s41 offen\n\t" \
   "s_mov_b32 s41, 11008\n\t" "buffer_load_dwordx4 v[84:87], v116, %[rx], s41 offen\n\t" "s_mov_b32 s41, 896\n\t" \
   "buffer_load_dwordx4 v[88:91], v116, %[rx], s41 offen\n\t" "s_mov_b32 s41, 11136\n\t" "buffer_load_dwordx4 v[92:95], v116, %[rx], s41 offen\n\t" \
   "s_mov_b32 s40, 0x3f803f80\n\t" "v_mov_b32 v120, 0\n\t" "v_mov_b32 v121, 0\n\t" "v_mov_b32 v122, 0\n\t" "v_mov_b32 v123, 0\n\t" \
   "s_waitcnt vmcnt(16)\n\t" "v_add_u32 v26, 1, v26\n\t" "buffer_store_dword v26, v114, %[rw], 0 offen sc1\n\t" "ds_write_b32 v115, v26\n\t" LDSTAMP(0) \
   "s_waitcnt vmcnt(15)\n\t" LDSTAMP(1) "ds_write_b64 v117, v[32:33] offset:0\n\t" "ds_write_b64 v117, v[34:35] offset:256\n\t" \
   "s_mov_b32 s41, 1024\n\t" "buffer_load_dwordx4 v[96:99], v116, %[rx], s41 offen\n\t" "v_dot2c_f32_bf16 v120, s40, v32\n\t" \
   "v_dot2c_f32_bf16 v121, v32, v32\n\t" "v_dot2c_f32_bf16 v120, s40, v33\n\t" "v_dot2c_f32_bf16 v121, v33, v33\n\t" \
   "v_dot2c_f32_bf16 v120, s40, v34\n\t" "v_dot2c_f32_bf16 v121, v34, v34\n\t" "v_dot2c_f32_bf16 v120, s40, v35\n\t" \
   "v_dot2c_f32_bf16 v121, v35, v35\n\t" "s_waitcnt vmcnt(15)\n\t" "ds_write_b64 v117, v[36:37] offset:128\n\t" \
   "ds_write_b64 v117, v[38:39] offset:384\n\t" "s_mov_b32 s41, 11264\n\t" "buffer_load_dwordx4 v[100:103], v116, %[rx], s41 offen\n\t" \
   "v_dot2c_f32_bf16 v122, s40, v36\n\t" "v_dot2c_f32_bf16 v123, v36, v36\n\t" "v_dot2c_f32_bf16 v122, s40, v37\n\t" \
   "v_dot2c_f32_bf16 v123, v37, v37\n\t" "v_dot2c_f32_bf16 v122, s40, v38\n\t" "v_dot2c_f32_bf16 v123, v38, v38\n\t" \
   "v_dot2c_f32_bf16 v122, s40, v39\n\t" "v_dot2c_f32_bf16 v123, v39, v39\n\t" "s_waitcnt vmcnt(15)\n\t" "ds_write_b64 v117, v[40:41] offset:2048\n\t" \
   "ds_write_b64 v117, v[42:43] offset:2304\n\t" "s_mov_b32 s41, 1152\n\t" "buffer_load_dwordx4 v[104:107], v116, %[rx], s41 offen\n\t" \
   "v_dot2c_f32_bf16 v120, s40, v40\n\t" "v_dot2c_f32_bf16 v121, v40, v40\n\t" "v_dot2c_f32_bf16 v120, s40, v41\n\t" \
   "v_dot2c_f32_bf16 v121, v41, v41\n\t" "v_dot2c_f32_bf16 v120, s40, v42\n\t" "v_dot2c_f32_bf16 v121, v42, v42\n\t" \
   "v_dot2c_f32_bf16 v120, s40, v43\n\t" "v_dot2c_f32_bf16 v121, v43, v43\n\t" "s_waitcnt vmcnt(15)\n\t" "ds_write_b64 v117, v[44:45] offset:2176\n\t" \
   "ds_write_b64 v117, v[46:47] offset:2432\n\t" "s_mov_b32 s41, 11392\n\t" "buffer_load_dwordx4 v[108:111], v116, %[rx], s41 offen\n\t" \
   "v_dot2c_f32_bf16 v122, s40, v44\n\t" "v_dot2c_f32_bf16 v123, v44, v44\n\t" "v_dot2c_f32_bf16 v122, s40, v45\n\t" \
   "v_dot2c_f32_bf16 v123, v45, v45\n\t" "v_dot2c_f32_bf16 v122, s40, v46\n\t" "v_dot2c_f32_bf16 v123, v46, v46\n\t" \
   "v_dot2c_f32_bf16 v122, s40, v47\n\t" "v_dot2c_f32_bf16 v123, v47, v47\n\t" "s_waitcnt vmcnt(15)\n\t" "ds_write_b64 v117, v[48:49] offset:4096\n\t" \
   "ds_write_b64 v117, v[50:51] offset:4352\n\t" "s_mov_b32 s41, 0\n\t" "buffer_load_dwordx4 a[0:3], v16, %[ra], s41 offen\n\t" \
   "v_dot2c_f32_bf16 v120, s40, v48\n\t" "v_dot2c_f32_bf16 v121, v48, v48\n\t" "v_dot2c_f32_bf16 v120, s40, v49\n\t" \
   "v_dot2c_f32_bf16 v121, v49, v49\n\t" "v_dot2c_f32_bf16 v120, s40, v50\n\t" "v_dot2c_f32_bf16 v121, v50, v50\n\t" \
   "v_dot2c_f32_bf16 v120, s40, v51\n\t" "v_dot2c_f32_bf16 v121, v51, v51\n\t" "s_waitcnt vmcnt(15)\n\t" "ds_write_b64 v117, v[52:53] offset:4224\n\t" \
   "ds_write_b64 v117, v[54:55] offset:4480\n\t" "s_mov_b32 s41, 131072\n\t" "buffer_load_dwordx4 a[4:7], v16, %[ra], s41 offen\n\t" \
   "v_dot2c_f32_bf16 v122, s40, v52\n\t" "v_dot2c_f32_bf16 v123, v52, v52\n\t" "v_dot2c_f32_bf16 v122, s40, v53\n\t" \
   "v_dot2c_f32_bf16 v123, v53, v53\n\t" "v_dot2c_f32_bf16 v122, s40, v54\n\t" "v_dot2c_f32_bf16 v123, v54, v54\n\t" \
   "v_dot2c_f32_bf16 v122, s40, v55\n\t" "v_dot2c_f32_bf16 v123, v55, v55\n\t" "s_waitcnt vmcnt(15)\n\t" "ds_write_b64 v117, v[56:57] offset:6144\n\t" \
   "ds_write_b64 v117, v[58:59] offset:6400\n\t" "s_mov_b32 s41, 262144\n\t" "buffer_load_dwordx4 a[8:11], v16, %[ra], s41 offen\n\t" \
   "v_dot2c_f32_bf16 v120, s40, v56\n\t" "v_dot2c_f32_bf16 v121, v56, v56\n\t" "v_dot2c_f32_bf16 v120, s40, v57\n\t" \
   "v_dot2c_f32_bf16 v121, v57, v57\n\t" "v_dot2c_f32_bf16 v120, s40, v58\n\t" "v_dot2c_f32_bf16 v121, v58, v58\n\t" \
   "v_dot2c_f32_bf16 v120, s40, v59\n\t" "v_dot2c_f32_bf16 v121, v59, v59\n\t" "s_waitcnt vmcnt(15)\n\t" "ds_write_b64 v117, v[60:61] offset:6272\n\t" \
   "ds_write_b64 v117, v[62:63] offset:6528\n\t" "s_mov_b32 s41, 393216\n\t" "buffer_load_dwordx4 a[12:15], v16, %[ra], s41 offen\n\t" \
   "v_dot2c_f32_bf16 v122, s40, v60\n\t" "v_dot2c_f32_bf16 v123, v60, v60\n\t" "v_dot2c_f32_bf16 v122, s40, v61\n\t" \
   "v_dot2c_f32_bf16 v123, v61, v61\n\t" "v_dot2c_f32_bf16 v122, s40, v62\n\t" "v_dot2c_f32_bf16 v123, v62, v62\n\t" \
   "v_dot2c_f32_bf16 v122, s40, v63\n\t" "v_dot2c_f32_bf16 v123, v63, v63\n\t" "s_waitcnt vmcnt(15)\n\t" "ds_write_b64 v117, v[64:65] offset:8192\n\t" \
   "ds_write_b64 v117, v[66:67] offset:8448\n\t" "s_mov_b32 s41, 524288\n\t" "buffer_load_dwordx4 a[16:19], v16, %[ra], s41 offen\n\t" \
   "v_dot2c_f32_bf16 v120, s40, v64\n\t" "v_dot2c_f32_bf16 v121, v64, v64\n\t" "v_dot2c_f32_bf16 v120, s40, v65\n\t" \
   "v_dot2c_f32_bf16 v121, v65, v65\n\t" "v_dot2c_f32_bf16 v120, s40, v66\n\t" "v_dot2c_f32_bf16 v121, v66, v66\n\t" \
   "v_dot2c_f32_bf16 v120, s40, v67\n\t" "v_dot2c_f32_bf16 v121, v67, v67\n\t" "s_waitcnt vmcnt(15)\n\t" "ds_write_b64 v117, v[68:69] offset:8320\n\t" \
   "ds_write_b64 v117, v[70:71] offset:8576\n\t" "s_mov_b32 s41, 655360\n\t" "buffer_load_dwordx4 a[20:23], v16, %[ra], s41 offen\n\t" \
   "v_dot2c_f32_bf16 v122, s40, v68\n\t" "v_dot2c_f32_bf16 v123, v68, v68\n\t" "v_dot2c_f32_bf16 v122, s40, v69\n\t" \
   "v_dot2c_f32_bf16 v123, v69, v69\n\t" "v_dot2c_f32_bf16 v122, s40, v70\n\t" "v_dot2c_f32_bf16 v123, v70, v70\n\t" \
   "v_dot2c_f32_bf16 v122, s40, v71\n\t" "v_dot2c_f32_bf16 v123, v71, v71\n\t" "s_waitcnt vmcnt(15)\n\t" "ds_write_b64 v117, v[72:73] offset:10240\n\t" \
   "ds_write_b64 v117, v[74:75] offset:10496\n\t" "s_mov_b32 s41, 786432\n\t" "buffer_load_dwordx4 a[24:27], v16, %[ra], s41 offen\n\t" \
   "v_dot2c_f32_bf16 v120, s40, v72\n\t" "v_dot2c_f32_bf16 v121, v72, v72\n\t" "v_dot2c_f32_bf16 v120, s40, v73\n\t" \
   "v_dot2c_f32_bf16 v121, v73, v73\n\t" "v_dot2c_f32_bf16 v120, s40, v74\n\t" "v_dot2c_f32_bf16 v121, v74, v74\n\t" \
   "v_dot2c_f32_bf16 v120, s40, v75\n\t" "v_dot2c_f32_bf16 v121, v75, v75\n\t" "s_waitcnt vmcnt(15)\n\t" "ds_write_b64 v117, v[76:77] offset:10368\n\t" \
   "ds_write_b64 v117, v[78:79] offset:10624\n\t" "s_mov_b32 s41, 917504\n\t" "buffer_load_dwordx4 a[28:31], v16, %[ra], s41 offen\n\t" \
   "v_dot2c_f32_bf16 v122, s40, v76\n\t" "v_dot2c_f32_bf16 v123, v76, v76\n\t" "v_dot2c_f32_bf16 v122, s40, v77\n\t" \
   "v_dot2c_f32_bf16 v123, v77, v77\n\t" "v_dot2c_f32_bf16 v122, s40, v78\n\t" "v_dot2c_f32_bf16 v123, v78, v78\n\t" \
   "v_dot2c_f32_bf16 v122, s40, v79\n\t" "v_dot2c_f32_bf16 v123, v79, v79\n\t" "s_waitcnt vmcnt(15)\n\t" "ds_write_b64 v117, v[80:81] offset:12288\n\t" \
   "ds_write_b64 v117, v[82:83] offset:12544\n\t" "s_mov_b32 s41, 1048576\n\t" "buffer_load_dwordx4 a[32:35], v16, %[ra], s41 offen\n\t" \
   "v_dot2c_f32_bf16 v120, s40, v80\n\t" "v_dot2c_f32_bf16 v121, v80, v80\n\t" "v_dot2c_f32_bf16 v120, s40, v81\n\t" \
   "v_dot2c_f32_bf16 v121, v81, v81\n\t" "v_dot2c_f32_bf16 v120, s40, v82\n\t" "v_dot2c_f32_bf16 v121, v82, v82\n\t" \
   "v_dot2c_f32_bf16 v120, s40, v83\n\t" "v_dot2c_f32_bf16 v121, v83, v83\n\t" "s_waitcnt vmcnt(15)\n\t" "ds_write_b64 v117, v[84:85] offset:12416\n\t" \
   "ds_write_b64 v117, v[86:87] offset:12672\n\t" "s_mov_b32 s41, 1179648\n\t" "buffer_load_dwordx4 a[36:39], v16, %[ra], s41 offen\n\t" \
   "v_dot2c_f32_bf16 v122, s40, v84\n\t" "v_dot2c_f32_bf16 v123, v84, v84\n\t" "v_dot2c_f32_bf16 v122, s40, v85\n\t" \
   "v_dot2c_f32_bf16 v123, v85, v85\n\t" "v_dot2c_f32_bf16 v122, s40, v86\n\t" "v_dot2c_f32_bf16 v123, v86, v86\n\t" \
   "v_dot2c_f32_bf16 v122, s40, v87\n\t" "v_dot2c_f32_bf16 v123, v87, v87\n\t" "s_waitcnt vmcnt(15)\n\t" "ds_write_b64 v117, v[88:89] offset:14336\n\t" \
   "ds_write_b64 v117, v[90:91] offset:14592\n\t" "s_mov_b32 s41, 1310720\n\t" "buffer_load_dwordx4 a[40:43], v16, %[ra], s41 offen\n\t" \
   "v_dot2c_f32_bf16 v120, s40, v88\n\t" "v_dot2c_f32_bf16 v121, v88, v88\n\t" "v_dot2c_f32_bf16 v120, s40, v89\n\t" \
   "v_dot2c_f32_bf16 v121, v89, v89\n\t" "v_dot2c_f32_bf16 v120, s40, v90\n\t" "v_dot2c_f32_bf16 v121, v90, v90\n\t" \
   "v_dot2c_f32_bf16 v120, s40, v91\n\t" "v_dot2c_f32_bf16 v121, v91, v91\n\t" "s_waitcnt vmcnt(15)\n\t" "ds_write_b64 v117, v[92:93] offset:14464\n\t" \
   "ds_write_b64 v117, v[94:95] offset:14720\n\t" "s_mov_b32 s41, 1441792\n\t" "buffer_load_dwordx4 a[44:47], v16, %[ra], s41 offen\n\t" \
   "v_dot2c_f32_bf16 v122, s40, v92\n\t" "v_dot2c_f32_bf16 v123, v92, v92\n\t" "v_dot2c_f32_bf16 v122, s40, v93\n\t" \
   "v_dot2c_f32_bf16 v123, v93, v93\n\t" "v_dot2c_f32_bf16 v122, s40, v94\n\t" "v_dot2c_f32_bf16 v123, v94, v94\n\t" \
   "v_dot2c_f32_bf16 v122, s40, v95\n\t" "v_dot2c_f32_bf16 v123, v95, v95\n\t" "s_waitcnt vmcnt(15)\n\t" "ds_write_b64 v117, v[96:97] offset:16384\n\t" \
   "ds_write_b64 v117, v[98:99] offset:16640\n\t" "s_mov_b32 s41, 1572864\n\t" "buffer_load_dwordx4 a[48:51], v16, %[ra], s41 offen\n\t" \
   "v_dot2c_f32_bf16 v120, s40, v96\n\t" "v_dot2c_f32_bf16 v121, v96, v96\n\t" "v_dot2c_f32_bf16 v120, s40, v97\n\t" \
   "v_dot2c_f32_bf16 v121, v97, v97\n\t" "v_dot2c_f32_bf16 v120, s40, v98\n\t" "v_dot2c_f32_bf16 v121, v98, v98\n\t" \
   "v_dot2c_f32_bf16 v120, s40, v99\n\t" "v_dot2c_f32_bf16 v121, v99, v99\n\t" "s_waitcnt vmcnt(15)\n\t" \
   "ds_write_b64 v117, v[100:101] offset:16512\n\t" "ds_write_b64 v117, v[102:103] offset:16768\n\t" "s_mov_b32 s41, 1703936\n\t" \
   "buffer_load_dwordx4 a[52:55], v16, %[ra], s41 offen\n\t" "v_dot2c_f32_bf16 v122, s40, v100\n\t" "v_dot2c_f32_bf16 v123, v100, v100\n\t" \
   "v_dot2c_f32_bf16 v122, s40, v101\n\t" "v_dot2c_f32_bf16 v123, v101, v101\n\t" "v_dot2c_f32_bf16 v122, s40, v102\n\t" \
   "v_dot2c_f32_bf16 v123, v102, v102\n\t" "v_dot2c_f32_bf16 v122, s40, v103\n\t" "v_dot2c_f32_bf16 v123, v103, v103\n\t" "s_waitcnt vmcnt(15)\n\t" \
   "ds_write_b64 v117, v[104:105] offset:18432\n\t" "ds_write_b64 v117, v[106:107] offset:18688\n\t" "s_mov_b32 s41, 1835008\n\t" \
   "buffer_load_dwordx4 a[56:59], v16, %[ra], s41 offen\n\t" "v_dot2c_f32_bf16 v120, s40, v104\n\t" "v_dot2c_f32_bf16 v121, v104, v104\n\t" \
   "v_dot2c_f32_bf16 v120, s40, v105\n\t" "v_dot2c_f32_bf16 v121, v105, v105\n\t" "v_dot2c_f32_bf16 v120, s40, v106\n\t" \
   "v_dot2c_f32_bf16 v121, v106, v106\n\t" "v_dot2c_f32_bf16 v120, s40, v107\n\t" "v_dot2c_f32_bf16 v121, v107, v107\n\t" "s_waitcnt vmcnt(15)\n\t" \
   "ds_write_b64 v117, v[108:109] offset:18560\n\t" "ds_write_b64 v117, v[110:111] offset:18816\n\t" "s_mov_b32 s41, 1966080\n\t" \
   "buffer_load_dwordx4 a[60:63], v16, %[ra], s41 offen\n\t" "v_dot2c_f32_bf16 v122, s40, v108\n\t" "v_dot2c_f32_bf16 v123, v108, v108\n\t" \
   "v_dot2c_f32_bf16 v122, s40, v109\n\t" "v_dot2c_f32_bf16 v123, v109, v109\n\t" "v_dot2c_f32_bf16 v122, s40, v110\n\t" \
   "v_dot2c_f32_bf16 v123, v110, v110\n\t" "v_dot2c_f32_bf16 v122, s40, v111\n\t" "v_dot2c_f32_bf16 v123, v111, v111\n\t" "s_nop 3\n\t" \
   "v_add_f32_dpp v120, v120, v120 quad_perm:[1,0,3,2] row_mask:0xf bank_mask:0xf\n\t" \
   "v_add_f32_dpp v121, v121, v121 quad_perm:[1,0,3,2] row_mask:0xf bank_mask:0xf\n\t" \
   "v_add_f32_dpp v122, v122, v122 quad_perm:[1,0,3,2] row_mask:0xf bank_mask:0xf\n\t" \
   "v_add_f32_dpp v123, v123, v123 quad_perm:[1,0,3,2] row_mask:0xf bank_mask:0xf\n\t" \
   "v_add_f32_dpp v120, v120, v120 quad_perm:[2,3,0,1] row_mask:0xf bank_mask:0xf\n\t" \
   "v_add_f32_dpp v121, v121, v121 quad_perm:[2,3,0,1] row_mask:0xf bank_mask:0xf\n\t" \
   "v_add_f32_dpp v122, v122, v122 quad_perm:[2,3,0,1] row_mask:0xf bank_mask:0xf\n\t" \
   "v_add_f32_dpp v123, v123, v123 quad_perm:[2,3,0,1] row_mask:0xf bank_mask:0xf\n\t" \
   "v_add_f32_dpp v120, v120, v120 row_half_mirror row_mask:0xf bank_mask:0xf\n\t" \
   "v_add_f32_dpp v121, v121, v121 row_half_mirror row_mask:0xf bank_mask:0xf\n\t" \
   "v_add_f32_dpp v122, v122, v122 row_half_mirror row_mask:0xf bank_mask:0xf\n\t" \
   "v_add_f32_dpp v123, v123, v123 row_half_mirror row_mask:0xf bank_mask:0xf\n\t" "v_and_b32 v120, v119, v120\n\t" "v_and_b32 v121, v119, v121\n\t" \
   "v_and_b32 v122, v119, v122\n\t" "v_and_b32 v123, v119, v123\n\t" "ds_write_b64 v118, v[120:121]\n\t" "ds_write_b64 v118, v[122:123] offset:384\n\t" \
   "s_waitcnt vmcnt(15)\n\t" "ds_write_b128 v20, a[0:3] offset:0\n\t" "s_mov_b32 s41, 2097152\n\t" \
   "buffer_load_dwordx4 a[64:67], v16, %[ra], s41 offen\n\t" "s_waitcnt vmcnt(15)\n\t" "ds_write_b128 v20, a[4:7] offset:4096\n\t" \
   "s_mov_b32 s41, 2228224\n\t" "buffer_load_dwordx4 a[68:71], v16, %[ra], s41 offen\n\t" "s_waitcnt vmcnt(15)\n\t" \
   "ds_write_b128 v20, a[8:11] offset:8192\n\t" "s_mov_b32 s41, 2359296\n\t" "buffer_load_dwordx4 a[72:75], v16, %[ra], s41 offen\n\t" \
   "s_waitcnt vmcnt(15)\n\t" "ds_write_b128 v20, a[12:15] offset:12288\n\t" "s_mov_b32 s41, 2490368\n\t" \
   "buffer_load_dwordx4 a[76:79], v16, %[ra], s41 offen\n\t" "s_waitcnt vmcnt(15)\n\t" "ds_write_b128 v20, a[16:19] offset:16384\n\t" \
   "s_mov_b32 s41, 2621440\n\t" "buffer_load_dwordx4 a[80:83], v16, %[ra], s41 offen\n\t" "s_waitcnt vmcnt(15)\n\t" \
   "ds_write_b128 v20, a[20:23] offset:20480\n\t" "s_mov_b32 s41, 2752512\n\t" "buffer_load_dwordx4 a[84:87], v16, %[ra], s41 offen\n\t" \
   "s_waitcnt vmcnt(15)\n\t" "ds_write_b128 v20, a[24:27] offset:24576\n\t" "s_mov_b32 s41, 2883584\n\t" \
   "buffer_load_dwordx4 a[88:91], v16, %[ra], s41 offen\n\t" "s_waitcnt vmcnt(15)\n\t" "ds_write_b128 v20, a[28:31] offset:28672\n\t" \
   "s_mov_b32 s41, 3014656\n\t" "buffer_load_dwordx4 a[92:95], v16, %[ra], s41 offen\n\t" "s_waitcnt vmcnt(15)\n\t" \
   "ds_write_b128 v20, a[32:35] offset:32768\n\t" "s_mov_b32 s41, 3145728\n\t" "buffer_load_dwordx4 a[96:99], v16, %[ra], s41 offen\n\t" \
   "s_waitcnt vmcnt(15)\n\t" "ds_write_b128 v20, a[36:39] offset:36864\n\t" "s_mov_b32 s41, 3276800\n\t" \
   "buffer_load_dwordx4 a[100:103], v16, %[ra], s41 offen\n\t" "s_waitcnt lgkmcnt(0)\n\t" "s_barrier\n\t" LDSTAMP(2) \
   "ds_read_b64_tr_b16 a[0:1], v18 offset:0\n\t" "ds_read_b64_tr_b16 a[2:3], v18 offset:4096\n\t" "ds_read_b64_tr_b16 a[4:5], v19 offset:0\n\t" \
   "ds_read_b64_tr_b16 a[6:7], v19 offset:4096\n\t" "ds_read_b64_tr_b16 a[8:9], v18 offset:8192\n\t" \
   "ds_read_b64_tr_b16 a[10:11], v18 offset:12288\n\t" "ds_read_b64_tr_b16 a[12:13], v19 offset:8192\n\t" \
   "ds_read_b64_tr_b16 a[14:15], v19 offset:12288\n\t" "ds_read_b64_tr_b16 a[16:17], v18 offset:16384\n\t" \
   "ds_read_b64_tr_b16 a[18:19], v18 offset:20480\n\t" "ds_read_b64_tr_b16 a[20:21], v19 offset:16384\n\t" \
   "ds_read_b64_tr_b16 a[22:23], v19 offset:20480\n\t" "ds_read_b64_tr_b16 a[24:25], v18 offset:24576\n\t" \
   "ds_read_b64_tr_b16 a[26:27], v18 offset:28672\n\t" "ds_read_b64_tr_b16 a[28:29], v19 offset:24576\n\t" \
   "ds_read_b64_tr_b16 a[30:31], v19 offset:28672\n\t" "ds_read_b64_tr_b16 a[32:33], v18 offset:32768\n\t" \
   "ds_read_b64_tr_b16 a[34:35], v18 offset:36864\n\t" "ds_read_b64_tr_b16 a[36:37], v19 offset:32768\n\t" \
   "ds_read_b64_tr_b16 a[38:39], v19 offset:36864\n\t" "s_waitcnt vmcnt(15)\n\t" "s_waitcnt lgkmcnt(0)\n\t" "s_barrier\n\t" \
   "v_mov_b32 v64, 0x3f803f80\n\t" "v_mov_b32 v65, 0x3f803f80\n\t" "v_mov_b32 v66, 0x3f803f80\n\t" "v_mov_b32 v67, 0x3f803f80\n\t" \
   "ds_write_b128 v20, a[40:43] offset:0\n\t" "s_mov_b32 s41, 3407872\n\t" "buffer_load_dwordx4 a[104:107], v16, %[ra], s41 offen\n\t" \
   "v_mfma_f32_32x32x16_bf16 v[32:47], a[0:3], v[64:67], 0\n\t" "s_waitcnt vmcnt(15)\n\t" "ds_write_b128 v20, a[44:47] offset:4096\n\t" \
   "s_mov_b32 s41, 3538944\n\t" "buffer_load_dwordx4 a[108:111], v16, %[ra], s41 offen\n\t" \
   "v_mfma_f32_32x32x16_bf16 v[32:47], a[4:7], v[64:67], v[32:47]\n\t" "s_waitcnt vmcnt(15)\n\t" "ds_write_b128 v20, a[48:51] offset:8192\n\t" \
   "s_mov_b32 s41, 3670016\n\t" "buffer_load_dwordx4 a[112:115], v16, %[ra], s41 offen\n\t" \
   "v_mfma_f32_32x32x16_bf16 v[32:47], a[8:11], v[64:67], v[32:47]\n\t" "s_waitcnt vmcnt(15)\n\t" "ds_write_b128 v20, a[52:55] offset:12288\n\t" \
   "s_mov_b32 s41, 3801088\n\t" "buffer_load_dwordx4 a[116:119], v16, %[ra], s41 offen\n\t" \
   "v_mfma_f32_32x32x16_bf16 v[32:47], a[12:15], v[64:67], v[32:47]\n\t" "s_waitcnt vmcnt(15)\n\t" "ds_write_b128 v20, a[56:59] offset:16384\n\t" \
   "s_mov_b32 s41, 3932160\n\t" "buffer_load_dwordx4 a[120:123], v16, %[ra], s41 offen\n\t" \
   "v_mfma_f32_32x32x16_bf16 v[32:47], a[16:19], v[64:67], v[32:47]\n\t" "s_waitcnt vmcnt(15)\n\t" "ds_write_b128 v20, a[60:63] offset:20480\n\t" \
   "s_mov_b32 s41, 4063232\n\t" "buffer_load_dwordx4 a[124:127], v16, %[ra], s41 offen\n\t" \
   "v_mfma_f32_32x32x16_bf16 v[32:47], a[20:23], v[64:67], v[32:47]\n\t" "s_waitcnt vmcnt(15)\n\t" "ds_write_b128 v20, a[64:67] offset:24576\n\t" \
   "s_mov_b32 s41, 4194304\n\t" "buffer_load_dwordx4 a[128:131], v16, %[ra], s41 offen\n\t" \
   "v_mfma_f32_32x32x16_bf16 v[32:47], a[24:27], v[64:67], v[32:47]\n\t" "s_waitcnt vmcnt(15)\n\t" "ds_write_b128 v20, a[68:71] offset:28672\n\t" \
   "s_mov_b32 s41, 4325376\n\t" "buffer_load_dwordx4 a[132:135], v16, %[ra], s41 offen\n\t" \
   "v_mfma_f32_32x32x16_bf16 v[32:47], a[28:31], v[64:67], v[32:47]\n\t" "s_waitcnt vmcnt(15)\n\t" "ds_write_b128 v20, a[72:75] offset:32768\n\t" \
   "s_mov_b32 s41, 4456448\n\t" "buffer_load_dwordx4 a[136:139], v16, %[ra], s41 offen\n\t" \
   "v_mfma_f32_32x32x16_bf16 v[32:47], a[32:35], v[64:67], v[32:47]\n\t" "s_waitcnt vmcnt(15)\n\t" "ds_write_b128 v20, a[76:79] offset:36864\n\t" \
   "s_mov_b32 s41, 4587520\n\t" "buffer_load_dwordx4 a[140:143], v16, %[ra], s41 offen\n\t" \
   "v_mfma_f32_32x32x16_bf16 v[32:47], a[36:39], v[64:67], v[32:47]\n\t" "s_waitcnt lgkmcnt(0)\n\t" "s_barrier\n\t" LDSTAMP(3) \
   "ds_read_b64_tr_b16 a[40:41], v18 offset:0\n\t" "ds_read_b64_tr_b16 a[42:43], v18 offset:4096\n\t" "ds_read_b64_tr_b16 a[44:45], v19 offset:0\n\t" \
   "ds_read_b64_tr_b16 a[46:47], v19 offset:4096\n\t" "ds_read_b64_tr_b16 a[48:49], v18 offset:8192\n\t" \
   "ds_read_b64_tr_b16 a[50:51], v18 offset:12288\n\t" "ds_read_b64_tr_b16 a[52:53], v19 offset:8192\n\t" \
   "ds_read_b64_tr_b16 a[54:55], v19 offset:12288\n\t" "ds_read_b64_tr_b16 a[56:57], v18 offset:16384\n\t" \
   "ds_read_b64_tr_b16 a[58:59], v18 offset:20480\n\t" "ds_read_b64_tr_b16 a[60:61], v19 offset:16384\n\t" \
   "ds_read_b64_tr_b16 a[62:63], v19 offset:20480\n\t" "ds_read_b64_tr_b16 a[64:65], v18 offset:24576\n\t" \
   "ds_read_b64_tr_b16 a[66:67], v18 offset:28672\n\t" "ds_read_b64_tr_b16 a[68:69], v19 offset:24576\n\t" \
   "ds_read_b64_tr_b16 a[70:71], v19 offset:28672\n\t" "ds_read_b64_tr_b16 a[72:73], v18 offset:32768\n\t" \
   "ds_read_b64_tr_b16 a[74:75], v18 offset:36864\n\t" "ds_read_b64_tr_b16 a[76:77], v19 offset:32768\n\t" \
   "ds_read_b64_tr_b16 a[78:79], v19 offset:36864\n\t" "s_waitcnt vmcnt(15)\n\t" "s_waitcnt lgkmcnt(0)\n\t" "s_barrier\n\t" \
   "ds_write_b128 v20, a[80:83] offset:0\n\t" "s_mov_b32 s41, 4718592\n\t" "buffer_load_dwordx4 a[144:147], v16, %[ra], s41 offen\n\t" \
   "v_mfma_f32_32x32x16_bf16 v[32:47], a[40:43], v[64:67], v[32:47]\n\t" "s_waitcnt vmcnt(15)\n\t" "ds_write_b128 v20, a[84:87] offset:4096\n\t" \
   "s_mov_b32 s41, 4849664\n\t" "buffer_load_dwordx4 a[148:151], v16, %[ra], s41 offen\n\t" \
   "v_mfma_f32_32x32x16_bf16 v[32:47], a[44:47], v[64:67], v[32:47]\n\t" "s_waitcnt vmcnt(15)\n\t" "ds_write_b128 v20, a[88:91] offset:8192\n\t" \
   "s_mov_b32 s41, 4980736\n\t" "buffer_load_dwordx4 a[152:155], v16, %[ra], s41 offen\n\t" \
   "v_mfma_f32_32x32x16_bf16 v[32:47], a[48:51], v[64:67], v[32:47]\n\t" "s_waitcnt vmcnt(15)\n\t" "ds_write_b128 v20, a[92:95] offset:12288\n\t" \
   "s_mov_b32 s41, 5111808\n\t" "buffer_load_dwordx4 a[156:159], v16, %[ra], s41 offen\n\t" \
   "v_mfma_f32_32x32x16_bf16 v[32:47], a[52:55], v[64:67], v[32:47]\n\t" "s_waitcnt vmcnt(15)\n\t" "ds_write_b128 v20, a[96:99] offset:16384\n\t" \
   "s_mov_b32 s41, 0\n\t" "buffer_load_dwordx4 a[160:163], v17, %[rb], s41 offen\n\t" \
   "v_mfma_f32_32x32x16_bf16 v[32:47], a[56:59], v[64:67], v[32:47]\n\t" "s_waitcnt vmcnt(15)\n\t" "ds_write_b128 v20, a[100:103] offset:20480\n\t" \
   "s_mov_b32 s41, 256\n\t" "buffer_load_dwordx4 a[176:179], v17, %[rb], s41 offen\n\t" \
   "v_mfma_f32_32x32x16_bf16 v[32:47], a[60:63], v[64:67], v[32:47]\n\t" "s_waitcnt vmcnt(15)\n\t" "ds_write_b128 v20, a[104:107] offset:24576\n\t" \
   "s_mov_b32 s41, 512\n\t" "buffer_load_dwordx4 a[192:195], v17, %[rb], s41 offen\n\t" \
   "v_mfma_f32_32x32x16_bf16 v[32:47], a[64:67], v[64:67], v[32:47]\n\t" "s_waitcnt vmcnt(15)\n\t" "ds_write_b128 v20, a[108:111] offset:28672\n\t" \
   "s_mov_b32 s41, 768\n\t" "buffer_load_dwordx4 a[208:211], v17, %[rb], s41 offen\n\t" \
   "v_mfma_f32_32x32x16_bf16 v[32:47], a[68:71], v[64:67], v[32:47]\n\t" "s_waitcnt vmcnt(15)\n\t" "ds_write_b128 v20, a[112:115] offset:32768\n\t" \
   "s_mov_b32 s41, 1024\n\t" "buffer_load_dwordx4 a[224:227], v17, %[rb], s41 offen\n\t" \
   "v_mfma_f32_32x32x16_bf16 v[32:47], a[72:75], v[64:67], v[32:47]\n\t" "s_waitcnt vmcnt(15)\n\t" "ds_write_b128 v20, a[116:119] offset:36864\n\t" \
   "s_mov_b32 s41, 20480\n\t" "buffer_load_dwordx4 a[240:243], v17, %[rb], s41 offen\n\t" \
   "v_mfma_f32_32x32x16_bf16 v[32:47], a[76:79], v[64:67], v[32:47]\n\t" "s_waitcnt lgkmcnt(0)\n\t" "s_barrier\n\t" LDSTAMP(4) \
   "ds_read_b64_tr_b16 a[80:81], v18 offset:0\n\t" "ds_read_b64_tr_b16 a[82:83], v18 offset:4096\n\t" "ds_read_b64_tr_b16 a[84:85], v19 offset:0\n\t" \
   "ds_read_b64_tr_b16 a[86:87], v19 offset:4096\n\t" "ds_read_b64_tr_b16 a[88:89], v18 offset:8192\n\t" \
   "ds_read_b64_tr_b16 a[90:91], v18 offset:12288\n\t" "ds_read_b64_tr_b16 a[92:93], v19 offset:8192\n\t" \
   "ds_read_b64_tr_b16 a[94:95], v19 offset:12288\n\t" "ds_read_b64_tr_b16 a[96:97], v18 offset:16384\n\t" \
   "ds_read_b64_tr_b16 a[98:99], v18 offset:20480\n\t" "ds_read_b64_tr_b16 a[100:101], v19 offset:16384\n\t" \
   "ds_read_b64_tr_b16 a[102:103], v19 offset:20480\n\t" "ds_read_b64_tr_b16 a[104:105], v18 offset:24576\n\t" \
   "ds_read_b64_tr_b16 a[106:107], v18 offset:28672\n\t" "ds_read_b64_tr_b16 a[108:109], v19 offset:24576\n\t" \
   "ds_read_b64_tr_b16 a[110:111], v19 offset:28672\n\t" "ds_read_b64_tr_b16 a[112:113], v18 offset:32768\n\t" \
   "ds_read_b64_tr_b16 a[114:115], v18 offset:36864\n\t" "ds_read_b64_tr_b16 a[116:117], v19 offset:32768\n\t" \
   "ds_read_b64_tr_b16 a[118:119], v19 offset:36864\n\t" "s_waitcnt vmcnt(15)\n\t" "s_waitcnt lgkmcnt(0)\n\t" "s_barrier\n\t" \
   "ds_write_b128 v20, a[120:123] offset:0\n\t" "s_mov_b32 s41, 20736\n\t" "buffer_load_dwordx4 v[192:195], v17, %[rb], s41 offen\n\t" \
   "v_mfma_f32_32x32x16_bf16 v[32:47], a[80:83], v[64:67], v[32:47]\n\t" "s_waitcnt vmcnt(15)\n\t" "ds_write_b128 v20, a[124:127] offset:4096\n\t" \
   "s_mov_b32 s41, 20992\n\t" "buffer_load_dwordx4 v[208:211], v17, %[rb], s41 offen\n\t" \
   "v_mfma_f32_32x32x16_bf16 v[32:47], a[84:87], v[64:67], v[32:47]\n\t" "s_waitcnt vmcnt(15)\n\t" "ds_write_b128 v20, a[128:131] offset:8192\n\t" \
   "s_mov_b32 s41, 21248\n\t" "buffer_load_dwordx4 v[224:227], v17, %[rb], s41 offen\n\t" \
   "v_mfma_f32_32x32x16_bf16 v[32:47], a[88:91], v[64:67], v[32:47]\n\t" "s_waitcnt vmcnt(15)\n\t" "ds_write_b128 v20, a[132:135] offset:12288\n\t" \
   "s_mov_b32 s41, 21504\n\t" "buffer_load_dwordx4 v[240:243], v17, %[rb], s41 offen\n\t" \
   "v_mfma_f32_32x32x16_bf16 v[32:47], a[92:95], v[64:67], v[32:47]\n\t" "s_waitcnt vmcnt(15)\n\t" "ds_write_b128 v20, a[136:139] offset:16384\n\t" \
   "s_mov_b32 s41, 40960\n\t" "buffer_load_dwordx4 a[164:167], v17, %[rb], s41 offen\n\t" \
   "v_mfma_f32_32x32x16_bf16 v[32:47], a[96:99], v[64:67], v[32:47]\n\t" "s_waitcnt vmcnt(15)\n\t" "ds_write_b128 v20, a[140:143] offset:20480\n\t" \
   "s_mov_b32 s41, 41216\n\t" "buffer_load_dwordx4 a[180:183], v17, %[rb], s41 offen\n\t" \
   "v_mfma_f32_32x32x16_bf16 v[32:47], a[100:103], v[64:67], v[32:47]\n\t" "s_waitcnt vmcnt(15)\n\t" "ds_write_b128 v20, a[144:147] offset:24576\n\t" \
   "s_mov_b32 s41, 41472\n\t" "buffer_load_dwordx4 a[196:199], v17, %[rb], s41 offen\n\t" \
   "v_mfma_f32_32x32x16_bf16 v[32:47], a[104:107], v[64:67], v[32:47]\n\t" "s_waitcnt vmcnt(15)\n\t" "ds_write_b128 v20, a[148:151] offset:28672\n\t" \
   "s_mov_b32 s41, 41728\n\t" "buffer_load_dwordx4 a[212:215], v17, %[rb], s41 offen\n\t" \
   "v_mfma_f32_32x32x16_bf16 v[32:47], a[108:111], v[64:67], v[32:47]\n\t" "s_waitcnt vmcnt(15)\n\t" "ds_write_b128 v20, a[152:155] offset:32768\n\t" \
   "s_mov_b32 s41, 41984\n\t" "buffer_load_dwordx4 a[228:231], v17, %[rb], s41 offen\n\t" \
   "v_mfma_f32_32x32x16_bf16 v[32:47], a[112:115], v[64:67], v[32:47]\n\t" "s_waitcnt vmcnt(15)\n\t" "ds_write_b128 v20, a[156:159] offset:36864\n\t" \
   "s_mov_b32 s41, 61440\n\t" "buffer_load_dwordx4 a[244:247], v17, %[rb], s41 offen\n\t" \
   "v_mfma_f32_32x32x16_bf16 v[32:47], a[116:119], v[64:67], v[32:47]\n\t" "s_waitcnt lgkmcnt(0)\n\t" "s_barrier\n\t" LDSTAMP(5) \
   "ds_read_b64_tr_b16 a[120:121], v18 offset:0\n\t" "ds_read_b64_tr_b16 a[122:123], v18 offset:4096\n\t" \
   "ds_read_b64_tr_b16 a[124:125], v19 offset:0\n\t" "ds_read_b64_tr_b16 a[126:127], v19 offset:4096\n\t" \
   "ds_read_b64_tr_b16 a[128:129], v18 offset:8192\n\t" "ds_read_b64_tr_b16 a[130:131], v18 offset:12288\n\t" \
   "ds_read_b64_tr_b16 a[132:133], v19 offset:8192\n\t" "ds_read_b64_tr_b16 a[134:135], v19 offset:12288\n\t" \
   "ds_read_b64_tr_b16 a[136:137], v18 offset:16384\n\t" "ds_read_b64_tr_b16 a[138:139], v18 offset:20480\n\t" \
   "ds_read_b64_tr_b16 a[140:141], v19 offset:16384\n\t" "ds_read_b64_tr_b16 a[142:143], v19 offset:20480\n\t" \
   "ds_read_b64_tr_b16 a[144:145], v18 offset:24576\n\t" "ds_read_b64_tr_b16 a[146:147], v18 offset:28672\n\t" \
   "ds_read_b64_tr_b16 a[148:149], v19 offset:24576\n\t" "ds_read_b64_tr_b16 a[150:151], v19 offset:28672\n\t" \
   "ds_read_b64_tr_b16 a[152:153], v18 offset:32768\n\t" "ds_read_b64_tr_b16 a[154:155], v18 offset:36864\n\t" \
   "ds_read_b64_tr_b16 a[156:157], v19 offset:32768\n\t" "ds_read_b64_tr_b16 a[158:159], v19 offset:36864\n\t" "s_waitcnt vmcnt(15)\n\t" \
   "s_waitcnt lgkmcnt(0)\n\t" "s_barrier\n\t" "ds_write_b32 v21, a160 offset:0\n\t" "ds_write_b32 v21, a161 offset:21120\n\t" \
   "ds_write_b32 v21, a162 offset:10560\n\t" "ds_write_b32 v21, a163 offset:31680\n\t" "s_mov_b32 s41, 61696\n\t" \
   "buffer_load_dwordx4 v[196:199], v17, %[rb], s41 offen\n\t" "v_mfma_f32_32x32x16_bf16 v[32:47], a[120:123], v[64:67], v[32:47]\n\t" \
   "s_waitcnt vmcnt(15)\n\t" "ds_write_b32 v21, a176 offset:64\n\t" "ds_write_b32 v21, a177 offset:21184\n\t" "ds_write_b32 v21, a178 offset:10624\n\t" \
   "ds_write_b32 v21, a179 offset:31744\n\t" "s_mov_b32 s41, 61952\n\t" "buffer_load_dwordx4 v[212:215], v17, %[rb], s41 offen\n\t" \
   "v_mfma_f32_32x32x16_bf16 v[32:47], a[124:127], v[64:67], v[32:47]\n\t" "s_waitcnt vmcnt(15)\n\t" "ds_write_b32 v21, a192 offset:128\n\t" \
   "ds_write_b32 v21, a193 offset:21248\n\t" "ds_write_b32 v21, a194 offset:10688\n\t" "ds_write_b32 v21, a195 offset:31808\n\t" \
   "s_mov_b32 s41, 62208\n\t" "buffer_load_dwordx4 v[228:231], v17, %[rb], s41 offen\n\t" \
   "v_mfma_f32_32x32x16_bf16 v[32:47], a[128:131], v[64:67], v[32:47]\n\t" "s_waitcnt vmcnt(15)\n\t" "ds_write_b32 v21, a208 offset:192\n\t" \
   "ds_write_b32 v21, a209 offset:21312\n\t" "ds_write_b32 v21, a210 offset:10752\n\t" "ds_write_b32 v21, a211 offset:31872\n\t" \
   "s_mov_b32 s41, 62464\n\t" "buffer_load_dwordx4 v[244:247], v17, %[rb], s41 offen\n\t" \
   "v_mfma_f32_32x32x16_bf16 v[32:47], a[132:135], v[64:67], v[32:47]\n\t" "s_waitcnt vmcnt(15)\n\t" "ds_write_b32 v21, a224 offset:256\n\t" \
   "ds_write_b32 v21, a225 offset:21376\n\t" "ds_write_b32 v21, a226 offset:10816\n\t" "ds_write_b32 v21, a227 offset:31936\n\t" \
   "s_mov_b32 s41, 81920\n\t" "buffer_load_dwordx4 a[168:171], v17, %[rb], s41 offen\n\t" \
   "v_mfma_f32_32x32x16_bf16 v[32:47], a[136:139], v[64:67], v[32:47]\n\t" "s_waitcnt vmcnt(15)\n\t" "ds_write_b32 v21, a240 offset:5248\n\t" \
   "ds_write_b32 v21, a241 offset:26368\n\t" "ds_write_b32 v21, a242 offset:15808\n\t" "ds_write_b32 v21, a243 offset:36928\n\t" \
   "s_mov_b32 s41, 82176\n\t" "buffer_load_dwordx4 a[184:187], v17, %[rb], s41 offen\n\t" \
   "v_mfma_f32_32x32x16_bf16 v[32:47], a[140:143], v[64:67], v[32:47]\n\t" "s_waitcnt vmcnt(15)\n\t" "ds_write_b32 v21, v192 offset:5312\n\t" \
   "ds_write_b32 v21, v193 offset:26432\n\t" "ds_write_b32 v21, v194 offset:15872\n\t" "ds_write_b32 v21, v195 offset:36992\n\t" \
   "s_mov_b32 s41, 82432\n\t" "buffer_load_dwordx4 a[200:203], v17, %[rb], s41 offen\n\t" \
   "v_mfma_f32_32x32x16_bf16 v[32:47], a[144:147], v[64:67], v[32:47]\n\t" "s_waitcnt vmcnt(15)\n\t" "ds_write_b32 v21, v208 offset:5376\n\t" \
   "ds_write_b32 v21, v209 offset:26496\n\t" "ds_write_b32 v21, v210 offset:15936\n\t" "ds_write_b32 v21, v211 offset:37056\n\t" \
   "s_mov_b32 s41, 82688\n\t" "buffer_load_dwordx4 a[216:219], v17, %[rb], s41 offen\n\t" \
   "v_mfma_f32_32x32x16_bf16 v[32:47], a[148:151], v[64:67], v[32:47]\n\t" "s_waitcnt vmcnt(15)\n\t" "ds_write_b32 v21, v224 offset:5440\n\t" \
   "ds_write_b32 v21, v225 offset:26560\n\t" "ds_write_b32 v21, v226 offset:16000\n\t" "ds_write_b32 v21, v227 offset:37120\n\t" \
   "s_mov_b32 s41, 82944\n\t" "buffer_load_dwordx4 a[232:235], v17, %[rb], s41 offen\n\t" \
   "v_mfma_f32_32x32x16_bf16 v[32:47], a[152:155], v[64:67], v[32:47]\n\t" "s_waitcnt vmcnt(15)\n\t" "ds_write_b32 v21, v240 offset:5504\n\t" \
   "ds_write_b32 v21, v241 offset:26624\n\t" "ds_write_b32 v21, v242 offset:16064\n\t" "ds_write_b32 v21, v243 offset:37184\n\t" \
   "s_mov_b32 s41, 102400\n\t" "buffer_load_dwordx4 a[248:251], v17, %[rb], s41 offen\n\t" \
   "v_mfma_f32_32x32x16_bf16 v[32:47], a[156:159], v[64:67], v[32:47]\n\t" "s_waitcnt lgkmcnt(0)\n\t" "s_barrier\n\t" LDSTAMP(6) \
   "ds_read_b64_tr_b16 a[160:161], v22 offset:0\n\t" "ds_read_b64_tr_b16 a[162:163], v22 offset:2624\n\t" \
   "ds_read_b64_tr_b16 a[176:177], v22 offset:5248\n\t" "ds_read_b64_tr_b16 a[178:179], v22 offset:7872\n\t" \
   "ds_read_b64_tr_b16 a[192:193], v22 offset:16\n\t" "ds_read_b64_tr_b16 a[194:195], v22 offset:2640\n\t" \
   "ds_read_b64_tr_b16 a[208:209], v22 offset:5264\n\t" "ds_read_b64_tr_b16 a[210:211], v22 offset:7888\n\t" \
   "ds_read_b64_tr_b16 a[224:225], v22 offset:32\n\t" "ds_read_b64_tr_b16 a[226:227], v22 offset:2656\n\t" \
   "ds_read_b64_tr_b16 a[240:241], v22 offset:5280\n\t" "ds_read_b64_tr_b16 a[242:243], v22 offset:7904\n\t" \
   "ds_read_b64_tr_b16 v[192:193], v22 offset:48\n\t" "ds_read_b64_tr_b16 v[194:195], v22 offset:2672\n\t" \
   "ds_read_b64_tr_b16 v[208:209], v22 offset:5296\n\t" "ds_read_b64_tr_b16 v[210:211], v22 offset:7920\n\t" \
   "ds_read_b64_tr_b16 v[224:225], v22 offset:64\n\t" "ds_read_b64_tr_b16 v[226:227], v22 offset:2688\n\t" \
   "ds_read_b64_tr_b16 v[240:241], v22 offset:5312\n\t" "ds_read_b64_tr_b16 v[242:243], v22 offset:7936\n\t" "s_waitcnt vmcnt(15)\n\t" \
   "s_waitcnt lgkmcnt(0)\n\t" "s_barrier\n\t" "ds_write_b32 v21, a164 offset:0\n\t" "ds_write_b32 v21, a165 offset:21120\n\t" \
   "ds_write_b32 v21, a166 offset:10560\n\t" "ds_write_b32 v21, a167 offset:31680\n\t" "s_mov_b32 s41, 102656\n\t" \
   "buffer_load_dwordx4 v[200:203], v17, %[rb], s41 offen\n\t" "s_waitcnt vmcnt(15)\n\t" "ds_write_b32 v21, a180 offset:64\n\t" \
   "ds_write_b32 v21, a181 offset:21184\n\t" "ds_write_b32 v21, a182 offset:10624\n\t" "ds_write_b32 v21, a183 offset:31744\n\t" \
   "s_mov_b32 s41, 102912\n\t" "buffer_load_dwordx4 v[216:219], v17, %[rb], s41 offen\n\t" "s_waitcnt vmcnt(15)\n\t" \
   "ds_write_b32 v21, a196 offset:128\n\t" "ds_write_b32 v21, a197 offset:21248\n\t" "ds_write_b32 v21, a198 offset:10688\n\t" \
   "ds_write_b32 v21, a199 offset:31808\n\t" "s_mov_b32 s41, 103168\n\t" "buffer_load_dwordx4 v[232:235], v17, %[rb], s41 offen\n\t" \
   "s_waitcnt vmcnt(15)\n\t" "ds_write_b32 v21, a212 offset:192\n\t" "ds_write_b32 v21, a213 offset:21312\n\t" \
   "ds_write_b32 v21, a214 offset:10752\n\t" "ds_write_b32 v21, a215 offset:31872\n\t" "s_mov_b32 s41, 103424\n\t" \
   "buffer_load_dwordx4 v[248:251], v17, %[rb], s41 offen\n\t" "s_waitcnt vmcnt(15)\n\t" "ds_write_b32 v21, a228 offset:256\n\t" \
   "ds_write_b32 v21, a229 offset:21376\n\t" "ds_write_b32 v21, a230 offset:10816\n\t" "ds_write_b32 v21, a231 offset:31936\n\t" \
   "s_mov_b32 s41, 122880\n\t" "buffer_load_dwordx4 a[172:175], v17, %[rb], s41 offen\n\t" "s_waitcnt vmcnt(15)\n\t" \
   "ds_write_b32 v21, a244 offset:5248\n\t" "ds_write_b32 v21, a245 offset:26368\n\t" "ds_write_b32 v21, a246 offset:15808\n\t" \
   "ds_write_b32 v21, a247 offset:36928\n\t" "s_mov_b32 s41, 123136\n\t" "buffer_load_dwordx4 a[188:191], v17, %[rb], s41 offen\n\t" \
   "s_waitcnt vmcnt(15)\n\t" "ds_write_b32 v21, v196 offset:5312\n\t" "ds_write_b32 v21, v197 offset:26432\n\t" \
   "ds_write_b32 v21, v198 offset:15872\n\t" "ds_write_b32 v21, v199 offset:36992\n\t" "s_mov_b32 s41, 123392\n\t" \
   "buffer_load_dwordx4 a[204:207], v17, %[rb], s41 offen\n\t" "s_waitcnt vmcnt(15)\n\t" "ds_write_b32 v21, v212 offset:5376\n\t" \
   "ds_write_b32 v21, v213 offset:26496\n\t" "ds_write_b32 v21, v214 offset:15936\n\t" "ds_write_b32 v21, v215 offset:37056\n\t" \
   "s_mov_b32 s41, 123648\n\t" "buffer_load_dwordx4 a[220:223], v17, %[rb], s41 offen\n\t" "s_waitcnt vmcnt(15)\n\t" \
   "ds_write_b32 v21, v228 offset:5440\n\t" "ds_write_b32 v21, v229 offset:26560\n\t" "ds_write_b32 v21, v230 offset:16000\n\t" \
   "ds_write_b32 v21, v231 offset:37120\n\t" "s_mov_b32 s41, 123904\n\t" "buffer_load_dwordx4 a[236:239], v17, %[rb], s41 offen\n\t" \
   "s_waitcnt vmcnt(15)\n\t" "ds_write_b32 v21, v244 offset:5504\n\t" "ds_write_b32 v21, v245 offset:26624\n\t" \
   "ds_write_b32 v21, v246 offset:16064\n\t" "ds_write_b32 v21, v247 offset:37184\n\t" "s_mov_b32 s41, 143360\n\t" \
   "buffer_load_dwordx4 a[252:255], v17, %[rb], s41 offen\n\t" "s_waitcnt lgkmcnt(0)\n\t" "s_barrier\n\t" LDSTAMP(7) \
   "ds_read_b64_tr_b16 a[164:165], v22 offset:0\n\t" "ds_read_b64_tr_b16 a[166:167], v22 offset:2624\n\t" \
   "ds_read_b64_tr_b16 a[180:181], v22 offset:5248\n\t" "ds_read_b64_tr_b16 a[182:183], v22 offset:7872\n\t" \
   "ds_read_b64_tr_b16 a[196:197], v22 offset:16\n\t" "ds_read_b64_tr_b16 a[198:199], v22 offset:2640\n\t" \
   "ds_read_b64_tr_b16 a[212:213], v22 offset:5264\n\t" "ds_read_b64_tr_b16 a[214:215], v22 offset:7888\n\t" \
   "ds_read_b64_tr_b16 a[228:229], v22 offset:32\n\t" "ds_read_b64_tr_b16 a[230:231], v22 offset:2656\n\t" \
   "ds_read_b64_tr_b16 a[244:245], v22 offset:5280\n\t" "ds_read_b64_tr_b16 a[246:247], v22 offset:7904\n\t" \
   "ds_read_b64_tr_b16 v[196:197], v22 offset:48\n\t" "ds_read_b64_tr_b16 v[198:199], v22 offset:2672\n\t" \
   "ds_read_b64_tr_b16 v[212:213], v22 offset:5296\n\t" "ds_read_b64_tr_b16 v[214:215], v22 offset:7920\n\t" \
   "ds_read_b64_tr_b16 v[228:229], v22 offset:64\n\t" "ds_read_b64_tr_b16 v[230:231], v22 offset:2688\n\t" \
   "ds_read_b64_tr_b16 v[244:245], v22 offset:5312\n\t" "ds_read_b64_tr_b16 v[246:247], v22 offset:7936\n\t" "s_waitcnt vmcnt(15)\n\t" \
   "s_waitcnt lgkmcnt(0)\n\t" "s_barrier\n\t" "ds_write_b32 v21, a168 offset:0\n\t" "ds_write_b32 v21, a169 offset:21120\n\t" \
   "ds_write_b32 v21, a170 offset:10560\n\t" "ds_write_b32 v21, a171 offset:31680\n\t" "s_mov_b32 s41, 143616\n\t" \
   "buffer_load_dwordx4 v[204:207], v17, %[rb], s41 offen\n\t" "s_waitcnt vmcnt(15)\n\t" "ds_write_b32 v21, a184 offset:64\n\t" \
   "ds_write_b32 v21, a185 offset:21184\n\t" "ds_write_b32 v21, a186 offset:10624\n\t" "ds_write_b32 v21, a187 offset:31744\n\t" \
   "s_mov_b32 s41, 143872\n\t" "buffer_load_dwordx4 v[220:223], v17, %[rb], s41 offen\n\t" "s_waitcnt vmcnt(15)\n\t" \
   "ds_write_b32 v21, a200 offset:128\n\t" "ds_write_b32 v21, a201 offset:21248\n\t" "ds_write_b32 v21, a202 offset:10688\n\t" \
   "ds_write_b32 v21, a203 offset:31808\n\t" "s_mov_b32 s41, 144128\n\t" "buffer_load_dwordx4 v[236:239], v17, %[rb], s41 offen\n\t" \
   "s_waitcnt vmcnt(15)\n\t" "ds_write_b32 v21, a216 offset:192\n\t" "ds_write_b32 v21, a217 offset:21312\n\t" \
   "ds_write_b32 v21, a218 offset:10752\n\t" "ds_write_b32 v21, a219 offset:31872\n\t" "s_mov_b32 s41, 144384\n\t" \
   "buffer_load_dwordx4 v[252:255], v17, %[rb], s41 offen\n\t" "s_waitcnt vmcnt(15)\n\t" "ds_write_b32 v21, a232 offset:256\n\t" \
   "ds_write_b32 v21, a233 offset:21376\n\t" "ds_write_b32 v21, a234 offset:10816\n\t" "ds_write_b32 v21, a235 offset:31936\n\t" \
   "s_waitcnt vmcnt(14)\n\t" "ds_write_b32 v21, a248 offset:5248\n\t" "ds_write_b32 v21, a249 offset:26368\n\t" \
   "ds_write_b32 v21, a250 offset:15808\n\t" "ds_write_b32 v21, a251 offset:36928\n\t" "s_waitcnt vmcnt(13)\n\t" \
   "ds_write_b32 v21, v200 offset:5312\n\t" "ds_write_b32 v21, v201 offset:26432\n\t" "ds_write_b32 v21, v202 offset:15872\n\t" \
   "ds_write_b32 v21, v203 offset:36992\n\t" "s_waitcnt vmcnt(12)\n\t" "ds_write_b32 v21, v216 offset:5376\n\t" \
   "ds_write_b32 v21, v217 offset:26496\n\t" "ds_write_b32 v21, v218 offset:15936\n\t" "ds_write_b32 v21, v219 offset:37056\n\t" \
   "s_waitcnt vmcnt(11)\n\t" "ds_write_b32 v21, v232 offset:5440\n\t" "ds_write_b32 v21, v233 offset:26560\n\t" \
   "ds_write_b32 v21, v234 offset:16000\n\t" "ds_write_b32 v21, v235 offset:37120\n\t" "s_waitcnt vmcnt(10)\n\t" \
   "ds_write_b32 v21, v248 offset:5504\n\t" "ds_write_b32 v21, v249 offset:26624\n\t" "ds_write_b32 v21, v250 offset:16064\n\t" \
   "ds_write_b32 v21, v251 offset:37184\n\t" "buffer_load_dwordx4 v[28:31], v112, %[rw], 0 offen sc1\n\t" "s_waitcnt lgkmcnt(0)\n\t" "s_barrier\n\t" \
   LDSTAMP(8) "ds_read_b64_tr_b16 a[168:169], v22 offset:0\n\t" "ds_read_b64_tr_b16 a[170:171], v22 offset:2624\n\t" \
   "ds_read_b64_tr_b16 a[184:185], v22 offset:5248\n\t" "ds_read_b64_tr_b16 a[186:187], v22 offset:7872\n\t" \
   "ds_read_b64_tr_b16 a[200:201], v22 offset:16\n\t" "ds_read_b64_tr_b16 a[202:203], v22 offset:2640\n\t" \
   "ds_read_b64_tr_b16 a[216:217], v22 offset:5264\n\t" "ds_read_b64_tr_b16 a[218:219], v22 offset:7888\n\t" \
   "ds_read_b64_tr_b16 a[232:233], v22 offset:32\n\t" "ds_read_b64_tr_b16 a[234:235], v22 offset:2656\n\t" \
   "ds_read_b64_tr_b16 a[248:249], v22 offset:5280\n\t" "ds_read_b64_tr_b16 a[250:251], v22 offset:7904\n\t" \
   "ds_read_b64_tr_b16 v[200:201], v22 offset:48\n\t" "ds_read_b64_tr_b16 v[202:203], v22 offset:2672\n\t" \
   "ds_read_b64_tr_b16 v[216:217], v22 offset:5296\n\t" "ds_read_b64_tr_b16 v[218:219], v22 offset:7920\n\t" \
   "ds_read_b64_tr_b16 v[232:233], v22 offset:64\n\t" "ds_read_b64_tr_b16 v[234:235], v22 offset:2688\n\t" \
   "ds_read_b64_tr_b16 v[248:249], v22 offset:5312\n\t" "ds_read_b64_tr_b16 v[250:251], v22 offset:7936\n\t" "s_waitcnt vmcnt(10)\n\t" \
   "s_waitcnt lgkmcnt(0)\n\t" "s_barrier\n\t" "ds_write_b32 v21, a172 offset:0\n\t" "ds_write_b32 v21, a173 offset:21120\n\t" \
   "ds_write_b32 v21, a174 offset:10560\n\t" "ds_write_b32 v21, a175 offset:31680\n\t" "s_waitcnt vmcnt(9)\n\t" "ds_write_b32 v21, a188 offset:64\n\t" \
   "ds_write_b32 v21, a189 offset:21184\n\t" "ds_write_b32 v21, a190 offset:10624\n\t" "ds_write_b32 v21, a191 offset:31744\n\t" \
   "s_waitcnt vmcnt(8)\n\t" "ds_write_b32 v21, a204 offset:128\n\t" "ds_write_b32 v21, a205 offset:21248\n\t" "ds_write_b32 v21, a206 offset:10688\n\t" \
   "ds_write_b32 v21, a207 offset:31808\n\t" "s_waitcnt vmcnt(7)\n\t" "ds_write_b32 v21, a220 offset:192\n\t" "ds_write_b32 v21, a221 offset:21312\n\t" \
   "ds_write_b32 v21, a222 offset:10752\n\t" "ds_write_b32 v21, a223 offset:31872\n\t" "s_waitcnt vmcnt(6)\n\t" "ds_write_b32 v21, a236 offset:256\n\t" \
   "ds_write_b32 v21, a237 offset:21376\n\t" "ds_write_b32 v21, a238 offset:10816\n\t" "ds_write_b32 v21, a239 offset:31936\n\t" \
   "s_waitcnt vmcnt(5)\n\t" "ds_write_b32 v21, a252 offset:5248\n\t" "ds_write_b32 v21, a253 offset:26368\n\t" \
   "ds_write_b32 v21, a254 offset:15808\n\t" "ds_write_b32 v21, a255 offset:36928\n\t" "s_waitcnt vmcnt(4)\n\t" \
   "ds_write_b32 v21, v204 offset:5312\n\t" "ds_write_b32 v21, v205 offset:26432\n\t" "ds_write_b32 v21, v206 offset:15872\n\t" \
   "ds_write_b32 v21, v207 offset:36992\n\t" "s_waitcnt vmcnt(3)\n\t" "ds_write_b32 v21, v220 offset:5376\n\t" \
   "ds_write_b32 v21, v221 offset:26496\n\t" "ds_write_b32 v21, v222 offset:15936\n\t" "ds_write_b32 v21, v223 offset:37056\n\t" \
   "s_waitcnt vmcnt(2)\n\t" "ds_write_b32 v21, v236 offset:5440\n\t" "ds_write_b32 v21, v237 offset:26560\n\t" \
   "ds_write_b32 v21, v238 offset:16000\n\t" "ds_write_b32 v21, v239 offset:37120\n\t" "s_waitcnt vmcnt(1)\n\t" \
   "ds_write_b32 v21, v252 offset:5504\n\t" "ds_write_b32 v21, v253 offset:26624\n\t" "ds_write_b32 v21, v254 offset:16064\n\t" \
   "ds_write_b32 v21, v255 offset:37184\n\t" "s_waitcnt lgkmcnt(0)\n\t" "s_barrier\n\t" LDSTAMP(9) "ds_read_b64_tr_b16 a[172:173], v22 offset:0\n\t" \
   "ds_read_b64_tr_b16 a[174:175], v22 offset:2624\n\t" "ds_read_b64_tr_b16 a[188:189], v22 offset:5248\n\t" \
   "ds_read_b64_tr_b16 a[190:191], v22 offset:7872\n\t" "ds_read_b64_tr_b16 a[204:205], v22 offset:16\n\t" \
   "ds_read_b64_tr_b16 a[206:207], v22 offset:2640\n\t" "ds_read_b64_tr_b16 a[220:221], v22 offset:5264\n\t" \
   "ds_read_b64_tr_b16 a[222:223], v22 offset:7888\n\t" "ds_read_b64_tr_b16 a[236:237], v22 offset:32\n\t" \
   "ds_read_b64_tr_b16 a[238:239], v22 offset:2656\n\t" "ds_read_b64_tr_b16 a[252:253], v22 offset:5280\n\t" \
   "ds_read_b64_tr_b16 a[254:255], v22 offset:7904\n\t" "ds_read_b64_tr_b16 v[204:205], v22 offset:48\n\t" \
   "ds_read_b64_tr_b16 v[206:207], v22 offset:2672\n\t" "ds_read_b64_tr_b16 v[220:221], v22 offset:5296\n\t" \
   "ds_read_b64_tr_b16 v[222:223], v22 offset:7920\n\t" "ds_read_b64_tr_b16 v[236:237], v22 offset:64\n\t" \
   "ds_read_b64_tr_b16 v[238:239], v22 offset:2688\n\t" "ds_read_b64_tr_b16 v[252:253], v22 offset:5312\n\t" \
   "ds_read_b64_tr_b16 v[254:255], v22 offset:7936\n\t" "s_waitcnt vmcnt(0) lgkmcnt(0)\n\t" "s_mov_b32 exec_lo, 1\n\t" "s_mov_b32 exec_hi, 1\n\t" \
   "s_nop 7\n\t" "s_nop 3\n\t" "ds_write_b128 v27, v[32:35] offset:0\n\t" "ds_write_b128 v27, v[36:39] offset:16\n\t" \
   "ds_write_b128 v27, v[40:43] offset:32\n\t" "ds_write_b128 v27, v[44:47] offset:48\n\t" "ds_write_b128 v27, v[48:51] offset:64\n\t" \
   "ds_write_b128 v27, v[52:55] offset:80\n\t" "ds_write_b128 v27, v[56:59] offset:96\n\t" "ds_write_b128 v27, v[60:63] offset:112\n\t" \
   "s_mov_b64 exec, -1\n\t" "s_waitcnt lgkmcnt(0)\n\t" "s_barrier"
#define LDV_CLOB "v24", "v25", "v26", "v32", "v33", "v34", "v35", "v36", "v37", "v38", "v39", "v40", "v41", "v42", "v43", "v44", "v45", "v46", "v47", "v48", "v49", "v50", "v51", "v52", "v53", "v54", "v55", "v56", "v57", "v58", "v59", "v60", "v61", "v62", "v63", "v64", "v65", "v66", "v67", "v68", "v69", "v70", "v71", "v72", "v73", "v74", "v75", "v76", "v77", "v78", "v79", "v80", "v81", "v82", "v83", "v84", "v85", "v86", "v87", "v88", "v89", "v90", "v91", "v92", "v93", "v94", "v95", "v96", "v97", "v98", "v99", "v100", "v101", "v102", "v103", "v104", "v105", "v106", "v107", "v108", "v109", "v110", "v111", "v120", "v121", "v122", "v123", "s40", "s41"
#define LDY_ASM \
   "buffer_load_dword v26, v113, %[rw], 0 offen sc1\n\t" "s_mov_b32 s41, 0\n\t" "buffer_load_dwordx4 a[0:3], v16, %[ra], s41 offen\n\t" \
   "s_mov_b32 s41, 131072\n\t" "buffer_load_dwordx4 a[64:67], v16, %[ra], s41 offen\n\t" "s_mov_b32 s41, 524288\n\t" \
   "buffer_load_dwordx4 a[8:11], v16, %[ra], s41 offen\n\t" "s_mov_b32 s41, 655360\n\t" "buffer_load_dwordx4 a[72:75], v16, %[ra], s41 offen\n\t" \
   "s_mov_b32 s41, 1048576\n\t" "buffer_load_dwordx4 a[16:19], v16, %[ra], s41 offen\n\t" "s_mov_b32 s41, 1179648\n\t" \
   "buffer_load_dwordx4 a[80:83], v16, %[ra], s41 offen\n\t" "s_mov_b32 s41, 1572864\n\t" "buffer_load_dwordx4 a[24:27], v16, %[ra], s41 offen\n\t" \
   "s_mov_b32 s41, 1703936\n\t" "buffer_load_dwordx4 a[88:91], v16, %[ra], s41 offen\n\t" "s_mov_b32 s41, 2097152\n\t" \
   "buffer_load_dwordx4 a[32:35], v16, %[ra], s41 offen\n\t" "s_mov_b32 s41, 2228224\n\t" "buffer_load_dwordx4 a[96:99], v16, %[ra], s41 offen\n\t" \
   "s_mov_b32 s41, 2621440\n\t" "buffer_load_dwordx4 a[40:43], v16, %[ra], s41 offen\n\t" "s_mov_b32 s41, 2752512\n\t" \
   "buffer_load_dwordx4 a[104:107], v16, %[ra], s41 offen\n\t" "s_mov_b32 s41, 3145728\n\t" "buffer_load_dwordx4 a[48:51], v16, %[ra], s41 offen\n\t" \
   "s_mov_b32 s41, 3276800\n\t" "buffer_load_dwordx4 a[112:115], v16, %[ra], s41 offen\n\t" "s_mov_b32 s41, 3670016\n\t" \
   "buffer_load_dwordx4 a[56:59], v16, %[ra], s41 offen\n\t" "s_mov_b32 s41, 3801088\n\t" "buffer_load_dwordx4 a[120:123], v16, %[ra], s41 offen\n\t" \
   "s_waitcnt vmcnt(16)\n\t" "v_add_u32 v26, 1, v26\n\t" "buffer_store_dword v26, v114, %[rw], 0 offen sc1\n\t" "ds_write_b32 v115, v26\n\t" LDSTAMP(0) \
   "s_waitcnt vmcnt(15)\n\t" LDSTAMP(1) "ds_write_b128 v20, a[0:3] offset:0\n\t" "s_mov_b32 s41, 4194304\n\t" \
   "buffer_load_dwordx4 v[192:195], v16, %[ra], s41 offen\n\t" "s_waitcnt vmcnt(15)\n\t" "ds_write_b128 v20, a[64:67] offset:4096\n\t" \
   "s_mov_b32 s41, 4325376\n\t" "buffer_load_dwordx4 v[208:211], v16, %[ra], s41 offen\n\t" "s_waitcnt vmcnt(15)\n\t" \
   "ds_write_b128 v20, a[8:11] offset:8192\n\t" "s_mov_b32 s41, 4718592\n\t" "buffer_load_dwordx4 v[200:203], v16, %[ra], s41 offen\n\t" \
   "s_waitcnt vmcnt(15)\n\t" "ds_write_b128 v20, a[72:75] offset:12288\n\t" "s_mov_b32 s41, 4849664\n\t" \
   "buffer_load_dwordx4 v[216:219], v16, %[ra], s41 offen\n\t" "s_waitcnt vmcnt(15)\n\t" "ds_write_b128 v20, a[16:19] offset:16384\n\t" \
   "s_mov_b32 s41, 262144\n\t" "buffer_load_dwordx4 a[4:7], v16, %[ra], s41 offen\n\t" "s_waitcnt vmcnt(15)\n\t" \
   "ds_write_b128 v20, a[80:83] offset:20480\n\t" "s_mov_b32 s41, 393216\n\t" "buffer_load_dwordx4 a[68:71], v16, %[ra], s41 offen\n\t" \
   "s_waitcnt vmcnt(15)\n\t" "ds_write_b128 v20, a[24:27] offset:24576\n\t" "s_mov_b32 s41, 786432\n\t" \
   "buffer_load_dwordx4 a[12:15], v16, %[ra], s41 offen\n\t" "s_waitcnt vmcnt(15)\n\t" "ds_write_b128 v20, a[88:91] offset:28672\n\t" \
   "s_mov_b32 s41, 917504\n\t" "buffer_load_dwordx4 a[76:79], v16, %[ra], s41 offen\n\t" "s_waitcnt vmcnt(15)\n\t" \
   "ds_write_b128 v20, a[32:35] offset:32768\n\t" "s_mov_b32 s41, 1310720\n\t" "buffer_load_dwordx4 a[20:23], v16, %[ra], s41 offen\n\t" \
   "s_waitcnt vmcnt(15)\n\t" "ds_write_b128 v20, a[96:99] offset:36864\n\t" "s_mov_b32 s41, 1441792\n\t" \
   "buffer_load_dwordx4 a[84:87], v16, %[ra], s41 offen\n\t" "s_waitcnt vmcnt(15)\n\t" "ds_write_b128 v20, a[40:43] offset:40960\n\t" \
   "s_mov_b32 s41, 1835008\n\t" "buffer_load_dwordx4 a[28:31], v16, %[ra], s41 offen\n\t" "s_waitcnt vmcnt(15)\n\t" \
   "ds_write_b128 v20, a[104:107] offset:45056\n\t" "s_mov_b32 s41, 1966080\n\t" "buffer_load_dwordx4 a[92:95], v16, %[ra], s41 offen\n\t" \
   "s_waitcnt vmcnt(15)\n\t" "ds_write_b128 v20, a[48:51] offset:49152\n\t" "s_mov_b32 s41, 2359296\n\t" \
   "buffer_load_dwordx4 a[36:39], v16, %[ra], s41 offen\n\t" "s_waitcnt vmcnt(15)\n\t" "ds_write_b128 v20, a[112:115] offset:53248\n\t" \
   "s_mov_b32 s41, 2490368\n\t" "buffer_load_dwordx4 a[100:103], v16, %[ra], s41 offen\n\t" "s_waitcnt vmcnt(15)\n\t" \
   "ds_write_b128 v20, a[56:59] offset:57344\n\t" "s_mov_b32 s41, 2883584\n\t" "buffer_load_dwordx4 a[44:47], v16, %[ra], s41 offen\n\t" \
   "s_waitcnt vmcnt(15)\n\t" "ds_write_b128 v20, a[120:123] offset:61440\n\t" "s_mov_b32 s41, 3014656\n\t" \
   "buffer_load_dwordx4 a[108:111], v16, %[ra], s41 offen\n\t" "s_waitcnt vmcnt(15)\n\t" "ds_write_b128 v23, v[192:195] offset:0\n\t" \
   "s_mov_b32 s41, 3407872\n\t" "buffer_load_dwordx4 a[52:55], v16, %[ra], s41 offen\n\t" "s_waitcnt vmcnt(15)\n\t" \
   "ds_write_b128 v23, v[208:211] offset:4096\n\t" "s_mov_b32 s41, 3538944\n\t" "buffer_load_dwordx4 a[116:119], v16, %[ra], s41 offen\n\t" \
   "s_waitcnt vmcnt(15)\n\t" "ds_write_b128 v23, v[200:203] offset:8192\n\t" "s_mov_b32 s41, 3932160\n\t" \
   "buffer_load_dwordx4 a[60:63], v16, %[ra], s41 offen\n\t" "s_waitcnt vmcnt(15)\n\t" "ds_write_b128 v23, v[216:219] offset:12288\n\t" \
   "s_mov_b32 s41, 4063232\n\t" "buffer_load_dwordx4 a[124:127], v16, %[ra], s41 offen\n\t" "s_waitcnt lgkmcnt(0)\n\t" "s_barrier\n\t" LDSTAMP(2) \
   "s_lshl_b32 s40, %[p0], 13\n\t" "v_add_u32 v24, s40, v18\n\t" "v_add_u32 v25, s40, v19\n\t" "s_add_u32 s40, s40, 8192\n\t" \
   "s_cmp_eq_u32 s40, 81920\n\t" "s_cselect_b32 s40, 0, s40\n\t" "ds_read_b64_tr_b16 a[0:1], v24 offset:0\n\t" \
   "ds_read_b64_tr_b16 a[2:3], v24 offset:4096\n\t" "ds_read_b64_tr_b16 a[64:65], v25 offset:0\n\t" "ds_read_b64_tr_b16 a[66:67], v25 offset:4096\n\t" \
   "v_add_u32 v24, s40, v18\n\t" "v_add_u32 v25, s40, v19\n\t" "s_add_u32 s40, s40, 8192\n\t" "s_cmp_eq_u32 s40, 81920\n\t" \
   "s_cselect_b32 s40, 0, s40\n\t" "ds_read_b64_tr_b16 a[8:9], v24 offset:0\n\t" "ds_read_b64_tr_b16 a[10:11], v24 offset:4096\n\t" \
   "ds_read_b64_tr_b16 a[72:73], v25 offset:0\n\t" "ds_read_b64_tr_b16 a[74:75], v25 offset:4096\n\t" "v_add_u32 v24, s40, v18\n\t" \
   "v_add_u32 v25, s40, v19\n\t" "s_add_u32 s40, s40, 8192\n\t" "s_cmp_eq_u32 s40, 81920\n\t" "s_cselect_b32 s40, 0, s40\n\t" \
   "ds_read_b64_tr_b16 a[16:17], v24 offset:0\n\t" "ds_read_b64_tr_b16 a[18:19], v24 offset:4096\n\t" "ds_read_b64_tr_b16 a[80:81], v25 offset:0\n\t" \
   "ds_read_b64_tr_b16 a[82:83], v25 offset:4096\n\t" "v_add_u32 v24, s40, v18\n\t" "v_add_u32 v25, s40, v19\n\t" "s_add_u32 s40, s40, 8192\n\t" \
   "s_cmp_eq_u32 s40, 81920\n\t" "s_cselect_b32 s40, 0, s40\n\t" "ds_read_b64_tr_b16 a[24:25], v24 offset:0\n\t" \
   "ds_read_b64_tr_b16 a[26:27], v24 offset:4096\n\t" "ds_read_b64_tr_b16 a[88:89], v25 offset:0\n\t" \
   "ds_read_b64_tr_b16 a[90:91], v25 offset:4096\n\t" "v_add_u32 v24, s40, v18\n\t" "v_add_u32 v25, s40, v19\n\t" "s_add_u32 s40, s40, 8192\n\t" \
   "s_cmp_eq_u32 s40, 81920\n\t" "s_cselect_b32 s40, 0, s40\n\t" "ds_read_b64_tr_b16 a[32:33], v24 offset:0\n\t" \
   "ds_read_b64_tr_b16 a[34:35], v24 offset:4096\n\t" "ds_read_b64_tr_b16 a[96:97], v25 offset:0\n\t" \
   "ds_read_b64_tr_b16 a[98:99], v25 offset:4096\n\t" "v_add_u32 v24, s40, v18\n\t" "v_add_u32 v25, s40, v19\n\t" "s_add_u32 s40, s40, 8192\n\t" \
   "s_cmp_eq_u32 s40, 81920\n\t" "s_cselect_b32 s40, 0, s40\n\t" "ds_read_b64_tr_b16 a[40:41], v24 offset:0\n\t" \
   "ds_read_b64_tr_b16 a[42:43], v24 offset:4096\n\t" "ds_read_b64_tr_b16 a[104:105], v25 offset:0\n\t" \
   "ds_read_b64_tr_b16 a[106:107], v25 offset:4096\n\t" "v_add_u32 v24, s40, v18\n\t" "v_add_u32 v25, s40, v19\n\t" "s_add_u32 s40, s40, 8192\n\t" \
   "s_cmp_eq_u32 s40, 81920\n\t" "s_cselect_b32 s40, 0, s40\n\t" "ds_read_b64_tr_b16 a[48:49], v24 offset:0\n\t" \
   "ds_read_b64_tr_b16 a[50:51], v24 offset:4096\n\t" "ds_read_b64_tr_b16 a[112:113], v25 offset:0\n\t" \
   "ds_read_b64_tr_b16 a[114:115], v25 offset:4096\n\t" "v_add_u32 v24, s40, v18\n\t" "v_add_u32 v25, s40, v19\n\t" "s_add_u32 s40, s40, 8192\n\t" \
   "s_cmp_eq_u32 s40, 81920\n\t" "s_cselect_b32 s40, 0, s40\n\t" "ds_read_b64_tr_b16 a[56:57], v24 offset:0\n\t" \
   "ds_read_b64_tr_b16 a[58:59], v24 offset:4096\n\t" "ds_read_b64_tr_b16 a[120:121], v25 offset:0\n\t" \
   "ds_read_b64_tr_b16 a[122:123], v25 offset:4096\n\t" "v_add_u32 v24, s40, v18\n\t" "v_add_u32 v25, s40, v19\n\t" "s_add_u32 s40, s40, 8192\n\t" \
   "s_cmp_eq_u32 s40, 81920\n\t" "s_cselect_b32 s40, 0, s40\n\t" "ds_read_b64_tr_b16 v[192:193], v24 offset:0\n\t" \
   "ds_read_b64_tr_b16 v[194:195], v24 offset:4096\n\t" "ds_read_b64_tr_b16 v[208:209], v25 offset:0\n\t" \
   "ds_read_b64_tr_b16 v[210:211], v25 offset:4096\n\t" "v_add_u32 v24, s40, v18\n\t" "v_add_u32 v25, s40, v19\n\t" "s_add_u32 s40, s40, 8192\n\t" \
   "s_cmp_eq_u32 s40, 81920\n\t" "s_cselect_b32 s40, 0, s40\n\t" "ds_read_b64_tr_b16 v[200:201], v24 offset:0\n\t" \
   "ds_read_b64_tr_b16 v[202:203], v24 offset:4096\n\t" "ds_read_b64_tr_b16 v[216:217], v25 offset:0\n\t" \
   "ds_read_b64_tr_b16 v[218:219], v25 offset:4096\n\t" "s_waitcnt vmcnt(15)\n\t" "s_waitcnt lgkmcnt(0)\n\t" "s_barrier\n\t" \
   "ds_write_b128 v20, a[4:7] offset:0\n\t" "s_mov_b32 s41, 4456448\n\t" "buffer_load_dwordx4 v[196:199], v16, %[ra], s41 offen\n\t" \
   "s_waitcnt vmcnt(15)\n\t" "ds_write_b128 v20, a[68:71] offset:4096\n\t" "s_mov_b32 s41, 4587520\n\t" \
   "buffer_load_dwordx4 v[212:215], v16, %[ra], s41 offen\n\t" "s_waitcnt vmcnt(15)\n\t" "ds_write_b128 v20, a[12:15] offset:8192\n\t" \
   "s_mov_b32 s41, 4980736\n\t" "buffer_load_dwordx4 v[204:207], v16, %[ra], s41 offen\n\t" "s_waitcnt vmcnt(15)\n\t" \
   "ds_write_b128 v20, a[76:79] offset:12288\n\t" "s_mov_b32 s41, 5111808\n\t" "buffer_load_dwordx4 v[220:223], v16, %[ra], s41 offen\n\t" \
   "s_waitcnt vmcnt(15)\n\t" "ds_write_b128 v20, a[20:23] offset:16384\n\t" "s_mov_b32 s41, 0\n\t" \
   "buffer_load_dwordx4 a[128:131], v17, %[rb], s41 offen\n\t" "s_waitcnt vmcnt(15)\n\t" "ds_write_b128 v20, a[84:87] offset:20480\n\t" \
   "s_mov_b32 s41, 256\n\t" "buffer_load_dwordx4 a[132:135], v17, %[rb], s41 offen\n\t" "s_waitcnt vmcnt(15)\n\t" \
   "ds_write_b128 v20, a[28:31] offset:24576\n\t" "s_mov_b32 s41, 512\n\t" "buffer_load_dwordx4 a[160:163], v17, %[rb], s41 offen\n\t" \
   "s_waitcnt vmcnt(15)\n\t" "ds_write_b128 v20, a[92:95] offset:28672\n\t" "s_mov_b32 s41, 768\n\t" \
   "buffer_load_dwordx4 a[164:167], v17, %[rb], s41 offen\n\t" "s_waitcnt vmcnt(15)\n\t" "ds_write_b128 v20, a[36:39] offset:32768\n\t" \
   "s_mov_b32 s41, 1024\n\t" "buffer_load_dwordx4 a[192:195], v17, %[rb], s41 offen\n\t" "s_waitcnt vmcnt(15)\n\t" \
   "ds_write_b128 v20, a[100:103] offset:36864\n\t" "s_mov_b32 s41, 20480\n\t" "buffer_load_dwordx4 a[196:199], v17, %[rb], s41 offen\n\t" \
   "s_waitcnt vmcnt(15)\n\t" "ds_write_b128 v20, a[44:47] offset:40960\n\t" "s_mov_b32 s41, 20736\n\t" \
   "buffer_load_dwordx4 a[224:227], v17, %[rb], s41 offen\n\t" "s_waitcnt vmcnt(15)\n\t" "ds_write_b128 v20, a[108:111] offset:45056\n\t" \
   "s_mov_b32 s41, 20992\n\t" "buffer_load_dwordx4 a[228:231], v17, %[rb], s41 offen\n\t" "s_waitcnt vmcnt(15)\n\t" \
   "ds_write_b128 v20, a[52:55] offset:49152\n\t" "s_mov_b32 s41, 21248\n\t" "buffer_load_dwordx4 v[224:227], v17, %[rb], s41 offen\n\t" \
   "s_waitcnt vmcnt(15)\n\t" "ds_write_b128 v20, a[116:119] offset:53248\n\t" "s_mov_b32 s41, 21504\n\t" \
   "buffer_load_dwordx4 v[228:231], v17, %[rb], s41 offen\n\t" "s_waitcnt vmcnt(15)\n\t" "ds_write_b128 v20, a[60:63] offset:57344\n\t" \
   "s_mov_b32 s41, 40960\n\t" "buffer_load_dwordx4 a[136:139], v17, %[rb], s41 offen\n\t" "s_waitcnt vmcnt(15)\n\t" \
   "ds_write_b128 v20, a[124:127] offset:61440\n\t" "s_mov_b32 s41, 41216\n\t" "buffer_load_dwordx4 a[140:143], v17, %[rb], s41 offen\n\t" \
   "s_waitcnt vmcnt(15)\n\t" "ds_write_b128 v23, v[196:199] offset:0\n\t" "s_mov_b32 s41, 41472\n\t" \
   "buffer_load_dwordx4 a[168:171], v17, %[rb], s41 offen\n\t" "s_waitcnt vmcnt(15)\n\t" "ds_write_b128 v23, v[212:215] offset:4096\n\t" \
   "s_mov_b32 s41, 41728\n\t" "buffer_load_dwordx4 a[172:175], v17, %[rb], s41 offen\n\t" "s_waitcnt vmcnt(15)\n\t" \
   "ds_write_b128 v23, v[204:207] offset:8192\n\t" "s_mov_b32 s41, 41984\n\t" "buffer_load_dwordx4 a[200:203], v17, %[rb], s41 offen\n\t" \
   "s_waitcnt vmcnt(15)\n\t" "ds_write_b128 v23, v[220:223] offset:12288\n\t" "s_mov_b32 s41, 61440\n\t" \
   "buffer_load_dwordx4 a[204:207], v17, %[rb], s41 offen\n\t" "s_waitcnt lgkmcnt(0)\n\t" "s_barrier\n\t" LDSTAMP(3) "s_lshl_b32 s40, %[p0], 13\n\t" \
   "v_add_u32 v24, s40, v18\n\t" "v_add_u32 v25, s40, v19\n\t" "s_add_u32 s40, s40, 8192\n\t" "s_cmp_eq_u32 s40, 81920\n\t" \
   "s_cselect_b32 s40, 0, s40\n\t" "ds_read_b64_tr_b16 a[4:5], v24 offset:0\n\t" "ds_read_b64_tr_b16 a[6:7], v24 offset:4096\n\t" \
   "ds_read_b64_tr_b16 a[68:69], v25 offset:0\n\t" "ds_read_b64_tr_b16 a[70:71], v25 offset:4096\n\t" "v_add_u32 v24, s40, v18\n\t" \
   "v_add_u32 v25, s40, v19\n\t" "s_add_u32 s40, s40, 8192\n\t" "s_cmp_eq_u32 s40, 81920\n\t" "s_cselect_b32 s40, 0, s40\n\t" \
   "ds_read_b64_tr_b16 a[12:13], v24 offset:0\n\t" "ds_read_b64_tr_b16 a[14:15], v24 offset:4096\n\t" "ds_read_b64_tr_b16 a[76:77], v25 offset:0\n\t" \
   "ds_read_b64_tr_b16 a[78:79], v25 offset:4096\n\t" "v_add_u32 v24, s40, v18\n\t" "v_add_u32 v25, s40, v19\n\t" "s_add_u32 s40, s40, 8192\n\t" \
   "s_cmp_eq_u32 s40, 81920\n\t" "s_cselect_b32 s40, 0, s40\n\t" "ds_read_b64_tr_b16 a[20:21], v24 offset:0\n\t" \
   "ds_read_b64_tr_b16 a[22:23], v24 offset:4096\n\t" "ds_read_b64_tr_b16 a[84:85], v25 offset:0\n\t" \
   "ds_read_b64_tr_b16 a[86:87], v25 offset:4096\n\t" "v_add_u32 v24, s40, v18\n\t" "v_add_u32 v25, s40, v19\n\t" "s_add_u32 s40, s40, 8192\n\t" \
   "s_cmp_eq_u32 s40, 81920\n\t" "s_cselect_b32 s40, 0, s40\n\t" "ds_read_b64_tr_b16 a[28:29], v24 offset:0\n\t" \
   "ds_read_b64_tr_b16 a[30:31], v24 offset:4096\n\t" "ds_read_b64_tr_b16 a[92:93], v25 offset:0\n\t" \
   "ds_read_b64_tr_b16 a[94:95], v25 offset:4096\n\t" "v_add_u32 v24, s40, v18\n\t" "v_add_u32 v25, s40, v19\n\t" "s_add_u32 s40, s40, 8192\n\t" \
   "s_cmp_eq_u32 s40, 81920\n\t" "s_cselect_b32 s40, 0, s40\n\t" "ds_read_b64_tr_b16 a[36:37], v24 offset:0\n\t" \
   "ds_read_b64_tr_b16 a[38:39], v24 offset:4096\n\t" "ds_read_b64_tr_b16 a[100:101], v25 offset:0\n\t" \
   "ds_read_b64_tr_b16 a[102:103], v25 offset:4096\n\t" "v_add_u32 v24, s40, v18\n\t" "v_add_u32 v25, s40, v19\n\t" "s_add_u32 s40, s40, 8192\n\t" \
   "s_cmp_eq_u32 s40, 81920\n\t" "s_cselect_b32 s40, 0, s40\n\t" "ds_read_b64_tr_b16 a[44:45], v24 offset:0\n\t" \
   "ds_read_b64_tr_b16 a[46:47], v24 offset:4096\n\t" "ds_read_b64_tr_b16 a[108:109], v25 offset:0\n\t" \
   "ds_read_b64_tr_b16 a[110:111], v25 offset:4096\n\t" "v_add_u32 v24, s40, v18\n\t" "v_add_u32 v25, s40, v19\n\t" "s_add_u32 s40, s40, 8192\n\t" \
   "s_cmp_eq_u32 s40, 81920\n\t" "s_cselect_b32 s40, 0, s40\n\t" "ds_read_b64_tr_b16 a[52:53], v24 offset:0\n\t" \
   "ds_read_b64_tr_b16 a[54:55], v24 offset:4096\n\t" "ds_read_b64_tr_b16 a[116:117], v25 offset:0\n\t" \
   "ds_read_b64_tr_b16 a[118:119], v25 offset:4096\n\t" "v_add_u32 v24, s40, v18\n\t" "v_add_u32 v25, s40, v19\n\t" "s_add_u32 s40, s40, 8192\n\t" \
   "s_cmp_eq_u32 s40, 81920\n\t" "s_cselect_b32 s40, 0, s40\n\t" "ds_read_b64_tr_b16 a[60:61], v24 offset:0\n\t" \
   "ds_read_b64_tr_b16 a[62:63], v24 offset:4096\n\t" "ds_read_b64_tr_b16 a[124:125], v25 offset:0\n\t" \
   "ds_read_b64_tr_b16 a[126:127], v25 offset:4096\n\t" "v_add_u32 v24, s40, v18\n\t" "v_add_u32 v25, s40, v19\n\t" "s_add_u32 s40, s40, 8192\n\t" \
   "s_cmp_eq_u32 s40, 81920\n\t" "s_cselect_b32 s40, 0, s40\n\t" "ds_read_b64_tr_b16 v[196:197], v24 offset:0\n\t" \
   "ds_read_b64_tr_b16 v[198:199], v24 offset:4096\n\t" "ds_read_b64_tr_b16 v[212:213], v25 offset:0\n\t" \
   "ds_read_b64_tr_b16 v[214:215], v25 offset:4096\n\t" "v_add_u32 v24, s40, v18\n\t" "v_add_u32 v25, s40, v19\n\t" "s_add_u32 s40, s40, 8192\n\t" \
   "s_cmp_eq_u32 s40, 81920\n\t" "s_cselect_b32 s40, 0, s40\n\t" "ds_read_b64_tr_b16 v[204:205], v24 offset:0\n\t" \
   "ds_read_b64_tr_b16 v[206:207], v24 offset:4096\n\t" "ds_read_b64_tr_b16 v[220:221], v25 offset:0\n\t" \
   "ds_read_b64_tr_b16 v[222:223], v25 offset:4096\n\t" "s_waitcnt vmcnt(15)\n\t" "s_waitcnt lgkmcnt(0)\n\t" "s_barrier\n\t" \
   "ds_write_b32 v21, a128 offset:0\n\t" "ds_write_b32 v21, a129 offset:21120\n\t" "ds_write_b32 v21, a130 offset:10560\n\t" \
   "ds_write_b32 v21, a131 offset:31680\n\t" "s_mov_b32 s41, 61696\n\t" "buffer_load_dwordx4 a[232:235], v17, %[rb], s41 offen\n\t" \
   "s_waitcnt vmcnt(15)\n\t" "ds_write_b32 v21, a132 offset:64\n\t" "ds_write_b32 v21, a133 offset:21184\n\t" "ds_write_b32 v21, a134 offset:10624\n\t" \
   "ds_write_b32 v21, a135 offset:31744\n\t" "s_mov_b32 s41, 61952\n\t" "buffer_load_dwordx4 a[236:239], v17, %[rb], s41 offen\n\t" \
   "s_waitcnt vmcnt(15)\n\t" "ds_write_b32 v21, a160 offset:128\n\t" "ds_write_b32 v21, a161 offset:21248\n\t" \
   "ds_write_b32 v21, a162 offset:10688\n\t" "ds_write_b32 v21, a163 offset:31808\n\t" "s_mov_b32 s41, 62208\n\t" \
   "buffer_load_dwordx4 v[232:235], v17, %[rb], s41 offen\n\t" "s_waitcnt vmcnt(15)\n\t" "ds_write_b32 v21, a164 offset:192\n\t" \
   "ds_write_b32 v21, a165 offset:21312\n\t" "ds_write_b32 v21, a166 offset:10752\n\t" "ds_write_b32 v21, a167 offset:31872\n\t" \
   "s_mov_b32 s41, 62464\n\t" "buffer_load_dwordx4 v[236:239], v17, %[rb], s41 offen\n\t" "s_waitcnt vmcnt(15)\n\t" \
   "ds_write_b32 v21, a192 offset:256\n\t" "ds_write_b32 v21, a193 offset:21376\n\t" "ds_write_b32 v21, a194 offset:10816\n\t" \
   "ds_write_b32 v21, a195 offset:31936\n\t" "s_mov_b32 s41, 81920\n\t" "buffer_load_dwordx4 a[144:147], v17, %[rb], s41 offen\n\t" \
   "s_waitcnt vmcnt(15)\n\t" "ds_write_b32 v21, a196 offset:5248\n\t" "ds_write_b32 v21, a197 offset:26368\n\t" \
   "ds_write_b32 v21, a198 offset:15808\n\t" "ds_write_b32 v21, a199 offset:36928\n\t" "s_mov_b32 s41, 82176\n\t" \
   "buffer_load_dwordx4 a[148:151], v17, %[rb], s41 offen\n\t" "s_waitcnt vmcnt(15)\n\t" "ds_write_b32 v21, a224 offset:5312\n\t" \
   "ds_write_b32 v21, a225 offset:26432\n\t" "ds_write_b32 v21, a226 offset:15872\n\t" "ds_write_b32 v21, a227 offset:36992\n\t" \
   "s_mov_b32 s41, 82432\n\t" "buffer_load_dwordx4 a[176:179], v17, %[rb], s41 offen\n\t" "s_waitcnt vmcnt(15)\n\t" \
   "ds_write_b32 v21, a228 offset:5376\n\t" "ds_write_b32 v21, a229 offset:26496\n\t" "ds_write_b32 v21, a230 offset:15936\n\t" \
   "ds_write_b32 v21, a231 offset:37056\n\t" "s_mov_b32 s41, 82688\n\t" "buffer_load_dwordx4 a[180:183], v17, %[rb], s41 offen\n\t" \
   "s_waitcnt vmcnt(15)\n\t" "ds_write_b32 v21, v224 offset:5440\n\t" "ds_write_b32 v21, v225 offset:26560\n\t" \
   "ds_write_b32 v21, v226 offset:16000\n\t" "ds_write_b32 v21, v227 offset:37120\n\t" "s_mov_b32 s41, 82944\n\t" \
   "buffer_load_dwordx4 a[208:211], v17, %[rb], s41 offen\n\t" "s_waitcnt vmcnt(15)\n\t" "ds_write_b32 v21, v228 offset:5504\n\t" \
   "ds_write_b32 v21, v229 offset:26624\n\t" "ds_write_b32 v21, v230 offset:16064\n\t" "ds_write_b32 v21, v231 offset:37184\n\t" \
   "s_mov_b32 s41, 102400\n\t" "buffer_load_dwordx4 a[212:215], v17, %[rb], s41 offen\n\t" "s_waitcnt lgkmcnt(0)\n\t" "s_barrier\n\t" LDSTAMP(4) \
   "ds_read_b64_tr_b16 a[128:129], v22 offset:0\n\t" "ds_read_b64_tr_b16 a[130:131], v22 offset:656\n\t" \
   "ds_read_b64_tr_b16 a[132:133], v22 offset:2624\n\t" "ds_read_b64_tr_b16 a[134:135], v22 offset:3280\n\t" \
   "ds_read_b64_tr_b16 a[160:161], v22 offset:16\n\t" "ds_read_b64_tr_b16 a[162:163], v22 offset:672\n\t" \
   "ds_read_b64_tr_b16 a[164:165], v22 offset:2640\n\t" "ds_read_b64_tr_b16 a[166:167], v22 offset:3296\n\t" \
   "ds_read_b64_tr_b16 a[192:193], v22 offset:32\n\t" "ds_read_b64_tr_b16 a[194:195], v22 offset:688\n\t" \
   "ds_read_b64_tr_b16 a[196:197], v22 offset:2656\n\t" "ds_read_b64_tr_b16 a[198:199], v22 offset:3312\n\t" \
   "ds_read_b64_tr_b16 a[224:225], v22 offset:48\n\t" "ds_read_b64_tr_b16 a[226:227], v22 offset:704\n\t" \
   "ds_read_b64_tr_b16 a[228:229], v22 offset:2672\n\t" "ds_read_b64_tr_b16 a[230:231], v22 offset:3328\n\t" \
   "ds_read_b64_tr_b16 v[224:225], v22 offset:64\n\t" "ds_read_b64_tr_b16 v[226:227], v22 offset:720\n\t" \
   "ds_read_b64_tr_b16 v[228:229], v22 offset:2688\n\t" "ds_read_b64_tr_b16 v[230:231], v22 offset:3344\n\t" "s_waitcnt vmcnt(15)\n\t" \
   "s_waitcnt lgkmcnt(0)\n\t" "s_barrier\n\t" "ds_write_b32 v21, a136 offset:0\n\t" "ds_write_b32 v21, a137 offset:21120\n\t" \
   "ds_write_b32 v21, a138 offset:10560\n\t" "ds_write_b32 v21, a139 offset:31680\n\t" "s_mov_b32 s41, 102656\n\t" \
   "buffer_load_dwordx4 a[240:243], v17, %[rb], s41 offen\n\t" "s_waitcnt vmcnt(15)\n\t" "ds_write_b32 v21, a140 offset:64\n\t" \
   "ds_write_b32 v21, a141 offset:21184\n\t" "ds_write_b32 v21, a142 offset:10624\n\t" "ds_write_b32 v21, a143 offset:31744\n\t" \
   "s_mov_b32 s41, 102912\n\t" "buffer_load_dwordx4 a[244:247], v17, %[rb], s41 offen\n\t" "s_waitcnt vmcnt(15)\n\t" \
   "ds_write_b32 v21, a168 offset:128\n\t" "ds_write_b32 v21, a169 offset:21248\n\t" "ds_write_b32 v21, a170 offset:10688\n\t" \
   "ds_write_b32 v21, a171 offset:31808\n\t" "s_mov_b32 s41, 103168\n\t" "buffer_load_dwordx4 v[240:243], v17, %[rb], s41 offen\n\t" \
   "s_waitcnt vmcnt(15)\n\t" "ds_write_b32 v21, a172 offset:192\n\t" "ds_write_b32 v21, a173 offset:21312\n\t" \
   "ds_write_b32 v21, a174 offset:10752\n\t" "ds_write_b32 v21, a175 offset:31872\n\t" "s_mov_b32 s41, 103424\n\t" \
   "buffer_load_dwordx4 v[244:247], v17, %[rb], s41 offen\n\t" "s_waitcnt vmcnt(15)\n\t" "ds_write_b32 v21, a200 offset:256\n\t" \
   "ds_write_b32 v21, a201 offset:21376\n\t" "ds_write_b32 v21, a202 offset:10816\n\t" "ds_write_b32 v21, a203 offset:31936\n\t" \
   "s_mov_b32 s41, 122880\n\t" "buffer_load_dwordx4 a[152:155], v17, %[rb], s41 offen\n\t" "s_waitcnt vmcnt(15)\n\t" \
   "ds_write_b32 v21, a204 offset:5248\n\t" "ds_write_b32 v21, a205 offset:26368\n\t" "ds_write_b32 v21, a206 offset:15808\n\t" \
   "ds_write_b32 v21, a207 offset:36928\n\t" "s_mov_b32 s41, 123136\n\t" "buffer_load_dwordx4 a[156:159], v17, %[rb], s41 offen\n\t" \
   "s_waitcnt vmcnt(15)\n\t" "ds_write_b32 v21, a232 offset:5312\n\t" "ds_write_b32 v21, a233 offset:26432\n\t" \
   "ds_write_b32 v21, a234 offset:15872\n\t" "ds_write_b32 v21, a235 offset:36992\n\t" "s_mov_b32 s41, 123392\n\t" \
   "buffer_load_dwordx4 a[184:187], v17, %[rb], s41 offen\n\t" "s_waitcnt vmcnt(15)\n\t" "ds_write_b32 v21, a236 offset:5376\n\t" \
   "ds_write_b32 v21, a237 offset:26496\n\t" "ds_write_b32 v21, a238 offset:15936\n\t" "ds_write_b32 v21, a239 offset:37056\n\t" \
   "s_mov_b32 s41, 123648\n\t" "buffer_load_dwordx4 a[188:191], v17, %[rb], s41 offen\n\t" "s_waitcnt vmcnt(15)\n\t" \
   "ds_write_b32 v21, v232 offset:5440\n\t" "ds_write_b32 v21, v233 offset:26560\n\t" "ds_write_b32 v21, v234 offset:16000\n\t" \
   "ds_write_b32 v21, v235 offset:37120\n\t" "s_mov_b32 s41, 123904\n\t" "buffer_load_dwordx4 a[216:219], v17, %[rb], s41 offen\n\t" \
   "s_waitcnt vmcnt(15)\n\t" "ds_write_b32 v21, v236 offset:5504\n\t" "ds_write_b32 v21, v237 offset:26624\n\t" \
   "ds_write_b32 v21, v238 offset:16064\n\t" "ds_write_b32 v21, v239 offset:37184\n\t" "s_mov_b32 s41, 143360\n\t" \
   "buffer_load_dwordx4 a[220:223], v17, %[rb], s41 offen\n\t" "s_waitcnt lgkmcnt(0)\n\t" "s_barrier\n\t" LDSTAMP(5) \
   "ds_read_b64_tr_b16 a[136:137], v22 offset:0\n\t" "ds_read_b64_tr_b16 a[138:139], v22 offset:656\n\t" \
   "ds_read_b64_tr_b16 a[140:141], v22 offset:2624\n\t" "ds_read_b64_tr_b16 a[142:143], v22 offset:3280\n\t" \
   "ds_read_b64_tr_b16 a[168:169], v22 offset:16\n\t" "ds_read_b64_tr_b16 a[170:171], v22 offset:672\n\t" \
   "ds_read_b64_tr_b16 a[172:173], v22 offset:2640\n\t" "ds_read_b64_tr_b16 a[174:175], v22 offset:3296\n\t" \
   "ds_read_b64_tr_b16 a[200:201], v22 offset:32\n\t" "ds_read_b64_tr_b16 a[202:203], v22 offset:688\n\t" \
   "ds_read_b64_tr_b16 a[204:205], v22 offset:2656\n\t" "ds_read_b64_tr_b16 a[206:207], v22 offset:3312\n\t" \
   "ds_read_b64_tr_b16 a[232:233], v22 offset:48\n\t" "ds_read_b64_tr_b16 a[234:235], v22 offset:704\n\t" \
   "ds_read_b64_tr_b16 a[236:237], v22 offset:2672\n\t" "ds_read_b64_tr_b16 a[238:239], v22 offset:3328\n\t" \
   "ds_read_b64_tr_b16 v[232:233], v22 offset:64\n\t" "ds_read_b64_tr_b16 v[234:235], v22 offset:720\n\t" \
   "ds_read_b64_tr_b16 v[236:237], v22 offset:2688\n\t" "ds_read_b64_tr_b16 v[238:239], v22 offset:3344\n\t" "s_waitcnt vmcnt(15)\n\t" \
   "s_waitcnt lgkmcnt(0)\n\t" "s_barrier\n\t" "ds_write_b32 v21, a144 offset:0\n\t" "ds_write_b32 v21, a145 offset:21120\n\t" \
   "ds_write_b32 v21, a146 offset:10560\n\t" "ds_write_b32 v21, a147 offset:31680\n\t" "s_mov_b32 s41, 143616\n\t" \
   "buffer_load_dwordx4 a[248:251], v17, %[rb], s41 offen\n\t" "s_waitcnt vmcnt(15)\n\t" "ds_write_b32 v21, a148 offset:64\n\t" \
   "ds_write_b32 v21, a149 offset:21184\n\t" "ds_write_b32 v21, a150 offset:10624\n\t" "ds_write_b32 v21, a151 offset:31744\n\t" \
   "s_mov_b32 s41, 143872\n\t" "buffer_load_dwordx4 a[252:255], v17, %[rb], s41 offen\n\t" "s_waitcnt vmcnt(15)\n\t" \
   "ds_write_b32 v21, a176 offset:128\n\t" "ds_write_b32 v21, a177 offset:21248\n\t" "ds_write_b32 v21, a178 offset:10688\n\t" \
   "ds_write_b32 v21, a179 offset:31808\n\t" "s_mov_b32 s41, 144128\n\t" "buffer_load_dwordx4 v[248:251], v17, %[rb], s41 offen\n\t" \
   "s_waitcnt vmcnt(15)\n\t" "ds_write_b32 v21, a180 offset:192\n\t" "ds_write_b32 v21, a181 offset:21312\n\t" \
   "ds_write_b32 v21, a182 offset:10752\n\t" "ds_write_b32 v21, a183 offset:31872\n\t" "s_mov_b32 s41, 144384\n\t" \
   "buffer_load_dwordx4 v[252:255], v17, %[rb], s41 offen\n\t" "s_waitcnt vmcnt(15)\n\t" "ds_write_b32 v21, a208 offset:256\n\t" \
   "ds_write_b32 v21, a209 offset:21376\n\t" "ds_write_b32 v21, a210 offset:10816\n\t" "ds_write_b32 v21, a211 offset:31936\n\t" \
   "s_waitcnt vmcnt(14)\n\t" "ds_write_b32 v21, a212 offset:5248\n\t" "ds_write_b32 v21, a213 offset:26368\n\t" \
   "ds_write_b32 v21, a214 offset:15808\n\t" "ds_write_b32 v21, a215 offset:36928\n\t" "s_waitcnt vmcnt(13)\n\t" \
   "ds_write_b32 v21, a240 offset:5312\n\t" "ds_write_b32 v21, a241 offset:26432\n\t" "ds_write_b32 v21, a242 offset:15872\n\t" \
   "ds_write_b32 v21, a243 offset:36992\n\t" "s_waitcnt vmcnt(12)\n\t" "ds_write_b32 v21, a244 offset:5376\n\t" \
   "ds_write_b32 v21, a245 offset:26496\n\t" "ds_write_b32 v21, a246 offset:15936\n\t" "ds_write_b32 v21, a247 offset:37056\n\t" \
   "s_waitcnt vmcnt(11)\n\t" "ds_write_b32 v21, v240 offset:5440\n\t" "ds_write_b32 v21, v241 offset:26560\n\t" \
   "ds_write_b32 v21, v242 offset:16000\n\t" "ds_write_b32 v21, v243 offset:37120\n\t" "s_waitcnt vmcnt(10)\n\t" \
   "ds_write_b32 v21, v244 offset:5504\n\t" "ds_write_b32 v21, v245 offset:26624\n\t" "ds_write_b32 v21, v246 offset:16064\n\t" \
   "ds_write_b32 v21, v247 offset:37184\n\t" "buffer_load_dwordx4 v[28:31], v112, %[rw], 0 offen sc1\n\t" "s_waitcnt lgkmcnt(0)\n\t" "s_barrier\n\t" \
   LDSTAMP(6) "ds_read_b64_tr_b16 a[144:145], v22 offset:0\n\t" "ds_read_b64_tr_b16 a[146:147], v22 offset:656\n\t" \
   "ds_read_b64_tr_b16 a[148:149], v22 offset:2624\n\t" "ds_read_b64_tr_b16 a[150:151], v22 offset:3280\n\t" \
   "ds_read_b64_tr_b16 a[176:177], v22 offset:16\n\t" "ds_read_b64_tr_b16 a[178:179], v22 offset:672\n\t" \
   "ds_read_b64_tr_b16 a[180:181], v22 offset:2640\n\t" "ds_read_b64_tr_b16 a[182:183], v22 offset:3296\n\t" \
   "ds_read_b64_tr_b16 a[208:209], v22 offset:32\n\t" "ds_read_b64_tr_b16 a[210:211], v22 offset:688\n\t" \
   "ds_read_b64_tr_b16 a[212:213], v22 offset:2656\n\t" "ds_read_b64_tr_b16 a[214:215], v22 offset:3312\n\t" \
   "ds_read_b64_tr_b16 a[240:241], v22 offset:48\n\t" "ds_read_b64_tr_b16 a[242:243], v22 offset:704\n\t" \
   "ds_read_b64_tr_b16 a[244:245], v22 offset:2672\n\t" "ds_read_b64_tr_b16 a[246:247], v22 offset:3328\n\t" \
   "ds_read_b64_tr_b16 v[240:241], v22 offset:64\n\t" "ds_read_b64_tr_b16 v[242:243], v22 offset:720\n\t" \
   "ds_read_b64_tr_b16 v[244:245], v22 offset:2688\n\t" "ds_read_b64_tr_b16 v[246:247], v22 offset:3344\n\t" "s_waitcnt vmcnt(10)\n\t" \
   "s_waitcnt lgkmcnt(0)\n\t" "s_barrier\n\t" "ds_write_b32 v21, a152 offset:0\n\t" "ds_write_b32 v21, a153 offset:21120\n\t" \
   "ds_write_b32 v21, a154 offset:10560\n\t" "ds_write_b32 v21, a155 offset:31680\n\t" "s_waitcnt vmcnt(9)\n\t" "ds_write_b32 v21, a156 offset:64\n\t" \
   "ds_write_b32 v21, a157 offset:21184\n\t" "ds_write_b32 v21, a158 offset:10624\n\t" "ds_write_b32 v21, a159 offset:31744\n\t" \
   "s_waitcnt vmcnt(8)\n\t" "ds_write_b32 v21, a184 offset:128\n\t" "ds_write_b32 v21, a185 offset:21248\n\t" "ds_write_b32 v21, a186 offset:10688\n\t" \
   "ds_write_b32 v21, a187 offset:31808\n\t" "s_waitcnt vmcnt(7)\n\t" "ds_write_b32 v21, a188 offset:192\n\t" "ds_write_b32 v21, a189 offset:21312\n\t" \
   "ds_write_b32 v21, a190 offset:10752\n\t" "ds_write_b32 v21, a191 offset:31872\n\t" "s_waitcnt vmcnt(6)\n\t" "ds_write_b32 v21, a216 offset:256\n\t" \
   "ds_write_b32 v21, a217 offset:21376\n\t" "ds_write_b32 v21, a218 offset:10816\n\t" "ds_write_b32 v21, a219 offset:31936\n\t" \
   "s_waitcnt vmcnt(5)\n\t" "ds_write_b32 v21, a220 offset:5248\n\t" "ds_write_b32 v21, a221 offset:26368\n\t" \
   "ds_write_b32 v21, a222 offset:15808\n\t" "ds_write_b32 v21, a223 offset:36928\n\t" "s_waitcnt vmcnt(4)\n\t" \
   "ds_write_b32 v21, a248 offset:5312\n\t" "ds_write_b32 v21, a249 offset:26432\n\t" "ds_write_b32 v21, a250 offset:15872\n\t" \
   "ds_write_b32 v21, a251 offset:36992\n\t" "s_waitcnt vmcnt(3)\n\t" "ds_write_b32 v21, a252 offset:5376\n\t" \
   "ds_write_b32 v21, a253 offset:26496\n\t" "ds_write_b32 v21, a254 offset:15936\n\t" "ds_write_b32 v21, a255 offset:37056\n\t" \
   "s_waitcnt vmcnt(2)\n\t" "ds_write_b32 v21, v248 offset:5440\n\t" "ds_write_b32 v21, v249 offset:26560\n\t" \
   "ds_write_b32 v21, v250 offset:16000\n\t" "ds_write_b32 v21, v251 offset:37120\n\t" "s_waitcnt vmcnt(1)\n\t" \
   "ds_write_b32 v21, v252 offset:5504\n\t" "ds_write_b32 v21, v253 offset:26624\n\t" "ds_write_b32 v21, v254 offset:16064\n\t" \
   "ds_write_b32 v21, v255 offset:37184\n\t" "s_waitcnt lgkmcnt(0)\n\t" "s_barrier\n\t" LDSTAMP(7) "ds_read_b64_tr_b16 a[152:153], v22 offset:0\n\t" \
   "ds_read_b64_tr_b16 a[154:155], v22 offset:656\n\t" "ds_read_b64_tr_b16 a[156:157], v22 offset:2624\n\t" \
   "ds_read_b64_tr_b16 a[158:159], v22 offset:3280\n\t" "ds_read_b64_tr_b16 a[184:185], v22 offset:16\n\t" \
   "ds_read_b64_tr_b16 a[186:187], v22 offset:672\n\t" "ds_read_b64_tr_b16 a[188:189], v22 offset:2640\n\t" \
   "ds_read_b64_tr_b16 a[190:191], v22 offset:3296\n\t" "ds_read_b64_tr_b16 a[216:217], v22 offset:32\n\t" \
   "ds_read_b64_tr_b16 a[218:219], v22 offset:688\n\t" "ds_read_b64_tr_b16 a[220:221], v22 offset:2656\n\t" \
   "ds_read_b64_tr_b16 a[222:223], v22 offset:3312\n\t" "ds_read_b64_tr_b16 a[248:249], v22 offset:48\n\t" \
   "ds_read_b64_tr_b16 a[250:251], v22 offset:704\n\t" "ds_read_b64_tr_b16 a[252:253], v22 offset:2672\n\t" \
   "ds_read_b64_tr_b16 a[254:255], v22 offset:3328\n\t" "ds_read_b64_tr_b16 v[248:249], v22 offset:64\n\t" \
   "ds_read_b64_tr_b16 v[250:251], v22 offset:720\n\t" "ds_read_b64_tr_b16 v[252:253], v22 offset:2688\n\t" \
   "ds_read_b64_tr_b16 v[254:255], v22 offset:3344\n\t" "s_waitcnt vmcnt(0) lgkmcnt(0)\n\t" "s_barrier"
#define LDY_CLOB "v24", "v25", "v26", "v32", "v33", "v34", "v35", "v36", "v37", "v38", "v39", "v40", "v41", "v42", "v43", "v44", "v45", "v46", "v47", "v48", "v49", "v50", "v51", "v52", "v53", "v54", "v55", "v56", "v57", "v58", "v59", "v60", "v61", "v62", "v63", "v64", "v65", "v66", "v67", "v68", "v69", "v70", "v71", "v72", "v73", "v74", "v75", "v76", "v77", "v78", "v79", "v80", "v81", "v82", "v83", "v84", "v85", "v86", "v87", "v88", "v89", "v90", "v91", "v92", "v93", "v94", "v95", "v96", "v97", "v98", "v99", "v100", "v101", "v102", "v103", "v104", "v105", "v106", "v107", "v108", "v109", "v110", "v111", "v120", "v121", "v122", "v123", "s40", "s41"
#define W_OPS "{a[0:31]}"(w[0]), "{a[32:63]}"(w[1]), "{a[64:95]}"(w[2]), "{a[96:127]}"(w[3]), "{a[128:159]}"(w[4]), "{a[160:191]}"(w[5]), \
              "{a[192:223]}"(w[6]), "{a[224:255]}"(w[7]), "{v[192:223]}"(w[8]), "{v[224:255]}"(w[9])

__device__ __forceinline__ int mem_lane(int lane) { return lane * 16; }
__device__ __forceinline__ int pair_of(int g) { return (g & 1) * 2 + (g >> 1); }

__device__ __forceinline__ void rec_p1(int wv_, const v32u (&w)[10], unsigned char* ws, const Pool& pool, int layer, int h, int xs, RecTune tn, unsigned long long* stamps) {
  const Clk0 c0 = clk_sample();
  const unsigned lds0 = (unsigned)(uintptr_t)(__attribute__((address_space(3))) unsigned char*)dyn_lds;
  const int lane = lane_id(), wv = wv_, g = lane >> 4, r = lane & 15;
  const v4u rs = make_rs(ws, (unsigned)OFF_END);
  const int p0 = wv < 3 ? 3 * wv : 8;
  v16u ad, la = (unsigned)lane;
  const int kqo = (int)(wv < 2 ? OFF_KF : OFF_QF) + ((h * RT + (wv & 1)) * 4 * 64 + lane) * 16;
  const int progo = (wv == 0 && lane == 0) ? prog_off(layer, 0, h) : OOB;
  {
    const int p2 = wv < 3 ? p0 + 2 : 0;
    ad[0] = W_MEM + (p0 >> 1) * WIN + (p0 & 1) * 1024 + mem_lane(lane);
    ad[1] = W_MEM + ((p0 + 1) >> 1) * WIN + ((p0 + 1) & 1) * 1024 + mem_lane(lane);
    ad[2] = W_MEM + (p2 >> 1) * WIN + (p2 & 1) * 1024 + mem_lane(lane);
    ad[3] = lane == 0 ? W_MISC + h * 64 + wv * 4 : OOB;
    ad[4] = copy_off(wv) + (h >> 2) * WIN + (h & 3) * 512 + (g >> 1) * 256 + (2 * (r & 7) + (r >> 3)) * 16 + (g & 1) * 8;
    ad[5] = g < 2 ? copy_off(wv + 4 * (r >> 3)) + (8 + (h >> 4)) * WIN + ((h >> 3) & 1) * 1024 + (((h >> 1) & 3) * 16 + (h & 1) * 8 + (r & 7)) * 16 + g * 8 : OOB;
    ad[6] = (r & 1) ? 0xEEEEu : 0x4444u;
    ad[7] = lds0 + (32 * wv + r) * 16 + g * 4;
    ad[8] = lds0 + (4 * g + (r >> 2)) * 16 + (r & 1) * 8;
    ad[9] = lds0 + L_M2 + wv * 3072 + lane * 16;
    ad[10] = kqo;
    ad[11] = M_TM + xs * 128 + (lane & 1) * 12;
    ad[12] = kqf_off(layer, 0, h) + (lane & (h < NFILL ? 7 : 3)) * 4;
    ad[13] = wv > 1 ? (int)OFF_PXF + ((h * 2 + (wv & 1)) * 64 + lane) * 16 : OOB;
    ad[14] = (g < 2 && wv > 1) ? (int)OFF_PXM + (((h >> 2) * 2 + (wv & 1)) * 64 + (h & 3) * 16 + r) * 16 + g * 8 : OOB;
    ad[15] = progo;
#pragma unroll
    for (int i = 0; i < 10; ++i) { int p = p0 + i; p = p >= 10 ? p - 10 : p; la[i] = lds0 + p * 1024 + lane * 16; }
    la[10] = lds0 + L_KQ + lane * 16;
    la[12] = lds0 + REC_ST + wv * 8;
    asm volatile("v_mov_b32 %0, 0xceabfb8f" : "=v"(la[13]));
    la[14] = 16 * (wv & 1) + r - 4 * g;
    la[15] = lds0 + L_KQ + (wv & 1) * 4096 + lane * 16;
  }
  __builtin_amdgcn_s_waitcnt(0x0F70);
  if (layer) __builtin_amdgcn_s_sleep(64);
  v4u clk = clk_init(c0);
  unsigned long long stat;
  asm volatile(P1_ASM
               : "=&{s[76:77]}"(stat), "+{s[56:59]}"(clk)
               : W_OPS, "{v[128:143]}"(ad), "{v[112:127]}"(la), [rs] "s"(rs), [sel] "s"(pool.sel), [pool] "s"(pool.base), [xs] "s"(xs),
                 [lad0] "s"(ladder(xs, xs & 1)), [s3] "s"(pool.s3), [m1] "s"(tn.m1), [l2s] "s"(lds0 + L_M2 + wv * 3072),
                 [hi8] "s"(0xFF00FF00FF00FF00ull), [wv] "s"(wv), [kf0] "s"((int)OFF_KF + h * RT * 4096),
                 [esc] "s"(pool.base + (NSEG - 1) * REG)
               : P1_CLOB RST_CLOB, "vcc", "scc", "m0", "memory");
  LDSBAR();
  {
    const unsigned one = 1u;
    const int so = (NSEG - 1) * 4096;
    VM_ST32(one, progo, so);
    VM_WAIT(0);
  }
}

__device__ __forceinline__ void rec_pv(int wv_, const v32u (&w)[10], unsigned char* ws, const Pool& pool, int layer, int h, int xs, RecTune tn, unsigned long long* stamps) {
  const Clk0 c0 = clk_sample();
  const unsigned lds0 = (unsigned)(uintptr_t)(__attribute__((address_space(3))) unsigned char*)dyn_lds;
  const int lane = lane_id(), wv = wv_, g = lane >> 4, r = lane & 15;
  const v4u rs = make_rs(ws, (unsigned)OFF_END);
  const int p0 = wv < 3 ? 3 * wv : 8, p2 = wv < 3 ? p0 + 2 : 0;
  v16u ad = (unsigned)lane, la = (unsigned)lane;
  ad[0] = W_MEM + (p0 >> 1) * WIN + (p0 & 1) * 1024 + mem_lane(lane);
  ad[1] = W_MEM + ((p0 + 1) >> 1) * WIN + ((p0 + 1) & 1) * 1024 + mem_lane(lane);
  ad[2] = W_MEM + (p2 >> 1) * WIN + (p2 & 1) * 1024 + mem_lane(lane);
  ad[3] = (r & 8) ? 0xEEEEu : 0x4444u;
  ad[4] = (wv * 10 + (g >> 1)) * 16384 + h * 256 + r * 16 + (g & 1) * 8;
  ad[5] = h * 16384 + wv * 2048 + lane * 16;
  ad[6] = (r & 1) ? 0xEEEEu : 0x4444u;
  ad[7] = lds0 + (32 * wv + r) * 16 + pair_of(g) * 4;
  ad[8] = lds0 + (4 * g + (r >> 2)) * 16 + (r & 1) * 8;
  ad[11] = M_TM + (4 + xs) * 128 + (lane & 1) * 12;
  ad[15] = (wv == 0 && lane == 0) ? prog_off(layer, 0, 32 + h) : OOB;
#pragma unroll
  for (int i = 0; i < 10; ++i) { int p = p0 + i; p = p >= 10 ? p - 10 : p; la[i] = lds0 + p * 1024 + lane * 16; }
  la[12] = lds0 + REC_ST + wv * 8;
  __builtin_amdgcn_s_waitcnt(0x0F70);
  if (layer) __builtin_amdgcn_s_sleep(64);
  v4u clk = clk_init(c0);
  unsigned long long stat;
  asm volatile(PV_ASM
               : "=&{s[76:77]}"(stat), "+{s[56:59]}"(clk)
               : W_OPS, "{v[128:143]}"(ad), "{v[112:127]}"(la), [rs] "s"(rs), [sel] "s"(pool.sel), [pool] "s"(pool.base), [xs] "s"(xs),
                 [lad0] "s"(ladder(xs, xs & 1)), [s3] "s"(pool.s3), [m1c] "s"(tn.m1 + tn.c2), [esc] "s"(pool.base + (NSEG - 1) * REG),
                 [ysel] "s"(pool.ysel), [yb] "s"(pool.base + P_Y)
               : PV_CLOB RST_CLOB, "vcc", "scc", "memory");
}

#define TSTART 5300
#define TSTARU 3600
#define TD1 900u
#define TR2 400u
#define TEMA 1
#define TEMB 2
#define TBUP 48
#define TBDN 6
#define TLATE 520
#define TSEC 50
__device__ __forceinline__ unsigned track_dur(unsigned e, int m, int rob, int s) {
  const int lo = (int)e - (s < 8 ? 64 : (rob & 255)), hi = (int)e + (rob >> 8);
  if (s >= 8) m = (int)e + ((m - (int)e + (m > (int)e ? (1 << TEMA) - 1 : 0)) >> (m > (int)e ? TEMA : TEMB));
  return (unsigned)min(max(m, lo), hi);
}
#define TTS 200
#define TTU 240
__device__ __forceinline__ void rec_sched(unsigned char* ws, const Pool& pool, int layer, int c16, int xs, RecTune tn, unsigned long long* stamps) {
  const Clk0 c0 = clk_sample();
  const unsigned lds0 = (unsigned)(uintptr_t)(__attribute__((address_space(3))) unsigned char*)dyn_lds;
  const int lane = lane_id();
  const v4u rs = make_rs(ws, (unsigned)OFF_END);
  unsigned long long stat = 0;
  const int sm = M_STAMP + min(lane, 39) * 64, sp = W_MISC + (lane & 31) * 64;
  const int yo = M_TM + xs * 128, oc = (c16 == 0 && lane < 16) ? M_TM + lane * 128 : OOB;
  const unsigned cv = flag_finish<2>(rs, kqf_off(layer, 0, lane), 0xFFFFFFFFu);
  v4u clk = clk_init(c0);
  PH(3 + layer * 6)
  unsigned r2a = TR2, d1a = TD1, dd = TD1 + tn.m2, tp = 0, tm = 0, anchor = clk.x - dd;
  unsigned rr = (unsigned)min(max(wave_imax((int)(cv * 8u - clk.x) + (lane < 32 ? TSTART : TSTARU)), 300), 4000);
  int bonus = 0;
  bool clean = false;
#pragma unroll 1
  for (int s = 0; s < NSEG - 1; ++s) {
    const int rgm = pool.region((s + NSEG - 1) & (NSEG - 1)), rg = pool.region(s);
    const unsigned slot = lds0 + L_T + (s & 1) * 32, ao = anchor;
    unsigned b2 = 1u;
    v4u b1 = {1u, 1u, 1u, 1u};
    if (s > 0) {
      asm volatile(WAIT_ASM : "+{s[56:59]}"(clk) : [ta] "s"(tm + TTS) : WAIT_CLOB, "scc", "memory");
      VM_LD32(b2, sm, rgm);
      VM_LD128(b1, sp, rgm, 0);
      asm volatile("s_waitcnt vmcnt(0)" : "+v"(b2), "+v"(b1)::"memory");
      anchor = tm;
      if (__builtin_expect(__any(b2 == 0xFFFFFFFFu || (int)(b2 - (tm + tn.m1 - tn.vis)) > 0), 0)) {
        STAT(3);
        for (unsigned spins = 0; __any(b2 == 0xFFFFFFFFu) && spins < SPIN_CAP; ++spins) {
          __builtin_amdgcn_s_sleep(2);
          VM_LD32(b2, sm, rgm);
          VM_WAIT1(0, b2);
        }
        const int d = wave_imax((int)(b2 - tm)) + tn.vis - tn.m1;
        anchor = tm + (d <= 0 ? 0 : (d <= tn.dlt ? tn.dlt + TSEC : d + TLATE));
      }
    }
    const unsigned tpo = tp, tmo = tm;
    tp = anchor + dd;
    tm = nosent(tp + rr);
    LDS_V4(slot) = v4u{tp, tm, (unsigned)(s + 1), anchor};
    if (anchor != tmo && s > 2) bonus = TBUP; else bonus = max(bonus - TBDN, 0);
    dd = d1a + tn.m2 + bonus;
    rr = r2a + bonus;
    {
      const v4u wq = v4u{tm, dd, rr, dd + rr};
      VM_ST128(wq, oc, rg);
    }
    if (s > 1 && !__any((b2 & 1u) != 0u)) r2a = track_dur(r2a, wave_imax((int)(b2 - tpo)), tn.rob, s);
    if (s > 1 && clean && !__any(((b1.x | b1.y | b1.z | b1.w) & 1u) != 0u))
      d1a = track_dur(d1a, wave_imax(max(max((int)(b1.x - ao), (int)(b1.y - ao)), max((int)(b1.z - ao), (int)(b1.w - ao)))), tn.rob, s);
    clean = anchor == tmo;
    while ((int)(tp + 40u - rtc() * 8u) > 0 && (int)(tp - rtc() * 8u) < 32000) __builtin_amdgcn_s_sleep(1);
    v4u lw;
    VM_LD128(lw, yo, rg, 0);
    VM_WAIT1(0, lw);
    const unsigned l0 = (unsigned)__builtin_amdgcn_readfirstlane(lw.x), l1 = (unsigned)__builtin_amdgcn_readfirstlane(lw.y),
                   l2 = (unsigned)__builtin_amdgcn_readfirstlane(lw.z);
    if (max(max(l0, l1), l2) != 0xFFFFFFFFu) { tm = l0; dd = l1; rr = l2; }
  }
}

__device__ __forceinline__ void rec_fin(unsigned char* ws, const Pool& pool, int layer, int c16, int xs, RecTune tn, unsigned long long* stamps) {
  const Clk0 c0 = clk_sample();
  const unsigned lds0 = (unsigned)(uintptr_t)(__attribute__((address_space(3))) unsigned char*)dyn_lds;
  const int lane = lane_id(), g = lane >> 4, r = lane & 15;
  const v4u rs = make_rs(ws, (unsigned)OFF_END);
  v8u ad;
  ad[0] = lane * 16;
  ad[1] = g * 256 + r * 8;
  ad[2] = (r & 1) ? 0x03020706u : 0x05040100u;
  ad[3] = (lane & 31) * 64;
  ad[4] = W_MEM + copy_off(((r >> 1) & 1) * 5) + (c16 >> 3) * WIN + (c16 & 7) * 256 + (2 * (2 * g + (r & 1)) + (r >> 3)) * 16 + ((r >> 2) & 1) * 8;
  ad[5] = lane == 0 ? M_STAMP + c16 * 64 : OOB;
  ad[6] = (r & 1) ? 0xEEEEu : 0x4444u;
  ad[7] = lds0 + L_T;
  __builtin_amdgcn_s_sleep(64);
  v4u clk = clk_init(c0);
  unsigned long long stat;
  asm volatile(FIN_ASM
               : "=&{s[76:77]}"(stat), "+{s[56:59]}"(clk), "+{v[16:23]}"(ad)
               : [rs] "s"(rs), [sel] "s"(pool.sel), [pool] "s"(pool.base), [xs] "s"(xs), [lad0] "s"(ladder(xs, xs & 1)), [s3] "s"(pool.s3),
                 [su] "s"((int)OFF_UF + c16 * 32768), [yb] "s"(pool.base + P_Y + c16 * 16384), [ysel] "s"(pool.ysel), [tu] "s"(TTU),
                 [stl] "s"(lds0 + REC_ST - 32), [uf] "s"(kqf_off(layer, 0, 32))
               : FIN_CLOB RST_CLOB, "vcc", "scc", "memory");
}

constexpr int PJ_BUF = 43 * 1024, PJ_X = 16384, PJ_LDS = PJ_X + 3 * PJ_BUF;
#define KQ_ASM \
   "s_mov_b32 s44, %[rdy]\n\t" "s_mov_b32 s45, 0\n\t" "s_mov_b32 s46, 0\n\t" "s_cmp_eq_u32 s44, -1\n\t" "s_cbranch_scc1 68f\n\t" \
   "s_mul_i32 s52, s45, 1536\n\t" "s_mul_i32 s51, s45, 40960\n\t" "s_add_u32 m0, s46, %[ldx]\n\t" "s_mov_b32 s53, s46\n\t" \
   "buffer_load_dwordx4 v16, %[rs], s51 offen offset:0 sc1 lds\n\t" "buffer_load_dwordx4 v16, %[rs], s51 offen offset:1024 sc1 lds\n\t" \
   "buffer_load_dwordx4 v16, %[rs], s51 offen offset:2048 sc1 lds\n\t" "buffer_load_dwordx4 v16, %[rs], s51 offen offset:3072 sc1 lds\n\t" \
   "s_add_u32 m0, m0, 4096\n\t" "s_add_u32 s51, s51, 4096\n\t" "buffer_load_dwordx4 v16, %[rs], s51 offen offset:0 sc1 lds\n\t" \
   "buffer_load_dwordx4 v16, %[rs], s51 offen offset:1024 sc1 lds\n\t" "buffer_load_dwordx4 v16, %[rs], s51 offen offset:2048 sc1 lds\n\t" \
   "buffer_load_dwordx4 v16, %[rs], s51 offen offset:3072 sc1 lds\n\t" "s_add_u32 m0, m0, 4096\n\t" "s_add_u32 s51, s51, 4096\n\t" \
   "buffer_load_dwordx4 v16, %[rs], s51 offen offset:0 sc1 lds\n\t" "buffer_load_dwordx4 v16, %[rs], s51 offen offset:1024 sc1 lds\n\t" \
   "s_add_u32 m0, s53, %[ld1]\n\t" "s_nop 0\n\t" "buffer_load_dwordx4 v17, %[rs], s52 offen sc1 lds\n\t" "s_mov_b32 s45, 1\n\t" \
   "s_mov_b32 s46, 44032\n\t" "s_mul_i32 s52, s45, 1536\n\t" "s_mul_i32 s51, s45, 40960\n\t" "s_add_u32 m0, s46, %[ldx]\n\t" "s_mov_b32 s53, s46\n\t" \
   "buffer_load_dwordx4 v16, %[rs], s51 offen offset:0 sc1 lds\n\t" "buffer_load_dwordx4 v16, %[rs], s51 offen offset:1024 sc1 lds\n\t" \
   "buffer_load_dwordx4 v16, %[rs], s51 offen offset:2048 sc1 lds\n\t" "buffer_load_dwordx4 v16, %[rs], s51 offen offset:3072 sc1 lds\n\t" \
   "s_add_u32 m0, m0, 4096\n\t" "s_add_u32 s51, s51, 4096\n\t" "buffer_load_dwordx4 v16, %[rs], s51 offen offset:0 sc1 lds\n\t" \
   "buffer_load_dwordx4 v16, %[rs], s51 offen offset:1024 sc1 lds\n\t" "buffer_load_dwordx4 v16, %[rs], s51 offen offset:2048 sc1 lds\n\t" \
   "buffer_load_dwordx4 v16, %[rs], s51 offen offset:3072 sc1 lds\n\t" "s_add_u32 m0, m0, 4096\n\t" "s_add_u32 s51, s51, 4096\n\t" \
   "buffer_load_dwordx4 v16, %[rs], s51 offen offset:0 sc1 lds\n\t" "buffer_load_dwordx4 v16, %[rs], s51 offen offset:1024 sc1 lds\n\t" \
   "s_add_u32 m0, s53, %[ld1]\n\t" "s_nop 0\n\t" "buffer_load_dwordx4 v17, %[rs], s52 offen sc1 lds\n\t" "68:\n\t" "v_mov_b32 v138, 0\n\t" \
   "v_mov_b32 v13, %[flw]\n\t" "v_and_b32 v12, -16, v13\n\t" "ds_write_b32 v13, v138\n\t" "ds_read_b128 v[32:35], v26 offset:0\n\t" \
   "ds_read_b128 v[36:39], v26 offset:16\n\t" "ds_read_b128 v[40:43], v26 offset:32\n\t" "ds_read_b128 v[44:47], v26 offset:48\n\t" \
   "ds_read_b128 v[48:51], v26 offset:64\n\t" "ds_read_b128 v[52:55], v26 offset:80\n\t" "ds_read_b128 v[56:59], v26 offset:96\n\t" \
   "ds_read_b128 v[60:63], v26 offset:112\n\t" "s_mov_b32 s40, 0\n\t" "s_mov_b32 s41, 0\n\t" "s_mov_b32 s42, 0\n\t" "s_mov_b32 s43, 44032\n\t" \
   "v_mov_b32 v28, v22\n\t" "v_mov_b32 v29, v23\n\t" "s_mov_b32 s45, 2\n\t" "s_mov_b32 s46, 88064\n\t" "s_waitcnt vmcnt(11) lgkmcnt(0)\n\t" \
   "s_barrier\n\t" "s_memrealtime s[54:55]\n\t" "s_lshr_b32 s48, s45, 1\n\t" "s_bitcmp1_b32 s44, s48\n\t" "s_cbranch_scc0 172f\n\t" "72:\n\t" \
   "ds_read_b128 v[160:163], v28 offset:0\n\t" "ds_read_b128 v[164:167], v28 offset:512\n\t" "ds_read_b128 v[168:171], v28 offset:1024\n\t" \
   "ds_read_b128 v[172:175], v28 offset:1536\n\t" "s_waitcnt lgkmcnt(0)\n\t" "v_mov_b32 v31, s54\n\t" "v_min_u32 v31, -2, v31\n\t" "1:\n\t" \
   "v_mov_b32 v26, v28\n\t" "v_mov_b32 v27, v29\n\t" "s_waitcnt lgkmcnt(2)\n\t" "v_mfma_f32_32x32x16_bf16 v[64:79], a[0:3], v[160:163], 0\n\t" \
   "ds_read_b128 v[176:179], v26 offset:2048\n\t" "v_mfma_f32_32x32x16_bf16 v[80:95], a[8:11], v[160:163], 0\n\t" "v_mul_f32 v11, 0x3e0293ee, v10\n\t" \
   "s_lshl_b32 s47, s41, 13\n\t" "s_sub_i32 s47, s47, 8192\n\t" "s_max_i32 s47, s47, 0\n\t" "s_cmp_eq_u32 s40, 0\n\t" \
   "s_cselect_b32 s48, 0x7ffffff0, 0\n\t" "v_mfma_f32_32x32x16_bf16 v[64:79], a[4:7], v[164:167], v[64:79]\n\t" \
   "ds_read_b128 v[180:183], v26 offset:2560\n\t" "v_or_b32 v30, s48, v21\n\t" "v_fmac_f32 v96, v32, v8\n\t" "v_fmac_f32 v97, v33, v8\n\t" \
   "v_fmac_f32 v98, v34, v8\n\t" "v_mfma_f32_32x32x16_bf16 v[80:95], a[12:15], v[164:167], v[80:95]\n\t" "v_fmac_f32 v99, v35, v8\n\t" \
   "v_fmac_f32 v100, v36, v8\n\t" "v_fmac_f32 v101, v37, v8\n\t" "v_fmac_f32 v102, v38, v8\n\t" "v_fmac_f32 v103, v39, v8\n\t" \
   "s_waitcnt lgkmcnt(2)\n\t" "v_mfma_f32_32x32x16_bf16 v[64:79], a[16:19], v[168:171], v[64:79]\n\t" "ds_read_b128 v[184:187], v26 offset:3072\n\t" \
   "v_fmac_f32 v104, v40, v8\n\t" "v_fmac_f32 v105, v41, v8\n\t" "v_fmac_f32 v106, v42, v8\n\t" "v_fmac_f32 v107, v43, v8\n\t" \
   "v_mfma_f32_32x32x16_bf16 v[80:95], a[24:27], v[168:171], v[80:95]\n\t" "v_fmac_f32 v108, v44, v8\n\t" "v_fmac_f32 v109, v45, v8\n\t" \
   "v_fmac_f32 v110, v46, v8\n\t" "v_fmac_f32 v111, v47, v8\n\t" "v_mul_f32 v96, v96, v11\n\t" "v_mul_f32 v97, v97, v11\n\t" \
   "v_mfma_f32_32x32x16_bf16 v[64:79], a[20:23], v[172:175], v[64:79]\n\t" "ds_read_b128 v[188:191], v26 offset:3584\n\t" "v_mul_f32 v98, v98, v11\n\t" \
   "v_mul_f32 v99, v99, v11\n\t" "v_mul_f32 v100, v100, v11\n\t" "v_mul_f32 v101, v101, v11\n\t" \
   "v_mfma_f32_32x32x16_bf16 v[80:95], a[28:31], v[172:175], v[80:95]\n\t" "v_mul_f32 v102, v102, v11\n\t" "v_mul_f32 v103, v103, v11\n\t" \
   "v_mul_f32 v104, v104, v11\n\t" "v_mul_f32 v105, v105, v11\n\t" "v_mul_f32 v106, v106, v11\n\t" "s_waitcnt lgkmcnt(2)\n\t" \
   "v_mfma_f32_32x32x16_bf16 v[64:79], a[32:35], v[176:179], v[64:79]\n\t" "ds_read_b128 v[160:163], v26 offset:4096\n\t" \
   "v_mul_f32 v107, v107, v11\n\t" "v_mul_f32 v108, v108, v11\n\t" "v_mul_f32 v109, v109, v11\n\t" "v_mul_f32 v110, v110, v11\n\t" \
   "v_mfma_f32_32x32x16_bf16 v[80:95], a[40:43], v[176:179], v[80:95]\n\t" "v_mul_f32 v111, v111, v11\n\t" "s_add_u32 s48, s47, %[qf]\n\t" \
   "v_cvt_pk_bf16_f32 v96, v96, v97\n\t" "v_cvt_pk_bf16_f32 v97, v98, v99\n\t" "v_cvt_pk_bf16_f32 v98, v104, v105\n\t" \
   "v_cvt_pk_bf16_f32 v99, v106, v107\n\t" "v_mfma_f32_32x32x16_bf16 v[64:79], a[36:39], v[180:183], v[64:79]\n\t" \
   "ds_read_b128 v[164:167], v26 offset:4608\n\t" "v_mfma_f32_32x32x16_bf16 v[80:95], a[44:47], v[180:183], v[80:95]\n\t" \
   "buffer_store_dwordx4 v[96:99], v30, %[rs], s48 offen offset:0 sc1\n\t" "s_waitcnt lgkmcnt(2)\n\t" \
   "v_mfma_f32_32x32x16_bf16 v[64:79], a[48:51], v[184:187], v[64:79]\n\t" "ds_read_b128 v[168:171], v26 offset:5120\n\t" \
   "v_cvt_pk_bf16_f32 v100, v100, v101\n\t" "v_cvt_pk_bf16_f32 v101, v102, v103\n\t" "v_cvt_pk_bf16_f32 v102, v108, v109\n\t" \
   "v_cvt_pk_bf16_f32 v103, v110, v111\n\t" "v_mfma_f32_32x32x16_bf16 v[80:95], a[56:59], v[184:187], v[80:95]\n\t" \
   "buffer_store_dwordx4 v[100:103], v30, %[rs], s48 offen offset:512 sc1\n\t" "v_fmac_f32 v112, v48, v8\n\t" \
   "v_mfma_f32_32x32x16_bf16 v[64:79], a[52:55], v[188:191], v[64:79]\n\t" "ds_read_b128 v[172:175], v26 offset:5632\n\t" \
   "v_fmac_f32 v113, v49, v8\n\t" "v_fmac_f32 v114, v50, v8\n\t" "v_fmac_f32 v115, v51, v8\n\t" "v_fmac_f32 v116, v52, v8\n\t" \
   "v_mfma_f32_32x32x16_bf16 v[80:95], a[60:63], v[188:191], v[80:95]\n\t" "v_fmac_f32 v117, v53, v8\n\t" "v_fmac_f32 v118, v54, v8\n\t" \
   "v_fmac_f32 v119, v55, v8\n\t" "v_fmac_f32 v120, v56, v8\n\t" "v_fmac_f32 v121, v57, v8\n\t" "s_waitcnt lgkmcnt(2)\n\t" \
   "v_mfma_f32_32x32x16_bf16 v[64:79], a[64:67], v[160:163], v[64:79]\n\t" "ds_read_b128 v[176:179], v26 offset:6144\n\t" \
   "v_fmac_f32 v122, v58, v8\n\t" "v_fmac_f32 v123, v59, v8\n\t" "v_fmac_f32 v124, v60, v8\n\t" "v_fmac_f32 v125, v61, v8\n\t" \
   "v_mfma_f32_32x32x16_bf16 v[80:95], a[72:75], v[160:163], v[80:95]\n\t" "v_fmac_f32 v126, v62, v8\n\t" "v_fmac_f32 v127, v63, v8\n\t" \
   "v_mul_f32 v112, v112, v10\n\t" "v_mul_f32 v113, v113, v10\n\t" "v_mul_f32 v114, v114, v10\n\t" "v_mul_f32 v115, v115, v10\n\t" \
   "v_mfma_f32_32x32x16_bf16 v[64:79], a[68:71], v[164:167], v[64:79]\n\t" "ds_read_b128 v[180:183], v26 offset:6656\n\t" \
   "v_mul_f32 v116, v116, v10\n\t" "v_mul_f32 v117, v117, v10\n\t" "v_mul_f32 v118, v118, v10\n\t" "v_mul_f32 v119, v119, v10\n\t" \
   "v_mfma_f32_32x32x16_bf16 v[80:95], a[76:79], v[164:167], v[80:95]\n\t" "v_mul_f32 v120, v120, v10\n\t" "v_mul_f32 v121, v121, v10\n\t" \
   "v_mul_f32 v122, v122, v10\n\t" "v_mul_f32 v123, v123, v10\n\t" "v_mul_f32 v124, v124, v10\n\t" "s_waitcnt lgkmcnt(2)\n\t" \
   "v_mfma_f32_32x32x16_bf16 v[64:79], a[80:83], v[168:171], v[64:79]\n\t" "ds_read_b128 v[184:187], v26 offset:7168\n\t" \
   "v_mul_f32 v125, v125, v10\n\t" "v_mul_f32 v126, v126, v10\n\t" "v_mul_f32 v127, v127, v10\n\t" "s_add_u32 s48, s47, %[kf]\n\t" \
   "v_mfma_f32_32x32x16_bf16 v[80:95], a[88:91], v[168:171], v[80:95]\n\t" "v_cvt_pk_bf16_f32 v112, v112, v113\n\t" \
   "v_cvt_pk_bf16_f32 v113, v114, v115\n\t" "v_cvt_pk_bf16_f32 v114, v120, v121\n\t" "v_cvt_pk_bf16_f32 v115, v122, v123\n\t" \
   "ds_read_b128 v[128:131], v27 offset:0\n\t" "v_mfma_f32_32x32x16_bf16 v[64:79], a[84:87], v[172:175], v[64:79]\n\t" \
   "ds_read_b128 v[188:191], v26 offset:7680\n\t" "ds_read_b128 v[132:135], v27 offset:16\n\t" "ds_read_b64 v[136:137], v27 offset:32\n\t" \
   "v_mfma_f32_32x32x16_bf16 v[80:95], a[92:95], v[172:175], v[80:95]\n\t" "buffer_store_dwordx4 v[112:115], v30, %[rs], s48 offen offset:0 sc1\n\t" \
   "s_waitcnt lgkmcnt(5)\n\t" "v_mfma_f32_32x32x16_bf16 v[64:79], a[96:99], v[176:179], v[64:79]\n\t" "ds_read_b128 v[160:163], v26 offset:8192\n\t" \
   "v_cvt_pk_bf16_f32 v116, v116, v117\n\t" "v_cvt_pk_bf16_f32 v117, v118, v119\n\t" "v_cvt_pk_bf16_f32 v118, v124, v125\n\t" \
   "v_cvt_pk_bf16_f32 v119, v126, v127\n\t" "v_mfma_f32_32x32x16_bf16 v[80:95], a[104:107], v[176:179], v[80:95]\n\t" \
   "buffer_store_dwordx4 v[116:119], v30, %[rs], s48 offen offset:512 sc1\n\t" \
   "v_mfma_f32_32x32x16_bf16 v[64:79], a[100:103], v[180:183], v[64:79]\n\t" "ds_read_b128 v[164:167], v26 offset:8704\n\t" \
   "v_add_u32 v28, s43, v22\n\t" "v_add_u32 v29, s43, v23\n\t" "v_mfma_f32_32x32x16_bf16 v[80:95], a[108:111], v[180:183], v[80:95]\n\t" \
   "s_waitcnt lgkmcnt(4)\n\t" "v_mfma_f32_32x32x16_bf16 v[64:79], a[112:115], v[184:187], v[64:79]\n\t" "ds_read_b128 v[168:171], v26 offset:9216\n\t" \
   "v_mfma_f32_32x32x16_bf16 v[80:95], a[120:123], v[184:187], v[80:95]\n\t" "ds_read_b128 v[140:143], v12\n\t" "s_waitcnt lgkmcnt(4)\n\t" \
   "v_add_f32 v128, v128, v130\n\t" "v_add_f32 v129, v129, v131\n\t" "v_add_f32 v132, v132, v134\n\t" "v_add_f32 v133, v133, v135\n\t" \
   "v_mfma_f32_32x32x16_bf16 v[64:79], a[116:119], v[188:191], v[64:79]\n\t" "ds_read_b128 v[172:175], v26 offset:9728\n\t" \
   "v_add_f32 v128, v128, v136\n\t" "v_add_f32 v129, v129, v137\n\t" "v_add_f32 v128, v128, v132\n\t" "v_add_f32 v129, v129, v133\n\t" \
   "v_mfma_f32_32x32x16_bf16 v[80:95], a[124:127], v[188:191], v[80:95]\n\t" "v_mul_f32 v128, 0xbacccccd, v128\n\t" \
   "v_mul_f32 v129, 0x3acccccd, v129\n\t" "v_fma_f32 v129, -v128, v128, v129\n\t" "v_max_f32 v129, 0, v129\n\t" \
   "v_cvt_pk_bf16_f32 v129, v129, v128\n\t" "s_waitcnt lgkmcnt(3)\n\t" "v_mfma_f32_32x32x16_bf16 v[64:79], a[128:131], v[160:163], v[64:79]\n\t" \
   "ds_read_b128 v[176:179], v26 offset:10240\n\t" "s_waitcnt lgkmcnt(2)\n\t" "v_min3_u32 v140, v140, v141, v142\n\t" "v_min_u32 v140, v140, v143\n\t" \
   "v_cmp_ge_u32 vcc, v140, v138\n\t" "v_mfma_f32_32x32x16_bf16 v[80:95], a[136:139], v[160:163], v[80:95]\n\t" "s_cbranch_vccz 150f\n\t" "50:\n\t" \
   "s_mul_i32 s52, s45, 1536\n\t" "s_mul_i32 s51, s45, 40960\n\t" "s_add_u32 m0, s46, %[ldx]\n\t" "s_mov_b32 s53, s46\n\t" \
   "v_mfma_f32_32x32x16_bf16 v[64:79], a[132:135], v[164:167], v[64:79]\n\t" "ds_read_b128 v[180:183], v26 offset:10752\n\t" \
   "buffer_load_dwordx4 v16, %[rs], s51 offen offset:0 sc1 lds\n\t" "v_and_b32 v8, 0xffff0000, v129\n\t" \
   "v_mfma_f32_32x32x16_bf16 v[80:95], a[140:143], v[164:167], v[80:95]\n\t" "v_lshlrev_b32 v129, 16, v129\n\t" "v_add_f32 v129, 0x3727c5ac, v129\n\t" \
   "v_cvt_pk_bf16_f32 v129, v129, v129\n\t" "v_lshlrev_b32 v129, 16, v129\n\t" "s_waitcnt lgkmcnt(2)\n\t" \
   "v_mfma_f32_32x32x16_bf16 v[64:79], a[144:147], v[168:171], v[64:79]\n\t" "ds_read_b128 v[184:187], v26 offset:11264\n\t" \
   "v_sqrt_f32 v129, v129\n\t" "s_nop 0\n\t" "v_cvt_pk_bf16_f32 v129, v129, v129\n\t" \
   "v_mfma_f32_32x32x16_bf16 v[80:95], a[152:155], v[168:171], v[80:95]\n\t" "buffer_load_dwordx4 v16, %[rs], s51 offen offset:1024 sc1 lds\n\t" \
   "v_lshlrev_b32 v129, 16, v129\n\t" "v_rcp_f32 v10, v129\n\t" "v_mfma_f32_32x32x16_bf16 v[64:79], a[148:151], v[172:175], v[64:79]\n\t" \
   "ds_read_b128 v[188:191], v26 offset:11776\n\t" "v_mfma_f32_32x32x16_bf16 v[80:95], a[156:159], v[172:175], v[80:95]\n\t" "s_waitcnt lgkmcnt(2)\n\t" \
   "v_mfma_f32_32x32x16_bf16 v[64:79], a[160:163], v[176:179], v[64:79]\n\t" "ds_read_b128 v[160:163], v26 offset:12288\n\t" \
   "buffer_load_dwordx4 v16, %[rs], s51 offen offset:2048 sc1 lds\n\t" "v_mfma_f32_32x32x16_bf16 v[80:95], a[168:171], v[176:179], v[80:95]\n\t" \
   "v_mfma_f32_32x32x16_bf16 v[64:79], a[164:167], v[180:183], v[64:79]\n\t" "ds_read_b128 v[164:167], v26 offset:12800\n\t" \
   "v_mfma_f32_32x32x16_bf16 v[80:95], a[172:175], v[180:183], v[80:95]\n\t" "buffer_load_dwordx4 v16, %[rs], s51 offen offset:3072 sc1 lds\n\t" \
   "s_add_u32 m0, m0, 4096\n\t" "s_add_u32 s51, s51, 4096\n\t" "s_waitcnt lgkmcnt(2)\n\t" \
   "v_mfma_f32_32x32x16_bf16 v[64:79], a[176:179], v[184:187], v[64:79]\n\t" "ds_read_b128 v[168:171], v26 offset:13312\n\t" "s_waitcnt vmcnt(8)\n\t" \
   "v_add_u32 v138, 1, v138\n\t" "ds_write_b32 v13, v138\n\t" "v_mfma_f32_32x32x16_bf16 v[80:95], a[184:187], v[184:187], v[80:95]\n\t" \
   "v_mfma_f32_32x32x16_bf16 v[64:79], a[180:183], v[188:191], v[64:79]\n\t" "ds_read_b128 v[172:175], v26 offset:13824\n\t" \
   "buffer_load_dwordx4 v16, %[rs], s51 offen offset:0 sc1 lds\n\t" "v_mfma_f32_32x32x16_bf16 v[80:95], a[188:191], v[188:191], v[80:95]\n\t" \
   "s_waitcnt lgkmcnt(3)\n\t" "v_mfma_f32_32x32x16_bf16 v[64:79], a[192:195], v[160:163], v[64:79]\n\t" "ds_read_b128 v[176:179], v26 offset:14336\n\t" \
   "v_mfma_f32_32x32x16_bf16 v[80:95], a[200:203], v[160:163], v[80:95]\n\t" "buffer_load_dwordx4 v16, %[rs], s51 offen offset:1024 sc1 lds\n\t" \
   "v_mfma_f32_32x32x16_bf16 v[64:79], a[196:199], v[164:167], v[64:79]\n\t" "ds_read_b128 v[180:183], v26 offset:14848\n\t" \
   "v_mfma_f32_32x32x16_bf16 v[80:95], a[204:207], v[164:167], v[80:95]\n\t" "s_waitcnt lgkmcnt(2)\n\t" \
   "v_mfma_f32_32x32x16_bf16 v[64:79], a[208:211], v[168:171], v[64:79]\n\t" "ds_read_b128 v[184:187], v26 offset:15360\n\t" \
   "buffer_load_dwordx4 v16, %[rs], s51 offen offset:2048 sc1 lds\n\t" "v_mfma_f32_32x32x16_bf16 v[80:95], a[216:219], v[168:171], v[80:95]\n\t" \
   "v_mfma_f32_32x32x16_bf16 v[64:79], a[212:215], v[172:175], v[64:79]\n\t" "ds_read_b128 v[188:191], v26 offset:15872\n\t" \
   "v_mfma_f32_32x32x16_bf16 v[80:95], a[220:223], v[172:175], v[80:95]\n\t" "buffer_load_dwordx4 v16, %[rs], s51 offen offset:3072 sc1 lds\n\t" \
   "s_add_u32 m0, m0, 4096\n\t" "s_add_u32 s51, s51, 4096\n\t" "s_waitcnt lgkmcnt(2)\n\t" \
   "v_mfma_f32_32x32x16_bf16 v[64:79], a[224:227], v[176:179], v[64:79]\n\t" "ds_read_b128 v[160:163], v26 offset:16384\n\t" \
   "v_mfma_f32_32x32x16_bf16 v[80:95], a[232:235], v[176:179], v[80:95]\n\t" "v_mfma_f32_32x32x16_bf16 v[64:79], a[228:231], v[180:183], v[64:79]\n\t" \
   "ds_read_b128 v[164:167], v26 offset:16896\n\t" "buffer_load_dwordx4 v16, %[rs], s51 offen offset:0 sc1 lds\n\t" \
   "v_mfma_f32_32x32x16_bf16 v[80:95], a[236:239], v[180:183], v[80:95]\n\t" "s_waitcnt lgkmcnt(2)\n\t" \
   "v_mfma_f32_32x32x16_bf16 v[64:79], a[240:243], v[184:187], v[64:79]\n\t" "ds_read_b128 v[168:171], v26 offset:17408\n\t" "s_cmp_lt_i32 s40, 1\n\t" \
   "s_cselect_b32 s49, 0x7ffffff0, 0\n\t" "v_or_b32 v9, s49, v20\n\t" "s_sub_i32 s49, s41, 1\n\t" \
   "v_mfma_f32_32x32x16_bf16 v[80:95], a[248:251], v[184:187], v[80:95]\n\t" "s_max_i32 s49, s49, 0\n\t" "s_lshl_b32 s49, s49, 12\n\t" \
   "s_waitcnt vmcnt(9)\n\t" "buffer_store_dword v31, v9, %[rs], s49 offen sc1\n\t" \
   "v_mfma_f32_32x32x16_bf16 v[64:79], a[244:247], v[188:191], v[64:79]\n\t" "ds_read_b128 v[172:175], v26 offset:17920\n\t" \
   "v_mfma_f32_32x32x16_bf16 v[80:95], a[252:255], v[188:191], v[80:95]\n\t" "buffer_load_dwordx4 v16, %[rs], s51 offen offset:1024 sc1 lds\n\t" \
   "s_add_u32 m0, s53, %[ld1]\n\t" "s_nop 0\n\t" "s_waitcnt lgkmcnt(2)\n\t" "v_mfma_f32_32x32x16_bf16 v[64:79], v[192:195], v[160:163], v[64:79]\n\t" \
   "ds_read_b128 v[176:179], v26 offset:18432\n\t" "buffer_load_dwordx4 v17, %[rs], s52 offen sc1 lds\n\t" "ds_read_b128 v[140:143], v12\n\t" \
   "v_mfma_f32_32x32x16_bf16 v[80:95], v[200:203], v[160:163], v[80:95]\n\t" "v_mfma_f32_32x32x16_bf16 v[64:79], v[196:199], v[164:167], v[64:79]\n\t" \
   "ds_read_b128 v[180:183], v26 offset:18944\n\t" "v_mfma_f32_32x32x16_bf16 v[80:95], v[204:207], v[164:167], v[80:95]\n\t" "s_waitcnt lgkmcnt(3)\n\t" \
   "v_mfma_f32_32x32x16_bf16 v[64:79], v[208:211], v[168:171], v[64:79]\n\t" "ds_read_b128 v[184:187], v26 offset:19456\n\t" "s_waitcnt lgkmcnt(2)\n\t" \
   "v_min3_u32 v140, v140, v141, v142\n\t" "v_min_u32 v140, v140, v143\n\t" "v_cmp_ge_u32 vcc, v140, v138\n\t" \
   "v_mfma_f32_32x32x16_bf16 v[80:95], v[216:219], v[168:171], v[80:95]\n\t" "s_cbranch_vccz 152f\n\t" "52:\n\t" \
   "v_mfma_f32_32x32x16_bf16 v[64:79], v[212:215], v[172:175], v[64:79]\n\t" "ds_read_b128 v[188:191], v26 offset:19968\n\t" \
   "v_mfma_f32_32x32x16_bf16 v[80:95], v[220:223], v[172:175], v[80:95]\n\t" "v_add_u32 v138, 1, v138\n\t" "ds_write_b32 v13, v138\n\t" \
   "s_waitcnt lgkmcnt(3)\n\t" "v_mfma_f32_32x32x16_bf16 v[64:79], v[224:227], v[176:179], v[64:79]\n\t" "ds_read_b128 v[160:163], v28 offset:0\n\t" \
   "s_add_u32 s45, s40, 3\n\t" "s_min_u32 s45, s45, 63\n\t" "s_mov_b32 s46, s42\n\t" \
   "v_mfma_f32_32x32x16_bf16 v[80:95], v[232:235], v[176:179], v[80:95]\n\t" "s_lshr_b32 s48, s45, 1\n\t" "s_bitcmp1_b32 s44, s48\n\t" \
   "s_cbranch_scc0 174f\n\t" "74:\n\t" "s_mov_b32 s42, s43\n\t" "s_add_u32 s43, s43, 44032\n\t" \
   "v_mfma_f32_32x32x16_bf16 v[64:79], v[228:231], v[180:183], v[64:79]\n\t" "ds_read_b128 v[164:167], v28 offset:512\n\t" \
   "s_cmp_eq_u32 s43, 132096\n\t" "s_cselect_b32 s43, 0, s43\n\t" "s_add_u32 s41, s41, 1\n\t" "s_add_u32 s40, s40, 1\n\t" \
   "v_mfma_f32_32x32x16_bf16 v[80:95], v[236:239], v[180:183], v[80:95]\n\t" "s_waitcnt lgkmcnt(3)\n\t" \
   "v_mfma_f32_32x32x16_bf16 v[64:79], v[240:243], v[184:187], v[64:79]\n\t" "ds_read_b128 v[168:171], v28 offset:1024\n\t" \
   "v_mfma_f32_32x32x16_bf16 v[80:95], v[248:251], v[184:187], v[80:95]\n\t" "v_mfma_f32_32x32x16_bf16 v[64:79], v[244:247], v[188:191], v[64:79]\n\t" \
   "ds_read_b128 v[172:175], v28 offset:1536\n\t" "v_mfma_f32_32x32x16_bf16 v[80:95], v[252:255], v[188:191], v[80:95]\n\t" "v_mov_b32 v26, v28\n\t" \
   "v_mov_b32 v27, v29\n\t" "s_waitcnt lgkmcnt(2)\n\t" "v_mfma_f32_32x32x16_bf16 v[96:111], a[0:3], v[160:163], 0\n\t" \
   "ds_read_b128 v[176:179], v26 offset:2048\n\t" "v_mfma_f32_32x32x16_bf16 v[112:127], a[8:11], v[160:163], 0\n\t" \
   "v_mul_f32 v11, 0x3e0293ee, v10\n\t" "s_lshl_b32 s47, s41, 13\n\t" "s_sub_i32 s47, s47, 8192\n\t" "v_fmac_f32 v64, v32, v8\n\t" \
   "v_fmac_f32 v65, v33, v8\n\t" "v_fmac_f32 v66, v34, v8\n\t" "v_mfma_f32_32x32x16_bf16 v[96:111], a[4:7], v[164:167], v[96:111]\n\t" \
   "ds_read_b128 v[180:183], v26 offset:2560\n\t" "v_fmac_f32 v67, v35, v8\n\t" "v_fmac_f32 v68, v36, v8\n\t" "v_fmac_f32 v69, v37, v8\n\t" \
   "v_fmac_f32 v70, v38, v8\n\t" "v_mfma_f32_32x32x16_bf16 v[112:127], a[12:15], v[164:167], v[112:127]\n\t" "v_fmac_f32 v71, v39, v8\n\t" \
   "v_fmac_f32 v72, v40, v8\n\t" "v_fmac_f32 v73, v41, v8\n\t" "v_fmac_f32 v74, v42, v8\n\t" "v_fmac_f32 v75, v43, v8\n\t" "s_waitcnt lgkmcnt(2)\n\t" \
   "v_mfma_f32_32x32x16_bf16 v[96:111], a[16:19], v[168:171], v[96:111]\n\t" "ds_read_b128 v[184:187], v26 offset:3072\n\t" \
   "v_fmac_f32 v76, v44, v8\n\t" "v_fmac_f32 v77, v45, v8\n\t" "v_fmac_f32 v78, v46, v8\n\t" "v_fmac_f32 v79, v47, v8\n\t" \
   "v_mfma_f32_32x32x16_bf16 v[112:127], a[24:27], v[168:171], v[112:127]\n\t" "v_mul_f32 v64, v64, v11\n\t" "v_mul_f32 v65, v65, v11\n\t" \
   "v_mul_f32 v66, v66, v11\n\t" "v_mul_f32 v67, v67, v11\n\t" "v_mul_f32 v68, v68, v11\n\t" "v_mul_f32 v69, v69, v11\n\t" \
   "v_mfma_f32_32x32x16_bf16 v[96:111], a[20:23], v[172:175], v[96:111]\n\t" "ds_read_b128 v[188:191], v26 offset:3584\n\t" \
   "v_mul_f32 v70, v70, v11\n\t" "v_mul_f32 v71, v71, v11\n\t" "v_mul_f32 v72, v72, v11\n\t" "v_mul_f32 v73, v73, v11\n\t" \
   "v_mfma_f32_32x32x16_bf16 v[112:127], a[28:31], v[172:175], v[112:127]\n\t" "v_mul_f32 v74, v74, v11\n\t" "v_mul_f32 v75, v75, v11\n\t" \
   "v_mul_f32 v76, v76, v11\n\t" "v_mul_f32 v77, v77, v11\n\t" "v_mul_f32 v78, v78, v11\n\t" "s_waitcnt lgkmcnt(2)\n\t" \
   "v_mfma_f32_32x32x16_bf16 v[96:111], a[32:35], v[176:179], v[96:111]\n\t" "ds_read_b128 v[160:163], v26 offset:4096\n\t" \
   "v_mul_f32 v79, v79, v11\n\t" "s_add_u32 s48, s47, %[qf]\n\t" "v_cvt_pk_bf16_f32 v64, v64, v65\n\t" "v_cvt_pk_bf16_f32 v65, v66, v67\n\t" \
   "v_mfma_f32_32x32x16_bf16 v[112:127], a[40:43], v[176:179], v[112:127]\n\t" "v_cvt_pk_bf16_f32 v66, v72, v73\n\t" \
   "v_cvt_pk_bf16_f32 v67, v74, v75\n\t" "v_mfma_f32_32x32x16_bf16 v[96:111], a[36:39], v[180:183], v[96:111]\n\t" \
   "ds_read_b128 v[164:167], v26 offset:4608\n\t" "v_mfma_f32_32x32x16_bf16 v[112:127], a[44:47], v[180:183], v[112:127]\n\t" \
   "buffer_store_dwordx4 v[64:67], v21, %[rs], s48 offen offset:0 sc1\n\t" "s_waitcnt lgkmcnt(2)\n\t" \
   "v_mfma_f32_32x32x16_bf16 v[96:111], a[48:51], v[184:187], v[96:111]\n\t" "ds_read_b128 v[168:171], v26 offset:5120\n\t" \
   "v_cvt_pk_bf16_f32 v68, v68, v69\n\t" "v_cvt_pk_bf16_f32 v69, v70, v71\n\t" "v_cvt_pk_bf16_f32 v70, v76, v77\n\t" \
   "v_cvt_pk_bf16_f32 v71, v78, v79\n\t" "v_mfma_f32_32x32x16_bf16 v[112:127], a[56:59], v[184:187], v[112:127]\n\t" \
   "buffer_store_dwordx4 v[68:71], v21, %[rs], s48 offen offset:512 sc1\n\t" "v_fmac_f32 v80, v48, v8\n\t" \
   "v_mfma_f32_32x32x16_bf16 v[96:111], a[52:55], v[188:191], v[96:111]\n\t" "ds_read_b128 v[172:175], v26 offset:5632\n\t" \
   "v_fmac_f32 v81, v49, v8\n\t" "v_fmac_f32 v82, v50, v8\n\t" "v_fmac_f32 v83, v51, v8\n\t" "v_fmac_f32 v84, v52, v8\n\t" \
   "v_mfma_f32_32x32x16_bf16 v[112:127], a[60:63], v[188:191], v[112:127]\n\t" "v_fmac_f32 v85, v53, v8\n\t" "v_fmac_f32 v86, v54, v8\n\t" \
   "v_fmac_f32 v87, v55, v8\n\t" "v_fmac_f32 v88, v56, v8\n\t" "v_fmac_f32 v89, v57, v8\n\t" "s_waitcnt lgkmcnt(2)\n\t" \
   "v_mfma_f32_32x32x16_bf16 v[96:111], a[64:67], v[160:163], v[96:111]\n\t" "ds_read_b128 v[176:179], v26 offset:6144\n\t" \
   "v_fmac_f32 v90, v58, v8\n\t" "v_fmac_f32 v91, v59, v8\n\t" "v_fmac_f32 v92, v60, v8\n\t" "v_fmac_f32 v93, v61, v8\n\t" \
   "v_mfma_f32_32x32x16_bf16 v[112:127], a[72:75], v[160:163], v[112:127]\n\t" "v_fmac_f32 v94, v62, v8\n\t" "v_fmac_f32 v95, v63, v8\n\t" \
   "v_mul_f32 v80, v80, v10\n\t" "v_mul_f32 v81, v81, v10\n\t" "v_mul_f32 v82, v82, v10\n\t" "v_mul_f32 v83, v83, v10\n\t" \
   "v_mfma_f32_32x32x16_bf16 v[96:111], a[68:71], v[164:167], v[96:111]\n\t" "ds_read_b128 v[180:183], v26 offset:6656\n\t" \
   "v_mul_f32 v84, v84, v10\n\t" "v_mul_f32 v85, v85, v10\n\t" "v_mul_f32 v86, v86, v10\n\t" "v_mul_f32 v87, v87, v10\n\t" \
   "v_mfma_f32_32x32x16_bf16 v[112:127], a[76:79], v[164:167], v[112:127]\n\t" "v_mul_f32 v88, v88, v10\n\t" "v_mul_f32 v89, v89, v10\n\t" \
   "v_mul_f32 v90, v90, v10\n\t" "v_mul_f32 v91, v91, v10\n\t" "v_mul_f32 v92, v92, v10\n\t" "s_waitcnt lgkmcnt(2)\n\t" \
   "v_mfma_f32_32x32x16_bf16 v[96:111], a[80:83], v[168:171], v[96:111]\n\t" "ds_read_b128 v[184:187], v26 offset:7168\n\t" \
   "v_mul_f32 v93, v93, v10\n\t" "v_mul_f32 v94, v94, v10\n\t" "v_mul_f32 v95, v95, v10\n\t" "s_add_u32 s48, s47, %[kf]\n\t" \
   "v_mfma_f32_32x32x16_bf16 v[112:127], a[88:91], v[168:171], v[112:127]\n\t" "v_cvt_pk_bf16_f32 v80, v80, v81\n\t" \
   "v_cvt_pk_bf16_f32 v81, v82, v83\n\t" "v_cvt_pk_bf16_f32 v82, v88, v89\n\t" "v_cvt_pk_bf16_f32 v83, v90, v91\n\t" \
   "ds_read_b128 v[128:131], v27 offset:0\n\t" "v_mfma_f32_32x32x16_bf16 v[96:111], a[84:87], v[172:175], v[96:111]\n\t" \
   "ds_read_b128 v[188:191], v26 offset:7680\n\t" "ds_read_b128 v[132:135], v27 offset:16\n\t" "ds_read_b64 v[136:137], v27 offset:32\n\t" \
   "v_mfma_f32_32x32x16_bf16 v[112:127], a[92:95], v[172:175], v[112:127]\n\t" "buffer_store_dwordx4 v[80:83], v21, %[rs], s48 offen offset:0 sc1\n\t" \
   "s_waitcnt lgkmcnt(5)\n\t" "v_mfma_f32_32x32x16_bf16 v[96:111], a[96:99], v[176:179], v[96:111]\n\t" "ds_read_b128 v[160:163], v26 offset:8192\n\t" \
   "v_cvt_pk_bf16_f32 v84, v84, v85\n\t" "v_cvt_pk_bf16_f32 v85, v86, v87\n\t" "v_cvt_pk_bf16_f32 v86, v92, v93\n\t" \
   "v_cvt_pk_bf16_f32 v87, v94, v95\n\t" "v_mfma_f32_32x32x16_bf16 v[112:127], a[104:107], v[176:179], v[112:127]\n\t" \
   "buffer_store_dwordx4 v[84:87], v21, %[rs], s48 offen offset:512 sc1\n\t" \
   "v_mfma_f32_32x32x16_bf16 v[96:111], a[100:103], v[180:183], v[96:111]\n\t" "ds_read_b128 v[164:167], v26 offset:8704\n\t" \
   "v_add_u32 v28, s43, v22\n\t" "v_add_u32 v29, s43, v23\n\t" "v_mfma_f32_32x32x16_bf16 v[112:127], a[108:111], v[180:183], v[112:127]\n\t" \
   "s_waitcnt lgkmcnt(4)\n\t" "v_mfma_f32_32x32x16_bf16 v[96:111], a[112:115], v[184:187], v[96:111]\n\t" \
   "ds_read_b128 v[168:171], v26 offset:9216\n\t" "v_mfma_f32_32x32x16_bf16 v[112:127], a[120:123], v[184:187], v[112:127]\n\t" \
   "ds_read_b128 v[140:143], v12\n\t" "s_waitcnt lgkmcnt(4)\n\t" "v_add_f32 v128, v128, v130\n\t" "v_add_f32 v129, v129, v131\n\t" \
   "v_add_f32 v132, v132, v134\n\t" "v_add_f32 v133, v133, v135\n\t" "v_mfma_f32_32x32x16_bf16 v[96:111], a[116:119], v[188:191], v[96:111]\n\t" \
   "ds_read_b128 v[172:175], v26 offset:9728\n\t" "v_add_f32 v128, v128, v136\n\t" "v_add_f32 v129, v129, v137\n\t" "v_add_f32 v128, v128, v132\n\t" \
   "v_add_f32 v129, v129, v133\n\t" "v_mfma_f32_32x32x16_bf16 v[112:127], a[124:127], v[188:191], v[112:127]\n\t" \
   "v_mul_f32 v128, 0xbacccccd, v128\n\t" "v_mul_f32 v129, 0x3acccccd, v129\n\t" "v_fma_f32 v129, -v128, v128, v129\n\t" "v_max_f32 v129, 0, v129\n\t" \
   "v_cvt_pk_bf16_f32 v129, v129, v128\n\t" "s_waitcnt lgkmcnt(3)\n\t" "v_mfma_f32_32x32x16_bf16 v[96:111], a[128:131], v[160:163], v[96:111]\n\t" \
   "ds_read_b128 v[176:179], v26 offset:10240\n\t" "s_waitcnt lgkmcnt(2)\n\t" "v_min3_u32 v140, v140, v141, v142\n\t" "v_min_u32 v140, v140, v143\n\t" \
   "v_cmp_ge_u32 vcc, v140, v138\n\t" "v_mfma_f32_32x32x16_bf16 v[112:127], a[136:139], v[160:163], v[112:127]\n\t" "s_cbranch_vccz 151f\n\t" "51:\n\t" \
   "s_mul_i32 s52, s45, 1536\n\t" "s_mul_i32 s51, s45, 40960\n\t" "s_add_u32 m0, s46, %[ldx]\n\t" "s_mov_b32 s53, s46\n\t" \
   "v_mfma_f32_32x32x16_bf16 v[96:111], a[132:135], v[164:167], v[96:111]\n\t" "ds_read_b128 v[180:183], v26 offset:10752\n\t" \
   "buffer_load_dwordx4 v16, %[rs], s51 offen offset:0 sc1 lds\n\t" "v_and_b32 v8, 0xffff0000, v129\n\t" \
   "v_mfma_f32_32x32x16_bf16 v[112:127], a[140:143], v[164:167], v[112:127]\n\t" "v_lshlrev_b32 v129, 16, v129\n\t" \
   "v_add_f32 v129, 0x3727c5ac, v129\n\t" "v_cvt_pk_bf16_f32 v129, v129, v129\n\t" "v_lshlrev_b32 v129, 16, v129\n\t" "s_waitcnt lgkmcnt(2)\n\t" \
   "v_mfma_f32_32x32x16_bf16 v[96:111], a[144:147], v[168:171], v[96:111]\n\t" "ds_read_b128 v[184:187], v26 offset:11264\n\t" \
   "v_sqrt_f32 v129, v129\n\t" "s_nop 0\n\t" "v_cvt_pk_bf16_f32 v129, v129, v129\n\t" \
   "v_mfma_f32_32x32x16_bf16 v[112:127], a[152:155], v[168:171], v[112:127]\n\t" "buffer_load_dwordx4 v16, %[rs], s51 offen offset:1024 sc1 lds\n\t" \
   "v_lshlrev_b32 v129, 16, v129\n\t" "v_rcp_f32 v10, v129\n\t" "v_mfma_f32_32x32x16_bf16 v[96:111], a[148:151], v[172:175], v[96:111]\n\t" \
   "ds_read_b128 v[188:191], v26 offset:11776\n\t" "v_mfma_f32_32x32x16_bf16 v[112:127], a[156:159], v[172:175], v[112:127]\n\t" \
   "s_waitcnt lgkmcnt(2)\n\t" "v_mfma_f32_32x32x16_bf16 v[96:111], a[160:163], v[176:179], v[96:111]\n\t" \
   "ds_read_b128 v[160:163], v26 offset:12288\n\t" "buffer_load_dwordx4 v16, %[rs], s51 offen offset:2048 sc1 lds\n\t" \
   "v_mfma_f32_32x32x16_bf16 v[112:127], a[168:171], v[176:179], v[112:127]\n\t" \
   "v_mfma_f32_32x32x16_bf16 v[96:111], a[164:167], v[180:183], v[96:111]\n\t" "ds_read_b128 v[164:167], v26 offset:12800\n\t" \
   "v_mfma_f32_32x32x16_bf16 v[112:127], a[172:175], v[180:183], v[112:127]\n\t" "buffer_load_dwordx4 v16, %[rs], s51 offen offset:3072 sc1 lds\n\t" \
   "s_add_u32 m0, m0, 4096\n\t" "s_add_u32 s51, s51, 4096\n\t" "s_waitcnt lgkmcnt(2)\n\t" \
   "v_mfma_f32_32x32x16_bf16 v[96:111], a[176:179], v[184:187], v[96:111]\n\t" "ds_read_b128 v[168:171], v26 offset:13312\n\t" "s_waitcnt vmcnt(8)\n\t" \
   "v_add_u32 v138, 1, v138\n\t" "ds_write_b32 v13, v138\n\t" "v_mfma_f32_32x32x16_bf16 v[112:127], a[184:187], v[184:187], v[112:127]\n\t" \
   "v_mfma_f32_32x32x16_bf16 v[96:111], a[180:183], v[188:191], v[96:111]\n\t" "ds_read_b128 v[172:175], v26 offset:13824\n\t" \
   "buffer_load_dwordx4 v16, %[rs], s51 offen offset:0 sc1 lds\n\t" "v_mfma_f32_32x32x16_bf16 v[112:127], a[188:191], v[188:191], v[112:127]\n\t" \
   "s_waitcnt lgkmcnt(3)\n\t" "v_mfma_f32_32x32x16_bf16 v[96:111], a[192:195], v[160:163], v[96:111]\n\t" \
   "ds_read_b128 v[176:179], v26 offset:14336\n\t" "v_mfma_f32_32x32x16_bf16 v[112:127], a[200:203], v[160:163], v[112:127]\n\t" \
   "buffer_load_dwordx4 v16, %[rs], s51 offen offset:1024 sc1 lds\n\t" "v_mfma_f32_32x32x16_bf16 v[96:111], a[196:199], v[164:167], v[96:111]\n\t" \
   "ds_read_b128 v[180:183], v26 offset:14848\n\t" "v_mfma_f32_32x32x16_bf16 v[112:127], a[204:207], v[164:167], v[112:127]\n\t" \
   "s_waitcnt lgkmcnt(2)\n\t" "v_mfma_f32_32x32x16_bf16 v[96:111], a[208:211], v[168:171], v[96:111]\n\t" \
   "ds_read_b128 v[184:187], v26 offset:15360\n\t" "buffer_load_dwordx4 v16, %[rs], s51 offen offset:2048 sc1 lds\n\t" \
   "v_mfma_f32_32x32x16_bf16 v[112:127], a[216:219], v[168:171], v[112:127]\n\t" \
   "v_mfma_f32_32x32x16_bf16 v[96:111], a[212:215], v[172:175], v[96:111]\n\t" "ds_read_b128 v[188:191], v26 offset:15872\n\t" \
   "v_mfma_f32_32x32x16_bf16 v[112:127], a[220:223], v[172:175], v[112:127]\n\t" "buffer_load_dwordx4 v16, %[rs], s51 offen offset:3072 sc1 lds\n\t" \
   "s_add_u32 m0, m0, 4096\n\t" "s_add_u32 s51, s51, 4096\n\t" "s_waitcnt lgkmcnt(2)\n\t" \
   "v_mfma_f32_32x32x16_bf16 v[96:111], a[224:227], v[176:179], v[96:111]\n\t" "ds_read_b128 v[160:163], v26 offset:16384\n\t" \
   "v_mfma_f32_32x32x16_bf16 v[112:127], a[232:235], v[176:179], v[112:127]\n\t" \
   "v_mfma_f32_32x32x16_bf16 v[96:111], a[228:231], v[180:183], v[96:111]\n\t" "ds_read_b128 v[164:167], v26 offset:16896\n\t" \
   "buffer_load_dwordx4 v16, %[rs], s51 offen offset:0 sc1 lds\n\t" "v_mfma_f32_32x32x16_bf16 v[112:127], a[236:239], v[180:183], v[112:127]\n\t" \
   "s_waitcnt lgkmcnt(2)\n\t" "v_mfma_f32_32x32x16_bf16 v[96:111], a[240:243], v[184:187], v[96:111]\n\t" \
   "ds_read_b128 v[168:171], v26 offset:17408\n\t" "s_cmp_lt_i32 s40, 1\n\t" "s_cselect_b32 s49, 0x7ffffff0, 0\n\t" "v_or_b32 v9, s49, v20\n\t" \
   "s_sub_i32 s49, s41, 1\n\t" "v_mfma_f32_32x32x16_bf16 v[112:127], a[248:251], v[184:187], v[112:127]\n\t" "s_max_i32 s49, s49, 0\n\t" \
   "s_lshl_b32 s49, s49, 12\n\t" "s_waitcnt vmcnt(9)\n\t" "buffer_store_dword v31, v9, %[rs], s49 offen sc1\n\t" \
   "v_mfma_f32_32x32x16_bf16 v[96:111], a[244:247], v[188:191], v[96:111]\n\t" "ds_read_b128 v[172:175], v26 offset:17920\n\t" \
   "v_mfma_f32_32x32x16_bf16 v[112:127], a[252:255], v[188:191], v[112:127]\n\t" "buffer_load_dwordx4 v16, %[rs], s51 offen offset:1024 sc1 lds\n\t" \
   "s_add_u32 m0, s53, %[ld1]\n\t" "s_nop 0\n\t" "s_waitcnt lgkmcnt(2)\n\t" "v_mfma_f32_32x32x16_bf16 v[96:111], v[192:195], v[160:163], v[96:111]\n\t" \
   "ds_read_b128 v[176:179], v26 offset:18432\n\t" "buffer_load_dwordx4 v17, %[rs], s52 offen sc1 lds\n\t" "ds_read_b128 v[140:143], v12\n\t" \
   "v_mfma_f32_32x32x16_bf16 v[112:127], v[200:203], v[160:163], v[112:127]\n\t" \
   "v_mfma_f32_32x32x16_bf16 v[96:111], v[196:199], v[164:167], v[96:111]\n\t" "ds_read_b128 v[180:183], v26 offset:18944\n\t" \
   "v_mfma_f32_32x32x16_bf16 v[112:127], v[204:207], v[164:167], v[112:127]\n\t" "s_waitcnt lgkmcnt(3)\n\t" \
   "v_mfma_f32_32x32x16_bf16 v[96:111], v[208:211], v[168:171], v[96:111]\n\t" "ds_read_b128 v[184:187], v26 offset:19456\n\t" \
   "s_waitcnt lgkmcnt(2)\n\t" "v_min3_u32 v140, v140, v141, v142\n\t" "v_min_u32 v140, v140, v143\n\t" "v_cmp_ge_u32 vcc, v140, v138\n\t" \
   "v_mfma_f32_32x32x16_bf16 v[112:127], v[216:219], v[168:171], v[112:127]\n\t" "s_cbranch_vccz 153f\n\t" "53:\n\t" \
   "v_mfma_f32_32x32x16_bf16 v[96:111], v[212:215], v[172:175], v[96:111]\n\t" "ds_read_b128 v[188:191], v26 offset:19968\n\t" \
   "v_mfma_f32_32x32x16_bf16 v[112:127], v[220:223], v[172:175], v[112:127]\n\t" "v_add_u32 v138, 1, v138\n\t" "ds_write_b32 v13, v138\n\t" \
   "s_waitcnt lgkmcnt(3)\n\t" "v_mfma_f32_32x32x16_bf16 v[96:111], v[224:227], v[176:179], v[96:111]\n\t" "ds_read_b128 v[160:163], v28 offset:0\n\t" \
   "s_add_u32 s45, s40, 3\n\t" "s_min_u32 s45, s45, 63\n\t" "s_mov_b32 s46, s42\n\t" \
   "v_mfma_f32_32x32x16_bf16 v[112:127], v[232:235], v[176:179], v[112:127]\n\t" "s_lshr_b32 s48, s45, 1\n\t" "s_bitcmp1_b32 s44, s48\n\t" \
   "s_cbranch_scc0 178f\n\t" "78:\n\t" "s_mov_b32 s42, s43\n\t" "s_add_u32 s43, s43, 44032\n\t" \
   "v_mfma_f32_32x32x16_bf16 v[96:111], v[228:231], v[180:183], v[96:111]\n\t" "ds_read_b128 v[164:167], v28 offset:512\n\t" \
   "s_cmp_eq_u32 s43, 132096\n\t" "s_cselect_b32 s43, 0, s43\n\t" "s_add_u32 s41, s41, 1\n\t" "s_add_u32 s40, s40, 1\n\t" \
   "v_mfma_f32_32x32x16_bf16 v[112:127], v[236:239], v[180:183], v[112:127]\n\t" "s_waitcnt lgkmcnt(3)\n\t" \
   "v_mfma_f32_32x32x16_bf16 v[96:111], v[240:243], v[184:187], v[96:111]\n\t" "ds_read_b128 v[168:171], v28 offset:1024\n\t" \
   "v_mfma_f32_32x32x16_bf16 v[112:127], v[248:251], v[184:187], v[112:127]\n\t" \
   "v_mfma_f32_32x32x16_bf16 v[96:111], v[244:247], v[188:191], v[96:111]\n\t" "ds_read_b128 v[172:175], v28 offset:1536\n\t" \
   "v_mfma_f32_32x32x16_bf16 v[112:127], v[252:255], v[188:191], v[112:127]\n\t" "s_cmp_lt_u32 s40, 64\n\t" "s_cbranch_scc1 1b\n\t" "s_nop 7\n\t" \
   "v_mul_f32 v11, 0x3e0293ee, v10\n\t" "s_lshl_b32 s47, s41, 13\n\t" "s_sub_i32 s47, s47, 8192\n\t" "v_fmac_f32 v96, v32, v8\n\t" \
   "v_fmac_f32 v97, v33, v8\n\t" "v_fmac_f32 v98, v34, v8\n\t" "v_fmac_f32 v99, v35, v8\n\t" "v_fmac_f32 v100, v36, v8\n\t" \
   "v_fmac_f32 v101, v37, v8\n\t" "v_fmac_f32 v102, v38, v8\n\t" "v_fmac_f32 v103, v39, v8\n\t" "v_fmac_f32 v104, v40, v8\n\t" \
   "v_fmac_f32 v105, v41, v8\n\t" "v_fmac_f32 v106, v42, v8\n\t" "v_fmac_f32 v107, v43, v8\n\t" "v_fmac_f32 v108, v44, v8\n\t" \
   "v_fmac_f32 v109, v45, v8\n\t" "v_fmac_f32 v110, v46, v8\n\t" "v_fmac_f32 v111, v47, v8\n\t" "v_mul_f32 v96, v96, v11\n\t" \
   "v_mul_f32 v97, v97, v11\n\t" "v_mul_f32 v98, v98, v11\n\t" "v_mul_f32 v99, v99, v11\n\t" "v_mul_f32 v100, v100, v11\n\t" \
   "v_mul_f32 v101, v101, v11\n\t" "v_mul_f32 v102, v102, v11\n\t" "v_mul_f32 v103, v103, v11\n\t" "v_mul_f32 v104, v104, v11\n\t" \
   "v_mul_f32 v105, v105, v11\n\t" "v_mul_f32 v106, v106, v11\n\t" "v_mul_f32 v107, v107, v11\n\t" "v_mul_f32 v108, v108, v11\n\t" \
   "v_mul_f32 v109, v109, v11\n\t" "v_mul_f32 v110, v110, v11\n\t" "v_mul_f32 v111, v111, v11\n\t" "s_add_u32 s48, s47, %[qf]\n\t" \
   "v_cvt_pk_bf16_f32 v96, v96, v97\n\t" "v_cvt_pk_bf16_f32 v97, v98, v99\n\t" "v_cvt_pk_bf16_f32 v98, v104, v105\n\t" \
   "v_cvt_pk_bf16_f32 v99, v106, v107\n\t" "buffer_store_dwordx4 v[96:99], v21, %[rs], s48 offen offset:0 sc1\n\t" \
   "v_cvt_pk_bf16_f32 v100, v100, v101\n\t" "v_cvt_pk_bf16_f32 v101, v102, v103\n\t" "v_cvt_pk_bf16_f32 v102, v108, v109\n\t" \
   "v_cvt_pk_bf16_f32 v103, v110, v111\n\t" "buffer_store_dwordx4 v[100:103], v21, %[rs], s48 offen offset:512 sc1\n\t" "v_fmac_f32 v112, v48, v8\n\t" \
   "v_fmac_f32 v113, v49, v8\n\t" "v_fmac_f32 v114, v50, v8\n\t" "v_fmac_f32 v115, v51, v8\n\t" "v_fmac_f32 v116, v52, v8\n\t" \
   "v_fmac_f32 v117, v53, v8\n\t" "v_fmac_f32 v118, v54, v8\n\t" "v_fmac_f32 v119, v55, v8\n\t" "v_fmac_f32 v120, v56, v8\n\t" \
   "v_fmac_f32 v121, v57, v8\n\t" "v_fmac_f32 v122, v58, v8\n\t" "v_fmac_f32 v123, v59, v8\n\t" "v_fmac_f32 v124, v60, v8\n\t" \
   "v_fmac_f32 v125, v61, v8\n\t" "v_fmac_f32 v126, v62, v8\n\t" "v_fmac_f32 v127, v63, v8\n\t" "v_mul_f32 v112, v112, v10\n\t" \
   "v_mul_f32 v113, v113, v10\n\t" "v_mul_f32 v114, v114, v10\n\t" "v_mul_f32 v115, v115, v10\n\t" "v_mul_f32 v116, v116, v10\n\t" \
   "v_mul_f32 v117, v117, v10\n\t" "v_mul_f32 v118, v118, v10\n\t" "v_mul_f32 v119, v119, v10\n\t" "v_mul_f32 v120, v120, v10\n\t" \
   "v_mul_f32 v121, v121, v10\n\t" "v_mul_f32 v122, v122, v10\n\t" "v_mul_f32 v123, v123, v10\n\t" "v_mul_f32 v124, v124, v10\n\t" \
   "v_mul_f32 v125, v125, v10\n\t" "v_mul_f32 v126, v126, v10\n\t" "v_mul_f32 v127, v127, v10\n\t" "s_add_u32 s48, s47, %[kf]\n\t" \
   "v_cvt_pk_bf16_f32 v112, v112, v113\n\t" "v_cvt_pk_bf16_f32 v113, v114, v115\n\t" "v_cvt_pk_bf16_f32 v114, v120, v121\n\t" \
   "v_cvt_pk_bf16_f32 v115, v122, v123\n\t" "buffer_store_dwordx4 v[112:115], v21, %[rs], s48 offen offset:0 sc1\n\t" \
   "v_cvt_pk_bf16_f32 v116, v116, v117\n\t" "v_cvt_pk_bf16_f32 v117, v118, v119\n\t" "v_cvt_pk_bf16_f32 v118, v124, v125\n\t" \
   "v_cvt_pk_bf16_f32 v119, v126, v127\n\t" "buffer_store_dwordx4 v[116:119], v21, %[rs], s48 offen offset:512 sc1\n\t" \
   "s_waitcnt vmcnt(0) lgkmcnt(0)\n\t" "s_cmp_lt_i32 s40, 1\n\t" "s_cselect_b32 s47, 0x7ffffff0, 0\n\t" "v_or_b32 v30, s47, v20\n\t" \
   "s_sub_i32 s47, s41, 1\n\t" "s_max_i32 s47, s47, 0\n\t" "s_lshl_b32 s47, s47, 12\n\t" "buffer_store_dword v31, v30, %[rs], s47 offen sc1\n\t" \
   "s_branch 9f\n\t" "172:\n\t" "s_mov_b32 s47, 0\n\t" "buffer_load_dword v30, v19, %[rs], 0 offen sc1\n\t" "s_waitcnt vmcnt(0)\n\t" \
   "v_add_u32 v30, 1, v30\n\t" "v_cmp_lt_u32 vcc, 7, v30\n\t" "s_mov_b32 s44, vcc_lo\n\t" "s_bitcmp1_b32 s44, s48\n\t" "s_cbranch_scc1 72b\n\t" \
   "s_bitset1_b32 s44, s48\n\t" "s_lshl_b32 s48, s48, 7\n\t" "s_add_u32 s48, s48, %[cnt]\n\t" "73:\n\t" "s_sleep 10\n\t" \
   "buffer_load_dword v30, off, %[rs], s48 sc1\n\t" "s_add_u32 s47, s47, 1\n\t" "s_waitcnt vmcnt(0)\n\t" "v_add_u32 v30, 1, v30\n\t" \
   "v_cmp_lt_u32 vcc, 7, v30\n\t" "s_cmp_gt_u32 s47, 0x20000\n\t" "s_cbranch_scc1 72b\n\t" "s_cbranch_vccz 73b\n\t" "s_branch 72b\n\t" "174:\n\t" \
   "s_mov_b32 s47, 0\n\t" "buffer_load_dword v30, v19, %[rs], 0 offen sc1\n\t" "s_waitcnt vmcnt(0)\n\t" "v_add_u32 v30, 1, v30\n\t" \
   "v_cmp_lt_u32 vcc, 7, v30\n\t" "s_mov_b32 s44, vcc_lo\n\t" "s_bitcmp1_b32 s44, s48\n\t" "s_cbranch_scc1 74b\n\t" "s_bitset1_b32 s44, s48\n\t" \
   "s_lshl_b32 s48, s48, 7\n\t" "s_add_u32 s48, s48, %[cnt]\n\t" "75:\n\t" "s_sleep 10\n\t" "buffer_load_dword v30, off, %[rs], s48 sc1\n\t" \
   "s_add_u32 s47, s47, 1\n\t" "s_waitcnt vmcnt(0)\n\t" "v_add_u32 v30, 1, v30\n\t" "v_cmp_lt_u32 vcc, 7, v30\n\t" "s_cmp_gt_u32 s47, 0x20000\n\t" \
   "s_cbranch_scc1 74b\n\t" "s_cbranch_vccz 75b\n\t" "s_branch 74b\n\t" "178:\n\t" "s_mov_b32 s47, 0\n\t" \
   "buffer_load_dword v30, v19, %[rs], 0 offen sc1\n\t" "s_waitcnt vmcnt(0)\n\t" "v_add_u32 v30, 1, v30\n\t" "v_cmp_lt_u32 vcc, 7, v30\n\t" \
   "s_mov_b32 s44, vcc_lo\n\t" "s_bitcmp1_b32 s44, s48\n\t" "s_cbranch_scc1 78b\n\t" "s_bitset1_b32 s44, s48\n\t" "s_lshl_b32 s48, s48, 7\n\t" \
   "s_add_u32 s48, s48, %[cnt]\n\t" "79:\n\t" "s_sleep 10\n\t" "buffer_load_dword v30, off, %[rs], s48 sc1\n\t" "s_add_u32 s47, s47, 1\n\t" \
   "s_waitcnt vmcnt(0)\n\t" "v_add_u32 v30, 1, v30\n\t" "v_cmp_lt_u32 vcc, 7, v30\n\t" "s_cmp_gt_u32 s47, 0x20000\n\t" "s_cbranch_scc1 78b\n\t" \
   "s_cbranch_vccz 79b\n\t" "s_branch 78b\n\t" "150:\n\t" "ds_read_b128 v[140:143], v12\n\t" "s_waitcnt lgkmcnt(0)\n\t" \
   "v_min3_u32 v140, v140, v141, v142\n\t" "v_min_u32 v140, v140, v143\n\t" "v_cmp_ge_u32 vcc, v140, v138\n\t" "s_cbranch_vccz 150b\n\t" \
   "s_branch 50b\n\t" "152:\n\t" "ds_read_b128 v[140:143], v12\n\t" "s_waitcnt lgkmcnt(0)\n\t" "v_min3_u32 v140, v140, v141, v142\n\t" \
   "v_min_u32 v140, v140, v143\n\t" "v_cmp_ge_u32 vcc, v140, v138\n\t" "s_cbranch_vccz 152b\n\t" "s_branch 52b\n\t" "151:\n\t" \
   "ds_read_b128 v[140:143], v12\n\t" "s_waitcnt lgkmcnt(0)\n\t" "v_min3_u32 v140, v140, v141, v142\n\t" "v_min_u32 v140, v140, v143\n\t" \
   "v_cmp_ge_u32 vcc, v140, v138\n\t" "s_cbranch_vccz 151b\n\t" "s_branch 51b\n\t" "153:\n\t" "ds_read_b128 v[140:143], v12\n\t" \
   "s_waitcnt lgkmcnt(0)\n\t" "v_min3_u32 v140, v140, v141, v142\n\t" "v_min_u32 v140, v140, v143\n\t" "v_cmp_ge_u32 vcc, v140, v138\n\t" \
   "s_cbranch_vccz 153b\n\t" "s_branch 53b\n\t" "9:\n\t" "s_waitcnt vmcnt(0)"
#define KQ_CLOB "v8", "v9", "v10", "v11", "v12", "v13", "v14", "v15", "v32", "v33", "v34", "v35", "v36", "v37", "v38", "v39", "v40", "v41", "v42", "v43", "v44", "v45", "v46", "v47", "v48", "v49", "v50", "v51", "v52", "v53", "v54", "v55", "v56", "v57", "v58", "v59", "v60", "v61", "v62", "v63", "v64", "v65", "v66", "v67", "v68", "v69", "v70", "v71", "v72", "v73", "v74", "v75", "v76", "v77", "v78", "v79", "v80", "v81", "v82", "v83", "v84", "v85", "v86", "v87", "v88", "v89", "v90", "v91", "v92", "v93", "v94", "v95", "v96", "v97", "v98", "v99", "v100", "v101", "v102", "v103", "v104", "v105", "v106", "v107", "v108", "v109", "v110", "v111", "v112", "v113", "v114", "v115", "v116", "v117", "v118", "v119", "v120", "v121", "v122", "v123", "v124", "v125", "v126", "v127", "v128", "v129", "v130", "v131", "v132", "v133", "v134", "v135", "v136", "v137", "v138", "v139", "v140", "v141", "v142", "v143", "v144", "v145", "v146", "v147", "v148", "v149", "v150", "v151", "v160", "v161", "v162", "v163", "v164", "v165", "v166", "v167", "v168", "v169", "v170", "v171", "v172", "v173", "v174", "v175", "v176", "v177", "v178", "v179", "v180", "v181", "v182", "v183", "v184", "v185", "v186", "v187", "v188", "v189", "v190", "v191", "s40", "s41", "s42", "s43", "s44", "s45", "s46", "s47", "s48", "s49", "s50", "s51", "s52", "s53", "s54", "s55", "s60", "s61"
#define VU_ASM \
   "s_mov_b32 s44, %[rdy]\n\t" "s_mov_b32 s45, 0\n\t" "s_mov_b32 s46, 0\n\t" "s_cmp_eq_u32 s44, -1\n\t" "s_cbranch_scc1 68f\n\t" \
   "s_mul_i32 s52, s45, 1536\n\t" "s_mul_i32 s51, s45, 40960\n\t" "s_add_u32 m0, s46, %[ldx]\n\t" "s_mov_b32 s53, s46\n\t" \
   "buffer_load_dwordx4 v16, %[rs], s51 offen offset:0 sc1 lds\n\t" "buffer_load_dwordx4 v16, %[rs], s51 offen offset:1024 sc1 lds\n\t" \
   "buffer_load_dwordx4 v16, %[rs], s51 offen offset:2048 sc1 lds\n\t" "buffer_load_dwordx4 v16, %[rs], s51 offen offset:3072 sc1 lds\n\t" \
   "s_add_u32 m0, m0, 4096\n\t" "s_add_u32 s51, s51, 4096\n\t" "buffer_load_dwordx4 v16, %[rs], s51 offen offset:0 sc1 lds\n\t" \
   "buffer_load_dwordx4 v16, %[rs], s51 offen offset:1024 sc1 lds\n\t" "buffer_load_dwordx4 v16, %[rs], s51 offen offset:2048 sc1 lds\n\t" \
   "buffer_load_dwordx4 v16, %[rs], s51 offen offset:3072 sc1 lds\n\t" "s_add_u32 m0, m0, 4096\n\t" "s_add_u32 s51, s51, 4096\n\t" \
   "buffer_load_dwordx4 v16, %[rs], s51 offen offset:0 sc1 lds\n\t" "buffer_load_dwordx4 v16, %[rs], s51 offen offset:1024 sc1 lds\n\t" \
   "s_add_u32 m0, s53, %[ld1]\n\t" "s_nop 0\n\t" "buffer_load_dwordx4 v17, %[rs], s52 offen sc1 lds\n\t" "s_mov_b32 s45, 1\n\t" \
   "s_mov_b32 s46, 44032\n\t" "s_mul_i32 s52, s45, 1536\n\t" "s_mul_i32 s51, s45, 40960\n\t" "s_add_u32 m0, s46, %[ldx]\n\t" "s_mov_b32 s53, s46\n\t" \
   "buffer_load_dwordx4 v16, %[rs], s51 offen offset:0 sc1 lds\n\t" "buffer_load_dwordx4 v16, %[rs], s51 offen offset:1024 sc1 lds\n\t" \
   "buffer_load_dwordx4 v16, %[rs], s51 offen offset:2048 sc1 lds\n\t" "buffer_load_dwordx4 v16, %[rs], s51 offen offset:3072 sc1 lds\n\t" \
   "s_add_u32 m0, m0, 4096\n\t" "s_add_u32 s51, s51, 4096\n\t" "buffer_load_dwordx4 v16, %[rs], s51 offen offset:0 sc1 lds\n\t" \
   "buffer_load_dwordx4 v16, %[rs], s51 offen offset:1024 sc1 lds\n\t" "buffer_load_dwordx4 v16, %[rs], s51 offen offset:2048 sc1 lds\n\t" \
   "buffer_load_dwordx4 v16, %[rs], s51 offen offset:3072 sc1 lds\n\t" "s_add_u32 m0, m0, 4096\n\t" "s_add_u32 s51, s51, 4096\n\t" \
   "buffer_load_dwordx4 v16, %[rs], s51 offen offset:0 sc1 lds\n\t" "buffer_load_dwordx4 v16, %[rs], s51 offen offset:1024 sc1 lds\n\t" \
   "s_add_u32 m0, s53, %[ld1]\n\t" "s_nop 0\n\t" "buffer_load_dwordx4 v17, %[rs], s52 offen sc1 lds\n\t" "68:\n\t" "v_mov_b32 v138, 0\n\t" \
   "v_mov_b32 v13, %[flw]\n\t" "v_and_b32 v12, -16, v13\n\t" "ds_write_b32 v13, v138\n\t" "ds_read_b128 v[32:35], v26 offset:0\n\t" \
   "ds_read_b128 v[36:39], v26 offset:16\n\t" "ds_read_b128 v[40:43], v26 offset:32\n\t" "ds_read_b128 v[44:47], v26 offset:48\n\t" \
   "s_mov_b32 s40, 0\n\t" "s_mov_b32 s41, 0\n\t" "s_mov_b32 s42, 0\n\t" "s_mov_b32 s43, 44032\n\t" "v_mov_b32 v28, v22\n\t" "v_mov_b32 v29, v23\n\t" \
   "s_mov_b32 s45, 2\n\t" "s_mov_b32 s46, 88064\n\t" "s_waitcnt vmcnt(11) lgkmcnt(0)\n\t" "s_barrier\n\t" "s_lshr_b32 s48, s45, 1\n\t" \
   "s_bitcmp1_b32 s44, s48\n\t" "s_cbranch_scc0 172f\n\t" "72:\n\t" "ds_read_b128 v[160:163], v28 offset:0\n\t" \
   "ds_read_b128 v[164:167], v28 offset:512\n\t" "ds_read_b128 v[168:171], v28 offset:1024\n\t" "ds_read_b128 v[172:175], v28 offset:1536\n\t" \
   "ds_read_b128 v[176:179], v28 offset:2048\n\t" "ds_read_b128 v[180:183], v28 offset:2560\n\t" "ds_read_b128 v[184:187], v28 offset:3072\n\t" \
   "v_mov_b32 v14, v24\n\t" "v_mov_b32 v15, v25\n\t" "v_mov_b32 v18, 0x7ffffff0\n\t" "s_mov_b32 s49, 0\n\t" "v_mov_b32 v26, v28\n\t" \
   "v_mov_b32 v27, v29\n\t" "s_waitcnt lgkmcnt(5)\n\t" "v_mfma_f32_32x32x16_bf16 v[48:63], a[0:3], v[160:163], 0\n\t" \
   "ds_read_b128 v[188:191], v26 offset:3584\n\t" "s_mul_i32 s52, s45, 1536\n\t" "s_mul_i32 s51, s45, 40960\n\t" "s_add_u32 m0, s46, %[ldx]\n\t" \
   "s_mov_b32 s53, s46\n\t" "v_mfma_f32_32x32x16_bf16 v[48:63], a[4:7], v[164:167], v[48:63]\n\t" "ds_read_b128 v[160:163], v26 offset:4096\n\t" \
   "ds_read_b128 v[128:131], v27 offset:0\n\t" "ds_read_b128 v[132:135], v27 offset:16\n\t" "s_waitcnt lgkmcnt(7)\n\t" \
   "v_mfma_f32_32x32x16_bf16 v[48:63], a[8:11], v[168:171], v[48:63]\n\t" "ds_read_b128 v[164:167], v26 offset:4608\n\t" \
   "ds_read_b64 v[136:137], v27 offset:32\n\t" "v_add_u32 v28, s43, v22\n\t" "v_add_u32 v29, s43, v23\n\t" \
   "v_mfma_f32_32x32x16_bf16 v[48:63], a[12:15], v[172:175], v[48:63]\n\t" "ds_read_b128 v[168:171], v26 offset:5120\n\t" \
   "buffer_load_dwordx4 v16, %[rs], s51 offen offset:0 sc1 lds\n\t" "s_waitcnt lgkmcnt(8)\n\t" \
   "v_mfma_f32_32x32x16_bf16 v[48:63], a[16:19], v[176:179], v[48:63]\n\t" "ds_read_b128 v[172:175], v26 offset:5632\n\t" \
   "v_mfma_f32_32x32x16_bf16 v[48:63], a[20:23], v[180:183], v[48:63]\n\t" "ds_read_b128 v[176:179], v26 offset:6144\n\t" "s_waitcnt lgkmcnt(3)\n\t" \
   "v_add_f32 v128, v128, v130\n\t" "v_add_f32 v129, v129, v131\n\t" "v_add_f32 v132, v132, v134\n\t" \
   "v_mfma_f32_32x32x16_bf16 v[48:63], a[24:27], v[184:187], v[48:63]\n\t" "ds_read_b128 v[180:183], v26 offset:6656\n\t" \
   "buffer_load_dwordx4 v16, %[rs], s51 offen offset:1024 sc1 lds\n\t" "v_add_f32 v133, v133, v135\n\t" \
   "v_mfma_f32_32x32x16_bf16 v[48:63], a[28:31], v[188:191], v[48:63]\n\t" "ds_read_b128 v[184:187], v26 offset:7168\n\t" \
   "v_add_f32 v128, v128, v136\n\t" "v_add_f32 v129, v129, v137\n\t" "v_add_f32 v128, v128, v132\n\t" \
   "v_mfma_f32_32x32x16_bf16 v[48:63], a[32:35], v[160:163], v[48:63]\n\t" "ds_read_b128 v[188:191], v26 offset:7680\n\t" \
   "v_add_f32 v129, v129, v133\n\t" "v_mul_f32 v128, 0xbacccccd, v128\n\t" "v_mul_f32 v129, 0x3acccccd, v129\n\t" \
   "v_fma_f32 v129, -v128, v128, v129\n\t" "v_mfma_f32_32x32x16_bf16 v[48:63], a[36:39], v[164:167], v[48:63]\n\t" \
   "ds_read_b128 v[160:163], v26 offset:8192\n\t" "buffer_load_dwordx4 v16, %[rs], s51 offen offset:2048 sc1 lds\n\t" "s_waitcnt lgkmcnt(5)\n\t" \
   "v_mfma_f32_32x32x16_bf16 v[48:63], a[40:43], v[168:171], v[48:63]\n\t" "ds_read_b128 v[164:167], v26 offset:8704\n\t" "v_max_f32 v129, 0, v129\n\t" \
   "v_cvt_pk_bf16_f32 v129, v129, v128\n\t" "v_and_b32 v8, 0xffff0000, v129\n\t" "v_lshlrev_b32 v129, 16, v129\n\t" \
   "v_mfma_f32_32x32x16_bf16 v[48:63], a[44:47], v[172:175], v[48:63]\n\t" "ds_read_b128 v[168:171], v26 offset:9216\n\t" \
   "v_add_f32 v129, 0x3727c5ac, v129\n\t" "v_cvt_pk_bf16_f32 v129, v129, v129\n\t" "v_lshlrev_b32 v129, 16, v129\n\t" "s_waitcnt lgkmcnt(5)\n\t" \
   "v_mfma_f32_32x32x16_bf16 v[48:63], a[48:51], v[176:179], v[48:63]\n\t" "ds_read_b128 v[172:175], v26 offset:9728\n\t" "v_sqrt_f32 v129, v129\n\t" \
   "s_nop 0\n\t" "v_cvt_pk_bf16_f32 v129, v129, v129\n\t" "v_mfma_f32_32x32x16_bf16 v[48:63], a[52:55], v[180:183], v[48:63]\n\t" \
   "ds_read_b128 v[176:179], v26 offset:10240\n\t" "v_lshlrev_b32 v129, 16, v129\n\t" "v_rcp_f32 v10, v129\n\t" "s_waitcnt lgkmcnt(5)\n\t" \
   "v_mfma_f32_32x32x16_bf16 v[48:63], a[56:59], v[184:187], v[48:63]\n\t" "ds_read_b128 v[180:183], v26 offset:10752\n\t" \
   "buffer_load_dwordx4 v16, %[rs], s51 offen offset:3072 sc1 lds\n\t" "s_add_u32 m0, m0, 4096\n\t" "s_add_u32 s51, s51, 4096\n\t" \
   "v_mfma_f32_32x32x16_bf16 v[48:63], a[60:63], v[188:191], v[48:63]\n\t" "ds_read_b128 v[184:187], v26 offset:11264\n\t" \
   "buffer_load_dwordx4 v16, %[rs], s51 offen offset:0 sc1 lds\n\t" "s_waitcnt lgkmcnt(5)\n\t" \
   "v_mfma_f32_32x32x16_bf16 v[48:63], a[64:67], v[160:163], v[48:63]\n\t" "ds_read_b128 v[188:191], v26 offset:11776\n\t" \
   "v_mfma_f32_32x32x16_bf16 v[48:63], a[68:71], v[164:167], v[48:63]\n\t" "ds_read_b128 v[160:163], v26 offset:12288\n\t" "s_waitcnt lgkmcnt(5)\n\t" \
   "v_mfma_f32_32x32x16_bf16 v[48:63], a[72:75], v[168:171], v[48:63]\n\t" "ds_read_b128 v[164:167], v26 offset:12800\n\t" \
   "buffer_load_dwordx4 v16, %[rs], s51 offen offset:1024 sc1 lds\n\t" "v_mfma_f32_32x32x16_bf16 v[48:63], a[76:79], v[172:175], v[48:63]\n\t" \
   "ds_read_b128 v[168:171], v26 offset:13312\n\t" "s_waitcnt lgkmcnt(5)\n\t" "v_mfma_f32_32x32x16_bf16 v[48:63], a[80:83], v[176:179], v[48:63]\n\t" \
   "ds_read_b128 v[172:175], v26 offset:13824\n\t" "v_mfma_f32_32x32x16_bf16 v[48:63], a[84:87], v[180:183], v[48:63]\n\t" \
   "ds_read_b128 v[176:179], v26 offset:14336\n\t" "buffer_load_dwordx4 v16, %[rs], s51 offen offset:2048 sc1 lds\n\t" "s_waitcnt lgkmcnt(5)\n\t" \
   "v_mfma_f32_32x32x16_bf16 v[48:63], a[88:91], v[184:187], v[48:63]\n\t" "ds_read_b128 v[180:183], v26 offset:14848\n\t" \
   "v_mfma_f32_32x32x16_bf16 v[48:63], a[92:95], v[188:191], v[48:63]\n\t" "ds_read_b128 v[184:187], v26 offset:15360\n\t" "s_waitcnt lgkmcnt(5)\n\t" \
   "v_mfma_f32_32x32x16_bf16 v[48:63], a[96:99], v[160:163], v[48:63]\n\t" "ds_read_b128 v[188:191], v26 offset:15872\n\t" \
   "v_mfma_f32_32x32x16_bf16 v[48:63], a[100:103], v[164:167], v[48:63]\n\t" "ds_read_b128 v[160:163], v26 offset:16384\n\t" "s_waitcnt lgkmcnt(5)\n\t" \
   "v_mfma_f32_32x32x16_bf16 v[48:63], a[104:107], v[168:171], v[48:63]\n\t" "ds_read_b128 v[164:167], v26 offset:16896\n\t" \
   "buffer_load_dwordx4 v16, %[rs], s51 offen offset:3072 sc1 lds\n\t" "s_add_u32 m0, m0, 4096\n\t" "s_add_u32 s51, s51, 4096\n\t" \
   "v_mfma_f32_32x32x16_bf16 v[48:63], a[108:111], v[172:175], v[48:63]\n\t" "ds_read_b128 v[168:171], v26 offset:17408\n\t" \
   "buffer_load_dwordx4 v16, %[rs], s51 offen offset:0 sc1 lds\n\t" "s_waitcnt lgkmcnt(5)\n\t" \
   "v_mfma_f32_32x32x16_bf16 v[48:63], a[112:115], v[176:179], v[48:63]\n\t" "ds_read_b128 v[172:175], v26 offset:17920\n\t" \
   "v_mfma_f32_32x32x16_bf16 v[48:63], a[116:119], v[180:183], v[48:63]\n\t" "ds_read_b128 v[176:179], v26 offset:18432\n\t" "s_waitcnt lgkmcnt(5)\n\t" \
   "v_mfma_f32_32x32x16_bf16 v[48:63], a[120:123], v[184:187], v[48:63]\n\t" "ds_read_b128 v[180:183], v26 offset:18944\n\t" \
   "v_mfma_f32_32x32x16_bf16 v[48:63], a[124:127], v[188:191], v[48:63]\n\t" "ds_read_b128 v[184:187], v26 offset:19456\n\t" "s_waitcnt lgkmcnt(5)\n\t" \
   "v_mfma_f32_32x32x16_bf16 v[48:63], a[128:131], v[160:163], v[48:63]\n\t" "ds_read_b128 v[188:191], v26 offset:19968\n\t" \
   "buffer_load_dwordx4 v16, %[rs], s51 offen offset:1024 sc1 lds\n\t" "s_add_u32 m0, s53, %[ld1]\n\t" "s_nop 0\n\t" \
   "v_mfma_f32_32x32x16_bf16 v[48:63], a[132:135], v[164:167], v[48:63]\n\t" "buffer_load_dwordx4 v17, %[rs], s52 offen sc1 lds\n\t" \
   "s_waitcnt lgkmcnt(4)\n\t" "v_mfma_f32_32x32x16_bf16 v[48:63], a[136:139], v[168:171], v[48:63]\n\t" \
   "v_mfma_f32_32x32x16_bf16 v[48:63], a[140:143], v[172:175], v[48:63]\n\t" "s_waitcnt lgkmcnt(2)\n\t" \
   "v_mfma_f32_32x32x16_bf16 v[48:63], a[144:147], v[176:179], v[48:63]\n\t" "v_mfma_f32_32x32x16_bf16 v[48:63], a[148:151], v[180:183], v[48:63]\n\t" \
   "s_waitcnt lgkmcnt(0)\n\t" "v_mfma_f32_32x32x16_bf16 v[48:63], a[152:155], v[184:187], v[48:63]\n\t" \
   "v_mfma_f32_32x32x16_bf16 v[48:63], a[156:159], v[188:191], v[48:63]\n\t" "s_memrealtime s[54:55]\n\t" "s_nop 7\n\t" "s_nop 2\n\t" \
   "v_fmac_f32 v48, v32, v8\n\t" "v_fmac_f32 v49, v33, v8\n\t" "v_fmac_f32 v50, v34, v8\n\t" "v_fmac_f32 v51, v35, v8\n\t" \
   "v_fmac_f32 v52, v36, v8\n\t" "v_fmac_f32 v53, v37, v8\n\t" "v_fmac_f32 v54, v38, v8\n\t" "v_fmac_f32 v55, v39, v8\n\t" \
   "v_fmac_f32 v56, v40, v8\n\t" "v_fmac_f32 v57, v41, v8\n\t" "v_fmac_f32 v58, v42, v8\n\t" "v_fmac_f32 v59, v43, v8\n\t" \
   "v_fmac_f32 v60, v44, v8\n\t" "v_fmac_f32 v61, v45, v8\n\t" "v_fmac_f32 v62, v46, v8\n\t" "v_fmac_f32 v63, v47, v8\n\t" \
   "v_mul_f32 v48, v48, v10\n\t" "v_mul_f32 v49, v49, v10\n\t" "v_mul_f32 v50, v50, v10\n\t" "v_mul_f32 v51, v51, v10\n\t" \
   "v_mul_f32 v52, v52, v10\n\t" "v_mul_f32 v53, v53, v10\n\t" "v_mul_f32 v54, v54, v10\n\t" "v_mul_f32 v55, v55, v10\n\t" \
   "v_mul_f32 v56, v56, v10\n\t" "v_mul_f32 v57, v57, v10\n\t" "v_mul_f32 v58, v58, v10\n\t" "v_mul_f32 v59, v59, v10\n\t" \
   "v_mul_f32 v60, v60, v10\n\t" "v_mul_f32 v61, v61, v10\n\t" "v_mul_f32 v62, v62, v10\n\t" "v_mul_f32 v63, v63, v10\n\t" \
   "v_cvt_pk_bf16_f32 v48, v48, v49\n\t" "v_cvt_pk_bf16_f32 v49, v50, v51\n\t" "v_cvt_pk_bf16_f32 v50, v52, v53\n\t" \
   "v_cvt_pk_bf16_f32 v51, v54, v55\n\t" "ds_write_b128 v14, v[48:51] offset:0\n\t" "v_cvt_pk_bf16_f32 v56, v56, v57\n\t" \
   "v_cvt_pk_bf16_f32 v57, v58, v59\n\t" "v_cvt_pk_bf16_f32 v58, v60, v61\n\t" "v_cvt_pk_bf16_f32 v59, v62, v63\n\t" \
   "ds_write_b128 v14, v[56:59] offset:1024\n\t" "s_waitcnt vmcnt(11) lgkmcnt(0)\n\t" "s_barrier\n\t" "ds_read_b128 v[160:163], v28 offset:0\n\t" \
   "ds_read_b128 v[164:167], v28 offset:512\n\t" "ds_read_b128 v[168:171], v28 offset:1024\n\t" "ds_read_b128 v[172:175], v28 offset:1536\n\t" \
   "ds_read_b128 v[176:179], v28 offset:2048\n\t" "ds_read_b128 v[180:183], v28 offset:2560\n\t" "ds_read_b128 v[184:187], v28 offset:3072\n\t" \
   "s_add_u32 s45, s40, 3\n\t" "s_min_u32 s45, s45, 63\n\t" "s_mov_b32 s46, s42\n\t" "s_lshr_b32 s48, s45, 1\n\t" "s_bitcmp1_b32 s44, s48\n\t" \
   "s_cbranch_scc0 176f\n\t" "76:\n\t" "s_mov_b32 s42, s43\n\t" "s_add_u32 s43, s43, 44032\n\t" "s_cmp_eq_u32 s43, 132096\n\t" \
   "s_cselect_b32 s43, 0, s43\n\t" "s_add_u32 s41, s41, 1\n\t" "s_add_u32 s40, s40, 1\n\t" "v_xor_b32 v14, 0x2000, v14\n\t" \
   "v_xor_b32 v15, 0x2000, v15\n\t" "v_mov_b32 v31, s54\n\t" "v_min_u32 v31, -2, v31\n\t" "1:\n\t" "v_mov_b32 v26, v28\n\t" "v_mov_b32 v27, v29\n\t" \
   "s_waitcnt lgkmcnt(5)\n\t" "v_mfma_f32_32x32x16_bf16 v[48:63], a[0:3], v[160:163], 0\n\t" "ds_read_b128 v[188:191], v26 offset:3584\n\t" \
   "ds_read_b128 v[156:159], v12\n\t" "v_mfma_f32_32x32x16_bf16 v[48:63], a[4:7], v[164:167], v[48:63]\n\t" \
   "ds_read_b128 v[160:163], v26 offset:4096\n\t" "s_add_u32 s50, s49, 262144\n\t" "v_cvt_pk_bf16_f32 v140, v140, v141\n\t" \
   "v_cvt_pk_bf16_f32 v141, v148, v149\n\t" "s_waitcnt lgkmcnt(6)\n\t" "v_mfma_f32_32x32x16_bf16 v[48:63], a[8:11], v[168:171], v[48:63]\n\t" \
   "ds_read_b128 v[164:167], v26 offset:4608\n\t" "s_waitcnt lgkmcnt(2)\n\t" "v_min3_u32 v156, v156, v157, v158\n\t" "v_min_u32 v156, v156, v159\n\t" \
   "v_cmp_ge_u32 vcc, v156, v138\n\t" "v_mfma_f32_32x32x16_bf16 v[48:63], a[12:15], v[172:175], v[48:63]\n\t" \
   "ds_read_b128 v[168:171], v26 offset:5120\n\t" "s_cbranch_vccz 150f\n\t" "50:\n\t" "ds_read_b128 v[96:99], v15 offset:0\n\t" \
   "v_mfma_f32_32x32x16_bf16 v[48:63], a[16:19], v[176:179], v[48:63]\n\t" "ds_read_b128 v[172:175], v26 offset:5632\n\t" \
   "ds_read_b128 v[100:103], v15 offset:1024\n\t" "ds_read_b128 v[104:107], v15 offset:2048\n\t" "ds_read_b128 v[108:111], v15 offset:3072\n\t" \
   "v_mfma_f32_32x32x16_bf16 v[48:63], a[20:23], v[180:183], v[48:63]\n\t" "ds_read_b128 v[176:179], v26 offset:6144\n\t" \
   "ds_read_b128 v[112:115], v15 offset:4096\n\t" "ds_read_b128 v[116:119], v15 offset:5120\n\t" \
   "v_mfma_f32_32x32x16_bf16 v[48:63], a[24:27], v[184:187], v[48:63]\n\t" "ds_read_b128 v[180:183], v26 offset:6656\n\t" \
   "ds_read_b128 v[120:123], v15 offset:6144\n\t" "ds_read_b128 v[124:127], v15 offset:7168\n\t" "s_mul_i32 s52, s45, 1536\n\t" \
   "s_mul_i32 s51, s45, 40960\n\t" "s_add_u32 m0, s46, %[ldx]\n\t" "s_mov_b32 s53, s46\n\t" \
   "v_mfma_f32_32x32x16_bf16 v[48:63], a[28:31], v[188:191], v[48:63]\n\t" "ds_read_b128 v[184:187], v26 offset:7168\n\t" \
   "v_cvt_pk_bf16_f32 v142, v142, v143\n\t" "v_cvt_pk_bf16_f32 v143, v150, v151\n\t" "s_waitcnt lgkmcnt(9)\n\t" \
   "v_mfma_f32_32x32x16_bf16 v[64:79], v[96:99], a[160:163], 0\n\t" "buffer_store_dwordx4 v[140:143], v18, %[rs], s50 offen offset:0 sc1\n\t" \
   "v_mfma_f32_32x32x16_bf16 v[48:63], a[32:35], v[160:163], v[48:63]\n\t" "ds_read_b128 v[188:191], v26 offset:7680\n\t" \
   "v_cvt_pk_bf16_f32 v144, v144, v145\n\t" "v_cvt_pk_bf16_f32 v145, v152, v153\n\t" "v_cvt_pk_bf16_f32 v146, v146, v147\n\t" \
   "v_cvt_pk_bf16_f32 v147, v154, v155\n\t" "v_mfma_f32_32x32x16_bf16 v[48:63], a[36:39], v[164:167], v[48:63]\n\t" \
   "ds_read_b128 v[160:163], v26 offset:8192\n\t" "v_add_u32 v28, s43, v22\n\t" "v_add_u32 v29, s43, v23\n\t" \
   "v_mfma_f32_32x32x16_bf16 v[64:79], v[100:103], a[176:179], v[64:79]\n\t" \
   "buffer_store_dwordx4 v[144:147], v18, %[rs], s50 offen offset:512 sc1\n\t" "s_sub_i32 s49, s41, 1\n\t" "s_mul_i32 s49, s49, 1310720\n\t" \
   "v_mfma_f32_32x32x16_bf16 v[48:63], a[40:43], v[168:171], v[48:63]\n\t" "ds_read_b128 v[164:167], v26 offset:8704\n\t" \
   "ds_read_b128 v[128:131], v27 offset:0\n\t" "buffer_load_dwordx4 v16, %[rs], s51 offen offset:0 sc1 lds\n\t" \
   "v_mfma_f32_32x32x16_bf16 v[48:63], a[44:47], v[172:175], v[48:63]\n\t" "ds_read_b128 v[168:171], v26 offset:9216\n\t" \
   "ds_read_b128 v[132:135], v27 offset:16\n\t" "ds_read_b64 v[136:137], v27 offset:32\n\t" "s_waitcnt lgkmcnt(14)\n\t" \
   "v_mfma_f32_32x32x16_bf16 v[64:79], v[104:107], a[164:167], v[64:79]\n\t" "s_waitcnt lgkmcnt(10)\n\t" \
   "v_mfma_f32_32x32x16_bf16 v[48:63], a[48:51], v[176:179], v[48:63]\n\t" "ds_read_b128 v[172:175], v26 offset:9728\n\t" \
   "v_mfma_f32_32x32x16_bf16 v[48:63], a[52:55], v[180:183], v[48:63]\n\t" "ds_read_b128 v[176:179], v26 offset:10240\n\t" \
   "buffer_load_dwordx4 v16, %[rs], s51 offen offset:1024 sc1 lds\n\t" "v_mfma_f32_32x32x16_bf16 v[64:79], v[108:111], a[180:183], v[64:79]\n\t" \
   "s_waitcnt lgkmcnt(2)\n\t" "v_add_f32 v128, v128, v130\n\t" "v_add_f32 v129, v129, v131\n\t" "v_add_f32 v132, v132, v134\n\t" \
   "v_add_f32 v133, v133, v135\n\t" "v_mfma_f32_32x32x16_bf16 v[48:63], a[56:59], v[184:187], v[48:63]\n\t" \
   "ds_read_b128 v[180:183], v26 offset:10752\n\t" "v_add_f32 v128, v128, v136\n\t" "v_add_f32 v129, v129, v137\n\t" "v_add_f32 v128, v128, v132\n\t" \
   "v_add_f32 v129, v129, v133\n\t" "v_mfma_f32_32x32x16_bf16 v[48:63], a[60:63], v[188:191], v[48:63]\n\t" \
   "ds_read_b128 v[184:187], v26 offset:11264\n\t" "v_mul_f32 v128, 0xbacccccd, v128\n\t" "v_mul_f32 v129, 0x3acccccd, v129\n\t" \
   "v_fma_f32 v129, -v128, v128, v129\n\t" "v_max_f32 v129, 0, v129\n\t" "v_mfma_f32_32x32x16_bf16 v[64:79], v[112:115], a[168:171], v[64:79]\n\t" \
   "buffer_load_dwordx4 v16, %[rs], s51 offen offset:2048 sc1 lds\n\t" "v_cvt_pk_bf16_f32 v129, v129, v128\n\t" "v_and_b32 v8, 0xffff0000, v129\n\t" \
   "v_mfma_f32_32x32x16_bf16 v[48:63], a[64:67], v[160:163], v[48:63]\n\t" "ds_read_b128 v[188:191], v26 offset:11776\n\t" \
   "v_lshlrev_b32 v129, 16, v129\n\t" "v_add_f32 v129, 0x3727c5ac, v129\n\t" "v_cvt_pk_bf16_f32 v129, v129, v129\n\t" \
   "v_lshlrev_b32 v129, 16, v129\n\t" "v_mfma_f32_32x32x16_bf16 v[48:63], a[68:71], v[164:167], v[48:63]\n\t" \
   "ds_read_b128 v[160:163], v26 offset:12288\n\t" "v_sqrt_f32 v129, v129\n\t" "s_nop 0\n\t" "v_cvt_pk_bf16_f32 v129, v129, v129\n\t" \
   "v_mfma_f32_32x32x16_bf16 v[64:79], v[116:119], a[184:187], v[64:79]\n\t" "v_lshlrev_b32 v129, 16, v129\n\t" "v_rcp_f32 v10, v129\n\t" \
   "s_waitcnt lgkmcnt(5)\n\t" "v_mfma_f32_32x32x16_bf16 v[48:63], a[72:75], v[168:171], v[48:63]\n\t" "ds_read_b128 v[164:167], v26 offset:12800\n\t" \
   "v_mfma_f32_32x32x16_bf16 v[48:63], a[76:79], v[172:175], v[48:63]\n\t" "ds_read_b128 v[168:171], v26 offset:13312\n\t" \
   "v_mfma_f32_32x32x16_bf16 v[64:79], v[120:123], a[172:175], v[64:79]\n\t" "buffer_load_dwordx4 v16, %[rs], s51 offen offset:3072 sc1 lds\n\t" \
   "s_add_u32 m0, m0, 4096\n\t" "s_add_u32 s51, s51, 4096\n\t" "s_waitcnt lgkmcnt(5)\n\t" \
   "v_mfma_f32_32x32x16_bf16 v[48:63], a[80:83], v[176:179], v[48:63]\n\t" "ds_read_b128 v[172:175], v26 offset:13824\n\t" \
   "v_mfma_f32_32x32x16_bf16 v[48:63], a[84:87], v[180:183], v[48:63]\n\t" "ds_read_b128 v[176:179], v26 offset:14336\n\t" \
   "buffer_load_dwordx4 v16, %[rs], s51 offen offset:0 sc1 lds\n\t" "v_mfma_f32_32x32x16_bf16 v[64:79], v[124:127], a[188:191], v[64:79]\n\t" \
   "s_waitcnt lgkmcnt(5)\n\t" "v_mfma_f32_32x32x16_bf16 v[48:63], a[88:91], v[184:187], v[48:63]\n\t" "ds_read_b128 v[180:183], v26 offset:14848\n\t" \
   "v_mfma_f32_32x32x16_bf16 v[48:63], a[92:95], v[188:191], v[48:63]\n\t" "ds_read_b128 v[184:187], v26 offset:15360\n\t" "s_add_u32 s50, s49, 0\n\t" \
   "v_cvt_pk_bf16_f32 v64, v64, v65\n\t" "v_cvt_pk_bf16_f32 v65, v72, v73\n\t" "v_cvt_pk_bf16_f32 v66, v66, v67\n\t" \
   "v_mfma_f32_32x32x16_bf16 v[80:95], v[96:99], a[192:195], 0\n\t" "v_cvt_pk_bf16_f32 v67, v74, v75\n\t" \
   "buffer_load_dwordx4 v16, %[rs], s51 offen offset:1024 sc1 lds\n\t" "s_waitcnt lgkmcnt(5)\n\t" \
   "v_mfma_f32_32x32x16_bf16 v[48:63], a[96:99], v[160:163], v[48:63]\n\t" "ds_read_b128 v[188:191], v26 offset:15872\n\t" \
   "v_mfma_f32_32x32x16_bf16 v[48:63], a[100:103], v[164:167], v[48:63]\n\t" "ds_read_b128 v[160:163], v26 offset:16384\n\t" \
   "v_mfma_f32_32x32x16_bf16 v[80:95], v[100:103], a[208:211], v[80:95]\n\t" "buffer_store_dwordx4 v[64:67], v21, %[rs], s50 offen offset:0 sc1\n\t" \
   "s_waitcnt lgkmcnt(5)\n\t" "v_mfma_f32_32x32x16_bf16 v[48:63], a[104:107], v[168:171], v[48:63]\n\t" "ds_read_b128 v[164:167], v26 offset:16896\n\t" \
   "v_cvt_pk_bf16_f32 v68, v68, v69\n\t" "v_cvt_pk_bf16_f32 v69, v76, v77\n\t" "v_cvt_pk_bf16_f32 v70, v70, v71\n\t" \
   "v_cvt_pk_bf16_f32 v71, v78, v79\n\t" "v_mfma_f32_32x32x16_bf16 v[48:63], a[108:111], v[172:175], v[48:63]\n\t" \
   "ds_read_b128 v[168:171], v26 offset:17408\n\t" "s_waitcnt vmcnt(9)\n\t" "v_add_u32 v138, 1, v138\n\t" "ds_write_b32 v13, v138\n\t" \
   "v_mfma_f32_32x32x16_bf16 v[80:95], v[104:107], a[196:199], v[80:95]\n\t" "buffer_store_dwordx4 v[68:71], v21, %[rs], s50 offen offset:512 sc1\n\t" \
   "s_waitcnt lgkmcnt(6)\n\t" "v_mfma_f32_32x32x16_bf16 v[48:63], a[112:115], v[176:179], v[48:63]\n\t" "ds_read_b128 v[172:175], v26 offset:17920\n\t" \
   "buffer_load_dwordx4 v16, %[rs], s51 offen offset:2048 sc1 lds\n\t" "v_mfma_f32_32x32x16_bf16 v[48:63], a[116:119], v[180:183], v[48:63]\n\t" \
   "ds_read_b128 v[176:179], v26 offset:18432\n\t" "v_mfma_f32_32x32x16_bf16 v[80:95], v[108:111], a[212:215], v[80:95]\n\t" \
   "buffer_load_dwordx4 v16, %[rs], s51 offen offset:3072 sc1 lds\n\t" "s_add_u32 m0, m0, 4096\n\t" "s_add_u32 s51, s51, 4096\n\t" \
   "s_waitcnt lgkmcnt(6)\n\t" "v_mfma_f32_32x32x16_bf16 v[48:63], a[120:123], v[184:187], v[48:63]\n\t" "ds_read_b128 v[180:183], v26 offset:18944\n\t" \
   "v_mfma_f32_32x32x16_bf16 v[48:63], a[124:127], v[188:191], v[48:63]\n\t" "ds_read_b128 v[184:187], v26 offset:19456\n\t" \
   "v_mfma_f32_32x32x16_bf16 v[80:95], v[112:115], a[200:203], v[80:95]\n\t" "buffer_load_dwordx4 v16, %[rs], s51 offen offset:0 sc1 lds\n\t" \
   "s_waitcnt lgkmcnt(6)\n\t" "v_mfma_f32_32x32x16_bf16 v[48:63], a[128:131], v[160:163], v[48:63]\n\t" "ds_read_b128 v[188:191], v26 offset:19968\n\t" \
   "v_mfma_f32_32x32x16_bf16 v[48:63], a[132:135], v[164:167], v[48:63]\n\t" "v_mfma_f32_32x32x16_bf16 v[80:95], v[116:119], a[216:219], v[80:95]\n\t" \
   "s_waitcnt lgkmcnt(4)\n\t" "v_mfma_f32_32x32x16_bf16 v[48:63], a[136:139], v[168:171], v[48:63]\n\t" "s_cmp_lt_i32 s40, 2\n\t" \
   "s_cselect_b32 s47, 0x7ffffff0, 0\n\t" "v_or_b32 v30, s47, v20\n\t" "s_sub_i32 s47, s41, 2\n\t" "s_max_i32 s47, s47, 0\n\t" \
   "s_lshl_b32 s47, s47, 12\n\t" "v_mfma_f32_32x32x16_bf16 v[48:63], a[140:143], v[172:175], v[48:63]\n\t" "s_waitcnt vmcnt(11)\n\t" \
   "buffer_load_dwordx4 v16, %[rs], s51 offen offset:1024 sc1 lds\n\t" "s_add_u32 m0, s53, %[ld1]\n\t" "s_nop 0\n\t" \
   "v_mfma_f32_32x32x16_bf16 v[80:95], v[120:123], a[204:207], v[80:95]\n\t" "buffer_store_dword v31, v30, %[rs], s47 offen sc1\n\t" \
   "s_waitcnt lgkmcnt(2)\n\t" "v_mfma_f32_32x32x16_bf16 v[48:63], a[144:147], v[176:179], v[48:63]\n\t" "ds_read_b128 v[156:159], v12\n\t" \
   "v_mfma_f32_32x32x16_bf16 v[48:63], a[148:151], v[180:183], v[48:63]\n\t" "buffer_load_dwordx4 v17, %[rs], s52 offen sc1 lds\n\t" \
   "v_mfma_f32_32x32x16_bf16 v[80:95], v[124:127], a[220:223], v[80:95]\n\t" "s_waitcnt lgkmcnt(1)\n\t" \
   "v_mfma_f32_32x32x16_bf16 v[48:63], a[152:155], v[184:187], v[48:63]\n\t" "s_waitcnt lgkmcnt(0)\n\t" "v_min3_u32 v156, v156, v157, v158\n\t" \
   "v_min_u32 v156, v156, v159\n\t" "v_cmp_ge_u32 vcc, v156, v138\n\t" "s_cbranch_vccz 152f\n\t" "52:\n\t" \
   "v_mfma_f32_32x32x16_bf16 v[48:63], a[156:159], v[188:191], v[48:63]\n\t" "s_add_u32 s50, s49, 65536\n\t" "v_cvt_pk_bf16_f32 v80, v80, v81\n\t" \
   "v_cvt_pk_bf16_f32 v81, v88, v89\n\t" "v_cvt_pk_bf16_f32 v82, v82, v83\n\t" "v_cvt_pk_bf16_f32 v83, v90, v91\n\t" \
   "v_mfma_f32_32x32x16_bf16 v[64:79], v[96:99], a[224:227], 0\n\t" "buffer_store_dwordx4 v[80:83], v21, %[rs], s50 offen offset:0 sc1\n\t" \
   "v_cvt_pk_bf16_f32 v84, v84, v85\n\t" "v_mfma_f32_32x32x16_bf16 v[64:79], v[100:103], a[240:243], v[64:79]\n\t" "v_fmac_f32 v48, v32, v8\n\t" \
   "v_fmac_f32 v49, v33, v8\n\t" "v_fmac_f32 v50, v34, v8\n\t" "v_fmac_f32 v51, v35, v8\n\t" "v_fmac_f32 v52, v36, v8\n\t" \
   "v_fmac_f32 v53, v37, v8\n\t" "v_mfma_f32_32x32x16_bf16 v[64:79], v[104:107], a[228:231], v[64:79]\n\t" "v_fmac_f32 v54, v38, v8\n\t" \
   "v_fmac_f32 v55, v39, v8\n\t" "v_fmac_f32 v56, v40, v8\n\t" "v_fmac_f32 v57, v41, v8\n\t" "v_fmac_f32 v58, v42, v8\n\t" \
   "v_fmac_f32 v59, v43, v8\n\t" "v_mfma_f32_32x32x16_bf16 v[64:79], v[108:111], a[244:247], v[64:79]\n\t" "v_fmac_f32 v60, v44, v8\n\t" \
   "v_fmac_f32 v61, v45, v8\n\t" "v_fmac_f32 v62, v46, v8\n\t" "v_fmac_f32 v63, v47, v8\n\t" "v_mul_f32 v48, v48, v10\n\t" \
   "v_mul_f32 v49, v49, v10\n\t" "v_mfma_f32_32x32x16_bf16 v[64:79], v[112:115], a[232:235], v[64:79]\n\t" "v_mul_f32 v50, v50, v10\n\t" \
   "v_mul_f32 v51, v51, v10\n\t" "v_mul_f32 v52, v52, v10\n\t" "v_mul_f32 v53, v53, v10\n\t" "v_mul_f32 v54, v54, v10\n\t" \
   "v_mul_f32 v55, v55, v10\n\t" "v_mfma_f32_32x32x16_bf16 v[64:79], v[116:119], a[248:251], v[64:79]\n\t" "v_mul_f32 v56, v56, v10\n\t" \
   "v_mul_f32 v57, v57, v10\n\t" "v_mul_f32 v58, v58, v10\n\t" "v_mul_f32 v59, v59, v10\n\t" "v_mul_f32 v60, v60, v10\n\t" \
   "v_mul_f32 v61, v61, v10\n\t" "v_mfma_f32_32x32x16_bf16 v[64:79], v[120:123], a[236:239], v[64:79]\n\t" "v_cvt_pk_bf16_f32 v85, v92, v93\n\t" \
   "v_cvt_pk_bf16_f32 v86, v86, v87\n\t" "v_cvt_pk_bf16_f32 v87, v94, v95\n\t" \
   "buffer_store_dwordx4 v[84:87], v21, %[rs], s50 offen offset:512 sc1\n\t" "v_mfma_f32_32x32x16_bf16 v[64:79], v[124:127], a[252:255], v[64:79]\n\t" \
   "v_mul_f32 v62, v62, v10\n\t" "v_mul_f32 v63, v63, v10\n\t" "v_cvt_pk_bf16_f32 v48, v48, v49\n\t" "v_cvt_pk_bf16_f32 v49, v50, v51\n\t" \
   "v_cvt_pk_bf16_f32 v50, v52, v53\n\t" "v_cvt_pk_bf16_f32 v51, v54, v55\n\t" "v_mfma_f32_32x32x16_bf16 v[80:95], v[96:99], v[192:195], 0\n\t" \
   "ds_write_b128 v14, v[48:51] offset:0\n\t" "v_cvt_pk_bf16_f32 v56, v56, v57\n\t" "v_cvt_pk_bf16_f32 v57, v58, v59\n\t" \
   "v_mfma_f32_32x32x16_bf16 v[80:95], v[100:103], v[208:211], v[80:95]\n\t" "v_cvt_pk_bf16_f32 v58, v60, v61\n\t" \
   "v_cvt_pk_bf16_f32 v59, v62, v63\n\t" "ds_write_b128 v14, v[56:59] offset:1024\n\t" "v_add_u32 v138, 1, v138\n\t" "ds_write_b32 v13, v138\n\t" \
   "v_mfma_f32_32x32x16_bf16 v[80:95], v[104:107], v[196:199], v[80:95]\n\t" "s_add_u32 s50, s49, 131072\n\t" "v_cvt_pk_bf16_f32 v64, v64, v65\n\t" \
   "v_cvt_pk_bf16_f32 v65, v72, v73\n\t" "v_cvt_pk_bf16_f32 v66, v66, v67\n\t" "v_cvt_pk_bf16_f32 v67, v74, v75\n\t" "s_add_u32 s45, s40, 3\n\t" \
   "v_mfma_f32_32x32x16_bf16 v[80:95], v[108:111], v[212:215], v[80:95]\n\t" "buffer_store_dwordx4 v[64:67], v21, %[rs], s50 offen offset:0 sc1\n\t" \
   "v_cvt_pk_bf16_f32 v68, v68, v69\n\t" "v_mfma_f32_32x32x16_bf16 v[80:95], v[112:115], v[200:203], v[80:95]\n\t" \
   "v_cvt_pk_bf16_f32 v69, v76, v77\n\t" "v_cvt_pk_bf16_f32 v70, v70, v71\n\t" "v_cvt_pk_bf16_f32 v71, v78, v79\n\t" "s_min_u32 s45, s45, 63\n\t" \
   "s_mov_b32 s46, s42\n\t" "v_mfma_f32_32x32x16_bf16 v[80:95], v[116:119], v[216:219], v[80:95]\n\t" \
   "buffer_store_dwordx4 v[68:71], v21, %[rs], s50 offen offset:512 sc1\n\t" "v_mfma_f32_32x32x16_bf16 v[80:95], v[120:123], v[204:207], v[80:95]\n\t" \
   "s_lshr_b32 s48, s45, 1\n\t" "s_bitcmp1_b32 s44, s48\n\t" "s_cbranch_scc0 174f\n\t" "74:\n\t" "ds_read_b128 v[160:163], v28 offset:0\n\t" \
   "ds_read_b128 v[164:167], v28 offset:512\n\t" "v_mfma_f32_32x32x16_bf16 v[80:95], v[124:127], v[220:223], v[80:95]\n\t" "s_mov_b32 s42, s43\n\t" \
   "ds_read_b128 v[168:171], v28 offset:1024\n\t" "s_add_u32 s43, s43, 44032\n\t" "s_cmp_eq_u32 s43, 132096\n\t" "s_cselect_b32 s43, 0, s43\n\t" \
   "v_mfma_f32_32x32x16_bf16 v[140:155], v[96:99], v[224:227], 0\n\t" "ds_read_b128 v[172:175], v28 offset:1536\n\t" \
   "ds_read_b128 v[176:179], v28 offset:2048\n\t" "s_add_u32 s41, s41, 1\n\t" "s_add_u32 s40, s40, 1\n\t" "v_xor_b32 v14, 0x2000, v14\n\t" \
   "v_mfma_f32_32x32x16_bf16 v[140:155], v[100:103], v[240:243], v[140:155]\n\t" "s_add_u32 s50, s49, 196608\n\t" "v_cvt_pk_bf16_f32 v80, v80, v81\n\t" \
   "v_cvt_pk_bf16_f32 v81, v88, v89\n\t" "v_cvt_pk_bf16_f32 v82, v82, v83\n\t" "v_cvt_pk_bf16_f32 v83, v90, v91\n\t" "v_xor_b32 v15, 0x2000, v15\n\t" \
   "v_mfma_f32_32x32x16_bf16 v[140:155], v[104:107], v[228:231], v[140:155]\n\t" \
   "buffer_store_dwordx4 v[80:83], v21, %[rs], s50 offen offset:0 sc1\n\t" "v_cvt_pk_bf16_f32 v84, v84, v85\n\t" \
   "v_mfma_f32_32x32x16_bf16 v[140:155], v[108:111], v[244:247], v[140:155]\n\t" "v_cvt_pk_bf16_f32 v85, v92, v93\n\t" \
   "v_cvt_pk_bf16_f32 v86, v86, v87\n\t" "v_cvt_pk_bf16_f32 v87, v94, v95\n\t" "ds_read_b128 v[180:183], v28 offset:2560\n\t" \
   "ds_read_b128 v[184:187], v28 offset:3072\n\t" "v_mfma_f32_32x32x16_bf16 v[140:155], v[112:115], v[232:235], v[140:155]\n\t" \
   "buffer_store_dwordx4 v[84:87], v21, %[rs], s50 offen offset:512 sc1\n\t" "v_mov_b32 v18, v21\n\t" \
   "v_mfma_f32_32x32x16_bf16 v[140:155], v[116:119], v[248:251], v[140:155]\n\t" \
   "v_mfma_f32_32x32x16_bf16 v[140:155], v[120:123], v[236:239], v[140:155]\n\t" \
   "v_mfma_f32_32x32x16_bf16 v[140:155], v[124:127], v[252:255], v[140:155]\n\t" "s_cmp_lt_u32 s40, 64\n\t" "s_cbranch_scc1 1b\n\t" "s_nop 7\n\t" \
   "s_nop 2\n\t" "s_waitcnt lgkmcnt(0)\n\t" "s_barrier\n\t" "s_add_u32 s50, s49, 262144\n\t" "v_cvt_pk_bf16_f32 v140, v140, v141\n\t" \
   "v_cvt_pk_bf16_f32 v141, v148, v149\n\t" "v_cvt_pk_bf16_f32 v142, v142, v143\n\t" "v_cvt_pk_bf16_f32 v143, v150, v151\n\t" \
   "buffer_store_dwordx4 v[140:143], v21, %[rs], s50 offen offset:0 sc1\n\t" "v_cvt_pk_bf16_f32 v144, v144, v145\n\t" \
   "v_cvt_pk_bf16_f32 v145, v152, v153\n\t" "v_cvt_pk_bf16_f32 v146, v146, v147\n\t" "v_cvt_pk_bf16_f32 v147, v154, v155\n\t" \
   "buffer_store_dwordx4 v[144:147], v21, %[rs], s50 offen offset:512 sc1\n\t" "s_sub_i32 s49, s41, 1\n\t" "s_mul_i32 s49, s49, 1310720\n\t" \
   "ds_read_b128 v[96:99], v15 offset:0\n\t" "ds_read_b128 v[100:103], v15 offset:1024\n\t" "ds_read_b128 v[104:107], v15 offset:2048\n\t" \
   "ds_read_b128 v[108:111], v15 offset:3072\n\t" "ds_read_b128 v[112:115], v15 offset:4096\n\t" "ds_read_b128 v[116:119], v15 offset:5120\n\t" \
   "ds_read_b128 v[120:123], v15 offset:6144\n\t" "ds_read_b128 v[124:127], v15 offset:7168\n\t" "s_waitcnt lgkmcnt(6)\n\t" \
   "v_mfma_f32_32x32x16_bf16 v[64:79], v[96:99], a[160:163], 0\n\t" "v_mfma_f32_32x32x16_bf16 v[64:79], v[100:103], a[176:179], v[64:79]\n\t" \
   "s_waitcnt lgkmcnt(4)\n\t" "v_mfma_f32_32x32x16_bf16 v[64:79], v[104:107], a[164:167], v[64:79]\n\t" \
   "v_mfma_f32_32x32x16_bf16 v[64:79], v[108:111], a[180:183], v[64:79]\n\t" "s_waitcnt lgkmcnt(2)\n\t" \
   "v_mfma_f32_32x32x16_bf16 v[64:79], v[112:115], a[168:171], v[64:79]\n\t" "v_mfma_f32_32x32x16_bf16 v[64:79], v[116:119], a[184:187], v[64:79]\n\t" \
   "s_waitcnt lgkmcnt(0)\n\t" "v_mfma_f32_32x32x16_bf16 v[64:79], v[120:123], a[172:175], v[64:79]\n\t" \
   "v_mfma_f32_32x32x16_bf16 v[64:79], v[124:127], a[188:191], v[64:79]\n\t" "v_mfma_f32_32x32x16_bf16 v[80:95], v[96:99], a[192:195], 0\n\t" \
   "v_mfma_f32_32x32x16_bf16 v[80:95], v[100:103], a[208:211], v[80:95]\n\t" "s_add_u32 s50, s49, 0\n\t" "v_cvt_pk_bf16_f32 v64, v64, v65\n\t" \
   "v_cvt_pk_bf16_f32 v65, v72, v73\n\t" "v_cvt_pk_bf16_f32 v66, v66, v67\n\t" "v_cvt_pk_bf16_f32 v67, v74, v75\n\t" \
   "v_mfma_f32_32x32x16_bf16 v[80:95], v[104:107], a[196:199], v[80:95]\n\t" "buffer_store_dwordx4 v[64:67], v21, %[rs], s50 offen offset:0 sc1\n\t" \
   "v_cvt_pk_bf16_f32 v68, v68, v69\n\t" "v_mfma_f32_32x32x16_bf16 v[80:95], v[108:111], a[212:215], v[80:95]\n\t" \
   "v_cvt_pk_bf16_f32 v69, v76, v77\n\t" "v_cvt_pk_bf16_f32 v70, v70, v71\n\t" "v_cvt_pk_bf16_f32 v71, v78, v79\n\t" \
   "v_mfma_f32_32x32x16_bf16 v[80:95], v[112:115], a[200:203], v[80:95]\n\t" "buffer_store_dwordx4 v[68:71], v21, %[rs], s50 offen offset:512 sc1\n\t" \
   "v_mfma_f32_32x32x16_bf16 v[80:95], v[116:119], a[216:219], v[80:95]\n\t" "v_mfma_f32_32x32x16_bf16 v[80:95], v[120:123], a[204:207], v[80:95]\n\t" \
   "v_mfma_f32_32x32x16_bf16 v[80:95], v[124:127], a[220:223], v[80:95]\n\t" "v_mfma_f32_32x32x16_bf16 v[64:79], v[96:99], a[224:227], 0\n\t" \
   "v_mfma_f32_32x32x16_bf16 v[64:79], v[100:103], a[240:243], v[64:79]\n\t" "s_add_u32 s50, s49, 65536\n\t" "v_cvt_pk_bf16_f32 v80, v80, v81\n\t" \
   "v_cvt_pk_bf16_f32 v81, v88, v89\n\t" "v_cvt_pk_bf16_f32 v82, v82, v83\n\t" "v_cvt_pk_bf16_f32 v83, v90, v91\n\t" \
   "v_mfma_f32_32x32x16_bf16 v[64:79], v[104:107], a[228:231], v[64:79]\n\t" "buffer_store_dwordx4 v[80:83], v21, %[rs], s50 offen offset:0 sc1\n\t" \
   "v_cvt_pk_bf16_f32 v84, v84, v85\n\t" "v_mfma_f32_32x32x16_bf16 v[64:79], v[108:111], a[244:247], v[64:79]\n\t" \
   "v_cvt_pk_bf16_f32 v85, v92, v93\n\t" "v_cvt_pk_bf16_f32 v86, v86, v87\n\t" "v_cvt_pk_bf16_f32 v87, v94, v95\n\t" \
   "v_mfma_f32_32x32x16_bf16 v[64:79], v[112:115], a[232:235], v[64:79]\n\t" "buffer_store_dwordx4 v[84:87], v21, %[rs], s50 offen offset:512 sc1\n\t" \
   "v_mfma_f32_32x32x16_bf16 v[64:79], v[116:119], a[248:251], v[64:79]\n\t" "v_mfma_f32_32x32x16_bf16 v[64:79], v[120:123], a[236:239], v[64:79]\n\t" \
   "v_mfma_f32_32x32x16_bf16 v[64:79], v[124:127], a[252:255], v[64:79]\n\t" "v_mfma_f32_32x32x16_bf16 v[80:95], v[96:99], v[192:195], 0\n\t" \
   "v_mfma_f32_32x32x16_bf16 v[80:95], v[100:103], v[208:211], v[80:95]\n\t" "s_add_u32 s50, s49, 131072\n\t" "v_cvt_pk_bf16_f32 v64, v64, v65\n\t" \
   "v_cvt_pk_bf16_f32 v65, v72, v73\n\t" "v_cvt_pk_bf16_f32 v66, v66, v67\n\t" "v_cvt_pk_bf16_f32 v67, v74, v75\n\t" \
   "v_mfma_f32_32x32x16_bf16 v[80:95], v[104:107], v[196:199], v[80:95]\n\t" "buffer_store_dwordx4 v[64:67], v21, %[rs], s50 offen offset:0 sc1\n\t" \
   "v_cvt_pk_bf16_f32 v68, v68, v69\n\t" "v_mfma_f32_32x32x16_bf16 v[80:95], v[108:111], v[212:215], v[80:95]\n\t" \
   "v_cvt_pk_bf16_f32 v69, v76, v77\n\t" "v_cvt_pk_bf16_f32 v70, v70, v71\n\t" "v_cvt_pk_bf16_f32 v71, v78, v79\n\t" \
   "v_mfma_f32_32x32x16_bf16 v[80:95], v[112:115], v[200:203], v[80:95]\n\t" "buffer_store_dwordx4 v[68:71], v21, %[rs], s50 offen offset:512 sc1\n\t" \
   "v_mfma_f32_32x32x16_bf16 v[80:95], v[116:119], v[216:219], v[80:95]\n\t" "v_mfma_f32_32x32x16_bf16 v[80:95], v[120:123], v[204:207], v[80:95]\n\t" \
   "v_mfma_f32_32x32x16_bf16 v[80:95], v[124:127], v[220:223], v[80:95]\n\t" "v_mfma_f32_32x32x16_bf16 v[140:155], v[96:99], v[224:227], 0\n\t" \
   "v_mfma_f32_32x32x16_bf16 v[140:155], v[100:103], v[240:243], v[140:155]\n\t" "s_add_u32 s50, s49, 196608\n\t" "v_cvt_pk_bf16_f32 v80, v80, v81\n\t" \
   "v_cvt_pk_bf16_f32 v81, v88, v89\n\t" "v_cvt_pk_bf16_f32 v82, v82, v83\n\t" "v_cvt_pk_bf16_f32 v83, v90, v91\n\t" \
   "v_mfma_f32_32x32x16_bf16 v[140:155], v[104:107], v[228:231], v[140:155]\n\t" \
   "buffer_store_dwordx4 v[80:83], v21, %[rs], s50 offen offset:0 sc1\n\t" "v_cvt_pk_bf16_f32 v84, v84, v85\n\t" \
   "v_mfma_f32_32x32x16_bf16 v[140:155], v[108:111], v[244:247], v[140:155]\n\t" "v_cvt_pk_bf16_f32 v85, v92, v93\n\t" \
   "v_cvt_pk_bf16_f32 v86, v86, v87\n\t" "v_cvt_pk_bf16_f32 v87, v94, v95\n\t" \
   "v_mfma_f32_32x32x16_bf16 v[140:155], v[112:115], v[232:235], v[140:155]\n\t" \
   "buffer_store_dwordx4 v[84:87], v21, %[rs], s50 offen offset:512 sc1\n\t" \
   "v_mfma_f32_32x32x16_bf16 v[140:155], v[116:119], v[248:251], v[140:155]\n\t" \
   "v_mfma_f32_32x32x16_bf16 v[140:155], v[120:123], v[236:239], v[140:155]\n\t" \
   "v_mfma_f32_32x32x16_bf16 v[140:155], v[124:127], v[252:255], v[140:155]\n\t" "s_nop 7\n\t" "s_nop 2\n\t" "s_add_u32 s50, s49, 262144\n\t" \
   "v_cvt_pk_bf16_f32 v140, v140, v141\n\t" "v_cvt_pk_bf16_f32 v141, v148, v149\n\t" "v_cvt_pk_bf16_f32 v142, v142, v143\n\t" \
   "v_cvt_pk_bf16_f32 v143, v150, v151\n\t" "buffer_store_dwordx4 v[140:143], v21, %[rs], s50 offen offset:0 sc1\n\t" \
   "v_cvt_pk_bf16_f32 v144, v144, v145\n\t" "v_cvt_pk_bf16_f32 v145, v152, v153\n\t" "v_cvt_pk_bf16_f32 v146, v146, v147\n\t" \
   "v_cvt_pk_bf16_f32 v147, v154, v155\n\t" "buffer_store_dwordx4 v[144:147], v21, %[rs], s50 offen offset:512 sc1\n\t" "s_waitcnt vmcnt(0)\n\t" \
   "s_cmp_lt_i32 s40, 2\n\t" "s_cselect_b32 s47, 0x7ffffff0, 0\n\t" "v_or_b32 v30, s47, v20\n\t" "s_sub_i32 s47, s41, 2\n\t" \
   "s_max_i32 s47, s47, 0\n\t" "s_lshl_b32 s47, s47, 12\n\t" "buffer_store_dword v31, v30, %[rs], s47 offen sc1\n\t" "s_cmp_lt_i32 s40, 1\n\t" \
   "s_cselect_b32 s47, 0x7ffffff0, 0\n\t" "v_or_b32 v30, s47, v20\n\t" "s_sub_i32 s47, s41, 1\n\t" "s_max_i32 s47, s47, 0\n\t" \
   "s_lshl_b32 s47, s47, 12\n\t" "buffer_store_dword v31, v30, %[rs], s47 offen sc1\n\t" "s_branch 9f\n\t" "172:\n\t" "s_mov_b32 s47, 0\n\t" \
   "buffer_load_dword v30, v19, %[rs], 0 offen sc1\n\t" "s_waitcnt vmcnt(0)\n\t" "v_add_u32 v30, 1, v30\n\t" "v_cmp_lt_u32 vcc, 7, v30\n\t" \
   "s_mov_b32 s44, vcc_lo\n\t" "s_bitcmp1_b32 s44, s48\n\t" "s_cbranch_scc1 72b\n\t" "s_bitset1_b32 s44, s48\n\t" "s_lshl_b32 s48, s48, 7\n\t" \
   "s_add_u32 s48, s48, %[cnt]\n\t" "73:\n\t" "s_sleep 10\n\t" "buffer_load_dword v30, off, %[rs], s48 sc1\n\t" "s_add_u32 s47, s47, 1\n\t" \
   "s_waitcnt vmcnt(0)\n\t" "v_add_u32 v30, 1, v30\n\t" "v_cmp_lt_u32 vcc, 7, v30\n\t" "s_cmp_gt_u32 s47, 0x20000\n\t" "s_cbranch_scc1 72b\n\t" \
   "s_cbranch_vccz 73b\n\t" "s_branch 72b\n\t" "176:\n\t" "s_mov_b32 s47, 0\n\t" "buffer_load_dword v30, v19, %[rs], 0 offen sc1\n\t" \
   "s_waitcnt vmcnt(0)\n\t" "v_add_u32 v30, 1, v30\n\t" "v_cmp_lt_u32 vcc, 7, v30\n\t" "s_mov_b32 s44, vcc_lo\n\t" "s_bitcmp1_b32 s44, s48\n\t" \
   "s_cbranch_scc1 76b\n\t" "s_bitset1_b32 s44, s48\n\t" "s_lshl_b32 s48, s48, 7\n\t" "s_add_u32 s48, s48, %[cnt]\n\t" "77:\n\t" "s_sleep 10\n\t" \
   "buffer_load_dword v30, off, %[rs], s48 sc1\n\t" "s_add_u32 s47, s47, 1\n\t" "s_waitcnt vmcnt(0)\n\t" "v_add_u32 v30, 1, v30\n\t" \
   "v_cmp_lt_u32 vcc, 7, v30\n\t" "s_cmp_gt_u32 s47, 0x20000\n\t" "s_cbranch_scc1 76b\n\t" "s_cbranch_vccz 77b\n\t" "s_branch 76b\n\t" "174:\n\t" \
   "s_mov_b32 s47, 0\n\t" "buffer_load_dword v30, v19, %[rs], 0 offen sc1\n\t" "s_waitcnt vmcnt(0)\n\t" "v_add_u32 v30, 1, v30\n\t" \
   "v_cmp_lt_u32 vcc, 7, v30\n\t" "s_mov_b32 s44, vcc_lo\n\t" "s_bitcmp1_b32 s44, s48\n\t" "s_cbranch_scc1 74b\n\t" "s_bitset1_b32 s44, s48\n\t" \
   "s_lshl_b32 s48, s48, 7\n\t" "s_add_u32 s48, s48, %[cnt]\n\t" "75:\n\t" "s_sleep 10\n\t" "buffer_load_dword v30, off, %[rs], s48 sc1\n\t" \
   "s_add_u32 s47, s47, 1\n\t" "s_waitcnt vmcnt(0)\n\t" "v_add_u32 v30, 1, v30\n\t" "v_cmp_lt_u32 vcc, 7, v30\n\t" "s_cmp_gt_u32 s47, 0x20000\n\t" \
   "s_cbranch_scc1 74b\n\t" "s_cbranch_vccz 75b\n\t" "s_branch 74b\n\t" "150:\n\t" "ds_read_b128 v[156:159], v12\n\t" "s_waitcnt lgkmcnt(0)\n\t" \
   "v_min3_u32 v156, v156, v157, v158\n\t" "v_min_u32 v156, v156, v159\n\t" "v_cmp_ge_u32 vcc, v156, v138\n\t" "s_cbranch_vccz 150b\n\t" \
   "s_branch 50b\n\t" "152:\n\t" "ds_read_b128 v[156:159], v12\n\t" "s_waitcnt lgkmcnt(0)\n\t" "v_min3_u32 v156, v156, v157, v158\n\t" \
   "v_min_u32 v156, v156, v159\n\t" "v_cmp_ge_u32 vcc, v156, v138\n\t" "s_cbranch_vccz 152b\n\t" "s_branch 52b\n\t" "9:\n\t" "s_waitcnt vmcnt(0)"
#define VU_CLOB "v8", "v9", "v10", "v11", "v12", "v13", "v14", "v15", "v32", "v33", "v34", "v35", "v36", "v37", "v38", "v39", "v40", "v41", "v42", "v43", "v44", "v45", "v46", "v47", "v48", "v49", "v50", "v51", "v52", "v53", "v54", "v55", "v56", "v57", "v58", "v59", "v60", "v61", "v62", "v63", "v64", "v65", "v66", "v67", "v68", "v69", "v70", "v71", "v72", "v73", "v74", "v75", "v76", "v77", "v78", "v79", "v80", "v81", "v82", "v83", "v84", "v85", "v86", "v87", "v88", "v89", "v90", "v91", "v92", "v93", "v94", "v95", "v96", "v97", "v98", "v99", "v100", "v101", "v102", "v103", "v104", "v105", "v106", "v107", "v108", "v109", "v110", "v111", "v112", "v113", "v114", "v115", "v116", "v117", "v118", "v119", "v120", "v121", "v122", "v123", "v124", "v125", "v126", "v127", "v128", "v129", "v130", "v131", "v132", "v133", "v134", "v135", "v136", "v137", "v138", "v139", "v140", "v141", "v142", "v143", "v144", "v145", "v146", "v147", "v148", "v149", "v150", "v151", "v152", "v153", "v154", "v155", "v156", "v157", "v158", "v159", "v160", "v161", "v162", "v163", "v164", "v165", "v166", "v167", "v168", "v169", "v170", "v171", "v172", "v173", "v174", "v175", "v176", "v177", "v178", "v179", "v180", "v181", "v182", "v183", "v184", "v185", "v186", "v187", "v188", "v189", "v190", "v191", "s40", "s41", "s42", "s43", "s44", "s45", "s46", "s47", "s48", "s49", "s50", "s51", "s52", "s53", "s54", "s55"
#define DF_ASM \
   "v_mov_b32 v80, 0x3f803f80\n\t" "v_mov_b32 v81, 0x3f803f80\n\t" "v_mov_b32 v82, 0x3f803f80\n\t" "v_mov_b32 v83, 0x3f803f80\n\t" \
   "s_mov_b32 s45, %[set]\n\t" "s_mov_b32 s46, 0\n\t" "v_mfma_f32_16x16x32_bf16 v[32:35], a[0:3], v[80:83], 0\n\t" \
   "v_mfma_f32_16x16x32_bf16 v[32:35], a[4:7], v[80:83], v[32:35]\n\t" "v_mfma_f32_16x16x32_bf16 v[32:35], a[8:11], v[80:83], v[32:35]\n\t" \
   "v_mfma_f32_16x16x32_bf16 v[32:35], a[12:15], v[80:83], v[32:35]\n\t" "v_mfma_f32_16x16x32_bf16 v[32:35], a[16:19], v[80:83], v[32:35]\n\t" \
   "v_mfma_f32_16x16x32_bf16 v[32:35], a[20:23], v[80:83], v[32:35]\n\t" "v_mfma_f32_16x16x32_bf16 v[32:35], a[24:27], v[80:83], v[32:35]\n\t" \
   "v_mfma_f32_16x16x32_bf16 v[32:35], a[28:31], v[80:83], v[32:35]\n\t" "v_mfma_f32_16x16x32_bf16 v[32:35], a[32:35], v[80:83], v[32:35]\n\t" \
   "v_mfma_f32_16x16x32_bf16 v[32:35], a[36:39], v[80:83], v[32:35]\n\t" "v_mfma_f32_16x16x32_bf16 v[32:35], a[40:43], v[80:83], v[32:35]\n\t" \
   "v_mfma_f32_16x16x32_bf16 v[32:35], a[44:47], v[80:83], v[32:35]\n\t" "v_mfma_f32_16x16x32_bf16 v[32:35], a[48:51], v[80:83], v[32:35]\n\t" \
   "v_mfma_f32_16x16x32_bf16 v[32:35], a[52:55], v[80:83], v[32:35]\n\t" "v_mfma_f32_16x16x32_bf16 v[32:35], a[56:59], v[80:83], v[32:35]\n\t" \
   "v_mfma_f32_16x16x32_bf16 v[32:35], a[60:63], v[80:83], v[32:35]\n\t" "v_mfma_f32_16x16x32_bf16 v[32:35], a[64:67], v[80:83], v[32:35]\n\t" \
   "v_mfma_f32_16x16x32_bf16 v[32:35], a[68:71], v[80:83], v[32:35]\n\t" "v_mfma_f32_16x16x32_bf16 v[32:35], a[72:75], v[80:83], v[32:35]\n\t" \
   "v_mfma_f32_16x16x32_bf16 v[32:35], a[76:79], v[80:83], v[32:35]\n\t" "v_mfma_f32_16x16x32_bf16 v[36:39], a[80:83], v[80:83], 0\n\t" \
   "v_mfma_f32_16x16x32_bf16 v[36:39], a[84:87], v[80:83], v[36:39]\n\t" "v_mfma_f32_16x16x32_bf16 v[36:39], a[88:91], v[80:83], v[36:39]\n\t" \
   "v_mfma_f32_16x16x32_bf16 v[36:39], a[92:95], v[80:83], v[36:39]\n\t" "v_mfma_f32_16x16x32_bf16 v[36:39], a[96:99], v[80:83], v[36:39]\n\t" \
   "v_mfma_f32_16x16x32_bf16 v[36:39], a[100:103], v[80:83], v[36:39]\n\t" "v_mfma_f32_16x16x32_bf16 v[36:39], a[104:107], v[80:83], v[36:39]\n\t" \
   "v_mfma_f32_16x16x32_bf16 v[36:39], a[108:111], v[80:83], v[36:39]\n\t" "v_mfma_f32_16x16x32_bf16 v[36:39], a[112:115], v[80:83], v[36:39]\n\t" \
   "v_mfma_f32_16x16x32_bf16 v[36:39], a[116:119], v[80:83], v[36:39]\n\t" "v_mfma_f32_16x16x32_bf16 v[36:39], a[120:123], v[80:83], v[36:39]\n\t" \
   "v_mfma_f32_16x16x32_bf16 v[36:39], a[124:127], v[80:83], v[36:39]\n\t" "v_mfma_f32_16x16x32_bf16 v[36:39], a[128:131], v[80:83], v[36:39]\n\t" \
   "v_mfma_f32_16x16x32_bf16 v[36:39], a[132:135], v[80:83], v[36:39]\n\t" "v_mfma_f32_16x16x32_bf16 v[36:39], a[136:139], v[80:83], v[36:39]\n\t" \
   "v_mfma_f32_16x16x32_bf16 v[36:39], a[140:143], v[80:83], v[36:39]\n\t" "v_mfma_f32_16x16x32_bf16 v[36:39], a[144:147], v[80:83], v[36:39]\n\t" \
   "v_mfma_f32_16x16x32_bf16 v[36:39], a[148:151], v[80:83], v[36:39]\n\t" "v_mfma_f32_16x16x32_bf16 v[36:39], a[152:155], v[80:83], v[36:39]\n\t" \
   "v_mfma_f32_16x16x32_bf16 v[36:39], a[156:159], v[80:83], v[36:39]\n\t" "1:\n\t" "s_lshl_b32 s48, s45, 7\n\t" "s_mov_b32 s47, 0\n\t" \
   "s_cmp_lg_u32 %[ldx], %[ld0]\n\t" "s_cbranch_scc1 3f\n\t" "2:\n\t" "buffer_load_dword v30, v19, %[rs], s48 offen sc1\n\t" \
   "s_add_u32 s47, s47, 1\n\t" "s_waitcnt vmcnt(0)\n\t" "v_readfirstlane_b32 s44, v30\n\t" "s_add_u32 s44, s44, 1\n\t" "s_cmp_ge_u32 s44, 10\n\t" \
   "s_cbranch_scc1 3f\n\t" "s_cmp_gt_u32 s47, 0x20000\n\t" "s_cbranch_scc1 3f\n\t" "s_cmp_lt_u32 s45, 56\n\t" "s_cbranch_scc1 4f\n\t" "s_sleep 3\n\t" \
   "s_cmp_lg_u32 s44, 0\n\t" "s_cbranch_scc1 2b\n\t" "s_sleep 8\n\t" "s_branch 2b\n\t" "4:\n\t" "s_sleep 60\n\t" "s_branch 2b\n\t" "3:\n\t" \
   "s_barrier\n\t" "s_mul_i32 s52, s45, 1536\n\t" "s_mul_i32 s51, s45, 40960\n\t" "s_add_u32 m0, s46, %[ldx]\n\t" "s_mov_b32 s53, s46\n\t" \
   "buffer_load_dwordx4 v16, %[rs], s51 offen offset:0 sc1 lds\n\t" "buffer_load_dwordx4 v16, %[rs], s51 offen offset:1024 sc1 lds\n\t" \
   "buffer_load_dwordx4 v16, %[rs], s51 offen offset:2048 sc1 lds\n\t" "buffer_load_dwordx4 v16, %[rs], s51 offen offset:3072 sc1 lds\n\t" \
   "s_add_u32 m0, m0, 4096\n\t" "s_add_u32 s51, s51, 4096\n\t" "buffer_load_dwordx4 v16, %[rs], s51 offen offset:0 sc1 lds\n\t" \
   "buffer_load_dwordx4 v16, %[rs], s51 offen offset:1024 sc1 lds\n\t" "buffer_load_dwordx4 v16, %[rs], s51 offen offset:2048 sc1 lds\n\t" \
   "buffer_load_dwordx4 v16, %[rs], s51 offen offset:3072 sc1 lds\n\t" "s_add_u32 m0, m0, 4096\n\t" "s_add_u32 s51, s51, 4096\n\t" \
   "buffer_load_dwordx4 v16, %[rs], s51 offen offset:0 sc1 lds\n\t" "buffer_load_dwordx4 v16, %[rs], s51 offen offset:1024 sc1 lds\n\t" \
   "s_add_u32 m0, s53, %[ld1]\n\t" "s_nop 0\n\t" "buffer_load_dwordx4 v17, %[rs], s52 offen sc1 lds\n\t" "v_add_u32 v26, s46, v22\n\t" \
   "v_add_u32 v27, s46, v23\n\t" "s_waitcnt vmcnt(0)\n\t" "s_barrier\n\t" "ds_read_b128 v[80:83], v26 offset:0\n\t" \
   "ds_read_b128 v[84:87], v26 offset:1024\n\t" "ds_read_b128 v[88:91], v26 offset:2048\n\t" "ds_read_b128 v[92:95], v26 offset:3072\n\t" \
   "ds_read_b128 v[96:99], v26 offset:4096\n\t" "ds_read_b128 v[100:103], v26 offset:5120\n\t" "ds_read_b128 v[104:107], v26 offset:6144\n\t" \
   "s_waitcnt lgkmcnt(5)\n\t" "v_mfma_f32_16x16x32_bf16 v[52:55], a[80:83], v[80:83], 0\n\t" "ds_read_b128 v[112:115], v27 offset:0\n\t" \
   "v_mfma_f32_16x16x32_bf16 v[48:51], a[0:3], v[80:83], 0\n\t" "ds_read_b128 v[108:111], v26 offset:7168\n\t" \
   "ds_read_b128 v[116:119], v27 offset:16\n\t" "v_mfma_f32_16x16x32_bf16 v[52:55], a[84:87], v[84:87], v[52:55]\n\t" \
   "ds_read_b64 v[120:121], v27 offset:32\n\t" "v_mfma_f32_16x16x32_bf16 v[48:51], a[4:7], v[84:87], v[48:51]\n\t" \
   "ds_read_b128 v[80:83], v26 offset:8192\n\t" "s_waitcnt lgkmcnt(8)\n\t" "v_mfma_f32_16x16x32_bf16 v[52:55], a[88:91], v[88:91], v[52:55]\n\t" \
   "ds_read_b128 v[122:125], v27 offset:768\n\t" "v_mfma_f32_16x16x32_bf16 v[48:51], a[8:11], v[88:91], v[48:51]\n\t" \
   "ds_read_b128 v[84:87], v26 offset:9216\n\t" "ds_read_b128 v[126:129], v27 offset:784\n\t" \
   "v_mfma_f32_16x16x32_bf16 v[52:55], a[92:95], v[92:95], v[52:55]\n\t" "v_mfma_f32_16x16x32_bf16 v[48:51], a[12:15], v[92:95], v[48:51]\n\t" \
   "ds_read_b128 v[88:91], v26 offset:10240\n\t" "ds_read_b64 v[130:131], v27 offset:800\n\t" "s_waitcnt lgkmcnt(11)\n\t" \
   "v_mfma_f32_16x16x32_bf16 v[52:55], a[96:99], v[96:99], v[52:55]\n\t" "s_lshl_b32 s48, s45, 8\n\t" \
   "v_mfma_f32_16x16x32_bf16 v[48:51], a[16:19], v[96:99], v[48:51]\n\t" "ds_read_b128 v[92:95], v26 offset:11264\n\t" "s_add_u32 s47, s48, %[xs4]\n\t" \
   "v_mfma_f32_16x16x32_bf16 v[52:55], a[100:103], v[100:103], v[52:55]\n\t" "v_mfma_f32_16x16x32_bf16 v[48:51], a[20:23], v[100:103], v[48:51]\n\t" \
   "ds_read_b128 v[96:99], v26 offset:12288\n\t" "s_waitcnt lgkmcnt(8)\n\t" "v_add_f32 v112, v112, v114\n\t" \
   "v_mfma_f32_16x16x32_bf16 v[52:55], a[104:107], v[104:107], v[52:55]\n\t" "v_add_f32 v113, v113, v115\n\t" \
   "v_mfma_f32_16x16x32_bf16 v[48:51], a[24:27], v[104:107], v[48:51]\n\t" "ds_read_b128 v[100:103], v26 offset:13312\n\t" \
   "v_mfma_f32_16x16x32_bf16 v[52:55], a[108:111], v[108:111], v[52:55]\n\t" "v_add_f32 v116, v116, v118\n\t" \
   "v_mfma_f32_16x16x32_bf16 v[48:51], a[28:31], v[108:111], v[48:51]\n\t" "ds_read_b128 v[104:107], v26 offset:14336\n\t" \
   "v_add_f32 v117, v117, v119\n\t" "s_waitcnt lgkmcnt(7)\n\t" "v_mfma_f32_16x16x32_bf16 v[52:55], a[112:115], v[80:83], v[52:55]\n\t" \
   "v_mfma_f32_16x16x32_bf16 v[48:51], a[32:35], v[80:83], v[48:51]\n\t" "ds_read_b128 v[108:111], v26 offset:15360\n\t" \
   "v_add_f32 v112, v112, v120\n\t" "v_mfma_f32_16x16x32_bf16 v[52:55], a[116:119], v[84:87], v[52:55]\n\t" "v_add_f32 v113, v113, v121\n\t" \
   "v_mfma_f32_16x16x32_bf16 v[48:51], a[36:39], v[84:87], v[48:51]\n\t" "ds_read_b128 v[80:83], v26 offset:16384\n\t" "v_add_f32 v112, v112, v116\n\t" \
   "s_waitcnt lgkmcnt(5)\n\t" "v_mfma_f32_16x16x32_bf16 v[52:55], a[120:123], v[88:91], v[52:55]\n\t" \
   "v_mfma_f32_16x16x32_bf16 v[48:51], a[40:43], v[88:91], v[48:51]\n\t" "ds_read_b128 v[84:87], v26 offset:17408\n\t" "v_add_f32 v113, v113, v117\n\t" \
   "v_mfma_f32_16x16x32_bf16 v[52:55], a[124:127], v[92:95], v[52:55]\n\t" "v_mul_f32 v112, 0xbacccccd, v112\n\t" \
   "v_mfma_f32_16x16x32_bf16 v[48:51], a[44:47], v[92:95], v[48:51]\n\t" "ds_read_b128 v[88:91], v26 offset:18432\n\t" "s_waitcnt lgkmcnt(5)\n\t" \
   "v_mfma_f32_16x16x32_bf16 v[52:55], a[128:131], v[96:99], v[52:55]\n\t" "v_mul_f32 v113, 0x3acccccd, v113\n\t" \
   "v_mfma_f32_16x16x32_bf16 v[48:51], a[48:51], v[96:99], v[48:51]\n\t" "ds_read_b128 v[92:95], v26 offset:19456\n\t" \
   "v_fma_f32 v113, -v112, v112, v113\n\t" "v_mfma_f32_16x16x32_bf16 v[52:55], a[132:135], v[100:103], v[52:55]\n\t" "v_max_f32 v113, 0, v113\n\t" \
   "v_mfma_f32_16x16x32_bf16 v[48:51], a[52:55], v[100:103], v[48:51]\n\t" "ds_read_b128 v[96:99], v26 offset:20480\n\t" "s_waitcnt lgkmcnt(5)\n\t" \
   "v_mfma_f32_16x16x32_bf16 v[52:55], a[136:139], v[104:107], v[52:55]\n\t" "v_cvt_pk_bf16_f32 v113, v113, v112\n\t" \
   "v_mfma_f32_16x16x32_bf16 v[48:51], a[56:59], v[104:107], v[48:51]\n\t" "ds_read_b128 v[100:103], v26 offset:21504\n\t" \
   "v_and_b32 v8, 0xffff0000, v113\n\t" "v_mfma_f32_16x16x32_bf16 v[52:55], a[140:143], v[108:111], v[52:55]\n\t" \
   "v_mfma_f32_16x16x32_bf16 v[48:51], a[60:63], v[108:111], v[48:51]\n\t" "ds_read_b128 v[104:107], v26 offset:22528\n\t" \
   "v_lshlrev_b32 v113, 16, v113\n\t" "s_waitcnt lgkmcnt(5)\n\t" "v_mfma_f32_16x16x32_bf16 v[52:55], a[144:147], v[80:83], v[52:55]\n\t" \
   "v_add_f32 v113, 0x3727c5ac, v113\n\t" "v_mfma_f32_16x16x32_bf16 v[48:51], a[64:67], v[80:83], v[48:51]\n\t" \
   "ds_read_b128 v[108:111], v26 offset:23552\n\t" "v_mfma_f32_16x16x32_bf16 v[52:55], a[148:151], v[84:87], v[52:55]\n\t" \
   "v_cvt_pk_bf16_f32 v113, v113, v113\n\t" "v_mfma_f32_16x16x32_bf16 v[48:51], a[68:71], v[84:87], v[48:51]\n\t" \
   "ds_read_b128 v[80:83], v26 offset:24576\n\t" "v_lshlrev_b32 v113, 16, v113\n\t" "s_waitcnt lgkmcnt(5)\n\t" \
   "v_mfma_f32_16x16x32_bf16 v[52:55], a[152:155], v[88:91], v[52:55]\n\t" "v_sqrt_f32 v113, v113\n\t" \
   "v_mfma_f32_16x16x32_bf16 v[48:51], a[72:75], v[88:91], v[48:51]\n\t" "ds_read_b128 v[84:87], v26 offset:25600\n\t" \
   "v_mfma_f32_16x16x32_bf16 v[52:55], a[156:159], v[92:95], v[52:55]\n\t" "s_nop 0\n\t" \
   "v_mfma_f32_16x16x32_bf16 v[48:51], a[76:79], v[92:95], v[48:51]\n\t" "ds_read_b128 v[88:91], v26 offset:26624\n\t" \
   "v_cvt_pk_bf16_f32 v113, v113, v113\n\t" "s_waitcnt lgkmcnt(5)\n\t" "v_mfma_f32_16x16x32_bf16 v[60:63], a[80:83], v[96:99], 0\n\t" \
   "v_mfma_f32_16x16x32_bf16 v[56:59], a[0:3], v[96:99], 0\n\t" "ds_read_b128 v[92:95], v26 offset:27648\n\t" "v_lshlrev_b32 v113, 16, v113\n\t" \
   "v_mfma_f32_16x16x32_bf16 v[60:63], a[84:87], v[100:103], v[60:63]\n\t" "v_rcp_f32 v10, v113\n\t" \
   "v_mfma_f32_16x16x32_bf16 v[56:59], a[4:7], v[100:103], v[56:59]\n\t" "ds_read_b128 v[96:99], v26 offset:28672\n\t" "s_waitcnt lgkmcnt(5)\n\t" \
   "v_mfma_f32_16x16x32_bf16 v[60:63], a[88:91], v[104:107], v[60:63]\n\t" "v_fmac_f32 v48, v32, v8\n\t" "v_fmac_f32 v49, v33, v8\n\t" \
   "v_mfma_f32_16x16x32_bf16 v[56:59], a[8:11], v[104:107], v[56:59]\n\t" "ds_read_b128 v[100:103], v26 offset:29696\n\t" "v_fmac_f32 v50, v34, v8\n\t" \
   "v_mfma_f32_16x16x32_bf16 v[60:63], a[92:95], v[108:111], v[60:63]\n\t" "v_fmac_f32 v51, v35, v8\n\t" \
   "v_mfma_f32_16x16x32_bf16 v[56:59], a[12:15], v[108:111], v[56:59]\n\t" "ds_read_b128 v[104:107], v26 offset:30720\n\t" \
   "v_fmac_f32 v52, v36, v8\n\t" "s_waitcnt lgkmcnt(5)\n\t" "v_mfma_f32_16x16x32_bf16 v[60:63], a[96:99], v[80:83], v[60:63]\n\t" \
   "v_fmac_f32 v53, v37, v8\n\t" "v_fmac_f32 v54, v38, v8\n\t" "v_mfma_f32_16x16x32_bf16 v[56:59], a[16:19], v[80:83], v[56:59]\n\t" \
   "ds_read_b128 v[108:111], v26 offset:31744\n\t" "v_fmac_f32 v55, v39, v8\n\t" \
   "v_mfma_f32_16x16x32_bf16 v[60:63], a[100:103], v[84:87], v[60:63]\n\t" "v_mul_f32 v48, v48, v10\n\t" \
   "v_mfma_f32_16x16x32_bf16 v[56:59], a[20:23], v[84:87], v[56:59]\n\t" "ds_read_b128 v[80:83], v26 offset:32768\n\t" "v_mul_f32 v49, v49, v10\n\t" \
   "s_waitcnt lgkmcnt(5)\n\t" "v_mfma_f32_16x16x32_bf16 v[60:63], a[104:107], v[88:91], v[60:63]\n\t" "v_mul_f32 v50, v50, v10\n\t" \
   "v_mul_f32 v51, v51, v10\n\t" "v_mfma_f32_16x16x32_bf16 v[56:59], a[24:27], v[88:91], v[56:59]\n\t" "ds_read_b128 v[84:87], v26 offset:33792\n\t" \
   "v_mul_f32 v52, v52, v10\n\t" "v_mfma_f32_16x16x32_bf16 v[60:63], a[108:111], v[92:95], v[60:63]\n\t" "v_mul_f32 v53, v53, v10\n\t" \
   "v_mfma_f32_16x16x32_bf16 v[56:59], a[28:31], v[92:95], v[56:59]\n\t" "ds_read_b128 v[88:91], v26 offset:34816\n\t" "v_mul_f32 v54, v54, v10\n\t" \
   "s_waitcnt lgkmcnt(5)\n\t" "v_mfma_f32_16x16x32_bf16 v[60:63], a[112:115], v[96:99], v[60:63]\n\t" "v_mul_f32 v55, v55, v10\n\t" \
   "v_cvt_pk_bf16_f32 v64, v48, v49\n\t" "v_mfma_f32_16x16x32_bf16 v[56:59], a[32:35], v[96:99], v[56:59]\n\t" \
   "ds_read_b128 v[92:95], v26 offset:35840\n\t" "v_cvt_pk_bf16_f32 v65, v50, v51\n\t" \
   "v_mfma_f32_16x16x32_bf16 v[60:63], a[116:119], v[100:103], v[60:63]\n\t" "v_cvt_pk_bf16_f32 v66, v52, v53\n\t" \
   "v_mfma_f32_16x16x32_bf16 v[56:59], a[36:39], v[100:103], v[56:59]\n\t" "ds_read_b128 v[96:99], v26 offset:36864\n\t" \
   "v_cvt_pk_bf16_f32 v67, v54, v55\n\t" "s_waitcnt lgkmcnt(5)\n\t" "v_mfma_f32_16x16x32_bf16 v[60:63], a[120:123], v[104:107], v[60:63]\n\t" \
   "buffer_store_dwordx2 v[64:65], v21, %[rs], s48 offen offset:0 sc1\n\t" "buffer_store_dwordx2 v[66:67], v21, %[rs], s47 offen offset:0 sc1\n\t" \
   "v_mfma_f32_16x16x32_bf16 v[56:59], a[40:43], v[104:107], v[56:59]\n\t" "ds_read_b128 v[100:103], v26 offset:37888\n\t" \
   "v_add_f32 v122, v122, v124\n\t" "v_mfma_f32_16x16x32_bf16 v[60:63], a[124:127], v[108:111], v[60:63]\n\t" "v_add_f32 v123, v123, v125\n\t" \
   "v_mfma_f32_16x16x32_bf16 v[56:59], a[44:47], v[108:111], v[56:59]\n\t" "ds_read_b128 v[104:107], v26 offset:38912\n\t" \
   "v_add_f32 v126, v126, v128\n\t" "s_waitcnt lgkmcnt(5)\n\t" "v_mfma_f32_16x16x32_bf16 v[60:63], a[128:131], v[80:83], v[60:63]\n\t" \
   "v_add_f32 v127, v127, v129\n\t" "v_add_f32 v122, v122, v130\n\t" "v_mfma_f32_16x16x32_bf16 v[56:59], a[48:51], v[80:83], v[56:59]\n\t" \
   "ds_read_b128 v[108:111], v26 offset:39936\n\t" "v_add_f32 v123, v123, v131\n\t" \
   "v_mfma_f32_16x16x32_bf16 v[60:63], a[132:135], v[84:87], v[60:63]\n\t" "v_add_f32 v122, v122, v126\n\t" \
   "v_mfma_f32_16x16x32_bf16 v[56:59], a[52:55], v[84:87], v[56:59]\n\t" "v_add_f32 v123, v123, v127\n\t" "s_waitcnt lgkmcnt(4)\n\t" \
   "v_mfma_f32_16x16x32_bf16 v[60:63], a[136:139], v[88:91], v[60:63]\n\t" "v_mul_f32 v122, 0xbacccccd, v122\n\t" \
   "v_mul_f32 v123, 0x3acccccd, v123\n\t" "v_mfma_f32_16x16x32_bf16 v[56:59], a[56:59], v[88:91], v[56:59]\n\t" "v_fma_f32 v123, -v122, v122, v123\n\t" \
   "v_mfma_f32_16x16x32_bf16 v[60:63], a[140:143], v[92:95], v[60:63]\n\t" "v_max_f32 v123, 0, v123\n\t" \
   "v_mfma_f32_16x16x32_bf16 v[56:59], a[60:63], v[92:95], v[56:59]\n\t" "v_cvt_pk_bf16_f32 v123, v123, v122\n\t" "s_waitcnt lgkmcnt(2)\n\t" \
   "v_mfma_f32_16x16x32_bf16 v[60:63], a[144:147], v[96:99], v[60:63]\n\t" "v_and_b32 v12, 0xffff0000, v123\n\t" "v_lshlrev_b32 v123, 16, v123\n\t" \
   "v_mfma_f32_16x16x32_bf16 v[56:59], a[64:67], v[96:99], v[56:59]\n\t" "v_add_f32 v123, 0x3727c5ac, v123\n\t" \
   "v_mfma_f32_16x16x32_bf16 v[60:63], a[148:151], v[100:103], v[60:63]\n\t" "v_cvt_pk_bf16_f32 v123, v123, v123\n\t" \
   "v_mfma_f32_16x16x32_bf16 v[56:59], a[68:71], v[100:103], v[56:59]\n\t" "v_lshlrev_b32 v123, 16, v123\n\t" "s_waitcnt lgkmcnt(0)\n\t" \
   "v_mfma_f32_16x16x32_bf16 v[60:63], a[152:155], v[104:107], v[60:63]\n\t" "v_sqrt_f32 v123, v123\n\t" "s_nop 0\n\t" \
   "v_mfma_f32_16x16x32_bf16 v[56:59], a[72:75], v[104:107], v[56:59]\n\t" "v_cvt_pk_bf16_f32 v123, v123, v123\n\t" \
   "v_mfma_f32_16x16x32_bf16 v[60:63], a[156:159], v[108:111], v[60:63]\n\t" "v_lshlrev_b32 v123, 16, v123\n\t" \
   "v_mfma_f32_16x16x32_bf16 v[56:59], a[76:79], v[108:111], v[56:59]\n\t" "v_rcp_f32 v132, v123\n\t" "s_xor_b32 s46, s46, 44032\n\t" \
   "s_add_u32 s45, s45, 4\n\t" "s_nop 5\n\t" "v_fmac_f32 v56, v32, v12\n\t" "v_fmac_f32 v57, v33, v12\n\t" "v_fmac_f32 v58, v34, v12\n\t" \
   "v_fmac_f32 v59, v35, v12\n\t" "v_fmac_f32 v60, v36, v12\n\t" "v_fmac_f32 v61, v37, v12\n\t" "v_fmac_f32 v62, v38, v12\n\t" \
   "v_fmac_f32 v63, v39, v12\n\t" "v_mul_f32 v56, v56, v132\n\t" "v_mul_f32 v57, v57, v132\n\t" "v_mul_f32 v58, v58, v132\n\t" \
   "v_mul_f32 v59, v59, v132\n\t" "v_mul_f32 v60, v60, v132\n\t" "v_mul_f32 v61, v61, v132\n\t" "v_mul_f32 v62, v62, v132\n\t" \
   "v_mul_f32 v63, v63, v132\n\t" "v_cvt_pk_bf16_f32 v68, v56, v57\n\t" "v_cvt_pk_bf16_f32 v69, v58, v59\n\t" "v_cvt_pk_bf16_f32 v70, v60, v61\n\t" \
   "v_cvt_pk_bf16_f32 v71, v62, v63\n\t" "buffer_store_dwordx2 v[68:69], v21, %[rs], s48 offen offset:128 sc1\n\t" \
   "buffer_store_dwordx2 v[70:71], v21, %[rs], s47 offen offset:128 sc1\n\t" "s_cmp_lt_u32 s45, 64\n\t" "s_cbranch_scc1 1b\n\t" \
   "s_waitcnt vmcnt(0) lgkmcnt(0)"
#define DF_CLOB "v8", "v9", "v10", "v11", "v12", "v13", "v14", "v15", "v32", "v33", "v34", "v35", "v36", "v37", "v38", "v39", "v40", "v41", "v42", "v43", "v44", "v45", "v46", "v47", "v48", "v49", "v50", "v51", "v52", "v53", "v54", "v55", "v56", "v57", "v58", "v59", "v60", "v61", "v62", "v63", "v64", "v65", "v66", "v67", "v68", "v69", "v70", "v71", "v72", "v73", "v74", "v75", "v76", "v77", "v78", "v79", "v80", "v81", "v82", "v83", "v84", "v85", "v86", "v87", "v88", "v89", "v90", "v91", "v92", "v93", "v94", "v95", "v96", "v97", "v98", "v99", "v100", "v101", "v102", "v103", "v104", "v105", "v106", "v107", "v108", "v109", "v110", "v111", "v112", "v113", "v114", "v115", "v116", "v117", "v118", "v119", "v120", "v121", "v122", "v123", "v124", "v125", "v126", "v127", "v128", "v129", "v130", "v131", "v132", "v133", "v134", "v135", "s40", "s41", "s42", "s43", "s44", "s45", "s46", "s47", "s48", "s49", "s50", "s51", "s52", "s53"
template <bool VU>
__device__ __forceinline__ void job_proj(int wv_, const v32u (&w)[10], unsigned char* ws, const Pool& pool, int layer, int h) {
  const unsigned lds0 = (unsigned)(uintptr_t)(__attribute__((address_space(3))) unsigned char*)dyn_lds;
  const int lane = lane_id(), wv = wv_, g = lane >> 4, r = lane & 15;
  const v4u rs = make_rs(ws, (unsigned)OFF_END);
  v16u ad = (unsigned)lane;
  ad[0] = (int)OFF_XR + wv * 10240 + lane * 16;
  ad[1] = wv * 64 + lane < 96 ? (int)OFF_ST + wv * 1024 + lane * 16 : OOB;
  ad[3] = cnt_off(layer, CNT_IDFT, lane & 31);
  ad[4] = lane == 0 ? kqf_off(layer, 0, (VU ? 32 : 0) + h) + wv * 4 : OOB;
  const int q = (g >> 1) * 256 + r * 16;
  ad[5] = (VU ? (int)OFF_UF + (wv * 320 + h) * 1024 + (g & 1) * 32768 : (h * RT * 4 + wv) * 1024 + (g & 1) * 4096) + q;
  ad[6] = lds0 + PJ_X + (g & 1) * 20480 + q;
  ad[7] = lds0 + PJ_X + 40960 + (lane & 31) * 48;
  ad[8] = lds0 + wv * 2048 + lane * 16;
  ad[9] = lds0 + 8192 + lane * 16;
  ad[10] = lds0 + PJ_LDS + 1024 + wv * 256 + (lane >> 5) * 128;
  if (layer > 0) fan_wait<64, NFIRST>(wv_, ws, fan_off(1, 1, 0), h);
  PH(2 + layer * 6)
#define PJ_DBG
#define PJ_OPS                                                                                                                      \
  : W_OPS, [rs] "s"(rs), [rdy] "s"((layer - 1) | ((1 << NFIRST / 8) - 1)), [ldx] "s"(lds0 + PJ_X + wv * 10240),                        \
    [ld1] "s"(lds0 + PJ_X + 40960 + (wv < 2 ? wv : 2) * 1024), [qf] "s"((int)OFF_QF), [kf] "s"((int)OFF_KF), [cnt] "s"(cnt_off(layer, CNT_IDFT, 0)), \
    [flw] "s"(lds0 + PJ_LDS + 512 + wv * 4) PJ_DBG
  if constexpr (VU) asm volatile(VU_ASM : "+{v[16:31]}"(ad) PJ_OPS : VU_CLOB, "vcc", "scc", "m0", "memory");
  else asm volatile(KQ_ASM : "+{v[16:31]}"(ad) PJ_OPS : KQ_CLOB, "vcc", "scc", "m0", "memory");
  LDSBAR();
}

__device__ __forceinline__ unsigned dft_idx(int f, int d) { return (unsigned)((f == 1 ? 320 : f >> 1) * d + (((f & 1) && f != 1) ? 160 : 0)) % 640u; }
__device__ __forceinline__ float cos640(unsigned i) { return __builtin_amdgcn_cosf((float)i * (1.f / 640)); }
__device__ __forceinline__ void job_dft(int wv_, unsigned char* ws, int layer, int fg, int set) {
  const unsigned lds0 = (unsigned)(uintptr_t)(__attribute__((address_space(3))) unsigned char*)dyn_lds;
  const int lane = lane_id(), wv = wv_, g = lane >> 4, r = lane & 15;
  const v4u rs = make_rs(ws, (unsigned)OFF_END);
  v32u w[5];
#pragma unroll
  for (int t = 0; t < 2; ++t) {
    const int f = 16 * (fg * 8 + wv * 2 + t) + r;
    const unsigned step = dft_idx(f & ~(f != 1), 32);
#pragma unroll
    for (int q = 0; q < 4; ++q) {
      unsigned m0 = dft_idx(f, sigma(g, 2 * q)), m1 = dft_idx(f, sigma(g, 2 * q + 1));
#pragma unroll
      for (int kt = 0; kt < KT; ++kt) {
        const int i = t * KT + kt;
        w[i >> 3][(i & 7) * 4 + q] = pk(cos640(m0), cos640(m1));
        m0 += step; m0 = min(m0, m0 - 640u); m1 += step; m1 = min(m1, m1 - 640u);
      }
    }
  }
  v16u ad = (unsigned)lane;
  ad[0] = (int)OFF_XR + wv * 10240 + lane * 16;
  ad[1] = wv * 64 + lane < 96 ? (int)OFF_ST + wv * 1024 + lane * 16 : OOB;
  ad[3] = cnt_off(layer, CNT_OUT, 0);
  ad[5] = (int)OFF_X1T + (((fg * 8 + wv * 2) * 4 + g) * XS + r) * 8;
  ad[6] = lds0 + lane * 16;
  ad[7] = lds0 + 40960 + r * 48;
  asm volatile(DF_ASM
               : "+{v[16:31]}"(ad)
               : "{a[0:31]}"(w[0]), "{a[32:63]}"(w[1]), "{a[64:95]}"(w[2]), "{a[96:127]}"(w[3]), "{a[128:159]}"(w[4]), [rs] "s"(rs), [set] "s"(set), [xs4] "s"(4 * XS * 8),
                 [ldx] "s"(lds0 + wv * 10240), [ld0] "s"(lds0), [ld1] "s"(lds0 + 40960 + (wv < 2 ? wv : 2) * 1024)
               : DF_CLOB, "vcc", "scc", "m0", "memory");
}

constexpr int NOUT = 44, NLATE = 108 - NFILL;
constexpr int L_HS = PJ_LDS, MAIN_LDS = L_HS + 2048;
static_assert(MAIN_LDS >= JOB_LDS && MAIN_LDS >= REC_LDS && MAIN_LDS <= 160 * 1024, "LDS");
struct Go { Pool pool; int rot; };
__device__ __forceinline__ void st32(rsrc_t rs, int off, unsigned v) { __builtin_amdgcn_raw_buffer_store_b32(v, rs, off, 0, AUX_SC1); }
__device__ __forceinline__ unsigned hello_load(rsrc_t rs, int b) { return __builtin_amdgcn_raw_buffer_load_b32(rs, HS_GO + b * 16, 0, AUX_SC1); }
__device__ __forceinline__ void hello(int wv, rsrc_t rs, int b, unsigned t) {
  if (wv == 0) {
    st32(rs, lane_id() == 0 ? HS_HI + b * 8 : OOB, t + 1u);
    LDS_U32(lds_base() + L_HS) = t + 1u;
  }
}
__device__ __forceinline__ int go_off(int b) {
  const int l = lane_id() & 3;
  return l == 0 ? HS_GO + b * 16 : (l == 3 ? HS_ASK + (b & 127) * 16 : HS_PAY + (b & 31) * 32 + l * 16 - 16);
}
__device__ __forceinline__ Go wait_go(int wv, unsigned char* ws, int b, v4u rep, v4u m) {
  const v4u rs = make_rs(ws, (unsigned)OFF_END);
  const int l = lane_id();
  const unsigned a = __builtin_amdgcn_readfirstlane(LDS_U32(lds_base() + L_HS));
  for (unsigned spins = 0; spins < (1u << 18); ++spins) {
    if (spins || b >= 128) {
      VM_LD128(m, go_off(b), 0, 0);
      VM_WAIT1(0, m);
    }
    const unsigned n = __builtin_amdgcn_readlane(m.y, 0);
    if (__builtin_amdgcn_readlane(m.x, 0) == a && __builtin_amdgcn_readlane(m.x, 1) == n && __builtin_amdgcn_readlane(m.x, 2) == n) break;
    const unsigned t = (wv & 2) ? ((wv & 1) ? m.w : m.z) : ((wv & 1) ? m.y : m.x);
    const bool again = b >= 128 && l == 3 && t != rep.x;
    rep.x = t;
    VM_ST128(rep, again ? HS_CAL - 128 * 64 + b * 64 + wv * 16 : OOB, 0);
    if (spins) __builtin_amdgcn_s_sleep(6);
  }
  VM_ST32(m.z, (wv == 0 && l == 0) ? HS_HI + b * 8 + 4 : OOB, 0);
  if (b >= 128) { PH(1) }
  const auto u64 = [](unsigned lo, int i, unsigned hi, int k) {
    return (unsigned)__builtin_amdgcn_readlane(lo, i) | (unsigned long long)(unsigned)__builtin_amdgcn_readlane(hi, k) << 32;
  };
  Go g;
  g.pool.sel = u64(m.y, 1, m.z, 1); g.pool.s3 = u64(m.w, 1, m.w, 2); g.pool.ysel = u64(m.y, 2, m.z, 2);
  g.pool.base = __builtin_amdgcn_readfirstlane(pool_base(ws));
  g.rot = __builtin_amdgcn_readlane(m.w, 0) & 7;
  return g;
}
#define LDSTAMP(n)
#define LDX
#define LDC
__device__ __forceinline__ v4u load_rows(v32u (&w)[10], int kind, const u16* A, const u16* B, const u16* x, int h, int wv, unsigned slot, unsigned char* ws, int b, bool first) {
  unsigned tr;
  const int l = lane_id(), i = l & 15, g = l >> 4;
  const v4u ra = make_rs(A, 640u * 8192u), rb = make_rs(B, 640u * 8192u), rs = make_rs(ws, (unsigned)OFF_END), rx = make_rs(x, S_ * D_ * 2);
  v8u ad, ex;
  const bool pj = kind == 1 || kind == 2;
  const int wr = 4 * wv + g, rr = 4 * (pj ? g >> 1 : g) + (i >> 2), gt = pj ? g & 1 : 0;
  ad[0] = wr * 8192 + h * 256 + i * 16;
  ad[1] = (h * 128 + wr) * 1280 + i * 16;
  ad[2] = slot + rr * 256 + ((2 * wv + gt) ^ (rr & 7)) * 32 + (i & 3) * 8;
  ad[3] = pj ? ad[2] + 2048u : ad[2] ^ 32u;
  ad[4] = slot + wr * 256 + ((i >> 1) ^ (wr & 7)) * 32 + (i & 1) * 16;
  ad[5] = slot + wr * 328 + ((i >> 2) * 2 + (i & 1)) * 8 + ((i >> 1) & 1) * 4;
  ad[6] = slot + (kind == 3 ? (16 * (g & 1) + 4 * (g >> 1) + ((i >> 2) & 1)) * 328 + (i >> 3) * 8 : rr * 328 + gt * 8) + (i & 3) * 10560 + wv * 80;
  ad[7] = ad[4] + 65536u;
  const unsigned im = lds_base() + PJ_X + (wv >> 1) * PJ_BUF;
  const bool hi = first && wv == 0;
  ex[0] = go_off(b);
  ex[1] = hi ? HS_GO + b * 16 : OOB;
  ex[2] = hi && l == 0 ? HS_HI + b * 8 : OOB;
  ex[3] = lds_base() + L_HS + (hi ? 0 : 4);
  ex[4] = (wv * 16 + (l >> 3)) * 1280 + (l & 7) * 16;
  ex[5] = im + ((wv & 1) * KT + ((l >> 2) & 1)) * 1024 + ((l & 1) * 32 + (l >> 3)) * 16 + (l & 2) * 4;
  ex[6] = im + 40960 + ((wv & 1) * 16 + (l >> 3)) * 48 + min(l & 7, 5) * 8;
  ex[7] = (l & 7) ? 0u : ~0u;
  v4u m;
#define LD_OPS                                                                                                                                       \
  : "={a[0:31]}"(w[0]), "={a[32:63]}"(w[1]), "={a[64:95]}"(w[2]), "={a[96:127]}"(w[3]), "={a[128:159]}"(w[4]), "={a[160:191]}"(w[5]),                \
    "={a[192:223]}"(w[6]), "={a[224:255]}"(w[7]), "={v[192:223]}"(w[8]), "={v[224:255]}"(w[9]), "={v[28:31]}"(m) LDX                                 \
  : "{v[16:23]}"(ad), "{v[112:119]}"(ex), [ra] "s"(ra), [rb] "s"(rb), [rw] "s"(rs), [rx] "s"(rx), [p0] "s"(wv < 3 ? 3 * wv : 8),                     \
    "{v27}"(lds_base() + L_HS + 1024 + wv * 256 + (l >> 5) * 128)                                                                                   \
  : LDP_CLOB LDC, "scc", "memory"
  if (kind == 0) asm volatile(LDP_ASM LD_OPS);
  else if (kind == 1) asm volatile(LDK_ASM LD_OPS);
  else if (kind == 2) asm volatile(LDV_ASM LD_OPS);
  else asm volatile(LDY_ASM LD_OPS);
  return m;
}
__device__ __forceinline__ v4u prep_jobs(int wv, unsigned char* ws, rsrc_t rs, const u16* __restrict__ x, const u16* __restrict__ mem0, int j) {
  const int lane = lane_id(), g = lane >> 4, r = lane & 15, tid = wv * 64 + lane, pb = pool_base(ws);
  const int xc = (int)xcc_id(), u = (j >> 3) * 4 + (xc & 3), page = xc < 4 ? u >> 2 : 16 + u % NYP;
  const int cb = pb + page * PAGE + M_CAL + (xc < 4 ? wv * REG + xc * 512 : 3 * REG - u / NYP * WIN + wv * 512);
  stc(rs, lane < 32 ? cb + (lane >> 3) * 8192 + (lane & 7) * 16 : OOB, v4u{1u, 2u, 3u, 4u});
  const unsigned char* p = ws + __builtin_amdgcn_readfirstlane(cb);
  unsigned t0, t1, t2, t3, t4;
  asm volatile("s_load_dword %0, %4, 0x80\n\ts_load_dword %1, %4, 0x2080\n\ts_load_dword %2, %4, 0x4080\n\ts_load_dword %3, %4, 0x6080"
               : "={s40}"(t0), "={s41}"(t1), "={s42}"(t2), "={s43}"(t3) : "s"(p) : "memory");
  const unsigned tg = hello_load(rs, 128 + j);
  v2u c[5][2];
  unsigned t[4];
#pragma unroll
  for (int q = 0; q < 5; ++q) {
    const u16* xr = x + (size_t)(j * 16 + r) * D_ + 32 * (5 * wv + q) + 4 * g;
    c[q][0] = *(const v2u*)xr; c[q][1] = *(const v2u*)(xr + 16);
  }
#pragma unroll
  for (int k = 0; k < 4; ++k) t[k] = *(const unsigned*)(mem0 + (size_t)((tid >> 1) & 7) * D_ + colmap((min(j >> 3, 4) * 8 + ((tid >> 4) & 7)) * 2 + (tid & 1), 2 * k));
  __builtin_amdgcn_sched_barrier(0);
  const v4u ones = {0xFFFFFFFFu, 0xFFFFFFFFu, 0xFFFFFFFFu, 0xFFFFFFFFu};
  const int o = (j * 256 + tid) * 16;
  const bool cal = (o & 0x79800) == 0x79800;
  stc(rs, (int)OFF_HO + o, ones);
  stc(rs, o + REG < (int)HO_FIRST ? (int)OFF_HO + REG + o : OOB, ones);
  stc(rs, (cal || (o >= W_MEM && o < W_MISC)) ? OOB : pb + (NSEG - 1) * REG + o, ones);
  stc(rs, cal ? OOB : pb + o, ones);
  stc(rs, pb + P_Y + o, ones);
  stc(rs, o < 8 * 16384 ? pb + P_Y + REG + o : OOB, ones);
  hello(wv, rs, 128 + j, tg);
  asm volatile("" ::"v"(tg));
  __builtin_amdgcn_sched_barrier(0);
#define CAL_T(r, o) "s_load_dword " r ", %5, " o "\n\ts_waitcnt lgkmcnt(0)\n\ts_memtime s[46:47]\n\ts_waitcnt lgkmcnt(0)\n\ts_mov_b32 " r ", s46\n\t"
  asm volatile(CAL_T("%0", "0x80") CAL_T("%1", "0") CAL_T("%2", "0x2000") CAL_T("%3", "0x4000") CAL_T("%4", "0x6000")
               : "+{s40}"(t0), "+{s41}"(t1), "+{s42}"(t2), "+{s43}"(t3), "={s44}"(t4) : "s"(p) : "s46", "s47", "memory");
  PH(16)
  __builtin_amdgcn_sched_barrier(0);
  f32x2 st = {0.f, 0.f};
#pragma unroll
  for (int q = 0; q < 5; ++q) {
    const v4u f = v4u{c[q][0].x, c[q][0].y, c[q][1].x, c[q][1].y};
    stc(rs, (int)OFF_XR + ((j * KT + 5 * wv + q) * 64 + lane) * 16, f);
    st += frag_stat(f);
  }
  stc(rs, j < 40 ? pb + (NSEG - 1) * REG + W_MEM + o : OOB, v4u{nosent(t[0]), nosent(t[1]), nosent(t[2]), nosent(t[3])});
  st = rows_sum(st);
  __builtin_amdgcn_raw_buffer_store_b64(__builtin_bit_cast(v2u, g ? f32x2{0.f, 0.f} : st), rs,
                                        (g == 0 || (g == 1 && wv == 0)) ? (int)OFF_ST + ((j * 16 + r) * 6 + (g ? 4 : wv)) * 8 : OOB, 0, AUX_SC1);
  const unsigned ask = __builtin_amdgcn_raw_buffer_load_b32(rs, HS_ASK + j * 16 + wv * 4, 0, AUX_SC1);
  const unsigned b0[4] = {min(t1 - t0, 9999u), min(t2 - t1, 9999u), min(t3 - t2, 9999u), min(t4 - t3, 9999u)}, sh = ((xc ^ wv) & 1) * 16;
  const v4u rep = {ask, (unsigned)page, b0[0] << sh | b0[2] << (16 - sh), b0[1] << sh | b0[3] << (16 - sh)};
  asm volatile("s_waitcnt vmcnt(0)" ::: "memory");
  stc(rs, lane == 0 ? HS_CAL + j * 64 + wv * 16 : OOB, rep);
  LDSBAR();
  PH(18)
  return rep;
}
__device__ __forceinline__ int wave_isum(int v) {
  asm volatile("s_nop 1\n\tv_add_u32_dpp %0, %0, %0 quad_perm:[1,0,3,2] row_mask:0xf bank_mask:0xf\n\t"
               "s_nop 1\n\tv_add_u32_dpp %0, %0, %0 quad_perm:[2,3,0,1] row_mask:0xf bank_mask:0xf\n\t"
               "s_nop 1\n\tv_add_u32_dpp %0, %0, %0 row_half_mirror row_mask:0xf bank_mask:0xf\n\t"
               "s_nop 1\n\tv_add_u32_dpp %0, %0, %0 row_mirror row_mask:0xf bank_mask:0xf\n\ts_nop 0"
               : "+v"(v));
  return (__builtin_amdgcn_readlane(v, 0) + __builtin_amdgcn_readlane(v, 16)) + (__builtin_amdgcn_readlane(v, 32) + __builtin_amdgcn_readlane(v, 48));
}
__device__ __forceinline__ Go lead(int wv, unsigned char* ws, const u16* x, const u16* mem0, int rot) {
  const v4u rs = make_rs(ws, (unsigned)OFF_END);
  const int t = wv * 64 + lane_id();
  int* acc = (int*)(dyn_lds + L_HS + 128);
#define VM_LD64(dst, off) asm volatile("buffer_load_dwordx2 %0, %1, %2, 0 offen sc1" : "=v"(dst) : "v"(off), "s"(rs) : "memory")
  v4u c0, c1;
  v2u hi;
  unsigned old, n = 0;
  VM_LD128(c0, HS_CAL + t * 32, 0, 0);
  VM_LD128(c1, HS_CAL + t * 32, 0, 16);
  VM_LD64(hi, HS_HI + t * 8);
  VM_LD32(old, HS_PAY + (t & 63) * 16, 0);
  asm volatile("s_waitcnt vmcnt(0)" : "+v"(c0), "+v"(c1), "+v"(hi), "+v"(old)::"memory");
  const unsigned r0 = c0.x + 1u, r1 = c1.x + 1u, q = hi.y + 1u;
  __builtin_amdgcn_raw_buffer_store_b64(v2u{r0, r1}, ws_rsrc(ws), HS_ASK + t * 8, 0, AUX_SC1);
  while (__any(old == n)) ++n;
  if (t < 192) acc[t] = 9999;
  prep_jobs(wv, ws, ws_rsrc(ws), x, mem0, 127);
  for (unsigned spins = 0; spins < (1u << 18); ++spins) {
    VM_LD128(c0, HS_CAL + t * 32, 0, 0);
    VM_LD128(c1, HS_CAL + t * 32, 0, 16);
    VM_LD64(hi, HS_HI + t * 8);
    asm volatile("s_waitcnt vmcnt(0)" : "+v"(c0), "+v"(c1), "+v"(hi)::"memory");
    if (!__any(t < 254 && (c0.x != r0 || c1.x != r1))) break;
    __builtin_amdgcn_s_sleep(2);
  }
  if (t < 254) {
    int *a0 = acc + min(c0.y, 47u) * 4, *a1 = acc + min(c1.y, 47u) * 4;
    atomicMin(a0, c0.z & 0xFFFF); atomicMin(a0 + 1, c0.z >> 16); atomicMin(a0 + 2, c0.w & 0xFFFF); atomicMin(a0 + 3, c0.w >> 16);
    atomicMin(a1, c1.z & 0xFFFF); atomicMin(a1 + 1, c1.z >> 16); atomicMin(a1 + 2, c1.w & 0xFFFF); atomicMin(a1 + 3, c1.w >> 16);
  }
  __syncthreads();
  PH(19)
  const int s = lane_id(), fx = (s & 4) ? -1 : 1, fy = ((s * 43 >> 7) & 1) ? -1 : 1;
  const v4i u = ((const v4i*)acc)[s >> 2], v = ((const v4i*)acc)[16 + (s * 43 >> 7)];
  int ax = min(u.x, u.y) - min(u.z, u.w), yx = min(v.z, v.w) - min(v.x, v.y);
  const int n0 = ax > 0 ? u.z : u.x, n1 = ax > 0 ? u.w : u.y, lu = min(n0, n1), lv = min(min(v.x, v.y), min(v.z, v.w)), near = 110 - wave_imax(-min(lu, lv));
  const int sa = (lu < near && abs(ax) > 60) ? (ax > 0 ? fx : -fx) : 0, sy = (lv < near && abs(yx) > 60) ? (yx > 0 ? fy : -fy) : 0;
  const int g = wave_isum(sa + sy), m = wave_isum(abs(sa) + abs(sy));
  const bool with = sa * g > 0;
  const int bs = wave_imax(with ? -n1 : -9999) - wave_imax(with ? -n0 : -9999);
  int b = n0 - n1;
  if (abs(g) * 8 > m * 7) {
    if (sa * g >= 0) { ax = g * fx; b = bs; }
    if (sy == 0) yx = g * fy;
  }
  Go go;
  go.pool.sel = __ballot(ax > 0);
  go.pool.s3 = __ballot(b > 0) ^ 0xAAAAAAAAAAAAAAAAull;
  go.pool.ysel = __ballot(yx > 0);
  go.pool.base = __builtin_amdgcn_readfirstlane(pool_base(ws));
  go.rot = rot;
  PH(20)
  {
    const unsigned long long v = (s & 1) ? go.pool.ysel : go.pool.sel;
    VM_ST128((v4u{n, (unsigned)v, (unsigned)(v >> 32), (unsigned)(go.pool.s3 >> (s & 1) * 32)}), wv == 0 ? HS_PAY + s * 16 : OOB, 0);
  }
  for (unsigned spins = 0; spins < (1u << 18); ++spins) {
    VM_ST128((v4u{hi.x, n, q, (unsigned)rot}), HS_GO + t * 16, 0);
    const unsigned sent = hi.x;
    do {
      __builtin_amdgcn_s_sleep(8);
      VM_LD64(hi, HS_HI + t * 8);
      VM_WAIT1(0, hi);
    } while (!__any(hi.x != sent) && __any(t < 255 && hi.y != q) && ++spins < (1u << 18));
    if (!__any(t < 255 && hi.y != q)) break;
  }
  return go;
}
__device__ __forceinline__ void late_jobs(int wv_, unsigned char* ws, rsrc_t rs, const u16* __restrict__ wr, const u16* __restrict__ wi, int j) {
  const int lane = lane_id(), wv = wv_, g = lane >> 4, r = lane & 15;
  unsigned* t32 = (unsigned*)dyn_lds;
  for (int o = (int)HO_FIRST + (j * 256 + wv * 64 + lane) * 16; o < 2 * (int)HO_LAYER; o += NLATE * 4096)
    stc(rs, (int)OFF_HO + o, v4u{0xFFFFFFFFu, 0xFFFFFFFFu, 0xFFFFFFFFu, 0xFFFFFFFFu});
#pragma unroll 1
  for (int fi = j * 4 + wv; fi < 20 * NFK; fi += NLATE * 4) {
    v4u v;
#pragma unroll
    for (int q = 0; q < 4; ++q) {
      const int e = fi % NFK, d = 16 * (fi / NFK) + r, f = 4 * (32 * (e >> 2) + 8 * q + 2 * (e & 2) + g) + 2 * (e & 1);
      const float sc = f ? 2.f : 1.f;
      v[q] = pk(cos640(dft_idx(f, d)) * sc, cos640(dft_idx(f + 1, d)) * sc);
    }
    stc(rs, (int)OFF_INV + (fi * 64 + lane) * 16, v);
  }
#pragma unroll 1
  for (int b = j; b < 32 * 6; b += NLATE) {
    const int m0 = (b & 31) * 64, k0 = (b >> 5) * 64, n0 = (b & 24) * 2 + (b & 7) * 256;
#pragma unroll
    for (int q = 0; q < 16; ++q) {
      const size_t i = (size_t)(n0 + q + 64 * wv) * 321 + min(k0 + lane, 320);
      t32[(q * 4 + wv) * 65 + lane] = (unsigned)wr[i] | ((unsigned)wi[i] << 16);
    }
    __syncthreads();
#pragma unroll
    for (int q = 0; q < 16; ++q) {
      const int k = q * 4 + wv;
      st32(rs, k0 + k < 321 ? (int)OFF_WT + ((k0 + k) * S_ + m0 + lane) * 4 : OOB, t32[lane * 65 + k]);
    }
    __syncthreads();
  }
  dep_signal(wv_, ws, cnt_off(0, CNT_IDFT, 0));
}
__device__ __forceinline__ void late_wait(int wv_, unsigned char* ws) {
  if (TIDX == 0) {
    for (unsigned spins = 0; spins < (1u << 17); ++spins) {
      if (__hip_atomic_load((gu32*)(ws + cnt_off(0, CNT_IDFT, 0)), __ATOMIC_RELAXED, __HIP_MEMORY_SCOPE_AGENT) == NLATE - 1) break;
      __builtin_amdgcn_s_sleep(16);
    }
  }
  __syncthreads();
}
#define FPACE 1200
#define FILLK 12
#define STR_(x) #x
#define STR(x) STR_(x)
__device__ __forceinline__ void fill_run(int wv, unsigned char* ws, const Pool& pool, int f, int n0, int n1) {
  const v4u rs = make_rs(ws, (unsigned)OFF_END), ones = {~0u, ~0u, ~0u, ~0u};
  const int lane = lane_id();
  unsigned due = rtc() * 8u + (unsigned)(f * 4 + wv) * (FPACE / 576);
  int fo = OOB;
#pragma unroll 1
  for (int n = n0; n <= n1; ++n) {
    const int t = n & 63, rg = pool.region(t), yr = pool.base + P_Y + y_region(pool.ysel, t) - 32 * 16384;
    if (n == NSEG) { PH(44) late_wait(wv, ws); }
    if (n == NSEG + FILLK + 2) { PH(45) }
    if (n == 127) { PH(46) }
    if ((n == NSEG + 1 || n == NSEG + FILLK + 1) && n < n1) {
      unsigned c;
      for (unsigned spins = 0; spins < SPIN_CAP; ++spins) {
        VM_LD32(c, cnt_off(0, CNT_OUT, min(lane, t > 1 ? NSEG - 1 : FILLK + 1)), 0);
        VM_WAIT1(0, c);
        if (!__any(c != 9u)) break;
        __builtin_amdgcn_s_sleep(32);
      }
    }
#pragma unroll 1
    for (int k = 0; k < 144 / NFILL; ++k) {
      const int i = f * 4 + wv + 4 * NFILL * k;
      const bool on = t && n < n1 && (t < NSEG - 1 || i < 160 || i >= 240);
      const int so = (i < 256 ? rg : yr) + (i >> 3) * 16384 + (i & 7) * 1024;
      if (on) {
        unsigned now;
        while ((int)(due - (now = rtc() * 8u)) > 0) __builtin_amdgcn_s_sleep(1);
        due = max((int)(due - now), -FPACE * NFILL / 144) + now + FPACE * NFILL / 144;
      }
      VM_ST128(ones, on ? lane * 16 : OOB, so);
      if (k == FLAG - 1) {
        asm volatile("s_waitcnt vmcnt(" STR(FLAG) ")" ::: "memory");
        VM_ST32(1u, fo, 0);
      }
    }
    fo = t && lane < (t == NSEG - 1 ? 2 : 1) ? kqf_off(n >> 6, t - 1 + lane, f) + 16 + wv * 4 : OOB;
  }
}
#define TROB (24 * 256 + 16)
#define TC2 30
#define TVIS 32
#define TDLT 170
#define TM1 88
#define SPARE_US 200
#define SPARE_NAP 127
#define TM2 60
__global__ void __launch_bounds__(256, 1) k_main(unsigned char* ws, const u16* x, const u16* mem0, const u16* Wq, const u16* Wk, const u16* Wv, const u16* Wo,
                                                const u16* wr, const u16* wi, u16* out, long nlayer
                                                ) {
  v4u* smem = (v4u*)dyn_lds;
  const int wv_ = __builtin_amdgcn_readfirstlane(threadIdx.x >> 6);
  const RecTune tn = {TM1, TM2, 4, TROB, TC2, TVIS, TDLT, 1};
  unsigned long long* dbg = nullptr;
#define RSTAMP_(i)
  const rsrc_t rs = ws_rsrc(ws);
  PH(0)
  const int b = blockIdx.x, tt = b >> 3;
  const unsigned t00 = rtc();
  int rot = (int)(xcc_id() - b) & 7, lpl, ljb = -1;
  if (tt < 16) {
    v32u w[10];
    Go go;
    const unsigned lds0 = lds_base();
#pragma unroll 1
    for (int it = 0;; ++it) {
      const int p = (b + rot) & 7, h = (p & 3) * 8 + (tt & 7);
      const bool v = (tt & 8) != 0;
      const v4u m = load_rows(w, p < 4 ? (v ? 3 : 0) : (v ? 2 : 1), v ? Wv : Wq, v ? Wo : Wk, x, h, wv_, lds0 + (p < 4 ? 0 : PJ_X + 2 * PJ_BUF), ws, b, it == 0);
      PH(21 + (p >> 2))
      go = wait_go(wv_, ws, b, m, m);
      if (go.rot == rot || it) break;
      rot = go.rot;
    }
    Pool pool = go.pool;
    const int pl = (b + rot) & 7, role = pl < 4 ? (tt & 8) * 4 + pl * 8 + (tt & 7) : pl * 32 + tt;
    const int hd = (pl - 4) * 8 + (tt & 7);
#pragma unroll 1
    for (int layer = 0; layer < nlayer; ++layer) {
      int ro = role, xs = pl, wq = wv_, h2 = hd | (tt & 8) << 8;
      asm volatile("" : "+s"(ro), "+s"(xs), "+s"(wq), "+s"(h2), "+s"(ws), "+s"(pool.base));
      if (layer > 0) {
        const int jb = ro < 64 ? ro + 57 : (xs - 4) * 16 + (ro & 31);
        late_wait(wq, ws);
        if (ro < 64 || jb < 57) {
          job_col(wq, ws, ws_rsrc(ws), lds_base(), jb, fan_off(0, 1, 0), fan_off(1, 0, 0), false);
          __syncthreads();
          RSTAMP_(5)
          PH(7)
        } else fan_wait<64, 20>(wq, ws, fan_off(0, 1, 0), jb);
      }
      if (ro < 32) { rec_p1(wq, w, ws, pool, layer, ro, xs, tn, dbg); PH(4 + layer * 6) }
      else if (ro < 64) rec_pv(wq, w, ws, pool, layer, ro - 32, xs, tn, dbg);
      else {
        if (h2 >> 8) job_proj<true>(wq, w, ws, pool, layer, h2 & 31);
        else job_proj<false>(wq, w, ws, pool, layer, h2 & 31);
        RSTAMP_(2)
      }
    }
    lpl = pl;
    ljb = role < 64 ? role + 57 : (pl - 4) * 16 + (role & 31);
    if (role >= 64 && ljb >= 57) ljb = -1;
  } else {
  const Go go = b == 255 ? lead(wv_, ws, x, mem0, rot) : wait_go(wv_, ws, b, prep_jobs(wv_, ws, rs, x, mem0, b - 128), v4u{0u, 0u, 0u, 0u});
  Pool pool = go.pool;
  const int pl = (b + go.rot) & 7;
  const int role = pl < 4 ? (tt < 26 ? 64 + pl * 10 + tt - 16 : 104 + pl * 6 + tt - 26) : pl * 32 + tt;
  if (tt < (pl < 4 ? 32 - NFILL / 4 : 27)) late_jobs(wv_, ws, rs, wr, wi, pl < 4 ? 44 + pl * (16 - NFILL / 4) + tt - 16 : (pl - 4) * 11 + tt - 16);
  else if (pl < 4) fill_run(wv_, ws, pool, (31 - tt) * 4 + pl, 1, (int)nlayer * NSEG);
  PH(23)
  lpl = pl;
  if (role >= 104 && role < 128) {
    while (rtc() - t00 < 100u * SPARE_US) __builtin_amdgcn_s_sleep(127);
    late_wait(wv_, ws);
    prog_prewait(wv_, ws, kqf_off((int)nlayer - 1, NSEG - 1, ((pl + 1) & 3) * 8 + tt - 26));
    __syncthreads();
    __builtin_amdgcn_s_sleep(SPARE_NAP);
  } else if (role >= 64 && role < 104) {
#pragma unroll 1
    for (int layer = 0; layer < nlayer; ++layer) {
      if (layer > 0) {
        late_wait(wv_, ws);
        job_col(wv_, ws, rs, lds_base(), role + 57, fan_off(0, 1, 0), fan_off(1, 0, 0), false);
        __syncthreads();
        PH(7)
      }
      ((unsigned*)(dyn_lds + L_T))[lane_id() & 15] = 0u;
      __syncthreads();
      if (wv_ == 3) rec_fin(ws, pool, layer, role - 64, pl, tn, dbg);
      else if (wv_ == 0) rec_sched(ws, pool, layer, role - 64, pl, tn, dbg);
      __syncthreads();
    }
    ljb = role + 57;
  } else {
#pragma unroll 1
  for (int layer = 0; layer < nlayer; ++layer) {
    int wq = wv_;
    asm volatile("" : "+s"(wq));
    if (layer > 0) late_wait(wq, ws);
    if (layer > 0) job_idft(wq, ws, rs, smem, ((tt - 16) >> 1) * 4 + pl - 4, tt & 1, dbg + 60000 + role * 8);
    RSTAMP_(2)
    __syncthreads();
    if (tt >= 27) {
      job_dft(wq, ws, layer, tt - 27, pl - 4);
      fan_signal<64>(wq, ws, fan_off(0, 1, 0), layer * 20 + (pl - 4) * 5 + tt - 27);
      if (layer) { PH(14) }
    } else {
#pragma unroll 1
      for (int j = (tt - 16) * 4 + pl - 4; j < 640 - NFAST * 5; j += NOUT) {
        const int u = j / 5 - NFAST, s = u < 0 ? j / 5 : NFAST + (u >> 1);
        flags32_wait(wq, ws, kqf_off(layer, s, 32));
        job_out(wq, ws, rs, pool.ysel, layer, s, j % 5, u < 0 ? -1 : u & 1);
      }
    }
    RSTAMP_(3)
    PH((tt >= 27 ? 6 : 5) + layer * 6)
  }
  }
  }
  int wq = wv_, tl = tt;
  asm volatile("" : "+s"(wq), "+s"(ljb), "+s"(lpl), "+s"(tl), "+s"(ws));
  job_last(wq, ws, out, ljb, lpl * 16 + (tl >> 1), tl & 1);
}

extern "C" void kernel_launch(void* const* d_in, const int* in_sizes, int n_in, void* d_out, int out_size, void* d_ws, size_t ws_size,
                              hipStream_t stream) {
  const u16* x = (const u16*)d_in[0];
  const u16* mem0 = (const u16*)d_in[1];
  const u16* Wq = (const u16*)d_in[2];
  const u16* Wk = (const u16*)d_in[3];
  const u16* Wv = (const u16*)d_in[4];
  const u16* Wo = (const u16*)d_in[5];
  const u16* wr = (const u16*)d_in[8];
  const u16* wi = (const u16*)d_in[9];
  unsigned char* ws = (unsigned char*)d_ws;
  u16* out = (u16*)d_out;
  (void)hipFuncSetAttribute((const void*)k_main, hipFuncAttributeMaxDynamicSharedMemorySize, MAIN_LDS);
  k_main<<<256, 256, MAIN_LDS, stream>>>(ws, x, mem0, Wq, Wk, Wv, Wo, wr, wi, out, 2);
}
```

```cpp
#include <hip/hip_runtime.h>
#include <hip/hip_bf16.h>
#include <stdint.h>

typedef unsigned v4u __attribute__((ext_vector_type(4)));
typedef unsigned v2u __attribute__((ext_vector_type(2)));
typedef float f32x4 __attribute__((ext_vector_type(4)));
typedef float f32x2 __attribute__((ext_vector_type(2)));
typedef __bf16 bf16x8 __attribute__((ext_vector_type(8)));
typedef __bf16 bf16x2 __attribute__((ext_vector_type(2)));
typedef unsigned short u16;

constexpr int S_ = 2048, D_ = 640, NSEG = 64, KT = 20, RT = 128;
constexpr int NFT = 40;
constexpr int NFK = 20;
constexpr int NCOL = 161;
constexpr int WIN = 32768, REG = 16 * WIN;
constexpr int W_MEM = 10 * WIN, W_MISC = 15 * WIN;
__host__ __device__ constexpr int copy_off(int c) { return (c & 3) * 2048 + (c >> 2) * 16384; }
constexpr int M_STAMP = W_MISC + copy_off(4), M_TM = M_STAMP + 40 * 64, M_CAL = W_MISC + copy_off(3);
constexpr int PAGE = 4 * REG, NYP = 22, P_Y = NSEG * REG;
constexpr size_t SZ_POOL = (size_t)P_Y + (size_t)(NYP + 1) * PAGE;

constexpr size_t OFF_CNT = 0;
constexpr int CNT_IDFT = 0, CNT_OUT = 1;
constexpr size_t OFF_PROG = OFF_CNT + (size_t)3 * 32 * 128;
constexpr size_t OFF_KQF = OFF_PROG + (size_t)NSEG * 4096;
#define FNAP 6
#define FSP 16
#define FNG 32
constexpr int FAN_G = (161 * FSP + 127) / 128 * 128 > 3840 ? (161 * FSP + 127) / 128 * 128 : 3840;
constexpr size_t OFF_FAN = OFF_KQF + (size_t)NSEG * 4096;
constexpr size_t HO_LAYER = OFF_FAN + (size_t)2 * FNG * FAN_G;
constexpr size_t OFF_HO = SZ_POOL;
constexpr size_t HO_FIRST = OFF_FAN + (size_t)FNG * FAN_G;
constexpr size_t OFF_HS = OFF_HO + 2 * HO_LAYER;
constexpr int HS_GO = (int)OFF_HS, HS_PAY = HS_GO + 4096, HS_ASK = HS_PAY + 1024, HS_HI = HS_ASK + 2048, HS_CAL = HS_HI + 2048;
__host__ __device__ constexpr int prog_off(int layer, int rb, int slot) { return (int)(OFF_HO + layer * HO_LAYER + OFF_PROG) + (rb * 64 + slot) * 64; }
__host__ __device__ constexpr int kqf_off(int layer, int sg, int h) { return (int)(OFF_HO + layer * HO_LAYER + OFF_KQF) + (sg * 64 + h) * 64; }
__host__ __device__ constexpr int cnt_off(int layer, int kind, int rb) { return (int)(OFF_HO + layer * HO_LAYER + OFF_CNT) + (kind * 32 + rb) * 128; }
__host__ __device__ constexpr int fan_off(int layer, int set, int i) { return (int)(OFF_HO + layer * HO_LAYER + OFF_FAN) + ((set ^ layer ^ 1) * FNG + i) * FAN_G; }
constexpr size_t OFF_XR = (OFF_HS + 65536 + 4095) / 4096 * 4096;
constexpr size_t OFF_ST = OFF_XR + (size_t)RT * KT * 1024;
constexpr size_t OFF_QF = OFF_ST + (size_t)S_ * 48;
constexpr size_t OFF_KF = OFF_QF + (size_t)32 * RT * 4 * 1024;
constexpr size_t OFF_UF = OFF_KF + (size_t)32 * RT * 4 * 1024;
constexpr size_t OFF_PXF = OFF_UF + (size_t)NSEG * 80 * 16 * 1024;
constexpr size_t OFF_PXM = OFF_PXF + (size_t)NSEG * 32 * 2 * 1024;
constexpr size_t OFF_INV = OFF_PXM + (size_t)NSEG * 8 * 2 * 1024;
constexpr size_t OFF_X1T = OFF_INV + (size_t)20 * NFK * 1024;
#define ZPAD 32
#define XPAD 32
constexpr int XS = S_ + XPAD, ZS = S_ + ZPAD;
constexpr size_t OFF_ZT = OFF_X1T + (size_t)160 * XS * 8;
constexpr size_t OFF_WT = OFF_ZT + (size_t)161 * ZS * 8;
constexpr size_t OFF_DBG = OFF_WT + (size_t)321 * S_ * 4;
constexpr size_t OFF_END = OFF_DBG + (size_t)1048576;
static_assert(OFF_END <= ((size_t)256 << 20), "workspace too large");

constexpr int AUX_SC1 = 16;
#define PH(k)
#define PHV(k, v)
#define PHT(n)

__device__ __forceinline__ f32x4 mfma(v4u a, v4u b, f32x4 c) {
  return __builtin_amdgcn_mfma_f32_16x16x32_bf16(__builtin_bit_cast(bf16x8, a), __builtin_bit_cast(bf16x8, b), c, 0, 0, 0);
}
__device__ __forceinline__ unsigned pk(float a, float b) {
  f32x2 v = {a, b};
  return __builtin_bit_cast(unsigned, __builtin_convertvector(v, bf16x2));
}
__device__ __forceinline__ float lo_f(unsigned u) { return __builtin_bit_cast(float, u << 16); }
__device__ __forceinline__ float hi_f(unsigned u) { return __builtin_bit_cast(float, u & 0xFFFF0000u); }
__device__ __forceinline__ float bfr(float x) { return lo_f(pk(x, x)); }
__device__ __forceinline__ v4u pk8(f32x4 a, f32x4 b) { return v4u{pk(a.x, a.y), pk(a.z, a.w), pk(b.x, b.y), pk(b.z, b.w)}; }
__device__ __forceinline__ int sigma(int g, int jj) { return 16 * (jj >> 2) + 4 * g + (jj & 3); }
__device__ __forceinline__ int colmap(int c8, int jj) {
  const int p = c8 >> 3, ga = (c8 >> 1) & 3, half = c8 & 1;
  return 32 * (2 * p + (ga >> 1)) + 16 * ((jj >> 1) & 1) + 4 * (2 * (ga & 1) + (jj >> 2)) + 2 * half + (jj & 1);
}
__device__ __forceinline__ int pool_base(const void* ws) { return (int)((0u - (unsigned)(uintptr_t)ws) & (unsigned)(PAGE - 1)); }
__device__ __forceinline__ unsigned xcc_id() { return (unsigned)__builtin_amdgcn_s_getreg(20 | (3 << 11)) & 7u; }
__device__ __forceinline__ float shx(float v, int m) { return __shfl_xor(v, m); }
__device__ __forceinline__ unsigned nosent(unsigned x) { return x < 0xFFFFFFFEu ? x : 0xFFFFFFFEu; }

__device__ __forceinline__ f32x2 frag_stat(v4u c) {
  const float v[8] = {lo_f(c.x), hi_f(c.x), lo_f(c.y), hi_f(c.y), lo_f(c.z), hi_f(c.z), lo_f(c.w), hi_f(c.w)};
  float q = 0.f;
#pragma unroll
  for (int j = 0; j < 8; ++j) q = __builtin_fmaf(v[j], v[j], q);
  return f32x2{((v[0] + v[1]) + (v[2] + v[3])) + ((v[4] + v[5]) + (v[6] + v[7])), q};
}
__device__ __forceinline__ f32x2 rows_sum(f32x2 a) {
  a.x += shx(a.x, 16); a.y += shx(a.y, 16); a.x += shx(a.x, 32); a.y += shx(a.y, 32);
  return a;
}

typedef int v4i __attribute__((ext_vector_type(4)));
__device__ __forceinline__ int y_region(unsigned long long sel, int s) { return (s * 80 + (s * 43 >> 7) * 16 + (int)((sel >> s) & 1ull)) * 8192; }

__device__ __forceinline__ int lane_id() {
  int l;
  asm volatile("v_mbcnt_lo_u32_b32 %0, -1, 0\n\tv_mbcnt_hi_u32_b32 %0, -1, %0" : "=v"(l));
  return l;
}
#define TIDX (wv_ * 64 + lane_id())
typedef __amdgpu_buffer_rsrc_t rsrc_t;
__device__ __forceinline__ rsrc_t ws_rsrc(unsigned char* ws) { return __builtin_amdgcn_make_buffer_rsrc(ws, 0, (int)OFF_END, 0x00020000); }
__device__ __forceinline__ v4u ldc(rsrc_t rs, int off) { return __builtin_amdgcn_raw_buffer_load_b128(rs, off, 0, AUX_SC1); }
__device__ __forceinline__ void stc(rsrc_t rs, int off, v4u v) { __builtin_amdgcn_raw_buffer_store_b128(v, rs, off, 0, AUX_SC1); }

extern __shared__ __attribute__((aligned(16))) unsigned char dyn_lds[];
__device__ __forceinline__ unsigned lds_base() { return (unsigned)(uintptr_t)(__attribute__((address_space(3))) unsigned char*)dyn_lds; }
__device__ __forceinline__ unsigned rtc() { return (unsigned)__builtin_amdgcn_s_memrealtime(); }
__device__ __forceinline__ int wave_imax(int v) {
  asm volatile("s_nop 1\n\tv_max_i32_dpp %0, %0, %0 quad_perm:[1,0,3,2] row_mask:0xf bank_mask:0xf\n\t"
               "s_nop 1\n\tv_max_i32_dpp %0, %0, %0 quad_perm:[2,3,0,1] row_mask:0xf bank_mask:0xf\n\t"
               "s_nop 1\n\tv_max_i32_dpp %0, %0, %0 row_half_mirror row_mask:0xf bank_mask:0xf\n\t"
               "s_nop 1\n\tv_max_i32_dpp %0, %0, %0 row_mirror row_mask:0xf bank_mask:0xf\n\ts_nop 0"
               : "+v"(v));
  const int a = __builtin_amdgcn_readlane(v, 0), b = __builtin_amdgcn_readlane(v, 16), c = __builtin_amdgcn_readlane(v, 32),
            d = __builtin_amdgcn_readlane(v, 48);
  return max(max(a, b), max(c, d));
}

#define PWN 40
#define PWL 52
typedef __attribute__((address_space(1))) unsigned gu32;
__device__ __forceinline__ void prog_prewait(int wv_, unsigned char* ws, int off) {
  if (TIDX == 0) {
    gu32* p = (gu32*)(ws + off);
    for (unsigned spins = 0; spins < (1u << 16); ++spins) {
      if (__hip_atomic_load(p, __ATOMIC_RELAXED, __HIP_MEMORY_SCOPE_AGENT) != 0xFFFFFFFFu) break;
      __builtin_amdgcn_s_sleep(48);
    }
  }
}
template <bool BAR = true> __device__ __forceinline__ void prog_wait(int wv_, unsigned char* ws, int off, bool lazy) {
  if (wv_ == 0) {
    const int l = lane_id();
    gu32* p0 = (gu32*)(ws + off + l * 64);
    for (unsigned spins = 0; spins < (1u << 17); ++spins) {
      const unsigned a = __hip_atomic_load(p0, __ATOMIC_RELAXED, __HIP_MEMORY_SCOPE_AGENT);
      if (!__any(a == 0xFFFFFFFFu)) break;
      __builtin_amdgcn_s_sleep(8);
      if (lazy) __builtin_amdgcn_s_sleep(PWN);
    }
  }
  if (BAR) __syncthreads();
}
__device__ __forceinline__ void flags32_wait(int wv_, unsigned char* ws, int off) {
  if (wv_ == 0) {
    const int l = lane_id();
    __attribute__((address_space(1))) unsigned long long* p0 = (__attribute__((address_space(1))) unsigned long long*)(ws + off + (l & 31) * 64 + (l >> 5) * 8);
    for (unsigned spins = 0; spins < (1u << 17); ++spins) {
      const unsigned long long v = __hip_atomic_load(p0, __ATOMIC_RELAXED, __HIP_MEMORY_SCOPE_AGENT);
      if (!__any(max((unsigned)v, (unsigned)(v >> 32)) == 0xFFFFFFFFu)) break;
      __builtin_amdgcn_s_sleep(16);
    }
  }
  __syncthreads();
}
__device__ __forceinline__ void dep_signal(int wv_, unsigned char* ws, int off, unsigned inc = 1u) {
  asm volatile("s_waitcnt vmcnt(0)" ::: "memory");
  __syncthreads();
  if (TIDX == 0) __hip_atomic_fetch_add((gu32*)(ws + off), inc, __ATOMIC_RELAXED, __HIP_MEMORY_SCOPE_AGENT);
}

template <int SP> __device__ __forceinline__ void fan_signal(int wv_, unsigned char* ws, int fan, int i) {
  asm volatile("s_waitcnt vmcnt(0)" ::: "memory");
  __syncthreads();
  const int l = lane_id();
  if (wv_ == 0 && l < FNG) __hip_atomic_store((gu32*)(ws + fan + l * FAN_G + i * SP), nosent(rtc()), __ATOMIC_RELAXED, __HIP_MEMORY_SCOPE_AGENT);
}
constexpr int L_WHEN = 90112;
template <int SP, int N, bool BAR = true> __device__ __forceinline__ void fan_wait(int wv_, unsigned char* ws, int fan, int salt) {
  if (wv_ * 64 < N) {
    gu32* p = (gu32*)(ws + fan + (((int)xcc_id() * (FNG / 8) + (salt & (FNG / 8 - 1))) * FAN_G + min(TIDX, N - 1) * SP));
    unsigned v;
    for (unsigned spins = 0; spins < (1u << 17); ++spins) {
      v = __hip_atomic_load(p, __ATOMIC_RELAXED, __HIP_MEMORY_SCOPE_AGENT);
      const unsigned long long m = __ballot(v == 0xFFFFFFFFu);
      if (m == 0) break;
      __builtin_amdgcn_s_sleep(N == NCOL ? 24 : FNAP);
      if (N == 20 && (m & 0x3FF)) __builtin_amdgcn_s_sleep(40);
    }
    if (N == 20) {
      const unsigned t = rtc();
      *(unsigned*)(dyn_lds + L_WHEN) = t + (unsigned)wave_imax((int)(v - t));
    }
  }
  if (BAR) __syncthreads();
}

typedef unsigned v8u __attribute__((ext_vector_type(8)));
typedef unsigned v16u __attribute__((ext_vector_type(16)));
typedef unsigned v32u __attribute__((ext_vector_type(32)));
__device__ __forceinline__ float cosi(int i) { return __builtin_amdgcn_cosf((float)i * (1.f / 2048)); }
__device__ __forceinline__ float sini(int i) { return __builtin_amdgcn_sinf((float)i * (1.f / 2048)); }
__device__ __forceinline__ void cis8(v32u& d, int x0, int dx, int d4, unsigned m) {
#pragma unroll
  for (int q = 0; q < 4; ++q) {
    const int x = x0 + dx * 2 * (q & 1) + d4 * (q >> 1);
    d[q] = pk(cosi(x), cosi(x + dx)); d[4 + q] = d[q] ^ m;
    d[8 + q] = pk(sini(x), sini(x + dx)); d[12 + q] = d[8 + q] ^ m;
  }
}
#define LDS_V4(a) (*(__attribute__((address_space(3))) v4u*)(uintptr_t)(a))
#define LDS_U32(a) (*(__attribute__((address_space(3))) unsigned*)(uintptr_t)(a))
#define LDS_V2(a) (*(__attribute__((address_space(3))) v2u*)(uintptr_t)(a))
#define COL_ASM \
   "s_waitcnt lgkmcnt(0)\n\t" "s_barrier\n\t" "ds_read_b64_tr_b16 v[136:137], v16 offset:0\n\t" "ds_read_b64_tr_b16 v[138:139], v16 offset:1056\n\t" \
   "ds_read_b64_tr_b16 v[140:141], v16 offset:8448\n\t" "ds_read_b64_tr_b16 v[142:143], v16 offset:9504\n\t" \
   "ds_read_b64_tr_b16 v[144:145], v16 offset:8\n\t" "ds_read_b64_tr_b16 v[146:147], v16 offset:1064\n\t" \
   "ds_read_b64_tr_b16 v[148:149], v16 offset:8456\n\t" "ds_read_b64_tr_b16 v[150:151], v16 offset:9512\n\t" "s_waitcnt lgkmcnt(4)\n\t" \
   "v_mfma_f32_16x16x32_bf16 v[160:163], v[136:139], v[32:35], 0\n\t" "v_mfma_f32_16x16x32_bf16 v[164:167], v[136:139], v[40:43], 0\n\t" \
   "v_mfma_f32_16x16x32_bf16 v[160:163], v[140:143], v[36:39], v[160:163]\n\t" \
   "v_mfma_f32_16x16x32_bf16 v[164:167], v[140:143], v[44:47], v[164:167]\n\t" "ds_read_b64_tr_b16 v[152:153], v16 offset:16\n\t" \
   "ds_read_b64_tr_b16 v[154:155], v16 offset:1072\n\t" "ds_read_b64_tr_b16 v[156:157], v16 offset:8464\n\t" \
   "ds_read_b64_tr_b16 v[158:159], v16 offset:9520\n\t" "s_nop 3\n\t" "s_waitcnt lgkmcnt(4)\n\t" \
   "v_mfma_f32_16x16x32_bf16 v[168:171], v[144:147], v[32:35], 0\n\t" "v_add_f32 v160, v160, v165\n\t" "v_sub_f32 v161, v161, v164\n\t" \
   "v_mfma_f32_16x16x32_bf16 v[172:175], v[144:147], v[40:43], 0\n\t" "v_add_f32 v162, v162, v167\n\t" "v_sub_f32 v163, v163, v166\n\t" \
   "v_mfma_f32_16x16x32_bf16 v[168:171], v[148:151], v[36:39], v[168:171]\n\t" "v_mul_f32 v24, v161, v49\n\t" "v_mul_f32 v25, v161, v48\n\t" \
   "v_mfma_f32_16x16x32_bf16 v[172:175], v[148:151], v[44:47], v[172:175]\n\t" "v_mul_f32 v26, v163, v49\n\t" "v_mul_f32 v27, v163, v48\n\t" \
   "v_fma_f32 v177, v160, v49, v25\n\t" "v_fma_f32 v176, v160, v48, -v24\n\t" "v_fma_f32 v179, v162, v49, v27\n\t" \
   "v_fma_f32 v178, v162, v48, -v26\n\t" "ds_read_b64_tr_b16 v[136:137], v16 offset:24\n\t" "ds_read_b64_tr_b16 v[138:139], v16 offset:1080\n\t" \
   "ds_read_b64_tr_b16 v[140:141], v16 offset:8472\n\t" "ds_read_b64_tr_b16 v[142:143], v16 offset:9528\n\t" "s_waitcnt lgkmcnt(4)\n\t" \
   "v_mfma_f32_16x16x32_bf16 v[160:163], v[152:155], v[32:35], 0\n\t" "v_add_f32 v168, v168, v173\n\t" "v_sub_f32 v169, v169, v172\n\t" \
   "v_mfma_f32_16x16x32_bf16 v[164:167], v[152:155], v[40:43], 0\n\t" "v_add_f32 v170, v170, v175\n\t" "v_sub_f32 v171, v171, v174\n\t" \
   "v_mfma_f32_16x16x32_bf16 v[160:163], v[156:159], v[36:39], v[160:163]\n\t" "v_mul_f32 v24, v169, v51\n\t" "v_mul_f32 v25, v169, v50\n\t" \
   "v_mfma_f32_16x16x32_bf16 v[164:167], v[156:159], v[44:47], v[164:167]\n\t" "v_mul_f32 v26, v171, v51\n\t" "v_mul_f32 v27, v171, v50\n\t" \
   "v_fma_f32 v181, v168, v51, v25\n\t" "v_fma_f32 v180, v168, v50, -v24\n\t" "v_fma_f32 v183, v170, v51, v27\n\t" \
   "v_fma_f32 v182, v170, v50, -v26\n\t" "v_cvt_pk_bf16_f32 v12, v177, v181\n\t" "v_cvt_pk_bf16_f32 v8, v176, v180\n\t" \
   "v_cvt_pk_bf16_f32 v188, v179, v183\n\t" "v_cvt_pk_bf16_f32 v184, v178, v182\n\t" "ds_read_b64_tr_b16 v[144:145], v16 offset:128\n\t" \
   "ds_read_b64_tr_b16 v[146:147], v16 offset:1184\n\t" "ds_read_b64_tr_b16 v[148:149], v16 offset:8576\n\t" \
   "ds_read_b64_tr_b16 v[150:151], v16 offset:9632\n\t" "s_waitcnt lgkmcnt(4)\n\t" "v_mfma_f32_16x16x32_bf16 v[168:171], v[136:139], v[32:35], 0\n\t" \
   "v_add_f32 v160, v160, v165\n\t" "v_sub_f32 v161, v161, v164\n\t" "v_mfma_f32_16x16x32_bf16 v[172:175], v[136:139], v[40:43], 0\n\t" \
   "v_add_f32 v162, v162, v167\n\t" "v_sub_f32 v163, v163, v166\n\t" "v_mfma_f32_16x16x32_bf16 v[168:171], v[140:143], v[36:39], v[168:171]\n\t" \
   "v_mul_f32 v24, v161, v53\n\t" "v_mul_f32 v25, v161, v52\n\t" "v_mfma_f32_16x16x32_bf16 v[172:175], v[140:143], v[44:47], v[172:175]\n\t" \
   "v_mul_f32 v26, v163, v53\n\t" "v_mul_f32 v27, v163, v52\n\t" "v_fma_f32 v177, v160, v53, v25\n\t" "v_fma_f32 v176, v160, v52, -v24\n\t" \
   "v_fma_f32 v179, v162, v53, v27\n\t" "v_fma_f32 v178, v162, v52, -v26\n\t" "ds_read_b64_tr_b16 v[152:153], v16 offset:136\n\t" \
   "ds_read_b64_tr_b16 v[154:155], v16 offset:1192\n\t" "ds_read_b64_tr_b16 v[156:157], v16 offset:8584\n\t" \
   "ds_read_b64_tr_b16 v[158:159], v16 offset:9640\n\t" "s_waitcnt lgkmcnt(4)\n\t" "v_mfma_f32_16x16x32_bf16 v[160:163], v[144:147], v[32:35], 0\n\t" \
   "v_add_f32 v168, v168, v173\n\t" "v_sub_f32 v169, v169, v172\n\t" "v_mfma_f32_16x16x32_bf16 v[164:167], v[144:147], v[40:43], 0\n\t" \
   "v_add_f32 v170, v170, v175\n\t" "v_sub_f32 v171, v171, v174\n\t" "v_mfma_f32_16x16x32_bf16 v[160:163], v[148:151], v[36:39], v[160:163]\n\t" \
   "v_mul_f32 v24, v169, v55\n\t" "v_mul_f32 v25, v169, v54\n\t" "v_mfma_f32_16x16x32_bf16 v[164:167], v[148:151], v[44:47], v[164:167]\n\t" \
   "v_mul_f32 v26, v171, v55\n\t" "v_mul_f32 v27, v171, v54\n\t" "v_fma_f32 v181, v168, v55, v25\n\t" "v_fma_f32 v180, v168, v54, -v24\n\t" \
   "v_fma_f32 v183, v170, v55, v27\n\t" "v_fma_f32 v182, v170, v54, -v26\n\t" "v_cvt_pk_bf16_f32 v13, v177, v181\n\t" \
   "v_cvt_pk_bf16_f32 v9, v176, v180\n\t" "v_cvt_pk_bf16_f32 v189, v179, v183\n\t" "v_cvt_pk_bf16_f32 v185, v178, v182\n\t" \
   "ds_read_b64_tr_b16 v[136:137], v16 offset:144\n\t" "ds_read_b64_tr_b16 v[138:139], v16 offset:1200\n\t" \
   "ds_read_b64_tr_b16 v[140:141], v16 offset:8592\n\t" "ds_read_b64_tr_b16 v[142:143], v16 offset:9648\n\t" "s_waitcnt lgkmcnt(4)\n\t" \
   "v_mfma_f32_16x16x32_bf16 v[168:171], v[152:155], v[32:35], 0\n\t" "v_add_f32 v160, v160, v165\n\t" "v_sub_f32 v161, v161, v164\n\t" \
   "v_mfma_f32_16x16x32_bf16 v[172:175], v[152:155], v[40:43], 0\n\t" "v_add_f32 v162, v162, v167\n\t" "v_sub_f32 v163, v163, v166\n\t" \
   "v_mfma_f32_16x16x32_bf16 v[168:171], v[156:159], v[36:39], v[168:171]\n\t" "v_mul_f32 v24, v161, v57\n\t" "v_mul_f32 v25, v161, v56\n\t" \
   "v_mfma_f32_16x16x32_bf16 v[172:175], v[156:159], v[44:47], v[172:175]\n\t" "v_mul_f32 v26, v163, v57\n\t" "v_mul_f32 v27, v163, v56\n\t" \
   "v_fma_f32 v177, v160, v57, v25\n\t" "v_fma_f32 v176, v160, v56, -v24\n\t" "v_fma_f32 v179, v162, v57, v27\n\t" \
   "v_fma_f32 v178, v162, v56, -v26\n\t" "ds_read_b64_tr_b16 v[144:145], v16 offset:152\n\t" "ds_read_b64_tr_b16 v[146:147], v16 offset:1208\n\t" \
   "ds_read_b64_tr_b16 v[148:149], v16 offset:8600\n\t" "ds_read_b64_tr_b16 v[150:151], v16 offset:9656\n\t" "s_waitcnt lgkmcnt(4)\n\t" \
   "v_mfma_f32_16x16x32_bf16 v[160:163], v[136:139], v[32:35], 0\n\t" "v_add_f32 v168, v168, v173\n\t" "v_sub_f32 v169, v169, v172\n\t" \
   "v_mfma_f32_16x16x32_bf16 v[164:167], v[136:139], v[40:43], 0\n\t" "v_add_f32 v170, v170, v175\n\t" "v_sub_f32 v171, v171, v174\n\t" \
   "v_mfma_f32_16x16x32_bf16 v[160:163], v[140:143], v[36:39], v[160:163]\n\t" "v_mul_f32 v24, v169, v59\n\t" "v_mul_f32 v25, v169, v58\n\t" \
   "v_mfma_f32_16x16x32_bf16 v[164:167], v[140:143], v[44:47], v[164:167]\n\t" "v_mul_f32 v26, v171, v59\n\t" "v_mul_f32 v27, v171, v58\n\t" \
   "v_fma_f32 v181, v168, v59, v25\n\t" "v_fma_f32 v180, v168, v58, -v24\n\t" "v_fma_f32 v183, v170, v59, v27\n\t" \
   "v_fma_f32 v182, v170, v58, -v26\n\t" "v_cvt_pk_bf16_f32 v14, v177, v181\n\t" "v_cvt_pk_bf16_f32 v10, v176, v180\n\t" \
   "v_cvt_pk_bf16_f32 v190, v179, v183\n\t" "v_cvt_pk_bf16_f32 v186, v178, v182\n\t" "s_nop 1\n\t" "s_waitcnt lgkmcnt(0)\n\t" \
   "v_mfma_f32_16x16x32_bf16 v[168:171], v[144:147], v[32:35], 0\n\t" "v_add_f32 v160, v160, v165\n\t" "v_sub_f32 v161, v161, v164\n\t" \
   "v_mfma_f32_16x16x32_bf16 v[172:175], v[144:147], v[40:43], 0\n\t" "v_add_f32 v162, v162, v167\n\t" "v_sub_f32 v163, v163, v166\n\t" \
   "v_mfma_f32_16x16x32_bf16 v[168:171], v[148:151], v[36:39], v[168:171]\n\t" "v_mul_f32 v24, v161, v61\n\t" "v_mul_f32 v25, v161, v60\n\t" \
   "v_mfma_f32_16x16x32_bf16 v[172:175], v[148:151], v[44:47], v[172:175]\n\t" "v_mul_f32 v26, v163, v61\n\t" "v_mul_f32 v27, v163, v60\n\t" \
   "v_fma_f32 v177, v160, v61, v25\n\t" "v_fma_f32 v176, v160, v60, -v24\n\t" "v_fma_f32 v179, v162, v61, v27\n\t" \
   "v_fma_f32 v178, v162, v60, -v26\n\t" "s_nop 1\n\t" "v_add_f32 v168, v168, v173\n\t" "v_sub_f32 v169, v169, v172\n\t" "v_mul_f32 v24, v169, v63\n\t" \
   "v_mul_f32 v25, v169, v62\n\t" "v_fma_f32 v181, v168, v63, v25\n\t" "v_fma_f32 v180, v168, v62, -v24\n\t" "v_cvt_pk_bf16_f32 v15, v177, v181\n\t" \
   "v_cvt_pk_bf16_f32 v11, v176, v180\n\t" "s_nop 1\n\t" "v_mfma_f32_16x16x32_bf16 v[160:163], v[64:67], v[8:11], 0\n\t" \
   "v_add_f32 v170, v170, v175\n\t" "v_sub_f32 v171, v171, v174\n\t" "v_mfma_f32_16x16x32_bf16 v[164:167], v[64:67], v[12:15], 0\n\t" \
   "v_mul_f32 v24, v171, v63\n\t" "v_mfma_f32_16x16x32_bf16 v[160:163], v[72:75], v[12:15], v[160:163]\n\t" "v_mul_f32 v25, v171, v62\n\t" \
   "v_mfma_f32_16x16x32_bf16 v[164:167], v[128:131], v[8:11], v[164:167]\n\t" "v_fma_f32 v183, v170, v63, v25\n\t" \
   "v_fma_f32 v182, v170, v62, -v24\n\t" "v_cvt_pk_bf16_f32 v191, v179, v183\n\t" "v_cvt_pk_bf16_f32 v187, v178, v182\n\t" "s_nop 4\n\t" \
   "v_mfma_f32_16x16x32_bf16 v[168:171], v[68:71], v[8:11], 0\n\t" "v_mul_f32 v24, v164, v97\n\t" "v_mul_f32 v25, v164, v96\n\t" \
   "v_mul_f32 v26, v165, v99\n\t" "v_mfma_f32_16x16x32_bf16 v[172:175], v[68:71], v[12:15], 0\n\t" "v_mul_f32 v27, v165, v98\n\t" \
   "v_mul_f32 v28, v166, v101\n\t" "v_mul_f32 v29, v166, v100\n\t" "v_mfma_f32_16x16x32_bf16 v[168:171], v[76:79], v[12:15], v[168:171]\n\t" \
   "v_mul_f32 v30, v167, v103\n\t" "v_mul_f32 v31, v167, v102\n\t" "v_fma_f32 v164, v160, v97, v25\n\t" \
   "v_mfma_f32_16x16x32_bf16 v[172:175], v[132:135], v[8:11], v[172:175]\n\t" "v_fma_f32 v160, v160, v96, -v24\n\t" \
   "v_fma_f32 v165, v161, v99, v27\n\t" "v_fma_f32 v161, v161, v98, -v26\n\t" "v_fma_f32 v166, v162, v101, v29\n\t" \
   "v_fma_f32 v162, v162, v100, -v28\n\t" "v_fma_f32 v167, v163, v103, v31\n\t" "v_fma_f32 v163, v163, v102, -v30\n\t" \
   "v_cvt_pk_bf16_f32 v140, v164, v165\n\t" "v_cvt_pk_bf16_f32 v136, v160, v161\n\t" "v_cvt_pk_bf16_f32 v141, v166, v167\n\t" \
   "v_cvt_pk_bf16_f32 v137, v162, v163\n\t" "v_mfma_f32_16x16x32_bf16 v[160:163], v[64:67], v[184:187], 0\n\t" "v_mul_f32 v24, v172, v105\n\t" \
   "v_mul_f32 v25, v172, v104\n\t" "v_mul_f32 v26, v173, v107\n\t" "v_mfma_f32_16x16x32_bf16 v[164:167], v[64:67], v[188:191], 0\n\t" \
   "v_mul_f32 v27, v173, v106\n\t" "v_mul_f32 v28, v174, v109\n\t" "v_mul_f32 v29, v174, v108\n\t" \
   "v_mfma_f32_16x16x32_bf16 v[160:163], v[72:75], v[188:191], v[160:163]\n\t" "v_mul_f32 v30, v175, v111\n\t" "v_mul_f32 v31, v175, v110\n\t" \
   "v_fma_f32 v172, v168, v105, v25\n\t" "v_mfma_f32_16x16x32_bf16 v[164:167], v[128:131], v[184:187], v[164:167]\n\t" \
   "v_fma_f32 v168, v168, v104, -v24\n\t" "v_fma_f32 v173, v169, v107, v27\n\t" "v_fma_f32 v169, v169, v106, -v26\n\t" \
   "v_fma_f32 v174, v170, v109, v29\n\t" "v_fma_f32 v170, v170, v108, -v28\n\t" "v_fma_f32 v175, v171, v111, v31\n\t" \
   "v_fma_f32 v171, v171, v110, -v30\n\t" "v_cvt_pk_bf16_f32 v142, v172, v173\n\t" "v_cvt_pk_bf16_f32 v138, v168, v169\n\t" \
   "v_cvt_pk_bf16_f32 v143, v174, v175\n\t" "v_cvt_pk_bf16_f32 v139, v170, v171\n\t" "v_mfma_f32_16x16x32_bf16 v[168:171], v[68:71], v[184:187], 0\n\t" \
   "v_mul_f32 v24, v164, v113\n\t" "v_mul_f32 v25, v164, v112\n\t" "v_mul_f32 v26, v165, v115\n\t" \
   "v_mfma_f32_16x16x32_bf16 v[172:175], v[68:71], v[188:191], 0\n\t" "v_mul_f32 v27, v165, v114\n\t" "v_mul_f32 v28, v166, v117\n\t" \
   "v_mul_f32 v29, v166, v116\n\t" "v_mfma_f32_16x16x32_bf16 v[168:171], v[76:79], v[188:191], v[168:171]\n\t" "v_mul_f32 v30, v167, v119\n\t" \
   "v_mul_f32 v31, v167, v118\n\t" "v_fma_f32 v164, v160, v113, v25\n\t" "v_mfma_f32_16x16x32_bf16 v[172:175], v[132:135], v[184:187], v[172:175]\n\t" \
   "v_fma_f32 v160, v160, v112, -v24\n\t" "v_fma_f32 v165, v161, v115, v27\n\t" "v_fma_f32 v161, v161, v114, -v26\n\t" \
   "v_fma_f32 v166, v162, v117, v29\n\t" "v_fma_f32 v162, v162, v116, -v28\n\t" "v_fma_f32 v167, v163, v119, v31\n\t" \
   "v_fma_f32 v163, v163, v118, -v30\n\t" "v_cvt_pk_bf16_f32 v148, v164, v165\n\t" "v_cvt_pk_bf16_f32 v144, v160, v161\n\t" \
   "v_cvt_pk_bf16_f32 v149, v166, v167\n\t" "v_cvt_pk_bf16_f32 v145, v162, v163\n\t" "v_mfma_f32_16x16x32_bf16 v[160:163], v[136:139], v[64:67], 0\n\t" \
   "v_mul_f32 v24, v172, v121\n\t" "v_mul_f32 v25, v172, v120\n\t" "v_mul_f32 v26, v173, v123\n\t" \
   "v_mfma_f32_16x16x32_bf16 v[164:167], v[140:143], v[64:67], 0\n\t" "v_mul_f32 v27, v173, v122\n\t" "v_mul_f32 v28, v174, v125\n\t" \
   "v_mul_f32 v29, v174, v124\n\t" "v_mfma_f32_16x16x32_bf16 v[160:163], v[140:143], v[128:131], v[160:163]\n\t" "v_mul_f32 v30, v175, v127\n\t" \
   "v_mul_f32 v31, v175, v126\n\t" "v_fma_f32 v172, v168, v121, v25\n\t" "v_mfma_f32_16x16x32_bf16 v[164:167], v[136:139], v[72:75], v[164:167]\n\t" \
   "v_fma_f32 v168, v168, v120, -v24\n\t" "v_fma_f32 v173, v169, v123, v27\n\t" "v_fma_f32 v169, v169, v122, -v26\n\t" \
   "v_fma_f32 v174, v170, v125, v29\n\t" "v_fma_f32 v170, v170, v124, -v28\n\t" "v_fma_f32 v175, v171, v127, v31\n\t" \
   "v_fma_f32 v171, v171, v126, -v30\n\t" "v_cvt_pk_bf16_f32 v150, v172, v173\n\t" "v_cvt_pk_bf16_f32 v146, v168, v169\n\t" \
   "v_cvt_pk_bf16_f32 v151, v174, v175\n\t" "v_cvt_pk_bf16_f32 v147, v170, v171\n\t" "v_mfma_f32_16x16x32_bf16 v[168:171], v[136:139], v[68:71], 0\n\t" \
   "v_mul_f32 v24, v164, v81\n\t" "v_mul_f32 v25, v164, v80\n\t" "v_mul_f32 v26, v165, v83\n\t" \
   "v_mfma_f32_16x16x32_bf16 v[172:175], v[140:143], v[68:71], 0\n\t" "v_mul_f32 v27, v165, v82\n\t" "v_mul_f32 v28, v166, v85\n\t" \
   "v_mul_f32 v29, v166, v84\n\t" "v_mfma_f32_16x16x32_bf16 v[168:171], v[140:143], v[132:135], v[168:171]\n\t" "v_mul_f32 v30, v167, v87\n\t" \
   "v_mul_f32 v31, v167, v86\n\t" "v_fma_f32 v164, v160, v81, v25\n\t" "v_mfma_f32_16x16x32_bf16 v[172:175], v[136:139], v[76:79], v[172:175]\n\t" \
   "v_fma_f32 v160, v160, v80, -v24\n\t" "v_fma_f32 v165, v161, v83, v27\n\t" "v_fma_f32 v161, v161, v82, -v26\n\t" \
   "v_fma_f32 v166, v162, v85, v29\n\t" "v_fma_f32 v162, v162, v84, -v28\n\t" "v_fma_f32 v167, v163, v87, v31\n\t" \
   "v_fma_f32 v163, v163, v86, -v30\n\t" "v_cvt_pk_bf16_f32 v178, v164, v165\n\t" "v_cvt_pk_bf16_f32 v176, v160, v161\n\t" \
   "v_cvt_pk_bf16_f32 v179, v166, v167\n\t" "v_cvt_pk_bf16_f32 v177, v162, v163\n\t" "ds_write_b64 v17, v[178:179] offset:128\n\t" \
   "ds_write_b64 v17, v[176:177] offset:0\n\t" "v_mfma_f32_16x16x32_bf16 v[160:163], v[144:147], v[64:67], 0\n\t" "v_mul_f32 v24, v172, v89\n\t" \
   "v_mul_f32 v25, v172, v88\n\t" "v_mul_f32 v26, v173, v91\n\t" "v_mfma_f32_16x16x32_bf16 v[164:167], v[148:151], v[64:67], 0\n\t" \
   "v_mul_f32 v27, v173, v90\n\t" "v_mul_f32 v28, v174, v93\n\t" "v_mul_f32 v29, v174, v92\n\t" \
   "v_mfma_f32_16x16x32_bf16 v[160:163], v[148:151], v[128:131], v[160:163]\n\t" "v_mul_f32 v30, v175, v95\n\t" "v_mul_f32 v31, v175, v94\n\t" \
   "v_fma_f32 v172, v168, v89, v25\n\t" "v_mfma_f32_16x16x32_bf16 v[164:167], v[144:147], v[72:75], v[164:167]\n\t" \
   "v_fma_f32 v168, v168, v88, -v24\n\t" "v_fma_f32 v173, v169, v91, v27\n\t" "v_fma_f32 v169, v169, v90, -v26\n\t" \
   "v_fma_f32 v174, v170, v93, v29\n\t" "v_fma_f32 v170, v170, v92, -v28\n\t" "v_fma_f32 v175, v171, v95, v31\n\t" \
   "v_fma_f32 v171, v171, v94, -v30\n\t" "v_cvt_pk_bf16_f32 v182, v172, v173\n\t" "v_cvt_pk_bf16_f32 v180, v168, v169\n\t" \
   "v_cvt_pk_bf16_f32 v183, v174, v175\n\t" "v_cvt_pk_bf16_f32 v181, v170, v171\n\t" "ds_write_b64 v17, v[182:183] offset:8384\n\t" \
   "ds_write_b64 v17, v[180:181] offset:8256\n\t" "v_mfma_f32_16x16x32_bf16 v[168:171], v[144:147], v[68:71], 0\n\t" "v_mul_f32 v24, v164, v81\n\t" \
   "v_mul_f32 v25, v164, v80\n\t" "v_mul_f32 v26, v165, v83\n\t" "v_mfma_f32_16x16x32_bf16 v[172:175], v[148:151], v[68:71], 0\n\t" \
   "v_mul_f32 v27, v165, v82\n\t" "v_mul_f32 v28, v166, v85\n\t" "v_mul_f32 v29, v166, v84\n\t" \
   "v_mfma_f32_16x16x32_bf16 v[168:171], v[148:151], v[132:135], v[168:171]\n\t" "v_mul_f32 v30, v167, v87\n\t" "v_mul_f32 v31, v167, v86\n\t" \
   "v_fma_f32 v164, v160, v81, v25\n\t" "v_mfma_f32_16x16x32_bf16 v[172:175], v[144:147], v[76:79], v[172:175]\n\t" \
   "v_fma_f32 v160, v160, v80, -v24\n\t" "v_fma_f32 v165, v161, v83, v27\n\t" "v_fma_f32 v161, v161, v82, -v26\n\t" \
   "v_fma_f32 v166, v162, v85, v29\n\t" "v_fma_f32 v162, v162, v84, -v28\n\t" "v_fma_f32 v167, v163, v87, v31\n\t" \
   "v_fma_f32 v163, v163, v86, -v30\n\t" "v_cvt_pk_bf16_f32 v178, v164, v165\n\t" "v_cvt_pk_bf16_f32 v176, v160, v161\n\t" \
   "v_cvt_pk_bf16_f32 v179, v166, v167\n\t" "v_cvt_pk_bf16_f32 v177, v162, v163\n\t" "ds_write_b64 v18, v[178:179] offset:128\n\t" \
   "ds_write_b64 v18, v[176:177] offset:0\n\t" "v_mul_f32 v24, v172, v89\n\t" "v_mul_f32 v25, v172, v88\n\t" "v_mul_f32 v26, v173, v91\n\t" \
   "v_mul_f32 v27, v173, v90\n\t" "v_mul_f32 v28, v174, v93\n\t" "v_mul_f32 v29, v174, v92\n\t" "v_mul_f32 v30, v175, v95\n\t" \
   "v_mul_f32 v31, v175, v94\n\t" "v_fma_f32 v172, v168, v89, v25\n\t" "v_fma_f32 v168, v168, v88, -v24\n\t" "v_fma_f32 v173, v169, v91, v27\n\t" \
   "v_fma_f32 v169, v169, v90, -v26\n\t" "v_fma_f32 v174, v170, v93, v29\n\t" "v_fma_f32 v170, v170, v92, -v28\n\t" \
   "v_fma_f32 v175, v171, v95, v31\n\t" "v_fma_f32 v171, v171, v94, -v30\n\t" "v_cvt_pk_bf16_f32 v182, v172, v173\n\t" \
   "v_cvt_pk_bf16_f32 v180, v168, v169\n\t" "v_cvt_pk_bf16_f32 v183, v174, v175\n\t" "v_cvt_pk_bf16_f32 v181, v170, v171\n\t" \
   "ds_write_b64 v18, v[182:183] offset:8384\n\t" "ds_write_b64 v18, v[180:181] offset:8256\n\t" "s_waitcnt lgkmcnt(0)\n\t" "s_barrier\n\t" \
   "ds_read_b128 v[136:139], v19 offset:0\n\t" "ds_read_b128 v[140:143], v20 offset:0\n\t" "ds_read_b128 v[144:147], v19 offset:2064\n\t" \
   "ds_read_b128 v[148:151], v20 offset:2064\n\t" "s_waitcnt lgkmcnt(2)\n\t" "v_mfma_f32_16x16x32_bf16 v[160:163], v[136:139], v[32:35], 0\n\t" \
   "ds_read_b128 v[152:155], v19 offset:4128\n\t" "ds_read_b128 v[156:159], v20 offset:4128\n\t" \
   "v_mfma_f32_16x16x32_bf16 v[164:167], v[136:139], v[40:43], 0\n\t" "v_mfma_f32_16x16x32_bf16 v[160:163], v[140:143], v[36:39], v[160:163]\n\t" \
   "v_mfma_f32_16x16x32_bf16 v[164:167], v[140:143], v[44:47], v[164:167]\n\t" "s_waitcnt lgkmcnt(2)\n\t" \
   "v_mfma_f32_16x16x32_bf16 v[168:171], v[144:147], v[32:35], 0\n\t" "ds_read_b128 v[136:139], v19 offset:6192\n\t" \
   "ds_read_b128 v[140:143], v20 offset:6192\n\t" "v_mfma_f32_16x16x32_bf16 v[172:175], v[144:147], v[40:43], 0\n\t" "v_sub_f32 v160, v160, v165\n\t" \
   "v_add_f32 v161, v161, v164\n\t" "v_sub_f32 v162, v162, v167\n\t" "v_mfma_f32_16x16x32_bf16 v[168:171], v[148:151], v[36:39], v[168:171]\n\t" \
   "v_add_f32 v163, v163, v166\n\t" "v_cvt_pk_bf16_f32 v8, v160, v161\n\t" "v_cvt_pk_bf16_f32 v9, v162, v163\n\t" \
   "v_mfma_f32_16x16x32_bf16 v[172:175], v[148:151], v[44:47], v[172:175]\n\t" "s_waitcnt lgkmcnt(2)\n\t" \
   "v_mfma_f32_16x16x32_bf16 v[160:163], v[152:155], v[32:35], 0\n\t" "ds_read_b128 v[144:147], v19 offset:8256\n\t" \
   "ds_read_b128 v[148:151], v20 offset:8256\n\t" "v_mfma_f32_16x16x32_bf16 v[164:167], v[152:155], v[40:43], 0\n\t" "v_sub_f32 v168, v168, v173\n\t" \
   "v_add_f32 v169, v169, v172\n\t" "v_sub_f32 v170, v170, v175\n\t" "v_mfma_f32_16x16x32_bf16 v[160:163], v[156:159], v[36:39], v[160:163]\n\t" \
   "v_add_f32 v171, v171, v174\n\t" "v_cvt_pk_bf16_f32 v10, v168, v169\n\t" "v_cvt_pk_bf16_f32 v11, v170, v171\n\t" \
   "v_mfma_f32_16x16x32_bf16 v[164:167], v[156:159], v[44:47], v[164:167]\n\t" "buffer_store_dwordx4 v[8:11], v21, %[rs], 0 offen offset:0 sc1\n\t" \
   "s_waitcnt lgkmcnt(2)\n\t" "v_mfma_f32_16x16x32_bf16 v[168:171], v[136:139], v[32:35], 0\n\t" "ds_read_b128 v[152:155], v19 offset:10320\n\t" \
   "ds_read_b128 v[156:159], v20 offset:10320\n\t" "v_mfma_f32_16x16x32_bf16 v[172:175], v[136:139], v[40:43], 0\n\t" "v_sub_f32 v160, v160, v165\n\t" \
   "v_add_f32 v161, v161, v164\n\t" "v_sub_f32 v162, v162, v167\n\t" "v_mfma_f32_16x16x32_bf16 v[168:171], v[140:143], v[36:39], v[168:171]\n\t" \
   "v_add_f32 v163, v163, v166\n\t" "v_cvt_pk_bf16_f32 v12, v160, v161\n\t" "v_cvt_pk_bf16_f32 v13, v162, v163\n\t" \
   "v_mfma_f32_16x16x32_bf16 v[172:175], v[140:143], v[44:47], v[172:175]\n\t" "s_waitcnt lgkmcnt(2)\n\t" \
   "v_mfma_f32_16x16x32_bf16 v[160:163], v[144:147], v[32:35], 0\n\t" "ds_read_b128 v[136:139], v19 offset:12384\n\t" \
   "ds_read_b128 v[140:143], v20 offset:12384\n\t" "v_mfma_f32_16x16x32_bf16 v[164:167], v[144:147], v[40:43], 0\n\t" "v_sub_f32 v168, v168, v173\n\t" \
   "v_add_f32 v169, v169, v172\n\t" "v_sub_f32 v170, v170, v175\n\t" "v_mfma_f32_16x16x32_bf16 v[160:163], v[148:151], v[36:39], v[160:163]\n\t" \
   "v_add_f32 v171, v171, v174\n\t" "v_cvt_pk_bf16_f32 v14, v168, v169\n\t" "v_cvt_pk_bf16_f32 v15, v170, v171\n\t" \
   "v_mfma_f32_16x16x32_bf16 v[164:167], v[148:151], v[44:47], v[164:167]\n\t" "buffer_store_dwordx4 v[12:15], v21, %[rs], 0 offen offset:64 sc1\n\t" \
   "s_waitcnt lgkmcnt(2)\n\t" "v_mfma_f32_16x16x32_bf16 v[168:171], v[152:155], v[32:35], 0\n\t" "ds_read_b128 v[144:147], v19 offset:14448\n\t" \
   "ds_read_b128 v[148:151], v20 offset:14448\n\t" "v_mfma_f32_16x16x32_bf16 v[172:175], v[152:155], v[40:43], 0\n\t" "v_sub_f32 v160, v160, v165\n\t" \
   "v_add_f32 v161, v161, v164\n\t" "v_sub_f32 v162, v162, v167\n\t" "v_mfma_f32_16x16x32_bf16 v[168:171], v[156:159], v[36:39], v[168:171]\n\t" \
   "v_add_f32 v163, v163, v166\n\t" "v_cvt_pk_bf16_f32 v8, v160, v161\n\t" "v_cvt_pk_bf16_f32 v9, v162, v163\n\t" \
   "v_mfma_f32_16x16x32_bf16 v[172:175], v[156:159], v[44:47], v[172:175]\n\t" "s_waitcnt lgkmcnt(2)\n\t" \
   "v_mfma_f32_16x16x32_bf16 v[160:163], v[136:139], v[32:35], 0\n\t" "s_nop 0\n\t" "s_nop 0\n\t" \
   "v_mfma_f32_16x16x32_bf16 v[164:167], v[136:139], v[40:43], 0\n\t" "v_sub_f32 v168, v168, v173\n\t" "v_add_f32 v169, v169, v172\n\t" \
   "v_sub_f32 v170, v170, v175\n\t" "v_mfma_f32_16x16x32_bf16 v[160:163], v[140:143], v[36:39], v[160:163]\n\t" "v_add_f32 v171, v171, v174\n\t" \
   "v_cvt_pk_bf16_f32 v10, v168, v169\n\t" "v_cvt_pk_bf16_f32 v11, v170, v171\n\t" \
   "v_mfma_f32_16x16x32_bf16 v[164:167], v[140:143], v[44:47], v[164:167]\n\t" "buffer_store_dwordx4 v[8:11], v21, %[rs], 0 offen offset:128 sc1\n\t" \
   "s_waitcnt lgkmcnt(0)\n\t" "v_mfma_f32_16x16x32_bf16 v[168:171], v[144:147], v[32:35], 0\n\t" "s_nop 0\n\t" "s_nop 0\n\t" \
   "v_mfma_f32_16x16x32_bf16 v[172:175], v[144:147], v[40:43], 0\n\t" "v_sub_f32 v160, v160, v165\n\t" "v_add_f32 v161, v161, v164\n\t" \
   "v_sub_f32 v162, v162, v167\n\t" "v_mfma_f32_16x16x32_bf16 v[168:171], v[148:151], v[36:39], v[168:171]\n\t" "v_add_f32 v163, v163, v166\n\t" \
   "v_cvt_pk_bf16_f32 v12, v160, v161\n\t" "v_cvt_pk_bf16_f32 v13, v162, v163\n\t" \
   "v_mfma_f32_16x16x32_bf16 v[172:175], v[148:151], v[44:47], v[172:175]\n\t" "s_nop 7\n\t" "v_sub_f32 v168, v168, v173\n\t" \
   "v_add_f32 v169, v169, v172\n\t" "v_sub_f32 v170, v170, v175\n\t" "v_add_f32 v171, v171, v174\n\t" "v_cvt_pk_bf16_f32 v14, v168, v169\n\t" \
   "v_cvt_pk_bf16_f32 v15, v170, v171\n\t" "buffer_store_dwordx4 v[12:15], v21, %[rs], 0 offen offset:192 sc1\n\t" "s_nop 1"
#define COL_CLOB "v8", "v9", "v10", "v11", "v12", "v13", "v14", "v15", "v24", "v25", "v26", "v27", "v28", "v29", "v30", "v31", "v136", "v137", "v138", "v139", "v140", "v141", "v142", "v143", "v144", "v145", "v146", "v147", "v148", "v149", "v150", "v151", "v152", "v153", "v154", "v155", "v156", "v157", "v158", "v159", "v160", "v161", "v162", "v163", "v164", "v165", "v166", "v167", "v168", "v169", "v170", "v171", "v172", "v173", "v174", "v175", "v176", "v177", "v178", "v179", "v180", "v181", "v182", "v183", "v184", "v185", "v186", "v187", "v188", "v189", "v190", "v191"
#define IM_ASM \
   "s_mov_b32 s40, 0\n\t" "buffer_load_dwordx4 a[0:3], %[o0], %[rs], s40 offen offset:0 sc1\n\t" "s_mov_b32 s41, 20480\n\t" \
   "buffer_load_dwordx4 a[80:83], %[o0], %[rs], s41 offen offset:0 sc1\n\t" "s_mov_b32 s42, 40960\n\t" \
   "buffer_load_dwordx4 a[160:163], %[o1], %[rs], s42 offen offset:0 sc1\n\t" "buffer_load_dwordx4 a[4:7], %[o0], %[rs], s40 offen offset:1024 sc1\n\t" \
   "buffer_load_dwordx4 a[84:87], %[o0], %[rs], s41 offen offset:1024 sc1\n\t" \
   "buffer_load_dwordx4 a[164:167], %[o1], %[rs], s42 offen offset:1024 sc1\n\t" \
   "buffer_load_dwordx4 a[8:11], %[o0], %[rs], s40 offen offset:2048 sc1\n\t" \
   "buffer_load_dwordx4 a[88:91], %[o0], %[rs], s41 offen offset:2048 sc1\n\t" \
   "buffer_load_dwordx4 a[168:171], %[o1], %[rs], s42 offen offset:2048 sc1\n\t" \
   "buffer_load_dwordx4 a[12:15], %[o0], %[rs], s40 offen offset:3072 sc1\n\t" \
   "buffer_load_dwordx4 a[92:95], %[o0], %[rs], s41 offen offset:3072 sc1\n\t" \
   "buffer_load_dwordx4 a[172:175], %[o1], %[rs], s42 offen offset:3072 sc1\n\t" "s_mov_b32 s40, 4096\n\t" \
   "buffer_load_dwordx4 a[16:19], %[o0], %[rs], s40 offen offset:0 sc1\n\t" "s_mov_b32 s41, 24576\n\t" \
   "buffer_load_dwordx4 a[96:99], %[o0], %[rs], s41 offen offset:0 sc1\n\t" "s_mov_b32 s42, 45056\n\t" \
   "buffer_load_dwordx4 a[176:179], %[o1], %[rs], s42 offen offset:0 sc1\n\t" \
   "buffer_load_dwordx4 a[20:23], %[o0], %[rs], s40 offen offset:1024 sc1\n\t" \
   "buffer_load_dwordx4 a[100:103], %[o0], %[rs], s41 offen offset:1024 sc1\n\t" \
   "buffer_load_dwordx4 a[180:183], %[o1], %[rs], s42 offen offset:1024 sc1\n\t" \
   "buffer_load_dwordx4 a[24:27], %[o0], %[rs], s40 offen offset:2048 sc1\n\t" \
   "buffer_load_dwordx4 a[104:107], %[o0], %[rs], s41 offen offset:2048 sc1\n\t" \
   "buffer_load_dwordx4 a[184:187], %[o1], %[rs], s42 offen offset:2048 sc1\n\t" \
   "buffer_load_dwordx4 a[28:31], %[o0], %[rs], s40 offen offset:3072 sc1\n\t" \
   "buffer_load_dwordx4 a[108:111], %[o0], %[rs], s41 offen offset:3072 sc1\n\t" \
   "buffer_load_dwordx4 a[188:191], %[o1], %[rs], s42 offen offset:3072 sc1\n\t" "s_mov_b32 s40, 8192\n\t" \
   "buffer_load_dwordx4 a[32:35], %[o0], %[rs], s40 offen offset:0 sc1\n\t" "s_mov_b32 s41, 28672\n\t" \
   "buffer_load_dwordx4 a[112:115], %[o0], %[rs], s41 offen offset:0 sc1\n\t" "s_mov_b32 s42, 49152\n\t" \
   "buffer_load_dwordx4 a[192:195], %[o1], %[rs], s42 offen offset:0 sc1\n\t" \
   "buffer_load_dwordx4 a[36:39], %[o0], %[rs], s40 offen offset:1024 sc1\n\t" \
   "buffer_load_dwordx4 a[116:119], %[o0], %[rs], s41 offen offset:1024 sc1\n\t" \
   "buffer_load_dwordx4 a[196:199], %[o1], %[rs], s42 offen offset:1024 sc1\n\t" \
   "buffer_load_dwordx4 a[40:43], %[o0], %[rs], s40 offen offset:2048 sc1\n\t" \
   "buffer_load_dwordx4 a[120:123], %[o0], %[rs], s41 offen offset:2048 sc1\n\t" \
   "buffer_load_dwordx4 a[200:203], %[o1], %[rs], s42 offen offset:2048 sc1\n\t" \
   "buffer_load_dwordx4 a[44:47], %[o0], %[rs], s40 offen offset:3072 sc1\n\t" \
   "buffer_load_dwordx4 a[124:127], %[o0], %[rs], s41 offen offset:3072 sc1\n\t" \
   "buffer_load_dwordx4 a[204:207], %[o1], %[rs], s42 offen offset:3072 sc1\n\t" "s_mov_b32 s40, 12288\n\t" \
   "buffer_load_dwordx4 a[48:51], %[o0], %[rs], s40 offen offset:0 sc1\n\t" "s_mov_b32 s41, 32768\n\t" \
   "buffer_load_dwordx4 a[128:131], %[o0], %[rs], s41 offen offset:0 sc1\n\t" "s_mov_b32 s42, 53248\n\t" \
   "buffer_load_dwordx4 a[208:211], %[o1], %[rs], s42 offen offset:0 sc1\n\t" \
   "buffer_load_dwordx4 a[52:55], %[o0], %[rs], s40 offen offset:1024 sc1\n\t" \
   "buffer_load_dwordx4 a[132:135], %[o0], %[rs], s41 offen offset:1024 sc1\n\t" \
   "buffer_load_dwordx4 a[212:215], %[o1], %[rs], s42 offen offset:1024 sc1\n\t" \
   "buffer_load_dwordx4 a[56:59], %[o0], %[rs], s40 offen offset:2048 sc1\n\t" \
   "buffer_load_dwordx4 a[136:139], %[o0], %[rs], s41 offen offset:2048 sc1\n\t" \
   "buffer_load_dwordx4 a[216:219], %[o1], %[rs], s42 offen offset:2048 sc1\n\t" \
   "buffer_load_dwordx4 a[60:63], %[o0], %[rs], s40 offen offset:3072 sc1\n\t" \
   "buffer_load_dwordx4 a[140:143], %[o0], %[rs], s41 offen offset:3072 sc1\n\t" \
   "buffer_load_dwordx4 a[220:223], %[o1], %[rs], s42 offen offset:3072 sc1\n\t" "s_mov_b32 s40, 16384\n\t" \
   "buffer_load_dwordx4 a[64:67], %[o0], %[rs], s40 offen offset:0 sc1\n\t" "s_mov_b32 s41, 36864\n\t" \
   "buffer_load_dwordx4 a[144:147], %[o0], %[rs], s41 offen offset:0 sc1\n\t" "s_mov_b32 s42, 57344\n\t" \
   "buffer_load_dwordx4 a[224:227], %[o1], %[rs], s42 offen offset:0 sc1\n\t" \
   "buffer_load_dwordx4 a[68:71], %[o0], %[rs], s40 offen offset:1024 sc1\n\t" \
   "buffer_load_dwordx4 a[148:151], %[o0], %[rs], s41 offen offset:1024 sc1\n\t" \
   "buffer_load_dwordx4 a[228:231], %[o1], %[rs], s42 offen offset:1024 sc1\n\t" \
   "buffer_load_dwordx4 a[72:75], %[o0], %[rs], s40 offen offset:2048 sc1\n\t" \
   "buffer_load_dwordx4 a[152:155], %[o0], %[rs], s41 offen offset:2048 sc1\n\t" \
   "buffer_load_dwordx4 a[232:235], %[o1], %[rs], s42 offen offset:2048 sc1\n\t" \
   "buffer_load_dwordx4 a[76:79], %[o0], %[rs], s40 offen offset:3072 sc1\n\t" \
   "buffer_load_dwordx4 a[156:159], %[o0], %[rs], s41 offen offset:3072 sc1\n\t" \
   "buffer_load_dwordx4 a[236:239], %[o1], %[rs], s42 offen offset:3072 sc1\n\t" "s_waitcnt vmcnt(0)"
#define IM_CLOB "s40", "s41", "s42"
#define ID_ASM \
   "s_waitcnt lgkmcnt(0)\n\t" "s_barrier\n\t" "ds_read_b128 v[32:35], %[la] offset:0\n\t" "ds_read_b128 v[36:39], %[la] offset:10240\n\t" \
   "ds_read_b128 v[40:43], %[la] offset:1024\n\t" "ds_read_b128 v[44:47], %[la] offset:11264\n\t" "ds_read_b128 v[48:51], %[la] offset:2048\n\t" \
   "ds_read_b128 v[52:55], %[la] offset:12288\n\t" "ds_read_b128 v[56:59], %[la] offset:3072\n\t" "ds_read_b128 v[60:63], %[la] offset:13312\n\t" \
   "ds_read_b128 v[64:67], %[la] offset:4096\n\t" "ds_read_b128 v[68:71], %[la] offset:14336\n\t" "ds_read_b128 v[72:75], %[la] offset:5120\n\t" \
   "ds_read_b128 v[76:79], %[la] offset:15360\n\t" "ds_read_b128 v[80:83], %[la] offset:6144\n\t" "ds_read_b128 v[84:87], %[la] offset:16384\n\t" \
   "ds_read_b128 v[88:91], %[la] offset:7168\n\t" "ds_read_b128 v[92:95], %[la] offset:17408\n\t" "ds_read_b128 v[96:99], %[la] offset:8192\n\t" \
   "ds_read_b128 v[100:103], %[la] offset:18432\n\t" "ds_read_b128 v[104:107], %[la] offset:9216\n\t" "ds_read_b128 v[108:111], %[la] offset:19456\n\t" \
   "v_lshlrev_b32 v152, 16, v136\n\t" "v_and_b32 v153, 0xffff0000, v136\n\t" "v_lshlrev_b32 v154, 16, v137\n\t" "v_and_b32 v155, 0xffff0000, v137\n\t" \
   "v_lshlrev_b32 v156, 16, v140\n\t" "v_and_b32 v157, 0xffff0000, v140\n\t" "v_lshlrev_b32 v158, 16, v141\n\t" "v_and_b32 v159, 0xffff0000, v141\n\t" \
   "v_lshlrev_b32 v160, 16, v138\n\t" "v_and_b32 v161, 0xffff0000, v138\n\t" "v_lshlrev_b32 v162, 16, v139\n\t" "v_and_b32 v163, 0xffff0000, v139\n\t" \
   "s_waitcnt lgkmcnt(15)\n\t" "v_mfma_f32_16x16x32_bf16 v[112:115], a[0:3], v[32:35], 0\n\t" "v_lshlrev_b32 v164, 16, v142\n\t" \
   "v_and_b32 v165, 0xffff0000, v142\n\t" "v_mfma_f32_16x16x32_bf16 v[116:119], a[4:7], v[36:39], 0\n\t" "v_lshlrev_b32 v166, 16, v143\n\t" \
   "v_and_b32 v167, 0xffff0000, v143\n\t" "v_mfma_f32_16x16x32_bf16 v[112:115], a[8:11], v[40:43], v[112:115]\n\t" "v_lshlrev_b32 v168, 16, v144\n\t" \
   "v_and_b32 v169, 0xffff0000, v144\n\t" "v_mfma_f32_16x16x32_bf16 v[116:119], a[12:15], v[44:47], v[116:119]\n\t" "v_lshlrev_b32 v170, 16, v145\n\t" \
   "v_and_b32 v171, 0xffff0000, v145\n\t" "v_mfma_f32_16x16x32_bf16 v[112:115], a[16:19], v[48:51], v[112:115]\n\t" "v_lshlrev_b32 v172, 16, v146\n\t" \
   "v_and_b32 v173, 0xffff0000, v146\n\t" "s_waitcnt lgkmcnt(14)\n\t" "v_mfma_f32_16x16x32_bf16 v[116:119], a[20:23], v[52:55], v[116:119]\n\t" \
   "v_lshlrev_b32 v174, 16, v147\n\t" "v_and_b32 v175, 0xffff0000, v147\n\t" "s_waitcnt lgkmcnt(13)\n\t" \
   "v_mfma_f32_16x16x32_bf16 v[112:115], a[24:27], v[56:59], v[112:115]\n\t" "s_waitcnt lgkmcnt(12)\n\t" \
   "v_mfma_f32_16x16x32_bf16 v[116:119], a[28:31], v[60:63], v[116:119]\n\t" "s_waitcnt lgkmcnt(11)\n\t" \
   "v_mfma_f32_16x16x32_bf16 v[112:115], a[32:35], v[64:67], v[112:115]\n\t" "s_waitcnt lgkmcnt(10)\n\t" \
   "v_mfma_f32_16x16x32_bf16 v[116:119], a[36:39], v[68:71], v[116:119]\n\t" "s_waitcnt lgkmcnt(9)\n\t" \
   "v_mfma_f32_16x16x32_bf16 v[112:115], a[40:43], v[72:75], v[112:115]\n\t" "s_waitcnt lgkmcnt(8)\n\t" \
   "v_mfma_f32_16x16x32_bf16 v[116:119], a[44:47], v[76:79], v[116:119]\n\t" "s_waitcnt lgkmcnt(7)\n\t" \
   "v_mfma_f32_16x16x32_bf16 v[112:115], a[48:51], v[80:83], v[112:115]\n\t" "s_waitcnt lgkmcnt(6)\n\t" \
   "v_mfma_f32_16x16x32_bf16 v[116:119], a[52:55], v[84:87], v[116:119]\n\t" "s_waitcnt lgkmcnt(5)\n\t" \
   "v_mfma_f32_16x16x32_bf16 v[112:115], a[56:59], v[88:91], v[112:115]\n\t" "s_waitcnt lgkmcnt(4)\n\t" \
   "v_mfma_f32_16x16x32_bf16 v[116:119], a[60:63], v[92:95], v[116:119]\n\t" "s_waitcnt lgkmcnt(3)\n\t" \
   "v_mfma_f32_16x16x32_bf16 v[112:115], a[64:67], v[96:99], v[112:115]\n\t" "s_waitcnt lgkmcnt(2)\n\t" \
   "v_mfma_f32_16x16x32_bf16 v[116:119], a[68:71], v[100:103], v[116:119]\n\t" "s_waitcnt lgkmcnt(1)\n\t" \
   "v_mfma_f32_16x16x32_bf16 v[112:115], a[72:75], v[104:107], v[112:115]\n\t" "s_waitcnt lgkmcnt(0)\n\t" \
   "v_mfma_f32_16x16x32_bf16 v[116:119], a[76:79], v[108:111], v[116:119]\n\t" "v_mfma_f32_16x16x32_bf16 v[120:123], a[80:83], v[32:35], 0\n\t" \
   "v_mfma_f32_16x16x32_bf16 v[124:127], a[84:87], v[36:39], 0\n\t" "v_mfma_f32_16x16x32_bf16 v[120:123], a[88:91], v[40:43], v[120:123]\n\t" \
   "s_nop 1\n\t" "v_add_f32 v176, v112, v116\n\t" "v_sub_f32 v180, v112, v116\n\t" \
   "v_mfma_f32_16x16x32_bf16 v[124:127], a[92:95], v[44:47], v[124:127]\n\t" "v_add_f32 v177, v113, v117\n\t" "v_sub_f32 v181, v113, v117\n\t" \
   "v_mfma_f32_16x16x32_bf16 v[120:123], a[96:99], v[48:51], v[120:123]\n\t" "v_add_f32 v178, v114, v118\n\t" "v_sub_f32 v182, v114, v118\n\t" \
   "v_mfma_f32_16x16x32_bf16 v[124:127], a[100:103], v[52:55], v[124:127]\n\t" "v_add_f32 v179, v115, v119\n\t" "v_sub_f32 v183, v115, v119\n\t" \
   "v_mfma_f32_16x16x32_bf16 v[120:123], a[104:107], v[56:59], v[120:123]\n\t" "v_cvt_pk_bf16_f32 v184, v176, v177\n\t" \
   "v_cvt_pk_bf16_f32 v185, v178, v179\n\t" "v_mfma_f32_16x16x32_bf16 v[124:127], a[108:111], v[60:63], v[124:127]\n\t" \
   "v_cvt_pk_bf16_f32 v186, v180, v181\n\t" "v_cvt_pk_bf16_f32 v187, v182, v183\n\t" \
   "v_mfma_f32_16x16x32_bf16 v[120:123], a[112:115], v[64:67], v[120:123]\n\t" "v_lshlrev_b32 v176, 16, v184\n\t" \
   "v_and_b32 v177, 0xffff0000, v184\n\t" "v_mfma_f32_16x16x32_bf16 v[124:127], a[116:119], v[68:71], v[124:127]\n\t" \
   "v_lshlrev_b32 v178, 16, v185\n\t" "v_and_b32 v179, 0xffff0000, v185\n\t" \
   "v_mfma_f32_16x16x32_bf16 v[120:123], a[120:123], v[72:75], v[120:123]\n\t" "v_lshlrev_b32 v180, 16, v186\n\t" \
   "v_and_b32 v181, 0xffff0000, v186\n\t" "v_mfma_f32_16x16x32_bf16 v[124:127], a[124:127], v[76:79], v[124:127]\n\t" \
   "v_lshlrev_b32 v182, 16, v187\n\t" "v_and_b32 v183, 0xffff0000, v187\n\t" \
   "v_mfma_f32_16x16x32_bf16 v[120:123], a[128:131], v[80:83], v[120:123]\n\t" "v_add_f32 v176, v176, v152\n\t" "v_add_f32 v177, v177, v153\n\t" \
   "v_mfma_f32_16x16x32_bf16 v[124:127], a[132:135], v[84:87], v[124:127]\n\t" "v_add_f32 v178, v178, v154\n\t" "v_add_f32 v179, v179, v155\n\t" \
   "v_mfma_f32_16x16x32_bf16 v[120:123], a[136:139], v[88:91], v[120:123]\n\t" "v_add_f32 v180, v180, v156\n\t" "v_add_f32 v181, v181, v157\n\t" \
   "v_mfma_f32_16x16x32_bf16 v[124:127], a[140:143], v[92:95], v[124:127]\n\t" "v_add_f32 v182, v182, v158\n\t" "v_add_f32 v183, v183, v159\n\t" \
   "v_mfma_f32_16x16x32_bf16 v[120:123], a[144:147], v[96:99], v[120:123]\n\t" "v_cvt_pk_bf16_f32 v136, v176, v177\n\t" \
   "v_cvt_pk_bf16_f32 v137, v178, v179\n\t" "v_mfma_f32_16x16x32_bf16 v[124:127], a[148:151], v[100:103], v[124:127]\n\t" \
   "v_cvt_pk_bf16_f32 v140, v180, v181\n\t" "v_cvt_pk_bf16_f32 v141, v182, v183\n\t" \
   "v_mfma_f32_16x16x32_bf16 v[120:123], a[152:155], v[104:107], v[120:123]\n\t" \
   "v_mfma_f32_16x16x32_bf16 v[124:127], a[156:159], v[108:111], v[124:127]\n\t" "v_mfma_f32_16x16x32_bf16 v[128:131], a[160:163], v[32:35], 0\n\t" \
   "v_mfma_f32_16x16x32_bf16 v[132:135], a[164:167], v[36:39], 0\n\t" "v_mfma_f32_16x16x32_bf16 v[128:131], a[168:171], v[40:43], v[128:131]\n\t" \
   "s_nop 1\n\t" "v_add_f32 v176, v120, v124\n\t" "v_sub_f32 v180, v120, v124\n\t" \
   "v_mfma_f32_16x16x32_bf16 v[132:135], a[172:175], v[44:47], v[132:135]\n\t" "v_add_f32 v177, v121, v125\n\t" "v_sub_f32 v181, v121, v125\n\t" \
   "v_mfma_f32_16x16x32_bf16 v[128:131], a[176:179], v[48:51], v[128:131]\n\t" "v_add_f32 v178, v122, v126\n\t" "v_sub_f32 v182, v122, v126\n\t" \
   "v_mfma_f32_16x16x32_bf16 v[132:135], a[180:183], v[52:55], v[132:135]\n\t" "v_add_f32 v179, v123, v127\n\t" "v_sub_f32 v183, v123, v127\n\t" \
   "v_mfma_f32_16x16x32_bf16 v[128:131], a[184:187], v[56:59], v[128:131]\n\t" "v_cvt_pk_bf16_f32 v184, v176, v177\n\t" \
   "v_cvt_pk_bf16_f32 v185, v178, v179\n\t" "v_mfma_f32_16x16x32_bf16 v[132:135], a[188:191], v[60:63], v[132:135]\n\t" \
   "v_cvt_pk_bf16_f32 v186, v180, v181\n\t" "v_cvt_pk_bf16_f32 v187, v182, v183\n\t" \
   "v_mfma_f32_16x16x32_bf16 v[128:131], a[192:195], v[64:67], v[128:131]\n\t" "v_lshlrev_b32 v176, 16, v184\n\t" \
   "v_and_b32 v177, 0xffff0000, v184\n\t" "v_mfma_f32_16x16x32_bf16 v[132:135], a[196:199], v[68:71], v[132:135]\n\t" \
   "v_lshlrev_b32 v178, 16, v185\n\t" "v_and_b32 v179, 0xffff0000, v185\n\t" \
   "v_mfma_f32_16x16x32_bf16 v[128:131], a[200:203], v[72:75], v[128:131]\n\t" "v_lshlrev_b32 v180, 16, v186\n\t" \
   "v_and_b32 v181, 0xffff0000, v186\n\t" "v_mfma_f32_16x16x32_bf16 v[132:135], a[204:207], v[76:79], v[132:135]\n\t" \
   "v_lshlrev_b32 v182, 16, v187\n\t" "v_and_b32 v183, 0xffff0000, v187\n\t" \
   "v_mfma_f32_16x16x32_bf16 v[128:131], a[208:211], v[80:83], v[128:131]\n\t" "v_add_f32 v176, v176, v160\n\t" "v_add_f32 v177, v177, v161\n\t" \
   "v_mfma_f32_16x16x32_bf16 v[132:135], a[212:215], v[84:87], v[132:135]\n\t" "v_add_f32 v178, v178, v162\n\t" "v_add_f32 v179, v179, v163\n\t" \
   "v_mfma_f32_16x16x32_bf16 v[128:131], a[216:219], v[88:91], v[128:131]\n\t" "v_add_f32 v180, v180, v164\n\t" "v_add_f32 v181, v181, v165\n\t" \
   "v_mfma_f32_16x16x32_bf16 v[132:135], a[220:223], v[92:95], v[132:135]\n\t" "v_add_f32 v182, v182, v166\n\t" "v_add_f32 v183, v183, v167\n\t" \
   "v_mfma_f32_16x16x32_bf16 v[128:131], a[224:227], v[96:99], v[128:131]\n\t" "v_cvt_pk_bf16_f32 v138, v176, v177\n\t" \
   "v_cvt_pk_bf16_f32 v139, v178, v179\n\t" "v_mfma_f32_16x16x32_bf16 v[132:135], a[228:231], v[100:103], v[132:135]\n\t" \
   "v_cvt_pk_bf16_f32 v142, v180, v181\n\t" "v_cvt_pk_bf16_f32 v143, v182, v183\n\t" \
   "v_mfma_f32_16x16x32_bf16 v[128:131], a[232:235], v[104:107], v[128:131]\n\t" \
   "v_mfma_f32_16x16x32_bf16 v[132:135], a[236:239], v[108:111], v[132:135]\n\t" "s_nop 7\n\t" "v_add_f32 v176, v128, v132\n\t" \
   "v_sub_f32 v180, v128, v132\n\t" "v_add_f32 v177, v129, v133\n\t" "v_sub_f32 v181, v129, v133\n\t" "v_add_f32 v178, v130, v134\n\t" \
   "v_sub_f32 v182, v130, v134\n\t" "v_add_f32 v179, v131, v135\n\t" "v_sub_f32 v183, v131, v135\n\t" "v_cvt_pk_bf16_f32 v184, v176, v177\n\t" \
   "v_cvt_pk_bf16_f32 v185, v178, v179\n\t" "v_cvt_pk_bf16_f32 v186, v180, v181\n\t" "v_cvt_pk_bf16_f32 v187, v182, v183\n\t" \
   "v_lshlrev_b32 v176, 16, v184\n\t" "v_and_b32 v177, 0xffff0000, v184\n\t" "v_lshlrev_b32 v178, 16, v185\n\t" "v_and_b32 v179, 0xffff0000, v185\n\t" \
   "v_lshlrev_b32 v180, 16, v186\n\t" "v_and_b32 v181, 0xffff0000, v186\n\t" "v_lshlrev_b32 v182, 16, v187\n\t" "v_and_b32 v183, 0xffff0000, v187\n\t" \
   "v_add_f32 v176, v176, v168\n\t" "v_add_f32 v177, v177, v169\n\t" "v_add_f32 v178, v178, v170\n\t" "v_add_f32 v179, v179, v171\n\t" \
   "v_add_f32 v180, v180, v172\n\t" "v_add_f32 v181, v181, v173\n\t" "v_add_f32 v182, v182, v174\n\t" "v_add_f32 v183, v183, v175\n\t" \
   "v_cvt_pk_bf16_f32 v144, v176, v177\n\t" "v_cvt_pk_bf16_f32 v145, v178, v179\n\t" "v_cvt_pk_bf16_f32 v146, v180, v181\n\t" \
   "v_cvt_pk_bf16_f32 v147, v182, v183\n\t" "s_nop 1"
#define ID_CLOB "v32", "v33", "v34", "v35", "v36", "v37", "v38", "v39", "v40", "v41", "v42", "v43", "v44", "v45", "v46", "v47", "v48", "v49", "v50", "v51", "v52", "v53", "v54", "v55", "v56", "v57", "v58", "v59", "v60", "v61", "v62", "v63", "v64", "v65", "v66", "v67", "v68", "v69", "v70", "v71", "v72", "v73", "v74", "v75", "v76", "v77", "v78", "v79", "v80", "v81", "v82", "v83", "v84", "v85", "v86", "v87", "v88", "v89", "v90", "v91", "v92", "v93", "v94", "v95", "v96", "v97", "v98", "v99", "v100", "v101", "v102", "v103", "v104", "v105", "v106", "v107", "v108", "v109", "v110", "v111", "v112", "v113", "v114", "v115", "v116", "v117", "v118", "v119", "v120", "v121", "v122", "v123", "v124", "v125", "v126", "v127", "v128", "v129", "v130", "v131", "v132", "v133", "v134", "v135", "v152", "v153", "v154", "v155", "v156", "v157", "v158", "v159", "v160", "v161", "v162", "v163", "v164", "v165", "v166", "v167", "v168", "v169", "v170", "v171", "v172", "v173", "v174", "v175", "v176", "v177", "v178", "v179", "v180", "v181", "v182", "v183", "v184", "v185", "v186", "v187"
#define OU_ASM \
   "s_add_u32 s40, %[so], 0\n\t" "buffer_load_dwordx4 a[0:3], %[o], %[rs], s40 offen offset:0 sc1\n\t" \
   "buffer_load_dwordx4 a[32:35], %[o], %[rs], s40 offen offset:1024 sc1\n\t" \
   "buffer_load_dwordx4 a[64:67], %[o], %[rs], s40 offen offset:2048 sc1\n\t" \
   "buffer_load_dwordx4 a[96:99], %[o], %[rs], s40 offen offset:3072 sc1\n\t" "s_add_u32 s40, %[so], 4096\n\t" \
   "buffer_load_dwordx4 a[128:131], %[o], %[rs], s40 offen offset:0 sc1\n\t" \
   "buffer_load_dwordx4 a[160:163], %[o], %[rs], s40 offen offset:1024 sc1\n\t" \
   "buffer_load_dwordx4 a[192:195], %[o], %[rs], s40 offen offset:2048 sc1\n\t" \
   "buffer_load_dwordx4 a[224:227], %[o], %[rs], s40 offen offset:3072 sc1\n\t" "s_add_u32 s40, %[so], 32768\n\t" \
   "buffer_load_dwordx4 a[4:7], %[o], %[rs], s40 offen offset:0 sc1\n\t" "buffer_load_dwordx4 a[36:39], %[o], %[rs], s40 offen offset:1024 sc1\n\t" \
   "buffer_load_dwordx4 a[68:71], %[o], %[rs], s40 offen offset:2048 sc1\n\t" \
   "buffer_load_dwordx4 a[100:103], %[o], %[rs], s40 offen offset:3072 sc1\n\t" "s_add_u32 s40, %[so], 36864\n\t" \
   "buffer_load_dwordx4 a[132:135], %[o], %[rs], s40 offen offset:0 sc1\n\t" \
   "buffer_load_dwordx4 a[164:167], %[o], %[rs], s40 offen offset:1024 sc1\n\t" \
   "buffer_load_dwordx4 a[196:199], %[o], %[rs], s40 offen offset:2048 sc1\n\t" \
   "buffer_load_dwordx4 a[228:231], %[o], %[rs], s40 offen offset:3072 sc1\n\t" "s_add_u32 s40, %[so], 65536\n\t" \
   "buffer_load_dwordx4 a[8:11], %[o], %[rs], s40 offen offset:0 sc1\n\t" "buffer_load_dwordx4 a[40:43], %[o], %[rs], s40 offen offset:1024 sc1\n\t" \
   "buffer_load_dwordx4 a[72:75], %[o], %[rs], s40 offen offset:2048 sc1\n\t" \
   "buffer_load_dwordx4 a[104:107], %[o], %[rs], s40 offen offset:3072 sc1\n\t" "s_add_u32 s40, %[so], 69632\n\t" \
   "buffer_load_dwordx4 a[136:139], %[o], %[rs], s40 offen offset:0 sc1\n\t" \
   "buffer_load_dwordx4 a[168:171], %[o], %[rs], s40 offen offset:1024 sc1\n\t" \
   "buffer_load_dwordx4 a[200:203], %[o], %[rs], s40 offen offset:2048 sc1\n\t" \
   "buffer_load_dwordx4 a[232:235], %[o], %[rs], s40 offen offset:3072 sc1\n\t" "s_add_u32 s40, %[so], 98304\n\t" \
   "buffer_load_dwordx4 a[12:15], %[o], %[rs], s40 offen offset:0 sc1\n\t" "buffer_load_dwordx4 a[44:47], %[o], %[rs], s40 offen offset:1024 sc1\n\t" \
   "buffer_load_dwordx4 a[76:79], %[o], %[rs], s40 offen offset:2048 sc1\n\t" \
   "buffer_load_dwordx4 a[108:111], %[o], %[rs], s40 offen offset:3072 sc1\n\t" "s_add_u32 s40, %[so], 102400\n\t" \
   "buffer_load_dwordx4 a[140:143], %[o], %[rs], s40 offen offset:0 sc1\n\t" \
   "buffer_load_dwordx4 a[172:175], %[o], %[rs], s40 offen offset:1024 sc1\n\t" \
   "buffer_load_dwordx4 a[204:207], %[o], %[rs], s40 offen offset:2048 sc1\n\t" \
   "buffer_load_dwordx4 a[236:239], %[o], %[rs], s40 offen offset:3072 sc1\n\t" "s_add_u32 s40, %[so], 131072\n\t" \
   "buffer_load_dwordx4 a[16:19], %[o], %[rs], s40 offen offset:0 sc1\n\t" "buffer_load_dwordx4 a[48:51], %[o], %[rs], s40 offen offset:1024 sc1\n\t" \
   "buffer_load_dwordx4 a[80:83], %[o], %[rs], s40 offen offset:2048 sc1\n\t" \
   "buffer_load_dwordx4 a[112:115], %[o], %[rs], s40 offen offset:3072 sc1\n\t" "s_add_u32 s40, %[so], 135168\n\t" \
   "buffer_load_dwordx4 a[144:147], %[o], %[rs], s40 offen offset:0 sc1\n\t" \
   "buffer_load_dwordx4 a[176:179], %[o], %[rs], s40 offen offset:1024 sc1\n\t" \
   "buffer_load_dwordx4 a[208:211], %[o], %[rs], s40 offen offset:2048 sc1\n\t" \
   "buffer_load_dwordx4 a[240:243], %[o], %[rs], s40 offen offset:3072 sc1\n\t" "s_add_u32 s40, %[so], 163840\n\t" \
   "buffer_load_dwordx4 a[20:23], %[o], %[rs], s40 offen offset:0 sc1\n\t" "buffer_load_dwordx4 a[52:55], %[o], %[rs], s40 offen offset:1024 sc1\n\t" \
   "buffer_load_dwordx4 a[84:87], %[o], %[rs], s40 offen offset:2048 sc1\n\t" \
   "buffer_load_dwordx4 a[116:119], %[o], %[rs], s40 offen offset:3072 sc1\n\t" "s_add_u32 s40, %[so], 167936\n\t" \
   "buffer_load_dwordx4 a[148:151], %[o], %[rs], s40 offen offset:0 sc1\n\t" \
   "buffer_load_dwordx4 a[180:183], %[o], %[rs], s40 offen offset:1024 sc1\n\t" \
   "buffer_load_dwordx4 a[212:215], %[o], %[rs], s40 offen offset:2048 sc1\n\t" \
   "buffer_load_dwordx4 a[244:247], %[o], %[rs], s40 offen offset:3072 sc1\n\t" "s_add_u32 s40, %[so], 196608\n\t" \
   "buffer_load_dwordx4 a[24:27], %[o], %[rs], s40 offen offset:0 sc1\n\t" "buffer_load_dwordx4 a[56:59], %[o], %[rs], s40 offen offset:1024 sc1\n\t" \
   "buffer_load_dwordx4 a[88:91], %[o], %[rs], s40 offen offset:2048 sc1\n\t" \
   "buffer_load_dwordx4 a[120:123], %[o], %[rs], s40 offen offset:3072 sc1\n\t" "s_add_u32 s40, %[so], 200704\n\t" \
   "buffer_load_dwordx4 a[152:155], %[o], %[rs], s40 offen offset:0 sc1\n\t" \
   "buffer_load_dwordx4 a[184:187], %[o], %[rs], s40 offen offset:1024 sc1\n\t" \
   "buffer_load_dwordx4 a[216:219], %[o], %[rs], s40 offen offset:2048 sc1\n\t" \
   "buffer_load_dwordx4 a[248:251], %[o], %[rs], s40 offen offset:3072 sc1\n\t" "s_add_u32 s40, %[so], 229376\n\t" \
   "buffer_load_dwordx4 a[28:31], %[o], %[rs], s40 offen offset:0 sc1\n\t" "buffer_load_dwordx4 a[60:63], %[o], %[rs], s40 offen offset:1024 sc1\n\t" \
   "buffer_load_dwordx4 a[92:95], %[o], %[rs], s40 offen offset:2048 sc1\n\t" \
   "buffer_load_dwordx4 a[124:127], %[o], %[rs], s40 offen offset:3072 sc1\n\t" "s_add_u32 s40, %[so], 233472\n\t" \
   "buffer_load_dwordx4 a[156:159], %[o], %[rs], s40 offen offset:0 sc1\n\t" \
   "buffer_load_dwordx4 a[188:191], %[o], %[rs], s40 offen offset:1024 sc1\n\t" \
   "buffer_load_dwordx4 a[220:223], %[o], %[rs], s40 offen offset:2048 sc1\n\t" \
   "buffer_load_dwordx4 a[252:255], %[o], %[rs], s40 offen offset:3072 sc1\n\t" "s_waitcnt vmcnt(0)"
#define OU_CLOB "s40"
#define OY_ASM \
   "s_add_u32 s40, %[sy], 0\n\t" "buffer_load_dwordx4 v[192:195], %[yo], %[rs], s40 offen offset:0 sc1\n\t" "s_add_u32 s40, %[sy], 16384\n\t" \
   "buffer_load_dwordx4 v[196:199], %[yo], %[rs], s40 offen offset:0 sc1\n\t" "s_add_u32 s40, %[sy], 32768\n\t" \
   "buffer_load_dwordx4 v[200:203], %[yo], %[rs], s40 offen offset:0 sc1\n\t" "s_add_u32 s40, %[sy], 49152\n\t" \
   "buffer_load_dwordx4 v[204:207], %[yo], %[rs], s40 offen offset:0 sc1\n\t" "s_add_u32 s40, %[sy], 65536\n\t" \
   "buffer_load_dwordx4 v[208:211], %[yo], %[rs], s40 offen offset:0 sc1\n\t" "s_add_u32 s40, %[sy], 81920\n\t" \
   "buffer_load_dwordx4 v[212:215], %[yo], %[rs], s40 offen offset:0 sc1\n\t" "s_add_u32 s40, %[sy], 98304\n\t" \
   "buffer_load_dwordx4 v[216:219], %[yo], %[rs], s40 offen offset:0 sc1\n\t" "s_add_u32 s40, %[sy], 114688\n\t" \
   "buffer_load_dwordx4 v[220:223], %[yo], %[rs], s40 offen offset:0 sc1\n\t" "s_add_u32 s40, %[sy], 0\n\t" \
   "buffer_load_dwordx4 v[224:227], %[yo], %[rs], s40 offen offset:1024 sc1\n\t" "s_add_u32 s40, %[sy], 16384\n\t" \
   "buffer_load_dwordx4 v[228:231], %[yo], %[rs], s40 offen offset:1024 sc1\n\t" "s_add_u32 s40, %[sy], 32768\n\t" \
   "buffer_load_dwordx4 v[232:235], %[yo], %[rs], s40 offen offset:1024 sc1\n\t" "s_add_u32 s40, %[sy], 49152\n\t" \
   "buffer_load_dwordx4 v[236:239], %[yo], %[rs], s40 offen offset:1024 sc1\n\t" "s_add_u32 s40, %[sy], 65536\n\t" \
   "buffer_load_dwordx4 v[240:243], %[yo], %[rs], s40 offen offset:1024 sc1\n\t" "s_add_u32 s40, %[sy], 81920\n\t" \
   "buffer_load_dwordx4 v[244:247], %[yo], %[rs], s40 offen offset:1024 sc1\n\t" "s_add_u32 s40, %[sy], 98304\n\t" \
   "buffer_load_dwordx4 v[248:251], %[yo], %[rs], s40 offen offset:1024 sc1\n\t" "s_add_u32 s40, %[sy], 114688\n\t" \
   "buffer_load_dwordx4 v[252:255], %[yo], %[rs], s40 offen offset:1024 sc1\n\t" "v_mov_b32 %[m], 0\n\t" "s_waitcnt vmcnt(15)\n\t" \
   "v_max3_u32 %[m], %[m], v192, v194\n\t" "s_waitcnt vmcnt(14)\n\t" "v_max3_u32 %[m], %[m], v196, v198\n\t" "s_waitcnt vmcnt(13)\n\t" \
   "v_max3_u32 %[m], %[m], v200, v202\n\t" "s_waitcnt vmcnt(12)\n\t" "v_max3_u32 %[m], %[m], v204, v206\n\t" "s_waitcnt vmcnt(11)\n\t" \
   "v_max3_u32 %[m], %[m], v208, v210\n\t" "s_waitcnt vmcnt(10)\n\t" "v_max3_u32 %[m], %[m], v212, v214\n\t" "s_waitcnt vmcnt(9)\n\t" \
   "v_max3_u32 %[m], %[m], v216, v218\n\t" "s_waitcnt vmcnt(8)\n\t" "v_max3_u32 %[m], %[m], v220, v222\n\t" "s_waitcnt vmcnt(7)\n\t" \
   "v_max3_u32 %[m], %[m], v224, v226\n\t" "s_waitcnt vmcnt(6)\n\t" "v_max3_u32 %[m], %[m], v228, v230\n\t" "s_waitcnt vmcnt(5)\n\t" \
   "v_max3_u32 %[m], %[m], v232, v234\n\t" "s_waitcnt vmcnt(4)\n\t" "v_max3_u32 %[m], %[m], v236, v238\n\t" "s_waitcnt vmcnt(3)\n\t" \
   "v_max3_u32 %[m], %[m], v240, v242\n\t" "s_waitcnt vmcnt(2)\n\t" "v_max3_u32 %[m], %[m], v244, v246\n\t" "s_waitcnt vmcnt(1)\n\t" \
   "v_max3_u32 %[m], %[m], v248, v250\n\t" "s_waitcnt vmcnt(0)\n\t" "v_max3_u32 %[m], %[m], v252, v254\n\t" "s_nop 0"
#define OY_CLOB "s40"
#define O1_ASM \
   "s_add_u32 s40, %[sp], 0\n\t" "buffer_load_dwordx4 v[112:115], %[po], %[rs], s40 offen offset:0 sc1\n\t" \
   "buffer_load_dwordx4 v[116:119], %[po], %[rs], s40 offen offset:2048 sc1\n\t" "s_add_u32 s40, %[sp], 4096\n\t" \
   "buffer_load_dwordx4 v[120:123], %[po], %[rs], s40 offen offset:0 sc1\n\t" \
   "buffer_load_dwordx4 v[124:127], %[po], %[rs], s40 offen offset:2048 sc1\n\t" "s_add_u32 s40, %[sp], 8192\n\t" \
   "buffer_load_dwordx4 v[128:131], %[po], %[rs], s40 offen offset:0 sc1\n\t" \
   "buffer_load_dwordx4 v[132:135], %[po], %[rs], s40 offen offset:2048 sc1\n\t" "s_add_u32 s40, %[sp], 12288\n\t" \
   "buffer_load_dwordx4 v[136:139], %[po], %[rs], s40 offen offset:0 sc1\n\t" \
   "buffer_load_dwordx4 v[140:143], %[po], %[rs], s40 offen offset:2048 sc1\n\t" \
   "buffer_load_dwordx4 v[144:147], %[mo], %[rs], %[sm] offen offset:0 sc1\n\t" \
   "buffer_load_dwordx4 v[148:151], %[mo], %[rs], %[sm] offen offset:2048 sc1\n\t" "s_waitcnt vmcnt(9)\n\t" \
   "v_mfma_f32_16x16x32_bf16 v[32:35], a[0:3], v[112:115], 0\n\t" "v_mfma_f32_16x16x32_bf16 v[36:39], a[4:7], v[112:115], 0\n\t" \
   "v_mfma_f32_16x16x32_bf16 v[40:43], a[8:11], v[112:115], 0\n\t" "v_mfma_f32_16x16x32_bf16 v[44:47], a[12:15], v[112:115], 0\n\t" \
   "v_mfma_f32_16x16x32_bf16 v[48:51], a[16:19], v[112:115], 0\n\t" "v_mfma_f32_16x16x32_bf16 v[52:55], a[20:23], v[112:115], 0\n\t" \
   "v_mfma_f32_16x16x32_bf16 v[56:59], a[24:27], v[112:115], 0\n\t" "v_mfma_f32_16x16x32_bf16 v[60:63], a[28:31], v[112:115], 0\n\t" \
   "s_waitcnt vmcnt(8)\n\t" "v_mfma_f32_16x16x32_bf16 v[32:35], a[32:35], v[116:119], v[32:35]\n\t" \
   "v_mfma_f32_16x16x32_bf16 v[36:39], a[36:39], v[116:119], v[36:39]\n\t" "v_mfma_f32_16x16x32_bf16 v[40:43], a[40:43], v[116:119], v[40:43]\n\t" \
   "v_mfma_f32_16x16x32_bf16 v[44:47], a[44:47], v[116:119], v[44:47]\n\t" "v_mfma_f32_16x16x32_bf16 v[48:51], a[48:51], v[116:119], v[48:51]\n\t" \
   "v_mfma_f32_16x16x32_bf16 v[52:55], a[52:55], v[116:119], v[52:55]\n\t" "v_mfma_f32_16x16x32_bf16 v[56:59], a[56:59], v[116:119], v[56:59]\n\t" \
   "v_mfma_f32_16x16x32_bf16 v[60:63], a[60:63], v[116:119], v[60:63]\n\t" "s_waitcnt vmcnt(7)\n\t" \
   "v_mfma_f32_16x16x32_bf16 v[32:35], a[64:67], v[120:123], v[32:35]\n\t" "v_mfma_f32_16x16x32_bf16 v[36:39], a[68:71], v[120:123], v[36:39]\n\t" \
   "v_mfma_f32_16x16x32_bf16 v[40:43], a[72:75], v[120:123], v[40:43]\n\t" "v_mfma_f32_16x16x32_bf16 v[44:47], a[76:79], v[120:123], v[44:47]\n\t" \
   "v_mfma_f32_16x16x32_bf16 v[48:51], a[80:83], v[120:123], v[48:51]\n\t" "v_mfma_f32_16x16x32_bf16 v[52:55], a[84:87], v[120:123], v[52:55]\n\t" \
   "v_mfma_f32_16x16x32_bf16 v[56:59], a[88:91], v[120:123], v[56:59]\n\t" "v_mfma_f32_16x16x32_bf16 v[60:63], a[92:95], v[120:123], v[60:63]\n\t" \
   "s_waitcnt vmcnt(6)\n\t" "v_mfma_f32_16x16x32_bf16 v[32:35], a[96:99], v[124:127], v[32:35]\n\t" \
   "v_mfma_f32_16x16x32_bf16 v[36:39], a[100:103], v[124:127], v[36:39]\n\t" "v_mfma_f32_16x16x32_bf16 v[40:43], a[104:107], v[124:127], v[40:43]\n\t" \
   "v_mfma_f32_16x16x32_bf16 v[44:47], a[108:111], v[124:127], v[44:47]\n\t" "v_mfma_f32_16x16x32_bf16 v[48:51], a[112:115], v[124:127], v[48:51]\n\t" \
   "v_mfma_f32_16x16x32_bf16 v[52:55], a[116:119], v[124:127], v[52:55]\n\t" "v_mfma_f32_16x16x32_bf16 v[56:59], a[120:123], v[124:127], v[56:59]\n\t" \
   "v_mfma_f32_16x16x32_bf16 v[60:63], a[124:127], v[124:127], v[60:63]\n\t" "s_waitcnt vmcnt(5)\n\t" \
   "v_mfma_f32_16x16x32_bf16 v[32:35], a[128:131], v[128:131], v[32:35]\n\t" "v_mfma_f32_16x16x32_bf16 v[36:39], a[132:135], v[128:131], v[36:39]\n\t" \
   "v_mfma_f32_16x16x32_bf16 v[40:43], a[136:139], v[128:131], v[40:43]\n\t" "v_mfma_f32_16x16x32_bf16 v[44:47], a[140:143], v[128:131], v[44:47]\n\t" \
   "v_mfma_f32_16x16x32_bf16 v[48:51], a[144:147], v[128:131], v[48:51]\n\t" "v_mfma_f32_16x16x32_bf16 v[52:55], a[148:151], v[128:131], v[52:55]\n\t" \
   "v_mfma_f32_16x16x32_bf16 v[56:59], a[152:155], v[128:131], v[56:59]\n\t" "v_mfma_f32_16x16x32_bf16 v[60:63], a[156:159], v[128:131], v[60:63]\n\t" \
   "s_waitcnt vmcnt(4)\n\t" "v_mfma_f32_16x16x32_bf16 v[32:35], a[160:163], v[132:135], v[32:35]\n\t" \
   "v_mfma_f32_16x16x32_bf16 v[36:39], a[164:167], v[132:135], v[36:39]\n\t" "v_mfma_f32_16x16x32_bf16 v[40:43], a[168:171], v[132:135], v[40:43]\n\t" \
   "v_mfma_f32_16x16x32_bf16 v[44:47], a[172:175], v[132:135], v[44:47]\n\t" "v_mfma_f32_16x16x32_bf16 v[48:51], a[176:179], v[132:135], v[48:51]\n\t" \
   "v_mfma_f32_16x16x32_bf16 v[52:55], a[180:183], v[132:135], v[52:55]\n\t" "v_mfma_f32_16x16x32_bf16 v[56:59], a[184:187], v[132:135], v[56:59]\n\t" \
   "v_mfma_f32_16x16x32_bf16 v[60:63], a[188:191], v[132:135], v[60:63]\n\t" "s_waitcnt vmcnt(3)\n\t" \
   "v_mfma_f32_16x16x32_bf16 v[32:35], a[192:195], v[136:139], v[32:35]\n\t" "v_mfma_f32_16x16x32_bf16 v[36:39], a[196:199], v[136:139], v[36:39]\n\t" \
   "v_mfma_f32_16x16x32_bf16 v[40:43], a[200:203], v[136:139], v[40:43]\n\t" "v_mfma_f32_16x16x32_bf16 v[44:47], a[204:207], v[136:139], v[44:47]\n\t" \
   "v_mfma_f32_16x16x32_bf16 v[48:51], a[208:211], v[136:139], v[48:51]\n\t" "v_mfma_f32_16x16x32_bf16 v[52:55], a[212:215], v[136:139], v[52:55]\n\t" \
   "v_mfma_f32_16x16x32_bf16 v[56:59], a[216:219], v[136:139], v[56:59]\n\t" "v_mfma_f32_16x16x32_bf16 v[60:63], a[220:223], v[136:139], v[60:63]\n\t" \
   "s_waitcnt vmcnt(2)\n\t" "s_waitcnt vmcnt(0)\n\t" "v_mfma_f32_16x16x32_bf16 v[32:35], a[224:227], v[140:143], v[32:35]\n\t" \
   "v_swap_b32 v145, v146\n\t" "v_mfma_f32_16x16x32_bf16 v[36:39], a[228:231], v[140:143], v[36:39]\n\t" "v_swap_b32 v149, v150\n\t" \
   "v_mfma_f32_16x16x32_bf16 v[40:43], a[232:235], v[140:143], v[40:43]\n\t" "v_mfma_f32_16x16x32_bf16 v[44:47], a[236:239], v[140:143], v[44:47]\n\t" \
   "v_mfma_f32_16x16x32_bf16 v[48:51], a[240:243], v[140:143], v[48:51]\n\t" "v_mfma_f32_16x16x32_bf16 v[52:55], a[244:247], v[140:143], v[52:55]\n\t" \
   "v_mfma_f32_16x16x32_bf16 v[56:59], a[248:251], v[140:143], v[56:59]\n\t" "v_mfma_f32_16x16x32_bf16 v[60:63], a[252:255], v[140:143], v[60:63]\n\t" \
   "v_mfma_f32_16x16x32_bf16 v[32:35], v[192:195], v[144:147], v[32:35]\n\t" "v_mfma_f32_16x16x32_bf16 v[36:39], v[196:199], v[144:147], v[36:39]\n\t" \
   "v_mfma_f32_16x16x32_bf16 v[40:43], v[200:203], v[144:147], v[40:43]\n\t" "v_mfma_f32_16x16x32_bf16 v[44:47], v[204:207], v[144:147], v[44:47]\n\t" \
   "v_mfma_f32_16x16x32_bf16 v[48:51], v[208:211], v[144:147], v[48:51]\n\t" "v_mfma_f32_16x16x32_bf16 v[52:55], v[212:215], v[144:147], v[52:55]\n\t" \
   "v_mfma_f32_16x16x32_bf16 v[56:59], v[216:219], v[144:147], v[56:59]\n\t" "v_mfma_f32_16x16x32_bf16 v[60:63], v[220:223], v[144:147], v[60:63]\n\t" \
   "v_mfma_f32_16x16x32_bf16 v[32:35], v[224:227], v[148:151], v[32:35]\n\t" "v_mfma_f32_16x16x32_bf16 v[36:39], v[228:231], v[148:151], v[36:39]\n\t" \
   "v_mfma_f32_16x16x32_bf16 v[40:43], v[232:235], v[148:151], v[40:43]\n\t" "v_mfma_f32_16x16x32_bf16 v[44:47], v[236:239], v[148:151], v[44:47]\n\t" \
   "ds_write_b128 %[lw], v[32:35] offset:0\n\t" "v_mfma_f32_16x16x32_bf16 v[48:51], v[240:243], v[148:151], v[48:51]\n\t" \
   "ds_write_b128 %[lw], v[36:39] offset:1024\n\t" "v_mfma_f32_16x16x32_bf16 v[52:55], v[244:247], v[148:151], v[52:55]\n\t" \
   "ds_write_b128 %[lw], v[40:43] offset:2048\n\t" "v_mfma_f32_16x16x32_bf16 v[56:59], v[248:251], v[148:151], v[56:59]\n\t" \
   "ds_write_b128 %[lw], v[44:47] offset:3072\n\t" "v_mfma_f32_16x16x32_bf16 v[60:63], v[252:255], v[148:151], v[60:63]\n\t" \
   "ds_write_b128 %[lw], v[48:51] offset:4096\n\t" "s_nop 1\n\t" "s_nop 3\n\t" "ds_write_b128 %[lw], v[52:55] offset:5120\n\t" "s_nop 3\n\t" \
   "ds_write_b128 %[lw], v[56:59] offset:6144\n\t" "s_nop 3\n\t" "ds_write_b128 %[lw], v[60:63] offset:7168\n\t" "s_waitcnt lgkmcnt(0)\n\t" \
   "s_barrier\n\t" "ds_read_b128 v[112:115], %[lr] offset:0\n\t" "ds_read_b128 v[116:119], %[lr] offset:256\n\t" \
   "ds_read_b128 v[120:123], %[lr] offset:8192\n\t" "ds_read_b128 v[124:127], %[lr] offset:8448\n\t" "ds_read_b128 v[128:131], %[lr] offset:16384\n\t" \
   "ds_read_b128 v[132:135], %[lr] offset:16640\n\t" "ds_read_b128 v[136:139], %[lr] offset:24576\n\t" \
   "ds_read_b128 v[140:143], %[lr] offset:24832\n\t" "s_waitcnt lgkmcnt(4)\n\t" "v_add_f32 v112, v112, v120\n\t" "v_add_f32 v113, v113, v121\n\t" \
   "v_add_f32 v114, v114, v122\n\t" "v_add_f32 v115, v115, v123\n\t" "v_add_f32 v116, v116, v124\n\t" "v_add_f32 v117, v117, v125\n\t" \
   "v_add_f32 v118, v118, v126\n\t" "v_add_f32 v119, v119, v127\n\t" "s_waitcnt lgkmcnt(0)\n\t" "v_add_f32 v128, v128, v136\n\t" \
   "v_add_f32 v129, v129, v137\n\t" "v_add_f32 v130, v130, v138\n\t" "v_add_f32 v131, v131, v139\n\t" "v_add_f32 v132, v132, v140\n\t" \
   "v_add_f32 v133, v133, v141\n\t" "v_add_f32 v134, v134, v142\n\t" "v_add_f32 v135, v135, v143\n\t" "v_add_f32 v112, v112, v128\n\t" \
   "v_add_f32 v113, v113, v129\n\t" "v_add_f32 v114, v114, v130\n\t" "v_add_f32 v115, v115, v131\n\t" "v_add_f32 v116, v116, v132\n\t" \
   "v_add_f32 v117, v117, v133\n\t" "v_add_f32 v118, v118, v134\n\t" "v_add_f32 v119, v119, v135\n\t" "v_cvt_pk_bf16_f32 v96, v112, v113\n\t" \
   "v_cvt_pk_bf16_f32 v97, v114, v115\n\t" "v_cvt_pk_bf16_f32 v98, v116, v117\n\t" "v_cvt_pk_bf16_f32 v99, v118, v119\n\t" "s_nop 1"
#define O1_CLOB "v32", "v33", "v34", "v35", "v36", "v37", "v38", "v39", "v40", "v41", "v42", "v43", "v44", "v45", "v46", "v47", "v48", "v49", "v50", "v51", "v52", "v53", "v54", "v55", "v56", "v57", "v58", "v59", "v60", "v61", "v62", "v63", "v64", "v65", "v66", "v67", "v68", "v69", "v70", "v71", "v72", "v73", "v74", "v75", "v76", "v77", "v78", "v79", "v80", "v81", "v82", "v83", "v84", "v85", "v86", "v87", "v88", "v89", "v90", "v91", "v92", "v93", "v94", "v95", "v112", "v113", "v114", "v115", "v116", "v117", "v118", "v119", "v120", "v121", "v122", "v123", "v124", "v125", "v126", "v127", "v128", "v129", "v130", "v131", "v132", "v133", "v134", "v135", "v136", "v137", "v138", "v139", "v140", "v141", "v142", "v143", "v144", "v145", "v146", "v147", "v148", "v149", "v150", "v151", "v152", "v153", "v154", "v155", "v156", "v157", "v158", "v159", "v160", "v161", "v162", "v163", "v164", "v165", "v166", "v167", "v168", "v169", "v170", "v171", "v172", "v173", "v174", "v175", "v176", "v177", "v178", "v179", "v180", "v181", "v182", "v183", "v184", "v185", "v186", "v187", "v188", "v189", "v190", "v191", "s40"
#define O2_ASM \
   "s_add_u32 s40, %[sp], 0\n\t" "buffer_load_dwordx4 v[112:115], %[po], %[rs], s40 offen offset:0 sc1\n\t" \
   "buffer_load_dwordx4 v[116:119], %[po], %[rs], s40 offen offset:2048 sc1\n\t" "s_add_u32 s40, %[sp], 4096\n\t" \
   "buffer_load_dwordx4 v[120:123], %[po], %[rs], s40 offen offset:0 sc1\n\t" \
   "buffer_load_dwordx4 v[124:127], %[po], %[rs], s40 offen offset:2048 sc1\n\t" "s_add_u32 s40, %[sp], 8192\n\t" \
   "buffer_load_dwordx4 v[128:131], %[po], %[rs], s40 offen offset:0 sc1\n\t" \
   "buffer_load_dwordx4 v[132:135], %[po], %[rs], s40 offen offset:2048 sc1\n\t" "s_add_u32 s40, %[sp], 12288\n\t" \
   "buffer_load_dwordx4 v[136:139], %[po], %[rs], s40 offen offset:0 sc1\n\t" \
   "buffer_load_dwordx4 v[140:143], %[po], %[rs], s40 offen offset:2048 sc1\n\t" "s_add_u32 s40, %[sp], 0\n\t" \
   "buffer_load_dwordx4 v[152:155], %[po], %[rs], s40 offen offset:1024 sc1\n\t" \
   "buffer_load_dwordx4 v[156:159], %[po], %[rs], s40 offen offset:3072 sc1\n\t" "s_add_u32 s40, %[sp], 4096\n\t" \
   "buffer_load_dwordx4 v[160:163], %[po], %[rs], s40 offen offset:1024 sc1\n\t" \
   "buffer_load_dwordx4 v[164:167], %[po], %[rs], s40 offen offset:3072 sc1\n\t" "s_add_u32 s40, %[sp], 8192\n\t" \
   "buffer_load_dwordx4 v[168:171], %[po], %[rs], s40 offen offset:1024 sc1\n\t" \
   "buffer_load_dwordx4 v[172:175], %[po], %[rs], s40 offen offset:3072 sc1\n\t" "s_add_u32 s40, %[sp], 12288\n\t" \
   "buffer_load_dwordx4 v[176:179], %[po], %[rs], s40 offen offset:1024 sc1\n\t" \
   "buffer_load_dwordx4 v[180:183], %[po], %[rs], s40 offen offset:3072 sc1\n\t" \
   "buffer_load_dwordx4 v[144:147], %[mo], %[rs], %[sm] offen offset:0 sc1\n\t" \
   "buffer_load_dwordx4 v[148:151], %[mo], %[rs], %[sm] offen offset:2048 sc1\n\t" \
   "buffer_load_dwordx4 v[184:187], %[mo], %[rs], %[sm] offen offset:1024 sc1\n\t" \
   "buffer_load_dwordx4 v[188:191], %[mo], %[rs], %[sm] offen offset:3072 sc1\n\t" "s_add_u32 s40, %[sy], 0\n\t" \
   "buffer_load_dwordx4 v[192:195], %[yo], %[rs], s40 offen offset:0 sc1\n\t" "s_add_u32 s40, %[sy], 16384\n\t" \
   "buffer_load_dwordx4 v[196:199], %[yo], %[rs], s40 offen offset:0 sc1\n\t" "s_add_u32 s40, %[sy], 32768\n\t" \
   "buffer_load_dwordx4 v[200:203], %[yo], %[rs], s40 offen offset:0 sc1\n\t" "s_add_u32 s40, %[sy], 49152\n\t" \
   "buffer_load_dwordx4 v[204:207], %[yo], %[rs], s40 offen offset:0 sc1\n\t" "s_add_u32 s40, %[sy], 65536\n\t" \
   "buffer_load_dwordx4 v[208:211], %[yo], %[rs], s40 offen offset:0 sc1\n\t" "s_add_u32 s40, %[sy], 81920\n\t" \
   "buffer_load_dwordx4 v[212:215], %[yo], %[rs], s40 offen offset:0 sc1\n\t" "s_add_u32 s40, %[sy], 98304\n\t" \
   "buffer_load_dwordx4 v[216:219], %[yo], %[rs], s40 offen offset:0 sc1\n\t" "s_add_u32 s40, %[sy], 114688\n\t" \
   "buffer_load_dwordx4 v[220:223], %[yo], %[rs], s40 offen offset:0 sc1\n\t" "s_add_u32 s40, %[sy], 0\n\t" \
   "buffer_load_dwordx4 v[224:227], %[yo], %[rs], s40 offen offset:1024 sc1\n\t" "s_add_u32 s40, %[sy], 16384\n\t" \
   "buffer_load_dwordx4 v[228:231], %[yo], %[rs], s40 offen offset:1024 sc1\n\t" "s_add_u32 s40, %[sy], 32768\n\t" \
   "buffer_load_dwordx4 v[232:235], %[yo], %[rs], s40 offen offset:1024 sc1\n\t" "s_add_u32 s40, %[sy], 49152\n\t" \
   "buffer_load_dwordx4 v[236:239], %[yo], %[rs], s40 offen offset:1024 sc1\n\t" "s_add_u32 s40, %[sy], 65536\n\t" \
   "buffer_load_dwordx4 v[240:243], %[yo], %[rs], s40 offen offset:1024 sc1\n\t" "s_add_u32 s40, %[sy], 81920\n\t" \
   "buffer_load_dwordx4 v[244:247], %[yo], %[rs], s40 offen offset:1024 sc1\n\t" "s_add_u32 s40, %[sy], 98304\n\t" \
   "buffer_load_dwordx4 v[248:251], %[yo], %[rs], s40 offen offset:1024 sc1\n\t" "s_add_u32 s40, %[sy], 114688\n\t" \
   "buffer_load_dwordx4 v[252:255], %[yo], %[rs], s40 offen offset:1024 sc1\n\t" "s_waitcnt vmcnt(35)\n\t" \
   "v_mfma_f32_16x16x32_bf16 v[32:35], a[0:3], v[112:115], 0\n\t" "v_mfma_f32_16x16x32_bf16 v[36:39], a[4:7], v[112:115], 0\n\t" \
   "v_mfma_f32_16x16x32_bf16 v[40:43], a[8:11], v[112:115], 0\n\t" "v_mfma_f32_16x16x32_bf16 v[44:47], a[12:15], v[112:115], 0\n\t" \
   "v_mfma_f32_16x16x32_bf16 v[48:51], a[16:19], v[112:115], 0\n\t" "v_mfma_f32_16x16x32_bf16 v[52:55], a[20:23], v[112:115], 0\n\t" \
   "v_mfma_f32_16x16x32_bf16 v[56:59], a[24:27], v[112:115], 0\n\t" "v_mfma_f32_16x16x32_bf16 v[60:63], a[28:31], v[112:115], 0\n\t" \
   "s_waitcnt vmcnt(34)\n\t" "v_mfma_f32_16x16x32_bf16 v[32:35], a[32:35], v[116:119], v[32:35]\n\t" \
   "v_mfma_f32_16x16x32_bf16 v[36:39], a[36:39], v[116:119], v[36:39]\n\t" "v_mfma_f32_16x16x32_bf16 v[40:43], a[40:43], v[116:119], v[40:43]\n\t" \
   "v_mfma_f32_16x16x32_bf16 v[44:47], a[44:47], v[116:119], v[44:47]\n\t" "v_mfma_f32_16x16x32_bf16 v[48:51], a[48:51], v[116:119], v[48:51]\n\t" \
   "v_mfma_f32_16x16x32_bf16 v[52:55], a[52:55], v[116:119], v[52:55]\n\t" "v_mfma_f32_16x16x32_bf16 v[56:59], a[56:59], v[116:119], v[56:59]\n\t" \
   "v_mfma_f32_16x16x32_bf16 v[60:63], a[60:63], v[116:119], v[60:63]\n\t" "s_waitcnt vmcnt(33)\n\t" \
   "v_mfma_f32_16x16x32_bf16 v[32:35], a[64:67], v[120:123], v[32:35]\n\t" "v_mfma_f32_16x16x32_bf16 v[36:39], a[68:71], v[120:123], v[36:39]\n\t" \
   "v_mfma_f32_16x16x32_bf16 v[40:43], a[72:75], v[120:123], v[40:43]\n\t" "v_mfma_f32_16x16x32_bf16 v[44:47], a[76:79], v[120:123], v[44:47]\n\t" \
   "v_mfma_f32_16x16x32_bf16 v[48:51], a[80:83], v[120:123], v[48:51]\n\t" "v_mfma_f32_16x16x32_bf16 v[52:55], a[84:87], v[120:123], v[52:55]\n\t" \
   "v_mfma_f32_16x16x32_bf16 v[56:59], a[88:91], v[120:123], v[56:59]\n\t" "v_mfma_f32_16x16x32_bf16 v[60:63], a[92:95], v[120:123], v[60:63]\n\t" \
   "s_waitcnt vmcnt(32)\n\t" "v_mfma_f32_16x16x32_bf16 v[32:35], a[96:99], v[124:127], v[32:35]\n\t" \
   "v_mfma_f32_16x16x32_bf16 v[36:39], a[100:103], v[124:127], v[36:39]\n\t" "v_mfma_f32_16x16x32_bf16 v[40:43], a[104:107], v[124:127], v[40:43]\n\t" \
   "v_mfma_f32_16x16x32_bf16 v[44:47], a[108:111], v[124:127], v[44:47]\n\t" "v_mfma_f32_16x16x32_bf16 v[48:51], a[112:115], v[124:127], v[48:51]\n\t" \
   "v_mfma_f32_16x16x32_bf16 v[52:55], a[116:119], v[124:127], v[52:55]\n\t" "v_mfma_f32_16x16x32_bf16 v[56:59], a[120:123], v[124:127], v[56:59]\n\t" \
   "v_mfma_f32_16x16x32_bf16 v[60:63], a[124:127], v[124:127], v[60:63]\n\t" "s_waitcnt vmcnt(31)\n\t" \
   "v_mfma_f32_16x16x32_bf16 v[32:35], a[128:131], v[128:131], v[32:35]\n\t" "v_mfma_f32_16x16x32_bf16 v[36:39], a[132:135], v[128:131], v[36:39]\n\t" \
   "v_mfma_f32_16x16x32_bf16 v[40:43], a[136:139], v[128:131], v[40:43]\n\t" "v_mfma_f32_16x16x32_bf16 v[44:47], a[140:143], v[128:131], v[44:47]\n\t" \
   "v_mfma_f32_16x16x32_bf16 v[48:51], a[144:147], v[128:131], v[48:51]\n\t" "v_mfma_f32_16x16x32_bf16 v[52:55], a[148:151], v[128:131], v[52:55]\n\t" \
   "v_mfma_f32_16x16x32_bf16 v[56:59], a[152:155], v[128:131], v[56:59]\n\t" "v_mfma_f32_16x16x32_bf16 v[60:63], a[156:159], v[128:131], v[60:63]\n\t" \
   "s_waitcnt vmcnt(30)\n\t" "v_mfma_f32_16x16x32_bf16 v[32:35], a[160:163], v[132:135], v[32:35]\n\t" \
   "v_mfma_f32_16x16x32_bf16 v[36:39], a[164:167], v[132:135], v[36:39]\n\t" "v_mfma_f32_16x16x32_bf16 v[40:43], a[168:171], v[132:135], v[40:43]\n\t" \
   "v_mfma_f32_16x16x32_bf16 v[44:47], a[172:175], v[132:135], v[44:47]\n\t" "v_mfma_f32_16x16x32_bf16 v[48:51], a[176:179], v[132:135], v[48:51]\n\t" \
   "v_mfma_f32_16x16x32_bf16 v[52:55], a[180:183], v[132:135], v[52:55]\n\t" "v_mfma_f32_16x16x32_bf16 v[56:59], a[184:187], v[132:135], v[56:59]\n\t" \
   "v_mfma_f32_16x16x32_bf16 v[60:63], a[188:191], v[132:135], v[60:63]\n\t" "s_waitcnt vmcnt(29)\n\t" \
   "v_mfma_f32_16x16x32_bf16 v[32:35], a[192:195], v[136:139], v[32:35]\n\t" "v_mfma_f32_16x16x32_bf16 v[36:39], a[196:199], v[136:139], v[36:39]\n\t" \
   "v_mfma_f32_16x16x32_bf16 v[40:43], a[200:203], v[136:139], v[40:43]\n\t" "v_mfma_f32_16x16x32_bf16 v[44:47], a[204:207], v[136:139], v[44:47]\n\t" \
   "v_mfma_f32_16x16x32_bf16 v[48:51], a[208:211], v[136:139], v[48:51]\n\t" "v_mfma_f32_16x16x32_bf16 v[52:55], a[212:215], v[136:139], v[52:55]\n\t" \
   "v_mfma_f32_16x16x32_bf16 v[56:59], a[216:219], v[136:139], v[56:59]\n\t" "v_mfma_f32_16x16x32_bf16 v[60:63], a[220:223], v[136:139], v[60:63]\n\t" \
   "s_waitcnt vmcnt(28)\n\t" "v_mfma_f32_16x16x32_bf16 v[32:35], a[224:227], v[140:143], v[32:35]\n\t" \
   "v_mfma_f32_16x16x32_bf16 v[36:39], a[228:231], v[140:143], v[36:39]\n\t" "v_mfma_f32_16x16x32_bf16 v[40:43], a[232:235], v[140:143], v[40:43]\n\t" \
   "v_mfma_f32_16x16x32_bf16 v[44:47], a[236:239], v[140:143], v[44:47]\n\t" "v_mfma_f32_16x16x32_bf16 v[48:51], a[240:243], v[140:143], v[48:51]\n\t" \
   "v_mfma_f32_16x16x32_bf16 v[52:55], a[244:247], v[140:143], v[52:55]\n\t" "v_mfma_f32_16x16x32_bf16 v[56:59], a[248:251], v[140:143], v[56:59]\n\t" \
   "v_mfma_f32_16x16x32_bf16 v[60:63], a[252:255], v[140:143], v[60:63]\n\t" "s_waitcnt vmcnt(27)\n\t" \
   "v_mfma_f32_16x16x32_bf16 v[64:67], a[0:3], v[152:155], 0\n\t" "v_mfma_f32_16x16x32_bf16 v[68:71], a[4:7], v[152:155], 0\n\t" \
   "v_mfma_f32_16x16x32_bf16 v[72:75], a[8:11], v[152:155], 0\n\t" "v_mfma_f32_16x16x32_bf16 v[76:79], a[12:15], v[152:155], 0\n\t" \
   "v_mfma_f32_16x16x32_bf16 v[80:83], a[16:19], v[152:155], 0\n\t" "v_mfma_f32_16x16x32_bf16 v[84:87], a[20:23], v[152:155], 0\n\t" \
   "v_mfma_f32_16x16x32_bf16 v[88:91], a[24:27], v[152:155], 0\n\t" "v_mfma_f32_16x16x32_bf16 v[92:95], a[28:31], v[152:155], 0\n\t" \
   "s_waitcnt vmcnt(26)\n\t" "v_mfma_f32_16x16x32_bf16 v[64:67], a[32:35], v[156:159], v[64:67]\n\t" \
   "v_mfma_f32_16x16x32_bf16 v[68:71], a[36:39], v[156:159], v[68:71]\n\t" "v_mfma_f32_16x16x32_bf16 v[72:75], a[40:43], v[156:159], v[72:75]\n\t" \
   "v_mfma_f32_16x16x32_bf16 v[76:79], a[44:47], v[156:159], v[76:79]\n\t" "v_mfma_f32_16x16x32_bf16 v[80:83], a[48:51], v[156:159], v[80:83]\n\t" \
   "v_mfma_f32_16x16x32_bf16 v[84:87], a[52:55], v[156:159], v[84:87]\n\t" "v_mfma_f32_16x16x32_bf16 v[88:91], a[56:59], v[156:159], v[88:91]\n\t" \
   "v_mfma_f32_16x16x32_bf16 v[92:95], a[60:63], v[156:159], v[92:95]\n\t" "s_waitcnt vmcnt(25)\n\t" \
   "v_mfma_f32_16x16x32_bf16 v[64:67], a[64:67], v[160:163], v[64:67]\n\t" "v_mfma_f32_16x16x32_bf16 v[68:71], a[68:71], v[160:163], v[68:71]\n\t" \
   "v_mfma_f32_16x16x32_bf16 v[72:75], a[72:75], v[160:163], v[72:75]\n\t" "v_mfma_f32_16x16x32_bf16 v[76:79], a[76:79], v[160:163], v[76:79]\n\t" \
   "v_mfma_f32_16x16x32_bf16 v[80:83], a[80:83], v[160:163], v[80:83]\n\t" "v_mfma_f32_16x16x32_bf16 v[84:87], a[84:87], v[160:163], v[84:87]\n\t" \
   "v_mfma_f32_16x16x32_bf16 v[88:91], a[88:91], v[160:163], v[88:91]\n\t" "v_mfma_f32_16x16x32_bf16 v[92:95], a[92:95], v[160:163], v[92:95]\n\t" \
   "s_waitcnt vmcnt(24)\n\t" "v_mfma_f32_16x16x32_bf16 v[64:67], a[96:99], v[164:167], v[64:67]\n\t" \
   "v_mfma_f32_16x16x32_bf16 v[68:71], a[100:103], v[164:167], v[68:71]\n\t" "v_mfma_f32_16x16x32_bf16 v[72:75], a[104:107], v[164:167], v[72:75]\n\t" \
   "v_mfma_f32_16x16x32_bf16 v[76:79], a[108:111], v[164:167], v[76:79]\n\t" "v_mfma_f32_16x16x32_bf16 v[80:83], a[112:115], v[164:167], v[80:83]\n\t" \
   "v_mfma_f32_16x16x32_bf16 v[84:87], a[116:119], v[164:167], v[84:87]\n\t" "v_mfma_f32_16x16x32_bf16 v[88:91], a[120:123], v[164:167], v[88:91]\n\t" \
   "v_mfma_f32_16x16x32_bf16 v[92:95], a[124:127], v[164:167], v[92:95]\n\t" "s_waitcnt vmcnt(23)\n\t" \
   "v_mfma_f32_16x16x32_bf16 v[64:67], a[128:131], v[168:171], v[64:67]\n\t" "v_mfma_f32_16x16x32_bf16 v[68:71], a[132:135], v[168:171], v[68:71]\n\t" \
   "v_mfma_f32_16x16x32_bf16 v[72:75], a[136:139], v[168:171], v[72:75]\n\t" "v_mfma_f32_16x16x32_bf16 v[76:79], a[140:143], v[168:171], v[76:79]\n\t" \
   "v_mfma_f32_16x16x32_bf16 v[80:83], a[144:147], v[168:171], v[80:83]\n\t" "v_mfma_f32_16x16x32_bf16 v[84:87], a[148:151], v[168:171], v[84:87]\n\t" \
   "v_mfma_f32_16x16x32_bf16 v[88:91], a[152:155], v[168:171], v[88:91]\n\t" "v_mfma_f32_16x16x32_bf16 v[92:95], a[156:159], v[168:171], v[92:95]\n\t" \
   "s_waitcnt vmcnt(22)\n\t" "v_mfma_f32_16x16x32_bf16 v[64:67], a[160:163], v[172:175], v[64:67]\n\t" \
   "v_mfma_f32_16x16x32_bf16 v[68:71], a[164:167], v[172:175], v[68:71]\n\t" "v_mfma_f32_16x16x32_bf16 v[72:75], a[168:171], v[172:175], v[72:75]\n\t" \
   "v_mfma_f32_16x16x32_bf16 v[76:79], a[172:175], v[172:175], v[76:79]\n\t" "v_mfma_f32_16x16x32_bf16 v[80:83], a[176:179], v[172:175], v[80:83]\n\t" \
   "v_mfma_f32_16x16x32_bf16 v[84:87], a[180:183], v[172:175], v[84:87]\n\t" "v_mfma_f32_16x16x32_bf16 v[88:91], a[184:187], v[172:175], v[88:91]\n\t" \
   "v_mfma_f32_16x16x32_bf16 v[92:95], a[188:191], v[172:175], v[92:95]\n\t" "s_waitcnt vmcnt(21)\n\t" \
   "v_mfma_f32_16x16x32_bf16 v[64:67], a[192:195], v[176:179], v[64:67]\n\t" "v_mfma_f32_16x16x32_bf16 v[68:71], a[196:199], v[176:179], v[68:71]\n\t" \
   "v_mfma_f32_16x16x32_bf16 v[72:75], a[200:203], v[176:179], v[72:75]\n\t" "v_mfma_f32_16x16x32_bf16 v[76:79], a[204:207], v[176:179], v[76:79]\n\t" \
   "v_mfma_f32_16x16x32_bf16 v[80:83], a[208:211], v[176:179], v[80:83]\n\t" "v_mfma_f32_16x16x32_bf16 v[84:87], a[212:215], v[176:179], v[84:87]\n\t" \
   "v_mfma_f32_16x16x32_bf16 v[88:91], a[216:219], v[176:179], v[88:91]\n\t" "v_mfma_f32_16x16x32_bf16 v[92:95], a[220:223], v[176:179], v[92:95]\n\t" \
   "s_waitcnt vmcnt(20)\n\t" "s_waitcnt vmcnt(16)\n\t" "v_mfma_f32_16x16x32_bf16 v[64:67], a[224:227], v[180:183], v[64:67]\n\t" \
   "v_swap_b32 v145, v146\n\t" "v_mfma_f32_16x16x32_bf16 v[68:71], a[228:231], v[180:183], v[68:71]\n\t" "v_swap_b32 v149, v150\n\t" \
   "v_mfma_f32_16x16x32_bf16 v[72:75], a[232:235], v[180:183], v[72:75]\n\t" "v_swap_b32 v185, v186\n\t" \
   "v_mfma_f32_16x16x32_bf16 v[76:79], a[236:239], v[180:183], v[76:79]\n\t" "v_swap_b32 v189, v190\n\t" \
   "v_mfma_f32_16x16x32_bf16 v[80:83], a[240:243], v[180:183], v[80:83]\n\t" "v_mfma_f32_16x16x32_bf16 v[84:87], a[244:247], v[180:183], v[84:87]\n\t" \
   "v_mfma_f32_16x16x32_bf16 v[88:91], a[248:251], v[180:183], v[88:91]\n\t" "v_mfma_f32_16x16x32_bf16 v[92:95], a[252:255], v[180:183], v[92:95]\n\t" \
   "s_waitcnt vmcnt(15)\n\t" "v_mfma_f32_16x16x32_bf16 v[32:35], v[192:195], v[144:147], v[32:35]\n\t" "s_waitcnt vmcnt(14)\n\t" \
   "v_mfma_f32_16x16x32_bf16 v[36:39], v[196:199], v[144:147], v[36:39]\n\t" "s_waitcnt vmcnt(13)\n\t" \
   "v_mfma_f32_16x16x32_bf16 v[40:43], v[200:203], v[144:147], v[40:43]\n\t" "s_waitcnt vmcnt(12)\n\t" \
   "v_mfma_f32_16x16x32_bf16 v[44:47], v[204:207], v[144:147], v[44:47]\n\t" "s_waitcnt vmcnt(11)\n\t" \
   "v_mfma_f32_16x16x32_bf16 v[48:51], v[208:211], v[144:147], v[48:51]\n\t" "s_waitcnt vmcnt(10)\n\t" \
   "v_mfma_f32_16x16x32_bf16 v[52:55], v[212:215], v[144:147], v[52:55]\n\t" "s_waitcnt vmcnt(9)\n\t" \
   "v_mfma_f32_16x16x32_bf16 v[56:59], v[216:219], v[144:147], v[56:59]\n\t" "s_waitcnt vmcnt(8)\n\t" \
   "v_mfma_f32_16x16x32_bf16 v[60:63], v[220:223], v[144:147], v[60:63]\n\t" "s_waitcnt vmcnt(7)\n\t" \
   "v_mfma_f32_16x16x32_bf16 v[32:35], v[224:227], v[148:151], v[32:35]\n\t" "s_waitcnt vmcnt(6)\n\t" \
   "v_mfma_f32_16x16x32_bf16 v[36:39], v[228:231], v[148:151], v[36:39]\n\t" "s_waitcnt vmcnt(5)\n\t" \
   "v_mfma_f32_16x16x32_bf16 v[40:43], v[232:235], v[148:151], v[40:43]\n\t" "s_waitcnt vmcnt(4)\n\t" \
   "v_mfma_f32_16x16x32_bf16 v[44:47], v[236:239], v[148:151], v[44:47]\n\t" "s_waitcnt vmcnt(3)\n\t" \
   "v_mfma_f32_16x16x32_bf16 v[48:51], v[240:243], v[148:151], v[48:51]\n\t" "s_waitcnt vmcnt(2)\n\t" \
   "v_mfma_f32_16x16x32_bf16 v[52:55], v[244:247], v[148:151], v[52:55]\n\t" "s_waitcnt vmcnt(1)\n\t" \
   "v_mfma_f32_16x16x32_bf16 v[56:59], v[248:251], v[148:151], v[56:59]\n\t" "s_waitcnt vmcnt(0)\n\t" \
   "v_mfma_f32_16x16x32_bf16 v[60:63], v[252:255], v[148:151], v[60:63]\n\t" "v_mfma_f32_16x16x32_bf16 v[64:67], v[192:195], v[184:187], v[64:67]\n\t" \
   "v_mfma_f32_16x16x32_bf16 v[68:71], v[196:199], v[184:187], v[68:71]\n\t" "v_mfma_f32_16x16x32_bf16 v[72:75], v[200:203], v[184:187], v[72:75]\n\t" \
   "v_mfma_f32_16x16x32_bf16 v[76:79], v[204:207], v[184:187], v[76:79]\n\t" "v_mfma_f32_16x16x32_bf16 v[80:83], v[208:211], v[184:187], v[80:83]\n\t" \
   "v_mfma_f32_16x16x32_bf16 v[84:87], v[212:215], v[184:187], v[84:87]\n\t" "v_mfma_f32_16x16x32_bf16 v[88:91], v[216:219], v[184:187], v[88:91]\n\t" \
   "v_mfma_f32_16x16x32_bf16 v[92:95], v[220:223], v[184:187], v[92:95]\n\t" "v_mfma_f32_16x16x32_bf16 v[64:67], v[224:227], v[188:191], v[64:67]\n\t" \
   "ds_write_b128 %[lw], v[32:35] offset:0\n\t" "v_mfma_f32_16x16x32_bf16 v[68:71], v[228:231], v[188:191], v[68:71]\n\t" \
   "ds_write_b128 %[lw], v[36:39] offset:1024\n\t" "v_mfma_f32_16x16x32_bf16 v[72:75], v[232:235], v[188:191], v[72:75]\n\t" \
   "ds_write_b128 %[lw], v[40:43] offset:2048\n\t" "v_mfma_f32_16x16x32_bf16 v[76:79], v[236:239], v[188:191], v[76:79]\n\t" \
   "ds_write_b128 %[lw], v[44:47] offset:3072\n\t" "ds_write_b128 %[lw], v[64:67] offset:32768\n\t" \
   "v_mfma_f32_16x16x32_bf16 v[80:83], v[240:243], v[188:191], v[80:83]\n\t" "ds_write_b128 %[lw], v[48:51] offset:4096\n\t" \
   "ds_write_b128 %[lw], v[68:71] offset:33792\n\t" "v_mfma_f32_16x16x32_bf16 v[84:87], v[244:247], v[188:191], v[84:87]\n\t" \
   "ds_write_b128 %[lw], v[52:55] offset:5120\n\t" "ds_write_b128 %[lw], v[72:75] offset:34816\n\t" \
   "v_mfma_f32_16x16x32_bf16 v[88:91], v[248:251], v[188:191], v[88:91]\n\t" "ds_write_b128 %[lw], v[56:59] offset:6144\n\t" \
   "ds_write_b128 %[lw], v[76:79] offset:35840\n\t" "v_mfma_f32_16x16x32_bf16 v[92:95], v[252:255], v[188:191], v[92:95]\n\t" \
   "ds_write_b128 %[lw], v[60:63] offset:7168\n\t" "ds_write_b128 %[lw], v[80:83] offset:36864\n\t" "s_nop 1\n\t" "s_nop 3\n\t" \
   "ds_write_b128 %[lw], v[84:87] offset:37888\n\t" "s_nop 3\n\t" "ds_write_b128 %[lw], v[88:91] offset:38912\n\t" "s_nop 3\n\t" \
   "ds_write_b128 %[lw], v[92:95] offset:39936\n\t" "s_waitcnt lgkmcnt(0)\n\t" "s_barrier\n\t" "ds_read_b128 v[112:115], %[lr] offset:0\n\t" \
   "ds_read_b128 v[116:119], %[lr] offset:256\n\t" "ds_read_b128 v[120:123], %[lr] offset:8192\n\t" "ds_read_b128 v[124:127], %[lr] offset:8448\n\t" \
   "ds_read_b128 v[128:131], %[lr] offset:16384\n\t" "ds_read_b128 v[132:135], %[lr] offset:16640\n\t" \
   "ds_read_b128 v[136:139], %[lr] offset:24576\n\t" "ds_read_b128 v[140:143], %[lr] offset:24832\n\t" \
   "ds_read_b128 v[144:147], %[lr] offset:32768\n\t" "ds_read_b128 v[148:151], %[lr] offset:33024\n\t" \
   "ds_read_b128 v[152:155], %[lr] offset:40960\n\t" "ds_read_b128 v[156:159], %[lr] offset:41216\n\t" \
   "ds_read_b128 v[160:163], %[lr] offset:49152\n\t" "ds_read_b128 v[164:167], %[lr] offset:49408\n\t" \
   "ds_read_b128 v[168:171], %[lr] offset:57344\n\t" "ds_read_b128 v[172:175], %[lr] offset:57600\n\t" "s_waitcnt lgkmcnt(12)\n\t" \
   "v_add_f32 v112, v112, v120\n\t" "v_add_f32 v113, v113, v121\n\t" "v_add_f32 v114, v114, v122\n\t" "v_add_f32 v115, v115, v123\n\t" \
   "v_add_f32 v116, v116, v124\n\t" "v_add_f32 v117, v117, v125\n\t" "v_add_f32 v118, v118, v126\n\t" "v_add_f32 v119, v119, v127\n\t" \
   "s_waitcnt lgkmcnt(8)\n\t" "v_add_f32 v128, v128, v136\n\t" "v_add_f32 v129, v129, v137\n\t" "v_add_f32 v130, v130, v138\n\t" \
   "v_add_f32 v131, v131, v139\n\t" "v_add_f32 v132, v132, v140\n\t" "v_add_f32 v133, v133, v141\n\t" "v_add_f32 v134, v134, v142\n\t" \
   "v_add_f32 v135, v135, v143\n\t" "v_add_f32 v112, v112, v128\n\t" "v_add_f32 v113, v113, v129\n\t" "v_add_f32 v114, v114, v130\n\t" \
   "v_add_f32 v115, v115, v131\n\t" "v_add_f32 v116, v116, v132\n\t" "v_add_f32 v117, v117, v133\n\t" "v_add_f32 v118, v118, v134\n\t" \
   "v_add_f32 v119, v119, v135\n\t" "v_cvt_pk_bf16_f32 v96, v112, v113\n\t" "v_cvt_pk_bf16_f32 v97, v114, v115\n\t" \
   "v_cvt_pk_bf16_f32 v98, v116, v117\n\t" "v_cvt_pk_bf16_f32 v99, v118, v119\n\t" "s_waitcnt lgkmcnt(4)\n\t" "v_add_f32 v144, v144, v152\n\t" \
   "v_add_f32 v145, v145, v153\n\t" "v_add_f32 v146, v146, v154\n\t" "v_add_f32 v147, v147, v155\n\t" "v_add_f32 v148, v148, v156\n\t" \
   "v_add_f32 v149, v149, v157\n\t" "v_add_f32 v150, v150, v158\n\t" "v_add_f32 v151, v151, v159\n\t" "s_waitcnt lgkmcnt(0)\n\t" \
   "v_add_f32 v160, v160, v168\n\t" "v_add_f32 v161, v161, v169\n\t" "v_add_f32 v162, v162, v170\n\t" "v_add_f32 v163, v163, v171\n\t" \
   "v_add_f32 v164, v164, v172\n\t" "v_add_f32 v165, v165, v173\n\t" "v_add_f32 v166, v166, v174\n\t" "v_add_f32 v167, v167, v175\n\t" \
   "v_add_f32 v144, v144, v160\n\t" "v_add_f32 v145, v145, v161\n\t" "v_add_f32 v146, v146, v162\n\t" "v_add_f32 v147, v147, v163\n\t" \
   "v_add_f32 v148, v148, v164\n\t" "v_add_f32 v149, v149, v165\n\t" "v_add_f32 v150, v150, v166\n\t" "v_add_f32 v151, v151, v167\n\t" \
   "v_cvt_pk_bf16_f32 v100, v144, v145\n\t" "v_cvt_pk_bf16_f32 v101, v146, v147\n\t" "v_cvt_pk_bf16_f32 v102, v148, v149\n\t" \
   "v_cvt_pk_bf16_f32 v103, v150, v151\n\t" "s_nop 1"
#define O2_CLOB "v32", "v33", "v34", "v35", "v36", "v37", "v38", "v39", "v40", "v41", "v42", "v43", "v44", "v45", "v46", "v47", "v48", "v49", "v50", "v51", "v52", "v53", "v54", "v55", "v56", "v57", "v58", "v59", "v60", "v61", "v62", "v63", "v64", "v65", "v66", "v67", "v68", "v69", "v70", "v71", "v72", "v73", "v74", "v75", "v76", "v77", "v78", "v79", "v80", "v81", "v82", "v83", "v84", "v85", "v86", "v87", "v88", "v89", "v90", "v91", "v92", "v93", "v94", "v95", "v112", "v113", "v114", "v115", "v116", "v117", "v118", "v119", "v120", "v121", "v122", "v123", "v124", "v125", "v126", "v127", "v128", "v129", "v130", "v131", "v132", "v133", "v134", "v135", "v136", "v137", "v138", "v139", "v140", "v141", "v142", "v143", "v144", "v145", "v146", "v147", "v148", "v149", "v150", "v151", "v152", "v153", "v154", "v155", "v156", "v157", "v158", "v159", "v160", "v161", "v162", "v163", "v164", "v165", "v166", "v167", "v168", "v169", "v170", "v171", "v172", "v173", "v174", "v175", "v176", "v177", "v178", "v179", "v180", "v181", "v182", "v183", "v184", "v185", "v186", "v187", "v188", "v189", "v190", "v191", "s40"
#define TZ 430
#define TZM 450
constexpr int OOB = 0x7FFFFFF0;
#define TDC 190
__device__ __forceinline__ void when(unsigned tz) {
  const unsigned t = __builtin_amdgcn_readfirstlane(*(unsigned*)(dyn_lds + L_WHEN)) + tz;
  while ((int)(t - rtc()) > 0 && (int)(t - rtc()) < 1000) __builtin_amdgcn_s_sleep(1);
}
constexpr int COL_I2 = 64 * 264, COL_LDS = COL_I2 + 8 * 2064;
__device__ __forceinline__ void job_col(int wv, unsigned char* ws, rsrc_t rs, unsigned lds, int jb, int go, int clr, bool all) {
  const int lane = lane_id(), g = lane >> 4, r = lane & 15, j = wv * 64 + lane;
  const int xin = (int)OFF_X1T + (jb == 160 ? 0 : jb) * XS * 8 + j * 16;
  v4u x[4], wf[4];
#pragma unroll
  for (int q = 0; q < 4; ++q) stc(rs, (int)OFF_ZT + jb * ZS * 8 + q * 4096 + j * 16, v4u{0xFFFFFFFFu, 0xFFFFFFFFu, 0xFFFFFFFFu, 0xFFFFFFFFu});
#pragma unroll
  for (int q = 0; q < 4; ++q) {
    const int o = (int)OFF_WT + (jb == 160 ? 320 : 2 * jb + (q >> 1)) * S_ * 4 + ((wv * 2 + (q & 1)) * 64 + lane) * 16;
    wf[q] = ldc(rs, o);
  }
  v32u k0, k1, k2;
  v8u k3, ad = (unsigned)lane;
  {
    const int n = 16 * wv + r;
    cis8(k0, n * (512 * (g & 1) + 256 * (g >> 1)), n * 32, n * 128, (r & 1) ? 0x80008000u : 0u);
    cis8(k1, r * g * 256, r * 64, r * 1024, 0x80000000u);
#pragma unroll
    for (int q = 0; q < 8; ++q) {
      const int i = -n * sigma(g, q), k = (r + 16 * (q >> 2)) * (16 * wv + 4 * g + (q & 3));
      const float sc = 1.0f / ((float)S_ * (float)D_);
      k0[16 + 2 * q] = __builtin_bit_cast(unsigned, cosi(i)); k0[17 + 2 * q] = __builtin_bit_cast(unsigned, sini(i));
      k1[16 + 2 * q] = __builtin_bit_cast(unsigned, cosi(k) * sc); k1[17 + 2 * q] = __builtin_bit_cast(unsigned, sini(k) * sc);
      k3[q] = k1[8 + q] ^ 0x80008000u;
    }
    ad[0] = lds + 264 * (16 * (g & 1) + 8 * (g >> 1) + (r >> 2)) + 32 * (r & 3);
    ad[1] = lds + COL_I2 + (2 * (r >> 3) + (r & 1)) * 2064 + ((r >> 1) & 3) * 512 + (g & 1) * 8;
    ad[2] = ad[1] + 256 + (((4 * (wv >> 1) + 2 * (g >> 1) + (wv & 1)) ^ ((r & 6) + 1)) << 4);
    ad[1] += ((4 * (wv >> 1) + 2 * (g >> 1) + (wv & 1)) ^ (r & 6)) << 4;
    ad[3] = lds + COL_I2 + r * 128 + ((g ^ (r >> 1)) << 4);
    ad[4] = lds + COL_I2 + r * 128 + (((4 + g) ^ (r >> 1)) << 4);
    ad[5] = (int)OFF_ZT + (jb * ZS + 32 * (16 * wv + r) + 2 * g) * 8;
    asm volatile("" : "+{v[32:63]}"(k0), "+{v[64:95]}"(k1), "+{v[128:135]}"(k3), "+{v[16:23]}"(ad));
  }
  const unsigned sh = jb == 160 ? 16u : 0u, mk = jb == 0 ? 0xFFFFu : ~0u, d = lds + (j >> 4) * 264 + r * 16;
#pragma unroll
  for (int q = 0; q < 16; ++q) { k2[2 * q] = wf[q >> 2][q & 3] << 16; k2[2 * q + 1] = wf[q >> 2][q & 3] & 0xFFFF0000u; }
  asm volatile("" : "+{v[96:127]}"(k2));
  fan_signal<FSP>(wv, ws, clr, jb);
  fan_wait<64, 20>(wv, ws, go, jb);
  PH(all ? 27 : 47)
#pragma unroll
  for (int q = 0; q < 4; ++q) x[q] = ldc(rs, q == 3 && wv == 3 ? OOB : xin + q * 4096);
  if (all) fan_wait<FSP, NCOL, false>(wv, ws, clr, jb);
  if (wv == 3) {
    when(TDC);
    for (unsigned spins = 0; spins < (1u << 16); ++spins) {
      x[3] = ldc(rs, xin + 3 * 4096);
      if (__builtin_expect(!__any(max(x[3].x, x[3].z) == 0xFFFFFFFFu), 1)) break;
      __builtin_amdgcn_s_sleep(2);
      asm volatile("" ::: "memory");
    }
  }
#define CSTAMP(i)
  CSTAMP(0)
#pragma unroll
  for (int q = 0; q < 4; ++q) {
    LDS_V2(d + q * 16 * 264) = v2u{(x[q].x >> sh) & mk, x[q].y};
    LDS_V2(d + q * 16 * 264 + 8) = v2u{(x[q].z >> sh) & mk, x[q].w};
  }
  CSTAMP(1)
  PH(all ? 28 : 48)
  asm volatile(COL_ASM ::"{v[32:63]}"(k0), "{v[64:95]}"(k1), "{v[96:127]}"(k2), "{v[128:135]}"(k3), "{v[16:23]}"(ad), [rs] "s"(rs) : COL_CLOB, "memory");
  CSTAMP(2)
  PH(all ? 29 : 49)
  CSTAMP(3)
}
struct XRows { v4u p[2]; v2u q[2]; };
__device__ __forceinline__ int xr_off(int rt, int hf, int wv, int lane, bool third) {
  const int o = (int)OFF_XR + ((rt * KT + hf * 5) * 64 + (lane & 48) + 2 * (lane & 7) + ((lane >> 3) & 1)) * 16;
  return third ? (wv < 2 ? o + 4096 + wv * 8 : OOB) : o + wv * 1024;
}
__device__ __forceinline__ void idft_x(XRows& x, rsrc_t rs, int rt, int hf, int wv, int lane) {
#pragma unroll
  for (int m = 0; m < 2; ++m) {
    x.p[m] = __builtin_amdgcn_raw_buffer_load_b128(rs, xr_off(rt, hf, wv, lane, false), m * 10240, AUX_SC1);
    x.q[m] = __builtin_amdgcn_raw_buffer_load_b64(rs, xr_off(rt, hf, wv, lane, true), m * 10240, AUX_SC1);
  }
}
__device__ __forceinline__ void idft_stage(rsrc_t rs, v4u* lds, int rt, int wv, int lane) {
  const int o = (int)OFF_ZT + ((lane >> 3) * ZS + rt * 16 + 2 * (lane & 7)) * 8;
  const bool more = lane >> 4 == wv;
  int off[9];
  v4u t[9];
#pragma unroll
  for (int i = 0; i < 9; ++i) {
    off[i] = i < 4 ? o + (wv * 32 + i * 8) * ZS * 8 : (more && (i < 8 || lane < 8) ? o + (96 + i * 8) * ZS * 8 : OOB);
    t[i] = ldc(rs, off[i]);
  }
  for (unsigned spins = 0; spins < (1u << 16); ++spins) {
    unsigned m = 0;
#pragma unroll
    for (int i = 0; i < 9; ++i) m = max(m, max(t[i].x, t[i].z));
    if (__builtin_expect(!__any(m == 0xFFFFFFFFu), 1)) break;
    __builtin_amdgcn_s_sleep(2);
#pragma unroll
    for (int i = 0; i < 9; ++i) {
      const bool again = max(t[i].x, t[i].z) == 0xFFFFFFFFu;
      const v4u u = ldc(rs, again ? off[i] : OOB);
      if (again) t[i] = u;
    }
    asm volatile("" ::: "memory");
  }
  if (more && lane < 8) {
    t[0].x = (t[0].x & 0xFFFFu) | (t[8].x << 16);
    t[0].z = (t[0].z & 0xFFFFu) | (t[8].z << 16);
  }
  v4u* dst = lds + (lane >> 3) * 16 + (lane & 7);
#pragma unroll
  for (int c = 0; c < 4; ++c) dst[wv * 128 + (c & 1) * 640 + (c >> 1) * 8] = v4u{t[0][c], t[1][c], t[2][c], t[3][c]};
  if (more) {
#pragma unroll
    for (int c = 0; c < 4; ++c) dst[512 + (c & 1) * 640 + (c >> 1) * 8] = v4u{t[4][c], t[5][c], t[6][c], t[7][c]};
  }
}
#define A_IN(a) "{a[0:31]}"(a[0]), "{a[32:63]}"(a[1]), "{a[64:95]}"(a[2]), "{a[96:127]}"(a[3]), "{a[128:159]}"(a[4]), "{a[160:191]}"(a[5]), "{a[192:223]}"(a[6]), "{a[224:255]}"(a[7])
#define A_OUT(a) "={a[0:31]}"(a[0]), "={a[32:63]}"(a[1]), "={a[64:95]}"(a[2]), "={a[96:127]}"(a[3]), "={a[128:159]}"(a[4]), "={a[160:191]}"(a[5]), \
                 "={a[192:223]}"(a[6]), "={a[224:255]}"(a[7])
__device__ __forceinline__ void idft_mul(XRows& x, const v32u (&a)[8], unsigned la) {
  asm volatile(ID_ASM : "+{v[136:139]}"(x.p[0]), "+{v[140:143]}"(x.p[1]), "+{v[144:145]}"(x.q[0]), "+{v[146:147]}"(x.q[1])
               : A_IN(a), [la] "v"(la) : ID_CLOB, "memory");
}
__device__ __forceinline__ void idft_matrix(v32u (&a)[8], rsrc_t rs, int hf, int wv, int lane) {
  const int o = (int)OFF_INV + hf * 10 * NFK * 1024 + lane * 16;
  asm volatile(IM_ASM : A_OUT(a)
               : [o0] "v"(o + wv * 2 * NFK * 1024), [o1] "v"(wv < 2 ? o + (wv + 6) * NFK * 1024 : OOB), [rs] "s"(rs) : IM_CLOB, "memory");
}
constexpr int NFIRST = 40;
__device__ __forceinline__ void job_idft(int wv_, unsigned char* ws, rsrc_t rs, v4u* lds, int rt0, int hf, unsigned long long* dbg) {
  const int lane = lane_id(), wv = wv_;
  v32u a[8];
  idft_matrix(a, rs, hf, wv, lane);
  fan_wait<FSP, NCOL>(wv_, ws, fan_off(1, 0, 0), rt0 >> 2);
  fan_wait<64, 20>(wv_, ws, fan_off(0, 1, 0), rt0 >> 2);
#pragma unroll 1
  for (int rt = rt0; rt < RT; rt += 32) {
    XRows x;
    idft_x(x, rs, rt, hf, wv, lane);
    if (rt == rt0) { when(TZM); PH(30) }
    idft_stage(rs, lds, rt, wv, lane);
    if (rt == rt0) { PH(50) }
    idft_mul(x, a, lds_base() + lane * 16);
#pragma unroll
    for (int m = 0; m < 2; ++m) {
      __builtin_amdgcn_raw_buffer_store_b128(x.p[m], rs, xr_off(rt, hf, wv, lane, false), m * 10240, AUX_SC1);
      __builtin_amdgcn_raw_buffer_store_b64(x.q[m], rs, xr_off(rt, hf, wv, lane, true), m * 10240, AUX_SC1);
    }
    const f32x2 st = rows_sum(frag_stat(x.p[0]) + frag_stat(x.p[1]) + frag_stat(v4u{x.q[0].x, x.q[0].y, x.q[1].x, x.q[1].y}));
    f32x2* sp = (f32x2*)(lds + 5120) + (lane & 15);
    if (lane < 16) sp[wv * 16] = st;
    asm volatile("s_waitcnt lgkmcnt(0)\n\ts_barrier" ::: "memory");
    if (wv == 0) {
      const f32x2 s0 = sp[0] + sp[16], s1 = sp[32] + sp[48];
      const f32x4 sv = lane < 16 ? f32x4{s0.x, s0.y, s1.x, s1.y} : f32x4{0, 0, 0, 0};
      stc(rs, lane < 16 + hf * 16 ? (int)OFF_ST + (rt * 16 + 2 * (lane & 7) + ((lane >> 3) & 1)) * 48 + (hf + (lane >> 4)) * 16 : OOB, __builtin_bit_cast(v4u, sv));
    }
    if (rt == rt0) { PH(31) }
    if (rt < NFIRST / 2) fan_signal<64>(wv_, ws, fan_off(1, 1, 0), rt * 2 + hf);
    else dep_signal(wv_, ws, cnt_off(1, CNT_IDFT, rt >> 2));
    if (rt < NFIRST / 2) { PH(51) }
  }
}

constexpr int NFAST = 62;
#define EARLYP 0
#define TPL 2
#define TYL 40
__device__ __forceinline__ void job_out(int wv_, unsigned char* ws, rsrc_t rs, unsigned long long ysel, int layer, int s, int cg, int qt) {
  const int lane = lane_id(), wv = wv_, g = lane >> 4, r = lane & 15, q0 = max(qt, 0);
  const int yo = wv * 2048 + lane * 16;
  const int sy = __builtin_amdgcn_readfirstlane(pool_base(ws) + P_Y + y_region(ysel, s) + cg * 131072);
  const int xo = (int)OFF_XR + (((2 * s + q0) * KT + cg * 4 + wv) * 64 + lane) * 16;
  const v4u x0 = ldc(rs, xo), x1 = ldc(rs, qt < 0 ? xo + KT * 1024 : OOB);
  v32u u[8], y0, y1;
  asm volatile(OU_ASM : A_OUT(u) : [o] "v"(wv * 8192 + lane * 16), [so] "s"((int)OFF_UF + (s * 40 + cg * 8) * 32768), [rs] "s"(rs) : OU_CLOB, "scc", "memory");
  const int sw = min(s, NFAST - 1);
  if (s > 1) prog_prewait(wv_, ws, prog_off(layer, sw - 2, (s + cg * 7) & 31));
  prog_wait(wv_, ws, prog_off(layer, sw, 0), s < PWL);
  v8u acc;
#define OM_OPS A_IN(u), [po] "v"(wv * 16384 + lane * 16), [mo] "v"(wv * 4096 + lane * 16), [sp] "s"((int)OFF_PXF + s * 65536 + q0 * 1024),         \
               [sm] "s"((int)OFF_PXM + s * 16384 + q0 * 1024), [lw] "v"(lds_base() + wv * 8192 + lane * 16), [lr] "v"(lds_base() + wv * 2048 + g * 512 + r * 16), [rs] "s"(rs)
  PHT(1)
  if (qt >= 0) {
    if (s > NFAST) __builtin_amdgcn_s_sleep(TYL);
    for (int i = 0; i < 2 + EARLYP; ++i) {
      unsigned m;
      asm volatile(OY_ASM : "={v[192:223]}"(y0), "={v[224:255]}"(y1), [m] "=&v"(m) : [yo] "v"(yo), [sy] "s"(sy), [rs] "s"(rs) : OY_CLOB, "scc", "memory");
      if (i == 0) prog_wait(wv_, ws, prog_off(layer, EARLYP ? NSEG - 2 : s, 0), false);
      if (!__any(m == 0xFFFFFFFFu)) break;
      if (EARLYP && i == 1) prog_wait<false>(0, ws, prog_off(layer, s, 0), false);
    }
    if (EARLYP && s == NSEG - 1) __builtin_amdgcn_s_sleep(TPL);
    PHT(0)
    asm volatile(O1_ASM : "=&{v[96:103]}"(acc) : OM_OPS, "{v[192:223]}"(y0), "{v[224:255]}"(y1) : O1_CLOB, "scc", "memory");
    if (EARLYP && s == NSEG - 1) {
      unsigned* vote = (unsigned*)(dyn_lds + 83968);
      vote[wv] = __any((acc[0] & 0x7FFFu) > 0x7F80u);
      asm volatile("s_waitcnt lgkmcnt(0)\n\ts_barrier" ::: "memory");
      if (__any(vote[lane_id() & 3])) {
        prog_wait(wv_, ws, prog_off(layer, s, 0), false);
        const int lane = lane_id(), g = lane >> 4, r = lane & 15;
        asm volatile(O1_ASM : "=&{v[96:103]}"(acc) : OM_OPS, "{v[192:223]}"(y0), "{v[224:255]}"(y1) : O1_CLOB, "scc", "memory");
        PH(52)
      }
    }
    PHV(32 + (s - 61) * 4, pht0)
  } else {
    asm volatile(O2_ASM : "=&{v[96:103]}"(acc), "=&{v[192:223]}"(y0), "=&{v[224:255]}"(y1) : OM_OPS, [yo] "v"(yo), [sy] "s"(sy) : O2_CLOB, "scc", "memory");
  }
  f32x2* sp = (f32x2*)(dyn_lds + 81920);
#pragma unroll
  for (int q = 0; q < 2; ++q) {
    if (q && qt >= 0) break;
    const v4u x = q ? x1 : x0;
    const auto s0 = __builtin_amdgcn_permlane16_swap(acc[4 * q], acc[4 * q + 2], false, false);
    const auto s1 = __builtin_amdgcn_permlane16_swap(acc[4 * q + 1], acc[4 * q + 3], false, false);
    v4u y;
    y.x = pk(lo_f(x.x) + lo_f(s0[0]), hi_f(x.x) + hi_f(s0[0]));
    y.y = pk(lo_f(x.y) + lo_f(s0[1]), hi_f(x.y) + hi_f(s0[1]));
    y.z = pk(lo_f(x.z) + lo_f(s1[0]), hi_f(x.z) + hi_f(s1[0]));
    y.w = pk(lo_f(x.w) + lo_f(s1[1]), hi_f(x.w) + hi_f(s1[1]));
    stc(rs, xo + q * KT * 1024, y);
    const f32x2 st = rows_sum(frag_stat(y));
    if (lane < 16) sp[(wv * 2 + q) * 16 + lane] = st;
  }
  PHT(2)
  asm volatile("s_waitcnt lgkmcnt(0)\n\ts_barrier" ::: "memory");
  if (wv == 0) {
    sp += lane & 31;
    __builtin_amdgcn_raw_buffer_store_b64(__builtin_bit_cast(v2u, (sp[0] + sp[32]) + (sp[64] + sp[96])), rs,
                                          lane < (qt < 0 ? 32 : 16) ? (int)OFF_ST + ((s * 32 + q0 * 16 + lane) * 6 + cg) * 8 : OOB, 0, AUX_SC1);
  }
  dep_signal(wv_, ws, cnt_off(layer, CNT_OUT, s), qt < 0 ? 2u : 1u);
  PHT(3)
  if (s > 60) { PHV(33 + (s - 61) * 4, pht1) PHV(34 + (s - 61) * 4, pht2) PHV(35 + (s - 61) * 4, pht3) }
}
constexpr int JOB_LDS = 4 * NFK * 1024;
constexpr int FAN_LAST = fan_off(0, 1, 0) + 20 * 64;
#define OAUX 0
__device__ __forceinline__ void job_last(int wv_, unsigned char* ws, u16* out, int jb, int rt, int hf) {
  const rsrc_t rs = ws_rsrc(ws), ro = __builtin_amdgcn_make_buffer_rsrc(out, 0, S_ * D_ * 2, 0x00020000);
  const int wv = wv_;
  v32u a[8];
  idft_matrix(a, rs, hf, wv, lane_id());
  PH(26)
  if (jb >= 0) {
    job_col(wv_, ws, rs, lds_base(), jb, FAN_LAST, fan_off(0, 0, 0), true);
    PH(13)
  } else {
    fan_wait<FSP, NCOL>(wv_, ws, fan_off(0, 0, 0), rt);
    fan_wait<64, 20>(wv_, ws, FAN_LAST, rt);
  }
  const int lane = lane_id();
  v4u* lds = (v4u*)dyn_lds + 3072;
  XRows x;
  idft_x(x, rs, rt, hf, wv, lane);
  when(TZ);
  PHT(0)
  idft_stage(rs, lds, rt, wv, lane);
  PHT(1)
  idft_mul(x, a, lds_base() + 49152 + lane * 16);
  const int o = ((rt * 16 + 2 * (lane & 7) + ((lane >> 3) & 1)) * D_ + hf * 160 + (lane >> 5) * 8) * 2;
#pragma unroll
  for (int m = 0; m < 3; ++m) {
    const v4u y = m < 2 ? x.p[m] : v4u{x.q[0].x, x.q[0].y, x.q[1].x, x.q[1].y};
    const auto s0 = __builtin_amdgcn_permlane16_swap(y.x, y.z, false, false), s1 = __builtin_amdgcn_permlane16_swap(y.y, y.w, false, false);
    __builtin_amdgcn_raw_buffer_store_b128(v4u{s0[0], s1[0], s0[1], s1[1]}, ro,
                                           m < 2 ? o + m * 640 + wv * 64 + (lane & 16) * 2 : (wv < 2 ? o + 256 + wv * 32 + (lane & 16) * 40 : OOB), 0, OAUX);
  }
  PH(15)
  PHV(24, pht0) PHV(25, pht1)
}

constexpr unsigned SPIN_CAP = 1u << 16;
#define NFILL 24
#define FSPARE
#define FLAG 4

#define VM_LD128(dst, off, so, IMM) asm volatile("buffer_load_dwordx4 %0, %1, %2, %3 offen offset:" #IMM " sc1" : "=v"(dst) : "v"(off), "s"(rs), "s"(so) : "memory")
#define VM_LD32(dst, off, so) asm volatile("buffer_load_dword %0, %1, %2, %3 offen sc1" : "=v"(dst) : "v"(off), "s"(rs), "s"(so) : "memory")
#define VM_ST32(val, off, so) asm volatile("buffer_store_dword %0, %1, %2, %3 offen sc1" ::"v"(val), "v"(off), "s"(rs), "s"(so) : "memory")
#define VM_ST128(val, off, so) asm volatile("buffer_store_dwordx4 %0, %1, %2, %3 offen sc1\n\ts_nop 1" ::"v"(val), "v"(off), "s"(rs), "s"(so) : "memory")
#define VM_WAIT(N) asm volatile("s_waitcnt vmcnt(" #N ")" ::: "memory")
#define VM_WAIT1(N, a) asm volatile("s_waitcnt vmcnt(" #N ")" : "+v"(a)::"memory")
__device__ __forceinline__ v4u make_rs(const void* p, unsigned bytes) {
  const unsigned long long a = (unsigned long long)p;
  v4u r;
  r.x = (unsigned)__builtin_amdgcn_readfirstlane((unsigned)a);
  r.y = (unsigned)__builtin_amdgcn_readfirstlane((unsigned)(a >> 32) & 0xFFFFu);
  r.z = bytes;
  r.w = 0x00020000u;
  return r;
}
struct Clk0 { unsigned r, c; };
__device__ __forceinline__ Clk0 clk_sample() {
  unsigned long long r, c;
  asm volatile("s_memrealtime %0\n\ts_memtime %1\n\ts_waitcnt lgkmcnt(0)" : "=s"(r), "=s"(c)::"memory");
  return Clk0{(unsigned)r, (unsigned)c};
}
__device__ __forceinline__ v4u clk_init(Clk0 a) {
  const Clk0 b = clk_sample();
  unsigned k = 1400u;
  if (b.r - a.r >= 100u) k = (unsigned)((float)(b.r - a.r) * 32768.f / (float)(b.c - a.c));
  k = (unsigned)__builtin_amdgcn_readfirstlane((int)min(max(k, 1200u), 3000u));
  asm volatile("" : "+s"(k));
  return v4u{b.r * 8u + 16u, b.c, k, 0u};
}
#define LDSBAR() asm volatile("s_waitcnt lgkmcnt(0)\n\ts_barrier" ::: "memory")
#define MEMPIN() asm volatile("" ::: "memory")

__device__ __forceinline__ int ladder(int x, int i) { return copy_off(((x + (i >> 1)) & 3) | ((i & 1) << 2)); }

struct RecTune { int m1, m2, dl1, rob, c2, vis, dlt, du; };

#define STAT(r)
__device__ __forceinline__ unsigned max4(v4u v) { return max(max(v.x, v.y), max(v.z, v.w)); }
template <int NAP = 6>
__device__ __forceinline__ unsigned flag_finish(const v4u rs, int off, unsigned cv) {
  for (unsigned spins = 0; __any(cv == 0xFFFFFFFFu) && spins < SPIN_CAP; ++spins) {
    __builtin_amdgcn_s_sleep(NAP);
    v4u t;
    VM_LD128(t, off, 0, 0);
    VM_WAIT1(0, t);
    cv = max4(t);
  }
  return cv;
}
struct Pool {
  unsigned long long sel, s3, ysel; int base;
  __device__ __forceinline__ int near(int s) const { return (int)((s3 >> s) & 1ull) * 16384; }
  __device__ __forceinline__ int region(int s) const { return base + s * REG + (int)((sel >> s) & 1ull) * 8192; }
};
constexpr int L_QT = 10240, L_KT = 12288, L_CL = 14336, L_KQ = 16384;
constexpr int L_T = 14336;
constexpr int L_M2 = L_KQ + 65536;
constexpr int REC_ST = L_M2 + 12288;
constexpr int REC_LDS = REC_ST + 4096 + 64;

#define RSTAMP(i)
#define YSTAMP(n)
#define RST_CLOB
#define P1TRACE
#define PVTRACE
#define FINTRACE
#define P1_ASM \
   ".p2align 6\n\t" "s_nop 0\n\t" "s_nop 0\n\t" "s_nop 0\n\t" "s_nop 0\n\t" "s_mov_b32 s64, -1\n\t" "s_mov_b32 s76, 0\n\t" "s_mov_b32 s77, 0\n\t" \
   "s_mov_b32 s71, 63\n\t" "s_mov_b32 s47, 1300\n\t" "s_mov_b32 s49, 0\n\t" "s_mov_b32 s51, 0\n\t" "s_mov_b32 s52, 0\n\t" "s_mov_b32 s70, 0\n\t" \
   "s_mov_b32 s69, 0\n\t" "v_cmp_gt_i32 s[20:21], 0, v126\n\t" "v_cmp_gt_i32 s[22:23], 1, v126\n\t" "v_cmp_gt_i32 s[24:25], 2, v126\n\t" \
   "v_cmp_gt_i32 s[26:27], 3, v126\n\t" "v_cmp_gt_i32 s[28:29], 16, v126\n\t" "v_cmp_gt_i32 s[30:31], 17, v126\n\t" \
   "v_cmp_gt_i32 s[32:33], 18, v126\n\t" "v_cmp_gt_i32 s[34:35], 19, v126\n\t" "s_waitcnt vmcnt(0)\n\t" "s_lshr_b64 s[72:73], %[sel], s71\n\t" \
   "s_lshl_b32 s65, s71, 19\n\t" "s_and_b32 s72, s72, 1\n\t" "s_add_u32 s65, s65, %[pool]\n\t" "s_lshl_b32 s72, s72, 13\n\t" \
   "s_add_u32 s65, s65, s72\n\t" "s_lshr_b64 s[72:73], %[s3], s71\n\t" "s_and_b32 s54, s72, 1\n\t" "s_lshl_b32 s54, s54, 14\n\t" \
   "s_memtime s[62:63]\n\t" "s_memrealtime s[60:61]\n\t" "40:\n\t" "s_cmp_eq_u32 s64, 0\n\t" "s_cbranch_scc1 95f\n\t" "96:\n\t" \
   "s_add_i32 s72, s64, 2\n\t" "s_min_i32 s72, s72, 63\n\t" "s_lshl_b32 s59, s72, 12\n\t" "s_lshl_b32 s79, s72, 13\n\t" \
   "buffer_load_dword v108, v139, %[rs], s65 offen sc1\n\t" "s_cmp_gt_u32 %[wv], 1\n\t" "s_waitcnt vmcnt(4)\n\t" "s_cbranch_scc1 44f\n\t" \
   "buffer_load_dword v15, v140, %[rs], s59 offen sc1\n\t" "ds_write_b128 v127, v[16:19] offset:0\n\t" "ds_write_b128 v127, v[20:23] offset:1024\n\t" \
   "ds_write_b128 v127, v[24:27] offset:2048\n\t" "ds_write_b128 v127, v[28:31] offset:3072\n\t" "s_waitcnt lgkmcnt(0)\n\t" \
   "s_sub_u32 s74, s62, s57\n\t" "s_mul_i32 s74, s74, s58\n\t" "s_lshr_b32 s74, s74, 12\n\t" "s_add_u32 s74, s74, s56\n\t" "s_lshl_b32 s60, s60, 3\n\t" \
   "s_sub_u32 s75, s74, s60\n\t" "s_cmp_lt_u32 s75, 32\n\t" "s_cbranch_scc1 38f\n\t" "s_cmp_lt_i32 s75, 0\n\t" "s_cselect_b32 s61, 0, 31\n\t" \
   "s_add_u32 s74, s60, s61\n\t" "s_sub_i32 s75, s61, s75\n\t" "s_add_i32 s75, s75, 1\n\t" "s_ashr_i32 s75, s75, 1\n\t" "s_min_i32 s75, s75, 8\n\t" \
   "s_max_i32 s75, s75, -8\n\t" "s_mov_b32 s56, s74\n\t" "s_mov_b32 s57, s62\n\t" "s_add_u32 s58, s58, s75\n\t" "38:\n\t" "s_min_u32 s72, s70, 1\n\t" \
   "s_and_b32 s71, s74, -4\n\t" "s_or_b32 s71, s71, s72\n\t" "v_mov_b32 v111, s71\n\t" "buffer_store_dword v111, v131, %[rs], s65 offen sc1\n\t" \
   P1TRACE "s_barrier\n\t" "ds_read_b128 v[160:163], v122 offset:0\n\t" "ds_read_b128 v[164:167], v122 offset:1024\n\t" \
   "ds_read_b128 v[168:171], v122 offset:2048\n\t" "ds_read_b128 v[172:175], v122 offset:3072\n\t" "ds_read_b128 v[176:179], v122 offset:4096\n\t" \
   "ds_read_b128 v[180:183], v122 offset:5120\n\t" "ds_read_b128 v[184:187], v122 offset:6144\n\t" "ds_read_b128 v[188:191], v122 offset:7168\n\t" \
   "s_cmp_lt_i32 s64, 1\n\t" "s_cselect_b32 s72, 0x7ffffff0, 0\n\t" "s_max_i32 s71, s64, 1\n\t" "v_or_b32 v109, s72, v143\n\t" \
   "s_lshl_b32 s71, s71, 12\n\t" "v_mov_b32 v110, 1\n\t" "s_sub_u32 s71, s71, 4096\n\t" "buffer_store_dword v110, v109, %[rs], s71 offen sc1\n\t" \
   "s_add_u32 s48, s69, s47\n\t" "s_cmp_eq_u32 s70, 0\n\t" "s_cselect_b32 s48, s48, -1\n\t" "s_mov_b32 s66, s65\n\t" "s_add_i32 s64, s64, 1\n\t" \
   "s_and_b32 s71, s64, 63\n\t" "s_lshr_b64 s[72:73], %[sel], s71\n\t" "s_lshl_b32 s65, s71, 19\n\t" "s_and_b32 s72, s72, 1\n\t" \
   "s_add_u32 s65, s65, %[pool]\n\t" "s_lshl_b32 s72, s72, 13\n\t" "s_add_u32 s65, s65, s72\n\t" "s_xor_b32 s67, s54, %[lad0]\n\t" \
   "s_xor_b32 s68, s67, 0x4000\n\t" "s_add_u32 s67, s67, s66\n\t" "s_add_u32 s68, s68, s66\n\t" "s_lshr_b64 s[72:73], %[s3], s71\n\t" \
   "s_and_b32 s54, s72, 1\n\t" "s_lshl_b32 s54, s54, 14\n\t" "s_cmp_eq_u32 s64, 0\n\t" "s_cselect_b32 s70, 1, 0\n\t" "s_and_b32 s55, %[wv], 1\n\t" \
   "s_lshl_b32 s55, s55, 14\n\t" "s_xor_b32 s55, s55, s54\n\t" "s_xor_b32 s50, s55, 0x4000\n\t" "s_add_u32 s55, s55, s65\n\t" \
   "s_add_u32 s50, s50, s65\n\t" "v_mov_b32 v48, 0\n\t" "v_mov_b32 v49, 0\n\t" "v_mov_b32 v50, 0\n\t" "v_mov_b32 v51, 0\n\t" "v_mov_b32 v52, 0\n\t" \
   "v_mov_b32 v53, 0\n\t" "v_mov_b32 v54, 0\n\t" "v_mov_b32 v55, 0\n\t" "v_mov_b32 v56, 0\n\t" "v_mov_b32 v57, 0\n\t" "v_mov_b32 v58, 0\n\t" \
   "v_mov_b32 v59, 0\n\t" "v_mov_b32 v60, 0\n\t" "v_mov_b32 v61, 0\n\t" "v_mov_b32 v62, 0\n\t" "v_mov_b32 v63, 0\n\t" "s_waitcnt vmcnt(2)\n\t" \
   "v_cmp_eq_u32 vcc, -1, v15\n\t" "s_nop 0\n\t" "s_cbranch_vccnz 141f\n\t" "41:\n\t" "s_branch 48f\n\t" "44:\n\t" \
   "v_mfma_f32_16x16x32_bf16 v[32:35], v[160:163], v[16:19], 0\n\t" "s_max_i32 s51, s64, 0\n\t" "s_lshl_b32 s52, s51, 16\n\t" \
   "s_lshl_b32 s51, s51, 14\n\t" "v_mfma_f32_16x16x32_bf16 v[36:39], v[176:179], v[16:19], 0\n\t" "s_cmp_lt_i32 s64, 0\n\t" \
   "s_cselect_b32 s49, 0x7ffffff0, 0\n\t" "s_add_i32 s72, s64, 1\n\t" "v_mfma_f32_16x16x32_bf16 v[32:35], v[164:167], v[20:23], v[32:35]\n\t" \
   "s_min_i32 s72, s72, 63\n\t" "s_lshl_b32 s79, s72, 13\n\t" "v_mfma_f32_16x16x32_bf16 v[36:39], v[180:183], v[20:23], v[36:39]\n\t" \
   "v_mfma_f32_16x16x32_bf16 v[32:35], v[168:171], v[24:27], v[32:35]\n\t" "s_waitcnt lgkmcnt(0)\n\t" \
   "v_mfma_f32_16x16x32_bf16 v[36:39], v[184:187], v[24:27], v[36:39]\n\t" "s_sub_u32 s74, s62, s57\n\t" "s_mul_i32 s74, s74, s58\n\t" \
   "s_lshr_b32 s74, s74, 12\n\t" "v_mfma_f32_16x16x32_bf16 v[32:35], v[172:175], v[28:31], v[32:35]\n\t" "s_add_u32 s74, s74, s56\n\t" \
   "s_lshl_b32 s60, s60, 3\n\t" "s_sub_u32 s75, s74, s60\n\t" "v_mfma_f32_16x16x32_bf16 v[36:39], v[188:191], v[28:31], v[36:39]\n\t" \
   "s_cmp_lt_u32 s75, 32\n\t" "s_cbranch_scc1 39f\n\t" "s_cmp_lt_i32 s75, 0\n\t" "s_cselect_b32 s61, 0, 31\n\t" "s_add_u32 s74, s60, s61\n\t" \
   "s_sub_i32 s75, s61, s75\n\t" "s_add_i32 s75, s75, 1\n\t" "s_ashr_i32 s75, s75, 1\n\t" "s_min_i32 s75, s75, 8\n\t" "s_max_i32 s75, s75, -8\n\t" \
   "s_mov_b32 s56, s74\n\t" "s_mov_b32 s57, s62\n\t" "s_add_u32 s58, s58, s75\n\t" "39:\n\t" "s_barrier\n\t" \
   "v_mfma_f32_16x16x32_bf16 v[40:43], v[144:147], v[16:19], 0\n\t" "s_min_u32 s72, s70, 1\n\t" "s_and_b32 s71, s74, -4\n\t" \
   "s_or_b32 s71, s71, s72\n\t" "v_mov_b32 v111, s71\n\t" "v_mfma_f32_16x16x32_bf16 v[40:43], v[148:151], v[20:23], v[40:43]\n\t" \
   "buffer_store_dword v111, v131, %[rs], s65 offen sc1\n\t" P1TRACE "v_mov_b32 v48, 0\n\t" "v_mov_b32 v49, 0\n\t" \
   "v_mfma_f32_16x16x32_bf16 v[40:43], v[152:155], v[24:27], v[40:43]\n\t" "v_mov_b32 v50, 0\n\t" "v_mov_b32 v51, 0\n\t" "v_mov_b32 v52, 0\n\t" \
   "v_mov_b32 v53, 0\n\t" "v_mfma_f32_16x16x32_bf16 v[40:43], v[156:159], v[28:31], v[40:43]\n\t" "v_mov_b32 v54, 0\n\t" "v_mov_b32 v55, 0\n\t" \
   "v_cndmask_b32 v32, v32, v125, s[20:21]\n\t" "v_cndmask_b32 v33, v33, v125, s[22:23]\n\t" "v_cndmask_b32 v34, v34, v125, s[24:25]\n\t" \
   "v_cndmask_b32 v35, v35, v125, s[26:27]\n\t" "v_cndmask_b32 v36, v36, v125, s[28:29]\n\t" "v_cndmask_b32 v37, v37, v125, s[30:31]\n\t" \
   "v_cndmask_b32 v38, v38, v125, s[32:33]\n\t" "v_cndmask_b32 v39, v39, v125, s[34:35]\n\t" "v_exp_f32 v64, v32\n\t" "s_add_u32 s48, s69, s47\n\t" \
   "s_cmp_eq_u32 s70, 0\n\t" "s_cselect_b32 s48, s48, -1\n\t" "v_exp_f32 v65, v33\n\t" "s_mov_b32 s66, s65\n\t" "s_add_i32 s64, s64, 1\n\t" \
   "s_and_b32 s71, s64, 63\n\t" "v_exp_f32 v66, v34\n\t" "s_lshr_b64 s[72:73], %[sel], s71\n\t" "s_lshl_b32 s65, s71, 19\n\t" \
   "s_and_b32 s72, s72, 1\n\t" "v_exp_f32 v67, v35\n\t" "s_add_u32 s65, s65, %[pool]\n\t" "s_lshl_b32 s72, s72, 13\n\t" "s_add_u32 s65, s65, s72\n\t" \
   "v_exp_f32 v68, v36\n\t" "s_xor_b32 s67, s54, %[lad0]\n\t" "s_xor_b32 s68, s67, 0x4000\n\t" "s_add_u32 s67, s67, s66\n\t" "v_exp_f32 v69, v37\n\t" \
   "s_add_u32 s68, s68, s66\n\t" "s_lshr_b64 s[72:73], %[s3], s71\n\t" "s_and_b32 s54, s72, 1\n\t" "v_exp_f32 v70, v38\n\t" \
   "s_lshl_b32 s54, s54, 14\n\t" "s_cmp_eq_u32 s64, 0\n\t" "s_cselect_b32 s70, 1, 0\n\t" "v_exp_f32 v71, v39\n\t" "s_and_b32 s55, %[wv], 1\n\t" \
   "s_lshl_b32 s55, s55, 14\n\t" "s_xor_b32 s55, s55, s54\n\t" "v_exp_f32 v72, v40\n\t" "s_xor_b32 s50, s55, 0x4000\n\t" "s_add_u32 s55, s55, s65\n\t" \
   "s_add_u32 s50, s50, s65\n\t" "v_exp_f32 v73, v41\n\t" "v_mov_b32 v56, 0\n\t" "v_mov_b32 v57, 0\n\t" "v_mov_b32 v58, 0\n\t" "v_exp_f32 v74, v42\n\t" \
   "v_mov_b32 v59, 0\n\t" "v_mov_b32 v60, 0\n\t" "v_mov_b32 v61, 0\n\t" "v_exp_f32 v75, v43\n\t" "v_mov_b32 v62, 0\n\t" "v_mov_b32 v63, 0\n\t" \
   "v_add_f32 v80, v64, v65\n\t" "v_add_f32 v81, v66, v67\n\t" "v_add_f32 v82, v68, v69\n\t" "v_add_f32 v83, v70, v71\n\t" \
   "v_add_f32 v84, v72, v73\n\t" "v_add_f32 v85, v74, v75\n\t" "v_add_f32 v80, v80, v81\n\t" "v_add_f32 v82, v82, v83\n\t" \
   "v_add_f32 v84, v84, v85\n\t" "v_add_f32 v80, v80, v82\n\t" "v_fma_f32 v82, v80, 2.0, v84\n\t" "v_fma_f32 v80, v80, 2.0, v84\n\t" \
   "ds_read_b128 v[160:163], v122 offset:0\n\t" "ds_read_b128 v[164:167], v122 offset:1024\n\t" "v_permlane16_swap_b32 v80, v82\n\t" \
   "v_add_f32 v81, v80, v82\n\t" "v_add_f32 v80, v80, v82\n\t" "ds_read_b128 v[168:171], v122 offset:2048\n\t" \
   "ds_read_b128 v[172:175], v122 offset:3072\n\t" "v_permlane32_swap_b32 v80, v81\n\t" "v_add_f32 v80, v80, v81\n\t" "v_rcp_f32 v90, v80\n\t" \
   "v_add_u32 v81, 0xe0800000, v80\n\t" "v_add_f32 v90, v90, v90\n\t" "v_cmp_lt_u32 vcc, 0x40000000, v81\n\t" \
   "ds_read_b128 v[176:179], v122 offset:4096\n\t" "ds_read_b128 v[180:183], v122 offset:5120\n\t" "ds_read_b128 v[184:187], v122 offset:6144\n\t" \
   "ds_read_b128 v[188:191], v122 offset:7168\n\t" "s_cbranch_vccnz 74f\n\t" "75:\n\t" "v_pk_mul_f32 v[64:65], v[64:65], v[90:91] op_sel_hi:[1,0]\n\t" \
   "v_pk_mul_f32 v[66:67], v[66:67], v[90:91] op_sel_hi:[1,0]\n\t" "v_pk_mul_f32 v[68:69], v[68:69], v[90:91] op_sel_hi:[1,0]\n\t" \
   "v_pk_mul_f32 v[70:71], v[70:71], v[90:91] op_sel_hi:[1,0]\n\t" "v_pk_mul_f32 v[72:73], v[72:73], v[90:91] op_sel_hi:[1,0]\n\t" \
   "v_pk_mul_f32 v[74:75], v[74:75], v[90:91] op_sel_hi:[1,0]\n\t" "v_cvt_pk_bf16_f32 v44, v64, v65\n\t" "v_cvt_pk_bf16_f32 v46, v66, v67\n\t" \
   "v_cvt_pk_bf16_f32 v45, v68, v69\n\t" "v_cvt_pk_bf16_f32 v47, v70, v71\n\t" "v_cvt_pk_bf16_f32 v32, v72, v73\n\t" \
   "v_cvt_pk_bf16_f32 v33, v74, v75\n\t" "s_waitcnt vmcnt(1)\n\t" "48:\n\t" "v_readlane_b32 s69, v108, 0\n\t" "v_readlane_b32 s71, v108, 1\n\t" \
   "s_cmp_eq_u32 s64, 64\n\t" "s_cbranch_scc1 90f\n\t" "s_cmp_eq_u32 s69, -1\n\t" "s_cbranch_scc1 63f\n\t" "s_cmp_eq_u32 s71, -1\n\t" \
   "s_cselect_b32 s47, s47, s71\n\t" "50:\n\t" "s_add_u32 s73, s69, %[m1]\n\t" RSTAMP(5) "51:\n\t" "s_memrealtime s[60:61]\n\t" \
   "s_memtime s[62:63]\n\t" "s_waitcnt lgkmcnt(0)\n\t" "s_sub_u32 s74, s62, s57\n\t" "s_mul_i32 s74, s74, s58\n\t" "s_lshr_b32 s74, s74, 12\n\t" \
   "s_add_u32 s74, s74, s56\n\t" "s_lshl_b32 s60, s60, 3\n\t" "s_sub_u32 s75, s74, s60\n\t" "s_cmp_lt_u32 s75, 32\n\t" "s_cbranch_scc1 52f\n\t" \
   "s_cmp_lt_i32 s75, 0\n\t" "s_cselect_b32 s61, 0, 31\n\t" "s_add_u32 s74, s60, s61\n\t" "s_sub_i32 s75, s61, s75\n\t" "s_add_i32 s75, s75, 1\n\t" \
   "s_ashr_i32 s75, s75, 1\n\t" "s_min_i32 s75, s75, 8\n\t" "s_max_i32 s75, s75, -8\n\t" "s_mov_b32 s56, s74\n\t" "s_mov_b32 s57, s62\n\t" \
   "s_add_u32 s58, s58, s75\n\t" "52:\n\t" "s_sub_i32 s74, s73, s74\n\t" "s_cmp_le_i32 s74, 150\n\t" "s_cbranch_scc1 53f\n\t" \
   "s_cmp_lt_i32 s74, 260\n\t" "s_cbranch_scc1 51b\n\t" "s_sleep 2\n\t" "s_cmp_lt_i32 s74, 32000\n\t" "s_cbranch_scc1 51b\n\t" "s_mov_b32 s74, 0\n\t" \
   "53:\n\t" "s_sub_i32 s74, s74, 56\n\t" "s_max_i32 s74, s74, 0\n\t" "s_mul_i32 s74, s74, 23\n\t" "s_lshr_b32 s74, s74, 5\n\t" \
   "s_and_b32 s74, s74, -4\n\t" "s_getpc_b64 s[60:61]\n\t" "54:\n\t" "s_sub_u32 s74, 55f-54b, s74\n\t" "s_add_u32 s60, s60, s74\n\t" \
   "s_addc_u32 s61, s61, 0\n\t" "s_setpc_b64 s[60:61]\n\t" "s_nop 3\n\t" "s_nop 3\n\t" "s_nop 3\n\t" "s_nop 3\n\t" "s_nop 3\n\t" "s_nop 3\n\t" \
   "s_nop 3\n\t" "s_nop 3\n\t" "s_nop 3\n\t" "s_nop 3\n\t" "s_nop 3\n\t" "s_nop 3\n\t" "s_nop 3\n\t" "s_nop 3\n\t" "s_nop 3\n\t" "s_nop 3\n\t" \
   "s_nop 3\n\t" "s_nop 3\n\t" "s_nop 3\n\t" "s_nop 3\n\t" "s_nop 3\n\t" "s_nop 3\n\t" "s_nop 3\n\t" "s_nop 3\n\t" "55:\n\t" "20:\n\t" RSTAMP(4) \
   "buffer_load_dwordx4 v[64:67], v128, %[rs], s67 offen sc1\n\t" "buffer_load_dwordx4 v[68:71], v129, %[rs], s67 offen sc1\n\t" \
   "buffer_load_dwordx4 v[72:75], v130, %[rs], s67 offen sc1\n\t" "s_nop 7\n\t" "s_nop 7\n\t" "s_nop 7\n\t" "s_cmp_lg_u32 s70, 0\n\t" \
   "s_cbranch_scc1 27f\n\t" "buffer_load_dwordx4 v[16:19], v138, %[rs], s79 offen offset:0 sc1\n\t" \
   "buffer_load_dwordx4 v[20:23], v138, %[rs], s79 offen offset:1024 sc1\n\t" \
   "buffer_load_dwordx4 v[24:27], v138, %[rs], s79 offen offset:2048 sc1\n\t" \
   "buffer_load_dwordx4 v[28:31], v138, %[rs], s79 offen offset:3072 sc1\n\t" "v_or_b32 v108, s49, v141\n\t" "v_or_b32 v109, s49, v142\n\t" \
   "buffer_store_dwordx4 v[44:47], v108, %[rs], s52 offen sc1\n\t" "buffer_store_dwordx2 v[32:33], v109, %[rs], s51 offen sc1\n\t" "28:\n\t" RSTAMP(3) \
   "s_waitcnt vmcnt(8)\n\t" "ds_write_b128 v112, v[64:67]\n\t" "v_smfmac_f32_16x16x64_bf16 v[48:51], v[64:67], a[0:7], v134\n\t" \
   "s_waitcnt vmcnt(7)\n\t" "ds_write_b128 v113, v[68:71]\n\t" "s_waitcnt vmcnt(6)\n\t" "ds_write_b128 v114, v[72:75]\n\t" RSTAMP(7) "2:\n\t" \
   "v_smfmac_f32_16x16x64_bf16 v[52:55], v[64:67], a[64:71], v134\n\t" "v_max3_u32 v110, v64, v66, v68\n\t" "v_max3_u32 v111, v70, v72, v74\n\t" \
   "v_smfmac_f32_16x16x64_bf16 v[48:51], v[68:71], a[8:15], v134\n\t" "v_max_u32 v110, v110, v111\n\t" "v_cmp_eq_u32 vcc, -1, v110\n\t" \
   "v_smfmac_f32_16x16x64_bf16 v[52:55], v[68:71], a[72:79], v134\n\t" "s_cbranch_vccnz 8f\n\t" \
   "v_smfmac_f32_16x16x64_bf16 v[48:51], v[72:75], a[16:23], v134\n\t" "s_waitcnt lgkmcnt(0)\n\t" "s_barrier\n\t" RSTAMP(0) \
   "ds_read_b128 v[76:79], v115\n\t" "ds_read_b128 v[80:83], v116\n\t" "v_smfmac_f32_16x16x64_bf16 v[52:55], v[72:75], a[80:87], v134\n\t" \
   "ds_read_b128 v[84:87], v117\n\t" "ds_read_b128 v[88:91], v118\n\t" "v_smfmac_f32_16x16x64_bf16 v[56:59], v[64:67], a[128:135], v134\n\t" \
   "ds_read_b128 v[92:95], v119\n\t" "ds_read_b128 v[96:99], v120\n\t" "v_smfmac_f32_16x16x64_bf16 v[60:63], v[64:67], a[192:199], v134\n\t" \
   "ds_read_b128 v[100:103], v121\n\t" "v_smfmac_f32_16x16x64_bf16 v[56:59], v[68:71], a[136:143], v134\n\t" \
   "v_smfmac_f32_16x16x64_bf16 v[60:63], v[68:71], a[200:207], v134\n\t" "s_waitcnt lgkmcnt(6)\n\t" \
   "v_smfmac_f32_16x16x64_bf16 v[48:51], v[76:79], a[24:31], v134\n\t" "v_smfmac_f32_16x16x64_bf16 v[52:55], v[76:79], a[88:95], v134\n\t" \
   "s_waitcnt lgkmcnt(5)\n\t" "v_smfmac_f32_16x16x64_bf16 v[48:51], v[80:83], a[32:39], v134\n\t" \
   "v_smfmac_f32_16x16x64_bf16 v[52:55], v[80:83], a[96:103], v134\n\t" "s_waitcnt lgkmcnt(4)\n\t" \
   "v_smfmac_f32_16x16x64_bf16 v[48:51], v[84:87], a[40:47], v134\n\t" "v_smfmac_f32_16x16x64_bf16 v[52:55], v[84:87], a[104:111], v134\n\t" \
   "s_waitcnt lgkmcnt(3)\n\t" "v_smfmac_f32_16x16x64_bf16 v[48:51], v[88:91], a[48:55], v134\n\t" \
   "v_smfmac_f32_16x16x64_bf16 v[52:55], v[88:91], a[112:119], v134\n\t" "s_waitcnt lgkmcnt(2)\n\t" \
   "v_smfmac_f32_16x16x64_bf16 v[48:51], v[92:95], a[56:63], v134\n\t" "v_smfmac_f32_16x16x64_bf16 v[52:55], v[92:95], a[120:127], v134\n\t" \
   "s_waitcnt lgkmcnt(0)\n\t" "v_smfmac_f32_16x16x64_bf16 v[48:51], v[96:99], v[192:199], v134\n\t" \
   "v_smfmac_f32_16x16x64_bf16 v[48:51], v[100:103], v[200:207], v134\n\t" "v_smfmac_f32_16x16x64_bf16 v[52:55], v[96:99], v[208:215], v134\n\t" \
   "v_smfmac_f32_16x16x64_bf16 v[52:55], v[100:103], v[216:223], v134\n\t" "v_smfmac_f32_16x16x64_bf16 v[56:59], v[72:75], a[144:151], v134\n\t" \
   "v_add_f32 v48, v48, v49\n\t" "v_add_f32 v49, v50, v51\n\t" "v_smfmac_f32_16x16x64_bf16 v[60:63], v[72:75], a[208:215], v134\n\t" \
   "v_mul_f32 v48, 0x3e0293ee, v48\n\t" "v_mul_f32 v49, 0x3e0293ee, v49\n\t" "v_smfmac_f32_16x16x64_bf16 v[56:59], v[76:79], a[152:159], v134\n\t" \
   "v_add_f32 v52, v52, v53\n\t" "v_add_f32 v53, v54, v55\n\t" "v_smfmac_f32_16x16x64_bf16 v[56:59], v[80:83], a[160:167], v134\n\t" \
   "v_mul_f32 v52, 0x3e0293ee, v52\n\t" "v_mul_f32 v53, 0x3e0293ee, v53\n\t" "v_smfmac_f32_16x16x64_bf16 v[56:59], v[84:87], a[168:175], v134\n\t" \
   "v_cvt_pk_bf16_f32 v48, v48, v49\n\t" "ds_write_b32 v135, v48 offset:10240\n\t" \
   "v_smfmac_f32_16x16x64_bf16 v[56:59], v[88:91], a[176:183], v134\n\t" "v_cvt_pk_bf16_f32 v52, v52, v53\n\t" \
   "ds_write_b32 v135, v52 offset:10496\n\t" "v_smfmac_f32_16x16x64_bf16 v[56:59], v[92:95], a[184:191], v134\n\t" \
   "v_smfmac_f32_16x16x64_bf16 v[56:59], v[96:99], v[224:231], v134\n\t" "v_smfmac_f32_16x16x64_bf16 v[56:59], v[100:103], v[232:239], v134\n\t" \
   "s_waitcnt lgkmcnt(0)\n\t" "s_barrier\n\t" "ds_read_b64_tr_b16 v[64:65], v136 offset:10240\n\t" "ds_read_b64_tr_b16 v[66:67], v136 offset:10496\n\t" \
   "v_smfmac_f32_16x16x64_bf16 v[60:63], v[76:79], a[216:223], v134\n\t" "ds_read_b64_tr_b16 v[68:69], v136 offset:10752\n\t" \
   "v_smfmac_f32_16x16x64_bf16 v[60:63], v[80:83], a[224:231], v134\n\t" "ds_read_b64_tr_b16 v[70:71], v136 offset:11008\n\t" \
   "v_smfmac_f32_16x16x64_bf16 v[60:63], v[84:87], a[232:239], v134\n\t" "ds_read_b64_tr_b16 v[72:73], v136 offset:11264\n\t" \
   "v_smfmac_f32_16x16x64_bf16 v[60:63], v[88:91], a[240:247], v134\n\t" "ds_read_b64_tr_b16 v[74:75], v136 offset:11520\n\t" \
   "v_add_f32 v56, v56, v57\n\t" "v_add_f32 v57, v58, v59\n\t" "v_smfmac_f32_16x16x64_bf16 v[60:63], v[92:95], a[248:255], v134\n\t" \
   "ds_read_b64_tr_b16 v[104:105], v136 offset:11776\n\t" "ds_read_b64_tr_b16 v[106:107], v136 offset:12032\n\t" \
   "v_smfmac_f32_16x16x64_bf16 v[60:63], v[96:99], v[240:247], v134\n\t" "v_cvt_pk_bf16_f32 v56, v56, v57\n\t" \
   "ds_write_b32 v135, v56 offset:12288\n\t" "v_smfmac_f32_16x16x64_bf16 v[60:63], v[100:103], v[248:255], v134\n\t" RSTAMP(6) \
   "s_waitcnt lgkmcnt(7)\n\t" "v_mfma_f32_16x16x32_bf16 v[48:51], v[160:163], v[64:67], 0\n\t" \
   "v_mfma_f32_16x16x32_bf16 v[52:55], v[176:179], v[64:67], 0\n\t" "s_waitcnt lgkmcnt(3)\n\t" "s_nop 0\n\t" "v_add_f32 v60, v60, v61\n\t" \
   "v_add_f32 v61, v62, v63\n\t" "v_cvt_pk_bf16_f32 v60, v60, v61\n\t" "ds_write_b32 v135, v60 offset:12544\n\t" \
   "v_mfma_f32_16x16x32_bf16 v[48:51], v[164:167], v[68:71], v[48:51]\n\t" "v_mfma_f32_16x16x32_bf16 v[52:55], v[180:183], v[68:71], v[52:55]\n\t" \
   "v_mfma_f32_16x16x32_bf16 v[48:51], v[168:171], v[72:75], v[48:51]\n\t" "v_mfma_f32_16x16x32_bf16 v[52:55], v[184:187], v[72:75], v[52:55]\n\t" \
   "s_waitcnt lgkmcnt(2)\n\t" "v_mfma_f32_16x16x32_bf16 v[48:51], v[172:175], v[104:107], v[48:51]\n\t" \
   "v_mfma_f32_16x16x32_bf16 v[52:55], v[188:191], v[104:107], v[52:55]\n\t" "s_waitcnt lgkmcnt(0)\n\t" "s_barrier\n\t" \
   "ds_read_b64_tr_b16 v[144:145], v136 offset:12288\n\t" "ds_read_b64_tr_b16 v[146:147], v136 offset:12544\n\t" \
   "ds_read_b64_tr_b16 v[148:149], v136 offset:12800\n\t" "ds_read_b64_tr_b16 v[150:151], v136 offset:13056\n\t" \
   "ds_read_b64_tr_b16 v[152:153], v136 offset:13312\n\t" "ds_read_b64_tr_b16 v[154:155], v136 offset:13568\n\t" \
   "ds_read_b64_tr_b16 v[156:157], v136 offset:13824\n\t" "ds_read_b64_tr_b16 v[158:159], v136 offset:14080\n\t" "s_cmp_eq_u32 s64, 0\n\t" \
   "s_cbranch_scc1 92f\n\t" "93:\n\t" RSTAMP(1) "v_cndmask_b32 v48, v48, v52, %[hi8]\n\t" "v_cndmask_b32 v49, v49, v53, %[hi8]\n\t" \
   "v_cndmask_b32 v50, v50, v54, %[hi8]\n\t" "v_cndmask_b32 v51, v51, v55, %[hi8]\n\t" "v_exp_f32 v80, v48\n\t" "v_exp_f32 v81, v49\n\t" \
   "s_waitcnt lgkmcnt(6)\n\t" "v_mfma_f32_16x16x32_bf16 v[76:79], v[144:147], v[64:67], 0\n\t" "v_exp_f32 v82, v50\n\t" "v_exp_f32 v83, v51\n\t" \
   "s_waitcnt lgkmcnt(4)\n\t" "v_mfma_f32_16x16x32_bf16 v[76:79], v[148:151], v[68:71], v[76:79]\n\t" "v_add_f32 v96, v80, v81\n\t" \
   "v_add_f32 v103, v82, v83\n\t" "s_waitcnt lgkmcnt(2)\n\t" "v_mfma_f32_16x16x32_bf16 v[76:79], v[152:155], v[72:75], v[76:79]\n\t" \
   "v_add_f32 v94, v96, v103\n\t" "v_add_f32 v96, v96, v103\n\t" "s_waitcnt lgkmcnt(0)\n\t" \
   "v_mfma_f32_16x16x32_bf16 v[76:79], v[156:159], v[104:107], v[76:79]\n\t" "v_permlane32_swap_b32 v96, v94\n\t" "v_add_f32 v96, v96, v94\n\t" \
   "s_nop 1\n\t" "v_add_f32_dpp v96, v96, v96 row_ror:8 row_mask:0xf bank_mask:0xf\n\t" "s_nop 2\n\t" "v_exp_f32 v88, v76\n\t" "v_exp_f32 v89, v77\n\t" \
   "v_add_f32 v103, v96, v88\n\t" "v_exp_f32 v90, v78\n\t" "v_add_f32 v103, v103, v89\n\t" "v_exp_f32 v91, v79\n\t" "v_add_f32 v103, v103, v90\n\t" \
   "v_add_f32 v97, v103, v91\n\t" "v_add_f32 v103, v103, v91\n\t" "s_nop 1\n\t" "v_permlane16_swap_b32 v103, v97\n\t" "v_add_f32 v103, v103, v97\n\t" \
   "v_rcp_f32 v100, v103\n\t" "v_add_u32 v94, 0xe0800000, v103\n\t" "v_pk_mul_f32 v[80:81], v[80:81], v[100:101] op_sel_hi:[1,0]\n\t" \
   "v_cmp_lt_u32 vcc, 0x40000000, v94\n\t" "v_pk_mul_f32 v[82:83], v[82:83], v[100:101] op_sel_hi:[1,0]\n\t" "s_cbranch_vccnz 70f\n\t" \
   "v_cvt_pk_bf16_f32 v48, v80, v81\n\t" "v_cvt_pk_bf16_f32 v49, v82, v83\n\t" "buffer_store_dwordx2 v[48:49], v132, %[rs], s55 offen sc1\n\t" \
   "v_pk_mul_f32 v[88:89], v[88:89], v[100:101] op_sel_hi:[1,0]\n\t" "v_pk_mul_f32 v[90:91], v[90:91], v[100:101] op_sel_hi:[1,0]\n\t" \
   "v_cvt_pk_bf16_f32 v52, v88, v89\n\t" "v_cvt_pk_bf16_f32 v53, v90, v91\n\t" "71:\n\t" \
   "buffer_store_dwordx2 v[52:53], v133, %[rs], s65 offen sc1\n\t" "s_memtime s[62:63]\n\t" "s_memrealtime s[60:61]\n\t" \
   "buffer_store_dwordx2 v[48:49], v132, %[rs], s50 offen sc1\n\t" RSTAMP(2) "s_branch 40b\n\t" "70:\n\t" "v_max3_f32 v94, v48, v49, v50\n\t" \
   "v_max3_f32 v102, v76, v77, v78\n\t" "v_max3_f32 v94, v94, v51, v79\n\t" "v_max_f32 v92, v94, v102\n\t" "s_nop 1\n\t" \
   "v_max_f32_dpp v102, v92, v92 row_ror:8 row_mask:0xf bank_mask:0xf\n\t" "v_max_f32_dpp v94, v92, v92 row_ror:8 row_mask:0xf bank_mask:0xf\n\t" \
   "s_nop 1\n\t" "v_permlane32_swap_b32 v102, v94\n\t" "v_max_f32 v95, v102, v94\n\t" "v_max_f32 v102, v102, v94\n\t" "s_nop 1\n\t" \
   "v_permlane16_swap_b32 v102, v95\n\t" "v_max_f32 v102, v102, v95\n\t" "v_sub_f32 v80, v48, v102\n\t" "v_sub_f32 v88, v76, v102\n\t" \
   "v_sub_f32 v81, v49, v102\n\t" "v_sub_f32 v89, v77, v102\n\t" "v_sub_f32 v82, v50, v102\n\t" "v_sub_f32 v90, v78, v102\n\t" \
   "v_sub_f32 v83, v51, v102\n\t" "v_sub_f32 v91, v79, v102\n\t" "v_exp_f32 v80, v80\n\t" "v_exp_f32 v88, v88\n\t" "v_exp_f32 v81, v81\n\t" \
   "v_exp_f32 v89, v89\n\t" "v_exp_f32 v82, v82\n\t" "v_exp_f32 v90, v90\n\t" "v_exp_f32 v83, v83\n\t" "v_exp_f32 v91, v91\n\t" "s_nop 0\n\t" \
   "v_add_f32 v96, v80, v81\n\t" "v_add_f32 v103, v82, v83\n\t" "v_add_f32 v94, v96, v103\n\t" "v_add_f32 v96, v96, v103\n\t" "s_nop 1\n\t" \
   "v_permlane32_swap_b32 v96, v94\n\t" "v_add_f32 v96, v96, v94\n\t" "s_nop 1\n\t" \
   "v_add_f32_dpp v96, v96, v96 row_ror:8 row_mask:0xf bank_mask:0xf\n\t" "v_add_f32 v103, v96, v88\n\t" "v_add_f32 v103, v103, v89\n\t" \
   "v_add_f32 v103, v103, v90\n\t" "v_add_f32 v97, v103, v91\n\t" "v_add_f32 v103, v103, v91\n\t" "s_nop 1\n\t" "v_permlane16_swap_b32 v103, v97\n\t" \
   "v_add_f32 v103, v103, v97\n\t" "v_rcp_f32 v100, v103\n\t" "s_nop 0\n\t" "v_pk_mul_f32 v[80:81], v[80:81], v[100:101] op_sel_hi:[1,0]\n\t" \
   "v_pk_mul_f32 v[82:83], v[82:83], v[100:101] op_sel_hi:[1,0]\n\t" "v_pk_mul_f32 v[88:89], v[88:89], v[100:101] op_sel_hi:[1,0]\n\t" \
   "v_pk_mul_f32 v[90:91], v[90:91], v[100:101] op_sel_hi:[1,0]\n\t" "v_cvt_pk_bf16_f32 v48, v80, v81\n\t" "v_cvt_pk_bf16_f32 v49, v82, v83\n\t" \
   "v_cvt_pk_bf16_f32 v52, v88, v89\n\t" "v_cvt_pk_bf16_f32 v53, v90, v91\n\t" "v_min_u32 v48, -2, v48\n\t" "v_min_u32 v52, -2, v52\n\t" \
   "buffer_store_dwordx2 v[48:49], v132, %[rs], s55 offen sc1\n\t" "s_branch 71b\n\t" "74:\n\t" "v_max3_f32 v80, v32, v33, v34\n\t" \
   "v_max3_f32 v81, v35, v36, v37\n\t" "v_max3_f32 v82, v38, v39, v40\n\t" "v_max3_f32 v83, v41, v42, v43\n\t" "v_max3_f32 v80, v80, v81, v82\n\t" \
   "v_max_f32 v81, v80, v83\n\t" "v_max_f32 v80, v80, v83\n\t" "s_nop 1\n\t" "v_permlane16_swap_b32 v80, v81\n\t" "v_max_f32 v82, v80, v81\n\t" \
   "v_max_f32 v80, v80, v81\n\t" "s_nop 1\n\t" "v_permlane32_swap_b32 v80, v82\n\t" "v_max_f32 v80, v80, v82\n\t" "v_sub_f32 v64, v32, v80\n\t" \
   "v_sub_f32 v65, v33, v80\n\t" "v_sub_f32 v66, v34, v80\n\t" "v_sub_f32 v67, v35, v80\n\t" "v_sub_f32 v68, v36, v80\n\t" \
   "v_sub_f32 v69, v37, v80\n\t" "v_sub_f32 v70, v38, v80\n\t" "v_sub_f32 v71, v39, v80\n\t" "v_sub_f32 v72, v40, v80\n\t" \
   "v_sub_f32 v73, v41, v80\n\t" "v_sub_f32 v74, v42, v80\n\t" "v_sub_f32 v75, v43, v80\n\t" "v_exp_f32 v64, v64\n\t" "v_exp_f32 v65, v65\n\t" \
   "v_exp_f32 v66, v66\n\t" "v_exp_f32 v67, v67\n\t" "v_exp_f32 v68, v68\n\t" "v_exp_f32 v69, v69\n\t" "v_exp_f32 v70, v70\n\t" \
   "v_exp_f32 v71, v71\n\t" "v_exp_f32 v72, v72\n\t" "v_exp_f32 v73, v73\n\t" "v_exp_f32 v74, v74\n\t" "v_exp_f32 v75, v75\n\t" "s_nop 0\n\t" \
   "v_add_f32 v80, v64, v65\n\t" "v_add_f32 v81, v66, v67\n\t" "v_add_f32 v82, v68, v69\n\t" "v_add_f32 v83, v70, v71\n\t" \
   "v_add_f32 v84, v72, v73\n\t" "v_add_f32 v85, v74, v75\n\t" "v_add_f32 v80, v80, v81\n\t" "v_add_f32 v82, v82, v83\n\t" \
   "v_add_f32 v84, v84, v85\n\t" "v_add_f32 v80, v80, v82\n\t" "v_fma_f32 v82, v80, 2.0, v84\n\t" "v_fma_f32 v80, v80, 2.0, v84\n\t" "s_nop 0\n\t" \
   "s_nop 0\n\t" "v_permlane16_swap_b32 v80, v82\n\t" "v_add_f32 v81, v80, v82\n\t" "v_add_f32 v80, v80, v82\n\t" "s_nop 0\n\t" "s_nop 0\n\t" \
   "v_permlane32_swap_b32 v80, v81\n\t" "v_add_f32 v80, v80, v81\n\t" "v_rcp_f32 v90, v80\n\t" "s_nop 0\n\t" "v_add_f32 v90, v90, v90\n\t" \
   "s_branch 75b\n\t" "63:\n\t" "s_cmp_lg_u32 s64, 0\n\t" "s_cbranch_scc1 64f\n\t" "s_sub_u32 s69, s56, 0x10000\n\t" "s_and_b32 s69, s69, -2\n\t" \
   "s_branch 50b\n\t" "64:\n\t" "s_mov_b32 s69, s48\n\t" "s_cmp_lt_u32 s64, 3\n\t" "s_cselect_b32 s69, -1, s69\n\t" "s_cmp_lg_u32 s69, -1\n\t" \
   "s_cbranch_scc1 50b\n\t" "s_add_u32 s76, s76, 0x1000000\n\t" "s_mov_b32 s73, 0\n\t" "65:\n\t" "s_add_u32 s73, s73, 1\n\t" \
   "s_min_u32 s72, s73, 7\n\t" "s_mul_i32 s72, s72, 5\n\t" "s_add_u32 s72, s72, %[xs]\n\t" "s_and_b32 s72, s72, 7\n\t" "s_lshl_b32 s71, %[xs], 7\n\t" \
   "v_subrev_u32 v109, s71, v139\n\t" "s_lshl_b32 s72, s72, 7\n\t" "s_add_u32 s72, s72, s66\n\t" "s_cmp_lt_u32 s73, 7\n\t" "s_cbranch_scc1 66f\n\t" \
   "s_sleep 4\n\t" "66:\n\t" "buffer_load_dword v108, v109, %[rs], s72 offen sc1\n\t" "s_waitcnt vmcnt(0)\n\t" "v_readfirstlane_b32 s69, v108\n\t" \
   "s_cmp_gt_u32 s73, 0x10000\n\t" "s_cbranch_scc1 50b\n\t" "s_cmp_eq_u32 s69, -1\n\t" "s_cbranch_scc1 65b\n\t" "s_branch 50b\n\t" "8:\n\t" \
   "s_nop 7\n\t" "s_waitcnt vmcnt(6) lgkmcnt(0)\n\t" "s_add_u32 s70, s70, 1\n\t" "s_cmp_gt_u32 s70, 2\n\t" "s_cbranch_scc1 81f\n\t" \
   "s_lshl_b32 s71, s70, 3\n\t" "s_lshl_b32 s71, 1, s71\n\t" "s_add_u32 s76, s76, s71\n\t" "81:\n\t" "v_mov_b32 v48, 0\n\t" "v_mov_b32 v49, 0\n\t" \
   "v_mov_b32 v50, 0\n\t" "v_mov_b32 v51, 0\n\t" "v_mov_b32 v52, 0\n\t" "v_mov_b32 v53, 0\n\t" "v_mov_b32 v54, 0\n\t" "v_mov_b32 v55, 0\n\t" \
   "v_mov_b32 v56, 0\n\t" "v_mov_b32 v57, 0\n\t" "v_mov_b32 v58, 0\n\t" "v_mov_b32 v59, 0\n\t" "v_mov_b32 v60, 0\n\t" "v_mov_b32 v61, 0\n\t" \
   "v_mov_b32 v62, 0\n\t" "v_mov_b32 v63, 0\n\t" "s_min_u32 s71, s70, 7\n\t" "s_lshr_b32 s67, s71, 1\n\t" "s_add_u32 s67, s67, %[xs]\n\t" \
   "s_and_b32 s67, s67, 3\n\t" "s_lshl_b32 s67, s67, 11\n\t" "s_and_b32 s72, s71, 1\n\t" "s_lshl_b32 s72, s72, 14\n\t" "s_add_u32 s67, s67, s72\n\t" \
   "s_add_u32 s67, s67, s66\n\t" "s_cmp_eq_u32 s70, 1\n\t" "s_cselect_b32 s67, s68, s67\n\t" "s_cmp_lt_u32 s70, 7\n\t" "s_cbranch_scc1 20b\n\t" \
   "s_sleep 3\n\t" "s_cmp_lt_u32 s70, 0x20000\n\t" "s_cbranch_scc1 20b\n\t" "s_mov_b32 s67, %[esc]\n\t" "s_mov_b32 s68, %[esc]\n\t" "s_branch 20b\n\t" \
   "27:\n\t" "v_mov_b32 v98, 0x7ffffff0\n\t" "buffer_store_dword v98, v98, %[rs], 0 offen\n\t" "buffer_store_dword v98, v98, %[rs], 0 offen\n\t" \
   "buffer_store_dword v98, v98, %[rs], 0 offen\n\t" "buffer_store_dword v98, v98, %[rs], 0 offen\n\t" \
   "buffer_store_dword v98, v98, %[rs], 0 offen\n\t" "buffer_store_dword v98, v98, %[rs], 0 offen\n\t" "s_branch 28b\n\t" "141:\n\t" \
   "s_mov_b32 s74, 0\n\t" "s_cmp_eq_u32 s64, 0\n\t" "s_cbranch_scc1 41b\n\t" "42:\n\t" "buffer_load_dword v15, v140, %[rs], s59 offen sc1\n\t" \
   "s_add_u32 s74, s74, 1\n\t" "s_waitcnt vmcnt(0)\n\t" "v_cmp_eq_u32 vcc, -1, v15\n\t" "s_cmp_gt_u32 s74, 0x10000\n\t" "s_cbranch_scc1 41b\n\t" \
   "s_cbranch_vccz 41b\n\t" "s_sleep 2\n\t" "s_branch 42b\n\t" "95:\n\t" "s_movk_i32 s59, 0x1000\n\t" "s_branch 197f\n\t" "97:\n\t" \
   "buffer_load_dwordx4 v[16:19], v138, %[rs], s79 offen offset:0 sc1\n\t" "buffer_load_dwordx4 v[20:23], v138, %[rs], s79 offen offset:1024 sc1\n\t" \
   "buffer_load_dwordx4 v[24:27], v138, %[rs], s79 offen offset:2048 sc1\n\t" \
   "buffer_load_dwordx4 v[28:31], v138, %[rs], s79 offen offset:3072 sc1\n\t" "v_or_b32 v108, s49, v141\n\t" "v_or_b32 v109, s49, v142\n\t" \
   "buffer_store_dwordx4 v[44:47], v108, %[rs], s52 offen sc1\n\t" "buffer_store_dwordx2 v[32:33], v109, %[rs], s51 offen sc1\n\t" \
   "s_waitcnt vmcnt(0)\n\t" "s_branch 96b\n\t" "197:\n\t" "s_mov_b32 s74, 0\n\t" "98:\n\t" "buffer_load_dword v15, v140, %[rs], s59 offen sc1\n\t" \
   "s_add_u32 s74, s74, 1\n\t" "s_waitcnt vmcnt(0)\n\t" "v_cmp_eq_u32 vcc, -1, v15\n\t" "s_cmp_gt_u32 s74, 0x10000\n\t" "s_cbranch_scc1 97b\n\t" \
   "s_cbranch_vccz 97b\n\t" "s_sleep 2\n\t" "s_branch 98b\n\t" "92:\n\t" "s_mov_b32 s59, 0\n\t" "s_branch 145f\n\t" "45:\n\t" \
   "v_mbcnt_lo_u32_b32 v98, -1, 0\n\t" "v_mbcnt_hi_u32_b32 v98, -1, v98\n\t" "v_lshlrev_b32 v98, 4, v98\n\t" "s_add_u32 s72, %[kf0], 4096\n\t" \
   "buffer_load_dwordx4 v[160:163], v98, %[rs], %[kf0] offen offset:0 sc1\n\t" \
   "buffer_load_dwordx4 v[164:167], v98, %[rs], %[kf0] offen offset:1024 sc1\n\t" \
   "buffer_load_dwordx4 v[168:171], v98, %[rs], %[kf0] offen offset:2048 sc1\n\t" \
   "buffer_load_dwordx4 v[172:175], v98, %[rs], %[kf0] offen offset:3072 sc1\n\t" \
   "buffer_load_dwordx4 v[176:179], v98, %[rs], s72 offen offset:0 sc1\n\t" "buffer_load_dwordx4 v[180:183], v98, %[rs], s72 offen offset:1024 sc1\n\t" \
   "buffer_load_dwordx4 v[184:187], v98, %[rs], s72 offen offset:2048 sc1\n\t" \
   "buffer_load_dwordx4 v[188:191], v98, %[rs], s72 offen offset:3072 sc1\n\t" "s_waitcnt vmcnt(0)\n\t" \
   "v_mfma_f32_16x16x32_bf16 v[48:51], v[160:163], v[64:67], 0\n\t" "v_mfma_f32_16x16x32_bf16 v[52:55], v[176:179], v[64:67], 0\n\t" \
   "v_mfma_f32_16x16x32_bf16 v[48:51], v[164:167], v[68:71], v[48:51]\n\t" "v_mfma_f32_16x16x32_bf16 v[52:55], v[180:183], v[68:71], v[52:55]\n\t" \
   "v_mfma_f32_16x16x32_bf16 v[48:51], v[168:171], v[72:75], v[48:51]\n\t" "v_mfma_f32_16x16x32_bf16 v[52:55], v[184:187], v[72:75], v[52:55]\n\t" \
   "v_mfma_f32_16x16x32_bf16 v[48:51], v[172:175], v[104:107], v[48:51]\n\t" "v_mfma_f32_16x16x32_bf16 v[52:55], v[188:191], v[104:107], v[52:55]\n\t" \
   "s_nop 7\n\t" "s_branch 93b\n\t" "145:\n\t" "s_mov_b32 s74, 0\n\t" "46:\n\t" "buffer_load_dword v15, v140, %[rs], s59 offen sc1\n\t" \
   "s_add_u32 s74, s74, 1\n\t" "s_waitcnt vmcnt(0)\n\t" "v_cmp_eq_u32 vcc, -1, v15\n\t" "s_cmp_gt_u32 s74, 0x10000\n\t" "s_cbranch_scc1 45b\n\t" \
   "s_cbranch_vccz 45b\n\t" "s_sleep 2\n\t" "s_branch 46b\n\t" "90:\n\t" "v_or_b32 v108, s49, v141\n\t" "v_or_b32 v109, s49, v142\n\t" \
   "buffer_store_dwordx4 v[44:47], v108, %[rs], s52 offen sc1\n\t" "buffer_store_dwordx2 v[32:33], v109, %[rs], s51 offen sc1\n\t" \
   "s_waitcnt vmcnt(0) lgkmcnt(0)"
#define P1_CLOB "v15", "v16", "v17", "v18", "v19", "v20", "v21", "v22", "v23", "v24", "v25", "v26", "v27", "v28", "v29", "v30", "v31", "v32", "v33", "v34", "v35", "v36", "v37", "v38", "v39", "v40", "v41", "v42", "v43", "v44", "v45", "v46", "v47", "v48", "v49", "v50", "v51", "v52", "v53", "v54", "v55", "v56", "v57", "v58", "v59", "v60", "v61", "v62", "v63", "v64", "v65", "v66", "v67", "v68", "v69", "v70", "v71", "v72", "v73", "v74", "v75", "v76", "v77", "v78", "v79", "v80", "v81", "v82", "v83", "v84", "v85", "v86", "v87", "v88", "v89", "v90", "v91", "v92", "v93", "v94", "v95", "v96", "v97", "v98", "v99", "v100", "v101", "v102", "v103", "v104", "v105", "v106", "v107", "v108", "v109", "v110", "v111", "v144", "v145", "v146", "v147", "v148", "v149", "v150", "v151", "v152", "v153", "v154", "v155", "v156", "v157", "v158", "v159", "v160", "v161", "v162", "v163", "v164", "v165", "v166", "v167", "v168", "v169", "v170", "v171", "v172", "v173", "v174", "v175", "v176", "v177", "v178", "v179", "v180", "v181", "v182", "v183", "v184", "v185", "v186", "v187", "v188", "v189", "v190", "v191", "s60", "s61", "s62", "s63", "s64", "s65", "s66", "s67", "s68", "s69", "s70", "s71", "s72", "s73", "s74", "s75", "s47", "s48", "s49", "s50", "s51", "s52", "s54", "s55", "s79", "s20", "s21", "s22", "s23", "s24", "s25", "s26", "s27", "s28", "s29", "s30", "s31", "s32", "s33", "s34", "s35"
#define PV_ASM \
   ".p2align 6\n\t" "s_nop 0\n\t" "s_nop 0\n\t" "s_nop 0\n\t" "s_nop 0\n\t" "s_nop 0\n\t" "s_nop 0\n\t" "s_nop 0\n\t" "s_nop 0\n\t" \
   "s_mov_b32 s64, -1\n\t" "s_mov_b32 s76, 0\n\t" "s_mov_b32 s77, 0\n\t" "s_mov_b32 s71, 63\n\t" "s_mov_b32 s47, 1300\n\t" "s_mov_b32 s70, 0\n\t" \
   "s_mov_b32 s69, 0\n\t" "s_lshr_b64 s[72:73], %[sel], s71\n\t" "s_lshl_b32 s65, s71, 19\n\t" "s_and_b32 s72, s72, 1\n\t" \
   "s_add_u32 s65, s65, %[pool]\n\t" "s_lshl_b32 s72, s72, 13\n\t" "s_add_u32 s65, s65, s72\n\t" "s_lshr_b64 s[72:73], %[s3], s71\n\t" \
   "s_and_b32 s54, s72, 1\n\t" "s_lshl_b32 s54, s54, 14\n\t" "40:\n\t" "s_cmp_lt_i32 s64, 0\n\t" "s_cselect_b32 s72, 0x7ffffff0, 0\n\t" \
   "s_max_i32 s71, s64, 0\n\t" "v_or_b32 v109, s72, v143\n\t" "s_lshl_b32 s71, s71, 12\n\t" "v_mov_b32 v110, 1\n\t" "s_cmp_eq_u32 s64, 63\n\t" \
   "s_cbranch_scc1 70f\n\t" "71:\n\t" "s_waitcnt vmcnt(0)\n\t" RSTAMP(3) "s_barrier\n\t" "buffer_load_dword v108, v139, %[rs], s65 offen sc1\n\t" \
   "buffer_store_dword v110, v109, %[rs], s71 offen sc1\n\t" PVTRACE "s_add_u32 s48, s69, s47\n\t" "s_cmp_eq_u32 s70, 0\n\t" \
   "s_cselect_b32 s48, s48, -1\n\t" "s_mov_b32 s66, s65\n\t" "s_add_i32 s64, s64, 1\n\t" "s_and_b32 s71, s64, 63\n\t" \
   "s_lshr_b64 s[72:73], %[sel], s71\n\t" "s_lshl_b32 s65, s71, 19\n\t" "s_and_b32 s72, s72, 1\n\t" "s_add_u32 s65, s65, %[pool]\n\t" \
   "s_lshl_b32 s72, s72, 13\n\t" "s_add_u32 s65, s65, s72\n\t" "s_xor_b32 s67, s54, %[lad0]\n\t" "s_xor_b32 s68, s67, 0x4000\n\t" \
   "s_add_u32 s67, s67, s66\n\t" "s_add_u32 s68, s68, s66\n\t" "s_lshr_b64 s[72:73], %[s3], s71\n\t" "s_and_b32 s54, s72, 1\n\t" \
   "s_lshl_b32 s54, s54, 14\n\t" "s_mov_b32 s70, 0\n\t" "s_lshr_b64 s[72:73], %[ysel], s71\n\t" "s_mul_i32 s73, s71, 43\n\t" \
   "s_mul_i32 s49, s71, 80\n\t" "s_and_b32 s72, s72, 1\n\t" "s_lshr_b32 s73, s73, 7\n\t" "s_add_u32 s49, s49, s72\n\t" \
   "s_lshl4_add_u32 s49, s73, s49\n\t" "s_lshl_b32 s49, s49, 13\n\t" "s_add_u32 s49, s49, %[yb]\n\t" "s_add_u32 s50, s49, 32768\n\t" \
   "s_add_u32 s51, s49, 65536\n\t" "s_add_u32 s52, s49, 98304\n\t" "s_add_u32 s53, s49, 131072\n\t" "v_mov_b32 v48, 0\n\t" "v_mov_b32 v49, 0\n\t" \
   "v_mov_b32 v50, 0\n\t" "v_mov_b32 v51, 0\n\t" "v_mov_b32 v52, 0\n\t" "v_mov_b32 v53, 0\n\t" "v_mov_b32 v54, 0\n\t" "v_mov_b32 v55, 0\n\t" \
   "v_mov_b32 v144, 0\n\t" "v_mov_b32 v145, 0\n\t" "v_mov_b32 v146, 0\n\t" "v_mov_b32 v147, 0\n\t" "v_mov_b32 v148, 0\n\t" "v_mov_b32 v149, 0\n\t" \
   "v_mov_b32 v150, 0\n\t" "v_mov_b32 v151, 0\n\t" "v_mov_b32 v152, 0\n\t" "v_mov_b32 v153, 0\n\t" "v_mov_b32 v154, 0\n\t" "v_mov_b32 v155, 0\n\t" \
   "v_mov_b32 v156, 0\n\t" "v_mov_b32 v157, 0\n\t" "v_mov_b32 v158, 0\n\t" "v_mov_b32 v159, 0\n\t" "v_mov_b32 v160, 0\n\t" "v_mov_b32 v161, 0\n\t" \
   "v_mov_b32 v162, 0\n\t" "v_mov_b32 v163, 0\n\t" "s_cmp_eq_u32 s64, 64\n\t" "s_cbranch_scc1 90f\n\t" "s_waitcnt vmcnt(1)\n\t" \
   "v_readlane_b32 s69, v108, 0\n\t" "v_readlane_b32 s71, v108, 1\n\t" "s_cmp_eq_u32 s69, -1\n\t" "s_cbranch_scc1 63f\n\t" "s_cmp_eq_u32 s71, -1\n\t" \
   "s_cselect_b32 s47, s47, s71\n\t" "50:\n\t" "s_add_u32 s73, s69, %[m1c]\n\t" RSTAMP(5) "51:\n\t" "s_memrealtime s[60:61]\n\t" \
   "s_memtime s[62:63]\n\t" "s_waitcnt lgkmcnt(0)\n\t" "s_sub_u32 s74, s62, s57\n\t" "s_mul_i32 s74, s74, s58\n\t" "s_lshr_b32 s74, s74, 12\n\t" \
   "s_add_u32 s74, s74, s56\n\t" "s_lshl_b32 s60, s60, 3\n\t" "s_sub_u32 s75, s74, s60\n\t" "s_cmp_lt_u32 s75, 32\n\t" "s_cbranch_scc1 52f\n\t" \
   "s_cmp_lt_i32 s75, 0\n\t" "s_cselect_b32 s61, 0, 31\n\t" "s_add_u32 s74, s60, s61\n\t" "s_sub_i32 s75, s61, s75\n\t" "s_add_i32 s75, s75, 1\n\t" \
   "s_ashr_i32 s75, s75, 1\n\t" "s_min_i32 s75, s75, 8\n\t" "s_max_i32 s75, s75, -8\n\t" "s_mov_b32 s56, s74\n\t" "s_mov_b32 s57, s62\n\t" \
   "s_add_u32 s58, s58, s75\n\t" "52:\n\t" "s_sub_i32 s74, s73, s74\n\t" "s_cmp_le_i32 s74, 150\n\t" "s_cbranch_scc1 53f\n\t" \
   "s_cmp_lt_i32 s74, 260\n\t" "s_cbranch_scc1 51b\n\t" "s_sleep 2\n\t" "s_cmp_lt_i32 s74, 32000\n\t" "s_cbranch_scc1 51b\n\t" "s_mov_b32 s74, 0\n\t" \
   "53:\n\t" "s_sub_i32 s74, s74, 56\n\t" "s_max_i32 s74, s74, 0\n\t" "s_mul_i32 s74, s74, 23\n\t" "s_lshr_b32 s74, s74, 5\n\t" \
   "s_and_b32 s74, s74, -4\n\t" "s_getpc_b64 s[60:61]\n\t" "54:\n\t" "s_sub_u32 s74, 55f-54b, s74\n\t" "s_add_u32 s60, s60, s74\n\t" \
   "s_addc_u32 s61, s61, 0\n\t" "s_setpc_b64 s[60:61]\n\t" "s_nop 3\n\t" "s_nop 3\n\t" "s_nop 3\n\t" "s_nop 3\n\t" "s_nop 3\n\t" "s_nop 3\n\t" \
   "s_nop 3\n\t" "s_nop 3\n\t" "s_nop 3\n\t" "s_nop 3\n\t" "s_nop 3\n\t" "s_nop 3\n\t" "s_nop 3\n\t" "s_nop 3\n\t" "s_nop 3\n\t" "s_nop 3\n\t" \
   "s_nop 3\n\t" "s_nop 3\n\t" "s_nop 3\n\t" "s_nop 3\n\t" "s_nop 3\n\t" "s_nop 3\n\t" "s_nop 3\n\t" "s_nop 3\n\t" "55:\n\t" "20:\n\t" RSTAMP(4) \
   "buffer_load_dwordx4 v[64:67], v128, %[rs], s67 offen sc1\n\t" "buffer_load_dwordx4 v[68:71], v129, %[rs], s67 offen sc1\n\t" \
   "buffer_load_dwordx4 v[72:75], v130, %[rs], s67 offen sc1\n\t" "s_waitcnt vmcnt(2)\n\t" "ds_write_b128 v112, v[64:67]\n\t" \
   "v_smfmac_f32_16x16x64_bf16 v[48:51], v[64:67], a[0:7], v134\n\t" "s_waitcnt vmcnt(1)\n\t" "ds_write_b128 v113, v[68:71]\n\t" \
   "s_waitcnt vmcnt(0)\n\t" "ds_write_b128 v114, v[72:75]\n\t" RSTAMP(7) "v_smfmac_f32_16x16x64_bf16 v[52:55], v[64:67], a[64:71], v134\n\t" \
   "v_max3_u32 v110, v64, v66, v68\n\t" "v_max3_u32 v111, v70, v72, v74\n\t" "v_smfmac_f32_16x16x64_bf16 v[48:51], v[68:71], a[8:15], v134\n\t" \
   "v_max_u32 v110, v110, v111\n\t" "v_cmp_eq_u32 vcc, -1, v110\n\t" "v_smfmac_f32_16x16x64_bf16 v[52:55], v[68:71], a[72:79], v134\n\t" \
   "s_cbranch_vccnz 8f\n\t" "s_waitcnt lgkmcnt(0)\n\t" "s_barrier\n\t" RSTAMP(0) "ds_read_b128 v[76:79], v115\n\t" "ds_read_b128 v[80:83], v116\n\t" \
   "ds_read_b128 v[84:87], v117\n\t" "v_smfmac_f32_16x16x64_bf16 v[48:51], v[72:75], a[16:23], v134\n\t" "ds_read_b128 v[88:91], v118\n\t" \
   "ds_read_b128 v[92:95], v119\n\t" "v_smfmac_f32_16x16x64_bf16 v[52:55], v[72:75], a[80:87], v134\n\t" "ds_read_b128 v[96:99], v120\n\t" \
   "ds_read_b128 v[100:103], v121\n\t" "s_waitcnt lgkmcnt(6)\n\t" "v_smfmac_f32_16x16x64_bf16 v[48:51], v[76:79], a[24:31], v134\n\t" \
   "v_smfmac_f32_16x16x64_bf16 v[52:55], v[76:79], a[88:95], v134\n\t" "s_waitcnt lgkmcnt(5)\n\t" \
   "v_smfmac_f32_16x16x64_bf16 v[48:51], v[80:83], a[32:39], v134\n\t" "v_smfmac_f32_16x16x64_bf16 v[52:55], v[80:83], a[96:103], v134\n\t" \
   "s_waitcnt lgkmcnt(4)\n\t" "v_smfmac_f32_16x16x64_bf16 v[48:51], v[84:87], a[40:47], v134\n\t" \
   "v_smfmac_f32_16x16x64_bf16 v[52:55], v[84:87], a[104:111], v134\n\t" "s_waitcnt lgkmcnt(3)\n\t" \
   "v_smfmac_f32_16x16x64_bf16 v[48:51], v[88:91], a[48:55], v134\n\t" "v_smfmac_f32_16x16x64_bf16 v[52:55], v[88:91], a[112:119], v134\n\t" \
   "s_waitcnt lgkmcnt(0)\n\t" "v_smfmac_f32_16x16x64_bf16 v[48:51], v[92:95], a[56:63], v134\n\t" \
   "v_smfmac_f32_16x16x64_bf16 v[48:51], v[96:99], v[192:199], v134\n\t" "v_smfmac_f32_16x16x64_bf16 v[48:51], v[100:103], v[200:207], v134\n\t" \
   "v_smfmac_f32_16x16x64_bf16 v[52:55], v[92:95], a[120:127], v134\n\t" "v_smfmac_f32_16x16x64_bf16 v[52:55], v[96:99], v[208:215], v134\n\t" \
   "v_smfmac_f32_16x16x64_bf16 v[52:55], v[100:103], v[216:223], v134\n\t" "v_add_f32 v48, v48, v49\n\t" "v_add_f32 v49, v50, v51\n\t" \
   "v_cvt_pk_bf16_f32 v48, v48, v49\n\t" "ds_write_b32 v135, v48 offset:10240\n\t" "s_nop 3\n\t" "v_add_f32 v52, v52, v53\n\t" \
   "v_add_f32 v53, v54, v55\n\t" "v_cvt_pk_bf16_f32 v52, v52, v53\n\t" "ds_write_b32 v135, v52 offset:10496\n\t" "s_waitcnt lgkmcnt(0)\n\t" \
   "s_barrier\n\t" RSTAMP(6) "ds_read_b64_tr_b16 v[16:17], v136 offset:10240\n\t" "ds_read_b64_tr_b16 v[18:19], v136 offset:10496\n\t" \
   "ds_read_b64_tr_b16 v[20:21], v136 offset:10752\n\t" "ds_read_b64_tr_b16 v[22:23], v136 offset:11008\n\t" \
   "ds_read_b64_tr_b16 v[24:25], v136 offset:11264\n\t" "ds_read_b64_tr_b16 v[26:27], v136 offset:11520\n\t" \
   "ds_read_b64_tr_b16 v[28:29], v136 offset:11776\n\t" "ds_read_b64_tr_b16 v[30:31], v136 offset:12032\n\t" "s_waitcnt lgkmcnt(6)\n\t" \
   "v_smfmac_f32_16x16x64_bf16 v[144:147], v[16:19], a[128:135], v131\n\t" "v_smfmac_f32_16x16x64_bf16 v[148:151], v[16:19], a[160:167], v131\n\t" \
   "v_smfmac_f32_16x16x64_bf16 v[152:155], v[16:19], a[192:199], v131\n\t" "v_smfmac_f32_16x16x64_bf16 v[156:159], v[16:19], a[224:231], v131\n\t" \
   "v_smfmac_f32_16x16x64_bf16 v[160:163], v[16:19], v[224:231], v131\n\t" "s_waitcnt lgkmcnt(0)\n\t" \
   "v_smfmac_f32_16x16x64_bf16 v[144:147], v[20:23], a[136:143], v131\n\t" "v_smfmac_f32_16x16x64_bf16 v[144:147], v[24:27], a[144:151], v131\n\t" \
   "v_smfmac_f32_16x16x64_bf16 v[144:147], v[28:31], a[152:159], v131\n\t" "v_smfmac_f32_16x16x64_bf16 v[148:151], v[20:23], a[168:175], v131\n\t" \
   "v_smfmac_f32_16x16x64_bf16 v[148:151], v[24:27], a[176:183], v131\n\t" "v_smfmac_f32_16x16x64_bf16 v[148:151], v[28:31], a[184:191], v131\n\t" \
   "v_smfmac_f32_16x16x64_bf16 v[152:155], v[20:23], a[200:207], v131\n\t" "v_cvt_pk_bf16_f32 v164, v144, v145\n\t" \
   "v_cvt_pk_bf16_f32 v165, v146, v147\n\t" "v_smfmac_f32_16x16x64_bf16 v[152:155], v[24:27], a[208:215], v131\n\t" "v_min_u32 v164, -2, v164\n\t" \
   "buffer_store_dwordx2 v[164:165], v132, %[rs], s49 offen sc1\n\t" "v_smfmac_f32_16x16x64_bf16 v[152:155], v[28:31], a[216:223], v131\n\t" \
   "v_smfmac_f32_16x16x64_bf16 v[156:159], v[20:23], a[232:239], v131\n\t" "v_cvt_pk_bf16_f32 v166, v148, v149\n\t" \
   "v_cvt_pk_bf16_f32 v167, v150, v151\n\t" "v_smfmac_f32_16x16x64_bf16 v[156:159], v[24:27], a[240:247], v131\n\t" "v_min_u32 v166, -2, v166\n\t" \
   "buffer_store_dwordx2 v[166:167], v132, %[rs], s50 offen sc1\n\t" "v_smfmac_f32_16x16x64_bf16 v[156:159], v[28:31], a[248:255], v131\n\t" \
   "v_smfmac_f32_16x16x64_bf16 v[160:163], v[20:23], v[232:239], v131\n\t" "v_cvt_pk_bf16_f32 v168, v152, v153\n\t" \
   "v_cvt_pk_bf16_f32 v169, v154, v155\n\t" "v_smfmac_f32_16x16x64_bf16 v[160:163], v[24:27], v[240:247], v131\n\t" "v_min_u32 v168, -2, v168\n\t" \
   "buffer_store_dwordx2 v[168:169], v132, %[rs], s51 offen sc1\n\t" "v_smfmac_f32_16x16x64_bf16 v[160:163], v[28:31], v[248:255], v131\n\t" \
   "v_cvt_pk_bf16_f32 v170, v156, v157\n\t" "v_cvt_pk_bf16_f32 v171, v158, v159\n\t" "v_min_u32 v170, -2, v170\n\t" \
   "buffer_store_dwordx2 v[170:171], v132, %[rs], s52 offen sc1\n\t" "s_nop 3\n\t" "v_cvt_pk_bf16_f32 v172, v160, v161\n\t" \
   "v_cvt_pk_bf16_f32 v173, v162, v163\n\t" "v_min_u32 v172, -2, v172\n\t" "buffer_store_dwordx2 v[172:173], v132, %[rs], s53 offen sc1\n\t" RSTAMP(2) \
   "s_branch 40b\n\t" "70:\n\t" "s_mov_b32 s46, 0\n\t" "s_lshr_b64 s[72:73], %[sel], s46\n\t" "s_lshl_b32 s45, s46, 19\n\t" "s_and_b32 s72, s72, 1\n\t" \
   "s_add_u32 s45, s45, %[pool]\n\t" "s_lshl_b32 s72, s72, 13\n\t" "s_add_u32 s45, s45, s72\n\t" "v_mov_b32 v76, -1\n\t" "v_mov_b32 v77, -1\n\t" \
   "v_mov_b32 v78, -1\n\t" "v_mov_b32 v79, -1\n\t" "buffer_store_dwordx4 v[76:79], v133, %[rs], s45 offen sc1\n\t" \
   "buffer_store_dwordx4 v[76:79], v133, %[rs], s45 offen offset:1024 sc1\n\t" "s_branch 71b\n\t" "63:\n\t" "s_cmp_lg_u32 s64, 0\n\t" \
   "s_cbranch_scc1 64f\n\t" "s_sub_u32 s69, s56, 0x10000\n\t" "s_and_b32 s69, s69, -2\n\t" "s_branch 50b\n\t" "64:\n\t" "s_mov_b32 s69, s48\n\t" \
   "s_cmp_lt_u32 s64, 3\n\t" "s_cselect_b32 s69, -1, s69\n\t" "s_cmp_lg_u32 s69, -1\n\t" "s_cbranch_scc1 50b\n\t" "s_add_u32 s76, s76, 0x1000000\n\t" \
   "s_mov_b32 s73, 0\n\t" "65:\n\t" "s_add_u32 s73, s73, 1\n\t" "s_sleep 6\n\t" "s_and_b32 s72, s73, 3\n\t" "s_add_u32 s72, s72, 8\n\t" \
   "s_sub_u32 s72, s72, %[xs]\n\t" "s_lshl_b32 s72, s72, 7\n\t" "s_add_u32 s72, s72, s66\n\t" "buffer_load_dword v108, v139, %[rs], s72 offen sc1\n\t" \
   "s_waitcnt vmcnt(0)\n\t" "v_readfirstlane_b32 s69, v108\n\t" "s_cmp_gt_u32 s73, 0x10000\n\t" "s_cbranch_scc1 50b\n\t" "s_cmp_eq_u32 s69, -1\n\t" \
   "s_cbranch_scc1 65b\n\t" "s_branch 50b\n\t" "8:\n\t" "s_nop 7\n\t" "s_waitcnt lgkmcnt(0)\n\t" "s_add_u32 s70, s70, 1\n\t" "s_cmp_gt_u32 s70, 2\n\t" \
   "s_cbranch_scc1 81f\n\t" "s_lshl_b32 s71, s70, 3\n\t" "s_lshl_b32 s71, 1, s71\n\t" "s_add_u32 s76, s76, s71\n\t" "81:\n\t" "v_mov_b32 v48, 0\n\t" \
   "v_mov_b32 v49, 0\n\t" "v_mov_b32 v50, 0\n\t" "v_mov_b32 v51, 0\n\t" "v_mov_b32 v52, 0\n\t" "v_mov_b32 v53, 0\n\t" "v_mov_b32 v54, 0\n\t" \
   "v_mov_b32 v55, 0\n\t" "s_add_u32 s71, s70, 1\n\t" "s_min_u32 s71, s71, 7\n\t" "s_lshr_b32 s67, s71, 1\n\t" "s_add_u32 s67, s67, %[xs]\n\t" \
   "s_and_b32 s67, s67, 3\n\t" "s_lshl_b32 s67, s67, 11\n\t" "s_and_b32 s72, s71, 1\n\t" "s_lshl_b32 s72, s72, 14\n\t" "s_add_u32 s67, s67, s72\n\t" \
   "s_add_u32 s67, s67, s66\n\t" "s_cmp_lt_u32 s70, 6\n\t" "s_cbranch_scc1 20b\n\t" "s_sleep 3\n\t" "s_cmp_lt_u32 s70, 0x20000\n\t" \
   "s_cbranch_scc1 20b\n\t" "s_mov_b32 s67, %[esc]\n\t" "s_branch 20b\n\t" "90:\n\t" "s_waitcnt vmcnt(0) lgkmcnt(0)"
#define PV_CLOB "v16", "v17", "v18", "v19", "v20", "v21", "v22", "v23", "v24", "v25", "v26", "v27", "v28", "v29", "v30", "v31", "v48", "v49", "v50", "v51", "v52", "v53", "v54", "v55", "v56", "v57", "v58", "v59", "v60", "v61", "v62", "v63", "v64", "v65", "v66", "v67", "v68", "v69", "v70", "v71", "v72", "v73", "v74", "v75", "v76", "v77", "v78", "v79", "v80", "v81", "v82", "v83", "v84", "v85", "v86", "v87", "v88", "v89", "v90", "v91", "v92", "v93", "v94", "v95", "v96", "v97", "v98", "v99", "v100", "v101", "v102", "v103", "v104", "v105", "v106", "v107", "v108", "v109", "v110", "v111", "v144", "v145", "v146", "v147", "v148", "v149", "v150", "v151", "v152", "v153", "v154", "v155", "v156", "v157", "v158", "v159", "v160", "v161", "v162", "v163", "v164", "v165", "v166", "v167", "v168", "v169", "v170", "v171", "v172", "v173", "v174", "v175", "s60", "s61", "s62", "s63", "s64", "s65", "s66", "s67", "s68", "s69", "s70", "s71", "s72", "s73", "s74", "s75", "s45", "s46", "s47", "s48", "s49", "s50", "s51", "s52", "s53", "s54"
#define WAIT_ASM \
   "11:\n\t" "s_memrealtime s[60:61]\n\t" "s_memtime s[62:63]\n\t" "s_waitcnt lgkmcnt(0)\n\t" "s_sub_u32 s74, s62, s57\n\t" \
   "s_mul_i32 s74, s74, s58\n\t" "s_lshr_b32 s74, s74, 12\n\t" "s_add_u32 s74, s74, s56\n\t" "s_lshl_b32 s60, s60, 3\n\t" "s_sub_u32 s75, s74, s60\n\t" \
   "s_cmp_lt_u32 s75, 32\n\t" "s_cbranch_scc1 12f\n\t" "s_cmp_lt_i32 s75, 0\n\t" "s_cselect_b32 s61, 0, 31\n\t" "s_add_u32 s74, s60, s61\n\t" \
   "s_sub_i32 s75, s61, s75\n\t" "s_add_i32 s75, s75, 1\n\t" "s_ashr_i32 s75, s75, 1\n\t" "s_min_i32 s75, s75, 8\n\t" "s_max_i32 s75, s75, -8\n\t" \
   "s_mov_b32 s56, s74\n\t" "s_mov_b32 s57, s62\n\t" "s_add_u32 s58, s58, s75\n\t" "12:\n\t" "s_sub_i32 s74, %[ta], s74\n\t" \
   "s_cmp_le_i32 s74, 150\n\t" "s_cbranch_scc1 13f\n\t" "s_cmp_lt_i32 s74, 260\n\t" "s_cbranch_scc1 11b\n\t" "s_sleep 2\n\t" \
   "s_cmp_lt_i32 s74, 32000\n\t" "s_cbranch_scc1 11b\n\t" "s_mov_b32 s74, 0\n\t" "13:\n\t" "s_sub_i32 s74, s74, 56\n\t" "s_max_i32 s74, s74, 0\n\t" \
   "s_mul_i32 s74, s74, 23\n\t" "s_lshr_b32 s74, s74, 5\n\t" "s_and_b32 s74, s74, -4\n\t" "s_getpc_b64 s[60:61]\n\t" "14:\n\t" \
   "s_sub_u32 s74, 15f-14b, s74\n\t" "s_add_u32 s60, s60, s74\n\t" "s_addc_u32 s61, s61, 0\n\t" "s_setpc_b64 s[60:61]\n\t" "s_nop 3\n\t" "s_nop 3\n\t" \
   "s_nop 3\n\t" "s_nop 3\n\t" "s_nop 3\n\t" "s_nop 3\n\t" "s_nop 3\n\t" "s_nop 3\n\t" "s_nop 3\n\t" "s_nop 3\n\t" "s_nop 3\n\t" "s_nop 3\n\t" \
   "s_nop 3\n\t" "s_nop 3\n\t" "s_nop 3\n\t" "s_nop 3\n\t" "s_nop 3\n\t" "s_nop 3\n\t" "s_nop 3\n\t" "s_nop 3\n\t" "s_nop 3\n\t" "s_nop 3\n\t" \
   "s_nop 3\n\t" "s_nop 3\n\t" "15:\n\t" ""
#define WAIT_CLOB "s60", "s61", "s62", "s63", "s74", "s75"
#define FIN_ASM \
   ".p2align 6\n\t" "s_nop 0\n\t" "s_nop 0\n\t" "s_nop 0\n\t" "s_nop 0\n\t" "s_nop 0\n\t" "s_nop 0\n\t" "s_nop 0\n\t" "s_nop 0\n\t" "s_nop 0\n\t" \
   "s_nop 0\n\t" "s_nop 0\n\t" "s_mov_b32 s40, 0\n\t" "s_mov_b32 s76, 0\n\t" "s_mov_b32 s77, 0\n\t" "s_sub_u32 s45, s56, 0x10000\n\t" \
   "v_mov_b32 v13, -2\n\t" "v_mov_b32 v11, -1\n\t" "30:\n\t" "s_lshr_b64 s[72:73], %[sel], s40\n\t" "s_lshl_b32 s41, s40, 19\n\t" \
   "s_and_b32 s72, s72, 1\n\t" "s_add_u32 s41, s41, %[pool]\n\t" "s_lshl_b32 s72, s72, 13\n\t" "s_add_u32 s41, s41, s72\n\t" \
   "s_lshr_b64 s[72:73], %[s3], s40\n\t" "s_and_b32 s47, s72, 1\n\t" "s_lshl_b32 s47, s47, 14\n\t" "s_mul_i32 s49, s40, 1310720\n\t" \
   "s_add_u32 s49, s49, %[su]\n\t" "s_lshr_b64 s[72:73], %[ysel], s40\n\t" "s_mul_i32 s73, s40, 43\n\t" "s_mul_i32 s50, s40, 80\n\t" \
   "s_and_b32 s72, s72, 1\n\t" "s_lshr_b32 s73, s73, 7\n\t" "s_add_u32 s50, s50, s72\n\t" "s_lshl4_add_u32 s50, s73, s50\n\t" \
   "s_lshl_b32 s50, s50, 13\n\t" "s_add_u32 s50, s50, %[yb]\n\t" "s_add_u32 s51, s50, 4096\n\t" "s_lshl_b32 s52, s40, 12\n\t" \
   "s_add_u32 s52, s52, %[uf]\n\t" "s_add_u32 s69, s45, %[tu]\n\t" "v_max3_u32 v15, v8, v9, v10\n\t" "v_max_u32 v15, v15, v11\n\t" \
   "v_cmp_eq_u32 vcc, -1, v15\n\t" "s_add_u32 s40, s40, 1\n\t" "s_cbranch_vccnz 95f\n\t" "96:\n\t" "11:\n\t" "s_memrealtime s[60:61]\n\t" \
   "s_memtime s[62:63]\n\t" "s_waitcnt lgkmcnt(0)\n\t" "s_sub_u32 s74, s62, s57\n\t" "s_mul_i32 s74, s74, s58\n\t" "s_lshr_b32 s74, s74, 12\n\t" \
   "s_add_u32 s74, s74, s56\n\t" "s_lshl_b32 s60, s60, 3\n\t" "s_sub_u32 s75, s74, s60\n\t" "s_cmp_lt_u32 s75, 32\n\t" "s_cbranch_scc1 12f\n\t" \
   "s_cmp_lt_i32 s75, 0\n\t" "s_cselect_b32 s61, 0, 31\n\t" "s_add_u32 s74, s60, s61\n\t" "s_sub_i32 s75, s61, s75\n\t" "s_add_i32 s75, s75, 1\n\t" \
   "s_ashr_i32 s75, s75, 1\n\t" "s_min_i32 s75, s75, 8\n\t" "s_max_i32 s75, s75, -8\n\t" "s_mov_b32 s56, s74\n\t" "s_mov_b32 s57, s62\n\t" \
   "s_add_u32 s58, s58, s75\n\t" "12:\n\t" "s_sub_i32 s74, s69, s74\n\t" "s_cmp_le_i32 s74, 150\n\t" "s_cbranch_scc1 13f\n\t" \
   "s_cmp_lt_i32 s74, 260\n\t" "s_cbranch_scc1 11b\n\t" "s_sleep 2\n\t" "s_cmp_lt_i32 s74, 32000\n\t" "s_cbranch_scc1 11b\n\t" "s_mov_b32 s74, 0\n\t" \
   "13:\n\t" "s_sub_i32 s74, s74, 56\n\t" "s_max_i32 s74, s74, 0\n\t" "s_mul_i32 s74, s74, 23\n\t" "s_lshr_b32 s74, s74, 5\n\t" \
   "s_and_b32 s74, s74, -4\n\t" "s_getpc_b64 s[60:61]\n\t" "14:\n\t" "s_sub_u32 s74, 15f-14b, s74\n\t" "s_add_u32 s60, s60, s74\n\t" \
   "s_addc_u32 s61, s61, 0\n\t" "s_setpc_b64 s[60:61]\n\t" "s_nop 3\n\t" "s_nop 3\n\t" "s_nop 3\n\t" "s_nop 3\n\t" "s_nop 3\n\t" "s_nop 3\n\t" \
   "s_nop 3\n\t" "s_nop 3\n\t" "s_nop 3\n\t" "s_nop 3\n\t" "s_nop 3\n\t" "s_nop 3\n\t" "s_nop 3\n\t" "s_nop 3\n\t" "s_nop 3\n\t" "s_nop 3\n\t" \
   "s_nop 3\n\t" "s_nop 3\n\t" "s_nop 3\n\t" "s_nop 3\n\t" "s_nop 3\n\t" "s_nop 3\n\t" "s_nop 3\n\t" "s_nop 3\n\t" "15:\n\t" RSTAMP(4) \
   "buffer_load_dwordx4 a[0:3], v16, %[rs], s49 offen offset:0 sc1\n\t" "s_nop 0\n\t" \
   "buffer_load_dwordx4 a[4:7], v16, %[rs], s49 offen offset:1024 sc1\n\t" "s_nop 0\n\t" \
   "buffer_load_dwordx4 a[8:11], v16, %[rs], s49 offen offset:2048 sc1\n\t" "s_nop 0\n\t" \
   "buffer_load_dwordx4 a[12:15], v16, %[rs], s49 offen offset:3072 sc1\n\t" "s_nop 0\n\t" "s_add_u32 s49, s49, 4096\n\t" \
   "buffer_load_dwordx4 a[16:19], v16, %[rs], s49 offen offset:0 sc1\n\t" "s_nop 0\n\t" \
   "buffer_load_dwordx4 a[20:23], v16, %[rs], s49 offen offset:1024 sc1\n\t" "s_nop 0\n\t" \
   "buffer_load_dwordx4 a[24:27], v16, %[rs], s49 offen offset:2048 sc1\n\t" "s_nop 0\n\t" \
   "buffer_load_dwordx4 a[28:31], v16, %[rs], s49 offen offset:3072 sc1\n\t" "s_nop 0\n\t" "s_add_u32 s49, s49, 4096\n\t" \
   "buffer_load_dwordx4 a[32:35], v16, %[rs], s49 offen offset:0 sc1\n\t" "s_nop 0\n\t" \
   "buffer_load_dwordx4 a[36:39], v16, %[rs], s49 offen offset:1024 sc1\n\t" "s_nop 0\n\t" \
   "buffer_load_dwordx4 a[40:43], v16, %[rs], s49 offen offset:2048 sc1\n\t" "s_nop 0\n\t" \
   "buffer_load_dwordx4 a[44:47], v16, %[rs], s49 offen offset:3072 sc1\n\t" "s_nop 0\n\t" "s_add_u32 s49, s49, 4096\n\t" \
   "buffer_load_dwordx4 a[48:51], v16, %[rs], s49 offen offset:0 sc1\n\t" "s_nop 0\n\t" \
   "buffer_load_dwordx4 a[52:55], v16, %[rs], s49 offen offset:1024 sc1\n\t" "s_nop 0\n\t" \
   "buffer_load_dwordx4 a[56:59], v16, %[rs], s49 offen offset:2048 sc1\n\t" "s_nop 0\n\t" \
   "buffer_load_dwordx4 a[60:63], v16, %[rs], s49 offen offset:3072 sc1\n\t" "s_nop 0\n\t" "s_add_u32 s49, s49, 4096\n\t" \
   "buffer_load_dwordx4 a[64:67], v16, %[rs], s49 offen offset:0 sc1\n\t" "s_nop 0\n\t" \
   "buffer_load_dwordx4 a[68:71], v16, %[rs], s49 offen offset:1024 sc1\n\t" "s_nop 0\n\t" \
   "buffer_load_dwordx4 a[72:75], v16, %[rs], s49 offen offset:2048 sc1\n\t" "s_nop 0\n\t" \
   "buffer_load_dwordx4 a[76:79], v16, %[rs], s49 offen offset:3072 sc1\n\t" "s_nop 0\n\t" "s_add_u32 s49, s49, 4096\n\t" \
   "buffer_load_dwordx4 a[80:83], v16, %[rs], s49 offen offset:0 sc1\n\t" "s_nop 0\n\t" \
   "buffer_load_dwordx4 a[84:87], v16, %[rs], s49 offen offset:1024 sc1\n\t" "s_nop 0\n\t" \
   "buffer_load_dwordx4 a[88:91], v16, %[rs], s49 offen offset:2048 sc1\n\t" "s_nop 0\n\t" \
   "buffer_load_dwordx4 a[92:95], v16, %[rs], s49 offen offset:3072 sc1\n\t" "s_nop 0\n\t" "s_add_u32 s49, s49, 4096\n\t" \
   "buffer_load_dwordx4 a[96:99], v16, %[rs], s49 offen offset:0 sc1\n\t" "s_nop 0\n\t" \
   "buffer_load_dwordx4 a[100:103], v16, %[rs], s49 offen offset:1024 sc1\n\t" "s_nop 0\n\t" \
   "buffer_load_dwordx4 a[104:107], v16, %[rs], s49 offen offset:2048 sc1\n\t" "s_nop 0\n\t" \
   "buffer_load_dwordx4 a[108:111], v16, %[rs], s49 offen offset:3072 sc1\n\t" "s_nop 0\n\t" "s_add_u32 s49, s49, 4096\n\t" \
   "buffer_load_dwordx4 a[112:115], v16, %[rs], s49 offen offset:0 sc1\n\t" "s_nop 0\n\t" \
   "buffer_load_dwordx4 a[116:119], v16, %[rs], s49 offen offset:1024 sc1\n\t" "s_nop 0\n\t" \
   "buffer_load_dwordx4 a[120:123], v16, %[rs], s49 offen offset:2048 sc1\n\t" "s_nop 0\n\t" \
   "buffer_load_dwordx4 a[124:127], v16, %[rs], s49 offen offset:3072 sc1\n\t" "s_nop 0\n\t" "s_add_u32 s52, s52, 4096\n\t" \
   "buffer_load_dwordx4 v[8:11], v19, %[rs], s52 offen sc1\n\t" RSTAMP(0) "s_mov_b32 s46, 0\n\t" "s_xor_b32 s64, s47, %[lad0]\n\t" \
   "s_and_b32 s65, s64, 0x4000\n\t" "s_xor_b32 s64, s64, s65\n\t" "v_or_b32 v224, s65, v16\n\t" "v_or_b32 v225, s65, v17\n\t" \
   "v_xor_b32 v226, 0x4000, v224\n\t" "v_xor_b32 v227, 0x4000, v225\n\t" "s_add_u32 s64, s64, s41\n\t" "v_xor_b32 v14, s47, v20\n\t" \
   "v_xor_b32 v15, 0x4000, v14\n\t" "s_add_u32 s65, s64, 32768\n\t" "s_add_u32 s66, s64, 65536\n\t" "s_add_u32 s67, s64, 98304\n\t" \
   "s_add_u32 s68, s64, 131072\n\t" "s_add_u32 s69, s64, 163840\n\t" "s_add_u32 s70, s64, 196608\n\t" "s_add_u32 s71, s64, 229376\n\t" \
   "s_add_u32 s72, s64, 262144\n\t" "s_add_u32 s73, s64, 294912\n\t" "s_add_u32 s53, s41, 4096\n\t" "v_mov_b32 v28, 0\n\t" "v_mov_b32 v29, 0\n\t" \
   "v_mov_b32 v30, 0\n\t" "v_mov_b32 v31, 0\n\t" "19:\n\t" "ds_read_b128 v[24:27], v23\n\t" "s_waitcnt lgkmcnt(0)\n\t" \
   "v_readfirstlane_b32 s74, v26\n\t" "v_readfirstlane_b32 s44, v24\n\t" "v_readfirstlane_b32 s45, v25\n\t" "s_sub_u32 s48, s44, 110\n\t" \
   "s_cmp_lt_u32 s74, s40\n\t" "s_cbranch_scc1 18f\n\t" RSTAMP(7) "21:\n\t" "s_memrealtime s[60:61]\n\t" "s_memtime s[62:63]\n\t" \
   "s_waitcnt lgkmcnt(0)\n\t" "s_sub_u32 s74, s62, s57\n\t" "s_mul_i32 s74, s74, s58\n\t" "s_lshr_b32 s74, s74, 12\n\t" "s_add_u32 s74, s74, s56\n\t" \
   "s_lshl_b32 s60, s60, 3\n\t" "s_sub_u32 s75, s74, s60\n\t" "s_cmp_lt_u32 s75, 32\n\t" "s_cbranch_scc1 22f\n\t" "s_cmp_lt_i32 s75, 0\n\t" \
   "s_cselect_b32 s61, 0, 31\n\t" "s_add_u32 s74, s60, s61\n\t" "s_sub_i32 s75, s61, s75\n\t" "s_add_i32 s75, s75, 1\n\t" "s_ashr_i32 s75, s75, 1\n\t" \
   "s_min_i32 s75, s75, 8\n\t" "s_max_i32 s75, s75, -8\n\t" "s_mov_b32 s56, s74\n\t" "s_mov_b32 s57, s62\n\t" "s_add_u32 s58, s58, s75\n\t" "22:\n\t" \
   "s_sub_i32 s74, s48, s74\n\t" "s_cmp_le_i32 s74, 150\n\t" "s_cbranch_scc1 23f\n\t" "s_cmp_lt_i32 s74, 260\n\t" "s_cbranch_scc1 21b\n\t" \
   "s_sleep 2\n\t" "s_cmp_lt_i32 s74, 32000\n\t" "s_cbranch_scc1 21b\n\t" "s_mov_b32 s74, 0\n\t" "23:\n\t" "s_sub_i32 s74, s74, 56\n\t" \
   "s_max_i32 s74, s74, 0\n\t" "s_mul_i32 s74, s74, 23\n\t" "s_lshr_b32 s74, s74, 5\n\t" "s_and_b32 s74, s74, -4\n\t" "s_getpc_b64 s[60:61]\n\t" \
   "24:\n\t" "s_sub_u32 s74, 25f-24b, s74\n\t" "s_add_u32 s60, s60, s74\n\t" "s_addc_u32 s61, s61, 0\n\t" "s_setpc_b64 s[60:61]\n\t" "s_nop 3\n\t" \
   "s_nop 3\n\t" "s_nop 3\n\t" "s_nop 3\n\t" "s_nop 3\n\t" "s_nop 3\n\t" "s_nop 3\n\t" "s_nop 3\n\t" "s_nop 3\n\t" "s_nop 3\n\t" "s_nop 3\n\t" \
   "s_nop 3\n\t" "s_nop 3\n\t" "s_nop 3\n\t" "s_nop 3\n\t" "s_nop 3\n\t" "s_nop 3\n\t" "s_nop 3\n\t" "s_nop 3\n\t" "s_nop 3\n\t" "s_nop 3\n\t" \
   "s_nop 3\n\t" "s_nop 3\n\t" "s_nop 3\n\t" "25:\n\t" "buffer_load_dwordx4 v[32:35], v16, %[rs], s50 offen offset:0 sc1\n\t" \
   "buffer_load_dwordx4 v[36:39], v16, %[rs], s50 offen offset:1024 sc1\n\t" "buffer_load_dwordx4 v[40:43], v16, %[rs], s50 offen offset:2048 sc1\n\t" \
   "buffer_load_dwordx4 v[44:47], v16, %[rs], s50 offen offset:3072 sc1\n\t" "buffer_load_dwordx4 v[48:51], v16, %[rs], s51 offen offset:0 sc1\n\t" \
   "buffer_load_dwordx4 v[52:55], v16, %[rs], s51 offen offset:1024 sc1\n\t" "buffer_load_dwordx4 v[56:59], v16, %[rs], s51 offen offset:2048 sc1\n\t" \
   "buffer_load_dwordx4 v[60:63], v16, %[rs], s51 offen offset:3072 sc1\n\t" "s_nop 3\n\t" "s_nop 3\n\t" "s_nop 3\n\t" "s_nop 3\n\t" "s_nop 3\n\t" \
   "s_nop 3\n\t" "s_nop 3\n\t" "s_nop 3\n\t" "s_nop 3\n\t" "s_nop 3\n\t" "s_nop 3\n\t" "s_nop 3\n\t" "s_nop 3\n\t" "s_nop 3\n\t" "s_nop 3\n\t" \
   "s_nop 3\n\t" RSTAMP(6) "buffer_load_dwordx4 v[64:67], v224, %[rs], s64 offen offset:0 sc1\n\t" \
   "buffer_load_dwordx4 v[68:71], v224, %[rs], s64 offen offset:1024 sc1\n\t" "buffer_load_dwordx4 v[72:75], v224, %[rs], s65 offen offset:0 sc1\n\t" \
   "buffer_load_dwordx4 v[76:79], v224, %[rs], s65 offen offset:1024 sc1\n\t" "buffer_load_dwordx4 v[80:83], v224, %[rs], s66 offen offset:0 sc1\n\t" \
   "buffer_load_dwordx4 v[84:87], v224, %[rs], s66 offen offset:1024 sc1\n\t" "buffer_load_dwordx4 v[88:91], v224, %[rs], s67 offen offset:0 sc1\n\t" \
   "buffer_load_dwordx4 v[92:95], v224, %[rs], s67 offen offset:1024 sc1\n\t" "buffer_load_dwordx4 v[96:99], v224, %[rs], s68 offen offset:0 sc1\n\t" \
   "buffer_load_dwordx4 v[100:103], v224, %[rs], s68 offen offset:1024 sc1\n\t" \
   "buffer_load_dwordx4 v[104:107], v224, %[rs], s69 offen offset:0 sc1\n\t" \
   "buffer_load_dwordx4 v[108:111], v224, %[rs], s69 offen offset:1024 sc1\n\t" \
   "buffer_load_dwordx4 v[112:115], v224, %[rs], s70 offen offset:0 sc1\n\t" \
   "buffer_load_dwordx4 v[116:119], v224, %[rs], s70 offen offset:1024 sc1\n\t" \
   "buffer_load_dwordx4 v[120:123], v224, %[rs], s71 offen offset:0 sc1\n\t" \
   "buffer_load_dwordx4 v[124:127], v224, %[rs], s71 offen offset:1024 sc1\n\t" \
   "buffer_load_dwordx2 v[128:129], v225, %[rs], s72 offen offset:0 sc1\n\t" \
   "buffer_load_dwordx2 v[130:131], v225, %[rs], s72 offen offset:128 sc1\n\t" \
   "buffer_load_dwordx2 v[132:133], v225, %[rs], s72 offen offset:1024 sc1\n\t" \
   "buffer_load_dwordx2 v[134:135], v225, %[rs], s72 offen offset:1152 sc1\n\t" \
   "buffer_load_dwordx2 v[136:137], v225, %[rs], s73 offen offset:0 sc1\n\t" \
   "buffer_load_dwordx2 v[138:139], v225, %[rs], s73 offen offset:128 sc1\n\t" \
   "buffer_load_dwordx2 v[140:141], v225, %[rs], s73 offen offset:1024 sc1\n\t" \
   "buffer_load_dwordx2 v[142:143], v225, %[rs], s73 offen offset:1152 sc1\n\t" YSTAMP(24) "s_waitcnt vmcnt(23)\n\t" RSTAMP(1) \
   "v_smfmac_f32_16x16x64_bf16 v[28:31], v[64:67], a[0:7], v22\n\t" "s_waitcnt vmcnt(22)\n\t" \
   "v_smfmac_f32_16x16x64_bf16 v[28:31], v[68:71], a[8:15], v22\n\t" "s_waitcnt vmcnt(21)\n\t" \
   "v_smfmac_f32_16x16x64_bf16 v[28:31], v[72:75], a[16:23], v22\n\t" "s_waitcnt vmcnt(20)\n\t" \
   "v_smfmac_f32_16x16x64_bf16 v[28:31], v[76:79], a[24:31], v22\n\t" "s_waitcnt vmcnt(19)\n\t" \
   "v_smfmac_f32_16x16x64_bf16 v[28:31], v[80:83], a[32:39], v22\n\t" "s_waitcnt vmcnt(18)\n\t" \
   "v_smfmac_f32_16x16x64_bf16 v[28:31], v[84:87], a[40:47], v22\n\t" "s_waitcnt vmcnt(17)\n\t" \
   "v_smfmac_f32_16x16x64_bf16 v[28:31], v[88:91], a[48:55], v22\n\t" "s_waitcnt vmcnt(16)\n\t" \
   "v_smfmac_f32_16x16x64_bf16 v[28:31], v[92:95], a[56:63], v22\n\t" "s_waitcnt vmcnt(15)\n\t" \
   "v_smfmac_f32_16x16x64_bf16 v[28:31], v[96:99], a[64:71], v22\n\t" "s_waitcnt vmcnt(14)\n\t" \
   "v_smfmac_f32_16x16x64_bf16 v[28:31], v[100:103], a[72:79], v22\n\t" "s_waitcnt vmcnt(13)\n\t" \
   "v_smfmac_f32_16x16x64_bf16 v[28:31], v[104:107], a[80:87], v22\n\t" "s_waitcnt vmcnt(12)\n\t" \
   "v_smfmac_f32_16x16x64_bf16 v[28:31], v[108:111], a[88:95], v22\n\t" "s_waitcnt vmcnt(11)\n\t" \
   "v_smfmac_f32_16x16x64_bf16 v[28:31], v[112:115], a[96:103], v22\n\t" "s_waitcnt vmcnt(10)\n\t" \
   "v_smfmac_f32_16x16x64_bf16 v[28:31], v[116:119], a[104:111], v22\n\t" "s_waitcnt vmcnt(9)\n\t" \
   "v_smfmac_f32_16x16x64_bf16 v[28:31], v[120:123], a[112:119], v22\n\t" "s_waitcnt vmcnt(8)\n\t" \
   "v_smfmac_f32_16x16x64_bf16 v[28:31], v[124:127], a[120:127], v22\n\t" "s_waitcnt vmcnt(6)\n\t" \
   "v_smfmac_f32_16x16x64_bf16 v[28:31], v[128:131], v[32:39], v22\n\t" "s_waitcnt vmcnt(4)\n\t" \
   "v_smfmac_f32_16x16x64_bf16 v[28:31], v[132:135], v[40:47], v22\n\t" "s_waitcnt vmcnt(2)\n\t" \
   "v_smfmac_f32_16x16x64_bf16 v[28:31], v[136:139], v[48:55], v22\n\t" "s_waitcnt vmcnt(0)\n\t" \
   "v_smfmac_f32_16x16x64_bf16 v[28:31], v[140:143], v[56:63], v22\n\t" "s_nop 7\n\t" "v_add_f32 v28, v28, v29\n\t" "v_add_f32 v30, v30, v31\n\t" \
   "v_cvt_pk_bf16_f32 v24, v28, v30\n\t" "v_cmp_u_f32 vcc, v28, v30\n\t" "s_nop 0\n\t" \
   "v_mov_b32_dpp v25, v24 quad_perm:[1,0,3,2] row_mask:0xf bank_mask:0xf\n\t" "s_cbranch_vccnz 8f\n\t" "2:\n\t" RSTAMP(2) \
   "v_perm_b32 v26, v25, v24, v18\n\t" "s_nop 1\n\t" "v_min_u32_dpp v24, v26, v13 quad_perm:[0,1,0,1] row_mask:0xf bank_mask:0xf\n\t" \
   "v_mov_b32_dpp v25, v26 quad_perm:[2,3,2,3] row_mask:0xf bank_mask:0xf\n\t" "buffer_store_dwordx2 v[24:25], v14, %[rs], s41 offen sc1\n\t" \
   "buffer_store_dwordx2 v[24:25], v14, %[rs], s53 offen sc1\n\t" "s_memtime s[62:63]\n\t" "s_memrealtime s[60:61]\n\t" \
   "buffer_store_dwordx2 v[24:25], v15, %[rs], s41 offen sc1\n\t" "buffer_store_dwordx2 v[24:25], v15, %[rs], s53 offen sc1\n\t" RSTAMP(3) \
   "s_waitcnt lgkmcnt(0)\n\t" "s_sub_u32 s74, s62, s57\n\t" "s_mul_i32 s74, s74, s58\n\t" "s_lshr_b32 s74, s74, 12\n\t" "s_add_u32 s74, s74, s56\n\t" \
   "s_lshl_b32 s60, s60, 3\n\t" "s_sub_u32 s75, s74, s60\n\t" "s_cmp_lt_u32 s75, 32\n\t" "s_cbranch_scc1 38f\n\t" "s_cmp_lt_i32 s75, 0\n\t" \
   "s_cselect_b32 s61, 0, 31\n\t" "s_add_u32 s74, s60, s61\n\t" "s_sub_i32 s75, s61, s75\n\t" "s_add_i32 s75, s75, 1\n\t" "s_ashr_i32 s75, s75, 1\n\t" \
   "s_min_i32 s75, s75, 8\n\t" "s_max_i32 s75, s75, -8\n\t" "s_mov_b32 s56, s74\n\t" "s_mov_b32 s57, s62\n\t" "s_add_u32 s58, s58, s75\n\t" "38:\n\t" \
   "s_min_u32 s75, s46, 1\n\t" "s_and_b32 s74, s74, -4\n\t" "s_or_b32 s74, s74, s75\n\t" "v_mov_b32 v26, s74\n\t" \
   "buffer_store_dword v26, v21, %[rs], s41 offen sc1\n\t" FINTRACE "v_xor_b32 v23, 32, v23\n\t" "s_cmp_lt_u32 s40, 63\n\t" "s_cbranch_scc1 30b\n\t" \
   "s_branch 90f\n\t" "18:\n\t" "s_sleep 2\n\t" "s_branch 19b\n\t" "8:\n\t" "s_mov_b32 s74, 0\n\t" "87:\n\t" "v_max3_u32 v12, v32, v34, v36\n\t" \
   "v_max3_u32 v12, v12, v38, v40\n\t" "v_max3_u32 v12, v12, v42, v44\n\t" "v_max3_u32 v12, v12, v46, v48\n\t" "v_max3_u32 v12, v12, v50, v52\n\t" \
   "v_max3_u32 v12, v12, v54, v56\n\t" "v_max3_u32 v12, v12, v58, v60\n\t" "v_max_u32 v12, v12, v62\n\t" "v_cmp_eq_u32 vcc, -1, v12\n\t" \
   "s_cmp_gt_u32 s74, 0x10000\n\t" "s_cbranch_scc1 86f\n\t" "s_cbranch_vccz 86f\n\t" "s_cmp_eq_u32 s74, 0\n\t" "s_cselect_b32 s75, 0x100, 0\n\t" \
   "s_cbranch_scc1 88f\n\t" "s_sleep 3\n\t" "88:\n\t" "buffer_load_dwordx4 v[32:35], v16, %[rs], s50 offen offset:0 sc1\n\t" \
   "buffer_load_dwordx4 v[36:39], v16, %[rs], s50 offen offset:1024 sc1\n\t" "buffer_load_dwordx4 v[40:43], v16, %[rs], s50 offen offset:2048 sc1\n\t" \
   "buffer_load_dwordx4 v[44:47], v16, %[rs], s50 offen offset:3072 sc1\n\t" "buffer_load_dwordx4 v[48:51], v16, %[rs], s51 offen offset:0 sc1\n\t" \
   "buffer_load_dwordx4 v[52:55], v16, %[rs], s51 offen offset:1024 sc1\n\t" "buffer_load_dwordx4 v[56:59], v16, %[rs], s51 offen offset:2048 sc1\n\t" \
   "buffer_load_dwordx4 v[60:63], v16, %[rs], s51 offen offset:3072 sc1\n\t" "s_add_u32 s76, s76, s75\n\t" "s_add_u32 s74, s74, 1\n\t" \
   "s_waitcnt vmcnt(0)\n\t" "s_branch 87b\n\t" "86:\n\t" "v_mov_b32 v27, 0\n\t" "v_mov_b32 v28, 0\n\t" "v_mov_b32 v29, 0\n\t" "v_mov_b32 v30, 0\n\t" \
   "v_mov_b32 v31, 0\n\t" "v_max3_u32 v27, v27, v64, v66\n\t" "v_max3_u32 v27, v27, v68, v70\n\t" "v_max3_u32 v27, v27, v72, v74\n\t" \
   "v_max3_u32 v27, v27, v76, v78\n\t" "v_max3_u32 v27, v27, v80, v82\n\t" "v_max3_u32 v27, v27, v84, v86\n\t" "v_max3_u32 v27, v27, v88, v90\n\t" \
   "v_max3_u32 v27, v27, v92, v94\n\t" "v_max3_u32 v27, v27, v96, v98\n\t" "v_max3_u32 v27, v27, v100, v102\n\t" "v_max3_u32 v27, v27, v104, v106\n\t" \
   "v_max3_u32 v27, v27, v108, v110\n\t" "v_max3_u32 v27, v27, v112, v114\n\t" "v_max3_u32 v27, v27, v116, v118\n\t" \
   "v_max3_u32 v27, v27, v120, v122\n\t" "v_max3_u32 v27, v27, v124, v126\n\t" "v_max3_u32 v27, v27, v128, v130\n\t" \
   "v_max3_u32 v27, v27, v132, v134\n\t" "v_max3_u32 v27, v27, v136, v138\n\t" "v_max3_u32 v27, v27, v140, v142\n\t" "v_cmp_eq_u32 vcc, -1, v27\n\t" \
   "s_nop 0\n\t" "s_cbranch_vccnz 85f\n\t" "v_smfmac_f32_16x16x64_bf16 v[28:31], v[64:67], a[0:7], v22\n\t" \
   "v_smfmac_f32_16x16x64_bf16 v[28:31], v[68:71], a[8:15], v22\n\t" "v_smfmac_f32_16x16x64_bf16 v[28:31], v[72:75], a[16:23], v22\n\t" \
   "v_smfmac_f32_16x16x64_bf16 v[28:31], v[76:79], a[24:31], v22\n\t" "v_smfmac_f32_16x16x64_bf16 v[28:31], v[80:83], a[32:39], v22\n\t" \
   "v_smfmac_f32_16x16x64_bf16 v[28:31], v[84:87], a[40:47], v22\n\t" "v_smfmac_f32_16x16x64_bf16 v[28:31], v[88:91], a[48:55], v22\n\t" \
   "v_smfmac_f32_16x16x64_bf16 v[28:31], v[92:95], a[56:63], v22\n\t" "v_smfmac_f32_16x16x64_bf16 v[28:31], v[96:99], a[64:71], v22\n\t" \
   "v_smfmac_f32_16x16x64_bf16 v[28:31], v[100:103], a[72:79], v22\n\t" "v_smfmac_f32_16x16x64_bf16 v[28:31], v[104:107], a[80:87], v22\n\t" \
   "v_smfmac_f32_16x16x64_bf16 v[28:31], v[108:111], a[88:95], v22\n\t" "v_smfmac_f32_16x16x64_bf16 v[28:31], v[112:115], a[96:103], v22\n\t" \
   "v_smfmac_f32_16x16x64_bf16 v[28:31], v[116:119], a[104:111], v22\n\t" "v_smfmac_f32_16x16x64_bf16 v[28:31], v[120:123], a[112:119], v22\n\t" \
   "v_smfmac_f32_16x16x64_bf16 v[28:31], v[124:127], a[120:127], v22\n\t" "v_smfmac_f32_16x16x64_bf16 v[28:31], v[128:131], v[32:39], v22\n\t" \
   "v_smfmac_f32_16x16x64_bf16 v[28:31], v[132:135], v[40:47], v22\n\t" "v_smfmac_f32_16x16x64_bf16 v[28:31], v[136:139], v[48:55], v22\n\t" \
   "v_smfmac_f32_16x16x64_bf16 v[28:31], v[140:143], v[56:63], v22\n\t" "84:\n\t" "v_cmp_eq_u32 vcc, -1, v27\n\t" "s_cmp_gt_u32 s46, 0x10000\n\t" \
   "s_cbranch_scc1 83f\n\t" "s_cbranch_vccz 83f\n\t" "85:\n\t" "s_add_u32 s46, s46, 1\n\t" "s_cmp_gt_u32 s46, 2\n\t" "s_cbranch_scc1 81f\n\t" \
   "s_lshl_b32 s74, s46, 3\n\t" "s_lshl_b32 s74, 1, s74\n\t" "s_add_u32 s77, s77, s74\n\t" "81:\n\t" "s_min_u32 s74, s46, 7\n\t" \
   "s_lshr_b32 s64, s74, 1\n\t" "s_add_u32 s64, s64, %[xs]\n\t" "s_and_b32 s64, s64, 3\n\t" "s_lshl_b32 s64, s64, 11\n\t" "s_and_b32 s75, s74, 1\n\t" \
   "s_lshl_b32 s75, s75, 14\n\t" "s_xor_b32 s75, s75, s47\n\t" "s_xor_b32 s75, s75, %[lad0]\n\t" "s_and_b32 s75, s75, 0x4000\n\t" \
   "s_add_u32 s64, s64, s75\n\t" "s_add_u32 s64, s64, s41\n\t" "s_add_u32 s65, s64, 32768\n\t" "s_add_u32 s66, s64, 65536\n\t" \
   "s_add_u32 s67, s64, 98304\n\t" "s_add_u32 s68, s64, 131072\n\t" "s_add_u32 s69, s64, 163840\n\t" "s_add_u32 s70, s64, 196608\n\t" \
   "s_add_u32 s71, s64, 229376\n\t" "s_add_u32 s72, s64, 262144\n\t" "s_add_u32 s73, s64, 294912\n\t" "v_mov_b32 v28, 0\n\t" "v_mov_b32 v29, 0\n\t" \
   "v_mov_b32 v30, 0\n\t" "v_mov_b32 v31, 0\n\t" "v_mov_b32 v27, 0\n\t" "s_cmp_lt_u32 s46, 7\n\t" "s_cbranch_scc1 82f\n\t" "s_sleep 3\n\t" "82:\n\t" \
   RSTAMP(6) "buffer_load_dwordx4 v[64:67], v16, %[rs], s64 offen offset:0 sc1\n\t" \
   "buffer_load_dwordx4 v[68:71], v16, %[rs], s64 offen offset:1024 sc1\n\t" "buffer_load_dwordx4 v[72:75], v16, %[rs], s65 offen offset:0 sc1\n\t" \
   "buffer_load_dwordx4 v[76:79], v16, %[rs], s65 offen offset:1024 sc1\n\t" "buffer_load_dwordx4 v[80:83], v16, %[rs], s66 offen offset:0 sc1\n\t" \
   "buffer_load_dwordx4 v[84:87], v16, %[rs], s66 offen offset:1024 sc1\n\t" "buffer_load_dwordx4 v[88:91], v16, %[rs], s67 offen offset:0 sc1\n\t" \
   "buffer_load_dwordx4 v[92:95], v16, %[rs], s67 offen offset:1024 sc1\n\t" "buffer_load_dwordx4 v[96:99], v16, %[rs], s68 offen offset:0 sc1\n\t" \
   "buffer_load_dwordx4 v[100:103], v16, %[rs], s68 offen offset:1024 sc1\n\t" "buffer_load_dwordx4 v[104:107], v16, %[rs], s69 offen offset:0 sc1\n\t" \
   "buffer_load_dwordx4 v[108:111], v16, %[rs], s69 offen offset:1024 sc1\n\t" "buffer_load_dwordx4 v[112:115], v16, %[rs], s70 offen offset:0 sc1\n\t" \
   "buffer_load_dwordx4 v[116:119], v16, %[rs], s70 offen offset:1024 sc1\n\t" "buffer_load_dwordx4 v[120:123], v16, %[rs], s71 offen offset:0 sc1\n\t" \
   "buffer_load_dwordx4 v[124:127], v16, %[rs], s71 offen offset:1024 sc1\n\t" "buffer_load_dwordx2 v[128:129], v17, %[rs], s72 offen offset:0 sc1\n\t" \
   "buffer_load_dwordx2 v[130:131], v17, %[rs], s72 offen offset:128 sc1\n\t" \
   "buffer_load_dwordx2 v[132:133], v17, %[rs], s72 offen offset:1024 sc1\n\t" \
   "buffer_load_dwordx2 v[134:135], v17, %[rs], s72 offen offset:1152 sc1\n\t" "buffer_load_dwordx2 v[136:137], v17, %[rs], s73 offen offset:0 sc1\n\t" \
   "buffer_load_dwordx2 v[138:139], v17, %[rs], s73 offen offset:128 sc1\n\t" \
   "buffer_load_dwordx2 v[140:141], v17, %[rs], s73 offen offset:1024 sc1\n\t" \
   "buffer_load_dwordx2 v[142:143], v17, %[rs], s73 offen offset:1152 sc1\n\t" "s_waitcnt vmcnt(23)\n\t" \
   "v_smfmac_f32_16x16x64_bf16 v[28:31], v[64:67], a[0:7], v22\n\t" "v_max3_u32 v27, v27, v64, v66\n\t" "s_waitcnt vmcnt(22)\n\t" \
   "v_smfmac_f32_16x16x64_bf16 v[28:31], v[68:71], a[8:15], v22\n\t" "v_max3_u32 v27, v27, v68, v70\n\t" "s_waitcnt vmcnt(21)\n\t" \
   "v_smfmac_f32_16x16x64_bf16 v[28:31], v[72:75], a[16:23], v22\n\t" "v_max3_u32 v27, v27, v72, v74\n\t" "s_waitcnt vmcnt(20)\n\t" \
   "v_smfmac_f32_16x16x64_bf16 v[28:31], v[76:79], a[24:31], v22\n\t" "v_max3_u32 v27, v27, v76, v78\n\t" "s_waitcnt vmcnt(19)\n\t" \
   "v_smfmac_f32_16x16x64_bf16 v[28:31], v[80:83], a[32:39], v22\n\t" "v_max3_u32 v27, v27, v80, v82\n\t" "s_waitcnt vmcnt(18)\n\t" \
   "v_smfmac_f32_16x16x64_bf16 v[28:31], v[84:87], a[40:47], v22\n\t" "v_max3_u32 v27, v27, v84, v86\n\t" "s_waitcnt vmcnt(17)\n\t" \
   "v_smfmac_f32_16x16x64_bf16 v[28:31], v[88:91], a[48:55], v22\n\t" "v_max3_u32 v27, v27, v88, v90\n\t" "s_waitcnt vmcnt(16)\n\t" \
   "v_smfmac_f32_16x16x64_bf16 v[28:31], v[92:95], a[56:63], v22\n\t" "v_max3_u32 v27, v27, v92, v94\n\t" "s_waitcnt vmcnt(15)\n\t" \
   "v_smfmac_f32_16x16x64_bf16 v[28:31], v[96:99], a[64:71], v22\n\t" "v_max3_u32 v27, v27, v96, v98\n\t" "s_waitcnt vmcnt(14)\n\t" \
   "v_smfmac_f32_16x16x64_bf16 v[28:31], v[100:103], a[72:79], v22\n\t" "v_max3_u32 v27, v27, v100, v102\n\t" "s_waitcnt vmcnt(13)\n\t" \
   "v_smfmac_f32_16x16x64_bf16 v[28:31], v[104:107], a[80:87], v22\n\t" "v_max3_u32 v27, v27, v104, v106\n\t" "s_waitcnt vmcnt(12)\n\t" \
   "v_smfmac_f32_16x16x64_bf16 v[28:31], v[108:111], a[88:95], v22\n\t" "v_max3_u32 v27, v27, v108, v110\n\t" "s_waitcnt vmcnt(11)\n\t" \
   "v_smfmac_f32_16x16x64_bf16 v[28:31], v[112:115], a[96:103], v22\n\t" "v_max3_u32 v27, v27, v112, v114\n\t" "s_waitcnt vmcnt(10)\n\t" \
   "v_smfmac_f32_16x16x64_bf16 v[28:31], v[116:119], a[104:111], v22\n\t" "v_max3_u32 v27, v27, v116, v118\n\t" "s_waitcnt vmcnt(9)\n\t" \
   "v_smfmac_f32_16x16x64_bf16 v[28:31], v[120:123], a[112:119], v22\n\t" "v_max3_u32 v27, v27, v120, v122\n\t" "s_waitcnt vmcnt(8)\n\t" \
   "v_smfmac_f32_16x16x64_bf16 v[28:31], v[124:127], a[120:127], v22\n\t" "v_max3_u32 v27, v27, v124, v126\n\t" "s_waitcnt vmcnt(6)\n\t" \
   "v_smfmac_f32_16x16x64_bf16 v[28:31], v[128:131], v[32:39], v22\n\t" "v_max3_u32 v27, v27, v128, v130\n\t" "s_waitcnt vmcnt(4)\n\t" \
   "v_smfmac_f32_16x16x64_bf16 v[28:31], v[132:135], v[40:47], v22\n\t" "v_max3_u32 v27, v27, v132, v134\n\t" "s_waitcnt vmcnt(2)\n\t" \
   "v_smfmac_f32_16x16x64_bf16 v[28:31], v[136:139], v[48:55], v22\n\t" "v_max3_u32 v27, v27, v136, v138\n\t" "s_waitcnt vmcnt(0)\n\t" \
   "v_smfmac_f32_16x16x64_bf16 v[28:31], v[140:143], v[56:63], v22\n\t" "v_max3_u32 v27, v27, v140, v142\n\t" "s_branch 84b\n\t" "83:\n\t" \
   "s_or_b32 s46, s46, 0x10000000\n\t" "s_nop 4\n\t" "v_add_f32 v28, v28, v29\n\t" "v_add_f32 v30, v30, v31\n\t" "v_cvt_pk_bf16_f32 v24, v28, v30\n\t" \
   "s_nop 1\n\t" "v_mov_b32_dpp v25, v24 quad_perm:[1,0,3,2] row_mask:0xf bank_mask:0xf\n\t" "s_branch 2b\n\t" "95:\n\t" "s_mov_b32 s74, 0\n\t" \
   "97:\n\t" "buffer_load_dwordx4 v[8:11], v19, %[rs], s52 offen sc1\n\t" "s_add_u32 s74, s74, 1\n\t" "s_waitcnt vmcnt(0)\n\t" \
   "v_max3_u32 v15, v8, v9, v10\n\t" "v_max_u32 v15, v15, v11\n\t" "v_cmp_eq_u32 vcc, -1, v15\n\t" "s_cmp_gt_u32 s74, 0x10000\n\t" \
   "s_cbranch_scc1 96b\n\t" "s_cbranch_vccz 96b\n\t" "s_sleep 2\n\t" "s_branch 97b\n\t" "90:\n\t" "s_waitcnt vmcnt(0) lgkmcnt(0)"
#define FIN_CLOB "v8", "v9", "v10", "v11", "v12", "v13", "v14", "v15", "v24", "v25", "v26", "v27", "v28", "v29", "v30", "v31", "v32", "v33", "v34", "v35", "v36", "v37", "v38", "v39", "v40", "v41", "v42", "v43", "v44", "v45", "v46", "v47", "v48", "v49", "v50", "v51", "v52", "v53", "v54", "v55", "v56", "v57", "v58", "v59", "v60", "v61", "v62", "v63", "v64", "v65", "v66", "v67", "v68", "v69", "v70", "v71", "v72", "v73", "v74", "v75", "v76", "v77", "v78", "v79", "v80", "v81", "v82", "v83", "v84", "v85", "v86", "v87", "v88", "v89", "v90", "v91", "v92", "v93", "v94", "v95", "v96", "v97", "v98", "v99", "v100", "v101", "v102", "v103", "v104", "v105", "v106", "v107", "v108", "v109", "v110", "v111", "v112", "v113", "v114", "v115", "v116", "v117", "v118", "v119", "v120", "v121", "v122", "v123", "v124", "v125", "v126", "v127", "v128", "v129", "v130", "v131", "v132", "v133", "v134", "v135", "v136", "v137", "v138", "v139", "v140", "v141", "v142", "v143", "v144", "v145", "v146", "v147", "v148", "v149", "v150", "v151", "v152", "v153", "v154", "v155", "v156", "v157", "v158", "v159", "v160", "v161", "v162", "v163", "v164", "v165", "v166", "v167", "v168", "v169", "v170", "v171", "v172", "v173", "v174", "v175", "v176", "v177", "v178", "v179", "v180", "v181", "v182", "v183", "v184", "v185", "v186", "v187", "v188", "v189", "v190", "v191", "v192", "v193", "v194", "v195", "v196", "v197", "v198", "v199", "v200", "v201", "v202", "v203", "v204", "v205", "v206", "v207", "v208", "v209", "v210", "v211", "v212", "v213", "v214", "v215", "v216", "v217", "v218", "v219", "v220", "v221", "v222", "v223", "v224", "v225", "v226", "v227", "s40", "s41", "s42", "s43", "s44", "s45", "s46", "s47", "s48", "s49", "s50", "s51", "s52", "s53", "s60", "s61", "s62", "s63", "s64", "s65", "s66", "s67", "s68", "s69", "s70", "s71", "s72", "s73", "s74", "s75", "a0", "a1", "a2", "a3", "a4", "a5", "a6", "a7", "a8", "a9", "a10", "a11", "a12", "a13", "a14", "a15", "a16", "a17", "a18", "a19", "a20", "a21", "a22", "a23", "a24", "a25", "a26", "a27", "a28", "a29", "a30", "a31", "a32", "a33", "a34", "a35", "a36", "a37", "a38", "a39", "a40", "a41", "a42", "a43", "a44", "a45", "a46", "a47", "a48", "a49", "a50", "a51", "a52", "a53", "a54", "a55", "a56", "a57", "a58", "a59", "a60", "a61", "a62", "a63", "a64", "a65", "a66", "a67", "a68", "a69", "a70", "a71", "a72", "a73", "a74", "a75", "a76", "a77", "a78", "a79", "a80", "a81", "a82", "a83", "a84", "a85", "a86", "a87", "a88", "a89", "a90", "a91", "a92", "a93", "a94", "a95", "a96", "a97", "a98", "a99", "a100", "a101", "a102", "a103", "a104", "a105", "a106", "a107", "a108", "a109", "a110", "a111", "a112", "a113", "a114", "a115", "a116", "a117", "a118", "a119", "a120", "a121", "a122", "a123", "a124", "a125", "a126", "a127"
__device__ __forceinline__ void put4(v32u& d, int i, v4u v) { d[4 * i] = v.x; d[4 * i + 1] = v.y; d[4 * i + 2] = v.z; d[4 * i + 3] = v.w; }
#define LDP_ASM \
   "buffer_load_dword v26, v113, %[rw], 0 offen sc1\n\t" "s_mov_b32 s41, 0\n\t" "buffer_load_dwordx4 a[0:3], v16, %[ra], s41 offen\n\t" \
   "s_mov_b32 s41, 131072\n\t" "buffer_load_dwordx4 a[64:67], v16, %[ra], s41 offen\n\t" "s_mov_b32 s41, 524288\n\t" \
   "buffer_load_dwordx4 a[8:11], v16, %[ra], s41 offen\n\t" "s_mov_b32 s41, 655360\n\t" "buffer_load_dwordx4 a[72:75], v16, %[ra], s41 offen\n\t" \
   "s_mov_b32 s41, 1048576\n\t" "buffer_load_dwordx4 a[16:19], v16, %[ra], s41 offen\n\t" "s_mov_b32 s41, 1179648\n\t" \
   "buffer_load_dwordx4 a[80:83], v16, %[ra], s41 offen\n\t" "s_mov_b32 s41, 1572864\n\t" "buffer_load_dwordx4 a[24:27], v16, %[ra], s41 offen\n\t" \
   "s_mov_b32 s41, 1703936\n\t" "buffer_load_dwordx4 a[88:91], v16, %[ra], s41 offen\n\t" "s_mov_b32 s41, 2097152\n\t" \
   "buffer_load_dwordx4 a[32:35], v16, %[ra], s41 offen\n\t" "s_mov_b32 s41, 2228224\n\t" "buffer_load_dwordx4 a[96:99], v16, %[ra], s41 offen\n\t" \
   "s_mov_b32 s41, 2621440\n\t" "buffer_load_dwordx4 a[40:43], v16, %[ra], s41 offen\n\t" "s_mov_b32 s41, 2752512\n\t" \
   "buffer_load_dwordx4 a[104:107], v16, %[ra], s41 offen\n\t" "s_mov_b32 s41, 3145728\n\t" "buffer_load_dwordx4 a[48:51], v16, %[ra], s41 offen\n\t" \
   "s_mov_b32 s41, 3276800\n\t" "buffer_load_dwordx4 a[112:115], v16, %[ra], s41 offen\n\t" "s_mov_b32 s41, 3670016\n\t" \
   "buffer_load_dwordx4 a[56:59], v16, %[ra], s41 offen\n\t" "s_mov_b32 s41, 3801088\n\t" "buffer_load_dwordx4 a[120:123], v16, %[ra], s41 offen\n\t" \
   "s_waitcnt vmcnt(16)\n\t" "v_add_u32 v26, 1, v26\n\t" "buffer_store_dword v26, v114, %[rw], 0 offen sc1\n\t" "ds_write_b32 v115, v26\n\t" LDSTAMP(0) \
   "s_waitcnt vmcnt(15)\n\t" LDSTAMP(1) "ds_write_b128 v20, a[0:3] offset:0\n\t" "s_mov_b32 s41, 4194304\n\t" \
   "buffer_load_dwordx4 v[192:195], v16, %[ra], s41 offen\n\t" "s_waitcnt vmcnt(15)\n\t" "ds_write_b128 v20, a[64:67] offset:4096\n\t" \
   "s_mov_b32 s41, 4325376\n\t" "buffer_load_dwordx4 v[208:211], v16, %[ra], s41 offen\n\t" "s_waitcnt vmcnt(15)\n\t" \
   "ds_write_b128 v20, a[8:11] offset:8192\n\t" "s_mov_b32 s41, 4718592\n\t" "buffer_load_dwordx4 v[200:203], v16, %[ra], s41 offen\n\t" \
   "s_waitcnt vmcnt(15)\n\t" "ds_write_b128 v20, a[72:75] offset:12288\n\t" "s_mov_b32 s41, 4849664\n\t" \
   "buffer_load_dwordx4 v[216:219], v16, %[ra], s41 offen\n\t" "s_waitcnt vmcnt(15)\n\t" "ds_write_b128 v20, a[16:19] offset:16384\n\t" \
   "s_mov_b32 s41, 0\n\t" "buffer_load_dwordx4 a[128:131], v16, %[rb], s41 offen\n\t" "s_waitcnt vmcnt(15)\n\t" \
   "ds_write_b128 v20, a[80:83] offset:20480\n\t" "s_mov_b32 s41, 131072\n\t" "buffer_load_dwordx4 a[192:195], v16, %[rb], s41 offen\n\t" \
   "s_waitcnt vmcnt(15)\n\t" "ds_write_b128 v20, a[24:27] offset:24576\n\t" "s_mov_b32 s41, 524288\n\t" \
   "buffer_load_dwordx4 a[136:139], v16, %[rb], s41 offen\n\t" "s_waitcnt vmcnt(15)\n\t" "ds_write_b128 v20, a[88:91] offset:28672\n\t" \
   "s_mov_b32 s41, 655360\n\t" "buffer_load_dwordx4 a[200:203], v16, %[rb], s41 offen\n\t" "s_waitcnt vmcnt(15)\n\t" \
   "ds_write_b128 v20, a[32:35] offset:32768\n\t" "s_mov_b32 s41, 1048576\n\t" "buffer_load_dwordx4 a[144:147], v16, %[rb], s41 offen\n\t" \
   "s_waitcnt vmcnt(15)\n\t" "ds_write_b128 v20, a[96:99] offset:36864\n\t" "s_mov_b32 s41, 1179648\n\t" \
   "buffer_load_dwordx4 a[208:211], v16, %[rb], s41 offen\n\t" "s_waitcnt vmcnt(15)\n\t" "ds_write_b128 v20, a[40:43] offset:40960\n\t" \
   "s_mov_b32 s41, 1572864\n\t" "buffer_load_dwordx4 a[152:155], v16, %[rb], s41 offen\n\t" "s_waitcnt vmcnt(15)\n\t" \
   "ds_write_b128 v20, a[104:107] offset:45056\n\t" "s_mov_b32 s41, 1703936\n\t" "buffer_load_dwordx4 a[216:219], v16, %[rb], s41 offen\n\t" \
   "s_waitcnt vmcnt(15)\n\t" "ds_write_b128 v20, a[48:51] offset:49152\n\t" "s_mov_b32 s41, 2097152\n\t" \
   "buffer_load_dwordx4 a[160:163], v16, %[rb], s41 offen\n\t" "s_waitcnt vmcnt(15)\n\t" "ds_write_b128 v20, a[112:115] offset:53248\n\t" \
   "s_mov_b32 s41, 2228224\n\t" "buffer_load_dwordx4 a[224:227], v16, %[rb], s41 offen\n\t" "s_waitcnt vmcnt(15)\n\t" \
   "ds_write_b128 v20, a[56:59] offset:57344\n\t" "s_mov_b32 s41, 2621440\n\t" "buffer_load_dwordx4 a[168:171], v16, %[rb], s41 offen\n\t" \
   "s_waitcnt vmcnt(15)\n\t" "ds_write_b128 v20, a[120:123] offset:61440\n\t" "s_mov_b32 s41, 2752512\n\t" \
   "buffer_load_dwordx4 a[232:235], v16, %[rb], s41 offen\n\t" "s_waitcnt vmcnt(15)\n\t" "ds_write_b128 v23, v[192:195] offset:0\n\t" \
   "s_mov_b32 s41, 3145728\n\t" "buffer_load_dwordx4 a[176:179], v16, %[rb], s41 offen\n\t" "s_waitcnt vmcnt(15)\n\t" \
   "ds_write_b128 v23, v[208:211] offset:4096\n\t" "s_mov_b32 s41, 3276800\n\t" "buffer_load_dwordx4 a[240:243], v16, %[rb], s41 offen\n\t" \
   "s_waitcnt vmcnt(15)\n\t" "ds_write_b128 v23, v[200:203] offset:8192\n\t" "s_mov_b32 s41, 3670016\n\t" \
   "buffer_load_dwordx4 a[184:187], v16, %[rb], s41 offen\n\t" "s_waitcnt vmcnt(15)\n\t" "ds_write_b128 v23, v[216:219] offset:12288\n\t" \
   "s_mov_b32 s41, 3801088\n\t" "buffer_load_dwordx4 a[248:251], v16, %[rb], s41 offen\n\t" "s_waitcnt lgkmcnt(0)\n\t" "s_barrier\n\t" LDSTAMP(2) \
   "s_lshl_b32 s40, %[p0], 13\n\t" "v_add_u32 v24, s40, v18\n\t" "v_add_u32 v25, s40, v19\n\t" "s_add_u32 s40, s40, 8192\n\t" \
   "s_cmp_eq_u32 s40, 81920\n\t" "s_cselect_b32 s40, 0, s40\n\t" "ds_read_b64_tr_b16 a[0:1], v24 offset:0\n\t" \
   "ds_read_b64_tr_b16 a[2:3], v24 offset:4096\n\t" "ds_read_b64_tr_b16 a[64:65], v25 offset:0\n\t" "ds_read_b64_tr_b16 a[66:67], v25 offset:4096\n\t" \
   "v_add_u32 v24, s40, v18\n\t" "v_add_u32 v25, s40, v19\n\t" "s_add_u32 s40, s40, 8192\n\t" "s_cmp_eq_u32 s40, 81920\n\t" \
   "s_cselect_b32 s40, 0, s40\n\t" "ds_read_b64_tr_b16 a[8:9], v24 offset:0\n\t" "ds_read_b64_tr_b16 a[10:11], v24 offset:4096\n\t" \
   "ds_read_b64_tr_b16 a[72:73], v25 offset:0\n\t" "ds_read_b64_tr_b16 a[74:75], v25 offset:4096\n\t" "v_add_u32 v24, s40, v18\n\t" \
   "v_add_u32 v25, s40, v19\n\t" "s_add_u32 s40, s40, 8192\n\t" "s_cmp_eq_u32 s40, 81920\n\t" "s_cselect_b32 s40, 0, s40\n\t" \
   "ds_read_b64_tr_b16 a[16:17], v24 offset:0\n\t" "ds_read_b64_tr_b16 a[18:19], v24 offset:4096\n\t" "ds_read_b64_tr_b16 a[80:81], v25 offset:0\n\t" \
   "ds_read_b64_tr_b16 a[82:83], v25 offset:4096\n\t" "v_add_u32 v24, s40, v18\n\t" "v_add_u32 v25, s40, v19\n\t" "s_add_u32 s40, s40, 8192\n\t" \
   "s_cmp_eq_u32 s40, 81920\n\t" "s_cselect_b32 s40, 0, s40\n\t" "ds_read_b64_tr_b16 a[24:25], v24 offset:0\n\t" \
   "ds_read_b64_tr_b16 a[26:27], v24 offset:4096\n\t" "ds_read_b64_tr_b16 a[88:89], v25 offset:0\n\t" \
   "ds_read_b64_tr_b16 a[90:91], v25 offset:4096\n\t" "v_add_u32 v24, s40, v18\n\t" "v_add_u32 v25, s40, v19\n\t" "s_add_u32 s40, s40, 8192\n\t" \
   "s_cmp_eq_u32 s40, 81920\n\t" "s_cselect_b32 s40, 0, s40\n\t" "ds_read_b64_tr_b16 a[32:33], v24 offset:0\n\t" \
   "ds_read_b64_tr_b16 a[34:35], v24 offset:4096\n\t" "ds_read_b64_tr_b16 a[96:97], v25 offset:0\n\t" \
   "ds_read_b64_tr_b16 a[98:99], v25 offset:4096\n\t" "v_add_u32 v24, s40, v18\n\t" "v_add_u32 v25, s40, v19\n\t" "s_add_u32 s40, s40, 8192\n\t" \
   "s_cmp_eq_u32 s40, 81920\n\t" "s_cselect_b32 s40, 0, s40\n\t" "ds_read_b64_tr_b16 a[40:41], v24 offset:0\n\t" \
   "ds_read_b64_tr_b16 a[42:43], v24 offset:4096\n\t" "ds_read_b64_tr_b16 a[104:105], v25 offset:0\n\t" \
   "ds_read_b64_tr_b16 a[106:107], v25 offset:4096\n\t" "v_add_u32 v24, s40, v18\n\t" "v_add_u32 v25, s40, v19\n\t" "s_add_u32 s40, s40, 8192\n\t" \
   "s_cmp_eq_u32 s40, 81920\n\t" "s_cselect_b32 s40, 0, s40\n\t" "ds_read_b64_tr_b16 a[48:49], v24 offset:0\n\t" \
   "ds_read_b64_tr_b16 a[50:51], v24 offset:4096\n\t" "ds_read_b64_tr_b16 a[112:113], v25 offset:0\n\t" \
   "ds_read_b64_tr_b16 a[114:115], v25 offset:4096\n\t" "v_add_u32 v24, s40, v18\n\t" "v_add_u32 v25, s40, v19\n\t" "s_add_u32 s40, s40, 8192\n\t" \
   "s_cmp_eq_u32 s40, 81920\n\t" "s_cselect_b32 s40, 0, s40\n\t" "ds_read_b64_tr_b16 a[56:57], v24 offset:0\n\t" \
   "ds_read_b64_tr_b16 a[58:59], v24 offset:4096\n\t" "ds_read_b64_tr_b16 a[120:121], v25 offset:0\n\t" \
   "ds_read_b64_tr_b16 a[122:123], v25 offset:4096\n\t" "v_add_u32 v24, s40, v18\n\t" "v_add_u32 v25, s40, v19\n\t" "s_add_u32 s40, s40, 8192\n\t" \
   "s_cmp_eq_u32 s40, 81920\n\t" "s_cselect_b32 s40, 0, s40\n\t" "ds_read_b64_tr_b16 v[192:193], v24 offset:0\n\t" \
   "ds_read_b64_tr_b16 v[194:195], v24 offset:4096\n\t" "ds_read_b64_tr_b16 v[208:209], v25 offset:0\n\t" \
   "ds_read_b64_tr_b16 v[210:211], v25 offset:4096\n\t" "v_add_u32 v24, s40, v18\n\t" "v_add_u32 v25, s40, v19\n\t" "s_add_u32 s40, s40, 8192\n\t" \
   "s_cmp_eq_u32 s40, 81920\n\t" "s_cselect_b32 s40, 0, s40\n\t" "ds_read_b64_tr_b16 v[200:201], v24 offset:0\n\t" \
   "ds_read_b64_tr_b16 v[202:203], v24 offset:4096\n\t" "ds_read_b64_tr_b16 v[216:217], v25 offset:0\n\t" \
   "ds_read_b64_tr_b16 v[218:219], v25 offset:4096\n\t" "s_waitcnt vmcnt(15)\n\t" "s_waitcnt lgkmcnt(0)\n\t" "s_barrier\n\t" \
   "ds_write_b128 v20, a[128:131] offset:0\n\t" "s_mov_b32 s41, 4194304\n\t" "buffer_load_dwordx4 v[224:227], v16, %[rb], s41 offen\n\t" \
   "s_waitcnt vmcnt(15)\n\t" "ds_write_b128 v20, a[192:195] offset:4096\n\t" "s_mov_b32 s41, 4325376\n\t" \
   "buffer_load_dwordx4 v[240:243], v16, %[rb], s41 offen\n\t" "s_waitcnt vmcnt(15)\n\t" "ds_write_b128 v20, a[136:139] offset:8192\n\t" \
   "s_mov_b32 s41, 4718592\n\t" "buffer_load_dwordx4 v[232:235], v16, %[rb], s41 offen\n\t" "s_waitcnt vmcnt(15)\n\t" \
   "ds_write_b128 v20, a[200:203] offset:12288\n\t" "s_mov_b32 s41, 4849664\n\t" "buffer_load_dwordx4 v[248:251], v16, %[rb], s41 offen\n\t" \
   "s_waitcnt vmcnt(15)\n\t" "ds_write_b128 v20, a[144:147] offset:16384\n\t" "s_mov_b32 s41, 262144\n\t" \
   "buffer_load_dwordx4 a[4:7], v16, %[ra], s41 offen\n\t" "s_waitcnt vmcnt(15)\n\t" "ds_write_b128 v20, a[208:211] offset:20480\n\t" \
   "s_mov_b32 s41, 393216\n\t" "buffer_load_dwordx4 a[68:71], v16, %[ra], s41 offen\n\t" "s_waitcnt vmcnt(15)\n\t" \
   "ds_write_b128 v20, a[152:155] offset:24576\n\t" "s_mov_b32 s41, 786432\n\t" "buffer_load_dwordx4 a[12:15], v16, %[ra], s41 offen\n\t" \
   "s_waitcnt vmcnt(15)\n\t" "ds_write_b128 v20, a[216:219] offset:28672\n\t" "s_mov_b32 s41, 917504\n\t" \
   "buffer_load_dwordx4 a[76:79], v16, %[ra], s41 offen\n\t" "s_waitcnt vmcnt(15)\n\t" "ds_write_b128 v20, a[160:163] offset:32768\n\t" \
   "s_mov_b32 s41, 1310720\n\t" "buffer_load_dwordx4 a[20:23], v16, %[ra], s41 offen\n\t" "s_waitcnt vmcnt(15)\n\t" \
   "ds_write_b128 v20, a[224:227] offset:36864\n\t" "s_mov_b32 s41, 1441792\n\t" "buffer_load_dwordx4 a[84:87], v16, %[ra], s41 offen\n\t" \
   "s_waitcnt vmcnt(15)\n\t" "ds_write_b128 v20, a[168:171] offset:40960\n\t" "s_mov_b32 s41, 1835008\n\t" \
   "buffer_load_dwordx4 a[28:31], v16, %[ra], s41 offen\n\t" "s_waitcnt vmcnt(15)\n\t" "ds_write_b128 v20, a[232:235] offset:45056\n\t" \
   "s_mov_b32 s41, 1966080\n\t" "buffer_load_dwordx4 a[92:95], v16, %[ra], s41 offen\n\t" "s_waitcnt vmcnt(15)\n\t" \
   "ds_write_b128 v20, a[176:179] offset:49152\n\t" "s_mov_b32 s41, 2359296\n\t" "buffer_load_dwordx4 a[36:39], v16, %[ra], s41 offen\n\t" \
   "s_waitcnt vmcnt(15)\n\t" "ds_write_b128 v20, a[240:243] offset:53248\n\t" "s_mov_b32 s41, 2490368\n\t" \
   "buffer_load_dwordx4 a[100:103], v16, %[ra], s41 offen\n\t" "s_waitcnt vmcnt(15)\n\t" "ds_write_b128 v20, a[184:187] offset:57344\n\t" \
   "s_mov_b32 s41, 2883584\n\t" "buffer_load_dwordx4 a[44:47], v16, %[ra], s41 offen\n\t" "s_waitcnt vmcnt(15)\n\t" \
   "ds_write_b128 v20, a[248:251] offset:61440\n\t" "s_mov_b32 s41, 3014656\n\t" "buffer_load_dwordx4 a[108:111], v16, %[ra], s41 offen\n\t" \
   "s_waitcnt vmcnt(15)\n\t" "ds_write_b128 v23, v[224:227] offset:0\n\t" "s_mov_b32 s41, 3407872\n\t" \
   "buffer_load_dwordx4 a[52:55], v16, %[ra], s41 offen\n\t" "s_waitcnt vmcnt(15)\n\t" "ds_write_b128 v23, v[240:243] offset:4096\n\t" \
   "s_mov_b32 s41, 3538944\n\t" "buffer_load_dwordx4 a[116:119], v16, %[ra], s41 offen\n\t" "s_waitcnt vmcnt(15)\n\t" \
   "ds_write_b128 v23, v[232:235] offset:8192\n\t" "s_mov_b32 s41, 3932160\n\t" "buffer_load_dwordx4 a[60:63], v16, %[ra], s41 offen\n\t" \
   "s_waitcnt vmcnt(15)\n\t" "ds_write_b128 v23, v[248:251] offset:12288\n\t" "s_mov_b32 s41, 4063232\n\t" \
   "buffer_load_dwordx4 a[124:127], v16, %[ra], s41 offen\n\t" "s_waitcnt lgkmcnt(0)\n\t" "s_barrier\n\t" LDSTAMP(3) "s_lshl_b32 s40, %[p0], 13\n\t" \
   "v_add_u32 v24, s40, v18\n\t" "v_add_u32 v25, s40, v19\n\t" "s_add_u32 s40, s40, 8192\n\t" "s_cmp_eq_u32 s40, 81920\n\t" \
   "s_cselect_b32 s40, 0, s40\n\t" "ds_read_b64_tr_b16 a[128:129], v24 offset:0\n\t" "ds_read_b64_tr_b16 a[130:131], v24 offset:4096\n\t" \
   "ds_read_b64_tr_b16 a[192:193], v25 offset:0\n\t" "ds_read_b64_tr_b16 a[194:195], v25 offset:4096\n\t" "v_add_u32 v24, s40, v18\n\t" \
   "v_add_u32 v25, s40, v19\n\t" "s_add_u32 s40, s40, 8192\n\t" "s_cmp_eq_u32 s40, 81920\n\t" "s_cselect_b32 s40, 0, s40\n\t" \
   "ds_read_b64_tr_b16 a[136:137], v24 offset:0\n\t" "ds_read_b64_tr_b16 a[138:139], v24 offset:4096\n\t" \
   "ds_read_b64_tr_b16 a[200:201], v25 offset:0\n\t" "ds_read_b64_tr_b16 a[202:203], v25 offset:4096\n\t" "v_add_u32 v24, s40, v18\n\t" \
   "v_add_u32 v25, s40, v19\n\t" "s_add_u32 s40, s40, 8192\n\t" "s_cmp_eq_u32 s40, 81920\n\t" "s_cselect_b32 s40, 0, s40\n\t" \
   "ds_read_b64_tr_b16 a[144:145], v24 offset:0\n\t" "ds_read_b64_tr_b16 a[146:147], v24 offset:4096\n\t" \
   "ds_read_b64_tr_b16 a[208:209], v25 offset:0\n\t" "ds_read_b64_tr_b16 a[210:211], v25 offset:4096\n\t" "v_add_u32 v24, s40, v18\n\t" \
   "v_add_u32 v25, s40, v19\n\t" "s_add_u32 s40, s40, 8192\n\t" "s_cmp_eq_u32 s40, 81920\n\t" "s_cselect_b32 s40, 0, s40\n\t" \
   "ds_read_b64_tr_b16 a[152:153], v24 offset:0\n\t" "ds_read_b64_tr_b16 a[154:155], v24 offset:4096\n\t" \
   "ds_read_b64_tr_b16 a[216:217], v25 offset:0\n\t" "ds_read_b64_tr_b16 a[218:219], v25 offset:4096\n\t" "v_add_u32 v24, s40, v18\n\t" \
   "v_add_u32 v25, s40, v19\n\t" "s_add_u32 s40, s40, 8192\n\t" "s_cmp_eq_u32 s40, 81920\n\t" "s_cselect_b32 s40, 0, s40\n\t" \
   "ds_read_b64_tr_b16 a[160:161], v24 offset:0\n\t" "ds_read_b64_tr_b16 a[162:163], v24 offset:4096\n\t" \
   "ds_read_b64_tr_b16 a[224:225], v25 offset:0\n\t" "ds_read_b64_tr_b16 a[226:227], v25 offset:4096\n\t" "v_add_u32 v24, s40, v18\n\t" \
   "v_add_u32 v25, s40, v19\n\t" "s_add_u32 s40, s40, 8192\n\t" "s_cmp_eq_u32 s40, 81920\n\t" "s_cselect_b32 s40, 0, s40\n\t" \
   "ds_read_b64_tr_b16 a[168:169], v24 offset:0\n\t" "ds_read_b64_tr_b16 a[170:171], v24 offset:4096\n\t" \
   "ds_read_b64_tr_b16 a[232:233], v25 offset:0\n\t" "ds_read_b64_tr_b16 a[234:235], v25 offset:4096\n\t" "v_add_u32 v24, s40, v18\n\t" \
   "v_add_u32 v25, s40, v19\n\t" "s_add_u32 s40, s40, 8192\n\t" "s_cmp_eq_u32 s40, 81920\n\t" "s_cselect_b32 s40, 0, s40\n\t" \
   "ds_read_b64_tr_b16 a[176:177], v24 offset:0\n\t" "ds_read_b64_tr_b16 a[178:179], v24 offset:4096\n\t" \
   "ds_read_b64_tr_b16 a[240:241], v25 offset:0\n\t" "ds_read_b64_tr_b16 a[242:243], v25 offset:4096\n\t" "v_add_u32 v24, s40, v18\n\t" \
   "v_add_u32 v25, s40, v19\n\t" "s_add_u32 s40, s40, 8192\n\t" "s_cmp_eq_u32 s40, 81920\n\t" "s_cselect_b32 s40, 0, s40\n\t" \
   "ds_read_b64_tr_b16 a[184:185], v24 offset:0\n\t" "ds_read_b64_tr_b16 a[186:187], v24 offset:4096\n\t" \
   "ds_read_b64_tr_b16 a[248:249], v25 offset:0\n\t" "ds_read_b64_tr_b16 a[250:251], v25 offset:4096\n\t" "v_add_u32 v24, s40, v18\n\t" \
   "v_add_u32 v25, s40, v19\n\t" "s_add_u32 s40, s40, 8192\n\t" "s_cmp_eq_u32 s40, 81920\n\t" "s_cselect_b32 s40, 0, s40\n\t" \
   "ds_read_b64_tr_b16 v[224:225], v24 offset:0\n\t" "ds_read_b64_tr_b16 v[226:227], v24 offset:4096\n\t" \
   "ds_read_b64_tr_b16 v[240:241], v25 offset:0\n\t" "ds_read_b64_tr_b16 v[242:243], v25 offset:4096\n\t" "v_add_u32 v24, s40, v18\n\t" \
   "v_add_u32 v25, s40, v19\n\t" "s_add_u32 s40, s40, 8192\n\t" "s_cmp_eq_u32 s40, 81920\n\t" "s_cselect_b32 s40, 0, s40\n\t" \
   "ds_read_b64_tr_b16 v[232:233], v24 offset:0\n\t" "ds_read_b64_tr_b16 v[234:235], v24 offset:4096\n\t" \
   "ds_read_b64_tr_b16 v[248:249], v25 offset:0\n\t" "ds_read_b64_tr_b16 v[250:251], v25 offset:4096\n\t" "s_waitcnt vmcnt(15)\n\t" \
   "s_waitcnt lgkmcnt(0)\n\t" "s_barrier\n\t" "ds_write_b128 v20, a[4:7] offset:0\n\t" "s_mov_b32 s41, 4456448\n\t" \
   "buffer_load_dwordx4 v[196:199], v16, %[ra], s41 offen\n\t" "s_waitcnt vmcnt(15)\n\t" "ds_write_b128 v20, a[68:71] offset:4096\n\t" \
   "s_mov_b32 s41, 4587520\n\t" "buffer_load_dwordx4 v[212:215], v16, %[ra], s41 offen\n\t" "s_waitcnt vmcnt(15)\n\t" \
   "ds_write_b128 v20, a[12:15] offset:8192\n\t" "s_mov_b32 s41, 4980736\n\t" "buffer_load_dwordx4 v[204:207], v16, %[ra], s41 offen\n\t" \
   "s_waitcnt vmcnt(15)\n\t" "ds_write_b128 v20, a[76:79] offset:12288\n\t" "s_mov_b32 s41, 5111808\n\t" \
   "buffer_load_dwordx4 v[220:223], v16, %[ra], s41 offen\n\t" "s_waitcnt vmcnt(15)\n\t" "ds_write_b128 v20, a[20:23] offset:16384\n\t" \
   "s_mov_b32 s41, 262144\n\t" "buffer_load_dwordx4 a[132:135], v16, %[rb], s41 offen\n\t" "s_waitcnt vmcnt(15)\n\t" \
   "ds_write_b128 v20, a[84:87] offset:20480\n\t" "s_mov_b32 s41, 393216\n\t" "buffer_load_dwordx4 a[196:199], v16, %[rb], s41 offen\n\t" \
   "s_waitcnt vmcnt(15)\n\t" "ds_write_b128 v20, a[28:31] offset:24576\n\t" "s_mov_b32 s41, 786432\n\t" \
   "buffer_load_dwordx4 a[140:143], v16, %[rb], s41 offen\n\t" "s_waitcnt vmcnt(15)\n\t" "ds_write_b128 v20, a[92:95] offset:28672\n\t" \
   "s_mov_b32 s41, 917504\n\t" "buffer_load_dwordx4 a[204:207], v16, %[rb], s41 offen\n\t" "s_waitcnt vmcnt(15)\n\t" \
   "ds_write_b128 v20, a[36:39] offset:32768\n\t" "s_mov_b32 s41, 1310720\n\t" "buffer_load_dwordx4 a[148:151], v16, %[rb], s41 offen\n\t" \
   "s_waitcnt vmcnt(15)\n\t" "ds_write_b128 v20, a[100:103] offset:36864\n\t" "s_mov_b32 s41, 1441792\n\t" \
   "buffer_load_dwordx4 a[212:215], v16, %[rb], s41 offen\n\t" "s_waitcnt vmcnt(15)\n\t" "ds_write_b128 v20, a[44:47] offset:40960\n\t" \
   "s_mov_b32 s41, 1835008\n\t" "buffer_load_dwordx4 a[156:159], v16, %[rb], s41 offen\n\t" "s_waitcnt vmcnt(15)\n\t" \
   "ds_write_b128 v20, a[108:111] offset:45056\n\t" "s_mov_b32 s41, 1966080\n\t" "buffer_load_dwordx4 a[220:223], v16, %[rb], s41 offen\n\t" \
   "s_waitcnt vmcnt(15)\n\t" "ds_write_b128 v20, a[52:55] offset:49152\n\t" "s_mov_b32 s41, 2359296\n\t" \
   "buffer_load_dwordx4 a[164:167], v16, %[rb], s41 offen\n\t" "s_waitcnt vmcnt(15)\n\t" "ds_write_b128 v20, a[116:119] offset:53248\n\t" \
   "s_mov_b32 s41, 2490368\n\t" "buffer_load_dwordx4 a[228:231], v16, %[rb], s41 offen\n\t" "s_waitcnt vmcnt(15)\n\t" \
   "ds_write_b128 v20, a[60:63] offset:57344\n\t" "s_mov_b32 s41, 2883584\n\t" "buffer_load_dwordx4 a[172:175], v16, %[rb], s41 offen\n\t" \
   "s_waitcnt vmcnt(15)\n\t" "ds_write_b128 v20, a[124:127] offset:61440\n\t" "s_mov_b32 s41, 3014656\n\t" \
   "buffer_load_dwordx4 a[236:239], v16, %[rb], s41 offen\n\t" "s_waitcnt vmcnt(15)\n\t" "ds_write_b128 v23, v[196:199] offset:0\n\t" \
   "s_mov_b32 s41, 3407872\n\t" "buffer_load_dwordx4 a[180:183], v16, %[rb], s41 offen\n\t" "s_waitcnt vmcnt(15)\n\t" \
   "ds_write_b128 v23, v[212:215] offset:4096\n\t" "s_mov_b32 s41, 3538944\n\t" "buffer_load_dwordx4 a[244:247], v16, %[rb], s41 offen\n\t" \
   "s_waitcnt vmcnt(15)\n\t" "ds_write_b128 v23, v[204:207] offset:8192\n\t" "s_mov_b32 s41, 3932160\n\t" \
   "buffer_load_dwordx4 a[188:191], v16, %[rb], s41 offen\n\t" "s_waitcnt vmcnt(15)\n\t" "ds_write_b128 v23, v[220:223] offset:12288\n\t" \
   "s_mov_b32 s41, 4063232\n\t" "buffer_load_dwordx4 a[252:255], v16, %[rb], s41 offen\n\t" "s_waitcnt lgkmcnt(0)\n\t" "s_barrier\n\t" LDSTAMP(4) \
   "s_lshl_b32 s40, %[p0], 13\n\t" "v_add_u32 v24, s40, v18\n\t" "v_add_u32 v25, s40, v19\n\t" "s_add_u32 s40, s40, 8192\n\t" \
   "s_cmp_eq_u32 s40, 81920\n\t" "s_cselect_b32 s40, 0, s40\n\t" "ds_read_b64_tr_b16 a[4:5], v24 offset:0\n\t" \
   "ds_read_b64_tr_b16 a[6:7], v24 offset:4096\n\t" "ds_read_b64_tr_b16 a[68:69], v25 offset:0\n\t" "ds_read_b64_tr_b16 a[70:71], v25 offset:4096\n\t" \
   "v_add_u32 v24, s40, v18\n\t" "v_add_u32 v25, s40, v19\n\t" "s_add_u32 s40, s40, 8192\n\t" "s_cmp_eq_u32 s40, 81920\n\t" \
   "s_cselect_b32 s40, 0, s40\n\t" "ds_read_b64_tr_b16 a[12:13], v24 offset:0\n\t" "ds_read_b64_tr_b16 a[14:15], v24 offset:4096\n\t" \
   "ds_read_b64_tr_b16 a[76:77], v25 offset:0\n\t" "ds_read_b64_tr_b16 a[78:79], v25 offset:4096\n\t" "v_add_u32 v24, s40, v18\n\t" \
   "v_add_u32 v25, s40, v19\n\t" "s_add_u32 s40, s40, 8192\n\t" "s_cmp_eq_u32 s40, 81920\n\t" "s_cselect_b32 s40, 0, s40\n\t" \
   "ds_read_b64_tr_b16 a[20:21], v24 offset:0\n\t" "ds_read_b64_tr_b16 a[22:23], v24 offset:4096\n\t" "ds_read_b64_tr_b16 a[84:85], v25 offset:0\n\t" \
   "ds_read_b64_tr_b16 a[86:87], v25 offset:4096\n\t" "v_add_u32 v24, s40, v18\n\t" "v_add_u32 v25, s40, v19\n\t" "s_add_u32 s40, s40, 8192\n\t" \
   "s_cmp_eq_u32 s40, 81920\n\t" "s_cselect_b32 s40, 0, s40\n\t" "ds_read_b64_tr_b16 a[28:29], v24 offset:0\n\t" \
   "ds_read_b64_tr_b16 a[30:31], v24 offset:4096\n\t" "ds_read_b64_tr_b16 a[92:93], v25 offset:0\n\t" \
   "ds_read_b64_tr_b16 a[94:95], v25 offset:4096\n\t" "v_add_u32 v24, s40, v18\n\t" "v_add_u32 v25, s40, v19\n\t" "s_add_u32 s40, s40, 8192\n\t" \
   "s_cmp_eq_u32 s40, 81920\n\t" "s_cselect_b32 s40, 0, s40\n\t" "ds_read_b64_tr_b16 a[36:37], v24 offset:0\n\t" \
   "ds_read_b64_tr_b16 a[38:39], v24 offset:4096\n\t" "ds_read_b64_tr_b16 a[100:101], v25 offset:0\n\t" \
   "ds_read_b64_tr_b16 a[102:103], v25 offset:4096\n\t" "v_add_u32 v24, s40, v18\n\t" "v_add_u32 v25, s40, v19\n\t" "s_add_u32 s40, s40, 8192\n\t" \
   "s_cmp_eq_u32 s40, 81920\n\t" "s_cselect_b32 s40, 0, s40\n\t" "ds_read_b64_tr_b16 a[44:45], v24 offset:0\n\t" \
   "ds_read_b64_tr_b16 a[46:47], v24 offset:4096\n\t" "ds_read_b64_tr_b16 a[108:109], v25 offset:0\n\t" \
   "ds_read_b64_tr_b16 a[110:111], v25 offset:4096\n\t" "v_add_u32 v24, s40, v18\n\t" "v_add_u32 v25, s40, v19\n\t" "s_add_u32 s40, s40, 8192\n\t" \
   "s_cmp_eq_u32 s40, 81920\n\t" "s_cselect_b32 s40, 0, s40\n\t" "ds_read_b64_tr_b16 a[52:53], v24 offset:0\n\t" \
   "ds_read_b64_tr_b16 a[54:55], v24 offset:4096\n\t" "ds_read_b64_tr_b16 a[116:117], v25 offset:0\n\t" \
   "ds_read_b64_tr_b16 a[118:119], v25 offset:4096\n\t" "v_add_u32 v24, s40, v18\n\t" "v_add_u32 v25, s40, v19\n\t" "s_add_u32 s40, s40, 8192\n\t" \
   "s_cmp_eq_u32 s40, 81920\n\t" "s_cselect_b32 s40, 0, s40\n\t" "ds_read_b64_tr_b16 a[60:61], v24 offset:0\n\t" \
   "ds_read_b64_tr_b16 a[62:63], v24 offset:4096\n\t" "ds_read_b64_tr_b16 a[124:125], v25 offset:0\n\t" \
   "ds_read_b64_tr_b16 a[126:127], v25 offset:4096\n\t" "v_add_u32 v24, s40, v18\n\t" "v_add_u32 v25, s40, v19\n\t" "s_add_u32 s40, s40, 8192\n\t" \
   "s_cmp_eq_u32 s40, 81920\n\t" "s_cselect_b32 s40, 0, s40\n\t" "ds_read_b64_tr_b16 v[196:197], v24 offset:0\n\t" \
   "ds_read_b64_tr_b16 v[198:199], v24 offset:4096\n\t" "ds_read_b64_tr_b16 v[212:213], v25 offset:0\n\t" \
   "ds_read_b64_tr_b16 v[214:215], v25 offset:4096\n\t" "v_add_u32 v24, s40, v18\n\t" "v_add_u32 v25, s40, v19\n\t" "s_add_u32 s40, s40, 8192\n\t" \
   "s_cmp_eq_u32 s40, 81920\n\t" "s_cselect_b32 s40, 0, s40\n\t" "ds_read_b64_tr_b16 v[204:205], v24 offset:0\n\t" \
   "ds_read_b64_tr_b16 v[206:207], v24 offset:4096\n\t" "ds_read_b64_tr_b16 v[220:221], v25 offset:0\n\t" \
   "ds_read_b64_tr_b16 v[222:223], v25 offset:4096\n\t" "s_waitcnt vmcnt(15)\n\t" "s_waitcnt lgkmcnt(0)\n\t" "s_barrier\n\t" \
   "ds_write_b128 v20, a[132:135] offset:0\n\t" "s_mov_b32 s41, 4456448\n\t" "buffer_load_dwordx4 v[228:231], v16, %[rb], s41 offen\n\t" \
   "s_waitcnt vmcnt(15)\n\t" "ds_write_b128 v20, a[196:199] offset:4096\n\t" "s_mov_b32 s41, 4587520\n\t" \
   "buffer_load_dwordx4 v[244:247], v16, %[rb], s41 offen\n\t" "s_waitcnt vmcnt(15)\n\t" "ds_write_b128 v20, a[140:143] offset:8192\n\t" \
   "s_mov_b32 s41, 4980736\n\t" "buffer_load_dwordx4 v[236:239], v16, %[rb], s41 offen\n\t" "s_waitcnt vmcnt(15)\n\t" \
   "ds_write_b128 v20, a[204:207] offset:12288\n\t" "s_mov_b32 s41, 5111808\n\t" "buffer_load_dwordx4 v[252:255], v16, %[rb], s41 offen\n\t" \
   "s_waitcnt vmcnt(15)\n\t" "ds_write_b128 v20, a[148:151] offset:16384\n\t" "s_waitcnt vmcnt(14)\n\t" \
   "ds_write_b128 v20, a[212:215] offset:20480\n\t" "s_waitcnt vmcnt(13)\n\t" "ds_write_b128 v20, a[156:159] offset:24576\n\t" \
   "s_waitcnt vmcnt(12)\n\t" "ds_write_b128 v20, a[220:223] offset:28672\n\t" "s_waitcnt vmcnt(11)\n\t" \
   "ds_write_b128 v20, a[164:167] offset:32768\n\t" "s_waitcnt vmcnt(10)\n\t" "ds_write_b128 v20, a[228:231] offset:36864\n\t" \
   "buffer_load_dwordx4 v[28:31], v112, %[rw], 0 offen sc1\n\t" "s_waitcnt vmcnt(10)\n\t" "ds_write_b128 v20, a[172:175] offset:40960\n\t" \
   "s_waitcnt vmcnt(9)\n\t" "ds_write_b128 v20, a[236:239] offset:45056\n\t" "s_waitcnt vmcnt(8)\n\t" "ds_write_b128 v20, a[180:183] offset:49152\n\t" \
   "s_waitcnt vmcnt(7)\n\t" "ds_write_b128 v20, a[244:247] offset:53248\n\t" "s_waitcnt vmcnt(6)\n\t" "ds_write_b128 v20, a[188:191] offset:57344\n\t" \
   "s_waitcnt vmcnt(5)\n\t" "ds_write_b128 v20, a[252:255] offset:61440\n\t" "s_waitcnt vmcnt(4)\n\t" "ds_write_b128 v23, v[228:231] offset:0\n\t" \
   "s_waitcnt vmcnt(3)\n\t" "ds_write_b128 v23, v[244:247] offset:4096\n\t" "s_waitcnt vmcnt(2)\n\t" "ds_write_b128 v23, v[236:239] offset:8192\n\t" \
   "s_waitcnt vmcnt(1)\n\t" "ds_write_b128 v23, v[252:255] offset:12288\n\t" "s_waitcnt lgkmcnt(0)\n\t" "s_barrier\n\t" LDSTAMP(5) \
   "s_lshl_b32 s40, %[p0], 13\n\t" "v_add_u32 v24, s40, v18\n\t" "v_add_u32 v25, s40, v19\n\t" "s_add_u32 s40, s40, 8192\n\t" \
   "s_cmp_eq_u32 s40, 81920\n\t" "s_cselect_b32 s40, 0, s40\n\t" "ds_read_b64_tr_b16 a[132:133], v24 offset:0\n\t" \
   "ds_read_b64_tr_b16 a[134:135], v24 offset:4096\n\t" "ds_read_b64_tr_b16 a[196:197], v25 offset:0\n\t" \
   "ds_read_b64_tr_b16 a[198:199], v25 offset:4096\n\t" "v_add_u32 v24, s40, v18\n\t" "v_add_u32 v25, s40, v19\n\t" "s_add_u32 s40, s40, 8192\n\t" \
   "s_cmp_eq_u32 s40, 81920\n\t" "s_cselect_b32 s40, 0, s40\n\t" "ds_read_b64_tr_b16 a[140:141], v24 offset:0\n\t" \
   "ds_read_b64_tr_b16 a[142:143], v24 offset:4096\n\t" "ds_read_b64_tr_b16 a[204:205], v25 offset:0\n\t" \
   "ds_read_b64_tr_b16 a[206:207], v25 offset:4096\n\t" "v_add_u32 v24, s40, v18\n\t" "v_add_u32 v25, s40, v19\n\t" "s_add_u32 s40, s40, 8192\n\t" \
   "s_cmp_eq_u32 s40, 81920\n\t" "s_cselect_b32 s40, 0, s40\n\t" "ds_read_b64_tr_b16 a[148:149], v24 offset:0\n\t" \
   "ds_read_b64_tr_b16 a[150:151], v24 offset:4096\n\t" "ds_read_b64_tr_b16 a[212:213], v25 offset:0\n\t" \
   "ds_read_b64_tr_b16 a[214:215], v25 offset:4096\n\t" "v_add_u32 v24, s40, v18\n\t" "v_add_u32 v25, s40, v19\n\t" "s_add_u32 s40, s40, 8192\n\t" \
   "s_cmp_eq_u32 s40, 81920\n\t" "s_cselect_b32 s40, 0, s40\n\t" "ds_read_b64_tr_b16 a[156:157], v24 offset:0\n\t" \
   "ds_read_b64_tr_b16 a[158:159], v24 offset:4096\n\t" "ds_read_b64_tr_b16 a[220:221], v25 offset:0\n\t" \
   "ds_read_b64_tr_b16 a[222:223], v25 offset:4096\n\t" "v_add_u32 v24, s40, v18\n\t" "v_add_u32 v25, s40, v19\n\t" "s_add_u32 s40, s40, 8192\n\t" \
   "s_cmp_eq_u32 s40, 81920\n\t" "s_cselect_b32 s40, 0, s40\n\t" "ds_read_b64_tr_b16 a[164:165], v24 offset:0\n\t" \
   "ds_read_b64_tr_b16 a[166:167], v24 offset:4096\n\t" "ds_read_b64_tr_b16 a[228:229], v25 offset:0\n\t" \
   "ds_read_b64_tr_b16 a[230:231], v25 offset:4096\n\t" "v_add_u32 v24, s40, v18\n\t" "v_add_u32 v25, s40, v19\n\t" "s_add_u32 s40, s40, 8192\n\t" \
   "s_cmp_eq_u32 s40, 81920\n\t" "s_cselect_b32 s40, 0, s40\n\t" "ds_read_b64_tr_b16 a[172:173], v24 offset:0\n\t" \
   "ds_read_b64_tr_b16 a[174:175], v24 offset:4096\n\t" "ds_read_b64_tr_b16 a[236:237], v25 offset:0\n\t" \
   "ds_read_b64_tr_b16 a[238:239], v25 offset:4096\n\t" "v_add_u32 v24, s40, v18\n\t" "v_add_u32 v25, s40, v19\n\t" "s_add_u32 s40, s40, 8192\n\t" \
   "s_cmp_eq_u32 s40, 81920\n\t" "s_cselect_b32 s40, 0, s40\n\t" "ds_read_b64_tr_b16 a[180:181], v24 offset:0\n\t" \
   "ds_read_b64_tr_b16 a[182:183], v24 offset:4096\n\t" "ds_read_b64_tr_b16 a[244:245], v25 offset:0\n\t" \
   "ds_read_b64_tr_b16 a[246:247], v25 offset:4096\n\t" "v_add_u32 v24, s40, v18\n\t" "v_add_u32 v25, s40, v19\n\t" "s_add_u32 s40, s40, 8192\n\t" \
   "s_cmp_eq_u32 s40, 81920\n\t" "s_cselect_b32 s40, 0, s40\n\t" "ds_read_b64_tr_b16 a[188:189], v24 offset:0\n\t" \
   "ds_read_b64_tr_b16 a[190:191], v24 offset:4096\n\t" "ds_read_b64_tr_b16 a[252:253], v25 offset:0\n\t" \
   "ds_read_b64_tr_b16 a[254:255], v25 offset:4096\n\t" "v_add_u32 v24, s40, v18\n\t" "v_add_u32 v25, s40, v19\n\t" "s_add_u32 s40, s40, 8192\n\t" \
   "s_cmp_eq_u32 s40, 81920\n\t" "s_cselect_b32 s40, 0, s40\n\t" "ds_read_b64_tr_b16 v[228:229], v24 offset:0\n\t" \
   "ds_read_b64_tr_b16 v[230:231], v24 offset:4096\n\t" "ds_read_b64_tr_b16 v[244:245], v25 offset:0\n\t" \
   "ds_read_b64_tr_b16 v[246:247], v25 offset:4096\n\t" "v_add_u32 v24, s40, v18\n\t" "v_add_u32 v25, s40, v19\n\t" "s_add_u32 s40, s40, 8192\n\t" \
   "s_cmp_eq_u32 s40, 81920\n\t" "s_cselect_b32 s40, 0, s40\n\t" "ds_read_b64_tr_b16 v[236:237], v24 offset:0\n\t" \
   "ds_read_b64_tr_b16 v[238:239], v24 offset:4096\n\t" "ds_read_b64_tr_b16 v[252:253], v25 offset:0\n\t" \
   "ds_read_b64_tr_b16 v[254:255], v25 offset:4096\n\t" "s_waitcnt vmcnt(0) lgkmcnt(0)\n\t" "s_barrier"
#define LDP_CLOB "v24", "v25", "v26", "v32", "v33", "v34", "v35", "v36", "v37", "v38", "v39", "v40", "v41", "v42", "v43", "v44", "v45", "v46", "v47", "v48", "v49", "v50", "v51", "v52", "v53", "v54", "v55", "v56", "v57", "v58", "v59", "v60", "v61", "v62", "v63", "v64", "v65", "v66", "v67", "v68", "v69", "v70", "v71", "v72", "v73", "v74", "v75", "v76", "v77", "v78", "v79", "v80", "v81", "v82", "v83", "v84", "v85", "v86", "v87", "v88", "v89", "v90", "v91", "v92", "v93", "v94", "v95", "v96", "v97", "v98", "v99", "v100", "v101", "v102", "v103", "v104", "v105", "v106", "v107", "v108", "v109", "v110", "v111", "v120", "v121", "v122", "v123", "s40", "s41"
#define LDK_ASM \
   "buffer_load_dword v26, v113, %[rw], 0 offen sc1\n\t" "s_mov_b32 s41, 0\n\t" "buffer_load_dwordx4 v[32:35], v116, %[rx], s41 offen\n\t" \
   "s_mov_b32 s41, 10240\n\t" "buffer_load_dwordx4 v[36:39], v116, %[rx], s41 offen\n\t" "s_mov_b32 s41, 128\n\t" \
   "buffer_load_dwordx4 v[40:43], v116, %[rx], s41 offen\n\t" "s_mov_b32 s41, 10368\n\t" "buffer_load_dwordx4 v[44:47], v116, %[rx], s41 offen\n\t" \
   "s_mov_b32 s41, 256\n\t" "buffer_load_dwordx4 v[48:51], v116, %[rx], s41 offen\n\t" "s_mov_b32 s41, 10496\n\t" \
   "buffer_load_dwordx4 v[52:55], v116, %[rx], s41 offen\n\t" "s_mov_b32 s41, 384\n\t" "buffer_load_dwordx4 v[56:59], v116, %[rx], s41 offen\n\t" \
   "s_mov_b32 s41, 10624\n\t" "buffer_load_dwordx4 v[60:63], v116, %[rx], s41 offen\n\t" "s_mov_b32 s41, 512\n\t" \
   "buffer_load_dwordx4 v[64:67], v116, %[rx], s41 offen\n\t" "s_mov_b32 s41, 10752\n\t" "buffer_load_dwordx4 v[68:71], v116, %[rx], s41 offen\n\t" \
   "s_mov_b32 s41, 640\n\t" "buffer_load_dwordx4 v[72:75], v116, %[rx], s41 offen\n\t" "s_mov_b32 s41, 10880\n\t" \
   "buffer_load_dwordx4 v[76:79], v116, %[rx], s41 offen\n\t" "s_mov_b32 s41, 768\n\t" "buffer_load_dwordx4 v[80:83], v116, %[rx], s41 offen\n\t" \
   "s_mov_b32 s41, 11008\n\t" "buffer_load_dwordx4 v[84:87], v116, %[rx], s41 offen\n\t" "s_mov_b32 s41, 896\n\t" \
   "buffer_load_dwordx4 v[88:91], v116, %[rx], s41 offen\n\t" "s_mov_b32 s41, 11136\n\t" "buffer_load_dwordx4 v[92:95], v116, %[rx], s41 offen\n\t" \
   "s_mov_b32 s40, 0x3f803f80\n\t" "v_mov_b32 v120, 0\n\t" "v_mov_b32 v121, 0\n\t" "v_mov_b32 v122, 0\n\t" "v_mov_b32 v123, 0\n\t" \
   "s_waitcnt vmcnt(16)\n\t" "v_add_u32 v26, 1, v26\n\t" "buffer_store_dword v26, v114, %[rw], 0 offen sc1\n\t" "ds_write_b32 v115, v26\n\t" LDSTAMP(0) \
   "s_waitcnt vmcnt(15)\n\t" LDSTAMP(1) "ds_write_b64 v117, v[32:33] offset:0\n\t" "ds_write_b64 v117, v[34:35] offset:256\n\t" \
   "s_mov_b32 s41, 1024\n\t" "buffer_load_dwordx4 v[96:99], v116, %[rx], s41 offen\n\t" "v_dot2c_f32_bf16 v120, s40, v32\n\t" \
   "v_dot2c_f32_bf16 v121, v32, v32\n\t" "v_dot2c_f32_bf16 v120, s40, v33\n\t" "v_dot2c_f32_bf16 v121, v33, v33\n\t" \
   "v_dot2c_f32_bf16 v120, s40, v34\n\t" "v_dot2c_f32_bf16 v121, v34, v34\n\t" "v_dot2c_f32_bf16 v120, s40, v35\n\t" \
   "v_dot2c_f32_bf16 v121, v35, v35\n\t" "s_waitcnt vmcnt(15)\n\t" "ds_write_b64 v117, v[36:37] offset:128\n\t" \
   "ds_write_b64 v117, v[38:39] offset:384\n\t" "s_mov_b32 s41, 11264\n\t" "buffer_load_dwordx4 v[100:103], v116, %[rx], s41 offen\n\t" \
   "v_dot2c_f32_bf16 v122, s40, v36\n\t" "v_dot2c_f32_bf16 v123, v36, v36\n\t" "v_dot2c_f32_bf16 v122, s40, v37\n\t" \
   "v_dot2c_f32_bf16 v123, v37, v37\n\t" "v_dot2c_f32_bf16 v122, s40, v38\n\t" "v_dot2c_f32_bf16 v123, v38, v38\n\t" \
   "v_dot2c_f32_bf16 v122, s40, v39\n\t" "v_dot2c_f32_bf16 v123, v39, v39\n\t" "s_waitcnt vmcnt(15)\n\t" "ds_write_b64 v117, v[40:41] offset:2048\n\t" \
   "ds_write_b64 v117, v[42:43] offset:2304\n\t" "s_mov_b32 s41, 1152\n\t" "buffer_load_dwordx4 v[104:107], v116, %[rx], s41 offen\n\t" \
   "v_dot2c_f32_bf16 v120, s40, v40\n\t" "v_dot2c_f32_bf16 v121, v40, v40\n\t" "v_dot2c_f32_bf16 v120, s40, v41\n\t" \
   "v_dot2c_f32_bf16 v121, v41, v41\n\t" "v_dot2c_f32_bf16 v120, s40, v42\n\t" "v_dot2c_f32_bf16 v121, v42, v42\n\t" \
   "v_dot2c_f32_bf16 v120, s40, v43\n\t" "v_dot2c_f32_bf16 v121, v43, v43\n\t" "s_waitcnt vmcnt(15)\n\t" "ds_write_b64 v117, v[44:45] offset:2176\n\t" \
   "ds_write_b64 v117, v[46:47] offset:2432\n\t" "s_mov_b32 s41, 11392\n\t" "buffer_load_dwordx4 v[108:111], v116, %[rx], s41 offen\n\t" \
   "v_dot2c_f32_bf16 v122, s40, v44\n\t" "v_dot2c_f32_bf16 v123, v44, v44\n\t" "v_dot2c_f32_bf16 v122, s40, v45\n\t" \
   "v_dot2c_f32_bf16 v123, v45, v45\n\t" "v_dot2c_f32_bf16 v122, s40, v46\n\t" "v_dot2c_f32_bf16 v123, v46, v46\n\t" \
   "v_dot2c_f32_bf16 v122, s40, v47\n\t" "v_dot2c_f32_bf16 v123, v47, v47\n\t" "s_waitcnt vmcnt(15)\n\t" "ds_write_b64 v117, v[48:49] offset:4096\n\t" \
   "ds_write_b64 v117, v[50:51] offset:4352\n\t" "s_mov_b32 s41, 0\n\t" "buffer_load_dwordx4 a[0:3], v16, %[ra], s41 offen\n\t" \
   "v_dot2c_f32_bf16 v120, s40, v48\n\t" "v_dot2c_f32_bf16 v121, v48, v48\n\t" "v_dot2c_f32_bf16 v120, s40, v49\n\t" \
   "v_dot2c_f32_bf16 v121, v49, v49\n\t" "v_dot2c_f32_bf16 v120, s40, v50\n\t" "v_dot2c_f32_bf16 v121, v50, v50\n\t" \
   "v_dot2c_f32_bf16 v120, s40, v51\n\t" "v_dot2c_f32_bf16 v121, v51, v51\n\t" "s_waitcnt vmcnt(15)\n\t" "ds_write_b64 v117, v[52:53] offset:4224\n\t" \
   "ds_write_b64 v117, v[54:55] offset:4480\n\t" "s_mov_b32 s41, 131072\n\t" "buffer_load_dwordx4 a[4:7], v16, %[ra], s41 offen\n\t" \
   "v_dot2c_f32_bf16 v122, s40, v52\n\t" "v_dot2c_f32_bf16 v123, v52, v52\n\t" "v_dot2c_f32_bf16 v122, s40, v53\n\t" \
   "v_dot2c_f32_bf16 v123, v53, v53\n\t" "v_dot2c_f32_bf16 v122, s40, v54\n\t" "v_dot2c_f32_bf16 v123, v54, v54\n\t" \
   "v_dot2c_f32_bf16 v122, s40, v55\n\t" "v_dot2c_f32_bf16 v123, v55, v55\n\t" "s_waitcnt vmcnt(15)\n\t" "ds_write_b64 v117, v[56:57] offset:6144\n\t" \
   "ds_write_b64 v117, v[58:59] offset:6400\n\t" "s_mov_b32 s41, 262144\n\t" "buffer_load_dwordx4 a[16:19], v16, %[ra], s41 offen\n\t" \
   "v_dot2c_f32_bf16 v120, s40, v56\n\t" "v_dot2c_f32_bf16 v121, v56, v56\n\t" "v_dot2c_f32_bf16 v120, s40, v57\n\t" \
   "v_dot2c_f32_bf16 v121, v57, v57\n\t" "v_dot2c_f32_bf16 v120, s40, v58\n\t" "v_dot2c_f32_bf16 v121, v58, v58\n\t" \
   "v_dot2c_f32_bf16 v120, s40, v59\n\t" "v_dot2c_f32_bf16 v121, v59, v59\n\t" "s_waitcnt vmcnt(15)\n\t" "ds_write_b64 v117, v[60:61] offset:6272\n\t" \
   "ds_write_b64 v117, v[62:63] offset:6528\n\t" "s_mov_b32 s41, 393216\n\t" "buffer_load_dwordx4 a[20:23], v16, %[ra], s41 offen\n\t" \
   "v_dot2c_f32_bf16 v122, s40, v60\n\t" "v_dot2c_f32_bf16 v123, v60, v60\n\t" "v_dot2c_f32_bf16 v122, s40, v61\n\t" \
   "v_dot2c_f32_bf16 v123, v61, v61\n\t" "v_dot2c_f32_bf16 v122, s40, v62\n\t" "v_dot2c_f32_bf16 v123, v62, v62\n\t" \
   "v_dot2c_f32_bf16 v122, s40, v63\n\t" "v_dot2c_f32_bf16 v123, v63, v63\n\t" "s_waitcnt vmcnt(15)\n\t" "ds_write_b64 v117, v[64:65] offset:8192\n\t" \
   "ds_write_b64 v117, v[66:67] offset:8448\n\t" "s_mov_b32 s41, 524288\n\t" "buffer_load_dwordx4 a[32:35], v16, %[ra], s41 offen\n\t" \
   "v_dot2c_f32_bf16 v120, s40, v64\n\t" "v_dot2c_f32_bf16 v121, v64, v64\n\t" "v_dot2c_f32_bf16 v120, s40, v65\n\t" \
   "v_dot2c_f32_bf16 v121, v65, v65\n\t" "v_dot2c_f32_bf16 v120, s40, v66\n\t" "v_dot2c_f32_bf16 v121, v66, v66\n\t" \
   "v_dot2c_f32_bf16 v120, s40, v67\n\t" "v_dot2c_f32_bf16 v121, v67, v67\n\t" "s_waitcnt vmcnt(15)\n\t" "ds_write_b64 v117, v[68:69] offset:8320\n\t" \
   "ds_write_b64 v117, v[70:71] offset:8576\n\t" "s_mov_b32 s41, 655360\n\t" "buffer_load_dwordx4 a[36:39], v16, %[ra], s41 offen\n\t" \
   "v_dot2c_f32_bf16 v122, s40, v68\n\t" "v_dot2c_f32_bf16 v123, v68, v68\n\t" "v_dot2c_f32_bf16 v122, s40, v69\n\t" \
   "v_dot2c_f32_bf16 v123, v69, v69\n\t" "v_dot2c_f32_bf16 v122, s40, v70\n\t" "v_dot2c_f32_bf16 v123, v70, v70\n\t" \
   "v_dot2c_f32_bf16 v122, s40, v71\n\t" "v_dot2c_f32_bf16 v123, v71, v71\n\t" "s_waitcnt vmcnt(15)\n\t" "ds_write_b64 v117, v[72:73] offset:10240\n\t" \
   "ds_write_b64 v117, v[74:75] offset:10496\n\t" "s_mov_b32 s41, 786432\n\t" "buffer_load_dwordx4 a[48:51], v16, %[ra], s41 offen\n\t" \
   "v_dot2c_f32_bf16 v120, s40, v72\n\t" "v_dot2c_f32_bf16 v121, v72, v72\n\t" "v_dot2c_f32_bf16 v120, s40, v73\n\t" \
   "v_dot2c_f32_bf16 v121, v73, v73\n\t" "v_dot2c_f32_bf16 v120, s40, v74\n\t" "v_dot2c_f32_bf16 v121, v74, v74\n\t" \
   "v_dot2c_f32_bf16 v120, s40, v75\n\t" "v_dot2c_f32_bf16 v121, v75, v75\n\t" "s_waitcnt vmcnt(15)\n\t" "ds_write_b64 v117, v[76:77] offset:10368\n\t" \
   "ds_write_b64 v117, v[78:79] offset:10624\n\t" "s_mov_b32 s41, 917504\n\t" "buffer_load_dwordx4 a[52:55], v16, %[ra], s41 offen\n\t" \
   "v_dot2c_f32_bf16 v122, s40, v76\n\t" "v_dot2c_f32_bf16 v123, v76, v76\n\t" "v_dot2c_f32_bf16 v122, s40, v77\n\t" \
   "v_dot2c_f32_bf16 v123, v77, v77\n\t" "v_dot2c_f32_bf16 v122, s40, v78\n\t" "v_dot2c_f32_bf16 v123, v78, v78\n\t" \
   "v_dot2c_f32_bf16 v122, s40, v79\n\t" "v_dot2c_f32_bf16 v123, v79, v79\n\t" "s_waitcnt vmcnt(15)\n\t" "ds_write_b64 v117, v[80:81] offset:12288\n\t" \
   "ds_write_b64 v117, v[82:83] offset:12544\n\t" "s_mov_b32 s41, 1048576\n\t" "buffer_load_dwordx4 a[64:67], v16, %[ra], s41 offen\n\t" \
   "v_dot2c_f32_bf16 v120, s40, v80\n\t" "v_dot2c_f32_bf16 v121, v80, v80\n\t" "v_dot2c_f32_bf16 v120, s40, v81\n\t" \
   "v_dot2c_f32_bf16 v121, v81, v81\n\t" "v_dot2c_f32_bf16 v120, s40, v82\n\t" "v_dot2c_f32_bf16 v121, v82, v82\n\t" \
   "v_dot2c_f32_bf16 v120, s40, v83\n\t" "v_dot2c_f32_bf16 v121, v83, v83\n\t" "s_waitcnt vmcnt(15)\n\t" "ds_write_b64 v117, v[84:85] offset:12416\n\t" \
   "ds_write_b64 v117, v[86:87] offset:12672\n\t" "s_mov_b32 s41, 1179648\n\t" "buffer_load_dwordx4 a[68:71], v16, %[ra], s41 offen\n\t" \
   "v_dot2c_f32_bf16 v122, s40, v84\n\t" "v_dot2c_f32_bf16 v123, v84, v84\n\t" "v_dot2c_f32_bf16 v122, s40, v85\n\t" \
   "v_dot2c_f32_bf16 v123, v85, v85\n\t" "v_dot2c_f32_bf16 v122, s40, v86\n\t" "v_dot2c_f32_bf16 v123, v86, v86\n\t" \
   "v_dot2c_f32_bf16 v122, s40, v87\n\t" "v_dot2c_f32_bf16 v123, v87, v87\n\t" "s_waitcnt vmcnt(15)\n\t" "ds_write_b64 v117, v[88:89] offset:14336\n\t" \
   "ds_write_b64 v117, v[90:91] offset:14592\n\t" "s_mov_b32 s41, 0\n\t" "buffer_load_dwordx4 a[8:11], v16, %[rb], s41 offen\n\t" \
   "v_dot2c_f32_bf16 v120, s40, v88\n\t" "v_dot2c_f32_bf16 v121, v88, v88\n\t" "v_dot2c_f32_bf16 v120, s40, v89\n\t" \
   "v_dot2c_f32_bf16 v121, v89, v89\n\t" "v_dot2c_f32_bf16 v120, s40, v90\n\t" "v_dot2c_f32_bf16 v121, v90, v90\n\t" \
   "v_dot2c_f32_bf16 v120, s40, v91\n\t" "v_dot2c_f32_bf16 v121, v91, v91\n\t" "s_waitcnt vmcnt(15)\n\t" "ds_write_b64 v117, v[92:93] offset:14464\n\t" \
   "ds_write_b64 v117, v[94:95] offset:14720\n\t" "s_mov_b32 s41, 131072\n\t" "buffer_load_dwordx4 a[12:15], v16, %[rb], s41 offen\n\t" \
   "v_dot2c_f32_bf16 v122, s40, v92\n\t" "v_dot2c_f32_bf16 v123, v92, v92\n\t" "v_dot2c_f32_bf16 v122, s40, v93\n\t" \
   "v_dot2c_f32_bf16 v123, v93, v93\n\t" "v_dot2c_f32_bf16 v122, s40, v94\n\t" "v_dot2c_f32_bf16 v123, v94, v94\n\t" \
   "v_dot2c_f32_bf16 v122, s40, v95\n\t" "v_dot2c_f32_bf16 v123, v95, v95\n\t" "s_waitcnt vmcnt(15)\n\t" "ds_write_b64 v117, v[96:97] offset:16384\n\t" \
   "ds_write_b64 v117, v[98:99] offset:16640\n\t" "s_mov_b32 s41, 262144\n\t" "buffer_load_dwordx4 a[24:27], v16, %[rb], s41 offen\n\t" \
   "v_dot2c_f32_bf16 v120, s40, v96\n\t" "v_dot2c_f32_bf16 v121, v96, v96\n\t" "v_dot2c_f32_bf16 v120, s40, v97\n\t" \
   "v_dot2c_f32_bf16 v121, v97, v97\n\t" "v_dot2c_f32_bf16 v120, s40, v98\n\t" "v_dot2c_f32_bf16 v121, v98, v98\n\t" \
   "v_dot2c_f32_bf16 v120, s40, v99\n\t" "v_dot2c_f32_bf16 v121, v99, v99\n\t" "s_waitcnt vmcnt(15)\n\t" \
   "ds_write_b64 v117, v[100:101] offset:16512\n\t" "ds_write_b64 v117, v[102:103] offset:16768\n\t" "s_mov_b32 s41, 393216\n\t" \
   "buffer_load_dwordx4 a[28:31], v16, %[rb], s41 offen\n\t" "v_dot2c_f32_bf16 v122, s40, v100\n\t" "v_dot2c_f32_bf16 v123, v100, v100\n\t" \
   "v_dot2c_f32_bf16 v122, s40, v101\n\t" "v_dot2c_f32_bf16 v123, v101, v101\n\t" "v_dot2c_f32_bf16 v122, s40, v102\n\t" \
   "v_dot2c_f32_bf16 v123, v102, v102\n\t" "v_dot2c_f32_bf16 v122, s40, v103\n\t" "v_dot2c_f32_bf16 v123, v103, v103\n\t" "s_waitcnt vmcnt(15)\n\t" \
   "ds_write_b64 v117, v[104:105] offset:18432\n\t" "ds_write_b64 v117, v[106:107] offset:18688\n\t" "s_mov_b32 s41, 524288\n\t" \
   "buffer_load_dwordx4 a[40:43], v16, %[rb], s41 offen\n\t" "v_dot2c_f32_bf16 v120, s40, v104\n\t" "v_dot2c_f32_bf16 v121, v104, v104\n\t" \
   "v_dot2c_f32_bf16 v120, s40, v105\n\t" "v_dot2c_f32_bf16 v121, v105, v105\n\t" "v_dot2c_f32_bf16 v120, s40, v106\n\t" \
   "v_dot2c_f32_bf16 v121, v106, v106\n\t" "v_dot2c_f32_bf16 v120, s40, v107\n\t" "v_dot2c_f32_bf16 v121, v107, v107\n\t" "s_waitcnt vmcnt(15)\n\t" \
   "ds_write_b64 v117, v[108:109] offset:18560\n\t" "ds_write_b64 v117, v[110:111] offset:18816\n\t" "s_mov_b32 s41, 655360\n\t" \
   "buffer_load_dwordx4 a[44:47], v16, %[rb], s41 offen\n\t" "v_dot2c_f32_bf16 v122, s40, v108\n\t" "v_dot2c_f32_bf16 v123, v108, v108\n\t" \
   "v_dot2c_f32_bf16 v122, s40, v109\n\t" "v_dot2c_f32_bf16 v123, v109, v109\n\t" "v_dot2c_f32_bf16 v122, s40, v110\n\t" \
   "v_dot2c_f32_bf16 v123, v110, v110\n\t" "v_dot2c_f32_bf16 v122, s40, v111\n\t" "v_dot2c_f32_bf16 v123, v111, v111\n\t" "s_nop 3\n\t" \
   "v_add_f32_dpp v120, v120, v120 quad_perm:[1,0,3,2] row_mask:0xf bank_mask:0xf\n\t" \
   "v_add_f32_dpp v121, v121, v121 quad_perm:[1,0,3,2] row_mask:0xf bank_mask:0xf\n\t" \
   "v_add_f32_dpp v122, v122, v122 quad_perm:[1,0,3,2] row_mask:0xf bank_mask:0xf\n\t" \
   "v_add_f32_dpp v123, v123, v123 quad_perm:[1,0,3,2] row_mask:0xf bank_mask:0xf\n\t" \
   "v_add_f32_dpp v120, v120, v120 quad_perm:[2,3,0,1] row_mask:0xf bank_mask:0xf\n\t" \
   "v_add_f32_dpp v121, v121, v121 quad_perm:[2,3,0,1] row_mask:0xf bank_mask:0xf\n\t" \
   "v_add_f32_dpp v122, v122, v122 quad_perm:[2,3,0,1] row_mask:0xf bank_mask:0xf\n\t" \
   "v_add_f32_dpp v123, v123, v123 quad_perm:[2,3,0,1] row_mask:0xf bank_mask:0xf\n\t" \
   "v_add_f32_dpp v120, v120, v120 row_half_mirror row_mask:0xf bank_mask:0xf\n\t" \
   "v_add_f32_dpp v121, v121, v121 row_half_mirror row_mask:0xf bank_mask:0xf\n\t" \
   "v_add_f32_dpp v122, v122, v122 row_half_mirror row_mask:0xf bank_mask:0xf\n\t" \
   "v_add_f32_dpp v123, v123, v123 row_half_mirror row_mask:0xf bank_mask:0xf\n\t" "v_and_b32 v120, v119, v120\n\t" "v_and_b32 v121, v119, v121\n\t" \
   "v_and_b32 v122, v119, v122\n\t" "v_and_b32 v123, v119, v123\n\t" "ds_write_b64 v118, v[120:121]\n\t" "ds_write_b64 v118, v[122:123] offset:384\n\t" \
   "s_waitcnt vmcnt(15)\n\t" "ds_write_b128 v20, a[0:3] offset:0\n\t" "s_mov_b32 s41, 786432\n\t" \
   "buffer_load_dwordx4 a[56:59], v16, %[rb], s41 offen\n\t" "s_waitcnt vmcnt(15)\n\t" "ds_write_b128 v20, a[4:7] offset:4096\n\t" \
   "s_mov_b32 s41, 917504\n\t" "buffer_load_dwordx4 a[60:63], v16, %[rb], s41 offen\n\t" "s_waitcnt vmcnt(15)\n\t" \
   "ds_write_b128 v20, a[16:19] offset:8192\n\t" "s_mov_b32 s41, 1048576\n\t" "buffer_load_dwordx4 a[72:75], v16, %[rb], s41 offen\n\t" \
   "s_waitcnt vmcnt(15)\n\t" "ds_write_b128 v20, a[20:23] offset:12288\n\t" "s_mov_b32 s41, 1179648\n\t" \
   "buffer_load_dwordx4 a[76:79], v16, %[rb], s41 offen\n\t" "s_waitcnt vmcnt(15)\n\t" "ds_write_b128 v20, a[32:35] offset:16384\n\t" \
   "s_mov_b32 s41, 1310720\n\t" "buffer_load_dwordx4 a[80:83], v16, %[ra], s41 offen\n\t" "s_waitcnt vmcnt(15)\n\t" \
   "ds_write_b128 v20, a[36:39] offset:20480\n\t" "s_mov_b32 s41, 1441792\n\t" "buffer_load_dwordx4 a[84:87], v16, %[ra], s41 offen\n\t" \
   "s_waitcnt vmcnt(15)\n\t" "ds_write_b128 v20, a[48:51] offset:24576\n\t" "s_mov_b32 s41, 1572864\n\t" \
   "buffer_load_dwordx4 a[96:99], v16, %[ra], s41 offen\n\t" "s_waitcnt vmcnt(15)\n\t" "ds_write_b128 v20, a[52:55] offset:28672\n\t" \
   "s_mov_b32 s41, 1703936\n\t" "buffer_load_dwordx4 a[100:103], v16, %[ra], s41 offen\n\t" "s_waitcnt vmcnt(15)\n\t" \
   "ds_write_b128 v20, a[64:67] offset:32768\n\t" "s_mov_b32 s41, 1835008\n\t" "buffer_load_dwordx4 a[112:115], v16, %[ra], s41 offen\n\t" \
   "s_waitcnt vmcnt(15)\n\t" "ds_write_b128 v20, a[68:71] offset:36864\n\t" "s_mov_b32 s41, 1966080\n\t" \
   "buffer_load_dwordx4 a[116:119], v16, %[ra], s41 offen\n\t" "s_waitcnt lgkmcnt(0)\n\t" "s_barrier\n\t" LDSTAMP(2) \
   "ds_read_b64_tr_b16 a[0:1], v18 offset:0\n\t" "ds_read_b64_tr_b16 a[2:3], v18 offset:4096\n\t" "ds_read_b64_tr_b16 a[4:5], v19 offset:0\n\t" \
   "ds_read_b64_tr_b16 a[6:7], v19 offset:4096\n\t" "ds_read_b64_tr_b16 a[16:17], v18 offset:8192\n\t" \
   "ds_read_b64_tr_b16 a[18:19], v18 offset:12288\n\t" "ds_read_b64_tr_b16 a[20:21], v19 offset:8192\n\t" \
   "ds_read_b64_tr_b16 a[22:23], v19 offset:12288\n\t" "ds_read_b64_tr_b16 a[32:33], v18 offset:16384\n\t" \
   "ds_read_b64_tr_b16 a[34:35], v18 offset:20480\n\t" "ds_read_b64_tr_b16 a[36:37], v19 offset:16384\n\t" \
   "ds_read_b64_tr_b16 a[38:39], v19 offset:20480\n\t" "ds_read_b64_tr_b16 a[48:49], v18 offset:24576\n\t" \
   "ds_read_b64_tr_b16 a[50:51], v18 offset:28672\n\t" "ds_read_b64_tr_b16 a[52:53], v19 offset:24576\n\t" \
   "ds_read_b64_tr_b16 a[54:55], v19 offset:28672\n\t" "ds_read_b64_tr_b16 a[64:65], v18 offset:32768\n\t" \
   "ds_read_b64_tr_b16 a[66:67], v18 offset:36864\n\t" "ds_read_b64_tr_b16 a[68:69], v19 offset:32768\n\t" \
   "ds_read_b64_tr_b16 a[70:71], v19 offset:36864\n\t" "s_waitcnt vmcnt(15)\n\t" "s_waitcnt lgkmcnt(0)\n\t" "s_barrier\n\t" \
   "v_mov_b32 v64, 0x3f803f80\n\t" "v_mov_b32 v65, 0x3f803f80\n\t" "v_mov_b32 v66, 0x3f803f80\n\t" "v_mov_b32 v67, 0x3f803f80\n\t" \
   "ds_write_b128 v20, a[8:11] offset:0\n\t" "s_mov_b32 s41, 2097152\n\t" "buffer_load_dwordx4 a[128:131], v16, %[ra], s41 offen\n\t" \
   "v_mfma_f32_32x32x16_bf16 v[32:47], a[0:3], v[64:67], 0\n\t" "s_waitcnt vmcnt(15)\n\t" "ds_write_b128 v20, a[12:15] offset:4096\n\t" \
   "s_mov_b32 s41, 2228224\n\t" "buffer_load_dwordx4 a[132:135], v16, %[ra], s41 offen\n\t" \
   "v_mfma_f32_32x32x16_bf16 v[32:47], a[4:7], v[64:67], v[32:47]\n\t" "s_waitcnt vmcnt(15)\n\t" "ds_write_b128 v20, a[24:27] offset:8192\n\t" \
   "s_mov_b32 s41, 2359296\n\t" "buffer_load_dwordx4 a[144:147], v16, %[ra], s41 offen\n\t" \
   "v_mfma_f32_32x32x16_bf16 v[32:47], a[16:19], v[64:67], v[32:47]\n\t" "s_waitcnt vmcnt(15)\n\t" "ds_write_b128 v20, a[28:31] offset:12288\n\t" \
   "s_mov_b32 s41, 2490368\n\t" "buffer_load_dwordx4 a[148:151], v16, %[ra], s41 offen\n\t" \
   "v_mfma_f32_32x32x16_bf16 v[32:47], a[20:23], v[64:67], v[32:47]\n\t" "s_waitcnt vmcnt(15)\n\t" "ds_write_b128 v20, a[40:43] offset:16384\n\t" \
   "s_mov_b32 s41, 1310720\n\t" "buffer_load_dwordx4 a[88:91], v16, %[rb], s41 offen\n\t" \
   "v_mfma_f32_32x32x16_bf16 v[32:47], a[32:35], v[64:67], v[32:47]\n\t" "s_waitcnt vmcnt(15)\n\t" "ds_write_b128 v20, a[44:47] offset:20480\n\t" \
   "s_mov_b32 s41, 1441792\n\t" "buffer_load_dwordx4 a[92:95], v16, %[rb], s41 offen\n\t" \
   "v_mfma_f32_32x32x16_bf16 v[32:47], a[36:39], v[64:67], v[32:47]\n\t" "s_waitcnt vmcnt(15)\n\t" "ds_write_b128 v20, a[56:59] offset:24576\n\t" \
   "s_mov_b32 s41, 1572864\n\t" "buffer_load_dwordx4 a[104:107], v16, %[rb], s41 offen\n\t" \
   "v_mfma_f32_32x32x16_bf16 v[32:47], a[48:51], v[64:67], v[32:47]\n\t" "s_waitcnt vmcnt(15)\n\t" "ds_write_b128 v20, a[60:63] offset:28672\n\t" \
   "s_mov_b32 s41, 1703936\n\t" "buffer_load_dwordx4 a[108:111], v16, %[rb], s41 offen\n\t" \
   "v_mfma_f32_32x32x16_bf16 v[32:47], a[52:55], v[64:67], v[32:47]\n\t" "s_waitcnt vmcnt(15)\n\t" "ds_write_b128 v20, a[72:75] offset:32768\n\t" \
   "s_mov_b32 s41, 1835008\n\t" "buffer_load_dwordx4 a[120:123], v16, %[rb], s41 offen\n\t" \
   "v_mfma_f32_32x32x16_bf16 v[32:47], a[64:67], v[64:67], v[32:47]\n\t" "s_waitcnt vmcnt(15)\n\t" "ds_write_b128 v20, a[76:79] offset:36864\n\t" \
   "s_mov_b32 s41, 1966080\n\t" "buffer_load_dwordx4 a[124:127], v16, %[rb], s41 offen\n\t" \
   "v_mfma_f32_32x32x16_bf16 v[32:47], a[68:71], v[64:67], v[32:47]\n\t" "s_waitcnt lgkmcnt(0)\n\t" "s_barrier\n\t" LDSTAMP(3) \
   "ds_read_b64_tr_b16 a[8:9], v18 offset:0\n\t" "ds_read_b64_tr_b16 a[10:11], v18 offset:4096\n\t" "ds_read_b64_tr_b16 a[12:13], v19 offset:0\n\t" \
   "ds_read_b64_tr_b16 a[14:15], v19 offset:4096\n\t" "ds_read_b64_tr_b16 a[24:25], v18 offset:8192\n\t" \
   "ds_read_b64_tr_b16 a[26:27], v18 offset:12288\n\t" "ds_read_b64_tr_b16 a[28:29], v19 offset:8192\n\t" \
   "ds_read_b64_tr_b16 a[30:31], v19 offset:12288\n\t" "ds_read_b64_tr_b16 a[40:41], v18 offset:16384\n\t" \
   "ds_read_b64_tr_b16 a[42:43], v18 offset:20480\n\t" "ds_read_b64_tr_b16 a[44:45], v19 offset:16384\n\t" \
   "ds_read_b64_tr_b16 a[46:47], v19 offset:20480\n\t" "ds_read_b64_tr_b16 a[56:57], v18 offset:24576\n\t" \
   "ds_read_b64_tr_b16 a[58:59], v18 offset:28672\n\t" "ds_read_b64_tr_b16 a[60:61], v19 offset:24576\n\t" \
   "ds_read_b64_tr_b16 a[62:63], v19 offset:28672\n\t" "ds_read_b64_tr_b16 a[72:73], v18 offset:32768\n\t" \
   "ds_read_b64_tr_b16 a[74:75], v18 offset:36864\n\t" "ds_read_b64_tr_b16 a[76:77], v19 offset:32768\n\t" \
   "ds_read_b64_tr_b16 a[78:79], v19 offset:36864\n\t" "s_waitcnt vmcnt(15)\n\t" "s_waitcnt lgkmcnt(0)\n\t" "s_barrier\n\t" \
   "ds_write_b128 v20, a[80:83] offset:0\n\t" "s_mov_b32 s41, 2097152\n\t" "buffer_load_dwordx4 a[136:139], v16, %[rb], s41 offen\n\t" \
   "v_mfma_f32_32x32x16_bf16 v[48:63], a[8:11], v[64:67], 0\n\t" "s_waitcnt vmcnt(15)\n\t" "ds_write_b128 v20, a[84:87] offset:4096\n\t" \
   "s_mov_b32 s41, 2228224\n\t" "buffer_load_dwordx4 a[140:143], v16, %[rb], s41 offen\n\t" \
   "v_mfma_f32_32x32x16_bf16 v[48:63], a[12:15], v[64:67], v[48:63]\n\t" "s_waitcnt vmcnt(15)\n\t" "ds_write_b128 v20, a[96:99] offset:8192\n\t" \
   "s_mov_b32 s41, 2359296\n\t" "buffer_load_dwordx4 a[152:155], v16, %[rb], s41 offen\n\t" \
   "v_mfma_f32_32x32x16_bf16 v[48:63], a[24:27], v[64:67], v[48:63]\n\t" "s_waitcnt vmcnt(15)\n\t" "ds_write_b128 v20, a[100:103] offset:12288\n\t" \
   "s_mov_b32 s41, 2490368\n\t" "buffer_load_dwordx4 a[156:159], v16, %[rb], s41 offen\n\t" \
   "v_mfma_f32_32x32x16_bf16 v[48:63], a[28:31], v[64:67], v[48:63]\n\t" "s_waitcnt vmcnt(15)\n\t" "ds_write_b128 v20, a[112:115] offset:16384\n\t" \
   "s_mov_b32 s41, 2621440\n\t" "buffer_load_dwordx4 a[160:163], v16, %[ra], s41 offen\n\t" \
   "v_mfma_f32_32x32x16_bf16 v[48:63], a[40:43], v[64:67], v[48:63]\n\t" "s_waitcnt vmcnt(15)\n\t" "ds_write_b128 v20, a[116:119] offset:20480\n\t" \
   "s_mov_b32 s41, 2752512\n\t" "buffer_load_dwordx4 a[164:167], v16, %[ra], s41 offen\n\t" \
   "v_mfma_f32_32x32x16_bf16 v[48:63], a[44:47], v[64:67], v[48:63]\n\t" "s_waitcnt vmcnt(15)\n\t" "ds_write_b128 v20, a[128:131] offset:24576\n\t" \
   "s_mov_b32 s41, 2883584\n\t" "buffer_load_dwordx4 a[176:179], v16, %[ra], s41 offen\n\t" \
   "v_mfma_f32_32x32x16_bf16 v[48:63], a[56:59], v[64:67], v[48:63]\n\t" "s_waitcnt vmcnt(15)\n\t" "ds_write_b128 v20, a[132:135] offset:28672\n\t" \
   "s_mov_b32 s41, 3014656\n\t" "buffer_load_dwordx4 a[180:183], v16, %[ra], s41 offen\n\t" \
   "v_mfma_f32_32x32x16_bf16 v[48:63], a[60:63], v[64:67], v[48:63]\n\t" "s_waitcnt vmcnt(15)\n\t" "ds_write_b128 v20, a[144:147] offset:32768\n\t" \
   "s_mov_b32 s41, 3145728\n\t" "buffer_load_dwordx4 a[192:195], v16, %[ra], s41 offen\n\t" \
   "v_mfma_f32_32x32x16_bf16 v[48:63], a[72:75], v[64:67], v[48:63]\n\t" "s_waitcnt vmcnt(15)\n\t" "ds_write_b128 v20, a[148:151] offset:36864\n\t" \
   "s_mov_b32 s41, 3276800\n\t" "buffer_load_dwordx4 a[196:199], v16, %[ra], s41 offen\n\t" \
   "v_mfma_f32_32x32x16_bf16 v[48:63], a[76:79], v[64:67], v[48:63]\n\t" "s_waitcnt lgkmcnt(0)\n\t" "s_barrier\n\t" LDSTAMP(4) \
   "ds_read_b64_tr_b16 a[80:81], v18 offset:0\n\t" "ds_read_b64_tr_b16 a[82:83], v18 offset:4096\n\t" "ds_read_b64_tr_b16 a[84:85], v19 offset:0\n\t" \
   "ds_read_b64_tr_b16 a[86:87], v19 offset:4096\n\t" "ds_read_b64_tr_b16 a[96:97], v18 offset:8192\n\t" \
   "ds_read_b64_tr_b16 a[98:99], v18 offset:12288\n\t" "ds_read_b64_tr_b16 a[100:101], v19 offset:8192\n\t" \
   "ds_read_b64_tr_b16 a[102:103], v19 offset:12288\n\t" "ds_read_b64_tr_b16 a[112:113], v18 offset:16384\n\t" \
   "ds_read_b64_tr_b16 a[114:115], v18 offset:20480\n\t" "ds_read_b64_tr_b16 a[116:117], v19 offset:16384\n\t" \
   "ds_read_b64_tr_b16 a[118:119], v19 offset:20480\n\t" "ds_read_b64_tr_b16 a[128:129], v18 offset:24576\n\t" \
   "ds_read_b64_tr_b16 a[130:131], v18 offset:28672\n\t" "ds_read_b64_tr_b16 a[132:133], v19 offset:24576\n\t" \
   "ds_read_b64_tr_b16 a[134:135], v19 offset:28672\n\t" "ds_read_b64_tr_b16 a[144:145], v18 offset:32768\n\t" \
   "ds_read_b64_tr_b16 a[146:147], v18 offset:36864\n\t" "ds_read_b64_tr_b16 a[148:149], v19 offset:32768\n\t" \
   "ds_read_b64_tr_b16 a[150:151], v19 offset:36864\n\t" "s_waitcnt vmcnt(15)\n\t" "s_waitcnt lgkmcnt(0)\n\t" "s_barrier\n\t" \
   "ds_write_b128 v20, a[88:91] offset:0\n\t" "s_mov_b32 s41, 3407872\n\t" "buffer_load_dwordx4 a[208:211], v16, %[ra], s41 offen\n\t" \
   "v_mfma_f32_32x32x16_bf16 v[32:47], a[80:83], v[64:67], v[32:47]\n\t" "s_waitcnt vmcnt(15)\n\t" "ds_write_b128 v20, a[92:95] offset:4096\n\t" \
   "s_mov_b32 s41, 3538944\n\t" "buffer_load_dwordx4 a[212:215], v16, %[ra], s41 offen\n\t" \
   "v_mfma_f32_32x32x16_bf16 v[32:47], a[84:87], v[64:67], v[32:47]\n\t" "s_waitcnt vmcnt(15)\n\t" "ds_write_b128 v20, a[104:107] offset:8192\n\t" \
   "s_mov_b32 s41, 3670016\n\t" "buffer_load_dwordx4 a[224:227], v16, %[ra], s41 offen\n\t" \
   "v_mfma_f32_32x32x16_bf16 v[32:47], a[96:99], v[64:67], v[32:47]\n\t" "s_waitcnt vmcnt(15)\n\t" "ds_write_b128 v20, a[108:111] offset:12288\n\t" \
   "s_mov_b32 s41, 3801088\n\t" "buffer_load_dwordx4 a[228:231], v16, %[ra], s41 offen\n\t" \
   "v_mfma_f32_32x32x16_bf16 v[32:47], a[100:103], v[64:67], v[32:47]\n\t" "s_waitcnt vmcnt(15)\n\t" "ds_write_b128 v20, a[120:123] offset:16384\n\t" \
   "s_mov_b32 s41, 2621440\n\t" "buffer_load_dwordx4 a[168:171], v16, %[rb], s41 offen\n\t" \
   "v_mfma_f32_32x32x16_bf16 v[32:47], a[112:115], v[64:67], v[32:47]\n\t" "s_waitcnt vmcnt(15)\n\t" "ds_write_b128 v20, a[124:127] offset:20480\n\t" \
   "s_mov_b32 s41, 2752512\n\t" "buffer_load_dwordx4 a[172:175], v16, %[rb], s41 offen\n\t" \
   "v_mfma_f32_32x32x16_bf16 v[32:47], a[116:119], v[64:67], v[32:47]\n\t" "s_waitcnt vmcnt(15)\n\t" "ds_write_b128 v20, a[136:139] offset:24576\n\t" \
   "s_mov_b32 s41, 2883584\n\t" "buffer_load_dwordx4 a[184:187], v16, %[rb], s41 offen\n\t" \
   "v_mfma_f32_32x32x16_bf16 v[32:47], a[128:131], v[64:67], v[32:47]\n\t" "s_waitcnt vmcnt(15)\n\t" "ds_write_b128 v20, a[140:143] offset:28672\n\t" \
   "s_mov_b32 s41, 3014656\n\t" "buffer_load_dwordx4 a[188:191], v16, %[rb], s41 offen\n\t" \
   "v_mfma_f32_32x32x16_bf16 v[32:47], a[132:135], v[64:67], v[32:47]\n\t" "s_waitcnt vmcnt(15)\n\t" "ds_write_b128 v20, a[152:155] offset:32768\n\t" \
   "s_mov_b32 s41, 3145728\n\t" "buffer_load_dwordx4 a[200:203], v16, %[rb], s41 offen\n\t" \
   "v_mfma_f32_32x32x16_bf16 v[32:47], a[144:147], v[64:67], v[32:47]\n\t" "s_waitcnt vmcnt(15)\n\t" "ds_write_b128 v20, a[156:159] offset:36864\n\t" \
   "s_mov_b32 s41, 3276800\n\t" "buffer_load_dwordx4 a[204:207], v16, %[rb], s41 offen\n\t" \
   "v_mfma_f32_32x32x16_bf16 v[32:47], a[148:151], v[64:67], v[32:47]\n\t" "s_waitcnt lgkmcnt(0)\n\t" "s_barrier\n\t" LDSTAMP(5) \
   "ds_read_b64_tr_b16 a[88:89], v18 offset:0\n\t" "ds_read_b64_tr_b16 a[90:91], v18 offset:4096\n\t" "ds_read_b64_tr_b16 a[92:93], v19 offset:0\n\t" \
   "ds_read_b64_tr_b16 a[94:95], v19 offset:4096\n\t" "ds_read_b64_tr_b16 a[104:105], v18 offset:8192\n\t" \
   "ds_read_b64_tr_b16 a[106:107], v18 offset:12288\n\t" "ds_read_b64_tr_b16 a[108:109], v19 offset:8192\n\t" \
   "ds_read_b64_tr_b16 a[110:111], v19 offset:12288\n\t" "ds_read_b64_tr_b16 a[120:121], v18 offset:16384\n\t" \
   "ds_read_b64_tr_b16 a[122:123], v18 offset:20480\n\t" "ds_read_b64_tr_b16 a[124:125], v19 offset:16384\n\t" \
   "ds_read_b64_tr_b16 a[126:127], v19 offset:20480\n\t" "ds_read_b64_tr_b16 a[136:137], v18 offset:24576\n\t" \
   "ds_read_b64_tr_b16 a[138:139], v18 offset:28672\n\t" "ds_read_b64_tr_b16 a[140:141], v19 offset:24576\n\t" \
   "ds_read_b64_tr_b16 a[142:143], v19 offset:28672\n\t" "ds_read_b64_tr_b16 a[152:153], v18 offset:32768\n\t" \
   "ds_read_b64_tr_b16 a[154:155], v18 offset:36864\n\t" "ds_read_b64_tr_b16 a[156:157], v19 offset:32768\n\t" \
   "ds_read_b64_tr_b16 a[158:159], v19 offset:36864\n\t" "s_waitcnt vmcnt(15)\n\t" "s_waitcnt lgkmcnt(0)\n\t" "s_barrier\n\t" \
   "ds_write_b128 v20, a[160:163] offset:0\n\t" "s_mov_b32 s41, 3407872\n\t" "buffer_load_dwordx4 a[216:219], v16, %[rb], s41 offen\n\t" \
   "v_mfma_f32_32x32x16_bf16 v[48:63], a[88:91], v[64:67], v[48:63]\n\t" "s_waitcnt vmcnt(15)\n\t" "ds_write_b128 v20, a[164:167] offset:4096\n\t" \
   "s_mov_b32 s41, 3538944\n\t" "buffer_load_dwordx4 a[220:223], v16, %[rb], s41 offen\n\t" \
   "v_mfma_f32_32x32x16_bf16 v[48:63], a[92:95], v[64:67], v[48:63]\n\t" "s_waitcnt vmcnt(15)\n\t" "ds_write_b128 v20, a[176:179] offset:8192\n\t" \
   "s_mov_b32 s41, 3670016\n\t" "buffer_load_dwordx4 a[232:235], v16, %[rb], s41 offen\n\t" \
   "v_mfma_f32_32x32x16_bf16 v[48:63], a[104:107], v[64:67], v[48:63]\n\t" "s_waitcnt vmcnt(15)\n\t" "ds_write_b128 v20, a[180:183] offset:12288\n\t" \
   "s_mov_b32 s41, 3801088\n\t" "buffer_load_dwordx4 a[236:239], v16, %[rb], s41 offen\n\t" \
   "v_mfma_f32_32x32x16_bf16 v[48:63], a[108:111], v[64:67], v[48:63]\n\t" "s_waitcnt vmcnt(15)\n\t" "ds_write_b128 v20, a[192:195] offset:16384\n\t" \
   "s_mov_b32 s41, 3932160\n\t" "buffer_load_dwordx4 a[240:243], v16, %[ra], s41 offen\n\t" \
   "v_mfma_f32_32x32x16_bf16 v[48:63], a[120:123], v[64:67], v[48:63]\n\t" "s_waitcnt vmcnt(15)\n\t" "ds_write_b128 v20, a[196:199] offset:20480\n\t" \
   "s_mov_b32 s41, 4063232\n\t" "buffer_load_dwordx4 a[244:247], v16, %[ra], s41 offen\n\t" \
   "v_mfma_f32_32x32x16_bf16 v[48:63], a[124:127], v[64:67], v[48:63]\n\t" "s_waitcnt vmcnt(15)\n\t" "ds_write_b128 v20, a[208:211] offset:24576\n\t" \
   "s_mov_b32 s41, 4194304\n\t" "buffer_load_dwordx4 v[192:195], v16, %[ra], s41 offen\n\t" \
   "v_mfma_f32_32x32x16_bf16 v[48:63], a[136:139], v[64:67], v[48:63]\n\t" "s_waitcnt vmcnt(15)\n\t" "ds_write_b128 v20, a[212:215] offset:28672\n\t" \
   "s_mov_b32 s41, 4325376\n\t" "buffer_load_dwordx4 v[196:199], v16, %[ra], s41 offen\n\t" \
   "v_mfma_f32_32x32x16_bf16 v[48:63], a[140:143], v[64:67], v[48:63]\n\t" "s_waitcnt vmcnt(15)\n\t" "ds_write_b128 v20, a[224:227] offset:32768\n\t" \
   "s_mov_b32 s41, 4456448\n\t" "buffer_load_dwordx4 v[208:211], v16, %[ra], s41 offen\n\t" \
   "v_mfma_f32_32x32x16_bf16 v[48:63], a[152:155], v[64:67], v[48:63]\n\t" "s_waitcnt vmcnt(15)\n\t" "ds_write_b128 v20, a[228:231] offset:36864\n\t" \
   "s_mov_b32 s41, 4587520\n\t" "buffer_load_dwordx4 v[212:215], v16, %[ra], s41 offen\n\t" \
   "v_mfma_f32_32x32x16_bf16 v[48:63], a[156:159], v[64:67], v[48:63]\n\t" "s_waitcnt lgkmcnt(0)\n\t" "s_barrier\n\t" LDSTAMP(6) \
   "ds_read_b64_tr_b16 a[160:161], v18 offset:0\n\t" "ds_read_b64_tr_b16 a[162:163], v18 offset:4096\n\t" \
   "ds_read_b64_tr_b16 a[164:165], v19 offset:0\n\t" "ds_read_b64_tr_b16 a[166:167], v19 offset:4096\n\t" \
   "ds_read_b64_tr_b16 a[176:177], v18 offset:8192\n\t" "ds_read_b64_tr_b16 a[178:179], v18 offset:12288\n\t" \
   "ds_read_b64_tr_b16 a[180:181], v19 offset:8192\n\t" "ds_read_b64_tr_b16 a[182:183], v19 offset:12288\n\t" \
   "ds_read_b64_tr_b16 a[192:193], v18 offset:16384\n\t" "ds_read_b64_tr_b16 a[194:195], v18 offset:20480\n\t" \
   "ds_read_b64_tr_b16 a[196:197], v19 offset:16384\n\t" "ds_read_b64_tr_b16 a[198:199], v19 offset:20480\n\t" \
   "ds_read_b64_tr_b16 a[208:209], v18 offset:24576\n\t" "ds_read_b64_tr_b16 a[210:211], v18 offset:28672\n\t" \
   "ds_read_b64_tr_b16 a[212:213], v19 offset:24576\n\t" "ds_read_b64_tr_b16 a[214:215], v19 offset:28672\n\t" \
   "ds_read_b64_tr_b16 a[224:225], v18 offset:32768\n\t" "ds_read_b64_tr_b16 a[226:227], v18 offset:36864\n\t" \
   "ds_read_b64_tr_b16 a[228:229], v19 offset:32768\n\t" "ds_read_b64_tr_b16 a[230:231], v19 offset:36864\n\t" "s_waitcnt vmcnt(15)\n\t" \
   "s_waitcnt lgkmcnt(0)\n\t" "s_barrier\n\t" "ds_write_b128 v20, a[168:171] offset:0\n\t" "s_mov_b32 s41, 4718592\n\t" \
   "buffer_load_dwordx4 v[224:227], v16, %[ra], s41 offen\n\t" "v_mfma_f32_32x32x16_bf16 v[32:47], a[160:163], v[64:67], v[32:47]\n\t" \
   "s_waitcnt vmcnt(15)\n\t" "ds_write_b128 v20, a[172:175] offset:4096\n\t" "s_mov_b32 s41, 4849664\n\t" \
   "buffer_load_dwordx4 v[228:231], v16, %[ra], s41 offen\n\t" "v_mfma_f32_32x32x16_bf16 v[32:47], a[164:167], v[64:67], v[32:47]\n\t" \
   "s_waitcnt vmcnt(15)\n\t" "ds_write_b128 v20, a[184:187] offset:8192\n\t" "s_mov_b32 s41, 4980736\n\t" \
   "buffer_load_dwordx4 v[240:243], v16, %[ra], s41 offen\n\t" "v_mfma_f32_32x32x16_bf16 v[32:47], a[176:179], v[64:67], v[32:47]\n\t" \
   "s_waitcnt vmcnt(15)\n\t" "ds_write_b128 v20, a[188:191] offset:12288\n\t" "s_mov_b32 s41, 5111808\n\t" \
   "buffer_load_dwordx4 v[244:247], v16, %[ra], s41 offen\n\t" "v_mfma_f32_32x32x16_bf16 v[32:47], a[180:183], v[64:67], v[32:47]\n\t" \
   "s_waitcnt vmcnt(15)\n\t" "ds_write_b128 v20, a[200:203] offset:16384\n\t" "s_mov_b32 s41, 3932160\n\t" \
   "buffer_load_dwordx4 a[248:251], v16, %[rb], s41 offen\n\t" "v_mfma_f32_32x32x16_bf16 v[32:47], a[192:195], v[64:67], v[32:47]\n\t" \
   "s_waitcnt vmcnt(15)\n\t" "ds_write_b128 v20, a[204:207] offset:20480\n\t" "s_mov_b32 s41, 4063232\n\t" \
   "buffer_load_dwordx4 a[252:255], v16, %[rb], s41 offen\n\t" "v_mfma_f32_32x32x16_bf16 v[32:47], a[196:199], v[64:67], v[32:47]\n\t" \
   "s_waitcnt vmcnt(15)\n\t" "ds_write_b128 v20, a[216:219] offset:24576\n\t" "s_mov_b32 s41, 4194304\n\t" \
   "buffer_load_dwordx4 v[200:203], v16, %[rb], s41 offen\n\t" "v_mfma_f32_32x32x16_bf16 v[32:47], a[208:211], v[64:67], v[32:47]\n\t" \
   "s_waitcnt vmcnt(15)\n\t" "ds_write_b128 v20, a[220:223] offset:28672\n\t" "s_mov_b32 s41, 4325376\n\t" \
   "buffer_load_dwordx4 v[204:207], v16, %[rb], s41 offen\n\t" "v_mfma_f32_32x32x16_bf16 v[32:47], a[212:215], v[64:67], v[32:47]\n\t" \
   "s_waitcnt vmcnt(15)\n\t" "ds_write_b128 v20, a[232:235] offset:32768\n\t" "s_mov_b32 s41, 4456448\n\t" \
   "buffer_load_dwordx4 v[216:219], v16, %[rb], s41 offen\n\t" "v_mfma_f32_32x32x16_bf16 v[32:47], a[224:227], v[64:67], v[32:47]\n\t" \
   "s_waitcnt vmcnt(15)\n\t" "ds_write_b128 v20, a[236:239] offset:36864\n\t" "s_mov_b32 s41, 4587520\n\t" \
   "buffer_load_dwordx4 v[220:223], v16, %[rb], s41 offen\n\t" "v_mfma_f32_32x32x16_bf16 v[32:47], a[228:231], v[64:67], v[32:47]\n\t" \
   "s_waitcnt lgkmcnt(0)\n\t" "s_barrier\n\t" LDSTAMP(7) "ds_read_b64_tr_b16 a[168:169], v18 offset:0\n\t" \
   "ds_read_b64_tr_b16 a[170:171], v18 offset:4096\n\t" "ds_read_b64_tr_b16 a[172:173], v19 offset:0\n\t" \
   "ds_read_b64_tr_b16 a[174:175], v19 offset:4096\n\t" "ds_read_b64_tr_b16 a[184:185], v18 offset:8192\n\t" \
   "ds_read_b64_tr_b16 a[186:187], v18 offset:12288\n\t" "ds_read_b64_tr_b16 a[188:189], v19 offset:8192\n\t" \
   "ds_read_b64_tr_b16 a[190:191], v19 offset:12288\n\t" "ds_read_b64_tr_b16 a[200:201], v18 offset:16384\n\t" \
   "ds_read_b64_tr_b16 a[202:203], v18 offset:20480\n\t" "ds_read_b64_tr_b16 a[204:205], v19 offset:16384\n\t" \
   "ds_read_b64_tr_b16 a[206:207], v19 offset:20480\n\t" "ds_read_b64_tr_b16 a[216:217], v18 offset:24576\n\t" \
   "ds_read_b64_tr_b16 a[218:219], v18 offset:28672\n\t" "ds_read_b64_tr_b16 a[220:221], v19 offset:24576\n\t" \
   "ds_read_b64_tr_b16 a[222:223], v19 offset:28672\n\t" "ds_read_b64_tr_b16 a[232:233], v18 offset:32768\n\t" \
   "ds_read_b64_tr_b16 a[234:235], v18 offset:36864\n\t" "ds_read_b64_tr_b16 a[236:237], v19 offset:32768\n\t" \
   "ds_read_b64_tr_b16 a[238:239], v19 offset:36864\n\t" "s_waitcnt vmcnt(15)\n\t" "s_waitcnt lgkmcnt(0)\n\t" "s_barrier\n\t" \
   "ds_write_b128 v20, a[240:243] offset:0\n\t" "s_mov_b32 s41, 4718592\n\t" "buffer_load_dwordx4 v[232:235], v16, %[rb], s41 offen\n\t" \
   "v_mfma_f32_32x32x16_bf16 v[48:63], a[168:171], v[64:67], v[48:63]\n\t" "s_waitcnt vmcnt(15)\n\t" "ds_write_b128 v20, a[244:247] offset:4096\n\t" \
   "s_mov_b32 s41, 4849664\n\t" "buffer_load_dwordx4 v[236:239], v16, %[rb], s41 offen\n\t" \
   "v_mfma_f32_32x32x16_bf16 v[48:63], a[172:175], v[64:67], v[48:63]\n\t" "s_waitcnt vmcnt(15)\n\t" "ds_write_b128 v20, v[192:195] offset:8192\n\t" \
   "s_mov_b32 s41, 4980736\n\t" "buffer_load_dwordx4 v[248:251], v16, %[rb], s41 offen\n\t" \
   "v_mfma_f32_32x32x16_bf16 v[48:63], a[184:187], v[64:67], v[48:63]\n\t" "s_waitcnt vmcnt(15)\n\t" "ds_write_b128 v20, v[196:199] offset:12288\n\t" \
   "s_mov_b32 s41, 5111808\n\t" "buffer_load_dwordx4 v[252:255], v16, %[rb], s41 offen\n\t" \
   "v_mfma_f32_32x32x16_bf16 v[48:63], a[188:191], v[64:67], v[48:63]\n\t" "s_waitcnt vmcnt(15)\n\t" "ds_write_b128 v20, v[208:211] offset:16384\n\t" \
   "v_mfma_f32_32x32x16_bf16 v[48:63], a[200:203], v[64:67], v[48:63]\n\t" "s_waitcnt vmcnt(14)\n\t" "ds_write_b128 v20, v[212:215] offset:20480\n\t" \
   "v_mfma_f32_32x32x16_bf16 v[48:63], a[204:207], v[64:67], v[48:63]\n\t" "s_waitcnt vmcnt(13)\n\t" "ds_write_b128 v20, v[224:227] offset:24576\n\t" \
   "v_mfma_f32_32x32x16_bf16 v[48:63], a[216:219], v[64:67], v[48:63]\n\t" "s_waitcnt vmcnt(12)\n\t" "ds_write_b128 v20, v[228:231] offset:28672\n\t" \
   "v_mfma_f32_32x32x16_bf16 v[48:63], a[220:223], v[64:67], v[48:63]\n\t" "s_waitcnt vmcnt(11)\n\t" "ds_write_b128 v20, v[240:243] offset:32768\n\t" \
   "v_mfma_f32_32x32x16_bf16 v[48:63], a[232:235], v[64:67], v[48:63]\n\t" "s_waitcnt vmcnt(10)\n\t" "ds_write_b128 v20, v[244:247] offset:36864\n\t" \
   "v_mfma_f32_32x32x16_bf16 v[48:63], a[236:239], v[64:67], v[48:63]\n\t" "buffer_load_dwordx4 v[28:31], v112, %[rw], 0 offen sc1\n\t" \
   "s_waitcnt lgkmcnt(0)\n\t" "s_barrier\n\t" LDSTAMP(8) "ds_read_b64_tr_b16 a[240:241], v18 offset:0\n\t" \
   "ds_read_b64_tr_b16 a[242:243], v18 offset:4096\n\t" "ds_read_b64_tr_b16 a[244:245], v19 offset:0\n\t" \
   "ds_read_b64_tr_b16 a[246:247], v19 offset:4096\n\t" "ds_read_b64_tr_b16 v[192:193], v18 offset:8192\n\t" \
   "ds_read_b64_tr_b16 v[194:195], v18 offset:12288\n\t" "ds_read_b64_tr_b16 v[196:197], v19 offset:8192\n\t" \
   "ds_read_b64_tr_b16 v[198:199], v19 offset:12288\n\t" "ds_read_b64_tr_b16 v[208:209], v18 offset:16384\n\t" \
   "ds_read_b64_tr_b16 v[210:211], v18 offset:20480\n\t" "ds_read_b64_tr_b16 v[212:213], v19 offset:16384\n\t" \
   "ds_read_b64_tr_b16 v[214:215], v19 offset:20480\n\t" "ds_read_b64_tr_b16 v[224:225], v18 offset:24576\n\t" \
   "ds_read_b64_tr_b16 v[226:227], v18 offset:28672\n\t" "ds_read_b64_tr_b16 v[228:229], v19 offset:24576\n\t" \
   "ds_read_b64_tr_b16 v[230:231], v19 offset:28672\n\t" "ds_read_b64_tr_b16 v[240:241], v18 offset:32768\n\t" \
   "ds_read_b64_tr_b16 v[242:243], v18 offset:36864\n\t" "ds_read_b64_tr_b16 v[244:245], v19 offset:32768\n\t" \
   "ds_read_b64_tr_b16 v[246:247], v19 offset:36864\n\t" "s_waitcnt vmcnt(10)\n\t" "s_waitcnt lgkmcnt(0)\n\t" "s_barrier\n\t" \
   "ds_write_b128 v20, a[248:251] offset:0\n\t" "v_mfma_f32_32x32x16_bf16 v[32:47], a[240:243], v[64:67], v[32:47]\n\t" "s_waitcnt vmcnt(9)\n\t" \
   "ds_write_b128 v20, a[252:255] offset:4096\n\t" "v_mfma_f32_32x32x16_bf16 v[32:47], a[244:247], v[64:67], v[32:47]\n\t" "s_waitcnt vmcnt(8)\n\t" \
   "ds_write_b128 v20, v[200:203] offset:8192\n\t" "v_mfma_f32_32x32x16_bf16 v[32:47], v[192:195], v[64:67], v[32:47]\n\t" "s_waitcnt vmcnt(7)\n\t" \
   "ds_write_b128 v20, v[204:207] offset:12288\n\t" "v_mfma_f32_32x32x16_bf16 v[32:47], v[196:199], v[64:67], v[32:47]\n\t" "s_waitcnt vmcnt(6)\n\t" \
   "ds_write_b128 v20, v[216:219] offset:16384\n\t" "v_mfma_f32_32x32x16_bf16 v[32:47], v[208:211], v[64:67], v[32:47]\n\t" "s_waitcnt vmcnt(5)\n\t" \
   "ds_write_b128 v20, v[220:223] offset:20480\n\t" "v_mfma_f32_32x32x16_bf16 v[32:47], v[212:215], v[64:67], v[32:47]\n\t" "s_waitcnt vmcnt(4)\n\t" \
   "ds_write_b128 v20, v[232:235] offset:24576\n\t" "v_mfma_f32_32x32x16_bf16 v[32:47], v[224:227], v[64:67], v[32:47]\n\t" "s_waitcnt vmcnt(3)\n\t" \
   "ds_write_b128 v20, v[236:239] offset:28672\n\t" "v_mfma_f32_32x32x16_bf16 v[32:47], v[228:231], v[64:67], v[32:47]\n\t" "s_waitcnt vmcnt(2)\n\t" \
   "ds_write_b128 v20, v[248:251] offset:32768\n\t" "v_mfma_f32_32x32x16_bf16 v[32:47], v[240:243], v[64:67], v[32:47]\n\t" "s_waitcnt vmcnt(1)\n\t" \
   "ds_write_b128 v20, v[252:255] offset:36864\n\t" "v_mfma_f32_32x32x16_bf16 v[32:47], v[244:247], v[64:67], v[32:47]\n\t" "s_waitcnt lgkmcnt(0)\n\t" \
   "s_barrier\n\t" LDSTAMP(9) "ds_read_b64_tr_b16 a[248:249], v18 offset:0\n\t" "ds_read_b64_tr_b16 a[250:251], v18 offset:4096\n\t" \
   "ds_read_b64_tr_b16 a[252:253], v19 offset:0\n\t" "ds_read_b64_tr_b16 a[254:255], v19 offset:4096\n\t" \
   "ds_read_b64_tr_b16 v[200:201], v18 offset:8192\n\t" "ds_read_b64_tr_b16 v[202:203], v18 offset:12288\n\t" \
   "ds_read_b64_tr_b16 v[204:205], v19 offset:8192\n\t" "ds_read_b64_tr_b16 v[206:207], v19 offset:12288\n\t" \
   "ds_read_b64_tr_b16 v[216:217], v18 offset:16384\n\t" "ds_read_b64_tr_b16 v[218:219], v18 offset:20480\n\t" \
   "ds_read_b64_tr_b16 v[220:221], v19 offset:16384\n\t" "ds_read_b64_tr_b16 v[222:223], v19 offset:20480\n\t" \
   "ds_read_b64_tr_b16 v[232:233], v18 offset:24576\n\t" "ds_read_b64_tr_b16 v[234:235], v18 offset:28672\n\t" \
   "ds_read_b64_tr_b16 v[236:237], v19 offset:24576\n\t" "ds_read_b64_tr_b16 v[238:239], v19 offset:28672\n\t" \
   "ds_read_b64_tr_b16 v[248:249], v18 offset:32768\n\t" "ds_read_b64_tr_b16 v[250:251], v18 offset:36864\n\t" \
   "ds_read_b64_tr_b16 v[252:253], v19 offset:32768\n\t" "ds_read_b64_tr_b16 v[254:255], v19 offset:36864\n\t" "s_waitcnt vmcnt(0) lgkmcnt(0)\n\t" \
   "v_mfma_f32_32x32x16_bf16 v[48:63], a[248:251], v[64:67], v[48:63]\n\t" "v_mfma_f32_32x32x16_bf16 v[48:63], a[252:255], v[64:67], v[48:63]\n\t" \
   "v_mfma_f32_32x32x16_bf16 v[48:63], v[200:203], v[64:67], v[48:63]\n\t" "v_mfma_f32_32x32x16_bf16 v[48:63], v[204:207], v[64:67], v[48:63]\n\t" \
   "v_mfma_f32_32x32x16_bf16 v[48:63], v[216:219], v[64:67], v[48:63]\n\t" "v_mfma_f32_32x32x16_bf16 v[48:63], v[220:223], v[64:67], v[48:63]\n\t" \
   "v_mfma_f32_32x32x16_bf16 v[48:63], v[232:235], v[64:67], v[48:63]\n\t" "v_mfma_f32_32x32x16_bf16 v[48:63], v[236:239], v[64:67], v[48:63]\n\t" \
   "v_mfma_f32_32x32x16_bf16 v[48:63], v[248:251], v[64:67], v[48:63]\n\t" "v_mfma_f32_32x32x16_bf16 v[48:63], v[252:255], v[64:67], v[48:63]\n\t" \
   "s_mov_b32 exec_lo, 1\n\t" "s_mov_b32 exec_hi, 1\n\t" "s_nop 7\n\t" "s_nop 3\n\t" "ds_write_b128 v27, v[32:35] offset:0\n\t" \
   "ds_write_b128 v27, v[36:39] offset:16\n\t" "ds_write_b128 v27, v[40:43] offset:32\n\t" "ds_write_b128 v27, v[44:47] offset:48\n\t" \
   "ds_write_b128 v27, v[48:51] offset:64\n\t" "ds_write_b128 v27, v[52:55] offset:80\n\t" "ds_write_b128 v27, v[56:59] offset:96\n\t" \
   "ds_write_b128 v27, v[60:63] offset:112\n\t" "s_mov_b64 exec, -1\n\t" "s_waitcnt lgkmcnt(0)\n\t" "s_barrier"
#define LDK_CLOB "v24", "v25", "v26", "v32", "v33", "v34", "v35", "v36", "v37", "v38", "v39", "v40", "v41", "v42", "v43", "v44", "v45", "v46", "v47", "v48", "v49", "v50", "v51", "v52", "v53", "v54", "v55", "v56", "v57", "v58", "v59", "v60", "v61", "v62", "v63", "v64", "v65", "v66", "v67", "v68", "v69", "v70", "v71", "v72", "v73", "v74", "v75", "v76", "v77", "v78", "v79", "v80", "v81", "v82", "v83", "v84", "v85", "v86", "v87", "v88", "v89", "v90", "v91", "v92", "v93", "v94", "v95", "v96", "v97", "v98", "v99", "v100", "v101", "v102", "v103", "v104", "v105", "v106", "v107", "v108", "v109", "v110", "v111", "v120", "v121", "v122", "v123", "s40", "s41"
#define LDV_ASM \
   "buffer_load_dword v26, v113, %[rw], 0 offen sc1\n\t" "s_mov_b32 s41, 0\n\t" "buffer_load_dwordx4 v[32:35], v116, %[rx], s41 offen\n\t" \
   "s_mov_b32 s41, 10240\n\t" "buffer_load_dwordx4 v[36:39], v116, %[rx], s41 offen\n\t" "s_mov_b32 s41, 128\n\t" \
   "buffer_load_dwordx4 v[40:43], v116, %[rx], s41 offen\n\t" "s_mov_b32 s41, 10368\n\t" "buffer_load_dwordx4 v[44:47], v116, %[rx], s41 offen\n\t" \
   "s_mov_b32 s41, 256\n\t" "buffer_load_dwordx4 v[48:51], v116, %[rx], s41 offen\n\t" "s_mov_b32 s41, 10496\n\t" \
   "buffer_load_dwordx4 v[52:55], v116, %[rx], s41 offen\n\t" "s_mov_b32 s41, 384\n\t" "buffer_load_dwordx4 v[56:59], v116, %[rx], s41 offen\n\t" \
   "s_mov_b32 s41, 10624\n\t" "buffer_load_dwordx4 v[60:63], v116, %[rx], s41 offen\n\t" "s_mov_b32 s41, 512\n\t" \
   "buffer_load_dwordx4 v[64:67], v116, %[rx], s41 offen\n\t" "s_mov_b32 s41, 10752\n\t" "buffer_load_dwordx4 v[68:71], v116, %[rx], s41 offen\n\t" \
   "s_mov_b32 s41, 640\n\t" "buffer_load_dwordx4 v[72:75], v116, %[rx], s41 offen\n\t" "s_mov_b32 s41, 10880\n\t" \
   "buffer_load_dwordx4 v[76:79], v116, %[rx], s41 offen\n\t" "s_mov_b32 s41, 768\n\t" "buffer_load_dwordx4 v[80:83], v116, %[rx], s41 offen\n\t" \
   "s_mov_b32 s41, 11008\n\t" "buffer_load_dwordx4 v[84:87], v116, %[rx], s41 offen\n\t" "s_mov_b32 s41, 896\n\t" \
   "buffer_load_dwordx4 v[88:91], v116, %[rx], s41 offen\n\t" "s_mov_b32 s41, 11136\n\t" "buffer_load_dwordx4 v[92:95], v116, %[rx], s41 offen\n\t" \
   "s_mov_b32 s40, 0x3f803f80\n\t" "v_mov_b32 v120, 0\n\t" "v_mov_b32 v121, 0\n\t" "v_mov_b32 v122, 0\n\t" "v_mov_b32 v123, 0\n\t" \
   "s_waitcnt vmcnt(16)\n\t" "v_add_u32 v26, 1, v26\n\t" "buffer_store_dword v26, v114, %[rw], 0 offen sc1\n\t" "ds_write_b32 v115, v26\n\t" LDSTAMP(0) \
   "s_waitcnt vmcnt(15)\n\t" LDSTAMP(1) "ds_write_b64 v117, v[32:33] offset:0\n\t" "ds_write_b64 v117, v[34:35] offset:256\n\t" \
   "s_mov_b32 s41, 1024\n\t" "buffer_load_dwordx4 v[96:99], v116, %[rx], s41 offen\n\t" "v_dot2c_f32_bf16 v120, s40, v32\n\t" \
   "v_dot2c_f32_bf16 v121, v32, v32\n\t" "v_dot2c_f32_bf16 v120, s40, v33\n\t" "v_dot2c_f32_bf16 v121, v33, v33\n\t" \
   "v_dot2c_f32_bf16 v120, s40, v34\n\t" "v_dot2c_f32_bf16 v121, v34, v34\n\t" "v_dot2c_f32_bf16 v120, s40, v35\n\t" \
   "v_dot2c_f32_bf16 v121, v35, v35\n\t" "s_waitcnt vmcnt(15)\n\t" "ds_write_b64 v117, v[36:37] offset:128\n\t" \
   "ds_write_b64 v117, v[38:39] offset:384\n\t" "s_mov_b32 s41, 11264\n\t" "buffer_load_dwordx4 v[100:103], v116, %[rx], s41 offen\n\t" \
   "v_dot2c_f32_bf16 v122, s40, v36\n\t" "v_dot2c_f32_bf16 v123, v36, v36\n\t" "v_dot2c_f32_bf16 v122, s40, v37\n\t" \
   "v_dot2c_f32_bf16 v123, v37, v37\n\t" "v_dot2c_f32_bf16 v122, s40, v38\n\t" "v_dot2c_f32_bf16 v123, v38, v38\n\t" \
   "v_dot2c_f32_bf16 v122, s40, v39\n\t" "v_dot2c_f32_bf16 v123, v39, v39\n\t" "s_waitcnt vmcnt(15)\n\t" "ds_write_b64 v117, v[40:41] offset:2048\n\t" \
   "ds_write_b64 v117, v[42:43] offset:2304\n\t" "s_mov_b32 s41, 1152\n\t" "buffer_load_dwordx4 v[104:107], v116, %[rx], s41 offen\n\t" \
   "v_dot2c_f32_bf16 v120, s40, v40\n\t" "v_dot2c_f32_bf16 v121, v40, v40\n\t" "v_dot2c_f32_bf16 v120, s40, v41\n\t" \
   "v_dot2c_f32_bf16 v121, v41, v41\n\t" "v_dot2c_f32_bf16 v120, s40, v42\n\t" "v_dot2c_f32_bf16 v121, v42, v42\n\t" \
   "v_dot2c_f32_bf16 v120, s40, v43\n\t" "v_dot2c_f32_bf16 v121, v43, v43\n\t" "s_waitcnt vmcnt(15)\n\t" "ds_write_b64 v117, v[44:45] offset:2176\n\t" \
   "ds_write_b64 v117, v[46:47] offset:2432\n\t" "s_mov_b32 s41, 11392\n\t" "buffer_load_dwordx4 v[108:111], v116, %[rx], s41 offen\n\t" \
   "v_dot2c_f32_bf16 v122, s40, v44\n\t" "v_dot2c_f32_bf16 v123, v44, v44\n\t" "v_dot2c_f32_bf16 v122, s40, v45\n\t" \
   "v_dot2c_f32_bf16 v123, v45, v45\n\t" "v_dot2c_f32_bf16 v122, s40, v46\n\t" "v_dot2c_f32_bf16 v123, v46, v46\n\t" \
   "v_dot2c_f32_bf16 v122, s40, v47\n\t" "v_dot2c_f32_bf16 v123, v47, v47\n\t" "s_waitcnt vmcnt(15)\n\t" "ds_write_b64 v117, v[48:49] offset:4096\n\t" \
   "ds_write_b64 v117, v[50:51] offset:4352\n\t" "s_mov_b32 s41, 0\n\t" "buffer_load_dwordx4 a[0:3], v16, %[ra], s41 offen\n\t" \
   "v_dot2c_f32_bf16 v120, s40, v48\n\t" "v_dot2c_f32_bf16 v121, v48, v48\n\t" "v_dot2c_f32_bf16 v120, s40, v49\n\t" \
   "v_dot2c_f32_bf16 v121, v49, v49\n\t" "v_dot2c_f32_bf16 v120, s40, v50\n\t" "v_dot2c_f32_bf16 v121, v50, v50\n\t" \
   "v_dot2c_f32_bf16 v120, s40, v51\n\t" "v_dot2c_f32_bf16 v121, v51, v51\n\t" "s_waitcnt vmcnt(15)\n\t" "ds_write_b64 v117, v[52:53] offset:4224\n\t" \
   "ds_write_b64 v117, v[54:55] offset:4480\n\t" "s_mov_b32 s41, 131072\n\t" "buffer_load_dwordx4 a[4:7], v16, %[ra], s41 offen\n\t" \
   "v_dot2c_f32_bf16 v122, s40, v52\n\t" "v_dot2c_f32_bf16 v123, v52, v52\n\t" "v_dot2c_f32_bf16 v122, s40, v53\n\t" \
   "v_dot2c_f32_bf16 v123, v53, v53\n\t" "v_dot2c_f32_bf16 v122, s40, v54\n\t" "v_dot2c_f32_bf16 v123, v54, v54\n\t" \
   "v_dot2c_f32_bf16 v122, s40, v55\n\t" "v_dot2c_f32_bf16 v123, v55, v55\n\t" "s_waitcnt vmcnt(15)\n\t" "ds_write_b64 v117, v[56:57] offset:6144\n\t" \
   "ds_write_b64 v117, v[58:59] offset:6400\n\t" "s_mov_b32 s41, 262144\n\t" "buffer_load_dwordx4 a[8:11], v16, %[ra], s41 offen\n\t" \
   "v_dot2c_f32_bf16 v120, s40, v56\n\t" "v_dot2c_f32_bf16 v121, v56, v56\n\t" "v_dot2c_f32_bf16 v120, s40, v57\n\t" \
   "v_dot2c_f32_bf16 v121, v57, v57\n\t" "v_dot2c_f32_bf16 v120, s40, v58\n\t" "v_dot2c_f32_bf16 v121, v58, v58\n\t" \
   "v_dot2c_f32_bf16 v120, s40, v59\n\t" "v_dot2c_f32_bf16 v121, v59, v59\n\t" "s_waitcnt vmcnt(15)\n\t" "ds_write_b64 v117, v[60:61] offset:6272\n\t" \
   "ds_write_b64 v117, v[62:63] offset:6528\n\t" "s_mov_b32 s41, 393216\n\t" "buffer_load_dwordx4 a[12:15], v16, %[ra], s41 offen\n\t" \
   "v_dot2c_f32_bf16 v122, s40, v60\n\t" "v_dot2c_f32_bf16 v123, v60, v60\n\t" "v_dot2c_f32_bf16 v122, s40, v61\n\t" \
   "v_dot2c_f32_bf16 v123, v61, v61\n\t" "v_dot2c_f32_bf16 v122, s40, v62\n\t" "v_dot2c_f32_bf16 v123, v62, v62\n\t" \
   "v_dot2c_f32_bf16 v122, s40, v63\n\t" "v_dot2c_f32_bf16 v123, v63, v63\n\t" "s_waitcnt vmcnt(15)\n\t" "ds_write_b64 v117, v[64:65] offset:8192\n\t" \
   "ds_write_b64 v117, v[66:67] offset:8448\n\t" "s_mov_b32 s41, 524288\n\t" "buffer_load_dwordx4 a[16:19], v16, %[ra], s41 offen\n\t" \
   "v_dot2c_f32_bf16 v120, s40, v64\n\t" "v_dot2c_f32_bf16 v121, v64, v64\n\t" "v_dot2c_f32_bf16 v120, s40, v65\n\t" \
   "v_dot2c_f32_bf16 v121, v65, v65\n\t" "v_dot2c_f32_bf16 v120, s40, v66\n\t" "v_dot2c_f32_bf16 v121, v66, v66\n\t" \
   "v_dot2c_f32_bf16 v120, s40, v67\n\t" "v_dot2c_f32_bf16 v121, v67, v67\n\t" "s_waitcnt vmcnt(15)\n\t" "ds_write_b64 v117, v[68:69] offset:8320\n\t" \
   "ds_write_b64 v117, v[70:71] offset:8576\n\t" "s_mov_b32 s41, 655360\n\t" "buffer_load_dwordx4 a[20:23], v16, %[ra], s41 offen\n\t" \
   "v_dot2c_f32_bf16 v122, s40, v68\n\t" "v_dot2c_f32_bf16 v123, v68, v68\n\t" "v_dot2c_f32_bf16 v122, s40, v69\n\t" \
   "v_dot2c_f32_bf16 v123, v69, v69\n\t" "v_dot2c_f32_bf16 v122, s40, v70\n\t" "v_dot2c_f32_bf16 v123, v70, v70\n\t" \
   "v_dot2c_f32_bf16 v122, s40, v71\n\t" "v_dot2c_f32_bf16 v123, v71, v71\n\t" "s_waitcnt vmcnt(15)\n\t" "ds_write_b64 v117, v[72:73] offset:10240\n\t" \
   "ds_write_b64 v117, v[74:75] offset:10496\n\t" "s_mov_b32 s41, 786432\n\t" "buffer_load_dwordx4 a[24:27], v16, %[ra], s41 offen\n\t" \
   "v_dot2c_f32_bf16 v120, s40, v72\n\t" "v_dot2c_f32_bf16 v121, v72, v72\n\t" "v_dot2c_f32_bf16 v120, s40, v73\n\t" \
   "v_dot2c_f32_bf16 v121, v73, v73\n\t" "v_dot2c_f32_bf16 v120, s40, v74\n\t" "v_dot2c_f32_bf16 v121, v74, v74\n\t" \
   "v_dot2c_f32_bf16 v120, s40, v75\n\t" "v_dot2c_f32_bf16 v121, v75, v75\n\t" "s_waitcnt vmcnt(15)\n\t" "ds_write_b64 v117, v[76:77] offset:10368\n\t" \
   "ds_write_b64 v117, v[78:79] offset:10624\n\t" "s_mov_b32 s41, 917504\n\t" "buffer_load_dwordx4 a[28:31], v16, %[ra], s41 offen\n\t" \
   "v_dot2c_f32_bf16 v122, s40, v76\n\t" "v_dot2c_f32_bf16 v123, v76, v76\n\t" "v_dot2c_f32_bf16 v122, s40, v77\n\t" \
   "v_dot2c_f32_bf16 v123, v77, v77\n\t" "v_dot2c_f32_bf16 v122, s40, v78\n\t" "v_dot2c_f32_bf16 v123, v78, v78\n\t" \
   "v_dot2c_f32_bf16 v122, s40, v79\n\t" "v_dot2c_f32_bf16 v123, v79, v79\n\t" "s_waitcnt vmcnt(15)\n\t" "ds_write_b64 v117, v[80:81] offset:12288\n\t" \
   "ds_write_b64 v117, v[82:83] offset:12544\n\t" "s_mov_b32 s41, 1048576\n\t" "buffer_load_dwordx4 a[32:35], v16, %[ra], s41 offen\n\t" \
   "v_dot2c_f32_bf16 v120, s40, v80\n\t" "v_dot2c_f32_bf16 v121, v80, v80\n\t" "v_dot2c_f32_bf16 v120, s40, v81\n\t" \
   "v_dot2c_f32_bf16 v121, v81, v81\n\t" "v_dot2c_f32_bf16 v120, s40, v82\n\t" "v_dot2c_f32_bf16 v121, v82, v82\n\t" \
   "v_dot2c_f32_bf16 v120, s40, v83\n\t" "v_dot2c_f32_bf16 v121, v83, v83\n\t" "s_waitcnt vmcnt(15)\n\t" "ds_write_b64 v117, v[84:85] offset:12416\n\t" \
   "ds_write_b64 v117, v[86:87] offset:12672\n\t" "s_mov_b32 s41, 1179648\n\t" "buffer_load_dwordx4 a[36:39], v16, %[ra], s41 offen\n\t" \
   "v_dot2c_f32_bf16 v122, s40, v84\n\t" "v_dot2c_f32_bf16 v123, v84, v84\n\t" "v_dot2c_f32_bf16 v122, s40, v85\n\t" \
   "v_dot2c_f32_bf16 v123, v85, v85\n\t" "v_dot2c_f32_bf16 v122, s40, v86\n\t" "v_dot2c_f32_bf16 v123, v86, v86\n\t" \
   "v_dot2c_f32_bf16 v122, s40, v87\n\t" "v_dot2c_f32_bf16 v123, v87, v87\n\t" "s_waitcnt vmcnt(15)\n\t" "ds_write_b64 v117, v[88:89] offset:14336\n\t" \
   "ds_write_b64 v117, v[90:91] offset:14592\n\t" "s_mov_b32 s41, 1310720\n\t" "buffer_load_dwordx4 a[40:43], v16, %[ra], s41 offen\n\t" \
   "v_dot2c_f32_bf16 v120, s40, v88\n\t" "v_dot2c_f32_bf16 v121, v88, v88\n\t" "v_dot2c_f32_bf16 v120, s40, v89\n\t" \
   "v_dot2c_f32_bf16 v121, v89, v89\n\t" "v_dot2c_f32_bf16 v120, s40, v90\n\t" "v_dot2c_f32_bf16 v121, v90, v90\n\t" \
   "v_dot2c_f32_bf16 v120, s40, v91\n\t" "v_dot2c_f32_bf16 v121, v91, v91\n\t" "s_waitcnt vmcnt(15)\n\t" "ds_write_b64 v117, v[92:93] offset:14464\n\t" \
   "ds_write_b64 v117, v[94:95] offset:14720\n\t" "s_mov_b32 s41, 1441792\n\t" "buffer_load_dwordx4 a[44:47], v16, %[ra], s41 offen\n\t" \
   "v_dot2c_f32_bf16 v122, s40, v92\n\t" "v_dot2c_f32_bf16 v123, v92, v92\n\t" "v_dot2c_f32_bf16 v122, s40, v93\n\t" \
   "v_dot2c_f32_bf16 v123, v93, v93\n\t" "v_dot2c_f32_bf16 v122, s40, v94\n\t" "v_dot2c_f32_bf16 v123, v94, v94\n\t" \
   "v_dot2c_f32_bf16 v122, s40, v95\n\t" "v_dot2c_f32_bf16 v123, v95, v95\n\t" "s_waitcnt vmcnt(15)\n\t" "ds_write_b64 v117, v[96:97] offset:16384\n\t" \
   "ds_write_b64 v117, v[98:99] offset:16640\n\t" "s_mov_b32 s41, 1572864\n\t" "buffer_load_dwordx4 a[48:51], v16, %[ra], s41 offen\n\t" \
   "v_dot2c_f32_bf16 v120, s40, v96\n\t" "v_dot2c_f32_bf16 v121, v96, v96\n\t" "v_dot2c_f32_bf16 v120, s40, v97\n\t" \
   "v_dot2c_f32_bf16 v121, v97, v97\n\t" "v_dot2c_f32_bf16 v120, s40, v98\n\t" "v_dot2c_f32_bf16 v121, v98, v98\n\t" \
   "v_dot2c_f32_bf16 v120, s40, v99\n\t" "v_dot2c_f32_bf16 v121, v99, v99\n\t" "s_waitcnt vmcnt(15)\n\t" \
   "ds_write_b64 v117, v[100:101] offset:16512\n\t" "ds_write_b64 v117, v[102:103] offset:16768\n\t" "s_mov_b32 s41, 1703936\n\t" \
   "buffer_load_dwordx4 a[52:55], v16, %[ra], s41 offen\n\t" "v_dot2c_f32_bf16 v122, s40, v100\n\t" "v_dot2c_f32_bf16 v123, v100, v100\n\t" \
   "v_dot2c_f32_bf16 v122, s40, v101\n\t" "v_dot2c_f32_bf16 v123, v101, v101\n\t" "v_dot2c_f32_bf16 v122, s40, v102\n\t" \
   "v_dot2c_f32_bf16 v123, v102, v102\n\t" "v_dot2c_f32_bf16 v122, s40, v103\n\t" "v_dot2c_f32_bf16 v123, v103, v103\n\t" "s_waitcnt vmcnt(15)\n\t" \
   "ds_write_b64 v117, v[104:105] offset:18432\n\t" "ds_write_b64 v117, v[106:107] offset:18688\n\t" "s_mov_b32 s41, 1835008\n\t" \
   "buffer_load_dwordx4 a[56:59], v16, %[ra], s41 offen\n\t" "v_dot2c_f32_bf16 v120, s40, v104\n\t" "v_dot2c_f32_bf16 v121, v104, v104\n\t" \
   "v_dot2c_f32_bf16 v120, s40, v105\n\t" "v_dot2c_f32_bf16 v121, v105, v105\n\t" "v_dot2c_f32_bf16 v120, s40, v106\n\t" \
   "v_dot2c_f32_bf16 v121, v106, v106\n\t" "v_dot2c_f32_bf16 v120, s40, v107\n\t" "v_dot2c_f32_bf16 v121, v107, v107\n\t" "s_waitcnt vmcnt(15)\n\t" \
   "ds_write_b64 v117, v[108:109] offset:18560\n\t" "ds_write_b64 v117, v[110:111] offset:18816\n\t" "s_mov_b32 s41, 1966080\n\t" \
   "buffer_load_dwordx4 a[60:63], v16, %[ra], s41 offen\n\t" "v_dot2c_f32_bf16 v122, s40, v108\n\t" "v_dot2c_f32_bf16 v123, v108, v108\n\t" \
   "v_dot2c_f32_bf16 v122, s40, v109\n\t" "v_dot2c_f32_bf16 v123, v109, v109\n\t" "v_dot2c_f32_bf16 v122, s40, v110\n\t" \
   "v_dot2c_f32_bf16 v123, v110, v110\n\t" "v_dot2c_f32_bf16 v122, s40, v111\n\t" "v_dot2c_f32_bf16 v123, v111, v111\n\t" "s_nop 3\n\t" \
   "v_add_f32_dpp v120, v120, v120 quad_perm:[1,0,3,2] row_mask:0xf bank_mask:0xf\n\t" \
   "v_add_f32_dpp v121, v121, v121 quad_perm:[1,0,3,2] row_mask:0xf bank_mask:0xf\n\t" \
   "v_add_f32_dpp v122, v122, v122 quad_perm:[1,0,3,2] row_mask:0xf bank_mask:0xf\n\t" \
   "v_add_f32_dpp v123, v123, v123 quad_perm:[1,0,3,2] row_mask:0xf bank_mask:0xf\n\t" \
   "v_add_f32_dpp v120, v120, v120 quad_perm:[2,3,0,1] row_mask:0xf bank_mask:0xf\n\t" \
   "v_add_f32_dpp v121, v121, v121 quad_perm:[2,3,0,1] row_mask:0xf bank_mask:0xf\n\t" \
   "v_add_f32_dpp v122, v122, v122 quad_perm:[2,3,0,1] row_mask:0xf bank_mask:0xf\n\t" \
   "v_add_f32_dpp v123, v123, v123 quad_perm:[2,3,0,1] row_mask:0xf bank_mask:0xf\n\t" \
   "v_add_f32_dpp v120, v120, v120 row_half_mirror row_mask:0xf bank_mask:0xf\n\t" \
   "v_add_f32_dpp v121, v121, v121 row_half_mirror row_mask:0xf bank_mask:0xf\n\t" \
   "v_add_f32_dpp v122, v122, v122 row_half_mirror row_mask:0xf bank_mask:0xf\n\t" \
   "v_add_f32_dpp v123, v123, v123 row_half_mirror row_mask:0xf bank_mask:0xf\n\t" "v_and_b32 v120, v119, v120\n\t" "v_and_b32 v121, v119, v121\n\t" \
   "v_and_b32 v122, v119, v122\n\t" "v_and_b32 v123, v119, v123\n\t" "ds_write_b64 v118, v[120:121]\n\t" "ds_write_b64 v118, v[122:123] offset:384\n\t" \
   "s_waitcnt vmcnt(15)\n\t" "ds_write_b128 v20, a[0:3] offset:0\n\t" "s_mov_b32 s41, 2097152\n\t" \
   "buffer_load_dwordx4 a[64:67], v16, %[ra], s41 offen\n\t" "s_waitcnt vmcnt(15)\n\t" "ds_write_b128 v20, a[4:7] offset:4096\n\t" \
   "s_mov_b32 s41, 2228224\n\t" "buffer_load_dwordx4 a[68:71], v16, %[ra], s41 offen\n\t" "s_waitcnt vmcnt(15)\n\t" \
   "ds_write_b128 v20, a[8:11] offset:8192\n\t" "s_mov_b32 s41, 2359296\n\t" "buffer_load_dwordx4 a[72:75], v16, %[ra], s41 offen\n\t" \
   "s_waitcnt vmcnt(15)\n\t" "ds_write_b128 v20, a[12:15] offset:12288\n\t" "s_mov_b32 s41, 2490368\n\t" \
   "buffer_load_dwordx4 a[76:79], v16, %[ra], s41 offen\n\t" "s_waitcnt vmcnt(15)\n\t" "ds_write_b128 v20, a[16:19] offset:16384\n\t" \
   "s_mov_b32 s41, 2621440\n\t" "buffer_load_dwordx4 a[80:83], v16, %[ra], s41 offen\n\t" "s_waitcnt vmcnt(15)\n\t" \
   "ds_write_b128 v20, a[20:23] offset:20480\n\t" "s_mov_b32 s41, 2752512\n\t" "buffer_load_dwordx4 a[84:87], v16, %[ra], s41 offen\n\t" \
   "s_waitcnt vmcnt(15)\n\t" "ds_write_b128 v20, a[24:27] offset:24576\n\t" "s_mov_b32 s41, 2883584\n\t" \
   "buffer_load_dwordx4 a[88:91], v16, %[ra], s41 offen\n\t" "s_waitcnt vmcnt(15)\n\t" "ds_write_b128 v20, a[28:31] offset:28672\n\t" \
   "s_mov_b32 s41, 3014656\n\t" "buffer_load_dwordx4 a[92:95], v16, %[ra], s41 offen\n\t" "s_waitcnt vmcnt(15)\n\t" \
   "ds_write_b128 v20, a[32:35] offset:32768\n\t" "s_mov_b32 s41, 3145728\n\t" "buffer_load_dwordx4 a[96:99], v16, %[ra], s41 offen\n\t" \
   "s_waitcnt vmcnt(15)\n\t" "ds_write_b128 v20, a[36:39] offset:36864\n\t" "s_mov_b32 s41, 3276800\n\t" \
   "buffer_load_dwordx4 a[100:103], v16, %[ra], s41 offen\n\t" "s_waitcnt lgkmcnt(0)\n\t" "s_barrier\n\t" LDSTAMP(2) \
   "ds_read_b64_tr_b16 a[0:1], v18 offset:0\n\t" "ds_read_b64_tr_b16 a[2:3], v18 offset:4096\n\t" "ds_read_b64_tr_b16 a[4:5], v19 offset:0\n\t" \
   "ds_read_b64_tr_b16 a[6:7], v19 offset:4096\n\t" "ds_read_b64_tr_b16 a[8:9], v18 offset:8192\n\t" \
   "ds_read_b64_tr_b16 a[10:11], v18 offset:12288\n\t" "ds_read_b64_tr_b16 a[12:13], v19 offset:8192\n\t" \
   "ds_read_b64_tr_b16 a[14:15], v19 offset:12288\n\t" "ds_read_b64_tr_b16 a[16:17], v18 offset:16384\n\t" \
   "ds_read_b64_tr_b16 a[18:19], v18 offset:20480\n\t" "ds_read_b64_tr_b16 a[20:21], v19 offset:16384\n\t" \
   "ds_read_b64_tr_b16 a[22:23], v19 offset:20480\n\t" "ds_read_b64_tr_b16 a[24:25], v18 offset:24576\n\t" \
   "ds_read_b64_tr_b16 a[26:27], v18 offset:28672\n\t" "ds_read_b64_tr_b16 a[28:29], v19 offset:24576\n\t" \
   "ds_read_b64_tr_b16 a[30:31], v19 offset:28672\n\t" "ds_read_b64_tr_b16 a[32:33], v18 offset:32768\n\t" \
   "ds_read_b64_tr_b16 a[34:35], v18 offset:36864\n\t" "ds_read_b64_tr_b16 a[36:37], v19 offset:32768\n\t" \
   "ds_read_b64_tr_b16 a[38:39], v19 offset:36864\n\t" "s_waitcnt vmcnt(15)\n\t" "s_waitcnt lgkmcnt(0)\n\t" "s_barrier\n\t" \
   "v_mov_b32 v64, 0x3f803f80\n\t" "v_mov_b32 v65, 0x3f803f80\n\t" "v_mov_b32 v66, 0x3f803f80\n\t" "v_mov_b32 v67, 0x3f803f80\n\t" \
   "ds_write_b128 v20, a[40:43] offset:0\n\t" "s_mov_b32 s41, 3407872\n\t" "buffer_load_dwordx4 a[104:107], v16, %[ra], s41 offen\n\t" \
   "v_mfma_f32_32x32x16_bf16 v[32:47], a[0:3], v[64:67], 0\n\t" "s_waitcnt vmcnt(15)\n\t" "ds_write_b128 v20, a[44:47] offset:4096\n\t" \
   "s_mov_b32 s41, 3538944\n\t" "buffer_load_dwordx4 a[108:111], v16, %[ra], s41 offen\n\t" \
   "v_mfma_f32_32x32x16_bf16 v[32:47], a[4:7], v[64:67], v[32:47]\n\t" "s_waitcnt vmcnt(15)\n\t" "ds_write_b128 v20, a[48:51] offset:8192\n\t" \
   "s_mov_b32 s41, 3670016\n\t" "buffer_load_dwordx4 a[112:115], v16, %[ra], s41 offen\n\t" \
   "v_mfma_f32_32x32x16_bf16 v[32:47], a[8:11], v[64:67], v[32:47]\n\t" "s_waitcnt vmcnt(15)\n\t" "ds_write_b128 v20, a[52:55] offset:12288\n\t" \
   "s_mov_b32 s41, 3801088\n\t" "buffer_load_dwordx4 a[116:119], v16, %[ra], s41 offen\n\t" \
   "v_mfma_f32_32x32x16_bf16 v[32:47], a[12:15], v[64:67], v[32:47]\n\t" "s_waitcnt vmcnt(15)\n\t" "ds_write_b128 v20, a[56:59] offset:16384\n\t" \
   "s_mov_b32 s41, 3932160\n\t" "buffer_load_dwordx4 a[120:123], v16, %[ra], s41 offen\n\t" \
   "v_mfma_f32_32x32x16_bf16 v[32:47], a[16:19], v[64:67], v[32:47]\n\t" "s_waitcnt vmcnt(15)\n\t" "ds_write_b128 v20, a[60:63] offset:20480\n\t" \
   "s_mov_b32 s41, 4063232\n\t" "buffer_load_dwordx4 a[124:127], v16, %[ra], s41 offen\n\t" \
   "v_mfma_f32_32x32x16_bf16 v[32:47], a[20:23], v[64:67], v[32:47]\n\t" "s_waitcnt vmcnt(15)\n\t" "ds_write_b128 v20, a[64:67] offset:24576\n\t" \
   "s_mov_b32 s41, 4194304\n\t" "buffer_load_dwordx4 a[128:131], v16, %[ra], s41 offen\n\t" \
   "v_mfma_f32_32x32x16_bf16 v[32:47], a[24:27], v[64:67], v[32:47]\n\t" "s_waitcnt vmcnt(15)\n\t" "ds_write_b128 v20, a[68:71] offset:28672\n\t" \
   "s_mov_b32 s41, 4325376\n\t" "buffer_load_dwordx4 a[132:135], v16, %[ra], s41 offen\n\t" \
   "v_mfma_f32_32x32x16_bf16 v[32:47], a[28:31], v[64:67], v[32:47]\n\t" "s_waitcnt vmcnt(15)\n\t" "ds_write_b128 v20, a[72:75] offset:32768\n\t" \
   "s_mov_b32 s41, 4456448\n\t" "buffer_load_dwordx4 a[136:139], v16, %[ra], s41 offen\n\t" \
   "v_mfma_f32_32x32x16_bf16 v[32:47], a[32:35], v[64:67], v[32:47]\n\t" "s_waitcnt vmcnt(15)\n\t" "ds_write_b128 v20, a[76:79] offset:36864\n\t" \
   "s_mov_b32 s41, 4587520\n\t" "buffer_load_dwordx4 a[140:143], v16, %[ra], s41 offen\n\t" \
   "v_mfma_f32_32x32x16_bf16 v[32:47], a[36:39], v[64:67], v[32:47]\n\t" "s_waitcnt lgkmcnt(0)\n\t" "s_barrier\n\t" LDSTAMP(3) \
   "ds_read_b64_tr_b16 a[40:41], v18 offset:0\n\t" "ds_read_b64_tr_b16 a[42:43], v18 offset:4096\n\t" "ds_read_b64_tr_b16 a[44:45], v19 offset:0\n\t" \
   "ds_read_b64_tr_b16 a[46:47], v19 offset:4096\n\t" "ds_read_b64_tr_b16 a[48:49], v18 offset:8192\n\t" \
   "ds_read_b64_tr_b16 a[50:51], v18 offset:12288\n\t" "ds_read_b64_tr_b16 a[52:53], v19 offset:8192\n\t" \
   "ds_read_b64_tr_b16 a[54:55], v19 offset:12288\n\t" "ds_read_b64_tr_b16 a[56:57], v18 offset:16384\n\t" \
   "ds_read_b64_tr_b16 a[58:59], v18 offset:20480\n\t" "ds_read_b64_tr_b16 a[60:61], v19 offset:16384\n\t" \
   "ds_read_b64_tr_b16 a[62:63], v19 offset:20480\n\t" "ds_read_b64_tr_b16 a[64:65], v18 offset:24576\n\t" \
   "ds_read_b64_tr_b16 a[66:67], v18 offset:28672\n\t" "ds_read_b64_tr_b16 a[68:69], v19 offset:24576\n\t" \
   "ds_read_b64_tr_b16 a[70:71], v19 offset:28672\n\t" "ds_read_b64_tr_b16 a[72:73], v18 offset:32768\n\t" \
   "ds_read_b64_tr_b16 a[74:75], v18 offset:36864\n\t" "ds_read_b64_tr_b16 a[76:77], v19 offset:32768\n\t" \
   "ds_read_b64_tr_b16 a[78:79], v19 offset:36864\n\t" "s_waitcnt vmcnt(15)\n\t" "s_waitcnt lgkmcnt(0)\n\t" "s_barrier\n\t" \
   "ds_write_b128 v20, a[80:83] offset:0\n\t" "s_mov_b32 s41, 4718592\n\t" "buffer_load_dwordx4 a[144:147], v16, %[ra], s41 offen\n\t" \
   "v_mfma_f32_32x32x16_bf16 v[32:47], a[40:43], v[64:67], v[32:47]\n\t" "s_waitcnt vmcnt(15)\n\t" "ds_write_b128 v20, a[84:87] offset:4096\n\t" \
   "s_mov_b32 s41, 4849664\n\t" "buffer_load_dwordx4 a[148:151], v16, %[ra], s41 offen\n\t" \
   "v_mfma_f32_32x32x16_bf16 v[32:47], a[44:47], v[64:67], v[32:47]\n\t" "s_waitcnt vmcnt(15)\n\t" "ds_write_b128 v20, a[88:91] offset:8192\n\t" \
   "s_mov_b32 s41, 4980736\n\t" "buffer_load_dwordx4 a[152:155], v16, %[ra], s41 offen\n\t" \
   "v_mfma_f32_32x32x16_bf16 v[32:47], a[48:51], v[64:67], v[32:47]\n\t" "s_waitcnt vmcnt(15)\n\t" "ds_write_b128 v20, a[92:95] offset:12288\n\t" \
   "s_mov_b32 s41, 5111808\n\t" "buffer_load_dwordx4 a[156:159], v16, %[ra], s41 offen\n\t" \
   "v_mfma_f32_32x32x16_bf16 v[32:47], a[52:55], v[64:67], v[32:47]\n\t" "s_waitcnt vmcnt(15)\n\t" "ds_write_b128 v20, a[96:99] offset:16384\n\t" \
   "s_mov_b32 s41, 0\n\t" "buffer_load_dwordx4 a[160:163], v17, %[rb], s41 offen\n\t" \
   "v_mfma_f32_32x32x16_bf16 v[32:47], a[56:59], v[64:67], v[32:47]\n\t" "s_waitcnt vmcnt(15)\n\t" "ds_write_b128 v20, a[100:103] offset:20480\n\t" \
   "s_mov_b32 s41, 256\n\t" "buffer_load_dwordx4 a[176:179], v17, %[rb], s41 offen\n\t" \
   "v_mfma_f32_32x32x16_bf16 v[32:47], a[60:63], v[64:67], v[32:47]\n\t" "s_waitcnt vmcnt(15)\n\t" "ds_write_b128 v20, a[104:107] offset:24576\n\t" \
   "s_mov_b32 s41, 512\n\t" "buffer_load_dwordx4 a[192:195], v17, %[rb], s41 offen\n\t" \
   "v_mfma_f32_32x32x16_bf16 v[32:47], a[64:67], v[64:67], v[32:47]\n\t" "s_waitcnt vmcnt(15)\n\t" "ds_write_b128 v20, a[108:111] offset:28672\n\t" \
   "s_mov_b32 s41, 768\n\t" "buffer_load_dwordx4 a[208:211], v17, %[rb], s41 offen\n\t" \
   "v_mfma_f32_32x32x16_bf16 v[32:47], a[68:71], v[64:67], v[32:47]\n\t" "s_waitcnt vmcnt(15)\n\t" "ds_write_b128 v20, a[112:115] offset:32768\n\t" \
   "s_mov_b32 s41, 1024\n\t" "buffer_load_dwordx4 a[224:227], v17, %[rb], s41 offen\n\t" \
   "v_mfma_f32_32x32x16_bf16 v[32:47], a[72:75], v[64:67], v[32:47]\n\t" "s_waitcnt vmcnt(15)\n\t" "ds_write_b128 v20, a[116:119] offset:36864\n\t" \
   "s_mov_b32 s41, 20480\n\t" "buffer_load_dwordx4 a[240:243], v17, %[rb], s41 offen\n\t" \
   "v_mfma_f32_32x32x16_bf16 v[32:47], a[76:79], v[64:67], v[32:47]\n\t" "s_waitcnt lgkmcnt(0)\n\t" "s_barrier\n\t" LDSTAMP(4) \
   "ds_read_b64_tr_b16 a[80:81], v18 offset:0\n\t" "ds_read_b64_tr_b16 a[82:83], v18 offset:4096\n\t" "ds_read_b64_tr_b16 a[84:85], v19 offset:0\n\t" \
   "ds_read_b64_tr_b16 a[86:87], v19 offset:4096\n\t" "ds_read_b64_tr_b16 a[88:89], v18 offset:8192\n\t" \
   "ds_read_b64_tr_b16 a[90:91], v18 offset:12288\n\t" "ds_read_b64_tr_b16 a[92:93], v19 offset:8192\n\t" \
   "ds_read_b64_tr_b16 a[94:95], v19 offset:12288\n\t" "ds_read_b64_tr_b16 a[96:97], v18 offset:16384\n\t" \
   "ds_read_b64_tr_b16 a[98:99], v18 offset:20480\n\t" "ds_read_b64_tr_b16 a[100:101], v19 offset:16384\n\t" \
   "ds_read_b64_tr_b16 a[102:103], v19 offset:20480\n\t" "ds_read_b64_tr_b16 a[104:105], v18 offset:24576\n\t" \
   "ds_read_b64_tr_b16 a[106:107], v18 offset:28672\n\t" "ds_read_b64_tr_b16 a[108:109], v19 offset:24576\n\t" \
   "ds_read_b64_tr_b16 a[110:111], v19 offset:28672\n\t" "ds_read_b64_tr_b16 a[112:113], v18 offset:32768\n\t" \
   "ds_read_b64_tr_b16 a[114:115], v18 offset:36864\n\t" "ds_read_b64_tr_b16 a[116:117], v19 offset:32768\n\t" \
   "ds_read_b64_tr_b16 a[118:119], v19 offset:36864\n\t" "s_waitcnt vmcnt(15)\n\t" "s_waitcnt lgkmcnt(0)\n\t" "s_barrier\n\t" \
   "ds_write_b128 v20, a[120:123] offset:0\n\t" "s_mov_b32 s41, 20736\n\t" "buffer_load_dwordx4 v[192:195], v17, %[rb], s41 offen\n\t" \
   "v_mfma_f32_32x32x16_bf16 v[32:47], a[80:83], v[64:67], v[32:47]\n\t" "s_waitcnt vmcnt(15)\n\t" "ds_write_b128 v20, a[124:127] offset:4096\n\t" \
   "s_mov_b32 s41, 20992\n\t" "buffer_load_dwordx4 v[208:211], v17, %[rb], s41 offen\n\t" \
   "v_mfma_f32_32x32x16_bf16 v[32:47], a[84:87], v[64:67], v[32:47]\n\t" "s_waitcnt vmcnt(15)\n\t" "ds_write_b128 v20, a[128:131] offset:8192\n\t" \
   "s_mov_b32 s41, 21248\n\t" "buffer_load_dwordx4 v[224:227], v17, %[rb], s41 offen\n\t" \
   "v_mfma_f32_32x32x16_bf16 v[32:47], a[88:91], v[64:67], v[32:47]\n\t" "s_waitcnt vmcnt(15)\n\t" "ds_write_b128 v20, a[132:135] offset:12288\n\t" \
   "s_mov_b32 s41, 21504\n\t" "buffer_load_dwordx4 v[240:243], v17, %[rb], s41 offen\n\t" \
   "v_mfma_f32_32x32x16_bf16 v[32:47], a[92:95], v[64:67], v[32:47]\n\t" "s_waitcnt vmcnt(15)\n\t" "ds_write_b128 v20, a[136:139] offset:16384\n\t" \
   "s_mov_b32 s41, 40960\n\t" "buffer_load_dwordx4 a[164:167], v17, %[rb], s41 offen\n\t" \
   "v_mfma_f32_32x32x16_bf16 v[32:47], a[96:99], v[64:67], v[32:47]\n\t" "s_waitcnt vmcnt(15)\n\t" "ds_write_b128 v20, a[140:143] offset:20480\n\t" \
   "s_mov_b32 s41, 41216\n\t" "buffer_load_dwordx4 a[180:183], v17, %[rb], s41 offen\n\t" \
   "v_mfma_f32_32x32x16_bf16 v[32:47], a[100:103], v[64:67], v[32:47]\n\t" "s_waitcnt vmcnt(15)\n\t" "ds_write_b128 v20, a[144:147] offset:24576\n\t" \
   "s_mov_b32 s41, 41472\n\t" "buffer_load_dwordx4 a[196:199], v17, %[rb], s41 offen\n\t" \
   "v_mfma_f32_32x32x16_bf16 v[32:47], a[104:107], v[64:67], v[32:47]\n\t" "s_waitcnt vmcnt(15)\n\t" "ds_write_b128 v20, a[148:151] offset:28672\n\t" \
   "s_mov_b32 s41, 41728\n\t" "buffer_load_dwordx4 a[212:215], v17, %[rb], s41 offen\n\t" \
   "v_mfma_f32_32x32x16_bf16 v[32:47], a[108:111], v[64:67], v[32:47]\n\t" "s_waitcnt vmcnt(15)\n\t" "ds_write_b128 v20, a[152:155] offset:32768\n\t" \
   "s_mov_b32 s41, 41984\n\t" "buffer_load_dwordx4 a[228:231], v17, %[rb], s41 offen\n\t" \
   "v_mfma_f32_32x32x16_bf16 v[32:47], a[112:115], v[64:67], v[32:47]\n\t" "s_waitcnt vmcnt(15)\n\t" "ds_write_b128 v20, a[156:159] offset:36864\n\t" \
   "s_mov_b32 s41, 61440\n\t" "buffer_load_dwordx4 a[244:247], v17, %[rb], s41 offen\n\t" \
   "v_mfma_f32_32x32x16_bf16 v[32:47], a[116:119], v[64:67], v[32:47]\n\t" "s_waitcnt lgkmcnt(0)\n\t" "s_barrier\n\t" LDSTAMP(5) \
   "ds_read_b64_tr_b16 a[120:121], v18 offset:0\n\t" "ds_read_b64_tr_b16 a[122:123], v18 offset:4096\n\t" \
   "ds_read_b64_tr_b16 a[124:125], v19 offset:0\n\t" "ds_read_b64_tr_b16 a[126:127], v19 offset:4096\n\t" \
   "ds_read_b64_tr_b16 a[128:129], v18 offset:8192\n\t" "ds_read_b64_tr_b16 a[130:131], v18 offset:12288\n\t" \
   "ds_read_b64_tr_b16 a[132:133], v19 offset:8192\n\t" "ds_read_b64_tr_b16 a[134:135], v19 offset:12288\n\t" \
   "ds_read_b64_tr_b16 a[136:137], v18 offset:16384\n\t" "ds_read_b64_tr_b16 a[138:139], v18 offset:20480\n\t" \
   "ds_read_b64_tr_b16 a[140:141], v19 offset:16384\n\t" "ds_read_b64_tr_b16 a[142:143], v19 offset:20480\n\t" \
   "ds_read_b64_tr_b16 a[144:145], v18 offset:24576\n\t" "ds_read_b64_tr_b16 a[146:147], v18 offset:28672\n\t" \
   "ds_read_b64_tr_b16 a[148:149], v19 offset:24576\n\t" "ds_read_b64_tr_b16 a[150:151], v19 offset:28672\n\t" \
   "ds_read_b64_tr_b16 a[152:153], v18 offset:32768\n\t" "ds_read_b64_tr_b16 a[154:155], v18 offset:36864\n\t" \
   "ds_read_b64_tr_b16 a[156:157], v19 offset:32768\n\t" "ds_read_b64_tr_b16 a[158:159], v19 offset:36864\n\t" "s_waitcnt vmcnt(15)\n\t" \
   "s_waitcnt lgkmcnt(0)\n\t" "s_barrier\n\t" "ds_write_b32 v21, a160 offset:0\n\t" "ds_write_b32 v21, a161 offset:21120\n\t" \
   "ds_write_b32 v21, a162 offset:10560\n\t" "ds_write_b32 v21, a163 offset:31680\n\t" "s_mov_b32 s41, 61696\n\t" \
   "buffer_load_dwordx4 v[196:199], v17, %[rb], s41 offen\n\t" "v_mfma_f32_32x32x16_bf16 v[32:47], a[120:123], v[64:67], v[32:47]\n\t" \
   "s_waitcnt vmcnt(15)\n\t" "ds_write_b32 v21, a176 offset:64\n\t" "ds_write_b32 v21, a177 offset:21184\n\t" "ds_write_b32 v21, a178 offset:10624\n\t" \
   "ds_write_b32 v21, a179 offset:31744\n\t" "s_mov_b32 s41, 61952\n\t" "buffer_load_dwordx4 v[212:215], v17, %[rb], s41 offen\n\t" \
   "v_mfma_f32_32x32x16_bf16 v[32:47], a[124:127], v[64:67], v[32:47]\n\t" "s_waitcnt vmcnt(15)\n\t" "ds_write_b32 v21, a192 offset:128\n\t" \
   "ds_write_b32 v21, a193 offset:21248\n\t" "ds_write_b32 v21, a194 offset:10688\n\t" "ds_write_b32 v21, a195 offset:31808\n\t" \
   "s_mov_b32 s41, 62208\n\t" "buffer_load_dwordx4 v[228:231], v17, %[rb], s41 offen\n\t" \
   "v_mfma_f32_32x32x16_bf16 v[32:47], a[128:131], v[64:67], v[32:47]\n\t" "s_waitcnt vmcnt(15)\n\t" "ds_write_b32 v21, a208 offset:192\n\t" \
   "ds_write_b32 v21, a209 offset:21312\n\t" "ds_write_b32 v21, a210 offset:10752\n\t" "ds_write_b32 v21, a211 offset:31872\n\t" \
   "s_mov_b32 s41, 62464\n\t" "buffer_load_dwordx4 v[244:247], v17, %[rb], s41 offen\n\t" \
   "v_mfma_f32_32x32x16_bf16 v[32:47], a[132:135], v[64:67], v[32:47]\n\t" "s_waitcnt vmcnt(15)\n\t" "ds_write_b32 v21, a224 offset:256\n\t" \
   "ds_write_b32 v21, a225 offset:21376\n\t" "ds_write_b32 v21, a226 offset:10816\n\t" "ds_write_b32 v21, a227 offset:31936\n\t" \
   "s_mov_b32 s41, 81920\n\t" "buffer_load_dwordx4 a[168:171], v17, %[rb], s41 offen\n\t" \
   "v_mfma_f32_32x32x16_bf16 v[32:47], a[136:139], v[64:67], v[32:47]\n\t" "s_waitcnt vmcnt(15)\n\t" "ds_write_b32 v21, a240 offset:5248\n\t" \
   "ds_write_b32 v21, a241 offset:26368\n\t" "ds_write_b32 v21, a242 offset:15808\n\t" "ds_write_b32 v21, a243 offset:36928\n\t" \
   "s_mov_b32 s41, 82176\n\t" "buffer_load_dwordx4 a[184:187], v17, %[rb], s41 offen\n\t" \
   "v_mfma_f32_32x32x16_bf16 v[32:47], a[140:143], v[64:67], v[32:47]\n\t" "s_waitcnt vmcnt(15)\n\t" "ds_write_b32 v21, v192 offset:5312\n\t" \
   "ds_write_b32 v21, v193 offset:26432\n\t" "ds_write_b32 v21, v194 offset:15872\n\t" "ds_write_b32 v21, v195 offset:36992\n\t" \
   "s_mov_b32 s41, 82432\n\t" "buffer_load_dwordx4 a[200:203], v17, %[rb], s41 offen\n\t" \
   "v_mfma_f32_32x32x16_bf16 v[32:47], a[144:147], v[64:67], v[32:47]\n\t" "s_waitcnt vmcnt(15)\n\t" "ds_write_b32 v21, v208 offset:5376\n\t" \
   "ds_write_b32 v21, v209 offset:26496\n\t" "ds_write_b32 v21, v210 offset:15936\n\t" "ds_write_b32 v21, v211 offset:37056\n\t" \
   "s_mov_b32 s41, 82688\n\t" "buffer_load_dwordx4 a[216:219], v17, %[rb], s41 offen\n\t" \
   "v_mfma_f32_32x32x16_bf16 v[32:47], a[148:151], v[64:67], v[32:47]\n\t" "s_waitcnt vmcnt(15)\n\t" "ds_write_b32 v21, v224 offset:5440\n\t" \
   "ds_write_b32 v21, v225 offset:26560\n\t" "ds_write_b32 v21, v226 offset:16000\n\t" "ds_write_b32 v21, v227 offset:37120\n\t" \
   "s_mov_b32 s41, 82944\n\t" "buffer_load_dwordx4 a[232:235], v17, %[rb], s41 offen\n\t" \
   "v_mfma_f32_32x32x16_bf16 v[32:47], a[152:155], v[64:67], v[32:47]\n\t" "s_waitcnt vmcnt(15)\n\t" "ds_write_b32 v21, v240 offset:5504\n\t" \
   "ds_write_b32 v21, v241 offset:26624\n\t" "ds_write_b32 v21, v242 offset:16064\n\t" "ds_write_b32 v21, v243 offset:37184\n\t" \
   "s_mov_b32 s41, 102400\n\t" "buffer_load_dwordx4 a[248:251], v17, %[rb], s41 offen\n\t" \
   "v_mfma_f32_32x32x16_bf16 v[32:47], a[156:159], v[64:67], v[32:47]\n\t" "s_waitcnt lgkmcnt(0)\n\t" "s_barrier\n\t" LDSTAMP(6) \
   "ds_read_b64_tr_b16 a[160:161], v22 offset:0\n\t" "ds_read_b64_tr_b16 a[162:163], v22 offset:2624\n\t" \
   "ds_read_b64_tr_b16 a[176:177], v22 offset:5248\n\t" "ds_read_b64_tr_b16 a[178:179], v22 offset:7872\n\t" \
   "ds_read_b64_tr_b16 a[192:193], v22 offset:16\n\t" "ds_read_b64_tr_b16 a[194:195], v22 offset:2640\n\t" \
   "ds_read_b64_tr_b16 a[208:209], v22 offset:5264\n\t" "ds_read_b64_tr_b16 a[210:211], v22 offset:7888\n\t" \
   "ds_read_b64_tr_b16 a[224:225], v22 offset:32\n\t" "ds_read_b64_tr_b16 a[226:227], v22 offset:2656\n\t" \
   "ds_read_b64_tr_b16 a[240:241], v22 offset:5280\n\t" "ds_read_b64_tr_b16 a[242:243], v22 offset:7904\n\t" \
   "ds_read_b64_tr_b16 v[192:193], v22 offset:48\n\t" "ds_read_b64_tr_b16 v[194:195], v22 offset:2672\n\t" \
   "ds_read_b64_tr_b16 v[208:209], v22 offset:5296\n\t" "ds_read_b64_tr_b16 v[210:211], v22 offset:7920\n\t" \
   "ds_read_b64_tr_b16 v[224:225], v22 offset:64\n\t" "ds_read_b64_tr_b16 v[226:227], v22 offset:2688\n\t" \
   "ds_read_b64_tr_b16 v[240:241], v22 offset:5312\n\t" "ds_read_b64_tr_b16 v[242:243], v22 offset:7936\n\t" "s_waitcnt vmcnt(15)\n\t" \
   "s_waitcnt lgkmcnt(0)\n\t" "s_barrier\n\t" "ds_write_b32 v21, a164 offset:0\n\t" "ds_write_b32 v21, a165 offset:21120\n\t" \
   "ds_write_b32 v21, a166 offset:10560\n\t" "ds_write_b32 v21, a167 offset:31680\n\t" "s_mov_b32 s41, 102656\n\t" \
   "buffer_load_dwordx4 v[200:203], v17, %[rb], s41 offen\n\t" "s_waitcnt vmcnt(15)\n\t" "ds_write_b32 v21, a180 offset:64\n\t" \
   "ds_write_b32 v21, a181 offset:21184\n\t" "ds_write_b32 v21, a182 offset:10624\n\t" "ds_write_b32 v21, a183 offset:31744\n\t" \
   "s_mov_b32 s41, 102912\n\t" "buffer_load_dwordx4 v[216:219], v17, %[rb], s41 offen\n\t" "s_waitcnt vmcnt(15)\n\t" \
   "ds_write_b32 v21, a196 offset:128\n\t" "ds_write_b32 v21, a197 offset:21248\n\t" "ds_write_b32 v21, a198 offset:10688\n\t" \
   "ds_write_b32 v21, a199 offset:31808\n\t" "s_mov_b32 s41, 103168\n\t" "buffer_load_dwordx4 v[232:235], v17, %[rb], s41 offen\n\t" \
   "s_waitcnt vmcnt(15)\n\t" "ds_write_b32 v21, a212 offset:192\n\t" "ds_write_b32 v21, a213 offset:21312\n\t" \
   "ds_write_b32 v21, a214 offset:10752\n\t" "ds_write_b32 v21, a215 offset:31872\n\t" "s_mov_b32 s41, 103424\n\t" \
   "buffer_load_dwordx4 v[248:251], v17, %[rb], s41 offen\n\t" "s_waitcnt vmcnt(15)\n\t" "ds_write_b32 v21, a228 offset:256\n\t" \
   "ds_write_b32 v21, a229 offset:21376\n\t" "ds_write_b32 v21, a230 offset:10816\n\t" "ds_write_b32 v21, a231 offset:31936\n\t" \
   "s_mov_b32 s41, 122880\n\t" "buffer_load_dwordx4 a[172:175], v17, %[rb], s41 offen\n\t" "s_waitcnt vmcnt(15)\n\t" \
   "ds_write_b32 v21, a244 offset:5248\n\t" "ds_write_b32 v21, a245 offset:26368\n\t" "ds_write_b32 v21, a246 offset:15808\n\t" \
   "ds_write_b32 v21, a247 offset:36928\n\t" "s_mov_b32 s41, 123136\n\t" "buffer_load_dwordx4 a[188:191], v17, %[rb], s41 offen\n\t" \
   "s_waitcnt vmcnt(15)\n\t" "ds_write_b32 v21, v196 offset:5312\n\t" "ds_write_b32 v21, v197 offset:26432\n\t" \
   "ds_write_b32 v21, v198 offset:15872\n\t" "ds_write_b32 v21, v199 offset:36992\n\t" "s_mov_b32 s41, 123392\n\t" \
   "buffer_load_dwordx4 a[204:207], v17, %[rb], s41 offen\n\t" "s_waitcnt vmcnt(15)\n\t" "ds_write_b32 v21, v212 offset:5376\n\t" \
   "ds_write_b32 v21, v213 offset:26496\n\t" "ds_write_b32 v21, v214 offset:15936\n\t" "ds_write_b32 v21, v215 offset:37056\n\t" \
   "s_mov_b32 s41, 123648\n\t" "buffer_load_dwordx4 a[220:223], v17, %[rb], s41 offen\n\t" "s_waitcnt vmcnt(15)\n\t" \
   "ds_write_b32 v21, v228 offset:5440\n\t" "ds_write_b32 v21, v229 offset:26560\n\t" "ds_write_b32 v21, v230 offset:16000\n\t" \
   "ds_write_b32 v21, v231 offset:37120\n\t" "s_mov_b32 s41, 123904\n\t" "buffer_load_dwordx4 a[236:239], v17, %[rb], s41 offen\n\t" \
   "s_waitcnt vmcnt(15)\n\t" "ds_write_b32 v21, v244 offset:5504\n\t" "ds_write_b32 v21, v245 offset:26624\n\t" \
   "ds_write_b32 v21, v246 offset:16064\n\t" "ds_write_b32 v21, v247 offset:37184\n\t" "s_mov_b32 s41, 143360\n\t" \
   "buffer_load_dwordx4 a[252:255], v17, %[rb], s41 offen\n\t" "s_waitcnt lgkmcnt(0)\n\t" "s_barrier\n\t" LDSTAMP(7) \
   "ds_read_b64_tr_b16 a[164:165], v22 offset:0\n\t" "ds_read_b64_tr_b16 a[166:167], v22 offset:2624\n\t" \
   "ds_read_b64_tr_b16 a[180:181], v22 offset:5248\n\t" "ds_read_b64_tr_b16 a[182:183], v22 offset:7872\n\t" \
   "ds_read_b64_tr_b16 a[196:197], v22 offset:16\n\t" "ds_read_b64_tr_b16 a[198:199], v22 offset:2640\n\t" \
   "ds_read_b64_tr_b16 a[212:213], v22 offset:5264\n\t" "ds_read_b64_tr_b16 a[214:215], v22 offset:7888\n\t" \
   "ds_read_b64_tr_b16 a[228:229], v22 offset:32\n\t" "ds_read_b64_tr_b16 a[230:231], v22 offset:2656\n\t" \
   "ds_read_b64_tr_b16 a[244:245], v22 offset:5280\n\t" "ds_read_b64_tr_b16 a[246:247], v22 offset:7904\n\t" \
   "ds_read_b64_tr_b16 v[196:197], v22 offset:48\n\t" "ds_read_b64_tr_b16 v[198:199], v22 offset:2672\n\t" \
   "ds_read_b64_tr_b16 v[212:213], v22 offset:5296\n\t" "ds_read_b64_tr_b16 v[214:215], v22 offset:7920\n\t" \
   "ds_read_b64_tr_b16 v[228:229], v22 offset:64\n\t" "ds_read_b64_tr_b16 v[230:231], v22 offset:2688\n\t" \
   "ds_read_b64_tr_b16 v[244:245], v22 offset:5312\n\t" "ds_read_b64_tr_b16 v[246:247], v22 offset:7936\n\t" "s_waitcnt vmcnt(15)\n\t" \
   "s_waitcnt lgkmcnt(0)\n\t" "s_barrier\n\t" "ds_write_b32 v21, a168 offset:0\n\t" "ds_write_b32 v21, a169 offset:21120\n\t" \
   "ds_write_b32 v21, a170 offset:10560\n\t" "ds_write_b32 v21, a171 offset:31680\n\t" "s_mov_b32 s41, 143616\n\t" \
   "buffer_load_dwordx4 v[204:207], v17, %[rb], s41 offen\n\t" "s_waitcnt vmcnt(15)\n\t" "ds_write_b32 v21, a184 offset:64\n\t" \
   "ds_write_b32 v21, a185 offset:21184\n\t" "ds_write_b32 v21, a186 offset:10624\n\t" "ds_write_b32 v21, a187 offset:31744\n\t" \
   "s_mov_b32 s41, 143872\n\t" "buffer_load_dwordx4 v[220:223], v17, %[rb], s41 offen\n\t" "s_waitcnt vmcnt(15)\n\t" \
   "ds_write_b32 v21, a200 offset:128\n\t" "ds_write_b32 v21, a201 offset:21248\n\t" "ds_write_b32 v21, a202 offset:10688\n\t" \
   "ds_write_b32 v21, a203 offset:31808\n\t" "s_mov_b32 s41, 144128\n\t" "buffer_load_dwordx4 v[236:239], v17, %[rb], s41 offen\n\t" \
   "s_waitcnt vmcnt(15)\n\t" "ds_write_b32 v21, a216 offset:192\n\t" "ds_write_b32 v21, a217 offset:21312\n\t" \
   "ds_write_b32 v21, a218 offset:10752\n\t" "ds_write_b32 v21, a219 offset:31872\n\t" "s_mov_b32 s41, 144384\n\t" \
   "buffer_load_dwordx4 v[252:255], v17, %[rb], s41 offen\n\t" "s_waitcnt vmcnt(15)\n\t" "ds_write_b32 v21, a232 offset:256\n\t" \
   "ds_write_b32 v21, a233 offset:21376\n\t" "ds_write_b32 v21, a234 offset:10816\n\t" "ds_write_b32 v21, a235 offset:31936\n\t" \
   "s_waitcnt vmcnt(14)\n\t" "ds_write_b32 v21, a248 offset:5248\n\t" "ds_write_b32 v21, a249 offset:26368\n\t" \
   "ds_write_b32 v21, a250 offset:15808\n\t" "ds_write_b32 v21, a251 offset:36928\n\t" "s_waitcnt vmcnt(13)\n\t" \
   "ds_write_b32 v21, v200 offset:5312\n\t" "ds_write_b32 v21, v201 offset:26432\n\t" "ds_write_b32 v21, v202 offset:15872\n\t" \
   "ds_write_b32 v21, v203 offset:36992\n\t" "s_waitcnt vmcnt(12)\n\t" "ds_write_b32 v21, v216 offset:5376\n\t" \
   "ds_write_b32 v21, v217 offset:26496\n\t" "ds_write_b32 v21, v218 offset:15936\n\t" "ds_write_b32 v21, v219 offset:37056\n\t" \
   "s_waitcnt vmcnt(11)\n\t" "ds_write_b32 v21, v232 offset:5440\n\t" "ds_write_b32 v21, v233 offset:26560\n\t" \
   "ds_write_b32 v21, v234 offset:16000\n\t" "ds_write_b32 v21, v235 offset:37120\n\t" "s_waitcnt vmcnt(10)\n\t" \
   "ds_write_b32 v21, v248 offset:5504\n\t" "ds_write_b32 v21, v249 offset:26624\n\t" "ds_write_b32 v21, v250 offset:16064\n\t" \
   "ds_write_b32 v21, v251 offset:37184\n\t" "buffer_load_dwordx4 v[28:31], v112, %[rw], 0 offen sc1\n\t" "s_waitcnt lgkmcnt(0)\n\t" "s_barrier\n\t" \
   LDSTAMP(8) "ds_read_b64_tr_b16 a[168:169], v22 offset:0\n\t" "ds_read_b64_tr_b16 a[170:171], v22 offset:2624\n\t" \
   "ds_read_b64_tr_b16 a[184:185], v22 offset:5248\n\t" "ds_read_b64_tr_b16 a[186:187], v22 offset:7872\n\t" \
   "ds_read_b64_tr_b16 a[200:201], v22 offset:16\n\t" "ds_read_b64_tr_b16 a[202:203], v22 offset:2640\n\t" \
   "ds_read_b64_tr_b16 a[216:217], v22 offset:5264\n\t" "ds_read_b64_tr_b16 a[218:219], v22 offset:7888\n\t" \
   "ds_read_b64_tr_b16 a[232:233], v22 offset:32\n\t" "ds_read_b64_tr_b16 a[234:235], v22 offset:2656\n\t" \
   "ds_read_b64_tr_b16 a[248:249], v22 offset:5280\n\t" "ds_read_b64_tr_b16 a[250:251], v22 offset:7904\n\t" \
   "ds_read_b64_tr_b16 v[200:201], v22 offset:48\n\t" "ds_read_b64_tr_b16 v[202:203], v22 offset:2672\n\t" \
   "ds_read_b64_tr_b16 v[216:217], v22 offset:5296\n\t" "ds_read_b64_tr_b16 v[218:219], v22 offset:7920\n\t" \
   "ds_read_b64_tr_b16 v[232:233], v22 offset:64\n\t" "ds_read_b64_tr_b16 v[234:235], v22 offset:2688\n\t" \
   "ds_read_b64_tr_b16 v[248:249], v22 offset:5312\n\t" "ds_read_b64_tr_b16 v[250:251], v22 offset:7936\n\t" "s_waitcnt vmcnt(10)\n\t" \
   "s_waitcnt lgkmcnt(0)\n\t" "s_barrier\n\t" "ds_write_b32 v21, a172 offset:0\n\t" "ds_write_b32 v21, a173 offset:21120\n\t" \
   "ds_write_b32 v21, a174 offset:10560\n\t" "ds_write_b32 v21, a175 offset:31680\n\t" "s_waitcnt vmcnt(9)\n\t" "ds_write_b32 v21, a188 offset:64\n\t" \
   "ds_write_b32 v21, a189 offset:21184\n\t" "ds_write_b32 v21, a190 offset:10624\n\t" "ds_write_b32 v21, a191 offset:31744\n\t" \
   "s_waitcnt vmcnt(8)\n\t" "ds_write_b32 v21, a204 offset:128\n\t" "ds_write_b32 v21, a205 offset:21248\n\t" "ds_write_b32 v21, a206 offset:10688\n\t" \
   "ds_write_b32 v21, a207 offset:31808\n\t" "s_waitcnt vmcnt(7)\n\t" "ds_write_b32 v21, a220 offset:192\n\t" "ds_write_b32 v21, a221 offset:21312\n\t" \
   "ds_write_b32 v21, a222 offset:10752\n\t" "ds_write_b32 v21, a223 offset:31872\n\t" "s_waitcnt vmcnt(6)\n\t" "ds_write_b32 v21, a236 offset:256\n\t" \
   "ds_write_b32 v21, a237 offset:21376\n\t" "ds_write_b32 v21, a238 offset:10816\n\t" "ds_write_b32 v21, a239 offset:31936\n\t" \
   "s_waitcnt vmcnt(5)\n\t" "ds_write_b32 v21, a252 offset:5248\n\t" "ds_write_b32 v21, a253 offset:26368\n\t" \
   "ds_write_b32 v21, a254 offset:15808\n\t" "ds_write_b32 v21, a255 offset:36928\n\t" "s_waitcnt vmcnt(4)\n\t" \
   "ds_write_b32 v21, v204 offset:5312\n\t" "ds_write_b32 v21, v205 offset:26432\n\t" "ds_write_b32 v21, v206 offset:15872\n\t" \
   "ds_write_b32 v21, v207 offset:36992\n\t" "s_waitcnt vmcnt(3)\n\t" "ds_write_b32 v21, v220 offset:5376\n\t" \
   "ds_write_b32 v21, v221 offset:26496\n\t" "ds_write_b32 v21, v222 offset:15936\n\t" "ds_write_b32 v21, v223 offset:37056\n\t" \
   "s_waitcnt vmcnt(2)\n\t" "ds_write_b32 v21, v236 offset:5440\n\t" "ds_write_b32 v21, v237 offset:26560\n\t" \
   "ds_write_b32 v21, v238 offset:16000\n\t" "ds_write_b32 v21, v239 offset:37120\n\t" "s_waitcnt vmcnt(1)\n\t" \
   "ds_write_b32 v21, v252 offset:5504\n\t" "ds_write_b32 v21, v253 offset:26624\n\t" "ds_write_b32 v21, v254 offset:16064\n\t" \
   "ds_write_b32 v21, v255 offset:37184\n\t" "s_waitcnt lgkmcnt(0)\n\t" "s_barrier\n\t" LDSTAMP(9) "ds_read_b64_tr_b16 a[172:173], v22 offset:0\n\t" \
   "ds_read_b64_tr_b16 a[174:175], v22 offset:2624\n\t" "ds_read_b64_tr_b16 a[188:189], v22 offset:5248\n\t" \
   "ds_read_b64_tr_b16 a[190:191], v22 offset:7872\n\t" "ds_read_b64_tr_b16 a[204:205], v22 offset:16\n\t" \
   "ds_read_b64_tr_b16 a[206:207], v22 offset:2640\n\t" "ds_read_b64_tr_b16 a[220:221], v22 offset:5264\n\t" \
   "ds_read_b64_tr_b16 a[222:223], v22 offset:7888\n\t" "ds_read_b64_tr_b16 a[236:237], v22 offset:32\n\t" \
   "ds_read_b64_tr_b16 a[238:239], v22 offset:2656\n\t" "ds_read_b64_tr_b16 a[252:253], v22 offset:5280\n\t" \
   "ds_read_b64_tr_b16 a[254:255], v22 offset:7904\n\t" "ds_read_b64_tr_b16 v[204:205], v22 offset:48\n\t" \
   "ds_read_b64_tr_b16 v[206:207], v22 offset:2672\n\t" "ds_read_b64_tr_b16 v[220:221], v22 offset:5296\n\t" \
   "ds_read_b64_tr_b16 v[222:223], v22 offset:7920\n\t" "ds_read_b64_tr_b16 v[236:237], v22 offset:64\n\t" \
   "ds_read_b64_tr_b16 v[238:239], v22 offset:2688\n\t" "ds_read_b64_tr_b16 v[252:253], v22 offset:5312\n\t" \
   "ds_read_b64_tr_b16 v[254:255], v22 offset:7936\n\t" "s_waitcnt vmcnt(0) lgkmcnt(0)\n\t" "s_mov_b32 exec_lo, 1\n\t" "s_mov_b32 exec_hi, 1\n\t" \
   "s_nop 7\n\t" "s_nop 3\n\t" "ds_write_b128 v27, v[32:35] offset:0\n\t" "ds_write_b128 v27, v[36:39] offset:16\n\t" \
   "ds_write_b128 v27, v[40:43] offset:32\n\t" "ds_write_b128 v27, v[44:47] offset:48\n\t" "ds_write_b128 v27, v[48:51] offset:64\n\t" \
   "ds_write_b128 v27, v[52:55] offset:80\n\t" "ds_write_b128 v27, v[56:59] offset:96\n\t" "ds_write_b128 v27, v[60:63] offset:112\n\t" \
   "s_mov_b64 exec, -1\n\t" "s_waitcnt lgkmcnt(0)\n\t" "s_barrier"
#define LDV_CLOB "v24", "v25", "v26", "v32", "v33", "v34", "v35", "v36", "v37", "v38", "v39", "v40", "v41", "v42", "v43", "v44", "v45", "v46", "v47", "v48", "v49", "v50", "v51", "v52", "v53", "v54", "v55", "v56", "v57", "v58", "v59", "v60", "v61", "v62", "v63", "v64", "v65", "v66", "v67", "v68", "v69", "v70", "v71", "v72", "v73", "v74", "v75", "v76", "v77", "v78", "v79", "v80", "v81", "v82", "v83", "v84", "v85", "v86", "v87", "v88", "v89", "v90", "v91", "v92", "v93", "v94", "v95", "v96", "v97", "v98", "v99", "v100", "v101", "v102", "v103", "v104", "v105", "v106", "v107", "v108", "v109", "v110", "v111", "v120", "v121", "v122", "v123", "s40", "s41"
#define LDY_ASM \
   "buffer_load_dword v26, v113, %[rw], 0 offen sc1\n\t" "s_mov_b32 s41, 0\n\t" "buffer_load_dwordx4 a[0:3], v16, %[ra], s41 offen\n\t" \
   "s_mov_b32 s41, 131072\n\t" "buffer_load_dwordx4 a[64:67], v16, %[ra], s41 offen\n\t" "s_mov_b32 s41, 524288\n\t" \
   "buffer_load_dwordx4 a[8:11], v16, %[ra], s41 offen\n\t" "s_mov_b32 s41, 655360\n\t" "buffer_load_dwordx4 a[72:75], v16, %[ra], s41 offen\n\t" \
   "s_mov_b32 s41, 1048576\n\t" "buffer_load_dwordx4 a[16:19], v16, %[ra], s41 offen\n\t" "s_mov_b32 s41, 1179648\n\t" \
   "buffer_load_dwordx4 a[80:83], v16, %[ra], s41 offen\n\t" "s_mov_b32 s41, 1572864\n\t" "buffer_load_dwordx4 a[24:27], v16, %[ra], s41 offen\n\t" \
   "s_mov_b32 s41, 1703936\n\t" "buffer_load_dwordx4 a[88:91], v16, %[ra], s41 offen\n\t" "s_mov_b32 s41, 2097152\n\t" \
   "buffer_load_dwordx4 a[32:35], v16, %[ra], s41 offen\n\t" "s_mov_b32 s41, 2228224\n\t" "buffer_load_dwordx4 a[96:99], v16, %[ra], s41 offen\n\t" \
   "s_mov_b32 s41, 2621440\n\t" "buffer_load_dwordx4 a[40:43], v16, %[ra], s41 offen\n\t" "s_mov_b32 s41, 2752512\n\t" \
   "buffer_load_dwordx4 a[104:107], v16, %[ra], s41 offen\n\t" "s_mov_b32 s41, 3145728\n\t" "buffer_load_dwordx4 a[48:51], v16, %[ra], s41 offen\n\t" \
   "s_mov_b32 s41, 3276800\n\t" "buffer_load_dwordx4 a[112:115], v16, %[ra], s41 offen\n\t" "s_mov_b32 s41, 3670016\n\t" \
   "buffer_load_dwordx4 a[56:59], v16, %[ra], s41 offen\n\t" "s_mov_b32 s41, 3801088\n\t" "buffer_load_dwordx4 a[120:123], v16, %[ra], s41 offen\n\t" \
   "s_waitcnt vmcnt(16)\n\t" "v_add_u32 v26, 1, v26\n\t" "buffer_store_dword v26, v114, %[rw], 0 offen sc1\n\t" "ds_write_b32 v115, v26\n\t" LDSTAMP(0) \
   "s_waitcnt vmcnt(15)\n\t" LDSTAMP(1) "ds_write_b128 v20, a[0:3] offset:0\n\t" "s_mov_b32 s41, 4194304\n\t" \
   "buffer_load_dwordx4 v[192:195], v16, %[ra], s41 offen\n\t" "s_waitcnt vmcnt(15)\n\t" "ds_write_b128 v20, a[64:67] offset:4096\n\t" \
   "s_mov_b32 s41, 4325376\n\t" "buffer_load_dwordx4 v[208:211], v16, %[ra], s41 offen\n\t" "s_waitcnt vmcnt(15)\n\t" \
   "ds_write_b128 v20, a[8:11] offset:8192\n\t" "s_mov_b32 s41, 4718592\n\t" "buffer_load_dwordx4 v[200:203], v16, %[ra], s41 offen\n\t" \
   "s_waitcnt vmcnt(15)\n\t" "ds_write_b128 v20, a[72:75] offset:12288\n\t" "s_mov_b32 s41, 4849664\n\t" \
   "buffer_load_dwordx4 v[216:219], v16, %[ra], s41 offen\n\t" "s_waitcnt vmcnt(15)\n\t" "ds_write_b128 v20, a[16:19] offset:16384\n\t" \
   "s_mov_b32 s41, 262144\n\t" "buffer_load_dwordx4 a[4:7], v16, %[ra], s41 offen\n\t" "s_waitcnt vmcnt(15)\n\t" \
   "ds_write_b128 v20, a[80:83] offset:20480\n\t" "s_mov_b32 s41, 393216\n\t" "buffer_load_dwordx4 a[68:71], v16, %[ra], s41 offen\n\t" \
   "s_waitcnt vmcnt(15)\n\t" "ds_write_b128 v20, a[24:27] offset:24576\n\t" "s_mov_b32 s41, 786432\n\t" \
   "buffer_load_dwordx4 a[12:15], v16, %[ra], s41 offen\n\t" "s_waitcnt vmcnt(15)\n\t" "ds_write_b128 v20, a[88:91] offset:28672\n\t" \
   "s_mov_b32 s41, 917504\n\t" "buffer_load_dwordx4 a[76:79], v16, %[ra], s41 offen\n\t" "s_waitcnt vmcnt(15)\n\t" \
   "ds_write_b128 v20, a[32:35] offset:32768\n\t" "s_mov_b32 s41, 1310720\n\t" "buffer_load_dwordx4 a[20:23], v16, %[ra], s41 offen\n\t" \
   "s_waitcnt vmcnt(15)\n\t" "ds_write_b128 v20, a[96:99] offset:36864\n\t" "s_mov_b32 s41, 1441792\n\t" \
   "buffer_load_dwordx4 a[84:87], v16, %[ra], s41 offen\n\t" "s_waitcnt vmcnt(15)\n\t" "ds_write_b128 v20, a[40:43] offset:40960\n\t" \
   "s_mov_b32 s41, 1835008\n\t" "buffer_load_dwordx4 a[28:31], v16, %[ra], s41 offen\n\t" "s_waitcnt vmcnt(15)\n\t" \
   "ds_write_b128 v20, a[104:107] offset:45056\n\t" "s_mov_b32 s41, 1966080\n\t" "buffer_load_dwordx4 a[92:95], v16, %[ra], s41 offen\n\t" \
   "s_waitcnt vmcnt(15)\n\t" "ds_write_b128 v20, a[48:51] offset:49152\n\t" "s_mov_b32 s41, 2359296\n\t" \
   "buffer_load_dwordx4 a[36:39], v16, %[ra], s41 offen\n\t" "s_waitcnt vmcnt(15)\n\t" "ds_write_b128 v20, a[112:115] offset:53248\n\t" \
   "s_mov_b32 s41, 2490368\n\t" "buffer_load_dwordx4 a[100:103], v16, %[ra], s41 offen\n\t" "s_waitcnt vmcnt(15)\n\t" \
   "ds_write_b128 v20, a[56:59] offset:57344\n\t" "s_mov_b32 s41, 2883584\n\t" "buffer_load_dwordx4 a[44:47], v16, %[ra], s41 offen\n\t" \
   "s_waitcnt vmcnt(15)\n\t" "ds_write_b128 v20, a[120:123] offset:61440\n\t" "s_mov_b32 s41, 3014656\n\t" \
   "buffer_load_dwordx4 a[108:111], v16, %[ra], s41 offen\n\t" "s_waitcnt vmcnt(15)\n\t" "ds_write_b128 v23, v[192:195] offset:0\n\t" \
   "s_mov_b32 s41, 3407872\n\t" "buffer_load_dwordx4 a[52:55], v16, %[ra], s41 offen\n\t" "s_waitcnt vmcnt(15)\n\t" \
   "ds_write_b128 v23, v[208:211] offset:4096\n\t" "s_mov_b32 s41, 3538944\n\t" "buffer_load_dwordx4 a[116:119], v16, %[ra], s41 offen\n\t" \
   "s_waitcnt vmcnt(15)\n\t" "ds_write_b128 v23, v[200:203] offset:8192\n\t" "s_mov_b32 s41, 3932160\n\t" \
   "buffer_load_dwordx4 a[60:63], v16, %[ra], s41 offen\n\t" "s_waitcnt vmcnt(15)\n\t" "ds_write_b128 v23, v[216:219] offset:12288\n\t" \
   "s_mov_b32 s41, 4063232\n\t" "buffer_load_dwordx4 a[124:127], v16, %[ra], s41 offen\n\t" "s_waitcnt lgkmcnt(0)\n\t" "s_barrier\n\t" LDSTAMP(2) \
   "s_lshl_b32 s40, %[p0], 13\n\t" "v_add_u32 v24, s40, v18\n\t" "v_add_u32 v25, s40, v19\n\t" "s_add_u32 s40, s40, 8192\n\t" \
   "s_cmp_eq_u32 s40, 81920\n\t" "s_cselect_b32 s40, 0, s40\n\t" "ds_read_b64_tr_b16 a[0:1], v24 offset:0\n\t" \
   "ds_read_b64_tr_b16 a[2:3], v24 offset:4096\n\t" "ds_read_b64_tr_b16 a[64:65], v25 offset:0\n\t" "ds_read_b64_tr_b16 a[66:67], v25 offset:4096\n\t" \
   "v_add_u32 v24, s40, v18\n\t" "v_add_u32 v25, s40, v19\n\t" "s_add_u32 s40, s40, 8192\n\t" "s_cmp_eq_u32 s40, 81920\n\t" \
   "s_cselect_b32 s40, 0, s40\n\t" "ds_read_b64_tr_b16 a[8:9], v24 offset:0\n\t" "ds_read_b64_tr_b16 a[10:11], v24 offset:4096\n\t" \
   "ds_read_b64_tr_b16 a[72:73], v25 offset:0\n\t" "ds_read_b64_tr_b16 a[74:75], v25 offset:4096\n\t" "v_add_u32 v24, s40, v18\n\t" \
   "v_add_u32 v25, s40, v19\n\t" "s_add_u32 s40, s40, 8192\n\t" "s_cmp_eq_u32 s40, 81920\n\t" "s_cselect_b32 s40, 0, s40\n\t" \
   "ds_read_b64_tr_b16 a[16:17], v24 offset:0\n\t" "ds_read_b64_tr_b16 a[18:19], v24 offset:4096\n\t" "ds_read_b64_tr_b16 a[80:81], v25 offset:0\n\t" \
   "ds_read_b64_tr_b16 a[82:83], v25 offset:4096\n\t" "v_add_u32 v24, s40, v18\n\t" "v_add_u32 v25, s40, v19\n\t" "s_add_u32 s40, s40, 8192\n\t" \
   "s_cmp_eq_u32 s40, 81920\n\t" "s_cselect_b32 s40, 0, s40\n\t" "ds_read_b64_tr_b16 a[24:25], v24 offset:0\n\t" \
   "ds_read_b64_tr_b16 a[26:27], v24 offset:4096\n\t" "ds_read_b64_tr_b16 a[88:89], v25 offset:0\n\t" \
   "ds_read_b64_tr_b16 a[90:91], v25 offset:4096\n\t" "v_add_u32 v24, s40, v18\n\t" "v_add_u32 v25, s40, v19\n\t" "s_add_u32 s40, s40, 8192\n\t" \
   "s_cmp_eq_u32 s40, 81920\n\t" "s_cselect_b32 s40, 0, s40\n\t" "ds_read_b64_tr_b16 a[32:33], v24 offset:0\n\t" \
   "ds_read_b64_tr_b16 a[34:35], v24 offset:4096\n\t" "ds_read_b64_tr_b16 a[96:97], v25 offset:0\n\t" \
   "ds_read_b64_tr_b16 a[98:99], v25 offset:4096\n\t" "v_add_u32 v24, s40, v18\n\t" "v_add_u32 v25, s40, v19\n\t" "s_add_u32 s40, s40, 8192\n\t" \
   "s_cmp_eq_u32 s40, 81920\n\t" "s_cselect_b32 s40, 0, s40\n\t" "ds_read_b64_tr_b16 a[40:41], v24 offset:0\n\t" \
   "ds_read_b64_tr_b16 a[42:43], v24 offset:4096\n\t" "ds_read_b64_tr_b16 a[104:105], v25 offset:0\n\t" \
   "ds_read_b64_tr_b16 a[106:107], v25 offset:4096\n\t" "v_add_u32 v24, s40, v18\n\t" "v_add_u32 v25, s40, v19\n\t" "s_add_u32 s40, s40, 8192\n\t" \
   "s_cmp_eq_u32 s40, 81920\n\t" "s_cselect_b32 s40, 0, s40\n\t" "ds_read_b64_tr_b16 a[48:49], v24 offset:0\n\t" \
   "ds_read_b64_tr_b16 a[50:51], v24 offset:4096\n\t" "ds_read_b64_tr_b16 a[112:113], v25 offset:0\n\t" \
   "ds_read_b64_tr_b16 a[114:115], v25 offset:4096\n\t" "v_add_u32 v24, s40, v18\n\t" "v_add_u32 v25, s40, v19\n\t" "s_add_u32 s40, s40, 8192\n\t" \
   "s_cmp_eq_u32 s40, 81920\n\t" "s_cselect_b32 s40, 0, s40\n\t" "ds_read_b64_tr_b16 a[56:57], v24 offset:0\n\t" \
   "ds_read_b64_tr_b16 a[58:59], v24 offset:4096\n\t" "ds_read_b64_tr_b16 a[120:121], v25 offset:0\n\t" \
   "ds_read_b64_tr_b16 a[122:123], v25 offset:4096\n\t" "v_add_u32 v24, s40, v18\n\t" "v_add_u32 v25, s40, v19\n\t" "s_add_u32 s40, s40, 8192\n\t" \
   "s_cmp_eq_u32 s40, 81920\n\t" "s_cselect_b32 s40, 0, s40\n\t" "ds_read_b64_tr_b16 v[192:193], v24 offset:0\n\t" \
   "ds_read_b64_tr_b16 v[194:195], v24 offset:4096\n\t" "ds_read_b64_tr_b16 v[208:209], v25 offset:0\n\t" \
   "ds_read_b64_tr_b16 v[210:211], v25 offset:4096\n\t" "v_add_u32 v24, s40, v18\n\t" "v_add_u32 v25, s40, v19\n\t" "s_add_u32 s40, s40, 8192\n\t" \
   "s_cmp_eq_u32 s40, 81920\n\t" "s_cselect_b32 s40, 0, s40\n\t" "ds_read_b64_tr_b16 v[200:201], v24 offset:0\n\t" \
   "ds_read_b64_tr_b16 v[202:203], v24 offset:4096\n\t" "ds_read_b64_tr_b16 v[216:217], v25 offset:0\n\t" \
   "ds_read_b64_tr_b16 v[218:219], v25 offset:4096\n\t" "s_waitcnt vmcnt(15)\n\t" "s_waitcnt lgkmcnt(0)\n\t" "s_barrier\n\t" \
   "ds_write_b128 v20, a[4:7] offset:0\n\t" "s_mov_b32 s41, 4456448\n\t" "buffer_load_dwordx4 v[196:199], v16, %[ra], s41 offen\n\t" \
   "s_waitcnt vmcnt(15)\n\t" "ds_write_b128 v20, a[68:71] offset:4096\n\t" "s_mov_b32 s41, 4587520\n\t" \
   "buffer_load_dwordx4 v[212:215], v16, %[ra], s41 offen\n\t" "s_waitcnt vmcnt(15)\n\t" "ds_write_b128 v20, a[12:15] offset:8192\n\t" \
   "s_mov_b32 s41, 4980736\n\t" "buffer_load_dwordx4 v[204:207], v16, %[ra], s41 offen\n\t" "s_waitcnt vmcnt(15)\n\t" \
   "ds_write_b128 v20, a[76:79] offset:12288\n\t" "s_mov_b32 s41, 5111808\n\t" "buffer_load_dwordx4 v[220:223], v16, %[ra], s41 offen\n\t" \
   "s_waitcnt vmcnt(15)\n\t" "ds_write_b128 v20, a[20:23] offset:16384\n\t" "s_mov_b32 s41, 0\n\t" \
   "buffer_load_dwordx4 a[128:131], v17, %[rb], s41 offen\n\t" "s_waitcnt vmcnt(15)\n\t" "ds_write_b128 v20, a[84:87] offset:20480\n\t" \
   "s_mov_b32 s41, 256\n\t" "buffer_load_dwordx4 a[132:135], v17, %[rb], s41 offen\n\t" "s_waitcnt vmcnt(15)\n\t" \
   "ds_write_b128 v20, a[28:31] offset:24576\n\t" "s_mov_b32 s41, 512\n\t" "buffer_load_dwordx4 a[160:163], v17, %[rb], s41 offen\n\t" \
   "s_waitcnt vmcnt(15)\n\t" "ds_write_b128 v20, a[92:95] offset:28672\n\t" "s_mov_b32 s41, 768\n\t" \
   "buffer_load_dwordx4 a[164:167], v17, %[rb], s41 offen\n\t" "s_waitcnt vmcnt(15)\n\t" "ds_write_b128 v20, a[36:39] offset:32768\n\t" \
   "s_mov_b32 s41, 1024\n\t" "buffer_load_dwordx4 a[192:195], v17, %[rb], s41 offen\n\t" "s_waitcnt vmcnt(15)\n\t" \
   "ds_write_b128 v20, a[100:103] offset:36864\n\t" "s_mov_b32 s41, 20480\n\t" "buffer_load_dwordx4 a[196:199], v17, %[rb], s41 offen\n\t" \
   "s_waitcnt vmcnt(15)\n\t" "ds_write_b128 v20, a[44:47] offset:40960\n\t" "s_mov_b32 s41, 20736\n\t" \
   "buffer_load_dwordx4 a[224:227], v17, %[rb], s41 offen\n\t" "s_waitcnt vmcnt(15)\n\t" "ds_write_b128 v20, a[108:111] offset:45056\n\t" \
   "s_mov_b32 s41, 20992\n\t" "buffer_load_dwordx4 a[228:231], v17, %[rb], s41 offen\n\t" "s_waitcnt vmcnt(15)\n\t" \
   "ds_write_b128 v20, a[52:55] offset:49152\n\t" "s_mov_b32 s41, 21248\n\t" "buffer_load_dwordx4 v[224:227], v17, %[rb], s41 offen\n\t" \
   "s_waitcnt vmcnt(15)\n\t" "ds_write_b128 v20, a[116:119] offset:53248\n\t" "s_mov_b32 s41, 21504\n\t" \
   "buffer_load_dwordx4 v[228:231], v17, %[rb], s41 offen\n\t" "s_waitcnt vmcnt(15)\n\t" "ds_write_b128 v20, a[60:63] offset:57344\n\t" \
   "s_mov_b32 s41, 40960\n\t" "buffer_load_dwordx4 a[136:139], v17, %[rb], s41 offen\n\t" "s_waitcnt vmcnt(15)\n\t" \
   "ds_write_b128 v20, a[124:127] offset:61440\n\t" "s_mov_b32 s41, 41216\n\t" "buffer_load_dwordx4 a[140:143], v17, %[rb], s41 offen\n\t" \
   "s_waitcnt vmcnt(15)\n\t" "ds_write_b128 v23, v[196:199] offset:0\n\t" "s_mov_b32 s41, 41472\n\t" \
   "buffer_load_dwordx4 a[168:171], v17, %[rb], s41 offen\n\t" "s_waitcnt vmcnt(15)\n\t" "ds_write_b128 v23, v[212:215] offset:4096\n\t" \
   "s_mov_b32 s41, 41728\n\t" "buffer_load_dwordx4 a[172:175], v17, %[rb], s41 offen\n\t" "s_waitcnt vmcnt(15)\n\t" \
   "ds_write_b128 v23, v[204:207] offset:8192\n\t" "s_mov_b32 s41, 41984\n\t" "buffer_load_dwordx4 a[200:203], v17, %[rb], s41 offen\n\t" \
   "s_waitcnt vmcnt(15)\n\t" "ds_write_b128 v23, v[220:223] offset:12288\n\t" "s_mov_b32 s41, 61440\n\t" \
   "buffer_load_dwordx4 a[204:207], v17, %[rb], s41 offen\n\t" "s_waitcnt lgkmcnt(0)\n\t" "s_barrier\n\t" LDSTAMP(3) "s_lshl_b32 s40, %[p0], 13\n\t" \
   "v_add_u32 v24, s40, v18\n\t" "v_add_u32 v25, s40, v19\n\t" "s_add_u32 s40, s40, 8192\n\t" "s_cmp_eq_u32 s40, 81920\n\t" \
   "s_cselect_b32 s40, 0, s40\n\t" "ds_read_b64_tr_b16 a[4:5], v24 offset:0\n\t" "ds_read_b64_tr_b16 a[6:7], v24 offset:4096\n\t" \
   "ds_read_b64_tr_b16 a[68:69], v25 offset:0\n\t" "ds_read_b64_tr_b16 a[70:71], v25 offset:4096\n\t" "v_add_u32 v24, s40, v18\n\t" \
   "v_add_u32 v25, s40, v19\n\t" "s_add_u32 s40, s40, 8192\n\t" "s_cmp_eq_u32 s40, 81920\n\t" "s_cselect_b32 s40, 0, s40\n\t" \
   "ds_read_b64_tr_b16 a[12:13], v24 offset:0\n\t" "ds_read_b64_tr_b16 a[14:15], v24 offset:4096\n\t" "ds_read_b64_tr_b16 a[76:77], v25 offset:0\n\t" \
   "ds_read_b64_tr_b16 a[78:79], v25 offset:4096\n\t" "v_add_u32 v24, s40, v18\n\t" "v_add_u32 v25, s40, v19\n\t" "s_add_u32 s40, s40, 8192\n\t" \
   "s_cmp_eq_u32 s40, 81920\n\t" "s_cselect_b32 s40, 0, s40\n\t" "ds_read_b64_tr_b16 a[20:21], v24 offset:0\n\t" \
   "ds_read_b64_tr_b16 a[22:23], v24 offset:4096\n\t" "ds_read_b64_tr_b16 a[84:85], v25 offset:0\n\t" \
   "ds_read_b64_tr_b16 a[86:87], v25 offset:4096\n\t" "v_add_u32 v24, s40, v18\n\t" "v_add_u32 v25, s40, v19\n\t" "s_add_u32 s40, s40, 8192\n\t" \
   "s_cmp_eq_u32 s40, 81920\n\t" "s_cselect_b32 s40, 0, s40\n\t" "ds_read_b64_tr_b16 a[28:29], v24 offset:0\n\t" \
   "ds_read_b64_tr_b16 a[30:31], v24 offset:4096\n\t" "ds_read_b64_tr_b16 a[92:93], v25 offset:0\n\t" \
   "ds_read_b64_tr_b16 a[94:95], v25 offset:4096\n\t" "v_add_u32 v24, s40, v18\n\t" "v_add_u32 v25, s40, v19\n\t" "s_add_u32 s40, s40, 8192\n\t" \
   "s_cmp_eq_u32 s40, 81920\n\t" "s_cselect_b32 s40, 0, s40\n\t" "ds_read_b64_tr_b16 a[36:37], v24 offset:0\n\t" \
   "ds_read_b64_tr_b16 a[38:39], v24 offset:4096\n\t" "ds_read_b64_tr_b16 a[100:101], v25 offset:0\n\t" \
   "ds_read_b64_tr_b16 a[102:103], v25 offset:4096\n\t" "v_add_u32 v24, s40, v18\n\t" "v_add_u32 v25, s40, v19\n\t" "s_add_u32 s40, s40, 8192\n\t" \
   "s_cmp_eq_u32 s40, 81920\n\t" "s_cselect_b32 s40, 0, s40\n\t" "ds_read_b64_tr_b16 a[44:45], v24 offset:0\n\t" \
   "ds_read_b64_tr_b16 a[46:47], v24 offset:4096\n\t" "ds_read_b64_tr_b16 a[108:109], v25 offset:0\n\t" \
   "ds_read_b64_tr_b16 a[110:111], v25 offset:4096\n\t" "v_add_u32 v24, s40, v18\n\t" "v_add_u32 v25, s40, v19\n\t" "s_add_u32 s40, s40, 8192\n\t" \
   "s_cmp_eq_u32 s40, 81920\n\t" "s_cselect_b32 s40, 0, s40\n\t" "ds_read_b64_tr_b16 a[52:53], v24 offset:0\n\t" \
   "ds_read_b64_tr_b16 a[54:55], v24 offset:4096\n\t" "ds_read_b64_tr_b16 a[116:117], v25 offset:0\n\t" \
   "ds_read_b64_tr_b16 a[118:119], v25 offset:4096\n\t" "v_add_u32 v24, s40, v18\n\t" "v_add_u32 v25, s40, v19\n\t" "s_add_u32 s40, s40, 8192\n\t" \
   "s_cmp_eq_u32 s40, 81920\n\t" "s_cselect_b32 s40, 0, s40\n\t" "ds_read_b64_tr_b16 a[60:61], v24 offset:0\n\t" \
   "ds_read_b64_tr_b16 a[62:63], v24 offset:4096\n\t" "ds_read_b64_tr_b16 a[124:125], v25 offset:0\n\t" \
   "ds_read_b64_tr_b16 a[126:127], v25 offset:4096\n\t" "v_add_u32 v24, s40, v18\n\t" "v_add_u32 v25, s40, v19\n\t" "s_add_u32 s40, s40, 8192\n\t" \
   "s_cmp_eq_u32 s40, 81920\n\t" "s_cselect_b32 s40, 0, s40\n\t" "ds_read_b64_tr_b16 v[196:197], v24 offset:0\n\t" \
   "ds_read_b64_tr_b16 v[198:199], v24 offset:4096\n\t" "ds_read_b64_tr_b16 v[212:213], v25 offset:0\n\t" \
   "ds_read_b64_tr_b16 v[214:215], v25 offset:4096\n\t" "v_add_u32 v24, s40, v18\n\t" "v_add_u32 v25, s40, v19\n\t" "s_add_u32 s40, s40, 8192\n\t" \
   "s_cmp_eq_u32 s40, 81920\n\t" "s_cselect_b32 s40, 0, s40\n\t" "ds_read_b64_tr_b16 v[204:205], v24 offset:0\n\t" \
   "ds_read_b64_tr_b16 v[206:207], v24 offset:4096\n\t" "ds_read_b64_tr_b16 v[220:221], v25 offset:0\n\t" \
   "ds_read_b64_tr_b16 v[222:223], v25 offset:4096\n\t" "s_waitcnt vmcnt(15)\n\t" "s_waitcnt lgkmcnt(0)\n\t" "s_barrier\n\t" \
   "ds_write_b32 v21, a128 offset:0\n\t" "ds_write_b32 v21, a129 offset:21120\n\t" "ds_write_b32 v21, a130 offset:10560\n\t" \
   "ds_write_b32 v21, a131 offset:31680\n\t" "s_mov_b32 s41, 61696\n\t" "buffer_load_dwordx4 a[232:235], v17, %[rb], s41 offen\n\t" \
   "s_waitcnt vmcnt(15)\n\t" "ds_write_b32 v21, a132 offset:64\n\t" "ds_write_b32 v21, a133 offset:21184\n\t" "ds_write_b32 v21, a134 offset:10624\n\t" \
   "ds_write_b32 v21, a135 offset:31744\n\t" "s_mov_b32 s41, 61952\n\t" "buffer_load_dwordx4 a[236:239], v17, %[rb], s41 offen\n\t" \
   "s_waitcnt vmcnt(15)\n\t" "ds_write_b32 v21, a160 offset:128\n\t" "ds_write_b32 v21, a161 offset:21248\n\t" \
   "ds_write_b32 v21, a162 offset:10688\n\t" "ds_write_b32 v21, a163 offset:31808\n\t" "s_mov_b32 s41, 62208\n\t" \
   "buffer_load_dwordx4 v[232:235], v17, %[rb], s41 offen\n\t" "s_waitcnt vmcnt(15)\n\t" "ds_write_b32 v21, a164 offset:192\n\t" \
   "ds_write_b32 v21, a165 offset:21312\n\t" "ds_write_b32 v21, a166 offset:10752\n\t" "ds_write_b32 v21, a167 offset:31872\n\t" \
   "s_mov_b32 s41, 62464\n\t" "buffer_load_dwordx4 v[236:239], v17, %[rb], s41 offen\n\t" "s_waitcnt vmcnt(15)\n\t" \
   "ds_write_b32 v21, a192 offset:256\n\t" "ds_write_b32 v21, a193 offset:21376\n\t" "ds_write_b32 v21, a194 offset:10816\n\t" \
   "ds_write_b32 v21, a195 offset:31936\n\t" "s_mov_b32 s41, 81920\n\t" "buffer_load_dwordx4 a[144:147], v17, %[rb], s41 offen\n\t" \
   "s_waitcnt vmcnt(15)\n\t" "ds_write_b32 v21, a196 offset:5248\n\t" "ds_write_b32 v21, a197 offset:26368\n\t" \
   "ds_write_b32 v21, a198 offset:15808\n\t" "ds_write_b32 v21, a199 offset:36928\n\t" "s_mov_b32 s41, 82176\n\t" \
   "buffer_load_dwordx4 a[148:151], v17, %[rb], s41 offen\n\t" "s_waitcnt vmcnt(15)\n\t" "ds_write_b32 v21, a224 offset:5312\n\t" \
   "ds_write_b32 v21, a225 offset:26432\n\t" "ds_write_b32 v21, a226 offset:15872\n\t" "ds_write_b32 v21, a227 offset:36992\n\t" \
   "s_mov_b32 s41, 82432\n\t" "buffer_load_dwordx4 a[176:179], v17, %[rb], s41 offen\n\t" "s_waitcnt vmcnt(15)\n\t" \
   "ds_write_b32 v21, a228 offset:5376\n\t" "ds_write_b32 v21, a229 offset:26496\n\t" "ds_write_b32 v21, a230 offset:15936\n\t" \
   "ds_write_b32 v21, a231 offset:37056\n\t" "s_mov_b32 s41, 82688\n\t" "buffer_load_dwordx4 a[180:183], v17, %[rb], s41 offen\n\t" \
   "s_waitcnt vmcnt(15)\n\t" "ds_write_b32 v21, v224 offset:5440\n\t" "ds_write_b32 v21, v225 offset:26560\n\t" \
   "ds_write_b32 v21, v226 offset:16000\n\t" "ds_write_b32 v21, v227 offset:37120\n\t" "s_mov_b32 s41, 82944\n\t" \
   "buffer_load_dwordx4 a[208:211], v17, %[rb], s41 offen\n\t" "s_waitcnt vmcnt(15)\n\t" "ds_write_b32 v21, v228 offset:5504\n\t" \
   "ds_write_b32 v21, v229 offset:26624\n\t" "ds_write_b32 v21, v230 offset:16064\n\t" "ds_write_b32 v21, v231 offset:37184\n\t" \
   "s_mov_b32 s41, 102400\n\t" "buffer_load_dwordx4 a[212:215], v17, %[rb], s41 offen\n\t" "s_waitcnt lgkmcnt(0)\n\t" "s_barrier\n\t" LDSTAMP(4) \
   "ds_read_b64_tr_b16 a[128:129], v22 offset:0\n\t" "ds_read_b64_tr_b16 a[130:131], v22 offset:656\n\t" \
   "ds_read_b64_tr_b16 a[132:133], v22 offset:2624\n\t" "ds_read_b64_tr_b16 a[134:135], v22 offset:3280\n\t" \
   "ds_read_b64_tr_b16 a[160:161], v22 offset:16\n\t" "ds_read_b64_tr_b16 a[162:163], v22 offset:672\n\t" \
   "ds_read_b64_tr_b16 a[164:165], v22 offset:2640\n\t" "ds_read_b64_tr_b16 a[166:167], v22 offset:3296\n\t" \
   "ds_read_b64_tr_b16 a[192:193], v22 offset:32\n\t" "ds_read_b64_tr_b16 a[194:195], v22 offset:688\n\t" \
   "ds_read_b64_tr_b16 a[196:197], v22 offset:2656\n\t" "ds_read_b64_tr_b16 a[198:199], v22 offset:3312\n\t" \
   "ds_read_b64_tr_b16 a[224:225], v22 offset:48\n\t" "ds_read_b64_tr_b16 a[226:227], v22 offset:704\n\t" \
   "ds_read_b64_tr_b16 a[228:229], v22 offset:2672\n\t" "ds_read_b64_tr_b16 a[230:231], v22 offset:3328\n\t" \
   "ds_read_b64_tr_b16 v[224:225], v22 offset:64\n\t" "ds_read_b64_tr_b16 v[226:227], v22 offset:720\n\t" \
   "ds_read_b64_tr_b16 v[228:229], v22 offset:2688\n\t" "ds_read_b64_tr_b16 v[230:231], v22 offset:3344\n\t" "s_waitcnt vmcnt(15)\n\t" \
   "s_waitcnt lgkmcnt(0)\n\t" "s_barrier\n\t" "ds_write_b32 v21, a136 offset:0\n\t" "ds_write_b32 v21, a137 offset:21120\n\t" \
   "ds_write_b32 v21, a138 offset:10560\n\t" "ds_write_b32 v21, a139 offset:31680\n\t" "s_mov_b32 s41, 102656\n\t" \
   "buffer_load_dwordx4 a[240:243], v17, %[rb], s41 offen\n\t" "s_waitcnt vmcnt(15)\n\t" "ds_write_b32 v21, a140 offset:64\n\t" \
   "ds_write_b32 v21, a141 offset:21184\n\t" "ds_write_b32 v21, a142 offset:10624\n\t" "ds_write_b32 v21, a143 offset:31744\n\t" \
   "s_mov_b32 s41, 102912\n\t" "buffer_load_dwordx4 a[244:247], v17, %[rb], s41 offen\n\t" "s_waitcnt vmcnt(15)\n\t" \
   "ds_write_b32 v21, a168 offset:128\n\t" "ds_write_b32 v21, a169 offset:21248\n\t" "ds_write_b32 v21, a170 offset:10688\n\t" \
   "ds_write_b32 v21, a171 offset:31808\n\t" "s_mov_b32 s41, 103168\n\t" "buffer_load_dwordx4 v[240:243], v17, %[rb], s41 offen\n\t" \
   "s_waitcnt vmcnt(15)\n\t" "ds_write_b32 v21, a172 offset:192\n\t" "ds_write_b32 v21, a173 offset:21312\n\t" \
   "ds_write_b32 v21, a174 offset:10752\n\t" "ds_write_b32 v21, a175 offset:31872\n\t" "s_mov_b32 s41, 103424\n\t" \
   "buffer_load_dwordx4 v[244:247], v17, %[rb], s41 offen\n\t" "s_waitcnt vmcnt(15)\n\t" "ds_write_b32 v21, a200 offset:256\n\t" \
   "ds_write_b32 v21, a201 offset:21376\n\t" "ds_write_b32 v21, a202 offset:10816\n\t" "ds_write_b32 v21, a203 offset:31936\n\t" \
   "s_mov_b32 s41, 122880\n\t" "buffer_load_dwordx4 a[152:155], v17, %[rb], s41 offen\n\t" "s_waitcnt vmcnt(15)\n\t" \
   "ds_write_b32 v21, a204 offset:5248\n\t" "ds_write_b32 v21, a205 offset:26368\n\t" "ds_write_b32 v21, a206 offset:15808\n\t" \
   "ds_write_b32 v21, a207 offset:36928\n\t" "s_mov_b32 s41, 123136\n\t" "buffer_load_dwordx4 a[156:159], v17, %[rb], s41 offen\n\t" \
   "s_waitcnt vmcnt(15)\n\t" "ds_write_b32 v21, a232 offset:5312\n\t" "ds_write_b32 v21, a233 offset:26432\n\t" \
   "ds_write_b32 v21, a234 offset:15872\n\t" "ds_write_b32 v21, a235 offset:36992\n\t" "s_mov_b32 s41, 123392\n\t" \
   "buffer_load_dwordx4 a[184:187], v17, %[rb], s41 offen\n\t" "s_waitcnt vmcnt(15)\n\t" "ds_write_b32 v21, a236 offset:5376\n\t" \
   "ds_write_b32 v21, a237 offset:26496\n\t" "ds_write_b32 v21, a238 offset:15936\n\t" "ds_write_b32 v21, a239 offset:37056\n\t" \
   "s_mov_b32 s41, 123648\n\t" "buffer_load_dwordx4 a[188:191], v17, %[rb], s41 offen\n\t" "s_waitcnt vmcnt(15)\n\t" \
   "ds_write_b32 v21, v232 offset:5440\n\t" "ds_write_b32 v21, v233 offset:26560\n\t" "ds_write_b32 v21, v234 offset:16000\n\t" \
   "ds_write_b32 v21, v235 offset:37120\n\t" "s_mov_b32 s41, 123904\n\t" "buffer_load_dwordx4 a[216:219], v17, %[rb], s41 offen\n\t" \
   "s_waitcnt vmcnt(15)\n\t" "ds_write_b32 v21, v236 offset:5504\n\t" "ds_write_b32 v21, v237 offset:26624\n\t" \
   "ds_write_b32 v21, v238 offset:16064\n\t" "ds_write_b32 v21, v239 offset:37184\n\t" "s_mov_b32 s41, 143360\n\t" \
   "buffer_load_dwordx4 a[220:223], v17, %[rb], s41 offen\n\t" "s_waitcnt lgkmcnt(0)\n\t" "s_barrier\n\t" LDSTAMP(5) \
   "ds_read_b64_tr_b16 a[136:137], v22 offset:0\n\t" "ds_read_b64_tr_b16 a[138:139], v22 offset:656\n\t" \
   "ds_read_b64_tr_b16 a[140:141], v22 offset:2624\n\t" "ds_read_b64_tr_b16 a[142:143], v22 offset:3280\n\t" \
   "ds_read_b64_tr_b16 a[168:169], v22 offset:16\n\t" "ds_read_b64_tr_b16 a[170:171], v22 offset:672\n\t" \
   "ds_read_b64_tr_b16 a[172:173], v22 offset:2640\n\t" "ds_read_b64_tr_b16 a[174:175], v22 offset:3296\n\t" \
   "ds_read_b64_tr_b16 a[200:201], v22 offset:32\n\t" "ds_read_b64_tr_b16 a[202:203], v22 offset:688\n\t" \
   "ds_read_b64_tr_b16 a[204:205], v22 offset:2656\n\t" "ds_read_b64_tr_b16 a[206:207], v22 offset:3312\n\t" \
   "ds_read_b64_tr_b16 a[232:233], v22 offset:48\n\t" "ds_read_b64_tr_b16 a[234:235], v22 offset:704\n\t" \
   "ds_read_b64_tr_b16 a[236:237], v22 offset:2672\n\t" "ds_read_b64_tr_b16 a[238:239], v22 offset:3328\n\t" \
   "ds_read_b64_tr_b16 v[232:233], v22 offset:64\n\t" "ds_read_b64_tr_b16 v[234:235], v22 offset:720\n\t" \
   "ds_read_b64_tr_b16 v[236:237], v22 offset:2688\n\t" "ds_read_b64_tr_b16 v[238:239], v22 offset:3344\n\t" "s_waitcnt vmcnt(15)\n\t" \
   "s_waitcnt lgkmcnt(0)\n\t" "s_barrier\n\t" "ds_write_b32 v21, a144 offset:0\n\t" "ds_write_b32 v21, a145 offset:21120\n\t" \
   "ds_write_b32 v21, a146 offset:10560\n\t" "ds_write_b32 v21, a147 offset:31680\n\t" "s_mov_b32 s41, 143616\n\t" \
   "buffer_load_dwordx4 a[248:251], v17, %[rb], s41 offen\n\t" "s_waitcnt vmcnt(15)\n\t" "ds_write_b32 v21, a148 offset:64\n\t" \
   "ds_write_b32 v21, a149 offset:21184\n\t" "ds_write_b32 v21, a150 offset:10624\n\t" "ds_write_b32 v21, a151 offset:31744\n\t" \
   "s_mov_b32 s41, 143872\n\t" "buffer_load_dwordx4 a[252:255], v17, %[rb], s41 offen\n\t" "s_waitcnt vmcnt(15)\n\t" \
   "ds_write_b32 v21, a176 offset:128\n\t" "ds_write_b32 v21, a177 offset:21248\n\t" "ds_write_b32 v21, a178 offset:10688\n\t" \
   "ds_write_b32 v21, a179 offset:31808\n\t" "s_mov_b32 s41, 144128\n\t" "buffer_load_dwordx4 v[248:251], v17, %[rb], s41 offen\n\t" \
   "s_waitcnt vmcnt(15)\n\t" "ds_write_b32 v21, a180 offset:192\n\t" "ds_write_b32 v21, a181 offset:21312\n\t" \
   "ds_write_b32 v21, a182 offset:10752\n\t" "ds_write_b32 v21, a183 offset:31872\n\t" "s_mov_b32 s41, 144384\n\t" \
   "buffer_load_dwordx4 v[252:255], v17, %[rb], s41 offen\n\t" "s_waitcnt vmcnt(15)\n\t" "ds_write_b32 v21, a208 offset:256\n\t" \
   "ds_write_b32 v21, a209 offset:21376\n\t" "ds_write_b32 v21, a210 offset:10816\n\t" "ds_write_b32 v21, a211 offset:31936\n\t" \
   "s_waitcnt vmcnt(14)\n\t" "ds_write_b32 v21, a212 offset:5248\n\t" "ds_write_b32 v21, a213 offset:26368\n\t" \
   "ds_write_b32 v21, a214 offset:15808\n\t" "ds_write_b32 v21, a215 offset:36928\n\t" "s_waitcnt vmcnt(13)\n\t" \
   "ds_write_b32 v21, a240 offset:5312\n\t" "ds_write_b32 v21, a241 offset:26432\n\t" "ds_write_b32 v21, a242 offset:15872\n\t" \
   "ds_write_b32 v21, a243 offset:36992\n\t" "s_waitcnt vmcnt(12)\n\t" "ds_write_b32 v21, a244 offset:5376\n\t" \
   "ds_write_b32 v21, a245 offset:26496\n\t" "ds_write_b32 v21, a246 offset:15936\n\t" "ds_write_b32 v21, a247 offset:37056\n\t" \
   "s_waitcnt vmcnt(11)\n\t" "ds_write_b32 v21, v240 offset:5440\n\t" "ds_write_b32 v21, v241 offset:26560\n\t" \
   "ds_write_b32 v21, v242 offset:16000\n\t" "ds_write_b32 v21, v243 offset:37120\n\t" "s_waitcnt vmcnt(10)\n\t" \
   "ds_write_b32 v21, v244 offset:5504\n\t" "ds_write_b32 v21, v245 offset:26624\n\t" "ds_write_b32 v21, v246 offset:16064\n\t" \
   "ds_write_b32 v21, v247 offset:37184\n\t" "buffer_load_dwordx4 v[28:31], v112, %[rw], 0 offen sc1\n\t" "s_waitcnt lgkmcnt(0)\n\t" "s_barrier\n\t" \
   LDSTAMP(6) "ds_read_b64_tr_b16 a[144:145], v22 offset:0\n\t" "ds_read_b64_tr_b16 a[146:147], v22 offset:656\n\t" \
   "ds_read_b64_tr_b16 a[148:149], v22 offset:2624\n\t" "ds_read_b64_tr_b16 a[150:151], v22 offset:3280\n\t" \
   "ds_read_b64_tr_b16 a[176:177], v22 offset:16\n\t" "ds_read_b64_tr_b16 a[178:179], v22 offset:672\n\t" \
   "ds_read_b64_tr_b16 a[180:181], v22 offset:2640\n\t" "ds_read_b64_tr_b16 a[182:183], v22 offset:3296\n\t" \
   "ds_read_b64_tr_b16 a[208:209], v22 offset:32\n\t" "ds_read_b64_tr_b16 a[210:211], v22 offset:688\n\t" \
   "ds_read_b64_tr_b16 a[212:213], v22 offset:2656\n\t" "ds_read_b64_tr_b16 a[214:215], v22 offset:3312\n\t" \
   "ds_read_b64_tr_b16 a[240:241], v22 offset:48\n\t" "ds_read_b64_tr_b16 a[242:243], v22 offset:704\n\t" \
   "ds_read_b64_tr_b16 a[244:245], v22 offset:2672\n\t" "ds_read_b64_tr_b16 a[246:247], v22 offset:3328\n\t" \
   "ds_read_b64_tr_b16 v[240:241], v22 offset:64\n\t" "ds_read_b64_tr_b16 v[242:243], v22 offset:720\n\t" \
   "ds_read_b64_tr_b16 v[244:245], v22 offset:2688\n\t" "ds_read_b64_tr_b16 v[246:247], v22 offset:3344\n\t" "s_waitcnt vmcnt(10)\n\t" \
   "s_waitcnt lgkmcnt(0)\n\t" "s_barrier\n\t" "ds_write_b32 v21, a152 offset:0\n\t" "ds_write_b32 v21, a153 offset:21120\n\t" \
   "ds_write_b32 v21, a154 offset:10560\n\t" "ds_write_b32 v21, a155 offset:31680\n\t" "s_waitcnt vmcnt(9)\n\t" "ds_write_b32 v21, a156 offset:64\n\t" \
   "ds_write_b32 v21, a157 offset:21184\n\t" "ds_write_b32 v21, a158 offset:10624\n\t" "ds_write_b32 v21, a159 offset:31744\n\t" \
   "s_waitcnt vmcnt(8)\n\t" "ds_write_b32 v21, a184 offset:128\n\t" "ds_write_b32 v21, a185 offset:21248\n\t" "ds_write_b32 v21, a186 offset:10688\n\t" \
   "ds_write_b32 v21, a187 offset:31808\n\t" "s_waitcnt vmcnt(7)\n\t" "ds_write_b32 v21, a188 offset:192\n\t" "ds_write_b32 v21, a189 offset:21312\n\t" \
   "ds_write_b32 v21, a190 offset:10752\n\t" "ds_write_b32 v21, a191 offset:31872\n\t" "s_waitcnt vmcnt(6)\n\t" "ds_write_b32 v21, a216 offset:256\n\t" \
   "ds_write_b32 v21, a217 offset:21376\n\t" "ds_write_b32 v21, a218 offset:10816\n\t" "ds_write_b32 v21, a219 offset:31936\n\t" \
   "s_waitcnt vmcnt(5)\n\t" "ds_write_b32 v21, a220 offset:5248\n\t" "ds_write_b32 v21, a221 offset:26368\n\t" \
   "ds_write_b32 v21, a222 offset:15808\n\t" "ds_write_b32 v21, a223 offset:36928\n\t" "s_waitcnt vmcnt(4)\n\t" \
   "ds_write_b32 v21, a248 offset:5312\n\t" "ds_write_b32 v21, a249 offset:26432\n\t" "ds_write_b32 v21, a250 offset:15872\n\t" \
   "ds_write_b32 v21, a251 offset:36992\n\t" "s_waitcnt vmcnt(3)\n\t" "ds_write_b32 v21, a252 offset:5376\n\t" \
   "ds_write_b32 v21, a253 offset:26496\n\t" "ds_write_b32 v21, a254 offset:15936\n\t" "ds_write_b32 v21, a255 offset:37056\n\t" \
   "s_waitcnt vmcnt(2)\n\t" "ds_write_b32 v21, v248 offset:5440\n\t" "ds_write_b32 v21, v249 offset:26560\n\t" \
   "ds_write_b32 v21, v250 offset:16000\n\t" "ds_write_b32 v21, v251 offset:37120\n\t" "s_waitcnt vmcnt(1)\n\t" \
   "ds_write_b32 v21, v252 offset:5504\n\t" "ds_write_b32 v21, v253 offset:26624\n\t" "ds_write_b32 v21, v254 offset:16064\n\t" \
   "ds_write_b32 v21, v255 offset:37184\n\t" "s_waitcnt lgkmcnt(0)\n\t" "s_barrier\n\t" LDSTAMP(7) "ds_read_b64_tr_b16 a[152:153], v22 offset:0\n\t" \
   "ds_read_b64_tr_b16 a[154:155], v22 offset:656\n\t" "ds_read_b64_tr_b16 a[156:157], v22 offset:2624\n\t" \
   "ds_read_b64_tr_b16 a[158:159], v22 offset:3280\n\t" "ds_read_b64_tr_b16 a[184:185], v22 offset:16\n\t" \
   "ds_read_b64_tr_b16 a[186:187], v22 offset:672\n\t" "ds_read_b64_tr_b16 a[188:189], v22 offset:2640\n\t" \
   "ds_read_b64_tr_b16 a[190:191], v22 offset:3296\n\t" "ds_read_b64_tr_b16 a[216:217], v22 offset:32\n\t" \
   "ds_read_b64_tr_b16 a[218:219], v22 offset:688\n\t" "ds_read_b64_tr_b16 a[220:221], v22 offset:2656\n\t" \
   "ds_read_b64_tr_b16 a[222:223], v22 offset:3312\n\t" "ds_read_b64_tr_b16 a[248:249], v22 offset:48\n\t" \
   "ds_read_b64_tr_b16 a[250:251], v22 offset:704\n\t" "ds_read_b64_tr_b16 a[252:253], v22 offset:2672\n\t" \
   "ds_read_b64_tr_b16 a[254:255], v22 offset:3328\n\t" "ds_read_b64_tr_b16 v[248:249], v22 offset:64\n\t" \
   "ds_read_b64_tr_b16 v[250:251], v22 offset:720\n\t" "ds_read_b64_tr_b16 v[252:253], v22 offset:2688\n\t" \
   "ds_read_b64_tr_b16 v[254:255], v22 offset:3344\n\t" "s_waitcnt vmcnt(0) lgkmcnt(0)\n\t" "s_barrier"
#define LDY_CLOB "v24", "v25", "v26", "v32", "v33", "v34", "v35", "v36", "v37", "v38", "v39", "v40", "v41", "v42", "v43", "v44", "v45", "v46", "v47", "v48", "v49", "v50", "v51", "v52", "v53", "v54", "v55", "v56", "v57", "v58", "v59", "v60", "v61", "v62", "v63", "v64", "v65", "v66", "v67", "v68", "v69", "v70", "v71", "v72", "v73", "v74", "v75", "v76", "v77", "v78", "v79", "v80", "v81", "v82", "v83", "v84", "v85", "v86", "v87", "v88", "v89", "v90", "v91", "v92", "v93", "v94", "v95", "v96", "v97", "v98", "v99", "v100", "v101", "v102", "v103", "v104", "v105", "v106", "v107", "v108", "v109", "v110", "v111", "v120", "v121", "v122", "v123", "s40", "s41"
#define W_OPS "{a[0:31]}"(w[0]), "{a[32:63]}"(w[1]), "{a[64:95]}"(w[2]), "{a[96:127]}"(w[3]), "{a[128:159]}"(w[4]), "{a[160:191]}"(w[5]), \
              "{a[192:223]}"(w[6]), "{a[224:255]}"(w[7]), "{v[192:223]}"(w[8]), "{v[224:255]}"(w[9])

__device__ __forceinline__ int mem_lane(int lane) { return lane * 16; }
__device__ __forceinline__ int pair_of(int g) { return (g & 1) * 2 + (g >> 1); }

__device__ __forceinline__ void rec_p1(int wv_, const v32u (&w)[10], unsigned char* ws, const Pool& pool, int layer, int h, int xs, RecTune tn, unsigned long long* stamps) {
  const Clk0 c0 = clk_sample();
  const unsigned lds0 = (unsigned)(uintptr_t)(__attribute__((address_space(3))) unsigned char*)dyn_lds;
  const int lane = lane_id(), wv = wv_, g = lane >> 4, r = lane & 15;
  const v4u rs = make_rs(ws, (unsigned)OFF_END);
  const int p0 = wv < 3 ? 3 * wv : 8;
  v16u ad, la = (unsigned)lane;
  const int kqo = (int)(wv < 2 ? OFF_KF : OFF_QF) + ((h * RT + (wv & 1)) * 4 * 64 + lane) * 16;
  const int progo = (wv == 0 && lane == 0) ? prog_off(layer, 0, h) : OOB;
  {
    const int p2 = wv < 3 ? p0 + 2 : 0;
    ad[0] = W_MEM + (p0 >> 1) * WIN + (p0 & 1) * 1024 + mem_lane(lane);
    ad[1] = W_MEM + ((p0 + 1) >> 1) * WIN + ((p0 + 1) & 1) * 1024 + mem_lane(lane);
    ad[2] = W_MEM + (p2 >> 1) * WIN + (p2 & 1) * 1024 + mem_lane(lane);
    ad[3] = lane == 0 ? W_MISC + h * 64 + wv * 4 : OOB;
    ad[4] = copy_off(wv) + (h >> 2) * WIN + (h & 3) * 512 + (g >> 1) * 256 + (2 * (r & 7) + (r >> 3)) * 16 + (g & 1) * 8;
    ad[5] = g < 2 ? copy_off(wv + 4 * (r >> 3)) + (8 + (h >> 4)) * WIN + ((h >> 3) & 1) * 1024 + (((h >> 1) & 3) * 16 + (h & 1) * 8 + (r & 7)) * 16 + g * 8 : OOB;
    ad[6] = (r & 1) ? 0xEEEEu : 0x4444u;
    ad[7] = lds0 + (32 * wv + r) * 16 + g * 4;
    ad[8] = lds0 + (4 * g + (r >> 2)) * 16 + (r & 1) * 8;
    ad[9] = lds0 + L_M2 + wv * 3072 + lane * 16;
    ad[10] = kqo;
    ad[11] = M_TM + xs * 128 + (lane & 1) * 12;
    ad[12] = kqf_off(layer, 0, h) + (lane & (h < NFILL ? 7 : 3)) * 4;
    ad[13] = wv > 1 ? (int)OFF_PXF + ((h * 2 + (wv & 1)) * 64 + lane) * 16 : OOB;
    ad[14] = (g < 2 && wv > 1) ? (int)OFF_PXM + (((h >> 2) * 2 + (wv & 1)) * 64 + (h & 3) * 16 + r) * 16 + g * 8 : OOB;
    ad[15] = progo;
#pragma unroll
    for (int i = 0; i < 10; ++i) { int p = p0 + i; p = p >= 10 ? p - 10 : p; la[i] = lds0 + p * 1024 + lane * 16; }
    la[10] = lds0 + L_KQ + lane * 16;
    la[12] = lds0 + REC_ST + wv * 8;
    asm volatile("v_mov_b32 %0, 0xceabfb8f" : "=v"(la[13]));
    la[14] = 16 * (wv & 1) + r - 4 * g;
    la[15] = lds0 + L_KQ + (wv & 1) * 4096 + lane * 16;
  }
  __builtin_amdgcn_s_waitcnt(0x0F70);
  if (layer) __builtin_amdgcn_s_sleep(64);
  v4u clk = clk_init(c0);
  unsigned long long stat;
  asm volatile(P1_ASM
               : "=&{s[76:77]}"(stat), "+{s[56:59]}"(clk)
               : W_OPS, "{v[128:143]}"(ad), "{v[112:127]}"(la), [rs] "s"(rs), [sel] "s"(pool.sel), [pool] "s"(pool.base), [xs] "s"(xs),
                 [lad0] "s"(ladder(xs, xs & 1)), [s3] "s"(pool.s3), [m1] "s"(tn.m1), [l2s] "s"(lds0 + L_M2 + wv * 3072),
                 [hi8] "s"(0xFF00FF00FF00FF00ull), [wv] "s"(wv), [kf0] "s"((int)OFF_KF + h * RT * 4096),
                 [esc] "s"(pool.base + (NSEG - 1) * REG)
               : P1_CLOB RST_CLOB, "vcc", "scc", "m0", "memory");
  LDSBAR();
  {
    const unsigned one = 1u;
    const int so = (NSEG - 1) * 4096;
    VM_ST32(one, progo, so);
    VM_WAIT(0);
  }
}

__device__ __forceinline__ void rec_pv(int wv_, const v32u (&w)[10], unsigned char* ws, const Pool& pool, int layer, int h, int xs, RecTune tn, unsigned long long* stamps) {
  const Clk0 c0 = clk_sample();
  const unsigned lds0 = (unsigned)(uintptr_t)(__attribute__((address_space(3))) unsigned char*)dyn_lds;
  const int lane = lane_id(), wv = wv_, g = lane >> 4, r = lane & 15;
  const v4u rs = make_rs(ws, (unsigned)OFF_END);
  const int p0 = wv < 3 ? 3 * wv : 8, p2 = wv < 3 ? p0 + 2 : 0;
  v16u ad = (unsigned)lane, la = (unsigned)lane;
  ad[0] = W_MEM + (p0 >> 1) * WIN + (p0 & 1) * 1024 + mem_lane(lane);
  ad[1] = W_MEM + ((p0 + 1) >> 1) * WIN + ((p0 + 1) & 1) * 1024 + mem_lane(lane);
  ad[2] = W_MEM + (p2 >> 1) * WIN + (p2 & 1) * 1024 + mem_lane(lane);
  ad[3] = (r & 8) ? 0xEEEEu : 0x4444u;
  ad[4] = (wv * 10 + (g >> 1)) * 16384 + h * 256 + r * 16 + (g & 1) * 8;
  ad[5] = h * 16384 + wv * 2048 + lane * 16;
  ad[6] = (r & 1) ? 0xEEEEu : 0x4444u;
  ad[7] = lds0 + (32 * wv + r) * 16 + pair_of(g) * 4;
  ad[8] = lds0 + (4 * g + (r >> 2)) * 16 + (r & 1) * 8;
  ad[11] = M_TM + (4 + xs) * 128 + (lane & 1) * 12;
  ad[15] = (wv == 0 && lane == 0) ? prog_off(layer, 0, 32 + h) : OOB;
#pragma unroll
  for (int i = 0; i < 10; ++i) { int p = p0 + i; p = p >= 10 ? p - 10 : p; la[i] = lds0 + p * 1024 + lane * 16; }
  la[12] = lds0 + REC_ST + wv * 8;
  __builtin_amdgcn_s_waitcnt(0x0F70);
  if (layer) __builtin_amdgcn_s_sleep(64);
  v4u clk = clk_init(c0);
  unsigned long long stat;
  asm volatile(PV_ASM
               : "=&{s[76:77]}"(stat), "+{s[56:59]}"(clk)
               : W_OPS, "{v[128:143]}"(ad), "{v[112:127]}"(la), [rs] "s"(rs), [sel] "s"(pool.sel), [pool] "s"(pool.base), [xs] "s"(xs),
                 [lad0] "s"(ladder(xs, xs & 1)), [s3] "s"(pool.s3), [m1c] "s"(tn.m1 + tn.c2), [esc] "s"(pool.base + (NSEG - 1) * REG),
                 [ysel] "s"(pool.ysel), [yb] "s"(pool.base + P_Y)
               : PV_CLOB RST_CLOB, "vcc", "scc", "memory");
}

#define TSTART 5300
#define TSTART1 TSTART
#define TSTARU 3600
#define TD1 900u
#define TR2 400u
#define TEMA 1
#define TEMB 2
#define TBUP 48
#define TBDN 6
#define TLATE 520
#define TSEC 50
__device__ __forceinline__ unsigned track_dur(unsigned e, int m, int rob, int s) {
  const int lo = (int)e - (s < 8 ? 64 : (rob & 255)), hi = (int)e + (rob >> 8);
  if (s >= 8) m = (int)e + ((m - (int)e + (m > (int)e ? (1 << TEMA) - 1 : 0)) >> (m > (int)e ? TEMA : TEMB));
  return (unsigned)min(max(m, lo), hi);
}
#define TTS 200
#define TTU 240
__device__ __forceinline__ void rec_sched(unsigned char* ws, const Pool& pool, int layer, int c16, int xs, RecTune tn, unsigned long long* stamps) {
  const Clk0 c0 = clk_sample();
  const unsigned lds0 = (unsigned)(uintptr_t)(__attribute__((address_space(3))) unsigned char*)dyn_lds;
  const int lane = lane_id();
  const v4u rs = make_rs(ws, (unsigned)OFF_END);
  unsigned long long stat = 0;
  const int sm = M_STAMP + min(lane, 39) * 64, sp = W_MISC + (lane & 31) * 64;
  const int yo = M_TM + xs * 128, oc = (c16 == 0 && lane < 16) ? M_TM + lane * 128 : OOB;
  const unsigned cv = flag_finish<2>(rs, kqf_off(layer, 0, lane), 0xFFFFFFFFu);
  v4u clk = clk_init(c0);
  PH(3 + layer * 6)
  unsigned r2a = TR2, d1a = TD1, dd = TD1 + tn.m2, tp = 0, tm = 0, anchor = clk.x - dd;
  unsigned rr = (unsigned)min(max(wave_imax((int)(cv * 8u - clk.x) + (lane < 32 ? (layer ? TSTART1 : TSTART) : TSTARU)), 300), 4000);
  int bonus = 0;
  bool clean = false;
#pragma unroll 1
  for (int s = 0; s < NSEG - 1; ++s) {
    const int rgm = pool.region((s + NSEG - 1) & (NSEG - 1)), rg = pool.region(s);
    const unsigned slot = lds0 + L_T + (s & 1) * 32, ao = anchor;
    unsigned b2 = 1u;
    v4u b1 = {1u, 1u, 1u, 1u};
    if (s > 0) {
      asm volatile(WAIT_ASM : "+{s[56:59]}"(clk) : [ta] "s"(tm + TTS) : WAIT_CLOB, "scc", "memory");
      VM_LD32(b2, sm, rgm);
      VM_LD128(b1, sp, rgm, 0);
      asm volatile("s_waitcnt vmcnt(0)" : "+v"(b2), "+v"(b1)::"memory");
      anchor = tm;
      if (__builtin_expect(__any(b2 == 0xFFFFFFFFu || (int)(b2 - (tm + tn.m1 - tn.vis)) > 0), 0)) {
        STAT(3);
        for (unsigned spins = 0; __any(b2 == 0xFFFFFFFFu) && spins < SPIN_CAP; ++spins) {
          __builtin_amdgcn_s_sleep(2);
          VM_LD32(b2, sm, rgm);
          VM_WAIT1(0, b2);
        }
        const int d = wave_imax((int)(b2 - tm)) + tn.vis - tn.m1;
        anchor = tm + (d <= 0 ? 0 : (d <= tn.dlt ? tn.dlt + TSEC : d + TLATE));
      }
    }
    const unsigned tpo = tp, tmo = tm;
    tp = anchor + dd;
    tm = nosent(tp + rr);
    LDS_V4(slot) = v4u{tp, tm, (unsigned)(s + 1), anchor};
    if (anchor != tmo && s > 2) bonus = TBUP; else bonus = max(bonus - TBDN, 0);
    dd = d1a + tn.m2 + bonus;
    rr = r2a + bonus;
    {
      const v4u wq = v4u{tm, dd, rr, dd + rr};
      VM_ST128(wq, oc, rg);
    }
    if (s > 1 && !__any((b2 & 1u) != 0u)) r2a = track_dur(r2a, wave_imax((int)(b2 - tpo)), tn.rob, s);
    if (s > 1 && clean && !__any(((b1.x | b1.y | b1.z | b1.w) & 1u) != 0u))
      d1a = track_dur(d1a, wave_imax(max(max((int)(b1.x - ao), (int)(b1.y - ao)), max((int)(b1.z - ao), (int)(b1.w - ao)))), tn.rob, s);
    clean = anchor == tmo;
    while ((int)(tp + 40u - rtc() * 8u) > 0 && (int)(tp - rtc() * 8u) < 32000) __builtin_amdgcn_s_sleep(1);
    v4u lw;
    VM_LD128(lw, yo, rg, 0);
    VM_WAIT1(0, lw);
    const unsigned l0 = (unsigned)__builtin_amdgcn_readfirstlane(lw.x), l1 = (unsigned)__builtin_amdgcn_readfirstlane(lw.y),
                   l2 = (unsigned)__builtin_amdgcn_readfirstlane(lw.z);
    if (max(max(l0, l1), l2) != 0xFFFFFFFFu) { tm = l0; dd = l1; rr = l2; }
  }
}

__device__ __forceinline__ void rec_fin(unsigned char* ws, const Pool& pool, int layer, int c16, int xs, RecTune tn, unsigned long long* stamps) {
  const Clk0 c0 = clk_sample();
  const unsigned lds0 = (unsigned)(uintptr_t)(__attribute__((address_space(3))) unsigned char*)dyn_lds;
  const int lane = lane_id(), g = lane >> 4, r = lane & 15;
  const v4u rs = make_rs(ws, (unsigned)OFF_END);
  v8u ad;
  ad[0] = lane * 16;
  ad[1] = g * 256 + r * 8;
  ad[2] = (r & 1) ? 0x03020706u : 0x05040100u;
  ad[3] = (lane & 31) * 64;
  ad[4] = W_MEM + copy_off(((r >> 1) & 1) * 5) + (c16 >> 3) * WIN + (c16 & 7) * 256 + (2 * (2 * g + (r & 1)) + (r >> 3)) * 16 + ((r >> 2) & 1) * 8;
  ad[5] = lane == 0 ? M_STAMP + c16 * 64 : OOB;
  ad[6] = (r & 1) ? 0xEEEEu : 0x4444u;
  ad[7] = lds0 + L_T;
  __builtin_amdgcn_s_sleep(64);
  v4u clk = clk_init(c0);
  unsigned long long stat;
  asm volatile(FIN_ASM
               : "=&{s[76:77]}"(stat), "+{s[56:59]}"(clk), "+{v[16:23]}"(ad)
               : [rs] "s"(rs), [sel] "s"(pool.sel), [pool] "s"(pool.base), [xs] "s"(xs), [lad0] "s"(ladder(xs, xs & 1)), [s3] "s"(pool.s3),
                 [su] "s"((int)OFF_UF + c16 * 32768), [yb] "s"(pool.base + P_Y + c16 * 16384), [ysel] "s"(pool.ysel), [tu] "s"(TTU),
                 [stl] "s"(lds0 + REC_ST - 32), [uf] "s"(kqf_off(layer, 0, 32))
               : FIN_CLOB RST_CLOB, "vcc", "scc", "memory");
}

constexpr int PJ_BUF = 43 * 1024, PJ_X = 16384, PJ_LDS = PJ_X + 3 * PJ_BUF;
#define KQ_ASM \
   ".p2align 6\n\t" "s_nop 0\n\t" "s_mov_b32 s44, %[rdy]\n\t" "s_mov_b32 s45, 0\n\t" "s_mov_b32 s46, 0\n\t" "s_cmp_eq_u32 s44, -1\n\t" \
   "s_cbranch_scc1 68f\n\t" "s_mul_i32 s52, s45, 1536\n\t" "s_mul_i32 s51, s45, 40960\n\t" "s_add_u32 m0, s46, %[ldx]\n\t" "s_mov_b32 s53, s46\n\t" \
   "buffer_load_dwordx4 v16, %[rs], s51 offen offset:0 sc1 lds\n\t" "buffer_load_dwordx4 v16, %[rs], s51 offen offset:1024 sc1 lds\n\t" \
   "buffer_load_dwordx4 v16, %[rs], s51 offen offset:2048 sc1 lds\n\t" "buffer_load_dwordx4 v16, %[rs], s51 offen offset:3072 sc1 lds\n\t" \
   "s_add_u32 m0, m0, 4096\n\t" "s_add_u32 s51, s51, 4096\n\t" "buffer_load_dwordx4 v16, %[rs], s51 offen offset:0 sc1 lds\n\t" \
   "buffer_load_dwordx4 v16, %[rs], s51 offen offset:1024 sc1 lds\n\t" "buffer_load_dwordx4 v16, %[rs], s51 offen offset:2048 sc1 lds\n\t" \
   "buffer_load_dwordx4 v16, %[rs], s51 offen offset:3072 sc1 lds\n\t" "s_add_u32 m0, m0, 4096\n\t" "s_add_u32 s51, s51, 4096\n\t" \
   "buffer_load_dwordx4 v16, %[rs], s51 offen offset:0 sc1 lds\n\t" "buffer_load_dwordx4 v16, %[rs], s51 offen offset:1024 sc1 lds\n\t" \
   "s_add_u32 m0, s53, %[ld1]\n\t" "s_nop 0\n\t" "buffer_load_dwordx4 v17, %[rs], s52 offen sc1 lds\n\t" "s_mov_b32 s45, 1\n\t" \
   "s_mov_b32 s46, 44032\n\t" "s_mul_i32 s52, s45, 1536\n\t" "s_mul_i32 s51, s45, 40960\n\t" "s_add_u32 m0, s46, %[ldx]\n\t" "s_mov_b32 s53, s46\n\t" \
   "buffer_load_dwordx4 v16, %[rs], s51 offen offset:0 sc1 lds\n\t" "buffer_load_dwordx4 v16, %[rs], s51 offen offset:1024 sc1 lds\n\t" \
   "buffer_load_dwordx4 v16, %[rs], s51 offen offset:2048 sc1 lds\n\t" "buffer_load_dwordx4 v16, %[rs], s51 offen offset:3072 sc1 lds\n\t" \
   "s_add_u32 m0, m0, 4096\n\t" "s_add_u32 s51, s51, 4096\n\t" "buffer_load_dwordx4 v16, %[rs], s51 offen offset:0 sc1 lds\n\t" \
   "buffer_load_dwordx4 v16, %[rs], s51 offen offset:1024 sc1 lds\n\t" "buffer_load_dwordx4 v16, %[rs], s51 offen offset:2048 sc1 lds\n\t" \
   "buffer_load_dwordx4 v16, %[rs], s51 offen offset:3072 sc1 lds\n\t" "s_add_u32 m0, m0, 4096\n\t" "s_add_u32 s51, s51, 4096\n\t" \
   "buffer_load_dwordx4 v16, %[rs], s51 offen offset:0 sc1 lds\n\t" "buffer_load_dwordx4 v16, %[rs], s51 offen offset:1024 sc1 lds\n\t" \
   "s_add_u32 m0, s53, %[ld1]\n\t" "s_nop 0\n\t" "buffer_load_dwordx4 v17, %[rs], s52 offen sc1 lds\n\t" "68:\n\t" "v_mov_b32 v138, 0\n\t" \
   "v_mov_b32 v13, %[flw]\n\t" "v_and_b32 v12, -16, v13\n\t" "ds_write_b32 v13, v138\n\t" "ds_read_b128 v[32:35], v26 offset:0\n\t" \
   "ds_read_b128 v[36:39], v26 offset:16\n\t" "ds_read_b128 v[40:43], v26 offset:32\n\t" "ds_read_b128 v[44:47], v26 offset:48\n\t" \
   "ds_read_b128 v[48:51], v26 offset:64\n\t" "ds_read_b128 v[52:55], v26 offset:80\n\t" "ds_read_b128 v[56:59], v26 offset:96\n\t" \
   "ds_read_b128 v[60:63], v26 offset:112\n\t" "s_mov_b32 s40, 0\n\t" "s_mov_b32 s41, 0\n\t" "s_mov_b32 s42, 0\n\t" "s_mov_b32 s43, 44032\n\t" \
   "v_mov_b32 v28, v22\n\t" "v_mov_b32 v29, v23\n\t" "s_mov_b32 s45, 2\n\t" "s_mov_b32 s46, 88064\n\t" "s_waitcnt vmcnt(11) lgkmcnt(0)\n\t" \
   "s_barrier\n\t" "s_memrealtime s[54:55]\n\t" "v_mov_b32 v146, -1\n\t" "s_cmp_eq_u32 s44, -1\n\t" "s_cbranch_scc0 172f\n\t" "72:\n\t" \
   "ds_read_b128 v[160:163], v28 offset:0\n\t" "ds_read_b128 v[164:167], v28 offset:512\n\t" "ds_read_b128 v[168:171], v28 offset:1024\n\t" \
   "ds_read_b128 v[172:175], v28 offset:1536\n\t" "s_waitcnt lgkmcnt(0)\n\t" "v_mov_b32 v31, s54\n\t" "v_min_u32 v31, -2, v31\n\t" "1:\n\t" \
   "v_mov_b32 v26, v28\n\t" "v_mov_b32 v27, v29\n\t" "s_waitcnt lgkmcnt(2)\n\t" "v_mfma_f32_32x32x16_bf16 v[64:79], a[0:3], v[160:163], 0\n\t" \
   "ds_read_b128 v[176:179], v26 offset:2048\n\t" "v_mfma_f32_32x32x16_bf16 v[80:95], a[8:11], v[160:163], 0\n\t" "v_mul_f32 v11, 0x3e0293ee, v10\n\t" \
   "s_lshl_b32 s47, s41, 13\n\t" "s_sub_i32 s47, s47, 8192\n\t" "s_max_i32 s47, s47, 0\n\t" "s_cmp_eq_u32 s40, 0\n\t" \
   "s_cselect_b32 s48, 0x7ffffff0, 0\n\t" "v_mfma_f32_32x32x16_bf16 v[64:79], a[4:7], v[164:167], v[64:79]\n\t" \
   "ds_read_b128 v[180:183], v26 offset:2560\n\t" "v_or_b32 v30, s48, v21\n\t" "v_fmac_f32 v96, v32, v8\n\t" "v_fmac_f32 v97, v33, v8\n\t" \
   "v_fmac_f32 v98, v34, v8\n\t" "v_mfma_f32_32x32x16_bf16 v[80:95], a[12:15], v[164:167], v[80:95]\n\t" "v_fmac_f32 v99, v35, v8\n\t" \
   "v_fmac_f32 v100, v36, v8\n\t" "v_fmac_f32 v101, v37, v8\n\t" "v_fmac_f32 v102, v38, v8\n\t" "v_fmac_f32 v103, v39, v8\n\t" \
   "s_waitcnt lgkmcnt(2)\n\t" "v_mfma_f32_32x32x16_bf16 v[64:79], a[16:19], v[168:171], v[64:79]\n\t" "ds_read_b128 v[184:187], v26 offset:3072\n\t" \
   "v_fmac_f32 v104, v40, v8\n\t" "v_fmac_f32 v105, v41, v8\n\t" "v_fmac_f32 v106, v42, v8\n\t" "v_fmac_f32 v107, v43, v8\n\t" \
   "v_mfma_f32_32x32x16_bf16 v[80:95], a[24:27], v[168:171], v[80:95]\n\t" "v_fmac_f32 v108, v44, v8\n\t" "v_fmac_f32 v109, v45, v8\n\t" \
   "v_fmac_f32 v110, v46, v8\n\t" "v_fmac_f32 v111, v47, v8\n\t" "v_mul_f32 v96, v96, v11\n\t" "v_mul_f32 v97, v97, v11\n\t" \
   "v_mfma_f32_32x32x16_bf16 v[64:79], a[20:23], v[172:175], v[64:79]\n\t" "ds_read_b128 v[188:191], v26 offset:3584\n\t" "v_mul_f32 v98, v98, v11\n\t" \
   "v_mul_f32 v99, v99, v11\n\t" "v_mul_f32 v100, v100, v11\n\t" "v_mul_f32 v101, v101, v11\n\t" \
   "v_mfma_f32_32x32x16_bf16 v[80:95], a[28:31], v[172:175], v[80:95]\n\t" "v_mul_f32 v102, v102, v11\n\t" "v_mul_f32 v103, v103, v11\n\t" \
   "v_mul_f32 v104, v104, v11\n\t" "v_mul_f32 v105, v105, v11\n\t" "v_mul_f32 v106, v106, v11\n\t" "s_waitcnt lgkmcnt(2)\n\t" \
   "v_mfma_f32_32x32x16_bf16 v[64:79], a[32:35], v[176:179], v[64:79]\n\t" "ds_read_b128 v[160:163], v26 offset:4096\n\t" \
   "v_mul_f32 v107, v107, v11\n\t" "v_mul_f32 v108, v108, v11\n\t" "v_mul_f32 v109, v109, v11\n\t" "v_mul_f32 v110, v110, v11\n\t" \
   "v_mfma_f32_32x32x16_bf16 v[80:95], a[40:43], v[176:179], v[80:95]\n\t" "v_mul_f32 v111, v111, v11\n\t" "s_add_u32 s48, s47, %[qf]\n\t" \
   "v_cvt_pk_bf16_f32 v96, v96, v97\n\t" "v_cvt_pk_bf16_f32 v97, v98, v99\n\t" "v_cvt_pk_bf16_f32 v98, v104, v105\n\t" \
   "v_cvt_pk_bf16_f32 v99, v106, v107\n\t" "v_mfma_f32_32x32x16_bf16 v[64:79], a[36:39], v[180:183], v[64:79]\n\t" \
   "ds_read_b128 v[164:167], v26 offset:4608\n\t" "v_mfma_f32_32x32x16_bf16 v[80:95], a[44:47], v[180:183], v[80:95]\n\t" \
   "buffer_store_dwordx4 v[96:99], v30, %[rs], s48 offen offset:0 sc1\n\t" "s_waitcnt lgkmcnt(2)\n\t" \
   "v_mfma_f32_32x32x16_bf16 v[64:79], a[48:51], v[184:187], v[64:79]\n\t" "ds_read_b128 v[168:171], v26 offset:5120\n\t" \
   "v_cvt_pk_bf16_f32 v100, v100, v101\n\t" "v_cvt_pk_bf16_f32 v101, v102, v103\n\t" "v_cvt_pk_bf16_f32 v102, v108, v109\n\t" \
   "v_cvt_pk_bf16_f32 v103, v110, v111\n\t" "v_mfma_f32_32x32x16_bf16 v[80:95], a[56:59], v[184:187], v[80:95]\n\t" \
   "buffer_store_dwordx4 v[100:103], v30, %[rs], s48 offen offset:512 sc1\n\t" "v_fmac_f32 v112, v48, v8\n\t" \
   "v_mfma_f32_32x32x16_bf16 v[64:79], a[52:55], v[188:191], v[64:79]\n\t" "ds_read_b128 v[172:175], v26 offset:5632\n\t" \
   "v_fmac_f32 v113, v49, v8\n\t" "v_fmac_f32 v114, v50, v8\n\t" "v_fmac_f32 v115, v51, v8\n\t" "v_fmac_f32 v116, v52, v8\n\t" \
   "v_mfma_f32_32x32x16_bf16 v[80:95], a[60:63], v[188:191], v[80:95]\n\t" "v_fmac_f32 v117, v53, v8\n\t" "v_fmac_f32 v118, v54, v8\n\t" \
   "v_fmac_f32 v119, v55, v8\n\t" "v_fmac_f32 v120, v56, v8\n\t" "v_fmac_f32 v121, v57, v8\n\t" "s_waitcnt lgkmcnt(2)\n\t" \
   "v_mfma_f32_32x32x16_bf16 v[64:79], a[64:67], v[160:163], v[64:79]\n\t" "ds_read_b128 v[176:179], v26 offset:6144\n\t" \
   "v_fmac_f32 v122, v58, v8\n\t" "v_fmac_f32 v123, v59, v8\n\t" "v_fmac_f32 v124, v60, v8\n\t" "v_fmac_f32 v125, v61, v8\n\t" \
   "v_mfma_f32_32x32x16_bf16 v[80:95], a[72:75], v[160:163], v[80:95]\n\t" "v_fmac_f32 v126, v62, v8\n\t" "v_fmac_f32 v127, v63, v8\n\t" \
   "v_mul_f32 v112, v112, v10\n\t" "v_mul_f32 v113, v113, v10\n\t" "v_mul_f32 v114, v114, v10\n\t" "v_mul_f32 v115, v115, v10\n\t" \
   "v_mfma_f32_32x32x16_bf16 v[64:79], a[68:71], v[164:167], v[64:79]\n\t" "ds_read_b128 v[180:183], v26 offset:6656\n\t" \
   "v_mul_f32 v116, v116, v10\n\t" "v_mul_f32 v117, v117, v10\n\t" "v_mul_f32 v118, v118, v10\n\t" "v_mul_f32 v119, v119, v10\n\t" \
   "v_mfma_f32_32x32x16_bf16 v[80:95], a[76:79], v[164:167], v[80:95]\n\t" "v_mul_f32 v120, v120, v10\n\t" "v_mul_f32 v121, v121, v10\n\t" \
   "v_mul_f32 v122, v122, v10\n\t" "v_mul_f32 v123, v123, v10\n\t" "v_mul_f32 v124, v124, v10\n\t" "s_waitcnt lgkmcnt(2)\n\t" \
   "v_mfma_f32_32x32x16_bf16 v[64:79], a[80:83], v[168:171], v[64:79]\n\t" "ds_read_b128 v[184:187], v26 offset:7168\n\t" \
   "v_mul_f32 v125, v125, v10\n\t" "v_mul_f32 v126, v126, v10\n\t" "v_mul_f32 v127, v127, v10\n\t" "s_add_u32 s48, s47, %[kf]\n\t" \
   "v_mfma_f32_32x32x16_bf16 v[80:95], a[88:91], v[168:171], v[80:95]\n\t" "v_cvt_pk_bf16_f32 v112, v112, v113\n\t" \
   "v_cvt_pk_bf16_f32 v113, v114, v115\n\t" "v_cvt_pk_bf16_f32 v114, v120, v121\n\t" "v_cvt_pk_bf16_f32 v115, v122, v123\n\t" \
   "ds_read_b128 v[128:131], v27 offset:0\n\t" "v_mfma_f32_32x32x16_bf16 v[64:79], a[84:87], v[172:175], v[64:79]\n\t" \
   "ds_read_b128 v[188:191], v26 offset:7680\n\t" "ds_read_b128 v[132:135], v27 offset:16\n\t" "ds_read_b64 v[136:137], v27 offset:32\n\t" \
   "v_mfma_f32_32x32x16_bf16 v[80:95], a[92:95], v[172:175], v[80:95]\n\t" "buffer_store_dwordx4 v[112:115], v30, %[rs], s48 offen offset:0 sc1\n\t" \
   "s_waitcnt lgkmcnt(5)\n\t" "v_mfma_f32_32x32x16_bf16 v[64:79], a[96:99], v[176:179], v[64:79]\n\t" "ds_read_b128 v[160:163], v26 offset:8192\n\t" \
   "v_cvt_pk_bf16_f32 v116, v116, v117\n\t" "v_cvt_pk_bf16_f32 v117, v118, v119\n\t" "v_cvt_pk_bf16_f32 v118, v124, v125\n\t" \
   "v_cvt_pk_bf16_f32 v119, v126, v127\n\t" "v_mfma_f32_32x32x16_bf16 v[80:95], a[104:107], v[176:179], v[80:95]\n\t" \
   "buffer_store_dwordx4 v[116:119], v30, %[rs], s48 offen offset:512 sc1\n\t" \
   "v_mfma_f32_32x32x16_bf16 v[64:79], a[100:103], v[180:183], v[64:79]\n\t" "ds_read_b128 v[164:167], v26 offset:8704\n\t" \
   "v_add_u32 v28, s43, v22\n\t" "v_add_u32 v29, s43, v23\n\t" "v_mfma_f32_32x32x16_bf16 v[80:95], a[108:111], v[180:183], v[80:95]\n\t" \
   "s_waitcnt lgkmcnt(4)\n\t" "v_mfma_f32_32x32x16_bf16 v[64:79], a[112:115], v[184:187], v[64:79]\n\t" "ds_read_b128 v[168:171], v26 offset:9216\n\t" \
   "v_mfma_f32_32x32x16_bf16 v[80:95], a[120:123], v[184:187], v[80:95]\n\t" "ds_read_b128 v[140:143], v12\n\t" "s_waitcnt lgkmcnt(4)\n\t" \
   "v_add_f32 v128, v128, v130\n\t" "v_add_f32 v129, v129, v131\n\t" "v_add_f32 v132, v132, v134\n\t" "v_add_f32 v133, v133, v135\n\t" \
   "v_mfma_f32_32x32x16_bf16 v[64:79], a[116:119], v[188:191], v[64:79]\n\t" "ds_read_b128 v[172:175], v26 offset:9728\n\t" \
   "v_add_f32 v128, v128, v136\n\t" "v_add_f32 v129, v129, v137\n\t" "v_add_f32 v128, v128, v132\n\t" "v_add_f32 v129, v129, v133\n\t" \
   "v_mfma_f32_32x32x16_bf16 v[80:95], a[124:127], v[188:191], v[80:95]\n\t" "v_mul_f32 v128, 0xbacccccd, v128\n\t" \
   "v_mul_f32 v129, 0x3acccccd, v129\n\t" "v_fma_f32 v129, -v128, v128, v129\n\t" "v_max_f32 v129, 0, v129\n\t" \
   "v_cvt_pk_bf16_f32 v129, v129, v128\n\t" "s_waitcnt lgkmcnt(3)\n\t" "v_mfma_f32_32x32x16_bf16 v[64:79], a[128:131], v[160:163], v[64:79]\n\t" \
   "ds_read_b128 v[176:179], v26 offset:10240\n\t" "s_waitcnt lgkmcnt(2)\n\t" "v_min3_u32 v140, v140, v141, v142\n\t" "v_min_u32 v140, v140, v143\n\t" \
   "v_cmp_ge_u32 vcc, v140, v138\n\t" "v_mfma_f32_32x32x16_bf16 v[80:95], a[136:139], v[160:163], v[80:95]\n\t" "s_cbranch_vccz 150f\n\t" "50:\n\t" \
   "s_mul_i32 s52, s45, 1536\n\t" "s_mul_i32 s51, s45, 40960\n\t" "s_add_u32 m0, s46, %[ldx]\n\t" "s_mov_b32 s53, s46\n\t" \
   "v_mfma_f32_32x32x16_bf16 v[64:79], a[132:135], v[164:167], v[64:79]\n\t" "ds_read_b128 v[180:183], v26 offset:10752\n\t" \
   "buffer_load_dwordx4 v16, %[rs], s51 offen offset:0 sc1 lds\n\t" "v_and_b32 v8, 0xffff0000, v129\n\t" \
   "v_mfma_f32_32x32x16_bf16 v[80:95], a[140:143], v[164:167], v[80:95]\n\t" "v_lshlrev_b32 v129, 16, v129\n\t" "v_add_f32 v129, 0x3727c5ac, v129\n\t" \
   "v_cvt_pk_bf16_f32 v129, v129, v129\n\t" "v_lshlrev_b32 v129, 16, v129\n\t" "s_waitcnt lgkmcnt(2)\n\t" \
   "v_mfma_f32_32x32x16_bf16 v[64:79], a[144:147], v[168:171], v[64:79]\n\t" "ds_read_b128 v[184:187], v26 offset:11264\n\t" \
   "v_sqrt_f32 v129, v129\n\t" "s_nop 0\n\t" "v_cvt_pk_bf16_f32 v129, v129, v129\n\t" \
   "v_mfma_f32_32x32x16_bf16 v[80:95], a[152:155], v[168:171], v[80:95]\n\t" "buffer_load_dwordx4 v16, %[rs], s51 offen offset:1024 sc1 lds\n\t" \
   "v_lshlrev_b32 v129, 16, v129\n\t" "v_rcp_f32 v10, v129\n\t" "v_mfma_f32_32x32x16_bf16 v[64:79], a[148:151], v[172:175], v[64:79]\n\t" \
   "ds_read_b128 v[188:191], v26 offset:11776\n\t" "v_mfma_f32_32x32x16_bf16 v[80:95], a[156:159], v[172:175], v[80:95]\n\t" "s_waitcnt lgkmcnt(2)\n\t" \
   "v_mfma_f32_32x32x16_bf16 v[64:79], a[160:163], v[176:179], v[64:79]\n\t" "ds_read_b128 v[160:163], v26 offset:12288\n\t" \
   "buffer_load_dwordx4 v16, %[rs], s51 offen offset:2048 sc1 lds\n\t" "v_mfma_f32_32x32x16_bf16 v[80:95], a[168:171], v[176:179], v[80:95]\n\t" \
   "v_mfma_f32_32x32x16_bf16 v[64:79], a[164:167], v[180:183], v[64:79]\n\t" "ds_read_b128 v[164:167], v26 offset:12800\n\t" \
   "v_mfma_f32_32x32x16_bf16 v[80:95], a[172:175], v[180:183], v[80:95]\n\t" "buffer_load_dwordx4 v16, %[rs], s51 offen offset:3072 sc1 lds\n\t" \
   "s_add_u32 m0, m0, 4096\n\t" "s_add_u32 s51, s51, 4096\n\t" "s_waitcnt lgkmcnt(2)\n\t" \
   "v_mfma_f32_32x32x16_bf16 v[64:79], a[176:179], v[184:187], v[64:79]\n\t" "ds_read_b128 v[168:171], v26 offset:13312\n\t" "s_waitcnt vmcnt(8)\n\t" \
   "v_add_u32 v138, 1, v138\n\t" "ds_write_b32 v13, v138\n\t" "v_mfma_f32_32x32x16_bf16 v[80:95], a[184:187], v[184:187], v[80:95]\n\t" \
   "v_mfma_f32_32x32x16_bf16 v[64:79], a[180:183], v[188:191], v[64:79]\n\t" "ds_read_b128 v[172:175], v26 offset:13824\n\t" \
   "buffer_load_dwordx4 v16, %[rs], s51 offen offset:0 sc1 lds\n\t" "v_mfma_f32_32x32x16_bf16 v[80:95], a[188:191], v[188:191], v[80:95]\n\t" \
   "s_waitcnt lgkmcnt(3)\n\t" "v_mfma_f32_32x32x16_bf16 v[64:79], a[192:195], v[160:163], v[64:79]\n\t" "ds_read_b128 v[176:179], v26 offset:14336\n\t" \
   "v_mfma_f32_32x32x16_bf16 v[80:95], a[200:203], v[160:163], v[80:95]\n\t" "buffer_load_dwordx4 v16, %[rs], s51 offen offset:1024 sc1 lds\n\t" \
   "v_mfma_f32_32x32x16_bf16 v[64:79], a[196:199], v[164:167], v[64:79]\n\t" "ds_read_b128 v[180:183], v26 offset:14848\n\t" \
   "v_mfma_f32_32x32x16_bf16 v[80:95], a[204:207], v[164:167], v[80:95]\n\t" "s_waitcnt lgkmcnt(2)\n\t" \
   "v_mfma_f32_32x32x16_bf16 v[64:79], a[208:211], v[168:171], v[64:79]\n\t" "ds_read_b128 v[184:187], v26 offset:15360\n\t" \
   "buffer_load_dwordx4 v16, %[rs], s51 offen offset:2048 sc1 lds\n\t" "v_mfma_f32_32x32x16_bf16 v[80:95], a[216:219], v[168:171], v[80:95]\n\t" \
   "v_mfma_f32_32x32x16_bf16 v[64:79], a[212:215], v[172:175], v[64:79]\n\t" "ds_read_b128 v[188:191], v26 offset:15872\n\t" \
   "v_mfma_f32_32x32x16_bf16 v[80:95], a[220:223], v[172:175], v[80:95]\n\t" "buffer_load_dwordx4 v16, %[rs], s51 offen offset:3072 sc1 lds\n\t" \
   "s_add_u32 m0, m0, 4096\n\t" "s_add_u32 s51, s51, 4096\n\t" "s_waitcnt lgkmcnt(2)\n\t" \
   "v_mfma_f32_32x32x16_bf16 v[64:79], a[224:227], v[176:179], v[64:79]\n\t" "ds_read_b128 v[160:163], v26 offset:16384\n\t" \
   "v_mfma_f32_32x32x16_bf16 v[80:95], a[232:235], v[176:179], v[80:95]\n\t" "v_mfma_f32_32x32x16_bf16 v[64:79], a[228:231], v[180:183], v[64:79]\n\t" \
   "ds_read_b128 v[164:167], v26 offset:16896\n\t" "buffer_load_dwordx4 v16, %[rs], s51 offen offset:0 sc1 lds\n\t" \
   "v_mfma_f32_32x32x16_bf16 v[80:95], a[236:239], v[180:183], v[80:95]\n\t" "s_waitcnt lgkmcnt(2)\n\t" \
   "v_mfma_f32_32x32x16_bf16 v[64:79], a[240:243], v[184:187], v[64:79]\n\t" "ds_read_b128 v[168:171], v26 offset:17408\n\t" "s_cmp_lt_i32 s40, 1\n\t" \
   "s_cselect_b32 s49, 0x7ffffff0, 0\n\t" "v_or_b32 v9, s49, v20\n\t" "s_sub_i32 s49, s41, 1\n\t" \
   "v_mfma_f32_32x32x16_bf16 v[80:95], a[248:251], v[184:187], v[80:95]\n\t" "s_max_i32 s49, s49, 0\n\t" "s_lshl_b32 s49, s49, 12\n\t" \
   "s_waitcnt vmcnt(9)\n\t" "buffer_store_dword v31, v9, %[rs], s49 offen sc1\n\t" \
   "v_mfma_f32_32x32x16_bf16 v[64:79], a[244:247], v[188:191], v[64:79]\n\t" "ds_read_b128 v[172:175], v26 offset:17920\n\t" \
   "v_mfma_f32_32x32x16_bf16 v[80:95], a[252:255], v[188:191], v[80:95]\n\t" "buffer_load_dwordx4 v16, %[rs], s51 offen offset:1024 sc1 lds\n\t" \
   "s_add_u32 m0, s53, %[ld1]\n\t" "s_nop 0\n\t" "s_waitcnt lgkmcnt(2)\n\t" "v_mfma_f32_32x32x16_bf16 v[64:79], v[192:195], v[160:163], v[64:79]\n\t" \
   "ds_read_b128 v[176:179], v26 offset:18432\n\t" "buffer_load_dwordx4 v17, %[rs], s52 offen sc1 lds\n\t" "ds_read_b128 v[140:143], v12\n\t" \
   "v_mfma_f32_32x32x16_bf16 v[80:95], v[200:203], v[160:163], v[80:95]\n\t" "v_mfma_f32_32x32x16_bf16 v[64:79], v[196:199], v[164:167], v[64:79]\n\t" \
   "ds_read_b128 v[180:183], v26 offset:18944\n\t" "v_mfma_f32_32x32x16_bf16 v[80:95], v[204:207], v[164:167], v[80:95]\n\t" "s_waitcnt lgkmcnt(3)\n\t" \
   "v_mfma_f32_32x32x16_bf16 v[64:79], v[208:211], v[168:171], v[64:79]\n\t" "ds_read_b128 v[184:187], v26 offset:19456\n\t" "s_waitcnt lgkmcnt(2)\n\t" \
   "v_min3_u32 v140, v140, v141, v142\n\t" "v_min_u32 v140, v140, v143\n\t" "v_cmp_ge_u32 vcc, v140, v138\n\t" \
   "v_mfma_f32_32x32x16_bf16 v[80:95], v[216:219], v[168:171], v[80:95]\n\t" "s_cbranch_vccz 152f\n\t" "52:\n\t" \
   "v_mfma_f32_32x32x16_bf16 v[64:79], v[212:215], v[172:175], v[64:79]\n\t" "ds_read_b128 v[188:191], v26 offset:19968\n\t" \
   "v_mfma_f32_32x32x16_bf16 v[80:95], v[220:223], v[172:175], v[80:95]\n\t" "v_add_u32 v138, 1, v138\n\t" "ds_write_b32 v13, v138\n\t" \
   "s_waitcnt lgkmcnt(3)\n\t" "v_mfma_f32_32x32x16_bf16 v[64:79], v[224:227], v[176:179], v[64:79]\n\t" "ds_read_b128 v[160:163], v28 offset:0\n\t" \
   "s_add_u32 s45, s40, 3\n\t" "s_min_u32 s45, s45, 63\n\t" "s_mov_b32 s46, s42\n\t" \
   "v_mfma_f32_32x32x16_bf16 v[80:95], v[232:235], v[176:179], v[80:95]\n\t" "s_cmp_eq_u32 s44, -1\n\t" "s_cbranch_scc0 174f\n\t" "74:\n\t" \
   "s_mov_b32 s42, s43\n\t" "s_add_u32 s43, s43, 44032\n\t" "v_mfma_f32_32x32x16_bf16 v[64:79], v[228:231], v[180:183], v[64:79]\n\t" \
   "ds_read_b128 v[164:167], v28 offset:512\n\t" "s_cmp_eq_u32 s43, 132096\n\t" "s_cselect_b32 s43, 0, s43\n\t" "s_add_u32 s41, s41, 1\n\t" \
   "s_add_u32 s40, s40, 1\n\t" "v_mfma_f32_32x32x16_bf16 v[80:95], v[236:239], v[180:183], v[80:95]\n\t" "s_waitcnt lgkmcnt(3)\n\t" \
   "v_mfma_f32_32x32x16_bf16 v[64:79], v[240:243], v[184:187], v[64:79]\n\t" "ds_read_b128 v[168:171], v28 offset:1024\n\t" \
   "v_mfma_f32_32x32x16_bf16 v[80:95], v[248:251], v[184:187], v[80:95]\n\t" "v_mfma_f32_32x32x16_bf16 v[64:79], v[244:247], v[188:191], v[64:79]\n\t" \
   "ds_read_b128 v[172:175], v28 offset:1536\n\t" "v_mfma_f32_32x32x16_bf16 v[80:95], v[252:255], v[188:191], v[80:95]\n\t" "v_mov_b32 v26, v28\n\t" \
   "v_mov_b32 v27, v29\n\t" "s_waitcnt lgkmcnt(2)\n\t" "v_mfma_f32_32x32x16_bf16 v[96:111], a[0:3], v[160:163], 0\n\t" \
   "ds_read_b128 v[176:179], v26 offset:2048\n\t" "v_mfma_f32_32x32x16_bf16 v[112:127], a[8:11], v[160:163], 0\n\t" \
   "v_mul_f32 v11, 0x3e0293ee, v10\n\t" "s_lshl_b32 s47, s41, 13\n\t" "s_sub_i32 s47, s47, 8192\n\t" "v_fmac_f32 v64, v32, v8\n\t" \
   "v_fmac_f32 v65, v33, v8\n\t" "v_fmac_f32 v66, v34, v8\n\t" "v_mfma_f32_32x32x16_bf16 v[96:111], a[4:7], v[164:167], v[96:111]\n\t" \
   "ds_read_b128 v[180:183], v26 offset:2560\n\t" "v_fmac_f32 v67, v35, v8\n\t" "v_fmac_f32 v68, v36, v8\n\t" "v_fmac_f32 v69, v37, v8\n\t" \
   "v_fmac_f32 v70, v38, v8\n\t" "v_mfma_f32_32x32x16_bf16 v[112:127], a[12:15], v[164:167], v[112:127]\n\t" "v_fmac_f32 v71, v39, v8\n\t" \
   "v_fmac_f32 v72, v40, v8\n\t" "v_fmac_f32 v73, v41, v8\n\t" "v_fmac_f32 v74, v42, v8\n\t" "v_fmac_f32 v75, v43, v8\n\t" "s_waitcnt lgkmcnt(2)\n\t" \
   "v_mfma_f32_32x32x16_bf16 v[96:111], a[16:19], v[168:171], v[96:111]\n\t" "ds_read_b128 v[184:187], v26 offset:3072\n\t" \
   "v_fmac_f32 v76, v44, v8\n\t" "v_fmac_f32 v77, v45, v8\n\t" "v_fmac_f32 v78, v46, v8\n\t" "v_fmac_f32 v79, v47, v8\n\t" \
   "v_mfma_f32_32x32x16_bf16 v[112:127], a[24:27], v[168:171], v[112:127]\n\t" "v_mul_f32 v64, v64, v11\n\t" "v_mul_f32 v65, v65, v11\n\t" \
   "v_mul_f32 v66, v66, v11\n\t" "v_mul_f32 v67, v67, v11\n\t" "v_mul_f32 v68, v68, v11\n\t" "v_mul_f32 v69, v69, v11\n\t" \
   "v_mfma_f32_32x32x16_bf16 v[96:111], a[20:23], v[172:175], v[96:111]\n\t" "ds_read_b128 v[188:191], v26 offset:3584\n\t" \
   "v_mul_f32 v70, v70, v11\n\t" "v_mul_f32 v71, v71, v11\n\t" "v_mul_f32 v72, v72, v11\n\t" "v_mul_f32 v73, v73, v11\n\t" \
   "v_mfma_f32_32x32x16_bf16 v[112:127], a[28:31], v[172:175], v[112:127]\n\t" "v_mul_f32 v74, v74, v11\n\t" "v_mul_f32 v75, v75, v11\n\t" \
   "v_mul_f32 v76, v76, v11\n\t" "v_mul_f32 v77, v77, v11\n\t" "v_mul_f32 v78, v78, v11\n\t" "s_waitcnt lgkmcnt(2)\n\t" \
   "v_mfma_f32_32x32x16_bf16 v[96:111], a[32:35], v[176:179], v[96:111]\n\t" "ds_read_b128 v[160:163], v26 offset:4096\n\t" \
   "v_mul_f32 v79, v79, v11\n\t" "s_add_u32 s48, s47, %[qf]\n\t" "v_cvt_pk_bf16_f32 v64, v64, v65\n\t" "v_cvt_pk_bf16_f32 v65, v66, v67\n\t" \
   "v_mfma_f32_32x32x16_bf16 v[112:127], a[40:43], v[176:179], v[112:127]\n\t" "v_cvt_pk_bf16_f32 v66, v72, v73\n\t" \
   "v_cvt_pk_bf16_f32 v67, v74, v75\n\t" "v_mfma_f32_32x32x16_bf16 v[96:111], a[36:39], v[180:183], v[96:111]\n\t" \
   "ds_read_b128 v[164:167], v26 offset:4608\n\t" "v_mfma_f32_32x32x16_bf16 v[112:127], a[44:47], v[180:183], v[112:127]\n\t" \
   "buffer_store_dwordx4 v[64:67], v21, %[rs], s48 offen offset:0 sc1\n\t" "s_waitcnt lgkmcnt(2)\n\t" \
   "v_mfma_f32_32x32x16_bf16 v[96:111], a[48:51], v[184:187], v[96:111]\n\t" "ds_read_b128 v[168:171], v26 offset:5120\n\t" \
   "v_cvt_pk_bf16_f32 v68, v68, v69\n\t" "v_cvt_pk_bf16_f32 v69, v70, v71\n\t" "v_cvt_pk_bf16_f32 v70, v76, v77\n\t" \
   "v_cvt_pk_bf16_f32 v71, v78, v79\n\t" "v_mfma_f32_32x32x16_bf16 v[112:127], a[56:59], v[184:187], v[112:127]\n\t" \
   "buffer_store_dwordx4 v[68:71], v21, %[rs], s48 offen offset:512 sc1\n\t" "v_fmac_f32 v80, v48, v8\n\t" \
   "v_mfma_f32_32x32x16_bf16 v[96:111], a[52:55], v[188:191], v[96:111]\n\t" "ds_read_b128 v[172:175], v26 offset:5632\n\t" \
   "v_fmac_f32 v81, v49, v8\n\t" "v_fmac_f32 v82, v50, v8\n\t" "v_fmac_f32 v83, v51, v8\n\t" "v_fmac_f32 v84, v52, v8\n\t" \
   "v_mfma_f32_32x32x16_bf16 v[112:127], a[60:63], v[188:191], v[112:127]\n\t" "v_fmac_f32 v85, v53, v8\n\t" "v_fmac_f32 v86, v54, v8\n\t" \
   "v_fmac_f32 v87, v55, v8\n\t" "v_fmac_f32 v88, v56, v8\n\t" "v_fmac_f32 v89, v57, v8\n\t" "s_waitcnt lgkmcnt(2)\n\t" \
   "v_mfma_f32_32x32x16_bf16 v[96:111], a[64:67], v[160:163], v[96:111]\n\t" "ds_read_b128 v[176:179], v26 offset:6144\n\t" \
   "v_fmac_f32 v90, v58, v8\n\t" "v_fmac_f32 v91, v59, v8\n\t" "v_fmac_f32 v92, v60, v8\n\t" "v_fmac_f32 v93, v61, v8\n\t" \
   "v_mfma_f32_32x32x16_bf16 v[112:127], a[72:75], v[160:163], v[112:127]\n\t" "v_fmac_f32 v94, v62, v8\n\t" "v_fmac_f32 v95, v63, v8\n\t" \
   "v_mul_f32 v80, v80, v10\n\t" "v_mul_f32 v81, v81, v10\n\t" "v_mul_f32 v82, v82, v10\n\t" "v_mul_f32 v83, v83, v10\n\t" \
   "v_mfma_f32_32x32x16_bf16 v[96:111], a[68:71], v[164:167], v[96:111]\n\t" "ds_read_b128 v[180:183], v26 offset:6656\n\t" \
   "v_mul_f32 v84, v84, v10\n\t" "v_mul_f32 v85, v85, v10\n\t" "v_mul_f32 v86, v86, v10\n\t" "v_mul_f32 v87, v87, v10\n\t" \
   "v_mfma_f32_32x32x16_bf16 v[112:127], a[76:79], v[164:167], v[112:127]\n\t" "v_mul_f32 v88, v88, v10\n\t" "v_mul_f32 v89, v89, v10\n\t" \
   "v_mul_f32 v90, v90, v10\n\t" "v_mul_f32 v91, v91, v10\n\t" "v_mul_f32 v92, v92, v10\n\t" "s_waitcnt lgkmcnt(2)\n\t" \
   "v_mfma_f32_32x32x16_bf16 v[96:111], a[80:83], v[168:171], v[96:111]\n\t" "ds_read_b128 v[184:187], v26 offset:7168\n\t" \
   "v_mul_f32 v93, v93, v10\n\t" "v_mul_f32 v94, v94, v10\n\t" "v_mul_f32 v95, v95, v10\n\t" "s_add_u32 s48, s47, %[kf]\n\t" \
   "v_mfma_f32_32x32x16_bf16 v[112:127], a[88:91], v[168:171], v[112:127]\n\t" "v_cvt_pk_bf16_f32 v80, v80, v81\n\t" \
   "v_cvt_pk_bf16_f32 v81, v82, v83\n\t" "v_cvt_pk_bf16_f32 v82, v88, v89\n\t" "v_cvt_pk_bf16_f32 v83, v90, v91\n\t" \
   "ds_read_b128 v[128:131], v27 offset:0\n\t" "v_mfma_f32_32x32x16_bf16 v[96:111], a[84:87], v[172:175], v[96:111]\n\t" \
   "ds_read_b128 v[188:191], v26 offset:7680\n\t" "ds_read_b128 v[132:135], v27 offset:16\n\t" "ds_read_b64 v[136:137], v27 offset:32\n\t" \
   "v_mfma_f32_32x32x16_bf16 v[112:127], a[92:95], v[172:175], v[112:127]\n\t" "buffer_store_dwordx4 v[80:83], v21, %[rs], s48 offen offset:0 sc1\n\t" \
   "s_waitcnt lgkmcnt(5)\n\t" "v_mfma_f32_32x32x16_bf16 v[96:111], a[96:99], v[176:179], v[96:111]\n\t" "ds_read_b128 v[160:163], v26 offset:8192\n\t" \
   "v_cvt_pk_bf16_f32 v84, v84, v85\n\t" "v_cvt_pk_bf16_f32 v85, v86, v87\n\t" "v_cvt_pk_bf16_f32 v86, v92, v93\n\t" \
   "v_cvt_pk_bf16_f32 v87, v94, v95\n\t" "v_mfma_f32_32x32x16_bf16 v[112:127], a[104:107], v[176:179], v[112:127]\n\t" \
   "buffer_store_dwordx4 v[84:87], v21, %[rs], s48 offen offset:512 sc1\n\t" \
   "v_mfma_f32_32x32x16_bf16 v[96:111], a[100:103], v[180:183], v[96:111]\n\t" "ds_read_b128 v[164:167], v26 offset:8704\n\t" \
   "v_add_u32 v28, s43, v22\n\t" "v_add_u32 v29, s43, v23\n\t" "v_mfma_f32_32x32x16_bf16 v[112:127], a[108:111], v[180:183], v[112:127]\n\t" \
   "s_waitcnt lgkmcnt(4)\n\t" "v_mfma_f32_32x32x16_bf16 v[96:111], a[112:115], v[184:187], v[96:111]\n\t" \
   "ds_read_b128 v[168:171], v26 offset:9216\n\t" "v_mfma_f32_32x32x16_bf16 v[112:127], a[120:123], v[184:187], v[112:127]\n\t" \
   "ds_read_b128 v[140:143], v12\n\t" "s_waitcnt lgkmcnt(4)\n\t" "v_add_f32 v128, v128, v130\n\t" "v_add_f32 v129, v129, v131\n\t" \
   "v_add_f32 v132, v132, v134\n\t" "v_add_f32 v133, v133, v135\n\t" "v_mfma_f32_32x32x16_bf16 v[96:111], a[116:119], v[188:191], v[96:111]\n\t" \
   "ds_read_b128 v[172:175], v26 offset:9728\n\t" "v_add_f32 v128, v128, v136\n\t" "v_add_f32 v129, v129, v137\n\t" "v_add_f32 v128, v128, v132\n\t" \
   "v_add_f32 v129, v129, v133\n\t" "v_mfma_f32_32x32x16_bf16 v[112:127], a[124:127], v[188:191], v[112:127]\n\t" \
   "v_mul_f32 v128, 0xbacccccd, v128\n\t" "v_mul_f32 v129, 0x3acccccd, v129\n\t" "v_fma_f32 v129, -v128, v128, v129\n\t" "v_max_f32 v129, 0, v129\n\t" \
   "v_cvt_pk_bf16_f32 v129, v129, v128\n\t" "s_waitcnt lgkmcnt(3)\n\t" "v_mfma_f32_32x32x16_bf16 v[96:111], a[128:131], v[160:163], v[96:111]\n\t" \
   "ds_read_b128 v[176:179], v26 offset:10240\n\t" "s_waitcnt lgkmcnt(2)\n\t" "v_min3_u32 v140, v140, v141, v142\n\t" "v_min_u32 v140, v140, v143\n\t" \
   "v_cmp_ge_u32 vcc, v140, v138\n\t" "v_mfma_f32_32x32x16_bf16 v[112:127], a[136:139], v[160:163], v[112:127]\n\t" "s_cbranch_vccz 151f\n\t" "51:\n\t" \
   "s_mul_i32 s52, s45, 1536\n\t" "s_mul_i32 s51, s45, 40960\n\t" "s_add_u32 m0, s46, %[ldx]\n\t" "s_mov_b32 s53, s46\n\t" \
   "v_mfma_f32_32x32x16_bf16 v[96:111], a[132:135], v[164:167], v[96:111]\n\t" "ds_read_b128 v[180:183], v26 offset:10752\n\t" \
   "buffer_load_dwordx4 v16, %[rs], s51 offen offset:0 sc1 lds\n\t" "v_and_b32 v8, 0xffff0000, v129\n\t" \
   "v_mfma_f32_32x32x16_bf16 v[112:127], a[140:143], v[164:167], v[112:127]\n\t" "v_lshlrev_b32 v129, 16, v129\n\t" \
   "v_add_f32 v129, 0x3727c5ac, v129\n\t" "v_cvt_pk_bf16_f32 v129, v129, v129\n\t" "v_lshlrev_b32 v129, 16, v129\n\t" "s_waitcnt lgkmcnt(2)\n\t" \
   "v_mfma_f32_32x32x16_bf16 v[96:111], a[144:147], v[168:171], v[96:111]\n\t" "ds_read_b128 v[184:187], v26 offset:11264\n\t" \
   "v_sqrt_f32 v129, v129\n\t" "s_nop 0\n\t" "v_cvt_pk_bf16_f32 v129, v129, v129\n\t" \
   "v_mfma_f32_32x32x16_bf16 v[112:127], a[152:155], v[168:171], v[112:127]\n\t" "buffer_load_dwordx4 v16, %[rs], s51 offen offset:1024 sc1 lds\n\t" \
   "v_lshlrev_b32 v129, 16, v129\n\t" "v_rcp_f32 v10, v129\n\t" "v_mfma_f32_32x32x16_bf16 v[96:111], a[148:151], v[172:175], v[96:111]\n\t" \
   "ds_read_b128 v[188:191], v26 offset:11776\n\t" "v_mfma_f32_32x32x16_bf16 v[112:127], a[156:159], v[172:175], v[112:127]\n\t" \
   "s_waitcnt lgkmcnt(2)\n\t" "v_mfma_f32_32x32x16_bf16 v[96:111], a[160:163], v[176:179], v[96:111]\n\t" \
   "ds_read_b128 v[160:163], v26 offset:12288\n\t" "buffer_load_dwordx4 v16, %[rs], s51 offen offset:2048 sc1 lds\n\t" \
   "v_mfma_f32_32x32x16_bf16 v[112:127], a[168:171], v[176:179], v[112:127]\n\t" \
   "v_mfma_f32_32x32x16_bf16 v[96:111], a[164:167], v[180:183], v[96:111]\n\t" "ds_read_b128 v[164:167], v26 offset:12800\n\t" \
   "v_mfma_f32_32x32x16_bf16 v[112:127], a[172:175], v[180:183], v[112:127]\n\t" "buffer_load_dwordx4 v16, %[rs], s51 offen offset:3072 sc1 lds\n\t" \
   "s_add_u32 m0, m0, 4096\n\t" "s_add_u32 s51, s51, 4096\n\t" "s_waitcnt lgkmcnt(2)\n\t" \
   "v_mfma_f32_32x32x16_bf16 v[96:111], a[176:179], v[184:187], v[96:111]\n\t" "ds_read_b128 v[168:171], v26 offset:13312\n\t" "s_waitcnt vmcnt(8)\n\t" \
   "v_add_u32 v138, 1, v138\n\t" "ds_write_b32 v13, v138\n\t" "v_mfma_f32_32x32x16_bf16 v[112:127], a[184:187], v[184:187], v[112:127]\n\t" \
   "v_mfma_f32_32x32x16_bf16 v[96:111], a[180:183], v[188:191], v[96:111]\n\t" "ds_read_b128 v[172:175], v26 offset:13824\n\t" \
   "buffer_load_dwordx4 v16, %[rs], s51 offen offset:0 sc1 lds\n\t" "v_mfma_f32_32x32x16_bf16 v[112:127], a[188:191], v[188:191], v[112:127]\n\t" \
   "s_waitcnt lgkmcnt(3)\n\t" "v_mfma_f32_32x32x16_bf16 v[96:111], a[192:195], v[160:163], v[96:111]\n\t" \
   "ds_read_b128 v[176:179], v26 offset:14336\n\t" "v_mfma_f32_32x32x16_bf16 v[112:127], a[200:203], v[160:163], v[112:127]\n\t" \
   "buffer_load_dwordx4 v16, %[rs], s51 offen offset:1024 sc1 lds\n\t" "v_mfma_f32_32x32x16_bf16 v[96:111], a[196:199], v[164:167], v[96:111]\n\t" \
   "ds_read_b128 v[180:183], v26 offset:14848\n\t" "v_mfma_f32_32x32x16_bf16 v[112:127], a[204:207], v[164:167], v[112:127]\n\t" \
   "s_waitcnt lgkmcnt(2)\n\t" "v_mfma_f32_32x32x16_bf16 v[96:111], a[208:211], v[168:171], v[96:111]\n\t" \
   "ds_read_b128 v[184:187], v26 offset:15360\n\t" "buffer_load_dwordx4 v16, %[rs], s51 offen offset:2048 sc1 lds\n\t" \
   "v_mfma_f32_32x32x16_bf16 v[112:127], a[216:219], v[168:171], v[112:127]\n\t" \
   "v_mfma_f32_32x32x16_bf16 v[96:111], a[212:215], v[172:175], v[96:111]\n\t" "ds_read_b128 v[188:191], v26 offset:15872\n\t" \
   "v_mfma_f32_32x32x16_bf16 v[112:127], a[220:223], v[172:175], v[112:127]\n\t" "buffer_load_dwordx4 v16, %[rs], s51 offen offset:3072 sc1 lds\n\t" \
   "s_add_u32 m0, m0, 4096\n\t" "s_add_u32 s51, s51, 4096\n\t" "s_waitcnt lgkmcnt(2)\n\t" \
   "v_mfma_f32_32x32x16_bf16 v[96:111], a[224:227], v[176:179], v[96:111]\n\t" "ds_read_b128 v[160:163], v26 offset:16384\n\t" \
   "v_mfma_f32_32x32x16_bf16 v[112:127], a[232:235], v[176:179], v[112:127]\n\t" \
   "v_mfma_f32_32x32x16_bf16 v[96:111], a[228:231], v[180:183], v[96:111]\n\t" "ds_read_b128 v[164:167], v26 offset:16896\n\t" \
   "buffer_load_dwordx4 v16, %[rs], s51 offen offset:0 sc1 lds\n\t" "v_mfma_f32_32x32x16_bf16 v[112:127], a[236:239], v[180:183], v[112:127]\n\t" \
   "s_waitcnt lgkmcnt(2)\n\t" "v_mfma_f32_32x32x16_bf16 v[96:111], a[240:243], v[184:187], v[96:111]\n\t" \
   "ds_read_b128 v[168:171], v26 offset:17408\n\t" "s_cmp_lt_i32 s40, 1\n\t" "s_cselect_b32 s49, 0x7ffffff0, 0\n\t" "v_or_b32 v9, s49, v20\n\t" \
   "s_sub_i32 s49, s41, 1\n\t" "v_mfma_f32_32x32x16_bf16 v[112:127], a[248:251], v[184:187], v[112:127]\n\t" "s_max_i32 s49, s49, 0\n\t" \
   "s_lshl_b32 s49, s49, 12\n\t" "s_waitcnt vmcnt(9)\n\t" "buffer_store_dword v31, v9, %[rs], s49 offen sc1\n\t" \
   "v_mfma_f32_32x32x16_bf16 v[96:111], a[244:247], v[188:191], v[96:111]\n\t" "ds_read_b128 v[172:175], v26 offset:17920\n\t" \
   "v_mfma_f32_32x32x16_bf16 v[112:127], a[252:255], v[188:191], v[112:127]\n\t" "buffer_load_dwordx4 v16, %[rs], s51 offen offset:1024 sc1 lds\n\t" \
   "s_add_u32 m0, s53, %[ld1]\n\t" "s_nop 0\n\t" "s_waitcnt lgkmcnt(2)\n\t" "v_mfma_f32_32x32x16_bf16 v[96:111], v[192:195], v[160:163], v[96:111]\n\t" \
   "ds_read_b128 v[176:179], v26 offset:18432\n\t" "buffer_load_dwordx4 v17, %[rs], s52 offen sc1 lds\n\t" "ds_read_b128 v[140:143], v12\n\t" \
   "v_mfma_f32_32x32x16_bf16 v[112:127], v[200:203], v[160:163], v[112:127]\n\t" \
   "v_mfma_f32_32x32x16_bf16 v[96:111], v[196:199], v[164:167], v[96:111]\n\t" "ds_read_b128 v[180:183], v26 offset:18944\n\t" \
   "v_mfma_f32_32x32x16_bf16 v[112:127], v[204:207], v[164:167], v[112:127]\n\t" "s_waitcnt lgkmcnt(3)\n\t" \
   "v_mfma_f32_32x32x16_bf16 v[96:111], v[208:211], v[168:171], v[96:111]\n\t" "ds_read_b128 v[184:187], v26 offset:19456\n\t" \
   "s_waitcnt lgkmcnt(2)\n\t" "v_min3_u32 v140, v140, v141, v142\n\t" "v_min_u32 v140, v140, v143\n\t" "v_cmp_ge_u32 vcc, v140, v138\n\t" \
   "v_mfma_f32_32x32x16_bf16 v[112:127], v[216:219], v[168:171], v[112:127]\n\t" "s_cbranch_vccz 153f\n\t" "53:\n\t" \
   "v_mfma_f32_32x32x16_bf16 v[96:111], v[212:215], v[172:175], v[96:111]\n\t" "ds_read_b128 v[188:191], v26 offset:19968\n\t" \
   "v_mfma_f32_32x32x16_bf16 v[112:127], v[220:223], v[172:175], v[112:127]\n\t" "v_add_u32 v138, 1, v138\n\t" "ds_write_b32 v13, v138\n\t" \
   "s_waitcnt lgkmcnt(3)\n\t" "v_mfma_f32_32x32x16_bf16 v[96:111], v[224:227], v[176:179], v[96:111]\n\t" "ds_read_b128 v[160:163], v28 offset:0\n\t" \
   "s_add_u32 s45, s40, 3\n\t" "s_min_u32 s45, s45, 63\n\t" "s_mov_b32 s46, s42\n\t" \
   "v_mfma_f32_32x32x16_bf16 v[112:127], v[232:235], v[176:179], v[112:127]\n\t" "s_cmp_eq_u32 s44, -1\n\t" "s_cbranch_scc0 178f\n\t" "78:\n\t" \
   "s_mov_b32 s42, s43\n\t" "s_add_u32 s43, s43, 44032\n\t" "v_mfma_f32_32x32x16_bf16 v[96:111], v[228:231], v[180:183], v[96:111]\n\t" \
   "ds_read_b128 v[164:167], v28 offset:512\n\t" "s_cmp_eq_u32 s43, 132096\n\t" "s_cselect_b32 s43, 0, s43\n\t" "s_add_u32 s41, s41, 1\n\t" \
   "s_add_u32 s40, s40, 1\n\t" "v_mfma_f32_32x32x16_bf16 v[112:127], v[236:239], v[180:183], v[112:127]\n\t" "s_waitcnt lgkmcnt(3)\n\t" \
   "v_mfma_f32_32x32x16_bf16 v[96:111], v[240:243], v[184:187], v[96:111]\n\t" "ds_read_b128 v[168:171], v28 offset:1024\n\t" \
   "v_mfma_f32_32x32x16_bf16 v[112:127], v[248:251], v[184:187], v[112:127]\n\t" \
   "v_mfma_f32_32x32x16_bf16 v[96:111], v[244:247], v[188:191], v[96:111]\n\t" "ds_read_b128 v[172:175], v28 offset:1536\n\t" \
   "v_mfma_f32_32x32x16_bf16 v[112:127], v[252:255], v[188:191], v[112:127]\n\t" "s_cmp_lt_u32 s40, 64\n\t" "s_cbranch_scc1 1b\n\t" "s_nop 7\n\t" \
   "v_mul_f32 v11, 0x3e0293ee, v10\n\t" "s_lshl_b32 s47, s41, 13\n\t" "s_sub_i32 s47, s47, 8192\n\t" "v_fmac_f32 v96, v32, v8\n\t" \
   "v_fmac_f32 v97, v33, v8\n\t" "v_fmac_f32 v98, v34, v8\n\t" "v_fmac_f32 v99, v35, v8\n\t" "v_fmac_f32 v100, v36, v8\n\t" \
   "v_fmac_f32 v101, v37, v8\n\t" "v_fmac_f32 v102, v38, v8\n\t" "v_fmac_f32 v103, v39, v8\n\t" "v_fmac_f32 v104, v40, v8\n\t" \
   "v_fmac_f32 v105, v41, v8\n\t" "v_fmac_f32 v106, v42, v8\n\t" "v_fmac_f32 v107, v43, v8\n\t" "v_fmac_f32 v108, v44, v8\n\t" \
   "v_fmac_f32 v109, v45, v8\n\t" "v_fmac_f32 v110, v46, v8\n\t" "v_fmac_f32 v111, v47, v8\n\t" "v_mul_f32 v96, v96, v11\n\t" \
   "v_mul_f32 v97, v97, v11\n\t" "v_mul_f32 v98, v98, v11\n\t" "v_mul_f32 v99, v99, v11\n\t" "v_mul_f32 v100, v100, v11\n\t" \
   "v_mul_f32 v101, v101, v11\n\t" "v_mul_f32 v102, v102, v11\n\t" "v_mul_f32 v103, v103, v11\n\t" "v_mul_f32 v104, v104, v11\n\t" \
   "v_mul_f32 v105, v105, v11\n\t" "v_mul_f32 v106, v106, v11\n\t" "v_mul_f32 v107, v107, v11\n\t" "v_mul_f32 v108, v108, v11\n\t" \
   "v_mul_f32 v109, v109, v11\n\t" "v_mul_f32 v110, v110, v11\n\t" "v_mul_f32 v111, v111, v11\n\t" "s_add_u32 s48, s47, %[qf]\n\t" \
   "v_cvt_pk_bf16_f32 v96, v96, v97\n\t" "v_cvt_pk_bf16_f32 v97, v98, v99\n\t" "v_cvt_pk_bf16_f32 v98, v104, v105\n\t" \
   "v_cvt_pk_bf16_f32 v99, v106, v107\n\t" "buffer_store_dwordx4 v[96:99], v21, %[rs], s48 offen offset:0 sc1\n\t" \
   "v_cvt_pk_bf16_f32 v100, v100, v101\n\t" "v_cvt_pk_bf16_f32 v101, v102, v103\n\t" "v_cvt_pk_bf16_f32 v102, v108, v109\n\t" \
   "v_cvt_pk_bf16_f32 v103, v110, v111\n\t" "buffer_store_dwordx4 v[100:103], v21, %[rs], s48 offen offset:512 sc1\n\t" "v_fmac_f32 v112, v48, v8\n\t" \
   "v_fmac_f32 v113, v49, v8\n\t" "v_fmac_f32 v114, v50, v8\n\t" "v_fmac_f32 v115, v51, v8\n\t" "v_fmac_f32 v116, v52, v8\n\t" \
   "v_fmac_f32 v117, v53, v8\n\t" "v_fmac_f32 v118, v54, v8\n\t" "v_fmac_f32 v119, v55, v8\n\t" "v_fmac_f32 v120, v56, v8\n\t" \
   "v_fmac_f32 v121, v57, v8\n\t" "v_fmac_f32 v122, v58, v8\n\t" "v_fmac_f32 v123, v59, v8\n\t" "v_fmac_f32 v124, v60, v8\n\t" \
   "v_fmac_f32 v125, v61, v8\n\t" "v_fmac_f32 v126, v62, v8\n\t" "v_fmac_f32 v127, v63, v8\n\t" "v_mul_f32 v112, v112, v10\n\t" \
   "v_mul_f32 v113, v113, v10\n\t" "v_mul_f32 v114, v114, v10\n\t" "v_mul_f32 v115, v115, v10\n\t" "v_mul_f32 v116, v116, v10\n\t" \
   "v_mul_f32 v117, v117, v10\n\t" "v_mul_f32 v118, v118, v10\n\t" "v_mul_f32 v119, v119, v10\n\t" "v_mul_f32 v120, v120, v10\n\t" \
   "v_mul_f32 v121, v121, v10\n\t" "v_mul_f32 v122, v122, v10\n\t" "v_mul_f32 v123, v123, v10\n\t" "v_mul_f32 v124, v124, v10\n\t" \
   "v_mul_f32 v125, v125, v10\n\t" "v_mul_f32 v126, v126, v10\n\t" "v_mul_f32 v127, v127, v10\n\t" "s_add_u32 s48, s47, %[kf]\n\t" \
   "v_cvt_pk_bf16_f32 v112, v112, v113\n\t" "v_cvt_pk_bf16_f32 v113, v114, v115\n\t" "v_cvt_pk_bf16_f32 v114, v120, v121\n\t" \
   "v_cvt_pk_bf16_f32 v115, v122, v123\n\t" "buffer_store_dwordx4 v[112:115], v21, %[rs], s48 offen offset:0 sc1\n\t" \
   "v_cvt_pk_bf16_f32 v116, v116, v117\n\t" "v_cvt_pk_bf16_f32 v117, v118, v119\n\t" "v_cvt_pk_bf16_f32 v118, v124, v125\n\t" \
   "v_cvt_pk_bf16_f32 v119, v126, v127\n\t" "buffer_store_dwordx4 v[116:119], v21, %[rs], s48 offen offset:512 sc1\n\t" \
   "s_waitcnt vmcnt(0) lgkmcnt(0)\n\t" "s_cmp_lt_i32 s40, 1\n\t" "s_cselect_b32 s47, 0x7ffffff0, 0\n\t" "v_or_b32 v30, s47, v20\n\t" \
   "s_sub_i32 s47, s41, 1\n\t" "s_max_i32 s47, s47, 0\n\t" "s_lshl_b32 s47, s47, 12\n\t" "buffer_store_dword v31, v30, %[rs], s47 offen sc1\n\t" \
   "s_branch 9f\n\t" "172:\n\t" "v_cmp_lt_i32 vcc, 6, v146\n\t" "s_lshr_b32 s48, s45, 1\n\t" "s_or_b32 s44, s44, vcc_lo\n\t" "s_cmp_lt_u32 s40, 4\n\t" \
   "s_cbranch_scc1 272f\n\t" "buffer_load_dword v146, v19, %[rs], 0 offen sc1\n\t" "272:\n\t" "s_bitcmp1_b32 s44, s48\n\t" "s_cbranch_scc1 72b\n\t" \
   "s_mov_b32 s47, 0\n\t" "buffer_load_dword v30, v19, %[rs], 0 offen sc1\n\t" "s_waitcnt vmcnt(0)\n\t" "v_add_u32 v30, 1, v30\n\t" \
   "v_cmp_lt_u32 vcc, 7, v30\n\t" "s_or_b32 s44, s44, vcc_lo\n\t" "s_bitcmp1_b32 s44, s48\n\t" "s_cbranch_scc1 72b\n\t" "s_bitset1_b32 s44, s48\n\t" \
   "s_lshl_b32 s48, s48, 7\n\t" "s_add_u32 s48, s48, %[cnt]\n\t" "73:\n\t" "s_sleep 10\n\t" "buffer_load_dword v30, off, %[rs], s48 sc1\n\t" \
   "s_add_u32 s47, s47, 1\n\t" "s_waitcnt vmcnt(0)\n\t" "v_add_u32 v30, 1, v30\n\t" "v_cmp_lt_u32 vcc, 7, v30\n\t" "s_cmp_gt_u32 s47, 0x20000\n\t" \
   "s_cbranch_scc1 72b\n\t" "s_cbranch_vccz 73b\n\t" "s_branch 72b\n\t" "174:\n\t" "v_cmp_lt_i32 vcc, 6, v146\n\t" "s_lshr_b32 s48, s45, 1\n\t" \
   "s_or_b32 s44, s44, vcc_lo\n\t" "s_cmp_lt_u32 s40, 4\n\t" "s_cbranch_scc1 274f\n\t" "buffer_load_dword v146, v19, %[rs], 0 offen sc1\n\t" "274:\n\t" \
   "s_bitcmp1_b32 s44, s48\n\t" "s_cbranch_scc1 74b\n\t" "s_mov_b32 s47, 0\n\t" "buffer_load_dword v30, v19, %[rs], 0 offen sc1\n\t" \
   "s_waitcnt vmcnt(0)\n\t" "v_add_u32 v30, 1, v30\n\t" "v_cmp_lt_u32 vcc, 7, v30\n\t" "s_or_b32 s44, s44, vcc_lo\n\t" "s_bitcmp1_b32 s44, s48\n\t" \
   "s_cbranch_scc1 74b\n\t" "s_bitset1_b32 s44, s48\n\t" "s_lshl_b32 s48, s48, 7\n\t" "s_add_u32 s48, s48, %[cnt]\n\t" "75:\n\t" "s_sleep 10\n\t" \
   "buffer_load_dword v30, off, %[rs], s48 sc1\n\t" "s_add_u32 s47, s47, 1\n\t" "s_waitcnt vmcnt(0)\n\t" "v_add_u32 v30, 1, v30\n\t" \
   "v_cmp_lt_u32 vcc, 7, v30\n\t" "s_cmp_gt_u32 s47, 0x20000\n\t" "s_cbranch_scc1 74b\n\t" "s_cbranch_vccz 75b\n\t" "s_branch 74b\n\t" "178:\n\t" \
   "v_cmp_lt_i32 vcc, 6, v146\n\t" "s_lshr_b32 s48, s45, 1\n\t" "s_or_b32 s44, s44, vcc_lo\n\t" "s_cmp_lt_u32 s40, 4\n\t" "s_cbranch_scc1 278f\n\t" \
   "buffer_load_dword v146, v19, %[rs], 0 offen sc1\n\t" "278:\n\t" "s_bitcmp1_b32 s44, s48\n\t" "s_cbranch_scc1 78b\n\t" "s_mov_b32 s47, 0\n\t" \
   "buffer_load_dword v30, v19, %[rs], 0 offen sc1\n\t" "s_waitcnt vmcnt(0)\n\t" "v_add_u32 v30, 1, v30\n\t" "v_cmp_lt_u32 vcc, 7, v30\n\t" \
   "s_or_b32 s44, s44, vcc_lo\n\t" "s_bitcmp1_b32 s44, s48\n\t" "s_cbranch_scc1 78b\n\t" "s_bitset1_b32 s44, s48\n\t" "s_lshl_b32 s48, s48, 7\n\t" \
   "s_add_u32 s48, s48, %[cnt]\n\t" "79:\n\t" "s_sleep 10\n\t" "buffer_load_dword v30, off, %[rs], s48 sc1\n\t" "s_add_u32 s47, s47, 1\n\t" \
   "s_waitcnt vmcnt(0)\n\t" "v_add_u32 v30, 1, v30\n\t" "v_cmp_lt_u32 vcc, 7, v30\n\t" "s_cmp_gt_u32 s47, 0x20000\n\t" "s_cbranch_scc1 78b\n\t" \
   "s_cbranch_vccz 79b\n\t" "s_branch 78b\n\t" "150:\n\t" "ds_read_b128 v[140:143], v12\n\t" "s_waitcnt lgkmcnt(0)\n\t" \
   "v_min3_u32 v140, v140, v141, v142\n\t" "v_min_u32 v140, v140, v143\n\t" "v_cmp_ge_u32 vcc, v140, v138\n\t" "s_cbranch_vccz 150b\n\t" \
   "s_branch 50b\n\t" "152:\n\t" "ds_read_b128 v[140:143], v12\n\t" "s_waitcnt lgkmcnt(0)\n\t" "v_min3_u32 v140, v140, v141, v142\n\t" \
   "v_min_u32 v140, v140, v143\n\t" "v_cmp_ge_u32 vcc, v140, v138\n\t" "s_cbranch_vccz 152b\n\t" "s_branch 52b\n\t" "151:\n\t" \
   "ds_read_b128 v[140:143], v12\n\t" "s_waitcnt lgkmcnt(0)\n\t" "v_min3_u32 v140, v140, v141, v142\n\t" "v_min_u32 v140, v140, v143\n\t" \
   "v_cmp_ge_u32 vcc, v140, v138\n\t" "s_cbranch_vccz 151b\n\t" "s_branch 51b\n\t" "153:\n\t" "ds_read_b128 v[140:143], v12\n\t" \
   "s_waitcnt lgkmcnt(0)\n\t" "v_min3_u32 v140, v140, v141, v142\n\t" "v_min_u32 v140, v140, v143\n\t" "v_cmp_ge_u32 vcc, v140, v138\n\t" \
   "s_cbranch_vccz 153b\n\t" "s_branch 53b\n\t" "9:\n\t" "s_waitcnt vmcnt(0)"
#define KQ_CLOB "v8", "v9", "v10", "v11", "v12", "v13", "v14", "v15", "v32", "v33", "v34", "v35", "v36", "v37", "v38", "v39", "v40", "v41", "v42", "v43", "v44", "v45", "v46", "v47", "v48", "v49", "v50", "v51", "v52", "v53", "v54", "v55", "v56", "v57", "v58", "v59", "v60", "v61", "v62", "v63", "v64", "v65", "v66", "v67", "v68", "v69", "v70", "v71", "v72", "v73", "v74", "v75", "v76", "v77", "v78", "v79", "v80", "v81", "v82", "v83", "v84", "v85", "v86", "v87", "v88", "v89", "v90", "v91", "v92", "v93", "v94", "v95", "v96", "v97", "v98", "v99", "v100", "v101", "v102", "v103", "v104", "v105", "v106", "v107", "v108", "v109", "v110", "v111", "v112", "v113", "v114", "v115", "v116", "v117", "v118", "v119", "v120", "v121", "v122", "v123", "v124", "v125", "v126", "v127", "v128", "v129", "v130", "v131", "v132", "v133", "v134", "v135", "v136", "v137", "v138", "v139", "v140", "v141", "v142", "v143", "v144", "v145", "v146", "v147", "v148", "v149", "v150", "v151", "v160", "v161", "v162", "v163", "v164", "v165", "v166", "v167", "v168", "v169", "v170", "v171", "v172", "v173", "v174", "v175", "v176", "v177", "v178", "v179", "v180", "v181", "v182", "v183", "v184", "v185", "v186", "v187", "v188", "v189", "v190", "v191", "s40", "s41", "s42", "s43", "s44", "s45", "s46", "s47", "s48", "s49", "s50", "s51", "s52", "s53", "s54", "s55", "s60", "s61"
#define VU_ASM \
   ".p2align 6\n\t" "s_nop 0\n\t" "s_nop 0\n\t" "s_nop 0\n\t" "s_nop 0\n\t" "s_nop 0\n\t" "s_nop 0\n\t" "s_nop 0\n\t" "s_mov_b32 s44, %[rdy]\n\t" \
   "s_mov_b32 s45, 0\n\t" "s_mov_b32 s46, 0\n\t" "s_cmp_eq_u32 s44, -1\n\t" "s_cbranch_scc1 68f\n\t" "s_mul_i32 s52, s45, 1536\n\t" \
   "s_mul_i32 s51, s45, 40960\n\t" "s_add_u32 m0, s46, %[ldx]\n\t" "s_mov_b32 s53, s46\n\t" \
   "buffer_load_dwordx4 v16, %[rs], s51 offen offset:0 sc1 lds\n\t" "buffer_load_dwordx4 v16, %[rs], s51 offen offset:1024 sc1 lds\n\t" \
   "buffer_load_dwordx4 v16, %[rs], s51 offen offset:2048 sc1 lds\n\t" "buffer_load_dwordx4 v16, %[rs], s51 offen offset:3072 sc1 lds\n\t" \
   "s_add_u32 m0, m0, 4096\n\t" "s_add_u32 s51, s51, 4096\n\t" "buffer_load_dwordx4 v16, %[rs], s51 offen offset:0 sc1 lds\n\t" \
   "buffer_load_dwordx4 v16, %[rs], s51 offen offset:1024 sc1 lds\n\t" "buffer_load_dwordx4 v16, %[rs], s51 offen offset:2048 sc1 lds\n\t" \
   "buffer_load_dwordx4 v16, %[rs], s51 offen offset:3072 sc1 lds\n\t" "s_add_u32 m0, m0, 4096\n\t" "s_add_u32 s51, s51, 4096\n\t" \
   "buffer_load_dwordx4 v16, %[rs], s51 offen offset:0 sc1 lds\n\t" "buffer_load_dwordx4 v16, %[rs], s51 offen offset:1024 sc1 lds\n\t" \
   "s_add_u32 m0, s53, %[ld1]\n\t" "s_nop 0\n\t" "buffer_load_dwordx4 v17, %[rs], s52 offen sc1 lds\n\t" "s_mov_b32 s45, 1\n\t" \
   "s_mov_b32 s46, 44032\n\t" "s_mul_i32 s52, s45, 1536\n\t" "s_mul_i32 s51, s45, 40960\n\t" "s_add_u32 m0, s46, %[ldx]\n\t" "s_mov_b32 s53, s46\n\t" \
   "buffer_load_dwordx4 v16, %[rs], s51 offen offset:0 sc1 lds\n\t" "buffer_load_dwordx4 v16, %[rs], s51 offen offset:1024 sc1 lds\n\t" \
   "buffer_load_dwordx4 v16, %[rs], s51 offen offset:2048 sc1 lds\n\t" "buffer_load_dwordx4 v16, %[rs], s51 offen offset:3072 sc1 lds\n\t" \
   "s_add_u32 m0, m0, 4096\n\t" "s_add_u32 s51, s51, 4096\n\t" "buffer_load_dwordx4 v16, %[rs], s51 offen offset:0 sc1 lds\n\t" \
   "buffer_load_dwordx4 v16, %[rs], s51 offen offset:1024 sc1 lds\n\t" "buffer_load_dwordx4 v16, %[rs], s51 offen offset:2048 sc1 lds\n\t" \
   "buffer_load_dwordx4 v16, %[rs], s51 offen offset:3072 sc1 lds\n\t" "s_add_u32 m0, m0, 4096\n\t" "s_add_u32 s51, s51, 4096\n\t" \
   "buffer_load_dwordx4 v16, %[rs], s51 offen offset:0 sc1 lds\n\t" "buffer_load_dwordx4 v16, %[rs], s51 offen offset:1024 sc1 lds\n\t" \
   "s_add_u32 m0, s53, %[ld1]\n\t" "s_nop 0\n\t" "buffer_load_dwordx4 v17, %[rs], s52 offen sc1 lds\n\t" "68:\n\t" "v_mov_b32 v138, 0\n\t" \
   "v_mov_b32 v13, %[flw]\n\t" "v_and_b32 v12, -16, v13\n\t" "ds_write_b32 v13, v138\n\t" "ds_read_b128 v[32:35], v26 offset:0\n\t" \
   "ds_read_b128 v[36:39], v26 offset:16\n\t" "ds_read_b128 v[40:43], v26 offset:32\n\t" "ds_read_b128 v[44:47], v26 offset:48\n\t" \
   "s_mov_b32 s40, 0\n\t" "s_mov_b32 s41, 0\n\t" "s_mov_b32 s42, 0\n\t" "s_mov_b32 s43, 44032\n\t" "v_mov_b32 v28, v22\n\t" "v_mov_b32 v29, v23\n\t" \
   "s_mov_b32 s45, 2\n\t" "s_mov_b32 s46, 88064\n\t" "s_waitcnt vmcnt(11) lgkmcnt(0)\n\t" "s_barrier\n\t" "v_mov_b32 v14, v24\n\t" \
   "v_mov_b32 v15, v25\n\t" "v_mov_b32 v25, -1\n\t" "s_cmp_eq_u32 s44, -1\n\t" "s_cbranch_scc0 172f\n\t" "72:\n\t" \
   "ds_read_b128 v[160:163], v28 offset:0\n\t" "ds_read_b128 v[164:167], v28 offset:512\n\t" "ds_read_b128 v[168:171], v28 offset:1024\n\t" \
   "ds_read_b128 v[172:175], v28 offset:1536\n\t" "ds_read_b128 v[176:179], v28 offset:2048\n\t" "ds_read_b128 v[180:183], v28 offset:2560\n\t" \
   "ds_read_b128 v[184:187], v28 offset:3072\n\t" "v_mov_b32 v18, 0x7ffffff0\n\t" "s_mov_b32 s49, 0\n\t" "5:\n\t" "v_mov_b32 v26, v28\n\t" \
   "v_mov_b32 v27, v29\n\t" "s_waitcnt lgkmcnt(5)\n\t" "v_mfma_f32_32x32x16_bf16 v[48:63], a[0:3], v[160:163], 0\n\t" \
   "ds_read_b128 v[188:191], v26 offset:3584\n\t" "s_mul_i32 s52, s45, 1536\n\t" "s_mul_i32 s51, s45, 40960\n\t" "s_add_u32 m0, s46, %[ldx]\n\t" \
   "s_mov_b32 s53, s46\n\t" "v_mfma_f32_32x32x16_bf16 v[48:63], a[4:7], v[164:167], v[48:63]\n\t" "ds_read_b128 v[160:163], v26 offset:4096\n\t" \
   "ds_read_b128 v[128:131], v27 offset:0\n\t" "ds_read_b128 v[132:135], v27 offset:16\n\t" "s_waitcnt lgkmcnt(7)\n\t" \
   "v_mfma_f32_32x32x16_bf16 v[48:63], a[8:11], v[168:171], v[48:63]\n\t" "ds_read_b128 v[164:167], v26 offset:4608\n\t" \
   "ds_read_b64 v[136:137], v27 offset:32\n\t" "v_add_u32 v28, s43, v22\n\t" "v_add_u32 v29, s43, v23\n\t" \
   "v_mfma_f32_32x32x16_bf16 v[48:63], a[12:15], v[172:175], v[48:63]\n\t" "ds_read_b128 v[168:171], v26 offset:5120\n\t" \
   "buffer_load_dwordx4 v16, %[rs], s51 offen offset:0 sc1 lds\n\t" "s_waitcnt lgkmcnt(8)\n\t" \
   "v_mfma_f32_32x32x16_bf16 v[48:63], a[16:19], v[176:179], v[48:63]\n\t" "ds_read_b128 v[172:175], v26 offset:5632\n\t" \
   "v_mfma_f32_32x32x16_bf16 v[48:63], a[20:23], v[180:183], v[48:63]\n\t" "ds_read_b128 v[176:179], v26 offset:6144\n\t" "s_waitcnt lgkmcnt(3)\n\t" \
   "v_add_f32 v128, v128, v130\n\t" "v_add_f32 v129, v129, v131\n\t" "v_add_f32 v132, v132, v134\n\t" \
   "v_mfma_f32_32x32x16_bf16 v[48:63], a[24:27], v[184:187], v[48:63]\n\t" "ds_read_b128 v[180:183], v26 offset:6656\n\t" \
   "buffer_load_dwordx4 v16, %[rs], s51 offen offset:1024 sc1 lds\n\t" "v_add_f32 v133, v133, v135\n\t" \
   "v_mfma_f32_32x32x16_bf16 v[48:63], a[28:31], v[188:191], v[48:63]\n\t" "ds_read_b128 v[184:187], v26 offset:7168\n\t" \
   "v_add_f32 v128, v128, v136\n\t" "v_add_f32 v129, v129, v137\n\t" "v_add_f32 v128, v128, v132\n\t" \
   "v_mfma_f32_32x32x16_bf16 v[48:63], a[32:35], v[160:163], v[48:63]\n\t" "ds_read_b128 v[188:191], v26 offset:7680\n\t" \
   "v_add_f32 v129, v129, v133\n\t" "v_mul_f32 v128, 0xbacccccd, v128\n\t" "v_mul_f32 v129, 0x3acccccd, v129\n\t" \
   "v_fma_f32 v129, -v128, v128, v129\n\t" "v_mfma_f32_32x32x16_bf16 v[48:63], a[36:39], v[164:167], v[48:63]\n\t" \
   "ds_read_b128 v[160:163], v26 offset:8192\n\t" "buffer_load_dwordx4 v16, %[rs], s51 offen offset:2048 sc1 lds\n\t" "s_waitcnt lgkmcnt(5)\n\t" \
   "v_mfma_f32_32x32x16_bf16 v[48:63], a[40:43], v[168:171], v[48:63]\n\t" "ds_read_b128 v[164:167], v26 offset:8704\n\t" "v_max_f32 v129, 0, v129\n\t" \
   "v_cvt_pk_bf16_f32 v129, v129, v128\n\t" "v_and_b32 v8, 0xffff0000, v129\n\t" "v_lshlrev_b32 v129, 16, v129\n\t" \
   "v_mfma_f32_32x32x16_bf16 v[48:63], a[44:47], v[172:175], v[48:63]\n\t" "ds_read_b128 v[168:171], v26 offset:9216\n\t" \
   "v_add_f32 v129, 0x3727c5ac, v129\n\t" "v_cvt_pk_bf16_f32 v129, v129, v129\n\t" "v_lshlrev_b32 v129, 16, v129\n\t" "s_waitcnt lgkmcnt(5)\n\t" \
   "v_mfma_f32_32x32x16_bf16 v[48:63], a[48:51], v[176:179], v[48:63]\n\t" "ds_read_b128 v[172:175], v26 offset:9728\n\t" "v_sqrt_f32 v129, v129\n\t" \
   "s_nop 0\n\t" "v_cvt_pk_bf16_f32 v129, v129, v129\n\t" "v_mfma_f32_32x32x16_bf16 v[48:63], a[52:55], v[180:183], v[48:63]\n\t" \
   "ds_read_b128 v[176:179], v26 offset:10240\n\t" "v_lshlrev_b32 v129, 16, v129\n\t" "v_rcp_f32 v10, v129\n\t" "s_waitcnt lgkmcnt(5)\n\t" \
   "v_mfma_f32_32x32x16_bf16 v[48:63], a[56:59], v[184:187], v[48:63]\n\t" "ds_read_b128 v[180:183], v26 offset:10752\n\t" \
   "buffer_load_dwordx4 v16, %[rs], s51 offen offset:3072 sc1 lds\n\t" "s_add_u32 m0, m0, 4096\n\t" "s_add_u32 s51, s51, 4096\n\t" \
   "v_mfma_f32_32x32x16_bf16 v[48:63], a[60:63], v[188:191], v[48:63]\n\t" "ds_read_b128 v[184:187], v26 offset:11264\n\t" \
   "buffer_load_dwordx4 v16, %[rs], s51 offen offset:0 sc1 lds\n\t" "s_waitcnt lgkmcnt(5)\n\t" \
   "v_mfma_f32_32x32x16_bf16 v[48:63], a[64:67], v[160:163], v[48:63]\n\t" "ds_read_b128 v[188:191], v26 offset:11776\n\t" \
   "v_mfma_f32_32x32x16_bf16 v[48:63], a[68:71], v[164:167], v[48:63]\n\t" "ds_read_b128 v[160:163], v26 offset:12288\n\t" "s_waitcnt lgkmcnt(5)\n\t" \
   "v_mfma_f32_32x32x16_bf16 v[48:63], a[72:75], v[168:171], v[48:63]\n\t" "ds_read_b128 v[164:167], v26 offset:12800\n\t" \
   "buffer_load_dwordx4 v16, %[rs], s51 offen offset:1024 sc1 lds\n\t" "v_mfma_f32_32x32x16_bf16 v[48:63], a[76:79], v[172:175], v[48:63]\n\t" \
   "ds_read_b128 v[168:171], v26 offset:13312\n\t" "s_waitcnt lgkmcnt(5)\n\t" "v_mfma_f32_32x32x16_bf16 v[48:63], a[80:83], v[176:179], v[48:63]\n\t" \
   "ds_read_b128 v[172:175], v26 offset:13824\n\t" "v_mfma_f32_32x32x16_bf16 v[48:63], a[84:87], v[180:183], v[48:63]\n\t" \
   "ds_read_b128 v[176:179], v26 offset:14336\n\t" "buffer_load_dwordx4 v16, %[rs], s51 offen offset:2048 sc1 lds\n\t" "s_waitcnt lgkmcnt(5)\n\t" \
   "v_mfma_f32_32x32x16_bf16 v[48:63], a[88:91], v[184:187], v[48:63]\n\t" "ds_read_b128 v[180:183], v26 offset:14848\n\t" \
   "v_mfma_f32_32x32x16_bf16 v[48:63], a[92:95], v[188:191], v[48:63]\n\t" "ds_read_b128 v[184:187], v26 offset:15360\n\t" "s_waitcnt lgkmcnt(5)\n\t" \
   "v_mfma_f32_32x32x16_bf16 v[48:63], a[96:99], v[160:163], v[48:63]\n\t" "ds_read_b128 v[188:191], v26 offset:15872\n\t" \
   "v_mfma_f32_32x32x16_bf16 v[48:63], a[100:103], v[164:167], v[48:63]\n\t" "ds_read_b128 v[160:163], v26 offset:16384\n\t" "s_waitcnt lgkmcnt(5)\n\t" \
   "v_mfma_f32_32x32x16_bf16 v[48:63], a[104:107], v[168:171], v[48:63]\n\t" "ds_read_b128 v[164:167], v26 offset:16896\n\t" \
   "buffer_load_dwordx4 v16, %[rs], s51 offen offset:3072 sc1 lds\n\t" "s_add_u32 m0, m0, 4096\n\t" "s_add_u32 s51, s51, 4096\n\t" \
   "v_mfma_f32_32x32x16_bf16 v[48:63], a[108:111], v[172:175], v[48:63]\n\t" "ds_read_b128 v[168:171], v26 offset:17408\n\t" \
   "buffer_load_dwordx4 v16, %[rs], s51 offen offset:0 sc1 lds\n\t" "s_waitcnt lgkmcnt(5)\n\t" \
   "v_mfma_f32_32x32x16_bf16 v[48:63], a[112:115], v[176:179], v[48:63]\n\t" "ds_read_b128 v[172:175], v26 offset:17920\n\t" "s_cmp_lt_i32 s40, 1\n\t" \
   "s_cselect_b32 s47, 0x7ffffff0, 0\n\t" "v_or_b32 v30, s47, v20\n\t" "s_sub_i32 s47, s41, 1\n\t" \
   "v_mfma_f32_32x32x16_bf16 v[48:63], a[116:119], v[180:183], v[48:63]\n\t" "ds_read_b128 v[176:179], v26 offset:18432\n\t" \
   "s_max_i32 s47, s47, 0\n\t" "s_lshl_b32 s47, s47, 12\n\t" "s_waitcnt vmcnt(9)\n\t" "s_waitcnt lgkmcnt(5)\n\t" \
   "v_mfma_f32_32x32x16_bf16 v[48:63], a[120:123], v[184:187], v[48:63]\n\t" "ds_read_b128 v[180:183], v26 offset:18944\n\t" \
   "buffer_store_dword v31, v30, %[rs], s47 offen sc1\n\t" "v_mfma_f32_32x32x16_bf16 v[48:63], a[124:127], v[188:191], v[48:63]\n\t" \
   "ds_read_b128 v[184:187], v26 offset:19456\n\t" "s_waitcnt lgkmcnt(5)\n\t" "v_mfma_f32_32x32x16_bf16 v[48:63], a[128:131], v[160:163], v[48:63]\n\t" \
   "ds_read_b128 v[188:191], v26 offset:19968\n\t" "buffer_load_dwordx4 v16, %[rs], s51 offen offset:1024 sc1 lds\n\t" "s_add_u32 m0, s53, %[ld1]\n\t" \
   "s_nop 0\n\t" "v_mfma_f32_32x32x16_bf16 v[48:63], a[132:135], v[164:167], v[48:63]\n\t" "buffer_load_dwordx4 v17, %[rs], s52 offen sc1 lds\n\t" \
   "s_waitcnt lgkmcnt(4)\n\t" "v_mfma_f32_32x32x16_bf16 v[48:63], a[136:139], v[168:171], v[48:63]\n\t" \
   "v_mfma_f32_32x32x16_bf16 v[48:63], a[140:143], v[172:175], v[48:63]\n\t" "s_waitcnt lgkmcnt(2)\n\t" \
   "v_mfma_f32_32x32x16_bf16 v[48:63], a[144:147], v[176:179], v[48:63]\n\t" "v_mfma_f32_32x32x16_bf16 v[48:63], a[148:151], v[180:183], v[48:63]\n\t" \
   "s_waitcnt lgkmcnt(0)\n\t" "v_mfma_f32_32x32x16_bf16 v[48:63], a[152:155], v[184:187], v[48:63]\n\t" \
   "v_mfma_f32_32x32x16_bf16 v[48:63], a[156:159], v[188:191], v[48:63]\n\t" "s_memrealtime s[54:55]\n\t" "s_nop 7\n\t" "s_nop 2\n\t" \
   "v_fmac_f32 v48, v32, v8\n\t" "v_fmac_f32 v49, v33, v8\n\t" "v_fmac_f32 v50, v34, v8\n\t" "v_fmac_f32 v51, v35, v8\n\t" \
   "v_fmac_f32 v52, v36, v8\n\t" "v_fmac_f32 v53, v37, v8\n\t" "v_fmac_f32 v54, v38, v8\n\t" "v_fmac_f32 v55, v39, v8\n\t" \
   "v_fmac_f32 v56, v40, v8\n\t" "v_fmac_f32 v57, v41, v8\n\t" "v_fmac_f32 v58, v42, v8\n\t" "v_fmac_f32 v59, v43, v8\n\t" \
   "v_fmac_f32 v60, v44, v8\n\t" "v_fmac_f32 v61, v45, v8\n\t" "v_fmac_f32 v62, v46, v8\n\t" "v_fmac_f32 v63, v47, v8\n\t" \
   "v_mul_f32 v48, v48, v10\n\t" "v_mul_f32 v49, v49, v10\n\t" "v_mul_f32 v50, v50, v10\n\t" "v_mul_f32 v51, v51, v10\n\t" \
   "v_mul_f32 v52, v52, v10\n\t" "v_mul_f32 v53, v53, v10\n\t" "v_mul_f32 v54, v54, v10\n\t" "v_mul_f32 v55, v55, v10\n\t" \
   "v_mul_f32 v56, v56, v10\n\t" "v_mul_f32 v57, v57, v10\n\t" "v_mul_f32 v58, v58, v10\n\t" "v_mul_f32 v59, v59, v10\n\t" \
   "v_mul_f32 v60, v60, v10\n\t" "v_mul_f32 v61, v61, v10\n\t" "v_mul_f32 v62, v62, v10\n\t" "v_mul_f32 v63, v63, v10\n\t" \
   "v_cvt_pk_bf16_f32 v48, v48, v49\n\t" "v_cvt_pk_bf16_f32 v49, v50, v51\n\t" "v_cvt_pk_bf16_f32 v50, v52, v53\n\t" \
   "v_cvt_pk_bf16_f32 v51, v54, v55\n\t" "ds_write_b128 v14, v[48:51] offset:0\n\t" "v_cvt_pk_bf16_f32 v56, v56, v57\n\t" \
   "v_cvt_pk_bf16_f32 v57, v58, v59\n\t" "v_cvt_pk_bf16_f32 v58, v60, v61\n\t" "v_cvt_pk_bf16_f32 v59, v62, v63\n\t" \
   "ds_write_b128 v14, v[56:59] offset:1024\n\t" "s_waitcnt vmcnt(12) lgkmcnt(0)\n\t" "s_barrier\n\t" "s_mov_b32 s42, s43\n\t" \
   "s_add_u32 s43, s43, 44032\n\t" "s_cmp_eq_u32 s43, 132096\n\t" "s_cselect_b32 s43, 0, s43\n\t" "s_add_u32 s41, s41, 1\n\t" \
   "s_add_u32 s40, s40, 1\n\t" "v_xor_b32 v14, 0x2000, v14\n\t" "v_xor_b32 v15, 0x2000, v15\n\t" "s_cmp_eq_u32 s40, 1\n\t" "s_cbranch_scc0 6f\n\t" \
   "ds_read_b128 v[96:99], v15 offset:0\n\t" "ds_read_b128 v[100:103], v15 offset:1024\n\t" "ds_read_b128 v[104:107], v15 offset:2048\n\t" \
   "ds_read_b128 v[108:111], v15 offset:3072\n\t" "ds_read_b128 v[112:115], v15 offset:4096\n\t" "ds_read_b128 v[116:119], v15 offset:5120\n\t" \
   "ds_read_b128 v[120:123], v15 offset:6144\n\t" "ds_read_b128 v[124:127], v15 offset:7168\n\t" "s_sub_i32 s49, s41, 1\n\t" \
   "s_mul_i32 s49, s49, 1310720\n\t" "v_mov_b32 v31, s54\n\t" "v_min_u32 v31, -2, v31\n\t" "s_mov_b32 s45, 3\n\t" "s_mov_b32 s46, 0\n\t" \
   "s_waitcnt lgkmcnt(6)\n\t" "v_mfma_f32_32x32x16_bf16 v[64:79], v[96:99], a[160:163], 0\n\t" \
   "v_mfma_f32_32x32x16_bf16 v[64:79], v[100:103], a[176:179], v[64:79]\n\t" "s_waitcnt lgkmcnt(4)\n\t" \
   "v_mfma_f32_32x32x16_bf16 v[64:79], v[104:107], a[164:167], v[64:79]\n\t" "v_mfma_f32_32x32x16_bf16 v[64:79], v[108:111], a[180:183], v[64:79]\n\t" \
   "s_waitcnt lgkmcnt(2)\n\t" "v_mfma_f32_32x32x16_bf16 v[64:79], v[112:115], a[168:171], v[64:79]\n\t" \
   "v_mfma_f32_32x32x16_bf16 v[64:79], v[116:119], a[184:187], v[64:79]\n\t" "s_waitcnt lgkmcnt(0)\n\t" \
   "v_mfma_f32_32x32x16_bf16 v[64:79], v[120:123], a[172:175], v[64:79]\n\t" "v_mfma_f32_32x32x16_bf16 v[64:79], v[124:127], a[188:191], v[64:79]\n\t" \
   "v_mfma_f32_32x32x16_bf16 v[80:95], v[96:99], a[192:195], 0\n\t" "v_mfma_f32_32x32x16_bf16 v[80:95], v[100:103], a[208:211], v[80:95]\n\t" \
   "s_add_u32 s50, s49, 0\n\t" "v_cvt_pk_bf16_f32 v64, v64, v65\n\t" "v_cvt_pk_bf16_f32 v65, v72, v73\n\t" "v_cvt_pk_bf16_f32 v66, v66, v67\n\t" \
   "v_cvt_pk_bf16_f32 v67, v74, v75\n\t" "v_mfma_f32_32x32x16_bf16 v[80:95], v[104:107], a[196:199], v[80:95]\n\t" \
   "buffer_store_dwordx4 v[64:67], v21, %[rs], s50 offen offset:0 sc1\n\t" "v_cvt_pk_bf16_f32 v68, v68, v69\n\t" \
   "v_mfma_f32_32x32x16_bf16 v[80:95], v[108:111], a[212:215], v[80:95]\n\t" "v_cvt_pk_bf16_f32 v69, v76, v77\n\t" \
   "v_cvt_pk_bf16_f32 v70, v70, v71\n\t" "v_cvt_pk_bf16_f32 v71, v78, v79\n\t" \
   "v_mfma_f32_32x32x16_bf16 v[80:95], v[112:115], a[200:203], v[80:95]\n\t" "buffer_store_dwordx4 v[68:71], v21, %[rs], s50 offen offset:512 sc1\n\t" \
   "v_mfma_f32_32x32x16_bf16 v[80:95], v[116:119], a[216:219], v[80:95]\n\t" "v_mfma_f32_32x32x16_bf16 v[80:95], v[120:123], a[204:207], v[80:95]\n\t" \
   "v_mfma_f32_32x32x16_bf16 v[80:95], v[124:127], a[220:223], v[80:95]\n\t" "v_mfma_f32_32x32x16_bf16 v[64:79], v[96:99], a[224:227], 0\n\t" \
   "v_mfma_f32_32x32x16_bf16 v[64:79], v[100:103], a[240:243], v[64:79]\n\t" "s_add_u32 s50, s49, 65536\n\t" "v_cvt_pk_bf16_f32 v80, v80, v81\n\t" \
   "v_cvt_pk_bf16_f32 v81, v88, v89\n\t" "v_cvt_pk_bf16_f32 v82, v82, v83\n\t" "v_cvt_pk_bf16_f32 v83, v90, v91\n\t" \
   "v_mfma_f32_32x32x16_bf16 v[64:79], v[104:107], a[228:231], v[64:79]\n\t" "buffer_store_dwordx4 v[80:83], v21, %[rs], s50 offen offset:0 sc1\n\t" \
   "v_cvt_pk_bf16_f32 v84, v84, v85\n\t" "v_mfma_f32_32x32x16_bf16 v[64:79], v[108:111], a[244:247], v[64:79]\n\t" \
   "v_cvt_pk_bf16_f32 v85, v92, v93\n\t" "v_cvt_pk_bf16_f32 v86, v86, v87\n\t" "v_cvt_pk_bf16_f32 v87, v94, v95\n\t" \
   "v_mfma_f32_32x32x16_bf16 v[64:79], v[112:115], a[232:235], v[64:79]\n\t" "buffer_store_dwordx4 v[84:87], v21, %[rs], s50 offen offset:512 sc1\n\t" \
   "v_mfma_f32_32x32x16_bf16 v[64:79], v[116:119], a[248:251], v[64:79]\n\t" "ds_read_b128 v[160:163], v28 offset:0\n\t" \
   "ds_read_b128 v[164:167], v28 offset:512\n\t" "ds_read_b128 v[168:171], v28 offset:1024\n\t" "ds_read_b128 v[172:175], v28 offset:1536\n\t" \
   "v_mfma_f32_32x32x16_bf16 v[64:79], v[120:123], a[236:239], v[64:79]\n\t" "ds_read_b128 v[176:179], v28 offset:2048\n\t" \
   "ds_read_b128 v[180:183], v28 offset:2560\n\t" "ds_read_b128 v[184:187], v28 offset:3072\n\t" \
   "v_mfma_f32_32x32x16_bf16 v[64:79], v[124:127], a[252:255], v[64:79]\n\t" "v_mfma_f32_32x32x16_bf16 v[80:95], v[96:99], v[192:195], 0\n\t" \
   "v_mfma_f32_32x32x16_bf16 v[80:95], v[100:103], v[208:211], v[80:95]\n\t" "s_add_u32 s50, s49, 131072\n\t" "v_cvt_pk_bf16_f32 v64, v64, v65\n\t" \
   "v_cvt_pk_bf16_f32 v65, v72, v73\n\t" "v_cvt_pk_bf16_f32 v66, v66, v67\n\t" "v_cvt_pk_bf16_f32 v67, v74, v75\n\t" \
   "v_mfma_f32_32x32x16_bf16 v[80:95], v[104:107], v[196:199], v[80:95]\n\t" "buffer_store_dwordx4 v[64:67], v21, %[rs], s50 offen offset:0 sc1\n\t" \
   "v_cvt_pk_bf16_f32 v68, v68, v69\n\t" "v_mfma_f32_32x32x16_bf16 v[80:95], v[108:111], v[212:215], v[80:95]\n\t" \
   "v_cvt_pk_bf16_f32 v69, v76, v77\n\t" "v_cvt_pk_bf16_f32 v70, v70, v71\n\t" "v_cvt_pk_bf16_f32 v71, v78, v79\n\t" \
   "v_mfma_f32_32x32x16_bf16 v[80:95], v[112:115], v[200:203], v[80:95]\n\t" "buffer_store_dwordx4 v[68:71], v21, %[rs], s50 offen offset:512 sc1\n\t" \
   "v_mfma_f32_32x32x16_bf16 v[80:95], v[116:119], v[216:219], v[80:95]\n\t" "v_mfma_f32_32x32x16_bf16 v[80:95], v[120:123], v[204:207], v[80:95]\n\t" \
   "v_mfma_f32_32x32x16_bf16 v[80:95], v[124:127], v[220:223], v[80:95]\n\t" "v_mfma_f32_32x32x16_bf16 v[140:155], v[96:99], v[224:227], 0\n\t" \
   "v_mfma_f32_32x32x16_bf16 v[140:155], v[100:103], v[240:243], v[140:155]\n\t" "s_add_u32 s50, s49, 196608\n\t" "v_cvt_pk_bf16_f32 v80, v80, v81\n\t" \
   "v_cvt_pk_bf16_f32 v81, v88, v89\n\t" "v_cvt_pk_bf16_f32 v82, v82, v83\n\t" "v_cvt_pk_bf16_f32 v83, v90, v91\n\t" \
   "v_mfma_f32_32x32x16_bf16 v[140:155], v[104:107], v[228:231], v[140:155]\n\t" \
   "buffer_store_dwordx4 v[80:83], v21, %[rs], s50 offen offset:0 sc1\n\t" "v_cvt_pk_bf16_f32 v84, v84, v85\n\t" \
   "v_mfma_f32_32x32x16_bf16 v[140:155], v[108:111], v[244:247], v[140:155]\n\t" "v_cvt_pk_bf16_f32 v85, v92, v93\n\t" \
   "v_cvt_pk_bf16_f32 v86, v86, v87\n\t" "v_cvt_pk_bf16_f32 v87, v94, v95\n\t" \
   "v_mfma_f32_32x32x16_bf16 v[140:155], v[112:115], v[232:235], v[140:155]\n\t" \
   "buffer_store_dwordx4 v[84:87], v21, %[rs], s50 offen offset:512 sc1\n\t" \
   "v_mfma_f32_32x32x16_bf16 v[140:155], v[116:119], v[248:251], v[140:155]\n\t" \
   "v_mfma_f32_32x32x16_bf16 v[140:155], v[120:123], v[236:239], v[140:155]\n\t" \
   "v_mfma_f32_32x32x16_bf16 v[140:155], v[124:127], v[252:255], v[140:155]\n\t" "s_nop 7\n\t" "s_nop 2\n\t" "s_add_u32 s50, s49, 262144\n\t" \
   "v_cvt_pk_bf16_f32 v140, v140, v141\n\t" "v_cvt_pk_bf16_f32 v141, v148, v149\n\t" "v_cvt_pk_bf16_f32 v142, v142, v143\n\t" \
   "v_cvt_pk_bf16_f32 v143, v150, v151\n\t" "buffer_store_dwordx4 v[140:143], v21, %[rs], s50 offen offset:0 sc1\n\t" \
   "v_cvt_pk_bf16_f32 v144, v144, v145\n\t" "v_cvt_pk_bf16_f32 v145, v152, v153\n\t" "v_cvt_pk_bf16_f32 v146, v146, v147\n\t" \
   "v_cvt_pk_bf16_f32 v147, v154, v155\n\t" "buffer_store_dwordx4 v[144:147], v21, %[rs], s50 offen offset:512 sc1\n\t" "s_branch 5b\n\t" "6:\n\t" \
   "ds_read_b128 v[160:163], v28 offset:0\n\t" "ds_read_b128 v[164:167], v28 offset:512\n\t" "ds_read_b128 v[168:171], v28 offset:1024\n\t" \
   "ds_read_b128 v[172:175], v28 offset:1536\n\t" "ds_read_b128 v[176:179], v28 offset:2048\n\t" "ds_read_b128 v[180:183], v28 offset:2560\n\t" \
   "ds_read_b128 v[184:187], v28 offset:3072\n\t" "s_mov_b32 s45, 4\n\t" "s_mov_b32 s46, 44032\n\t" "s_cmp_eq_u32 s44, -1\n\t" \
   "s_cbranch_scc0 176f\n\t" "76:\n\t" "1:\n\t" "v_mov_b32 v26, v28\n\t" "v_mov_b32 v27, v29\n\t" "s_waitcnt lgkmcnt(5)\n\t" \
   "v_mfma_f32_32x32x16_bf16 v[48:63], a[0:3], v[160:163], 0\n\t" "ds_read_b128 v[188:191], v26 offset:3584\n\t" "ds_read_b128 v[156:159], v12\n\t" \
   "v_mfma_f32_32x32x16_bf16 v[48:63], a[4:7], v[164:167], v[48:63]\n\t" "ds_read_b128 v[160:163], v26 offset:4096\n\t" \
   "s_add_u32 s50, s49, 262144\n\t" "v_cvt_pk_bf16_f32 v140, v140, v141\n\t" "v_cvt_pk_bf16_f32 v141, v148, v149\n\t" "s_waitcnt lgkmcnt(6)\n\t" \
   "v_mfma_f32_32x32x16_bf16 v[48:63], a[8:11], v[168:171], v[48:63]\n\t" "ds_read_b128 v[164:167], v26 offset:4608\n\t" "s_waitcnt lgkmcnt(2)\n\t" \
   "v_min3_u32 v156, v156, v157, v158\n\t" "v_min_u32 v156, v156, v159\n\t" "v_cmp_ge_u32 vcc, v156, v138\n\t" \
   "v_mfma_f32_32x32x16_bf16 v[48:63], a[12:15], v[172:175], v[48:63]\n\t" "ds_read_b128 v[168:171], v26 offset:5120\n\t" "s_cbranch_vccz 150f\n\t" \
   "50:\n\t" "ds_read_b128 v[96:99], v15 offset:0\n\t" "v_mfma_f32_32x32x16_bf16 v[48:63], a[16:19], v[176:179], v[48:63]\n\t" \
   "ds_read_b128 v[172:175], v26 offset:5632\n\t" "ds_read_b128 v[100:103], v15 offset:1024\n\t" "ds_read_b128 v[104:107], v15 offset:2048\n\t" \
   "ds_read_b128 v[108:111], v15 offset:3072\n\t" "v_mfma_f32_32x32x16_bf16 v[48:63], a[20:23], v[180:183], v[48:63]\n\t" \
   "ds_read_b128 v[176:179], v26 offset:6144\n\t" "ds_read_b128 v[112:115], v15 offset:4096\n\t" "ds_read_b128 v[116:119], v15 offset:5120\n\t" \
   "v_mfma_f32_32x32x16_bf16 v[48:63], a[24:27], v[184:187], v[48:63]\n\t" "ds_read_b128 v[180:183], v26 offset:6656\n\t" \
   "ds_read_b128 v[120:123], v15 offset:6144\n\t" "ds_read_b128 v[124:127], v15 offset:7168\n\t" "s_mul_i32 s52, s45, 1536\n\t" \
   "s_mul_i32 s51, s45, 40960\n\t" "s_add_u32 m0, s46, %[ldx]\n\t" "s_mov_b32 s53, s46\n\t" \
   "v_mfma_f32_32x32x16_bf16 v[48:63], a[28:31], v[188:191], v[48:63]\n\t" "ds_read_b128 v[184:187], v26 offset:7168\n\t" \
   "v_cvt_pk_bf16_f32 v142, v142, v143\n\t" "v_cvt_pk_bf16_f32 v143, v150, v151\n\t" "s_waitcnt lgkmcnt(9)\n\t" \
   "v_mfma_f32_32x32x16_bf16 v[64:79], v[96:99], a[160:163], 0\n\t" "buffer_store_dwordx4 v[140:143], v18, %[rs], s50 offen offset:0 sc1\n\t" \
   "v_mfma_f32_32x32x16_bf16 v[48:63], a[32:35], v[160:163], v[48:63]\n\t" "ds_read_b128 v[188:191], v26 offset:7680\n\t" \
   "v_cvt_pk_bf16_f32 v144, v144, v145\n\t" "v_cvt_pk_bf16_f32 v145, v152, v153\n\t" "v_cvt_pk_bf16_f32 v146, v146, v147\n\t" \
   "v_cvt_pk_bf16_f32 v147, v154, v155\n\t" "v_mfma_f32_32x32x16_bf16 v[48:63], a[36:39], v[164:167], v[48:63]\n\t" \
   "ds_read_b128 v[160:163], v26 offset:8192\n\t" "v_add_u32 v28, s43, v22\n\t" "v_add_u32 v29, s43, v23\n\t" \
   "v_mfma_f32_32x32x16_bf16 v[64:79], v[100:103], a[176:179], v[64:79]\n\t" \
   "buffer_store_dwordx4 v[144:147], v18, %[rs], s50 offen offset:512 sc1\n\t" "s_sub_i32 s49, s41, 1\n\t" "s_mul_i32 s49, s49, 1310720\n\t" \
   "v_mfma_f32_32x32x16_bf16 v[48:63], a[40:43], v[168:171], v[48:63]\n\t" "ds_read_b128 v[164:167], v26 offset:8704\n\t" \
   "ds_read_b128 v[128:131], v27 offset:0\n\t" "buffer_load_dwordx4 v16, %[rs], s51 offen offset:0 sc1 lds\n\t" \
   "v_mfma_f32_32x32x16_bf16 v[48:63], a[44:47], v[172:175], v[48:63]\n\t" "ds_read_b128 v[168:171], v26 offset:9216\n\t" \
   "ds_read_b128 v[132:135], v27 offset:16\n\t" "ds_read_b64 v[136:137], v27 offset:32\n\t" "s_waitcnt lgkmcnt(14)\n\t" \
   "v_mfma_f32_32x32x16_bf16 v[64:79], v[104:107], a[164:167], v[64:79]\n\t" "s_waitcnt lgkmcnt(10)\n\t" \
   "v_mfma_f32_32x32x16_bf16 v[48:63], a[48:51], v[176:179], v[48:63]\n\t" "ds_read_b128 v[172:175], v26 offset:9728\n\t" \
   "v_mfma_f32_32x32x16_bf16 v[48:63], a[52:55], v[180:183], v[48:63]\n\t" "ds_read_b128 v[176:179], v26 offset:10240\n\t" \
   "buffer_load_dwordx4 v16, %[rs], s51 offen offset:1024 sc1 lds\n\t" "v_mfma_f32_32x32x16_bf16 v[64:79], v[108:111], a[180:183], v[64:79]\n\t" \
   "s_waitcnt lgkmcnt(2)\n\t" "v_add_f32 v128, v128, v130\n\t" "v_add_f32 v129, v129, v131\n\t" "v_add_f32 v132, v132, v134\n\t" \
   "v_add_f32 v133, v133, v135\n\t" "v_mfma_f32_32x32x16_bf16 v[48:63], a[56:59], v[184:187], v[48:63]\n\t" \
   "ds_read_b128 v[180:183], v26 offset:10752\n\t" "v_add_f32 v128, v128, v136\n\t" "v_add_f32 v129, v129, v137\n\t" "v_add_f32 v128, v128, v132\n\t" \
   "v_add_f32 v129, v129, v133\n\t" "v_mfma_f32_32x32x16_bf16 v[48:63], a[60:63], v[188:191], v[48:63]\n\t" \
   "ds_read_b128 v[184:187], v26 offset:11264\n\t" "v_mul_f32 v128, 0xbacccccd, v128\n\t" "v_mul_f32 v129, 0x3acccccd, v129\n\t" \
   "v_fma_f32 v129, -v128, v128, v129\n\t" "v_max_f32 v129, 0, v129\n\t" "v_mfma_f32_32x32x16_bf16 v[64:79], v[112:115], a[168:171], v[64:79]\n\t" \
   "buffer_load_dwordx4 v16, %[rs], s51 offen offset:2048 sc1 lds\n\t" "v_cvt_pk_bf16_f32 v129, v129, v128\n\t" "v_and_b32 v8, 0xffff0000, v129\n\t" \
   "v_mfma_f32_32x32x16_bf16 v[48:63], a[64:67], v[160:163], v[48:63]\n\t" "ds_read_b128 v[188:191], v26 offset:11776\n\t" \
   "v_lshlrev_b32 v129, 16, v129\n\t" "v_add_f32 v129, 0x3727c5ac, v129\n\t" "v_cvt_pk_bf16_f32 v129, v129, v129\n\t" \
   "v_lshlrev_b32 v129, 16, v129\n\t" "v_mfma_f32_32x32x16_bf16 v[48:63], a[68:71], v[164:167], v[48:63]\n\t" \
   "ds_read_b128 v[160:163], v26 offset:12288\n\t" "v_sqrt_f32 v129, v129\n\t" "s_nop 0\n\t" "v_cvt_pk_bf16_f32 v129, v129, v129\n\t" \
   "v_mfma_f32_32x32x16_bf16 v[64:79], v[116:119], a[184:187], v[64:79]\n\t" "v_lshlrev_b32 v129, 16, v129\n\t" "v_rcp_f32 v10, v129\n\t" \
   "s_waitcnt lgkmcnt(5)\n\t" "v_mfma_f32_32x32x16_bf16 v[48:63], a[72:75], v[168:171], v[48:63]\n\t" "ds_read_b128 v[164:167], v26 offset:12800\n\t" \
   "v_mfma_f32_32x32x16_bf16 v[48:63], a[76:79], v[172:175], v[48:63]\n\t" "ds_read_b128 v[168:171], v26 offset:13312\n\t" \
   "v_mfma_f32_32x32x16_bf16 v[64:79], v[120:123], a[172:175], v[64:79]\n\t" "buffer_load_dwordx4 v16, %[rs], s51 offen offset:3072 sc1 lds\n\t" \
   "s_add_u32 m0, m0, 4096\n\t" "s_add_u32 s51, s51, 4096\n\t" "s_waitcnt lgkmcnt(5)\n\t" \
   "v_mfma_f32_32x32x16_bf16 v[48:63], a[80:83], v[176:179], v[48:63]\n\t" "ds_read_b128 v[172:175], v26 offset:13824\n\t" \
   "v_mfma_f32_32x32x16_bf16 v[48:63], a[84:87], v[180:183], v[48:63]\n\t" "ds_read_b128 v[176:179], v26 offset:14336\n\t" \
   "buffer_load_dwordx4 v16, %[rs], s51 offen offset:0 sc1 lds\n\t" "v_mfma_f32_32x32x16_bf16 v[64:79], v[124:127], a[188:191], v[64:79]\n\t" \
   "s_waitcnt lgkmcnt(5)\n\t" "v_mfma_f32_32x32x16_bf16 v[48:63], a[88:91], v[184:187], v[48:63]\n\t" "ds_read_b128 v[180:183], v26 offset:14848\n\t" \
   "v_mfma_f32_32x32x16_bf16 v[48:63], a[92:95], v[188:191], v[48:63]\n\t" "ds_read_b128 v[184:187], v26 offset:15360\n\t" "s_add_u32 s50, s49, 0\n\t" \
   "v_cvt_pk_bf16_f32 v64, v64, v65\n\t" "v_cvt_pk_bf16_f32 v65, v72, v73\n\t" "v_cvt_pk_bf16_f32 v66, v66, v67\n\t" \
   "v_mfma_f32_32x32x16_bf16 v[80:95], v[96:99], a[192:195], 0\n\t" "v_cvt_pk_bf16_f32 v67, v74, v75\n\t" \
   "buffer_load_dwordx4 v16, %[rs], s51 offen offset:1024 sc1 lds\n\t" "s_waitcnt lgkmcnt(5)\n\t" \
   "v_mfma_f32_32x32x16_bf16 v[48:63], a[96:99], v[160:163], v[48:63]\n\t" "ds_read_b128 v[188:191], v26 offset:15872\n\t" \
   "v_mfma_f32_32x32x16_bf16 v[48:63], a[100:103], v[164:167], v[48:63]\n\t" "ds_read_b128 v[160:163], v26 offset:16384\n\t" \
   "v_mfma_f32_32x32x16_bf16 v[80:95], v[100:103], a[208:211], v[80:95]\n\t" "buffer_store_dwordx4 v[64:67], v21, %[rs], s50 offen offset:0 sc1\n\t" \
   "s_waitcnt lgkmcnt(5)\n\t" "v_mfma_f32_32x32x16_bf16 v[48:63], a[104:107], v[168:171], v[48:63]\n\t" "ds_read_b128 v[164:167], v26 offset:16896\n\t" \
   "v_cvt_pk_bf16_f32 v68, v68, v69\n\t" "v_cvt_pk_bf16_f32 v69, v76, v77\n\t" "v_cvt_pk_bf16_f32 v70, v70, v71\n\t" \
   "v_cvt_pk_bf16_f32 v71, v78, v79\n\t" "v_mfma_f32_32x32x16_bf16 v[48:63], a[108:111], v[172:175], v[48:63]\n\t" \
   "ds_read_b128 v[168:171], v26 offset:17408\n\t" "s_waitcnt vmcnt(9)\n\t" "v_add_u32 v138, 1, v138\n\t" "ds_write_b32 v13, v138\n\t" \
   "v_mfma_f32_32x32x16_bf16 v[80:95], v[104:107], a[196:199], v[80:95]\n\t" "buffer_store_dwordx4 v[68:71], v21, %[rs], s50 offen offset:512 sc1\n\t" \
   "s_waitcnt lgkmcnt(6)\n\t" "v_mfma_f32_32x32x16_bf16 v[48:63], a[112:115], v[176:179], v[48:63]\n\t" "ds_read_b128 v[172:175], v26 offset:17920\n\t" \
   "buffer_load_dwordx4 v16, %[rs], s51 offen offset:2048 sc1 lds\n\t" "v_mfma_f32_32x32x16_bf16 v[48:63], a[116:119], v[180:183], v[48:63]\n\t" \
   "ds_read_b128 v[176:179], v26 offset:18432\n\t" "v_mfma_f32_32x32x16_bf16 v[80:95], v[108:111], a[212:215], v[80:95]\n\t" \
   "buffer_load_dwordx4 v16, %[rs], s51 offen offset:3072 sc1 lds\n\t" "s_add_u32 m0, m0, 4096\n\t" "s_add_u32 s51, s51, 4096\n\t" \
   "s_waitcnt lgkmcnt(6)\n\t" "v_mfma_f32_32x32x16_bf16 v[48:63], a[120:123], v[184:187], v[48:63]\n\t" "ds_read_b128 v[180:183], v26 offset:18944\n\t" \
   "v_mfma_f32_32x32x16_bf16 v[48:63], a[124:127], v[188:191], v[48:63]\n\t" "ds_read_b128 v[184:187], v26 offset:19456\n\t" \
   "v_mfma_f32_32x32x16_bf16 v[80:95], v[112:115], a[200:203], v[80:95]\n\t" "buffer_load_dwordx4 v16, %[rs], s51 offen offset:0 sc1 lds\n\t" \
   "s_waitcnt lgkmcnt(6)\n\t" "v_mfma_f32_32x32x16_bf16 v[48:63], a[128:131], v[160:163], v[48:63]\n\t" "ds_read_b128 v[188:191], v26 offset:19968\n\t" \
   "v_mfma_f32_32x32x16_bf16 v[48:63], a[132:135], v[164:167], v[48:63]\n\t" "v_mfma_f32_32x32x16_bf16 v[80:95], v[116:119], a[216:219], v[80:95]\n\t" \
   "s_waitcnt lgkmcnt(4)\n\t" "v_mfma_f32_32x32x16_bf16 v[48:63], a[136:139], v[168:171], v[48:63]\n\t" "s_cmp_lt_i32 s40, 2\n\t" \
   "s_cselect_b32 s47, 0x7ffffff0, 0\n\t" "v_or_b32 v30, s47, v20\n\t" "s_sub_i32 s47, s41, 2\n\t" "s_max_i32 s47, s47, 0\n\t" \
   "s_lshl_b32 s47, s47, 12\n\t" "v_mfma_f32_32x32x16_bf16 v[48:63], a[140:143], v[172:175], v[48:63]\n\t" "s_waitcnt vmcnt(11)\n\t" \
   "buffer_load_dwordx4 v16, %[rs], s51 offen offset:1024 sc1 lds\n\t" "s_add_u32 m0, s53, %[ld1]\n\t" "s_nop 0\n\t" \
   "v_mfma_f32_32x32x16_bf16 v[80:95], v[120:123], a[204:207], v[80:95]\n\t" "buffer_store_dword v31, v30, %[rs], s47 offen sc1\n\t" \
   "s_waitcnt lgkmcnt(2)\n\t" "v_mfma_f32_32x32x16_bf16 v[48:63], a[144:147], v[176:179], v[48:63]\n\t" "ds_read_b128 v[156:159], v12\n\t" \
   "v_mfma_f32_32x32x16_bf16 v[48:63], a[148:151], v[180:183], v[48:63]\n\t" "buffer_load_dwordx4 v17, %[rs], s52 offen sc1 lds\n\t" \
   "v_mfma_f32_32x32x16_bf16 v[80:95], v[124:127], a[220:223], v[80:95]\n\t" "s_waitcnt lgkmcnt(1)\n\t" \
   "v_mfma_f32_32x32x16_bf16 v[48:63], a[152:155], v[184:187], v[48:63]\n\t" "s_waitcnt lgkmcnt(0)\n\t" "v_min3_u32 v156, v156, v157, v158\n\t" \
   "v_min_u32 v156, v156, v159\n\t" "v_cmp_ge_u32 vcc, v156, v138\n\t" "s_cbranch_vccz 152f\n\t" "52:\n\t" \
   "v_mfma_f32_32x32x16_bf16 v[48:63], a[156:159], v[188:191], v[48:63]\n\t" "s_add_u32 s50, s49, 65536\n\t" "v_cvt_pk_bf16_f32 v80, v80, v81\n\t" \
   "v_cvt_pk_bf16_f32 v81, v88, v89\n\t" "v_cvt_pk_bf16_f32 v82, v82, v83\n\t" "v_cvt_pk_bf16_f32 v83, v90, v91\n\t" \
   "v_mfma_f32_32x32x16_bf16 v[64:79], v[96:99], a[224:227], 0\n\t" "buffer_store_dwordx4 v[80:83], v21, %[rs], s50 offen offset:0 sc1\n\t" \
   "v_cvt_pk_bf16_f32 v84, v84, v85\n\t" "v_mfma_f32_32x32x16_bf16 v[64:79], v[100:103], a[240:243], v[64:79]\n\t" "v_fmac_f32 v48, v32, v8\n\t" \
   "v_fmac_f32 v49, v33, v8\n\t" "v_fmac_f32 v50, v34, v8\n\t" "v_fmac_f32 v51, v35, v8\n\t" "v_fmac_f32 v52, v36, v8\n\t" \
   "v_fmac_f32 v53, v37, v8\n\t" "v_mfma_f32_32x32x16_bf16 v[64:79], v[104:107], a[228:231], v[64:79]\n\t" "v_fmac_f32 v54, v38, v8\n\t" \
   "v_fmac_f32 v55, v39, v8\n\t" "v_fmac_f32 v56, v40, v8\n\t" "v_fmac_f32 v57, v41, v8\n\t" "v_fmac_f32 v58, v42, v8\n\t" \
   "v_fmac_f32 v59, v43, v8\n\t" "v_mfma_f32_32x32x16_bf16 v[64:79], v[108:111], a[244:247], v[64:79]\n\t" "v_fmac_f32 v60, v44, v8\n\t" \
   "v_fmac_f32 v61, v45, v8\n\t" "v_fmac_f32 v62, v46, v8\n\t" "v_fmac_f32 v63, v47, v8\n\t" "v_mul_f32 v48, v48, v10\n\t" \
   "v_mul_f32 v49, v49, v10\n\t" "v_mfma_f32_32x32x16_bf16 v[64:79], v[112:115], a[232:235], v[64:79]\n\t" "v_mul_f32 v50, v50, v10\n\t" \
   "v_mul_f32 v51, v51, v10\n\t" "v_mul_f32 v52, v52, v10\n\t" "v_mul_f32 v53, v53, v10\n\t" "v_mul_f32 v54, v54, v10\n\t" \
   "v_mul_f32 v55, v55, v10\n\t" "v_mfma_f32_32x32x16_bf16 v[64:79], v[116:119], a[248:251], v[64:79]\n\t" "v_mul_f32 v56, v56, v10\n\t" \
   "v_mul_f32 v57, v57, v10\n\t" "v_mul_f32 v58, v58, v10\n\t" "v_mul_f32 v59, v59, v10\n\t" "v_mul_f32 v60, v60, v10\n\t" \
   "v_mul_f32 v61, v61, v10\n\t" "v_mfma_f32_32x32x16_bf16 v[64:79], v[120:123], a[236:239], v[64:79]\n\t" "v_cvt_pk_bf16_f32 v85, v92, v93\n\t" \
   "v_cvt_pk_bf16_f32 v86, v86, v87\n\t" "v_cvt_pk_bf16_f32 v87, v94, v95\n\t" \
   "buffer_store_dwordx4 v[84:87], v21, %[rs], s50 offen offset:512 sc1\n\t" "v_mfma_f32_32x32x16_bf16 v[64:79], v[124:127], a[252:255], v[64:79]\n\t" \
   "v_mul_f32 v62, v62, v10\n\t" "v_mul_f32 v63, v63, v10\n\t" "v_cvt_pk_bf16_f32 v48, v48, v49\n\t" "v_cvt_pk_bf16_f32 v49, v50, v51\n\t" \
   "v_cvt_pk_bf16_f32 v50, v52, v53\n\t" "v_cvt_pk_bf16_f32 v51, v54, v55\n\t" "v_mfma_f32_32x32x16_bf16 v[80:95], v[96:99], v[192:195], 0\n\t" \
   "ds_write_b128 v14, v[48:51] offset:0\n\t" "v_cvt_pk_bf16_f32 v56, v56, v57\n\t" "v_cvt_pk_bf16_f32 v57, v58, v59\n\t" \
   "v_mfma_f32_32x32x16_bf16 v[80:95], v[100:103], v[208:211], v[80:95]\n\t" "v_cvt_pk_bf16_f32 v58, v60, v61\n\t" \
   "v_cvt_pk_bf16_f32 v59, v62, v63\n\t" "ds_write_b128 v14, v[56:59] offset:1024\n\t" "v_add_u32 v138, 1, v138\n\t" "ds_write_b32 v13, v138\n\t" \
   "v_mfma_f32_32x32x16_bf16 v[80:95], v[104:107], v[196:199], v[80:95]\n\t" "s_add_u32 s50, s49, 131072\n\t" "v_cvt_pk_bf16_f32 v64, v64, v65\n\t" \
   "v_cvt_pk_bf16_f32 v65, v72, v73\n\t" "v_cvt_pk_bf16_f32 v66, v66, v67\n\t" "v_cvt_pk_bf16_f32 v67, v74, v75\n\t" "s_add_u32 s45, s40, 3\n\t" \
   "v_mfma_f32_32x32x16_bf16 v[80:95], v[108:111], v[212:215], v[80:95]\n\t" "buffer_store_dwordx4 v[64:67], v21, %[rs], s50 offen offset:0 sc1\n\t" \
   "v_cvt_pk_bf16_f32 v68, v68, v69\n\t" "v_mfma_f32_32x32x16_bf16 v[80:95], v[112:115], v[200:203], v[80:95]\n\t" \
   "v_cvt_pk_bf16_f32 v69, v76, v77\n\t" "v_cvt_pk_bf16_f32 v70, v70, v71\n\t" "v_cvt_pk_bf16_f32 v71, v78, v79\n\t" "s_min_u32 s45, s45, 63\n\t" \
   "s_mov_b32 s46, s42\n\t" "v_mfma_f32_32x32x16_bf16 v[80:95], v[116:119], v[216:219], v[80:95]\n\t" \
   "buffer_store_dwordx4 v[68:71], v21, %[rs], s50 offen offset:512 sc1\n\t" "v_mfma_f32_32x32x16_bf16 v[80:95], v[120:123], v[204:207], v[80:95]\n\t" \
   "s_cmp_eq_u32 s44, -1\n\t" "s_cbranch_scc0 174f\n\t" "74:\n\t" "ds_read_b128 v[160:163], v28 offset:0\n\t" \
   "ds_read_b128 v[164:167], v28 offset:512\n\t" "v_mfma_f32_32x32x16_bf16 v[80:95], v[124:127], v[220:223], v[80:95]\n\t" "s_mov_b32 s42, s43\n\t" \
   "ds_read_b128 v[168:171], v28 offset:1024\n\t" "s_add_u32 s43, s43, 44032\n\t" "s_cmp_eq_u32 s43, 132096\n\t" "s_cselect_b32 s43, 0, s43\n\t" \
   "v_mfma_f32_32x32x16_bf16 v[140:155], v[96:99], v[224:227], 0\n\t" "ds_read_b128 v[172:175], v28 offset:1536\n\t" \
   "ds_read_b128 v[176:179], v28 offset:2048\n\t" "s_add_u32 s41, s41, 1\n\t" "s_add_u32 s40, s40, 1\n\t" "v_xor_b32 v14, 0x2000, v14\n\t" \
   "v_mfma_f32_32x32x16_bf16 v[140:155], v[100:103], v[240:243], v[140:155]\n\t" "s_add_u32 s50, s49, 196608\n\t" "v_cvt_pk_bf16_f32 v80, v80, v81\n\t" \
   "v_cvt_pk_bf16_f32 v81, v88, v89\n\t" "v_cvt_pk_bf16_f32 v82, v82, v83\n\t" "v_cvt_pk_bf16_f32 v83, v90, v91\n\t" "v_xor_b32 v15, 0x2000, v15\n\t" \
   "v_mfma_f32_32x32x16_bf16 v[140:155], v[104:107], v[228:231], v[140:155]\n\t" \
   "buffer_store_dwordx4 v[80:83], v21, %[rs], s50 offen offset:0 sc1\n\t" "v_cvt_pk_bf16_f32 v84, v84, v85\n\t" \
   "v_mfma_f32_32x32x16_bf16 v[140:155], v[108:111], v[244:247], v[140:155]\n\t" "v_cvt_pk_bf16_f32 v85, v92, v93\n\t" \
   "v_cvt_pk_bf16_f32 v86, v86, v87\n\t" "v_cvt_pk_bf16_f32 v87, v94, v95\n\t" "ds_read_b128 v[180:183], v28 offset:2560\n\t" \
   "ds_read_b128 v[184:187], v28 offset:3072\n\t" "v_mfma_f32_32x32x16_bf16 v[140:155], v[112:115], v[232:235], v[140:155]\n\t" \
   "buffer_store_dwordx4 v[84:87], v21, %[rs], s50 offen offset:512 sc1\n\t" "v_mov_b32 v18, v21\n\t" \
   "v_mfma_f32_32x32x16_bf16 v[140:155], v[116:119], v[248:251], v[140:155]\n\t" \
   "v_mfma_f32_32x32x16_bf16 v[140:155], v[120:123], v[236:239], v[140:155]\n\t" \
   "v_mfma_f32_32x32x16_bf16 v[140:155], v[124:127], v[252:255], v[140:155]\n\t" "s_cmp_lt_u32 s40, 64\n\t" "s_cbranch_scc1 1b\n\t" "s_nop 7\n\t" \
   "s_nop 2\n\t" "s_waitcnt lgkmcnt(0)\n\t" "s_barrier\n\t" "s_add_u32 s50, s49, 262144\n\t" "v_cvt_pk_bf16_f32 v140, v140, v141\n\t" \
   "v_cvt_pk_bf16_f32 v141, v148, v149\n\t" "v_cvt_pk_bf16_f32 v142, v142, v143\n\t" "v_cvt_pk_bf16_f32 v143, v150, v151\n\t" \
   "buffer_store_dwordx4 v[140:143], v21, %[rs], s50 offen offset:0 sc1\n\t" "v_cvt_pk_bf16_f32 v144, v144, v145\n\t" \
   "v_cvt_pk_bf16_f32 v145, v152, v153\n\t" "v_cvt_pk_bf16_f32 v146, v146, v147\n\t" "v_cvt_pk_bf16_f32 v147, v154, v155\n\t" \
   "buffer_store_dwordx4 v[144:147], v21, %[rs], s50 offen offset:512 sc1\n\t" "s_sub_i32 s49, s41, 1\n\t" "s_mul_i32 s49, s49, 1310720\n\t" \
   "ds_read_b128 v[96:99], v15 offset:0\n\t" "ds_read_b128 v[100:103], v15 offset:1024\n\t" "ds_read_b128 v[104:107], v15 offset:2048\n\t" \
   "ds_read_b128 v[108:111], v15 offset:3072\n\t" "ds_read_b128 v[112:115], v15 offset:4096\n\t" "ds_read_b128 v[116:119], v15 offset:5120\n\t" \
   "ds_read_b128 v[120:123], v15 offset:6144\n\t" "ds_read_b128 v[124:127], v15 offset:7168\n\t" "s_waitcnt lgkmcnt(6)\n\t" \
   "v_mfma_f32_32x32x16_bf16 v[64:79], v[96:99], a[160:163], 0\n\t" "v_mfma_f32_32x32x16_bf16 v[64:79], v[100:103], a[176:179], v[64:79]\n\t" \
   "s_waitcnt lgkmcnt(4)\n\t" "v_mfma_f32_32x32x16_bf16 v[64:79], v[104:107], a[164:167], v[64:79]\n\t" \
   "v_mfma_f32_32x32x16_bf16 v[64:79], v[108:111], a[180:183], v[64:79]\n\t" "s_waitcnt lgkmcnt(2)\n\t" \
   "v_mfma_f32_32x32x16_bf16 v[64:79], v[112:115], a[168:171], v[64:79]\n\t" "v_mfma_f32_32x32x16_bf16 v[64:79], v[116:119], a[184:187], v[64:79]\n\t" \
   "s_waitcnt lgkmcnt(0)\n\t" "v_mfma_f32_32x32x16_bf16 v[64:79], v[120:123], a[172:175], v[64:79]\n\t" \
   "v_mfma_f32_32x32x16_bf16 v[64:79], v[124:127], a[188:191], v[64:79]\n\t" "v_mfma_f32_32x32x16_bf16 v[80:95], v[96:99], a[192:195], 0\n\t" \
   "v_mfma_f32_32x32x16_bf16 v[80:95], v[100:103], a[208:211], v[80:95]\n\t" "s_add_u32 s50, s49, 0\n\t" "v_cvt_pk_bf16_f32 v64, v64, v65\n\t" \
   "v_cvt_pk_bf16_f32 v65, v72, v73\n\t" "v_cvt_pk_bf16_f32 v66, v66, v67\n\t" "v_cvt_pk_bf16_f32 v67, v74, v75\n\t" \
   "v_mfma_f32_32x32x16_bf16 v[80:95], v[104:107], a[196:199], v[80:95]\n\t" "buffer_store_dwordx4 v[64:67], v21, %[rs], s50 offen offset:0 sc1\n\t" \
   "v_cvt_pk_bf16_f32 v68, v68, v69\n\t" "v_mfma_f32_32x32x16_bf16 v[80:95], v[108:111], a[212:215], v[80:95]\n\t" \
   "v_cvt_pk_bf16_f32 v69, v76, v77\n\t" "v_cvt_pk_bf16_f32 v70, v70, v71\n\t" "v_cvt_pk_bf16_f32 v71, v78, v79\n\t" \
   "v_mfma_f32_32x32x16_bf16 v[80:95], v[112:115], a[200:203], v[80:95]\n\t" "buffer_store_dwordx4 v[68:71], v21, %[rs], s50 offen offset:512 sc1\n\t" \
   "v_mfma_f32_32x32x16_bf16 v[80:95], v[116:119], a[216:219], v[80:95]\n\t" "v_mfma_f32_32x32x16_bf16 v[80:95], v[120:123], a[204:207], v[80:95]\n\t" \
   "v_mfma_f32_32x32x16_bf16 v[80:95], v[124:127], a[220:223], v[80:95]\n\t" "v_mfma_f32_32x32x16_bf16 v[64:79], v[96:99], a[224:227], 0\n\t" \
   "v_mfma_f32_32x32x16_bf16 v[64:79], v[100:103], a[240:243], v[64:79]\n\t" "s_add_u32 s50, s49, 65536\n\t" "v_cvt_pk_bf16_f32 v80, v80, v81\n\t" \
   "v_cvt_pk_bf16_f32 v81, v88, v89\n\t" "v_cvt_pk_bf16_f32 v82, v82, v83\n\t" "v_cvt_pk_bf16_f32 v83, v90, v91\n\t" \
   "v_mfma_f32_32x32x16_bf16 v[64:79], v[104:107], a[228:231], v[64:79]\n\t" "buffer_store_dwordx4 v[80:83], v21, %[rs], s50 offen offset:0 sc1\n\t" \
   "v_cvt_pk_bf16_f32 v84, v84, v85\n\t" "v_mfma_f32_32x32x16_bf16 v[64:79], v[108:111], a[244:247], v[64:79]\n\t" \
   "v_cvt_pk_bf16_f32 v85, v92, v93\n\t" "v_cvt_pk_bf16_f32 v86, v86, v87\n\t" "v_cvt_pk_bf16_f32 v87, v94, v95\n\t" \
   "v_mfma_f32_32x32x16_bf16 v[64:79], v[112:115], a[232:235], v[64:79]\n\t" "buffer_store_dwordx4 v[84:87], v21, %[rs], s50 offen offset:512 sc1\n\t" \
   "v_mfma_f32_32x32x16_bf16 v[64:79], v[116:119], a[248:251], v[64:79]\n\t" "v_mfma_f32_32x32x16_bf16 v[64:79], v[120:123], a[236:239], v[64:79]\n\t" \
   "v_mfma_f32_32x32x16_bf16 v[64:79], v[124:127], a[252:255], v[64:79]\n\t" "v_mfma_f32_32x32x16_bf16 v[80:95], v[96:99], v[192:195], 0\n\t" \
   "v_mfma_f32_32x32x16_bf16 v[80:95], v[100:103], v[208:211], v[80:95]\n\t" "s_add_u32 s50, s49, 131072\n\t" "v_cvt_pk_bf16_f32 v64, v64, v65\n\t" \
   "v_cvt_pk_bf16_f32 v65, v72, v73\n\t" "v_cvt_pk_bf16_f32 v66, v66, v67\n\t" "v_cvt_pk_bf16_f32 v67, v74, v75\n\t" \
   "v_mfma_f32_32x32x16_bf16 v[80:95], v[104:107], v[196:199], v[80:95]\n\t" "buffer_store_dwordx4 v[64:67], v21, %[rs], s50 offen offset:0 sc1\n\t" \
   "v_cvt_pk_bf16_f32 v68, v68, v69\n\t" "v_mfma_f32_32x32x16_bf16 v[80:95], v[108:111], v[212:215], v[80:95]\n\t" \
   "v_cvt_pk_bf16_f32 v69, v76, v77\n\t" "v_cvt_pk_bf16_f32 v70, v70, v71\n\t" "v_cvt_pk_bf16_f32 v71, v78, v79\n\t" \
   "v_mfma_f32_32x32x16_bf16 v[80:95], v[112:115], v[200:203], v[80:95]\n\t" "buffer_store_dwordx4 v[68:71], v21, %[rs], s50 offen offset:512 sc1\n\t" \
   "v_mfma_f32_32x32x16_bf16 v[80:95], v[116:119], v[216:219], v[80:95]\n\t" "v_mfma_f32_32x32x16_bf16 v[80:95], v[120:123], v[204:207], v[80:95]\n\t" \
   "v_mfma_f32_32x32x16_bf16 v[80:95], v[124:127], v[220:223], v[80:95]\n\t" "v_mfma_f32_32x32x16_bf16 v[140:155], v[96:99], v[224:227], 0\n\t" \
   "v_mfma_f32_32x32x16_bf16 v[140:155], v[100:103], v[240:243], v[140:155]\n\t" "s_add_u32 s50, s49, 196608\n\t" "v_cvt_pk_bf16_f32 v80, v80, v81\n\t" \
   "v_cvt_pk_bf16_f32 v81, v88, v89\n\t" "v_cvt_pk_bf16_f32 v82, v82, v83\n\t" "v_cvt_pk_bf16_f32 v83, v90, v91\n\t" \
   "v_mfma_f32_32x32x16_bf16 v[140:155], v[104:107], v[228:231], v[140:155]\n\t" \
   "buffer_store_dwordx4 v[80:83], v21, %[rs], s50 offen offset:0 sc1\n\t" "v_cvt_pk_bf16_f32 v84, v84, v85\n\t" \
   "v_mfma_f32_32x32x16_bf16 v[140:155], v[108:111], v[244:247], v[140:155]\n\t" "v_cvt_pk_bf16_f32 v85, v92, v93\n\t" \
   "v_cvt_pk_bf16_f32 v86, v86, v87\n\t" "v_cvt_pk_bf16_f32 v87, v94, v95\n\t" \
   "v_mfma_f32_32x32x16_bf16 v[140:155], v[112:115], v[232:235], v[140:155]\n\t" \
   "buffer_store_dwordx4 v[84:87], v21, %[rs], s50 offen offset:512 sc1\n\t" \
   "v_mfma_f32_32x32x16_bf16 v[140:155], v[116:119], v[248:251], v[140:155]\n\t" \
   "v_mfma_f32_32x32x16_bf16 v[140:155], v[120:123], v[236:239], v[140:155]\n\t" \
   "v_mfma_f32_32x32x16_bf16 v[140:155], v[124:127], v[252:255], v[140:155]\n\t" "s_nop 7\n\t" "s_nop 2\n\t" "s_add_u32 s50, s49, 262144\n\t" \
   "v_cvt_pk_bf16_f32 v140, v140, v141\n\t" "v_cvt_pk_bf16_f32 v141, v148, v149\n\t" "v_cvt_pk_bf16_f32 v142, v142, v143\n\t" \
   "v_cvt_pk_bf16_f32 v143, v150, v151\n\t" "buffer_store_dwordx4 v[140:143], v21, %[rs], s50 offen offset:0 sc1\n\t" \
   "v_cvt_pk_bf16_f32 v144, v144, v145\n\t" "v_cvt_pk_bf16_f32 v145, v152, v153\n\t" "v_cvt_pk_bf16_f32 v146, v146, v147\n\t" \
   "v_cvt_pk_bf16_f32 v147, v154, v155\n\t" "buffer_store_dwordx4 v[144:147], v21, %[rs], s50 offen offset:512 sc1\n\t" "s_waitcnt vmcnt(0)\n\t" \
   "s_cmp_lt_i32 s40, 2\n\t" "s_cselect_b32 s47, 0x7ffffff0, 0\n\t" "v_or_b32 v30, s47, v20\n\t" "s_sub_i32 s47, s41, 2\n\t" \
   "s_max_i32 s47, s47, 0\n\t" "s_lshl_b32 s47, s47, 12\n\t" "buffer_store_dword v31, v30, %[rs], s47 offen sc1\n\t" "s_cmp_lt_i32 s40, 1\n\t" \
   "s_cselect_b32 s47, 0x7ffffff0, 0\n\t" "v_or_b32 v30, s47, v20\n\t" "s_sub_i32 s47, s41, 1\n\t" "s_max_i32 s47, s47, 0\n\t" \
   "s_lshl_b32 s47, s47, 12\n\t" "buffer_store_dword v31, v30, %[rs], s47 offen sc1\n\t" "s_branch 9f\n\t" "172:\n\t" "v_cmp_lt_i32 vcc, 6, v25\n\t" \
   "s_lshr_b32 s48, s45, 1\n\t" "s_or_b32 s44, s44, vcc_lo\n\t" "s_cmp_lt_u32 s40, 4\n\t" "s_cbranch_scc1 272f\n\t" \
   "buffer_load_dword v25, v19, %[rs], 0 offen sc1\n\t" "272:\n\t" "s_bitcmp1_b32 s44, s48\n\t" "s_cbranch_scc1 72b\n\t" "s_mov_b32 s47, 0\n\t" \
   "buffer_load_dword v30, v19, %[rs], 0 offen sc1\n\t" "s_waitcnt vmcnt(0)\n\t" "v_add_u32 v30, 1, v30\n\t" "v_cmp_lt_u32 vcc, 7, v30\n\t" \
   "s_or_b32 s44, s44, vcc_lo\n\t" "s_bitcmp1_b32 s44, s48\n\t" "s_cbranch_scc1 72b\n\t" "s_bitset1_b32 s44, s48\n\t" "s_lshl_b32 s48, s48, 7\n\t" \
   "s_add_u32 s48, s48, %[cnt]\n\t" "73:\n\t" "s_sleep 10\n\t" "buffer_load_dword v30, off, %[rs], s48 sc1\n\t" "s_add_u32 s47, s47, 1\n\t" \
   "s_waitcnt vmcnt(0)\n\t" "v_add_u32 v30, 1, v30\n\t" "v_cmp_lt_u32 vcc, 7, v30\n\t" "s_cmp_gt_u32 s47, 0x20000\n\t" "s_cbranch_scc1 72b\n\t" \
   "s_cbranch_vccz 73b\n\t" "s_branch 72b\n\t" "176:\n\t" "v_cmp_lt_i32 vcc, 6, v25\n\t" "s_lshr_b32 s48, s45, 1\n\t" "s_or_b32 s44, s44, vcc_lo\n\t" \
   "s_cmp_lt_u32 s40, 4\n\t" "s_cbranch_scc1 276f\n\t" "buffer_load_dword v25, v19, %[rs], 0 offen sc1\n\t" "276:\n\t" "s_bitcmp1_b32 s44, s48\n\t" \
   "s_cbranch_scc1 76b\n\t" "s_mov_b32 s47, 0\n\t" "buffer_load_dword v30, v19, %[rs], 0 offen sc1\n\t" "s_waitcnt vmcnt(0)\n\t" \
   "v_add_u32 v30, 1, v30\n\t" "v_cmp_lt_u32 vcc, 7, v30\n\t" "s_or_b32 s44, s44, vcc_lo\n\t" "s_bitcmp1_b32 s44, s48\n\t" "s_cbranch_scc1 76b\n\t" \
   "s_bitset1_b32 s44, s48\n\t" "s_lshl_b32 s48, s48, 7\n\t" "s_add_u32 s48, s48, %[cnt]\n\t" "77:\n\t" "s_sleep 10\n\t" \
   "buffer_load_dword v30, off, %[rs], s48 sc1\n\t" "s_add_u32 s47, s47, 1\n\t" "s_waitcnt vmcnt(0)\n\t" "v_add_u32 v30, 1, v30\n\t" \
   "v_cmp_lt_u32 vcc, 7, v30\n\t" "s_cmp_gt_u32 s47, 0x20000\n\t" "s_cbranch_scc1 76b\n\t" "s_cbranch_vccz 77b\n\t" "s_branch 76b\n\t" "174:\n\t" \
   "v_cmp_lt_i32 vcc, 6, v25\n\t" "s_lshr_b32 s48, s45, 1\n\t" "s_or_b32 s44, s44, vcc_lo\n\t" "s_cmp_lt_u32 s40, 4\n\t" "s_cbranch_scc1 274f\n\t" \
   "buffer_load_dword v25, v19, %[rs], 0 offen sc1\n\t" "274:\n\t" "s_bitcmp1_b32 s44, s48\n\t" "s_cbranch_scc1 74b\n\t" "s_mov_b32 s47, 0\n\t" \
   "buffer_load_dword v30, v19, %[rs], 0 offen sc1\n\t" "s_waitcnt vmcnt(0)\n\t" "v_add_u32 v30, 1, v30\n\t" "v_cmp_lt_u32 vcc, 7, v30\n\t" \
   "s_or_b32 s44, s44, vcc_lo\n\t" "s_bitcmp1_b32 s44, s48\n\t" "s_cbranch_scc1 74b\n\t" "s_bitset1_b32 s44, s48\n\t" "s_lshl_b32 s48, s48, 7\n\t" \
   "s_add_u32 s48, s48, %[cnt]\n\t" "75:\n\t" "s_sleep 10\n\t" "buffer_load_dword v30, off, %[rs], s48 sc1\n\t" "s_add_u32 s47, s47, 1\n\t" \
   "s_waitcnt vmcnt(0)\n\t" "v_add_u32 v30, 1, v30\n\t" "v_cmp_lt_u32 vcc, 7, v30\n\t" "s_cmp_gt_u32 s47, 0x20000\n\t" "s_cbranch_scc1 74b\n\t" \
   "s_cbranch_vccz 75b\n\t" "s_branch 74b\n\t" "150:\n\t" "ds_read_b128 v[156:159], v12\n\t" "s_waitcnt lgkmcnt(0)\n\t" \
   "v_min3_u32 v156, v156, v157, v158\n\t" "v_min_u32 v156, v156, v159\n\t" "v_cmp_ge_u32 vcc, v156, v138\n\t" "s_cbranch_vccz 150b\n\t" \
   "s_branch 50b\n\t" "152:\n\t" "ds_read_b128 v[156:159], v12\n\t" "s_waitcnt lgkmcnt(0)\n\t" "v_min3_u32 v156, v156, v157, v158\n\t" \
   "v_min_u32 v156, v156, v159\n\t" "v_cmp_ge_u32 vcc, v156, v138\n\t" "s_cbranch_vccz 152b\n\t" "s_branch 52b\n\t" "9:\n\t" "s_waitcnt vmcnt(0)"
#define VU_CLOB "v8", "v9", "v10", "v11", "v12", "v13", "v14", "v15", "v32", "v33", "v34", "v35", "v36", "v37", "v38", "v39", "v40", "v41", "v42", "v43", "v44", "v45", "v46", "v47", "v48", "v49", "v50", "v51", "v52", "v53", "v54", "v55", "v56", "v57", "v58", "v59", "v60", "v61", "v62", "v63", "v64", "v65", "v66", "v67", "v68", "v69", "v70", "v71", "v72", "v73", "v74", "v75", "v76", "v77", "v78", "v79", "v80", "v81", "v82", "v83", "v84", "v85", "v86", "v87", "v88", "v89", "v90", "v91", "v92", "v93", "v94", "v95", "v96", "v97", "v98", "v99", "v100", "v101", "v102", "v103", "v104", "v105", "v106", "v107", "v108", "v109", "v110", "v111", "v112", "v113", "v114", "v115", "v116", "v117", "v118", "v119", "v120", "v121", "v122", "v123", "v124", "v125", "v126", "v127", "v128", "v129", "v130", "v131", "v132", "v133", "v134", "v135", "v136", "v137", "v138", "v139", "v140", "v141", "v142", "v143", "v144", "v145", "v146", "v147", "v148", "v149", "v150", "v151", "v152", "v153", "v154", "v155", "v156", "v157", "v158", "v159", "v160", "v161", "v162", "v163", "v164", "v165", "v166", "v167", "v168", "v169", "v170", "v171", "v172", "v173", "v174", "v175", "v176", "v177", "v178", "v179", "v180", "v181", "v182", "v183", "v184", "v185", "v186", "v187", "v188", "v189", "v190", "v191", "s40", "s41", "s42", "s43", "s44", "s45", "s46", "s47", "s48", "s49", "s50", "s51", "s52", "s53", "s54", "s55"
#define DF_ASM \
   "v_mov_b32 v80, 0x3f803f80\n\t" "v_mov_b32 v81, 0x3f803f80\n\t" "v_mov_b32 v82, 0x3f803f80\n\t" "v_mov_b32 v83, 0x3f803f80\n\t" \
   "s_mov_b32 s45, %[set]\n\t" "s_mov_b32 s46, 0\n\t" "v_mfma_f32_16x16x32_bf16 v[32:35], a[0:3], v[80:83], 0\n\t" \
   "v_mfma_f32_16x16x32_bf16 v[32:35], a[4:7], v[80:83], v[32:35]\n\t" "v_mfma_f32_16x16x32_bf16 v[32:35], a[8:11], v[80:83], v[32:35]\n\t" \
   "v_mfma_f32_16x16x32_bf16 v[32:35], a[12:15], v[80:83], v[32:35]\n\t" "v_mfma_f32_16x16x32_bf16 v[32:35], a[16:19], v[80:83], v[32:35]\n\t" \
   "v_mfma_f32_16x16x32_bf16 v[32:35], a[20:23], v[80:83], v[32:35]\n\t" "v_mfma_f32_16x16x32_bf16 v[32:35], a[24:27], v[80:83], v[32:35]\n\t" \
   "v_mfma_f32_16x16x32_bf16 v[32:35], a[28:31], v[80:83], v[32:35]\n\t" "v_mfma_f32_16x16x32_bf16 v[32:35], a[32:35], v[80:83], v[32:35]\n\t" \
   "v_mfma_f32_16x16x32_bf16 v[32:35], a[36:39], v[80:83], v[32:35]\n\t" "v_mfma_f32_16x16x32_bf16 v[32:35], a[40:43], v[80:83], v[32:35]\n\t" \
   "v_mfma_f32_16x16x32_bf16 v[32:35], a[44:47], v[80:83], v[32:35]\n\t" "v_mfma_f32_16x16x32_bf16 v[32:35], a[48:51], v[80:83], v[32:35]\n\t" \
   "v_mfma_f32_16x16x32_bf16 v[32:35], a[52:55], v[80:83], v[32:35]\n\t" "v_mfma_f32_16x16x32_bf16 v[32:35], a[56:59], v[80:83], v[32:35]\n\t" \
   "v_mfma_f32_16x16x32_bf16 v[32:35], a[60:63], v[80:83], v[32:35]\n\t" "v_mfma_f32_16x16x32_bf16 v[32:35], a[64:67], v[80:83], v[32:35]\n\t" \
   "v_mfma_f32_16x16x32_bf16 v[32:35], a[68:71], v[80:83], v[32:35]\n\t" "v_mfma_f32_16x16x32_bf16 v[32:35], a[72:75], v[80:83], v[32:35]\n\t" \
   "v_mfma_f32_16x16x32_bf16 v[32:35], a[76:79], v[80:83], v[32:35]\n\t" "v_mfma_f32_16x16x32_bf16 v[36:39], a[80:83], v[80:83], 0\n\t" \
   "v_mfma_f32_16x16x32_bf16 v[36:39], a[84:87], v[80:83], v[36:39]\n\t" "v_mfma_f32_16x16x32_bf16 v[36:39], a[88:91], v[80:83], v[36:39]\n\t" \
   "v_mfma_f32_16x16x32_bf16 v[36:39], a[92:95], v[80:83], v[36:39]\n\t" "v_mfma_f32_16x16x32_bf16 v[36:39], a[96:99], v[80:83], v[36:39]\n\t" \
   "v_mfma_f32_16x16x32_bf16 v[36:39], a[100:103], v[80:83], v[36:39]\n\t" "v_mfma_f32_16x16x32_bf16 v[36:39], a[104:107], v[80:83], v[36:39]\n\t" \
   "v_mfma_f32_16x16x32_bf16 v[36:39], a[108:111], v[80:83], v[36:39]\n\t" "v_mfma_f32_16x16x32_bf16 v[36:39], a[112:115], v[80:83], v[36:39]\n\t" \
   "v_mfma_f32_16x16x32_bf16 v[36:39], a[116:119], v[80:83], v[36:39]\n\t" "v_mfma_f32_16x16x32_bf16 v[36:39], a[120:123], v[80:83], v[36:39]\n\t" \
   "v_mfma_f32_16x16x32_bf16 v[36:39], a[124:127], v[80:83], v[36:39]\n\t" "v_mfma_f32_16x16x32_bf16 v[36:39], a[128:131], v[80:83], v[36:39]\n\t" \
   "v_mfma_f32_16x16x32_bf16 v[36:39], a[132:135], v[80:83], v[36:39]\n\t" "v_mfma_f32_16x16x32_bf16 v[36:39], a[136:139], v[80:83], v[36:39]\n\t" \
   "v_mfma_f32_16x16x32_bf16 v[36:39], a[140:143], v[80:83], v[36:39]\n\t" "v_mfma_f32_16x16x32_bf16 v[36:39], a[144:147], v[80:83], v[36:39]\n\t" \
   "v_mfma_f32_16x16x32_bf16 v[36:39], a[148:151], v[80:83], v[36:39]\n\t" "v_mfma_f32_16x16x32_bf16 v[36:39], a[152:155], v[80:83], v[36:39]\n\t" \
   "v_mfma_f32_16x16x32_bf16 v[36:39], a[156:159], v[80:83], v[36:39]\n\t" "s_add_u32 s48, s45, 60\n\t" "s_lshl_b32 s48, s48, 8\n\t" \
   "s_add_u32 s47, s48, %[xs4]\n\t" "v_mov_b32 v64, -1\n\t" "v_mov_b32 v65, -1\n\t" \
   "buffer_store_dwordx2 v[64:65], v21, %[rs], s48 offen offset:0 sc1\n\t" "buffer_store_dwordx2 v[64:65], v21, %[rs], s47 offen offset:0 sc1\n\t" \
   "buffer_store_dwordx2 v[64:65], v21, %[rs], s48 offen offset:128 sc1\n\t" "buffer_store_dwordx2 v[64:65], v21, %[rs], s47 offen offset:128 sc1\n\t" \
   "1:\n\t" "s_lshl_b32 s48, s45, 7\n\t" "s_mov_b32 s47, 0\n\t" "s_cmp_lg_u32 %[ldx], %[ld0]\n\t" "s_cbranch_scc1 3f\n\t" "2:\n\t" \
   "buffer_load_dword v30, v19, %[rs], s48 offen sc1\n\t" "s_add_u32 s47, s47, 1\n\t" "s_waitcnt vmcnt(0)\n\t" "v_readfirstlane_b32 s44, v30\n\t" \
   "s_add_u32 s44, s44, 1\n\t" "s_cmp_ge_u32 s44, 10\n\t" "s_cbranch_scc1 3f\n\t" "s_cmp_gt_u32 s47, 0x20000\n\t" "s_cbranch_scc1 3f\n\t" \
   "s_cmp_lt_u32 s45, 56\n\t" "s_cbranch_scc1 4f\n\t" "s_sleep 3\n\t" "s_cmp_lg_u32 s44, 0\n\t" "s_cbranch_scc1 2b\n\t" "s_sleep 8\n\t" \
   "s_branch 2b\n\t" "4:\n\t" "s_sleep 60\n\t" "s_branch 2b\n\t" "3:\n\t" "s_waitcnt vmcnt(0)\n\t" "s_barrier\n\t" "s_memrealtime s[54:55]\n\t" \
   "s_mul_i32 s52, s45, 1536\n\t" "s_mul_i32 s51, s45, 40960\n\t" "s_add_u32 m0, s46, %[ldx]\n\t" "s_mov_b32 s53, s46\n\t" \
   "buffer_load_dwordx4 v16, %[rs], s51 offen offset:0 sc1 lds\n\t" "buffer_load_dwordx4 v16, %[rs], s51 offen offset:1024 sc1 lds\n\t" \
   "buffer_load_dwordx4 v16, %[rs], s51 offen offset:2048 sc1 lds\n\t" "buffer_load_dwordx4 v16, %[rs], s51 offen offset:3072 sc1 lds\n\t" \
   "s_add_u32 m0, m0, 4096\n\t" "s_add_u32 s51, s51, 4096\n\t" "buffer_load_dwordx4 v16, %[rs], s51 offen offset:0 sc1 lds\n\t" \
   "buffer_load_dwordx4 v16, %[rs], s51 offen offset:1024 sc1 lds\n\t" "buffer_load_dwordx4 v16, %[rs], s51 offen offset:2048 sc1 lds\n\t" \
   "buffer_load_dwordx4 v16, %[rs], s51 offen offset:3072 sc1 lds\n\t" "s_add_u32 m0, m0, 4096\n\t" "s_add_u32 s51, s51, 4096\n\t" \
   "buffer_load_dwordx4 v16, %[rs], s51 offen offset:0 sc1 lds\n\t" "buffer_load_dwordx4 v16, %[rs], s51 offen offset:1024 sc1 lds\n\t" \
   "s_add_u32 m0, s53, %[ld1]\n\t" "s_nop 0\n\t" "buffer_load_dwordx4 v17, %[rs], s52 offen sc1 lds\n\t" "s_cmp_lt_u32 s45, 60\n\t" \
   "s_cselect_b32 s47, 0x7ffffff0, 0\n\t" "v_or_b32 v30, s47, v20\n\t" "s_waitcnt lgkmcnt(0)\n\t" "v_mov_b32 v31, s54\n\t" "v_min_u32 v31, -2, v31\n\t" \
   "buffer_store_dword v31, v30, %[rs], 0 offen sc1\n\t" "v_add_u32 v26, s46, v22\n\t" "v_add_u32 v27, s46, v23\n\t" "s_waitcnt vmcnt(0)\n\t" \
   "s_barrier\n\t" "ds_read_b128 v[80:83], v26 offset:0\n\t" "ds_read_b128 v[84:87], v26 offset:1024\n\t" "ds_read_b128 v[88:91], v26 offset:2048\n\t" \
   "ds_read_b128 v[92:95], v26 offset:3072\n\t" "ds_read_b128 v[96:99], v26 offset:4096\n\t" "ds_read_b128 v[100:103], v26 offset:5120\n\t" \
   "ds_read_b128 v[104:107], v26 offset:6144\n\t" "s_waitcnt lgkmcnt(5)\n\t" "v_mfma_f32_16x16x32_bf16 v[52:55], a[80:83], v[80:83], 0\n\t" \
   "ds_read_b128 v[112:115], v27 offset:0\n\t" "v_mfma_f32_16x16x32_bf16 v[48:51], a[0:3], v[80:83], 0\n\t" \
   "ds_read_b128 v[108:111], v26 offset:7168\n\t" "ds_read_b128 v[116:119], v27 offset:16\n\t" \
   "v_mfma_f32_16x16x32_bf16 v[52:55], a[84:87], v[84:87], v[52:55]\n\t" "ds_read_b64 v[120:121], v27 offset:32\n\t" \
   "v_mfma_f32_16x16x32_bf16 v[48:51], a[4:7], v[84:87], v[48:51]\n\t" "ds_read_b128 v[80:83], v26 offset:8192\n\t" "s_waitcnt lgkmcnt(8)\n\t" \
   "v_mfma_f32_16x16x32_bf16 v[52:55], a[88:91], v[88:91], v[52:55]\n\t" "ds_read_b128 v[122:125], v27 offset:768\n\t" \
   "v_mfma_f32_16x16x32_bf16 v[48:51], a[8:11], v[88:91], v[48:51]\n\t" "ds_read_b128 v[84:87], v26 offset:9216\n\t" \
   "ds_read_b128 v[126:129], v27 offset:784\n\t" "v_mfma_f32_16x16x32_bf16 v[52:55], a[92:95], v[92:95], v[52:55]\n\t" \
   "v_mfma_f32_16x16x32_bf16 v[48:51], a[12:15], v[92:95], v[48:51]\n\t" "ds_read_b128 v[88:91], v26 offset:10240\n\t" \
   "ds_read_b64 v[130:131], v27 offset:800\n\t" "s_waitcnt lgkmcnt(11)\n\t" "v_mfma_f32_16x16x32_bf16 v[52:55], a[96:99], v[96:99], v[52:55]\n\t" \
   "s_lshl_b32 s48, s45, 8\n\t" "v_mfma_f32_16x16x32_bf16 v[48:51], a[16:19], v[96:99], v[48:51]\n\t" "ds_read_b128 v[92:95], v26 offset:11264\n\t" \
   "s_add_u32 s47, s48, %[xs4]\n\t" "v_mfma_f32_16x16x32_bf16 v[52:55], a[100:103], v[100:103], v[52:55]\n\t" \
   "v_mfma_f32_16x16x32_bf16 v[48:51], a[20:23], v[100:103], v[48:51]\n\t" "ds_read_b128 v[96:99], v26 offset:12288\n\t" "s_waitcnt lgkmcnt(8)\n\t" \
   "v_add_f32 v112, v112, v114\n\t" "v_mfma_f32_16x16x32_bf16 v[52:55], a[104:107], v[104:107], v[52:55]\n\t" "v_add_f32 v113, v113, v115\n\t" \
   "v_mfma_f32_16x16x32_bf16 v[48:51], a[24:27], v[104:107], v[48:51]\n\t" "ds_read_b128 v[100:103], v26 offset:13312\n\t" \
   "v_mfma_f32_16x16x32_bf16 v[52:55], a[108:111], v[108:111], v[52:55]\n\t" "v_add_f32 v116, v116, v118\n\t" \
   "v_mfma_f32_16x16x32_bf16 v[48:51], a[28:31], v[108:111], v[48:51]\n\t" "ds_read_b128 v[104:107], v26 offset:14336\n\t" \
   "v_add_f32 v117, v117, v119\n\t" "s_waitcnt lgkmcnt(7)\n\t" "v_mfma_f32_16x16x32_bf16 v[52:55], a[112:115], v[80:83], v[52:55]\n\t" \
   "v_mfma_f32_16x16x32_bf16 v[48:51], a[32:35], v[80:83], v[48:51]\n\t" "ds_read_b128 v[108:111], v26 offset:15360\n\t" \
   "v_add_f32 v112, v112, v120\n\t" "v_mfma_f32_16x16x32_bf16 v[52:55], a[116:119], v[84:87], v[52:55]\n\t" "v_add_f32 v113, v113, v121\n\t" \
   "v_mfma_f32_16x16x32_bf16 v[48:51], a[36:39], v[84:87], v[48:51]\n\t" "ds_read_b128 v[80:83], v26 offset:16384\n\t" "v_add_f32 v112, v112, v116\n\t" \
   "s_waitcnt lgkmcnt(5)\n\t" "v_mfma_f32_16x16x32_bf16 v[52:55], a[120:123], v[88:91], v[52:55]\n\t" \
   "v_mfma_f32_16x16x32_bf16 v[48:51], a[40:43], v[88:91], v[48:51]\n\t" "ds_read_b128 v[84:87], v26 offset:17408\n\t" "v_add_f32 v113, v113, v117\n\t" \
   "v_mfma_f32_16x16x32_bf16 v[52:55], a[124:127], v[92:95], v[52:55]\n\t" "v_mul_f32 v112, 0xbacccccd, v112\n\t" \
   "v_mfma_f32_16x16x32_bf16 v[48:51], a[44:47], v[92:95], v[48:51]\n\t" "ds_read_b128 v[88:91], v26 offset:18432\n\t" "s_waitcnt lgkmcnt(5)\n\t" \
   "v_mfma_f32_16x16x32_bf16 v[52:55], a[128:131], v[96:99], v[52:55]\n\t" "v_mul_f32 v113, 0x3acccccd, v113\n\t" \
   "v_mfma_f32_16x16x32_bf16 v[48:51], a[48:51], v[96:99], v[48:51]\n\t" "ds_read_b128 v[92:95], v26 offset:19456\n\t" \
   "v_fma_f32 v113, -v112, v112, v113\n\t" "v_mfma_f32_16x16x32_bf16 v[52:55], a[132:135], v[100:103], v[52:55]\n\t" "v_max_f32 v113, 0, v113\n\t" \
   "v_mfma_f32_16x16x32_bf16 v[48:51], a[52:55], v[100:103], v[48:51]\n\t" "ds_read_b128 v[96:99], v26 offset:20480\n\t" "s_waitcnt lgkmcnt(5)\n\t" \
   "v_mfma_f32_16x16x32_bf16 v[52:55], a[136:139], v[104:107], v[52:55]\n\t" "v_cvt_pk_bf16_f32 v113, v113, v112\n\t" \
   "v_mfma_f32_16x16x32_bf16 v[48:51], a[56:59], v[104:107], v[48:51]\n\t" "ds_read_b128 v[100:103], v26 offset:21504\n\t" \
   "v_and_b32 v8, 0xffff0000, v113\n\t" "v_mfma_f32_16x16x32_bf16 v[52:55], a[140:143], v[108:111], v[52:55]\n\t" \
   "v_mfma_f32_16x16x32_bf16 v[48:51], a[60:63], v[108:111], v[48:51]\n\t" "ds_read_b128 v[104:107], v26 offset:22528\n\t" \
   "v_lshlrev_b32 v113, 16, v113\n\t" "s_waitcnt lgkmcnt(5)\n\t" "v_mfma_f32_16x16x32_bf16 v[52:55], a[144:147], v[80:83], v[52:55]\n\t" \
   "v_add_f32 v113, 0x3727c5ac, v113\n\t" "v_mfma_f32_16x16x32_bf16 v[48:51], a[64:67], v[80:83], v[48:51]\n\t" \
   "ds_read_b128 v[108:111], v26 offset:23552\n\t" "v_mfma_f32_16x16x32_bf16 v[52:55], a[148:151], v[84:87], v[52:55]\n\t" \
   "v_cvt_pk_bf16_f32 v113, v113, v113\n\t" "v_mfma_f32_16x16x32_bf16 v[48:51], a[68:71], v[84:87], v[48:51]\n\t" \
   "ds_read_b128 v[80:83], v26 offset:24576\n\t" "v_lshlrev_b32 v113, 16, v113\n\t" "s_waitcnt lgkmcnt(5)\n\t" \
   "v_mfma_f32_16x16x32_bf16 v[52:55], a[152:155], v[88:91], v[52:55]\n\t" "v_sqrt_f32 v113, v113\n\t" \
   "v_mfma_f32_16x16x32_bf16 v[48:51], a[72:75], v[88:91], v[48:51]\n\t" "ds_read_b128 v[84:87], v26 offset:25600\n\t" \
   "v_mfma_f32_16x16x32_bf16 v[52:55], a[156:159], v[92:95], v[52:55]\n\t" "s_nop 0\n\t" \
   "v_mfma_f32_16x16x32_bf16 v[48:51], a[76:79], v[92:95], v[48:51]\n\t" "ds_read_b128 v[88:91], v26 offset:26624\n\t" \
   "v_cvt_pk_bf16_f32 v113, v113, v113\n\t" "s_waitcnt lgkmcnt(5)\n\t" "v_mfma_f32_16x16x32_bf16 v[60:63], a[80:83], v[96:99], 0\n\t" \
   "v_mfma_f32_16x16x32_bf16 v[56:59], a[0:3], v[96:99], 0\n\t" "ds_read_b128 v[92:95], v26 offset:27648\n\t" "v_lshlrev_b32 v113, 16, v113\n\t" \
   "v_mfma_f32_16x16x32_bf16 v[60:63], a[84:87], v[100:103], v[60:63]\n\t" "v_rcp_f32 v10, v113\n\t" \
   "v_mfma_f32_16x16x32_bf16 v[56:59], a[4:7], v[100:103], v[56:59]\n\t" "ds_read_b128 v[96:99], v26 offset:28672\n\t" "s_waitcnt lgkmcnt(5)\n\t" \
   "v_mfma_f32_16x16x32_bf16 v[60:63], a[88:91], v[104:107], v[60:63]\n\t" "v_fmac_f32 v48, v32, v8\n\t" "v_fmac_f32 v49, v33, v8\n\t" \
   "v_mfma_f32_16x16x32_bf16 v[56:59], a[8:11], v[104:107], v[56:59]\n\t" "ds_read_b128 v[100:103], v26 offset:29696\n\t" "v_fmac_f32 v50, v34, v8\n\t" \
   "v_mfma_f32_16x16x32_bf16 v[60:63], a[92:95], v[108:111], v[60:63]\n\t" "v_fmac_f32 v51, v35, v8\n\t" \
   "v_mfma_f32_16x16x32_bf16 v[56:59], a[12:15], v[108:111], v[56:59]\n\t" "ds_read_b128 v[104:107], v26 offset:30720\n\t" \
   "v_fmac_f32 v52, v36, v8\n\t" "s_waitcnt lgkmcnt(5)\n\t" "v_mfma_f32_16x16x32_bf16 v[60:63], a[96:99], v[80:83], v[60:63]\n\t" \
   "v_fmac_f32 v53, v37, v8\n\t" "v_fmac_f32 v54, v38, v8\n\t" "v_mfma_f32_16x16x32_bf16 v[56:59], a[16:19], v[80:83], v[56:59]\n\t" \
   "ds_read_b128 v[108:111], v26 offset:31744\n\t" "v_fmac_f32 v55, v39, v8\n\t" \
   "v_mfma_f32_16x16x32_bf16 v[60:63], a[100:103], v[84:87], v[60:63]\n\t" "v_mul_f32 v48, v48, v10\n\t" \
   "v_mfma_f32_16x16x32_bf16 v[56:59], a[20:23], v[84:87], v[56:59]\n\t" "ds_read_b128 v[80:83], v26 offset:32768\n\t" "v_mul_f32 v49, v49, v10\n\t" \
   "s_waitcnt lgkmcnt(5)\n\t" "v_mfma_f32_16x16x32_bf16 v[60:63], a[104:107], v[88:91], v[60:63]\n\t" "v_mul_f32 v50, v50, v10\n\t" \
   "v_mul_f32 v51, v51, v10\n\t" "v_mfma_f32_16x16x32_bf16 v[56:59], a[24:27], v[88:91], v[56:59]\n\t" "ds_read_b128 v[84:87], v26 offset:33792\n\t" \
   "v_mul_f32 v52, v52, v10\n\t" "v_mfma_f32_16x16x32_bf16 v[60:63], a[108:111], v[92:95], v[60:63]\n\t" "v_mul_f32 v53, v53, v10\n\t" \
   "v_mfma_f32_16x16x32_bf16 v[56:59], a[28:31], v[92:95], v[56:59]\n\t" "ds_read_b128 v[88:91], v26 offset:34816\n\t" "v_mul_f32 v54, v54, v10\n\t" \
   "s_waitcnt lgkmcnt(5)\n\t" "v_mfma_f32_16x16x32_bf16 v[60:63], a[112:115], v[96:99], v[60:63]\n\t" "v_mul_f32 v55, v55, v10\n\t" \
   "v_cvt_pk_bf16_f32 v64, v48, v49\n\t" "v_mfma_f32_16x16x32_bf16 v[56:59], a[32:35], v[96:99], v[56:59]\n\t" \
   "ds_read_b128 v[92:95], v26 offset:35840\n\t" "v_cvt_pk_bf16_f32 v65, v50, v51\n\t" \
   "v_mfma_f32_16x16x32_bf16 v[60:63], a[116:119], v[100:103], v[60:63]\n\t" "v_cvt_pk_bf16_f32 v66, v52, v53\n\t" \
   "v_mfma_f32_16x16x32_bf16 v[56:59], a[36:39], v[100:103], v[56:59]\n\t" "ds_read_b128 v[96:99], v26 offset:36864\n\t" \
   "v_cvt_pk_bf16_f32 v67, v54, v55\n\t" "s_waitcnt lgkmcnt(5)\n\t" "v_mfma_f32_16x16x32_bf16 v[60:63], a[120:123], v[104:107], v[60:63]\n\t" \
   "buffer_store_dwordx2 v[64:65], v21, %[rs], s48 offen offset:0 sc1\n\t" "buffer_store_dwordx2 v[66:67], v21, %[rs], s47 offen offset:0 sc1\n\t" \
   "v_mfma_f32_16x16x32_bf16 v[56:59], a[40:43], v[104:107], v[56:59]\n\t" "ds_read_b128 v[100:103], v26 offset:37888\n\t" \
   "v_add_f32 v122, v122, v124\n\t" "v_mfma_f32_16x16x32_bf16 v[60:63], a[124:127], v[108:111], v[60:63]\n\t" "v_add_f32 v123, v123, v125\n\t" \
   "v_mfma_f32_16x16x32_bf16 v[56:59], a[44:47], v[108:111], v[56:59]\n\t" "ds_read_b128 v[104:107], v26 offset:38912\n\t" \
   "v_add_f32 v126, v126, v128\n\t" "s_waitcnt lgkmcnt(5)\n\t" "v_mfma_f32_16x16x32_bf16 v[60:63], a[128:131], v[80:83], v[60:63]\n\t" \
   "v_add_f32 v127, v127, v129\n\t" "v_add_f32 v122, v122, v130\n\t" "v_mfma_f32_16x16x32_bf16 v[56:59], a[48:51], v[80:83], v[56:59]\n\t" \
   "ds_read_b128 v[108:111], v26 offset:39936\n\t" "v_add_f32 v123, v123, v131\n\t" \
   "v_mfma_f32_16x16x32_bf16 v[60:63], a[132:135], v[84:87], v[60:63]\n\t" "v_add_f32 v122, v122, v126\n\t" \
   "v_mfma_f32_16x16x32_bf16 v[56:59], a[52:55], v[84:87], v[56:59]\n\t" "v_add_f32 v123, v123, v127\n\t" "s_waitcnt lgkmcnt(4)\n\t" \
   "v_mfma_f32_16x16x32_bf16 v[60:63], a[136:139], v[88:91], v[60:63]\n\t" "v_mul_f32 v122, 0xbacccccd, v122\n\t" \
   "v_mul_f32 v123, 0x3acccccd, v123\n\t" "v_mfma_f32_16x16x32_bf16 v[56:59], a[56:59], v[88:91], v[56:59]\n\t" "v_fma_f32 v123, -v122, v122, v123\n\t" \
   "v_mfma_f32_16x16x32_bf16 v[60:63], a[140:143], v[92:95], v[60:63]\n\t" "v_max_f32 v123, 0, v123\n\t" \
   "v_mfma_f32_16x16x32_bf16 v[56:59], a[60:63], v[92:95], v[56:59]\n\t" "v_cvt_pk_bf16_f32 v123, v123, v122\n\t" "s_waitcnt lgkmcnt(2)\n\t" \
   "v_mfma_f32_16x16x32_bf16 v[60:63], a[144:147], v[96:99], v[60:63]\n\t" "v_and_b32 v12, 0xffff0000, v123\n\t" "v_lshlrev_b32 v123, 16, v123\n\t" \
   "v_mfma_f32_16x16x32_bf16 v[56:59], a[64:67], v[96:99], v[56:59]\n\t" "v_add_f32 v123, 0x3727c5ac, v123\n\t" \
   "v_mfma_f32_16x16x32_bf16 v[60:63], a[148:151], v[100:103], v[60:63]\n\t" "v_cvt_pk_bf16_f32 v123, v123, v123\n\t" \
   "v_mfma_f32_16x16x32_bf16 v[56:59], a[68:71], v[100:103], v[56:59]\n\t" "v_lshlrev_b32 v123, 16, v123\n\t" "s_waitcnt lgkmcnt(0)\n\t" \
   "v_mfma_f32_16x16x32_bf16 v[60:63], a[152:155], v[104:107], v[60:63]\n\t" "v_sqrt_f32 v123, v123\n\t" "s_nop 0\n\t" \
   "v_mfma_f32_16x16x32_bf16 v[56:59], a[72:75], v[104:107], v[56:59]\n\t" "v_cvt_pk_bf16_f32 v123, v123, v123\n\t" \
   "v_mfma_f32_16x16x32_bf16 v[60:63], a[156:159], v[108:111], v[60:63]\n\t" "v_lshlrev_b32 v123, 16, v123\n\t" \
   "v_mfma_f32_16x16x32_bf16 v[56:59], a[76:79], v[108:111], v[56:59]\n\t" "v_rcp_f32 v132, v123\n\t" "s_xor_b32 s46, s46, 44032\n\t" \
   "s_add_u32 s45, s45, 4\n\t" "s_nop 5\n\t" "v_fmac_f32 v56, v32, v12\n\t" "v_fmac_f32 v57, v33, v12\n\t" "v_fmac_f32 v58, v34, v12\n\t" \
   "v_fmac_f32 v59, v35, v12\n\t" "v_fmac_f32 v60, v36, v12\n\t" "v_fmac_f32 v61, v37, v12\n\t" "v_fmac_f32 v62, v38, v12\n\t" \
   "v_fmac_f32 v63, v39, v12\n\t" "v_mul_f32 v56, v56, v132\n\t" "v_mul_f32 v57, v57, v132\n\t" "v_mul_f32 v58, v58, v132\n\t" \
   "v_mul_f32 v59, v59, v132\n\t" "v_mul_f32 v60, v60, v132\n\t" "v_mul_f32 v61, v61, v132\n\t" "v_mul_f32 v62, v62, v132\n\t" \
   "v_mul_f32 v63, v63, v132\n\t" "v_cvt_pk_bf16_f32 v68, v56, v57\n\t" "v_cvt_pk_bf16_f32 v69, v58, v59\n\t" "v_cvt_pk_bf16_f32 v70, v60, v61\n\t" \
   "v_cvt_pk_bf16_f32 v71, v62, v63\n\t" "buffer_store_dwordx2 v[68:69], v21, %[rs], s48 offen offset:128 sc1\n\t" \
   "buffer_store_dwordx2 v[70:71], v21, %[rs], s47 offen offset:128 sc1\n\t" "s_cmp_lt_u32 s45, 64\n\t" "s_cbranch_scc1 1b\n\t" \
   "s_waitcnt vmcnt(0) lgkmcnt(0)"
#define DF_CLOB "v8", "v9", "v10", "v11", "v12", "v13", "v14", "v15", "v32", "v33", "v34", "v35", "v36", "v37", "v38", "v39", "v40", "v41", "v42", "v43", "v44", "v45", "v46", "v47", "v48", "v49", "v50", "v51", "v52", "v53", "v54", "v55", "v56", "v57", "v58", "v59", "v60", "v61", "v62", "v63", "v64", "v65", "v66", "v67", "v68", "v69", "v70", "v71", "v72", "v73", "v74", "v75", "v76", "v77", "v78", "v79", "v80", "v81", "v82", "v83", "v84", "v85", "v86", "v87", "v88", "v89", "v90", "v91", "v92", "v93", "v94", "v95", "v96", "v97", "v98", "v99", "v100", "v101", "v102", "v103", "v104", "v105", "v106", "v107", "v108", "v109", "v110", "v111", "v112", "v113", "v114", "v115", "v116", "v117", "v118", "v119", "v120", "v121", "v122", "v123", "v124", "v125", "v126", "v127", "v128", "v129", "v130", "v131", "v132", "v133", "v134", "v135", "s40", "s41", "s42", "s43", "s44", "s45", "s46", "s47", "s48", "s49", "s50", "s51", "s52", "s53", "s54", "s55"
template <bool VU>
__device__ __forceinline__ void job_proj(int wv_, const v32u (&w)[10], unsigned char* ws, const Pool& pool, int layer, int h) {
  const unsigned lds0 = (unsigned)(uintptr_t)(__attribute__((address_space(3))) unsigned char*)dyn_lds;
  const int lane = lane_id(), wv = wv_, g = lane >> 4, r = lane & 15;
  const v4u rs = make_rs(ws, (unsigned)OFF_END);
  v16u ad = (unsigned)lane;
  ad[0] = (int)OFF_XR + wv * 10240 + lane * 16;
  ad[1] = wv * 64 + lane < 96 ? (int)OFF_ST + wv * 1024 + lane * 16 : OOB;
  ad[3] = cnt_off(layer, CNT_IDFT, lane & 31);
  ad[4] = lane == 0 ? kqf_off(layer, 0, (VU ? 32 : 0) + h) + wv * 4 : OOB;
  const int q = (g >> 1) * 256 + r * 16;
  ad[5] = (VU ? (int)OFF_UF + (wv * 320 + h) * 1024 + (g & 1) * 32768 : (h * RT * 4 + wv) * 1024 + (g & 1) * 4096) + q;
  ad[6] = lds0 + PJ_X + (g & 1) * 20480 + q;
  ad[7] = lds0 + PJ_X + 40960 + (lane & 31) * 48;
  ad[8] = lds0 + wv * 2048 + lane * 16;
  ad[9] = lds0 + 8192 + lane * 16;
  ad[10] = lds0 + PJ_LDS + 1024 + wv * 256 + (lane >> 5) * 128;
  if (layer > 0) fan_wait<64, NFIRST>(wv_, ws, fan_off(1, 1, 0), h);
  PH(2 + layer * 6)
#define PJ_DBG
#define PJ_OPS                                                                                                                      \
  : W_OPS, [rs] "s"(rs), [rdy] "s"((layer - 1) | ((1 << NFIRST / 8) - 1)), [ldx] "s"(lds0 + PJ_X + wv * 10240),                        \
    [ld1] "s"(lds0 + PJ_X + 40960 + (wv < 2 ? wv : 2) * 1024), [qf] "s"((int)OFF_QF), [kf] "s"((int)OFF_KF), [cnt] "s"(cnt_off(layer, CNT_IDFT, 0)), \
    [flw] "s"(lds0 + PJ_LDS + 512 + wv * 4) PJ_DBG
  if constexpr (VU) asm volatile(VU_ASM : "+{v[16:31]}"(ad) PJ_OPS : VU_CLOB, "vcc", "scc", "m0", "memory");
  else asm volatile(KQ_ASM : "+{v[16:31]}"(ad) PJ_OPS : KQ_CLOB, "vcc", "scc", "m0", "memory");
  LDSBAR();
}

__device__ __forceinline__ unsigned dft_idx(int f, int d) { return (unsigned)((f == 1 ? 320 : f >> 1) * d + (((f & 1) && f != 1) ? 160 : 0)) % 640u; }
__device__ __forceinline__ float cos640(unsigned i) { return __builtin_amdgcn_cosf((float)i * (1.f / 640)); }
__device__ __forceinline__ void job_dft(int wv_, unsigned char* ws, int layer, int fg, int set) {
  const unsigned lds0 = (unsigned)(uintptr_t)(__attribute__((address_space(3))) unsigned char*)dyn_lds;
  const int lane = lane_id(), wv = wv_, g = lane >> 4, r = lane & 15;
  const v4u rs = make_rs(ws, (unsigned)OFF_END);
  v32u w[5];
#pragma unroll
  for (int t = 0; t < 2; ++t) {
    const int f = 16 * (fg * 8 + wv * 2 + t) + r;
    const unsigned step = dft_idx(f & ~(f != 1), 32);
#pragma unroll
    for (int q = 0; q < 4; ++q) {
      unsigned m0 = dft_idx(f, sigma(g, 2 * q)), m1 = dft_idx(f, sigma(g, 2 * q + 1));
#pragma unroll
      for (int kt = 0; kt < KT; ++kt) {
        const int i = t * KT + kt;
        w[i >> 3][(i & 7) * 4 + q] = pk(cos640(m0), cos640(m1));
        m0 += step; m0 = min(m0, m0 - 640u); m1 += step; m1 = min(m1, m1 - 640u);
      }
    }
  }
  v16u ad = (unsigned)lane;
  ad[0] = (int)OFF_XR + wv * 10240 + lane * 16;
  ad[1] = wv * 64 + lane < 96 ? (int)OFF_ST + wv * 1024 + lane * 16 : OOB;
  ad[3] = cnt_off(layer, CNT_OUT, 0);
  ad[4] = wv == 0 && lane < FNG ? fan_off(0, 1, 0) + lane * FAN_G + (layer * 20 + set * 5 + fg) * 64 : OOB;
  ad[5] = (int)OFF_X1T + (((fg * 8 + wv * 2) * 4 + g) * XS + r) * 8;
  ad[6] = lds0 + lane * 16;
  ad[7] = lds0 + 40960 + r * 48;
  asm volatile(DF_ASM
               : "+{v[16:31]}"(ad)
               : "{a[0:31]}"(w[0]), "{a[32:63]}"(w[1]), "{a[64:95]}"(w[2]), "{a[96:127]}"(w[3]), "{a[128:159]}"(w[4]), [rs] "s"(rs), [set] "s"(set), [xs4] "s"(4 * XS * 8),
                 [ldx] "s"(lds0 + wv * 10240), [ld0] "s"(lds0), [ld1] "s"(lds0 + 40960 + (wv < 2 ? wv : 2) * 1024)
               : DF_CLOB, "vcc", "scc", "m0", "memory");
}

constexpr int NOUT = 44, NLATE = 108 - NFILL;
constexpr int L_HS = PJ_LDS, MAIN_LDS = L_HS + 2048;
static_assert(MAIN_LDS >= JOB_LDS && MAIN_LDS >= REC_LDS && MAIN_LDS <= 160 * 1024, "LDS");
struct Go { Pool pool; int rot; };
__device__ __forceinline__ void st32(rsrc_t rs, int off, unsigned v) { __builtin_amdgcn_raw_buffer_store_b32(v, rs, off, 0, AUX_SC1); }
__device__ __forceinline__ unsigned hello_load(rsrc_t rs, int b) { return __builtin_amdgcn_raw_buffer_load_b32(rs, HS_GO + b * 16, 0, AUX_SC1); }
__device__ __forceinline__ void hello(int wv, rsrc_t rs, int b, unsigned t) {
  if (wv == 0) {
    st32(rs, lane_id() == 0 ? HS_HI + b * 8 : OOB, t + 1u);
    LDS_U32(lds_base() + L_HS) = t + 1u;
  }
}
__device__ __forceinline__ int go_off(int b) {
  const int l = lane_id() & 3;
  return l == 0 ? HS_GO + b * 16 : (l == 3 ? HS_ASK + (b & 127) * 16 : HS_PAY + (b & 31) * 32 + l * 16 - 16);
}
__device__ __forceinline__ Go wait_go(int wv, unsigned char* ws, int b, v4u rep, v4u m) {
  const v4u rs = make_rs(ws, (unsigned)OFF_END);
  const int l = lane_id();
  const unsigned a = __builtin_amdgcn_readfirstlane(LDS_U32(lds_base() + L_HS));
  for (unsigned spins = 0; spins < (1u << 18); ++spins) {
    if (spins || b >= 128) {
      VM_LD128(m, go_off(b), 0, 0);
      VM_WAIT1(0, m);
    }
    const unsigned n = __builtin_amdgcn_readlane(m.y, 0);
    if (__builtin_amdgcn_readlane(m.x, 0) == a && __builtin_amdgcn_readlane(m.x, 1) == n && __builtin_amdgcn_readlane(m.x, 2) == n) break;
    const unsigned t = (wv & 2) ? ((wv & 1) ? m.w : m.z) : ((wv & 1) ? m.y : m.x);
    const bool again = b >= 128 && l == 3 && t != rep.x;
    rep.x = t;
    VM_ST128(rep, again ? HS_CAL - 128 * 64 + b * 64 + wv * 16 : OOB, 0);
    if (spins) __builtin_amdgcn_s_sleep(6);
  }
  VM_ST32(m.z, (wv == 0 && l == 0) ? HS_HI + b * 8 + 4 : OOB, 0);
  if (b >= 128) { PH(1) }
  const auto u64 = [](unsigned lo, int i, unsigned hi, int k) {
    return (unsigned)__builtin_amdgcn_readlane(lo, i) | (unsigned long long)(unsigned)__builtin_amdgcn_readlane(hi, k) << 32;
  };
  Go g;
  g.pool.sel = u64(m.y, 1, m.z, 1); g.pool.s3 = u64(m.w, 1, m.w, 2); g.pool.ysel = u64(m.y, 2, m.z, 2);
  g.pool.base = __builtin_amdgcn_readfirstlane(pool_base(ws));
  g.rot = __builtin_amdgcn_readlane(m.w, 0) & 7;
  return g;
}
#define LDSTAMP(n)
#define LDX
#define LDC
__device__ __forceinline__ v4u load_rows(v32u (&w)[10], int kind, const u16* A, const u16* B, const u16* x, int h, int wv, unsigned slot, unsigned char* ws, int b, bool first) {
  unsigned tr;
  const int l = lane_id(), i = l & 15, g = l >> 4;
  const v4u ra = make_rs(A, 640u * 8192u), rb = make_rs(B, 640u * 8192u), rs = make_rs(ws, (unsigned)OFF_END), rx = make_rs(x, S_ * D_ * 2);
  v8u ad, ex;
  const bool pj = kind == 1 || kind == 2;
  const int wr = 4 * wv + g, rr = 4 * (pj ? g >> 1 : g) + (i >> 2), gt = pj ? g & 1 : 0;
  ad[0] = wr * 8192 + h * 256 + i * 16;
  ad[1] = (h * 128 + wr) * 1280 + i * 16;
  ad[2] = slot + rr * 256 + ((2 * wv + gt) ^ (rr & 7)) * 32 + (i & 3) * 8;
  ad[3] = pj ? ad[2] + 2048u : ad[2] ^ 32u;
  ad[4] = slot + wr * 256 + ((i >> 1) ^ (wr & 7)) * 32 + (i & 1) * 16;
  ad[5] = slot + wr * 328 + ((i >> 2) * 2 + (i & 1)) * 8 + ((i >> 1) & 1) * 4;
  ad[6] = slot + (kind == 3 ? (16 * (g & 1) + 4 * (g >> 1) + ((i >> 2) & 1)) * 328 + (i >> 3) * 8 : rr * 328 + gt * 8) + (i & 3) * 10560 + wv * 80;
  ad[7] = ad[4] + 65536u;
  const unsigned im = lds_base() + PJ_X + (wv >> 1) * PJ_BUF;
  const bool hi = first && wv == 0;
  ex[0] = go_off(b);
  ex[1] = hi ? HS_GO + b * 16 : OOB;
  ex[2] = hi && l == 0 ? HS_HI + b * 8 : OOB;
  ex[3] = lds_base() + L_HS + (hi ? 0 : 4);
  ex[4] = (wv * 16 + (l >> 3)) * 1280 + (l & 7) * 16;
  ex[5] = im + ((wv & 1) * KT + ((l >> 2) & 1)) * 1024 + ((l & 1) * 32 + (l >> 3)) * 16 + (l & 2) * 4;
  ex[6] = im + 40960 + ((wv & 1) * 16 + (l >> 3)) * 48 + min(l & 7, 5) * 8;
  ex[7] = (l & 7) ? 0u : ~0u;
  v4u m;
#define LD_OPS                                                                                                                                       \
  : "={a[0:31]}"(w[0]), "={a[32:63]}"(w[1]), "={a[64:95]}"(w[2]), "={a[96:127]}"(w[3]), "={a[128:159]}"(w[4]), "={a[160:191]}"(w[5]),                \
    "={a[192:223]}"(w[6]), "={a[224:255]}"(w[7]), "={v[192:223]}"(w[8]), "={v[224:255]}"(w[9]), "={v[28:31]}"(m) LDX                                 \
  : "{v[16:23]}"(ad), "{v[112:119]}"(ex), [ra] "s"(ra), [rb] "s"(rb), [rw] "s"(rs), [rx] "s"(rx), [p0] "s"(wv < 3 ? 3 * wv : 8),                     \
    "{v27}"(lds_base() + L_HS + 1024 + wv * 256 + (l >> 5) * 128)                                                                                   \
  : LDP_CLOB LDC, "scc", "memory"
  if (kind == 0) asm volatile(LDP_ASM LD_OPS);
  else if (kind == 1) asm volatile(LDK_ASM LD_OPS);
  else if (kind == 2) asm volatile(LDV_ASM LD_OPS);
  else asm volatile(LDY_ASM LD_OPS);
  return m;
}
__device__ __forceinline__ v4u prep_jobs(int wv, unsigned char* ws, rsrc_t rs, const u16* __restrict__ x, const u16* __restrict__ mem0, int j) {
  const int lane = lane_id(), g = lane >> 4, r = lane & 15, tid = wv * 64 + lane, pb = pool_base(ws);
  const int xc = (int)xcc_id(), u = (j >> 3) * 4 + (xc & 3), page = xc < 4 ? u >> 2 : 16 + u % NYP;
  const int cb = pb + page * PAGE + M_CAL + (xc < 4 ? wv * REG + xc * 512 : 3 * REG - u / NYP * WIN + wv * 512);
  stc(rs, lane < 32 ? cb + (lane >> 3) * 8192 + (lane & 7) * 16 : OOB, v4u{1u, 2u, 3u, 4u});
  const unsigned char* p = ws + __builtin_amdgcn_readfirstlane(cb);
  unsigned t0, t1, t2, t3, t4;
  asm volatile("s_load_dword %0, %4, 0x80\n\ts_load_dword %1, %4, 0x2080\n\ts_load_dword %2, %4, 0x4080\n\ts_load_dword %3, %4, 0x6080"
               : "={s40}"(t0), "={s41}"(t1), "={s42}"(t2), "={s43}"(t3) : "s"(p) : "memory");
  const unsigned tg = hello_load(rs, 128 + j);
  v2u c[5][2];
  unsigned t[4];
#pragma unroll
  for (int q = 0; q < 5; ++q) {
    const u16* xr = x + (size_t)(j * 16 + r) * D_ + 32 * (5 * wv + q) + 4 * g;
    c[q][0] = *(const v2u*)xr; c[q][1] = *(const v2u*)(xr + 16);
  }
#pragma unroll
  for (int k = 0; k < 4; ++k) t[k] = *(const unsigned*)(mem0 + (size_t)((tid >> 1) & 7) * D_ + colmap((min(j >> 3, 4) * 8 + ((tid >> 4) & 7)) * 2 + (tid & 1), 2 * k));
  __builtin_amdgcn_sched_barrier(0);
  const v4u ones = {0xFFFFFFFFu, 0xFFFFFFFFu, 0xFFFFFFFFu, 0xFFFFFFFFu};
  const int o = (j * 256 + tid) * 16;
  const bool cal = (o & 0x79800) == 0x79800;
  stc(rs, (int)OFF_HO + o, ones);
  stc(rs, o + REG < (int)HO_FIRST ? (int)OFF_HO + REG + o : OOB, ones);
  stc(rs, (cal || (o >= W_MEM && o < W_MISC)) ? OOB : pb + (NSEG - 1) * REG + o, ones);
  stc(rs, cal ? OOB : pb + o, ones);
  stc(rs, pb + P_Y + o, ones);
  stc(rs, o < 8 * 16384 ? pb + P_Y + REG + o : OOB, ones);
  hello(wv, rs, 128 + j, tg);
  asm volatile("" ::"v"(tg));
  __builtin_amdgcn_sched_barrier(0);
#define CAL_T(r, o) "s_load_dword " r ", %5, " o "\n\ts_waitcnt lgkmcnt(0)\n\ts_memtime s[46:47]\n\ts_waitcnt lgkmcnt(0)\n\ts_mov_b32 " r ", s46\n\t"
  asm volatile(CAL_T("%0", "0x80") CAL_T("%1", "0") CAL_T("%2", "0x2000") CAL_T("%3", "0x4000") CAL_T("%4", "0x6000")
               : "+{s40}"(t0), "+{s41}"(t1), "+{s42}"(t2), "+{s43}"(t3), "={s44}"(t4) : "s"(p) : "s46", "s47", "memory");
  PH(16)
  __builtin_amdgcn_sched_barrier(0);
  f32x2 st = {0.f, 0.f};
#pragma unroll
  for (int q = 0; q < 5; ++q) {
    const v4u f = v4u{c[q][0].x, c[q][0].y, c[q][1].x, c[q][1].y};
    stc(rs, (int)OFF_XR + ((j * KT + 5 * wv + q) * 64 + lane) * 16, f);
    st += frag_stat(f);
  }
  stc(rs, j < 40 ? pb + (NSEG - 1) * REG + W_MEM + o : OOB, v4u{nosent(t[0]), nosent(t[1]), nosent(t[2]), nosent(t[3])});
  st = rows_sum(st);
  __builtin_amdgcn_raw_buffer_store_b64(__builtin_bit_cast(v2u, g ? f32x2{0.f, 0.f} : st), rs,
                                        (g == 0 || (g == 1 && wv == 0)) ? (int)OFF_ST + ((j * 16 + r) * 6 + (g ? 4 : wv)) * 8 : OOB, 0, AUX_SC1);
  const unsigned ask = __builtin_amdgcn_raw_buffer_load_b32(rs, HS_ASK + j * 16 + wv * 4, 0, AUX_SC1);
  const unsigned b0[4] = {min(t1 - t0, 9999u), min(t2 - t1, 9999u), min(t3 - t2, 9999u), min(t4 - t3, 9999u)}, sh = ((xc ^ wv) & 1) * 16;
  const v4u rep = {ask, (unsigned)page, b0[0] << sh | b0[2] << (16 - sh), b0[1] << sh | b0[3] << (16 - sh)};
  asm volatile("s_waitcnt vmcnt(0)" ::: "memory");
  stc(rs, lane == 0 ? HS_CAL + j * 64 + wv * 16 : OOB, rep);
  LDSBAR();
  PH(18)
  return rep;
}
__device__ __forceinline__ int wave_isum(int v) {
  asm volatile("s_nop 1\n\tv_add_u32_dpp %0, %0, %0 quad_perm:[1,0,3,2] row_mask:0xf bank_mask:0xf\n\t"
               "s_nop 1\n\tv_add_u32_dpp %0, %0, %0 quad_perm:[2,3,0,1] row_mask:0xf bank_mask:0xf\n\t"
               "s_nop 1\n\tv_add_u32_dpp %0, %0, %0 row_half_mirror row_mask:0xf bank_mask:0xf\n\t"
               "s_nop 1\n\tv_add_u32_dpp %0, %0, %0 row_mirror row_mask:0xf bank_mask:0xf\n\ts_nop 0"
               : "+v"(v));
  return (__builtin_amdgcn_readlane(v, 0) + __builtin_amdgcn_readlane(v, 16)) + (__builtin_amdgcn_readlane(v, 32) + __builtin_amdgcn_readlane(v, 48));
}
__device__ __forceinline__ Go lead(int wv, unsigned char* ws, const u16* x, const u16* mem0, int rot) {
  const v4u rs = make_rs(ws, (unsigned)OFF_END);
  const int t = wv * 64 + lane_id();
  int* acc = (int*)(dyn_lds + L_HS + 128);
#define VM_LD64(dst, off) asm volatile("buffer_load_dwordx2 %0, %1, %2, 0 offen sc1" : "=v"(dst) : "v"(off), "s"(rs) : "memory")
  v4u c0, c1;
  v2u hi;
  unsigned old, n = 0;
  VM_LD128(c0, HS_CAL + t * 32, 0, 0);
  VM_LD128(c1, HS_CAL + t * 32, 0, 16);
  VM_LD64(hi, HS_HI + t * 8);
  VM_LD32(old, HS_PAY + (t & 63) * 16, 0);
  asm volatile("s_waitcnt vmcnt(0)" : "+v"(c0), "+v"(c1), "+v"(hi), "+v"(old)::"memory");
  const unsigned r0 = c0.x + 1u, r1 = c1.x + 1u, q = hi.y + 1u;
  __builtin_amdgcn_raw_buffer_store_b64(v2u{r0, r1}, ws_rsrc(ws), HS_ASK + t * 8, 0, AUX_SC1);
  while (__any(old == n)) ++n;
  if (t < 192) acc[t] = 9999;
  prep_jobs(wv, ws, ws_rsrc(ws), x, mem0, 127);
  for (unsigned spins = 0; spins < (1u << 18); ++spins) {
    VM_LD128(c0, HS_CAL + t * 32, 0, 0);
    VM_LD128(c1, HS_CAL + t * 32, 0, 16);
    VM_LD64(hi, HS_HI + t * 8);
    asm volatile("s_waitcnt vmcnt(0)" : "+v"(c0), "+v"(c1), "+v"(hi)::"memory");
    if (!__any(t < 254 && (c0.x != r0 || c1.x != r1))) break;
    __builtin_amdgcn_s_sleep(2);
  }
  if (t < 254) {
    int *a0 = acc + min(c0.y, 47u) * 4, *a1 = acc + min(c1.y, 47u) * 4;
    atomicMin(a0, c0.z & 0xFFFF); atomicMin(a0 + 1, c0.z >> 16); atomicMin(a0 + 2, c0.w & 0xFFFF); atomicMin(a0 + 3, c0.w >> 16);
    atomicMin(a1, c1.z & 0xFFFF); atomicMin(a1 + 1, c1.z >> 16); atomicMin(a1 + 2, c1.w & 0xFFFF); atomicMin(a1 + 3, c1.w >> 16);
  }
  __syncthreads();
  PH(19)
  const int s = lane_id(), fx = (s & 4) ? -1 : 1, fy = ((s * 43 >> 7) & 1) ? -1 : 1;
  const v4i u = ((const v4i*)acc)[s >> 2], v = ((const v4i*)acc)[16 + (s * 43 >> 7)];
  int ax = min(u.x, u.y) - min(u.z, u.w), yx = min(v.z, v.w) - min(v.x, v.y);
  const int n0 = ax > 0 ? u.z : u.x, n1 = ax > 0 ? u.w : u.y, lu = min(n0, n1), lv = min(min(v.x, v.y), min(v.z, v.w)), near = 110 - wave_imax(-min(lu, lv));
  const int sa = (lu < near && abs(ax) > 60) ? (ax > 0 ? fx : -fx) : 0, sy = (lv < near && abs(yx) > 60) ? (yx > 0 ? fy : -fy) : 0;
  const int g = wave_isum(sa + sy), m = wave_isum(abs(sa) + abs(sy));
  const bool with = sa * g > 0;
  const int bs = wave_imax(with ? -n1 : -9999) - wave_imax(with ? -n0 : -9999);
  int b = n0 - n1;
  if (abs(g) * 8 > m * 7) {
    if (sa * g >= 0) { ax = g * fx; b = bs; }
    if (sy == 0) yx = g * fy;
  }
  Go go;
  go.pool.sel = __ballot(ax > 0);
  go.pool.s3 = __ballot(b > 0) ^ 0xAAAAAAAAAAAAAAAAull;
  go.pool.ysel = __ballot(yx > 0);
  go.pool.base = __builtin_amdgcn_readfirstlane(pool_base(ws));
  go.rot = rot;
  PH(20)
  {
    const unsigned long long v = (s & 1) ? go.pool.ysel : go.pool.sel;
    VM_ST128((v4u{n, (unsigned)v, (unsigned)(v >> 32), (unsigned)(go.pool.s3 >> (s & 1) * 32)}), wv == 0 ? HS_PAY + s * 16 : OOB, 0);
  }
  for (unsigned spins = 0; spins < (1u << 18); ++spins) {
    VM_ST128((v4u{hi.x, n, q, (unsigned)rot}), HS_GO + t * 16, 0);
    const unsigned sent = hi.x;
    do {
      __builtin_amdgcn_s_sleep(8);
      VM_LD64(hi, HS_HI + t * 8);
      VM_WAIT1(0, hi);
    } while (!__any(hi.x != sent) && __any(t < 255 && hi.y != q) && ++spins < (1u << 18));
    if (!__any(t < 255 && hi.y != q)) break;
  }
  return go;
}
__device__ __forceinline__ void late_jobs(int wv_, unsigned char* ws, rsrc_t rs, const u16* __restrict__ wr, const u16* __restrict__ wi, int j) {
  const int lane = lane_id(), wv = wv_, g = lane >> 4, r = lane & 15;
  unsigned* t32 = (unsigned*)dyn_lds;
  for (int o = (int)HO_FIRST + (j * 256 + wv * 64 + lane) * 16; o < 2 * (int)HO_LAYER; o += NLATE * 4096)
    stc(rs, (int)OFF_HO + o, v4u{0xFFFFFFFFu, 0xFFFFFFFFu, 0xFFFFFFFFu, 0xFFFFFFFFu});
#pragma unroll 1
  for (int fi = j * 4 + wv; fi < 20 * NFK; fi += NLATE * 4) {
    v4u v;
#pragma unroll
    for (int q = 0; q < 4; ++q) {
      const int e = fi % NFK, d = 16 * (fi / NFK) + r, f = 4 * (32 * (e >> 2) + 8 * q + 2 * (e & 2) + g) + 2 * (e & 1);
      const float sc = f ? 2.f : 1.f;
      v[q] = pk(cos640(dft_idx(f, d)) * sc, cos640(dft_idx(f + 1, d)) * sc);
    }
    stc(rs, (int)OFF_INV + (fi * 64 + lane) * 16, v);
  }
#pragma unroll 1
  for (int b = j; b < 32 * 6; b += NLATE) {
    const int m0 = (b & 31) * 64, k0 = (b >> 5) * 64, n0 = (b & 24) * 2 + (b & 7) * 256;
#pragma unroll
    for (int q = 0; q < 16; ++q) {
      const size_t i = (size_t)(n0 + q + 64 * wv) * 321 + min(k0 + lane, 320);
      t32[(q * 4 + wv) * 65 + lane] = (unsigned)wr[i] | ((unsigned)wi[i] << 16);
    }
    __syncthreads();
#pragma unroll
    for (int q = 0; q < 16; ++q) {
      const int k = q * 4 + wv;
      st32(rs, k0 + k < 321 ? (int)OFF_WT + ((k0 + k) * S_ + m0 + lane) * 4 : OOB, t32[lane * 65 + k]);
    }
    __syncthreads();
  }
  dep_signal(wv_, ws, cnt_off(0, CNT_IDFT, 0));
}
__device__ __forceinline__ void late_wait(int wv_, unsigned char* ws) {
  if (TIDX == 0) {
    for (unsigned spins = 0; spins < (1u << 17); ++spins) {
      if (__hip_atomic_load((gu32*)(ws + cnt_off(0, CNT_IDFT, 0)), __ATOMIC_RELAXED, __HIP_MEMORY_SCOPE_AGENT) == NLATE - 1) break;
      __builtin_amdgcn_s_sleep(16);
    }
  }
  __syncthreads();
}
#define FPACE 1200
#define FILLK 12
#define STR_(x) #x
#define STR(x) STR_(x)
__device__ __forceinline__ void fill_run(int wv, unsigned char* ws, const Pool& pool, int f, int n0, int n1) {
  const v4u rs = make_rs(ws, (unsigned)OFF_END), ones = {~0u, ~0u, ~0u, ~0u};
  const int lane = lane_id();
  unsigned due = rtc() * 8u + (unsigned)(f * 4 + wv) * (FPACE / 576);
  int fo = OOB;
#pragma unroll 1
  for (int n = n0; n <= n1; ++n) {
    const int t = n & 63, rg = pool.region(t), yr = pool.base + P_Y + y_region(pool.ysel, t) - 32 * 16384;
    if (n == NSEG) { PH(44) late_wait(wv, ws); }
    if (n == NSEG + FILLK + 2) { PH(45) }
    if (n == 127) { PH(46) }
    if ((n == NSEG + 1 || n == NSEG + FILLK + 1) && n < n1) {
      unsigned c;
      for (unsigned spins = 0; spins < SPIN_CAP; ++spins) {
        VM_LD32(c, cnt_off(0, CNT_OUT, min(lane, t > 1 ? NSEG - 1 : FILLK + 1)), 0);
        VM_WAIT1(0, c);
        if (!__any(c != 9u)) break;
        __builtin_amdgcn_s_sleep(32);
      }
    }
#pragma unroll 1
    for (int k = 0; k < 144 / NFILL; ++k) {
      const int i = f * 4 + wv + 4 * NFILL * k;
      const bool on = t && n < n1 && (t < NSEG - 1 || i < 160 || i >= 240);
      const int so = (i < 256 ? rg : yr) + (i >> 3) * 16384 + (i & 7) * 1024;
      if (on) {
        unsigned now;
        while ((int)(due - (now = rtc() * 8u)) > 0) __builtin_amdgcn_s_sleep(1);
        due = max((int)(due - now), -FPACE * NFILL / 144) + now + FPACE * NFILL / 144;
      }
      VM_ST128(ones, on ? lane * 16 : OOB, so);
      if (k == FLAG - 1) {
        asm volatile("s_waitcnt vmcnt(" STR(FLAG) ")" ::: "memory");
        VM_ST32(1u, fo, 0);
      }
    }
    fo = t && lane < (t == NSEG - 1 ? 2 : 1) ? kqf_off(n >> 6, t - 1 + lane, f) + 16 + wv * 4 : OOB;
  }
}
#define TROB (24 * 256 + 16)
#define TC2 30
#define TVIS 32
#define TDLT 170
#define TM1 88
#define SPARE_US 200
#define SPARE_NAP 127
#define TM2 60
__global__ void __launch_bounds__(256, 1) k_main(unsigned char* ws, const u16* x, const u16* mem0, const u16* Wq, const u16* Wk, const u16* Wv, const u16* Wo,
                                                const u16* wr, const u16* wi, u16* out, long nlayer
                                                ) {
  v4u* smem = (v4u*)dyn_lds;
  const int wv_ = __builtin_amdgcn_readfirstlane(threadIdx.x >> 6);
  const RecTune tn = {TM1, TM2, 4, TROB, TC2, TVIS, TDLT, 1};
  unsigned long long* dbg = nullptr;
#define RSTAMP_(i)
  const rsrc_t rs = ws_rsrc(ws);
  PH(0)
  const int b = blockIdx.x, tt = b >> 3;
  const unsigned t00 = rtc();
  int rot = (int)(xcc_id() - b) & 7, lpl, ljb = -1;
  if (tt < 16) {
    v32u w[10];
    Go go;
    const unsigned lds0 = lds_base();
#pragma unroll 1
    for (int it = 0;; ++it) {
      const int p = (b + rot) & 7, h = (p & 3) * 8 + (tt & 7);
      const bool v = (tt & 8) != 0;
      const v4u m = load_rows(w, p < 4 ? (v ? 3 : 0) : (v ? 2 : 1), v ? Wv : Wq, v ? Wo : Wk, x, h, wv_, lds0 + (p < 4 ? 0 : PJ_X + 2 * PJ_BUF), ws, b, it == 0);
      PH(21 + (p >> 2))
      go = wait_go(wv_, ws, b, m, m);
      if (go.rot == rot || it) break;
      rot = go.rot;
    }
    Pool pool = go.pool;
    const int pl = (b + rot) & 7, role = pl < 4 ? (tt & 8) * 4 + pl * 8 + (tt & 7) : pl * 32 + tt;
    const int hd = (pl - 4) * 8 + (tt & 7);
#pragma unroll 1
    for (int layer = 0; layer < nlayer; ++layer) {
      int ro = role, xs = pl, wq = wv_, h2 = hd | (tt & 8) << 8;
      asm volatile("" : "+s"(ro), "+s"(xs), "+s"(wq), "+s"(h2), "+s"(ws), "+s"(pool.base));
      if (layer > 0) {
        const int jb = ro < 64 ? ro + 57 : (xs - 4) * 16 + (ro & 31);
        late_wait(wq, ws);
        if (ro < 64 || jb < 57) {
          job_col(wq, ws, ws_rsrc(ws), lds_base(), jb, fan_off(0, 1, 0), fan_off(1, 0, 0), false);
          __syncthreads();
          RSTAMP_(5)
          PH(7)
        } else fan_wait<64, 20>(wq, ws, fan_off(0, 1, 0), jb);
      }
      if (ro < 32) { rec_p1(wq, w, ws, pool, layer, ro, xs, tn, dbg); PH(4 + layer * 6) }
      else if (ro < 64) rec_pv(wq, w, ws, pool, layer, ro - 32, xs, tn, dbg);
      else {
        if (h2 >> 8) job_proj<true>(wq, w, ws, pool, layer, h2 & 31);
        else job_proj<false>(wq, w, ws, pool, layer, h2 & 31);
        RSTAMP_(2)
      }
    }
    lpl = pl;
    ljb = role < 64 ? role + 57 : (pl - 4) * 16 + (role & 31);
    if (role >= 64 && ljb >= 57) ljb = -1;
  } else {
  const Go go = b == 255 ? lead(wv_, ws, x, mem0, rot) : wait_go(wv_, ws, b, prep_jobs(wv_, ws, rs, x, mem0, b - 128), v4u{0u, 0u, 0u, 0u});
  Pool pool = go.pool;
  const int pl = (b + go.rot) & 7;
  const int role = pl < 4 ? (tt < 26 ? 64 + pl * 10 + tt - 16 : 104 + pl * 6 + tt - 26) : pl * 32 + tt;
  if (tt < (pl < 4 ? 32 - NFILL / 4 : 27)) late_jobs(wv_, ws, rs, wr, wi, pl < 4 ? 44 + pl * (16 - NFILL / 4) + tt - 16 : (pl - 4) * 11 + tt - 16);
  else if (pl < 4) fill_run(wv_, ws, pool, (31 - tt) * 4 + pl, 1, (int)nlayer * NSEG);
  PH(23)
  lpl = pl;
  if (role >= 104 && role < 128) {
    while (rtc() - t00 < 100u * SPARE_US) __builtin_amdgcn_s_sleep(127);
    late_wait(wv_, ws);
    prog_prewait(wv_, ws, kqf_off((int)nlayer - 1, NSEG - 1, ((pl + 1) & 3) * 8 + tt - 26));
    __syncthreads();
    __builtin_amdgcn_s_sleep(SPARE_NAP);
  } else if (role >= 64 && role < 104) {
#pragma unroll 1
    for (int layer = 0; layer < nlayer; ++layer) {
      if (layer > 0) {
        late_wait(wv_, ws);
        job_col(wv_, ws, rs, lds_base(), role + 57, fan_off(0, 1, 0), fan_off(1, 0, 0), false);
        __syncthreads();
        PH(7)
      }
      ((unsigned*)(dyn_lds + L_T))[lane_id() & 15] = 0u;
      __syncthreads();
      if (wv_ == 3) rec_fin(ws, pool, layer, role - 64, pl, tn, dbg);
      else if (wv_ == 0) rec_sched(ws, pool, layer, role - 64, pl, tn, dbg);
      __syncthreads();
    }
    ljb = role + 57;
  } else {
#pragma unroll 1
  for (int layer = 0; layer < nlayer; ++layer) {
    int wq = wv_;
    asm volatile("" : "+s"(wq));
    if (layer > 0) late_wait(wq, ws);
    if (layer > 0) job_idft(wq, ws, rs, smem, ((tt - 16) >> 1) * 4 + pl - 4, tt & 1, dbg + 60000 + role * 8);
    RSTAMP_(2)
    __syncthreads();
    if (tt >= 27) {
      job_dft(wq, ws, layer, tt - 27, pl - 4);
      __syncthreads();
      if (layer) { PH(14) }
    } else {
#pragma unroll 1
      for (int j = (tt - 16) * 4 + pl - 4; j < 640 - NFAST * 5; j += NOUT) {
        const int u = j / 5 - NFAST, s = u < 0 ? j / 5 : NFAST + (u >> 1);
        flags32_wait(wq, ws, kqf_off(layer, s, 32));
        job_out(wq, ws, rs, pool.ysel, layer, s, j % 5, u < 0 ? -1 : u & 1);
      }
    }
    RSTAMP_(3)
    PH((tt >= 27 ? 6 : 5) + layer * 6)
  }
  }
  }
  int wq = wv_, tl = tt;
  asm volatile("" : "+s"(wq), "+s"(ljb), "+s"(lpl), "+s"(tl), "+s"(ws));
  job_last(wq, ws, out, ljb, lpl * 16 + (tl >> 1), tl & 1);
}

extern "C" void kernel_launch(void* const* d_in, const int* in_sizes, int n_in, void* d_out, int out_size, void* d_ws, size_t ws_size,
                              hipStream_t stream) {
  const u16* x = (const u16*)d_in[0];
  const u16* mem0 = (const u16*)d_in[1];
  const u16* Wq = (const u16*)d_in[2];
  const u16* Wk = (const u16*)d_in[3];
  const u16* Wv = (const u16*)d_in[4];
  const u16* Wo = (const u16*)d_in[5];
  const u16* wr = (const u16*)d_in[8];
  const u16* wi = (const u16*)d_in[9];
  unsigned char* ws = (unsigned char*)d_ws;
  u16* out = (u16*)d_out;
  (void)hipFuncSetAttribute((const void*)k_main, hipFuncAttributeMaxDynamicSharedMemorySize, MAIN_LDS);
  k_main<<<256, 256, MAIN_LDS, stream>>>(ws, x, mem0, Wq, Wk, Wv, Wo, wr, wi, out, 2);
}
```

```cpp
#include <hip/hip_runtime.h>
#include <hip/hip_bf16.h>
#include <stdint.h>

typedef unsigned v4u __attribute__((ext_vector_type(4)));
typedef unsigned v2u __attribute__((ext_vector_type(2)));
typedef float f32x4 __attribute__((ext_vector_type(4)));
typedef float f32x2 __attribute__((ext_vector_type(2)));
typedef __bf16 bf16x8 __attribute__((ext_vector_type(8)));
typedef __bf16 bf16x2 __attribute__((ext_vector_type(2)));
typedef unsigned short u16;

constexpr int S_ = 2048, D_ = 640, NSEG = 64, KT = 20, RT = 128;
constexpr int NFT = 40;
constexpr int NFK = 20;
constexpr int NCOL = 161;
constexpr int WIN = 32768, REG = 16 * WIN;
constexpr int W_MEM = 10 * WIN, W_MISC = 15 * WIN;
__host__ __device__ constexpr int copy_off(int c) { return (c & 3) * 2048 + (c >> 2) * 16384; }
constexpr int M_STAMP = W_MISC + copy_off(4), M_TM = M_STAMP + 40 * 64, M_CAL = W_MISC + copy_off(3);
constexpr int PAGE = 4 * REG, NYP = 22, P_Y = NSEG * REG;
constexpr size_t SZ_POOL = (size_t)P_Y + (size_t)(NYP + 1) * PAGE;

constexpr size_t OFF_CNT = 0;
constexpr int CNT_IDFT = 0, CNT_OUT = 1;
constexpr size_t OFF_PROG = OFF_CNT + (size_t)3 * 32 * 128;
constexpr size_t OFF_KQF = OFF_PROG + (size_t)NSEG * 4096;
#define FNAP 6
#define FSP 16
#define FNG 32
constexpr int FAN_G = (161 * FSP + 127) / 128 * 128 > 3840 ? (161 * FSP + 127) / 128 * 128 : 3840;
constexpr size_t OFF_FAN = OFF_KQF + (size_t)NSEG * 4096;
constexpr size_t HO_LAYER = OFF_FAN + (size_t)2 * FNG * FAN_G;
constexpr size_t OFF_HO = SZ_POOL;
constexpr size_t HO_FIRST = OFF_FAN + (size_t)FNG * FAN_G;
constexpr size_t OFF_HS = OFF_HO + 2 * HO_LAYER;
constexpr int HS_GO = (int)OFF_HS, HS_PAY = HS_GO + 4096, HS_ASK = HS_PAY + 1024, HS_HI = HS_ASK + 2048, HS_CAL = HS_HI + 2048;
__host__ __device__ constexpr int prog_off(int layer, int rb, int slot) { return (int)(OFF_HO + layer * HO_LAYER + OFF_PROG) + (rb * 64 + slot) * 64; }
__host__ __device__ constexpr int kqf_off(int layer, int sg, int h) { return (int)(OFF_HO + layer * HO_LAYER + OFF_KQF) + (sg * 64 + h) * 64; }
__host__ __device__ constexpr int cnt_off(int layer, int kind, int rb) { return (int)(OFF_HO + layer * HO_LAYER + OFF_CNT) + (kind * 32 + rb) * 128; }
__host__ __device__ constexpr int fan_off(int layer, int set, int i) { return (int)(OFF_HO + layer * HO_LAYER + OFF_FAN) + ((set ^ layer ^ 1) * FNG + i) * FAN_G; }
constexpr size_t OFF_XR = (OFF_HS + 65536 + 4095) / 4096 * 4096;
constexpr size_t OFF_ST = OFF_XR + (size_t)RT * KT * 1024;
constexpr size_t OFF_QF = OFF_ST + (size_t)S_ * 48;
constexpr size_t OFF_KF = OFF_QF + (size_t)32 * RT * 4 * 1024;
constexpr size_t OFF_UF = OFF_KF + (size_t)32 * RT * 4 * 1024;
constexpr size_t OFF_PXF = OFF_UF + (size_t)NSEG * 80 * 16 * 1024;
constexpr size_t OFF_PXM = OFF_PXF + (size_t)NSEG * 32 * 2 * 1024;
constexpr size_t OFF_INV = OFF_PXM + (size_t)NSEG * 8 * 2 * 1024;
constexpr size_t OFF_X1T = OFF_INV + (size_t)20 * NFK * 1024;
#define ZPAD 32
#define XPAD 32
constexpr int XS = S_ + XPAD, ZS = S_ + ZPAD;
constexpr size_t OFF_ZT = OFF_X1T + (size_t)160 * XS * 8;
constexpr size_t OFF_WT = OFF_ZT + (size_t)161 * ZS * 8;
constexpr size_t OFF_DBG = OFF_WT + (size_t)321 * S_ * 4;
constexpr size_t OFF_END = OFF_DBG + (size_t)1048576;
static_assert(OFF_END <= ((size_t)256 << 20), "workspace too large");

constexpr int AUX_SC1 = 16;
#define PH(k)
#define PHV(k, v)
#define PHT(n)

__device__ __forceinline__ f32x4 mfma(v4u a, v4u b, f32x4 c) {
  return __builtin_amdgcn_mfma_f32_16x16x32_bf16(__builtin_bit_cast(bf16x8, a), __builtin_bit_cast(bf16x8, b), c, 0, 0, 0);
}
__device__ __forceinline__ unsigned pk(float a, float b) {
  f32x2 v = {a, b};
  return __builtin_bit_cast(unsigned, __builtin_convertvector(v, bf16x2));
}
__device__ __forceinline__ float lo_f(unsigned u) { return __builtin_bit_cast(float, u << 16); }
__device__ __forceinline__ float hi_f(unsigned u) { return __builtin_bit_cast(float, u & 0xFFFF0000u); }
__device__ __forceinline__ float bfr(float x) { return lo_f(pk(x, x)); }
__device__ __forceinline__ v4u pk8(f32x4 a, f32x4 b) { return v4u{pk(a.x, a.y), pk(a.z, a.w), pk(b.x, b.y), pk(b.z, b.w)}; }
__device__ __forceinline__ int sigma(int g, int jj) { return 16 * (jj >> 2) + 4 * g + (jj & 3); }
__device__ __forceinline__ int colmap(int c8, int jj) {
  const int p = c8 >> 3, ga = (c8 >> 1) & 3, half = c8 & 1;
  return 32 * (2 * p + (ga >> 1)) + 16 * ((jj >> 1) & 1) + 4 * (2 * (ga & 1) + (jj >> 2)) + 2 * half + (jj & 1);
}
__device__ __forceinline__ int pool_base(const void* ws) { return (int)((0u - (unsigned)(uintptr_t)ws) & (unsigned)(PAGE - 1)); }
__device__ __forceinline__ unsigned xcc_id() { return (unsigned)__builtin_amdgcn_s_getreg(20 | (3 << 11)) & 7u; }
__device__ __forceinline__ float row_sum(float v) {
  float w;
  asm volatile("v_mov_b32 %1, %0\n\ts_nop 1\n\tv_permlane16_swap_b32 %0, %1\n\ts_nop 1\n\tv_add_f32 %0, %0, %1\n\t"
      "v_mov_b32 %1, %0\n\ts_nop 1\n\tv_permlane32_swap_b32 %0, %1\n\ts_nop 1\n\tv_add_f32 %0, %0, %1" : "+v"(v), "=&v"(w));
  return v;
}
__device__ __forceinline__ unsigned nosent(unsigned x) { return x < 0xFFFFFFFEu ? x : 0xFFFFFFFEu; }

__device__ __forceinline__ f32x2 frag_stat(v4u c) {
  const float v[8] = {lo_f(c.x), hi_f(c.x), lo_f(c.y), hi_f(c.y), lo_f(c.z), hi_f(c.z), lo_f(c.w), hi_f(c.w)};
  float q = 0.f;
#pragma unroll
  for (int j = 0; j < 8; ++j) q = __builtin_fmaf(v[j], v[j], q);
  return f32x2{((v[0] + v[1]) + (v[2] + v[3])) + ((v[4] + v[5]) + (v[6] + v[7])), q};
}
__device__ __forceinline__ f32x2 rows_sum(f32x2 a) {
  return f32x2{row_sum(a.x), row_sum(a.y)};
}

typedef int v4i __attribute__((ext_vector_type(4)));
__device__ __forceinline__ int y_region(unsigned long long sel, int s) { return (s * 80 + (s * 43 >> 7) * 16 + (int)((sel >> s) & 1ull)) * 8192; }

__device__ __forceinline__ int lane_id() {
  int l;
  asm volatile("v_mbcnt_lo_u32_b32 %0, -1, 0\n\tv_mbcnt_hi_u32_b32 %0, -1, %0" : "=v"(l));
  return l;
}
#define TIDX (wv_ * 64 + lane_id())
typedef __amdgpu_buffer_rsrc_t rsrc_t;
__device__ __forceinline__ rsrc_t ws_rsrc(unsigned char* ws) { return __builtin_amdgcn_make_buffer_rsrc(ws, 0, (int)OFF_END, 0x00020000); }
__device__ __forceinline__ v4u ldc(rsrc_t rs, int off) { return __builtin_amdgcn_raw_buffer_load_b128(rs, off, 0, AUX_SC1); }
__device__ __forceinline__ void stc(rsrc_t rs, int off, v4u v) { __builtin_amdgcn_raw_buffer_store_b128(v, rs, off, 0, AUX_SC1); }

extern __shared__ __attribute__((aligned(16))) unsigned char dyn_lds[];
__device__ __forceinline__ unsigned lds_base() { return (unsigned)(uintptr_t)(__attribute__((address_space(3))) unsigned char*)dyn_lds; }
__device__ __forceinline__ unsigned rtc() { return (unsigned)__builtin_amdgcn_s_memrealtime(); }
__device__ __forceinline__ int wave_imax(int v) {
  asm volatile("s_nop 1\n\tv_max_i32_dpp %0, %0, %0 quad_perm:[1,0,3,2] row_mask:0xf bank_mask:0xf\n\t"
               "s_nop 1\n\tv_max_i32_dpp %0, %0, %0 quad_perm:[2,3,0,1] row_mask:0xf bank_mask:0xf\n\t"
               "s_nop 1\n\tv_max_i32_dpp %0, %0, %0 row_half_mirror row_mask:0xf bank_mask:0xf\n\t"
               "s_nop 1\n\tv_max_i32_dpp %0, %0, %0 row_mirror row_mask:0xf bank_mask:0xf\n\ts_nop 0"
               : "+v"(v));
  const int a = __builtin_amdgcn_readlane(v, 0), b = __builtin_amdgcn_readlane(v, 16), c = __builtin_amdgcn_readlane(v, 32),
            d = __builtin_amdgcn_readlane(v, 48);
  return max(max(a, b), max(c, d));
}

#define PWN 40
#define PWL 52
typedef __attribute__((address_space(1))) unsigned gu32;
__device__ __forceinline__ void prog_prewait(int wv_, unsigned char* ws, int off) {
  if (TIDX == 0) {
    gu32* p = (gu32*)(ws + off);
    for (unsigned spins = 0; spins < (1u << 16); ++spins) {
      if (__hip_atomic_load(p, __ATOMIC_RELAXED, __HIP_MEMORY_SCOPE_AGENT) != 0xFFFFFFFFu) break;
      __builtin_amdgcn_s_sleep(48);
    }
  }
}
constexpr int L_WHEN = 90112;
__device__ __forceinline__ void prog_wait(int wv_, unsigned char* ws, int off, bool lazy) {
  if (wv_ == 0) {
    const int l = lane_id();
    gu32* p0 = (gu32*)(ws + off + l * 64);
    unsigned a;
    for (unsigned spins = 0; spins < (1u << 17); ++spins) {
      a = __hip_atomic_load(p0, __ATOMIC_RELAXED, __HIP_MEMORY_SCOPE_AGENT);
      if (!__any(a == 0xFFFFFFFFu)) break;
      __builtin_amdgcn_s_sleep(8);
      if (lazy) __builtin_amdgcn_s_sleep(PWN);
    }
    if (l == 0) *(unsigned*)(dyn_lds + L_WHEN) = rtc() + ((int)(a - rtc() * 8u) >> 3);
  }
  __syncthreads();
}
__device__ __forceinline__ void flags32_wait(int wv_, unsigned char* ws, int off) {
  if (wv_ == 0) {
    const int l = lane_id();
    __attribute__((address_space(1))) unsigned long long* p0 = (__attribute__((address_space(1))) unsigned long long*)(ws + off + (l & 31) * 64 + (l >> 5) * 8);
    for (unsigned spins = 0; spins < (1u << 17); ++spins) {
      const unsigned long long v = __hip_atomic_load(p0, __ATOMIC_RELAXED, __HIP_MEMORY_SCOPE_AGENT);
      if (!__any(max((unsigned)v, (unsigned)(v >> 32)) == 0xFFFFFFFFu)) break;
      __builtin_amdgcn_s_sleep(16);
    }
  }
  __syncthreads();
}
__device__ __forceinline__ void dep_signal(int wv_, unsigned char* ws, int off, unsigned inc = 1u) {
  asm volatile("s_waitcnt vmcnt(0)" ::: "memory");
  __syncthreads();
  if (TIDX == 0) __hip_atomic_fetch_add((gu32*)(ws + off), inc, __ATOMIC_RELAXED, __HIP_MEMORY_SCOPE_AGENT);
}

template <int SP> __device__ __forceinline__ void fan_signal(int wv_, unsigned char* ws, int fan, int i) {
  asm volatile("s_waitcnt vmcnt(0)" ::: "memory");
  __syncthreads();
  const int l = lane_id();
  if (wv_ == 0 && l < FNG) __hip_atomic_store((gu32*)(ws + fan + l * FAN_G + i * SP), nosent(rtc()), __ATOMIC_RELAXED, __HIP_MEMORY_SCOPE_AGENT);
}
template <int SP, int N, bool BAR = true> __device__ __forceinline__ void fan_wait(int wv_, unsigned char* ws, int fan, int salt) {
  if (wv_ * 64 < N) {
    gu32* p = (gu32*)(ws + fan + (((int)xcc_id() * (FNG / 8) + (salt & (FNG / 8 - 1))) * FAN_G + min(TIDX, N - 1) * SP));
    unsigned v;
    for (unsigned spins = 0; spins < (1u << 17); ++spins) {
      v = __hip_atomic_load(p, __ATOMIC_RELAXED, __HIP_MEMORY_SCOPE_AGENT);
      const unsigned long long m = __ballot(v == 0xFFFFFFFFu);
      if (m == 0) break;
      __builtin_amdgcn_s_sleep(N == NCOL ? 24 : FNAP);
      if (N == 20 && (m & 0x3FF)) __builtin_amdgcn_s_sleep(40);
    }
    if (N == 20) {
      const unsigned t = rtc();
      *(unsigned*)(dyn_lds + L_WHEN) = t + (unsigned)wave_imax((int)(v - t));
    }
  }
  if (BAR) __syncthreads();
}

typedef unsigned v8u __attribute__((ext_vector_type(8)));
typedef unsigned v16u __attribute__((ext_vector_type(16)));
typedef unsigned v32u __attribute__((ext_vector_type(32)));
__device__ __forceinline__ float cosi(int i) { return __builtin_amdgcn_cosf((float)i * (1.f / 2048)); }
__device__ __forceinline__ float sini(int i) { return __builtin_amdgcn_sinf((float)i * (1.f / 2048)); }
__device__ __forceinline__ void cis8(v32u& d, int x0, int dx, int d4, unsigned m) {
#pragma unroll
  for (int q = 0; q < 4; ++q) {
    const int x = x0 + dx * 2 * (q & 1) + d4 * (q >> 1);
    d[q] = pk(cosi(x), cosi(x + dx)); d[4 + q] = d[q] ^ m;
    d[8 + q] = pk(sini(x), sini(x + dx)); d[12 + q] = d[8 + q] ^ m;
  }
}
#define LDS_V4(a) (*(__attribute__((address_space(3))) v4u*)(uintptr_t)(a))
#define LDS_U32(a) (*(__attribute__((address_space(3))) unsigned*)(uintptr_t)(a))
#define LDS_V2(a) (*(__attribute__((address_space(3))) v2u*)(uintptr_t)(a))
#define COL_ASM \
   "s_waitcnt lgkmcnt(0)\n\t" "s_barrier\n\t" "ds_read_b64_tr_b16 v[136:137], v16 offset:0\n\t" "ds_read_b64_tr_b16 v[138:139], v16 offset:1056\n\t" \
   "ds_read_b64_tr_b16 v[140:141], v16 offset:8448\n\t" "ds_read_b64_tr_b16 v[142:143], v16 offset:9504\n\t" \
   "ds_read_b64_tr_b16 v[144:145], v16 offset:8\n\t" "ds_read_b64_tr_b16 v[146:147], v16 offset:1064\n\t" \
   "ds_read_b64_tr_b16 v[148:149], v16 offset:8456\n\t" "ds_read_b64_tr_b16 v[150:151], v16 offset:9512\n\t" "s_waitcnt lgkmcnt(4)\n\t" \
   "v_mfma_f32_16x16x32_bf16 v[160:163], v[136:139], v[32:35], 0\n\t" "v_mfma_f32_16x16x32_bf16 v[164:167], v[136:139], v[40:43], 0\n\t" \
   "v_mfma_f32_16x16x32_bf16 v[160:163], v[140:143], v[36:39], v[160:163]\n\t" \
   "v_mfma_f32_16x16x32_bf16 v[164:167], v[140:143], v[44:47], v[164:167]\n\t" "ds_read_b64_tr_b16 v[152:153], v16 offset:16\n\t" \
   "ds_read_b64_tr_b16 v[154:155], v16 offset:1072\n\t" "ds_read_b64_tr_b16 v[156:157], v16 offset:8464\n\t" \
   "ds_read_b64_tr_b16 v[158:159], v16 offset:9520\n\t" "s_nop 3\n\t" "s_waitcnt lgkmcnt(4)\n\t" \
   "v_mfma_f32_16x16x32_bf16 v[168:171], v[144:147], v[32:35], 0\n\t" "v_add_f32 v160, v160, v165\n\t" "v_sub_f32 v161, v161, v164\n\t" \
   "v_mfma_f32_16x16x32_bf16 v[172:175], v[144:147], v[40:43], 0\n\t" "v_add_f32 v162, v162, v167\n\t" "v_sub_f32 v163, v163, v166\n\t" \
   "v_mfma_f32_16x16x32_bf16 v[168:171], v[148:151], v[36:39], v[168:171]\n\t" "v_mul_f32 v24, v161, v49\n\t" "v_mul_f32 v25, v161, v48\n\t" \
   "v_mfma_f32_16x16x32_bf16 v[172:175], v[148:151], v[44:47], v[172:175]\n\t" "v_mul_f32 v26, v163, v49\n\t" "v_mul_f32 v27, v163, v48\n\t" \
   "v_fma_f32 v177, v160, v49, v25\n\t" "v_fma_f32 v176, v160, v48, -v24\n\t" "v_fma_f32 v179, v162, v49, v27\n\t" \
   "v_fma_f32 v178, v162, v48, -v26\n\t" "ds_read_b64_tr_b16 v[136:137], v16 offset:24\n\t" "ds_read_b64_tr_b16 v[138:139], v16 offset:1080\n\t" \
   "ds_read_b64_tr_b16 v[140:141], v16 offset:8472\n\t" "ds_read_b64_tr_b16 v[142:143], v16 offset:9528\n\t" "s_waitcnt lgkmcnt(4)\n\t" \
   "v_mfma_f32_16x16x32_bf16 v[160:163], v[152:155], v[32:35], 0\n\t" "v_add_f32 v168, v168, v173\n\t" "v_sub_f32 v169, v169, v172\n\t" \
   "v_mfma_f32_16x16x32_bf16 v[164:167], v[152:155], v[40:43], 0\n\t" "v_add_f32 v170, v170, v175\n\t" "v_sub_f32 v171, v171, v174\n\t" \
   "v_mfma_f32_16x16x32_bf16 v[160:163], v[156:159], v[36:39], v[160:163]\n\t" "v_mul_f32 v24, v169, v51\n\t" "v_mul_f32 v25, v169, v50\n\t" \
   "v_mfma_f32_16x16x32_bf16 v[164:167], v[156:159], v[44:47], v[164:167]\n\t" "v_mul_f32 v26, v171, v51\n\t" "v_mul_f32 v27, v171, v50\n\t" \
   "v_fma_f32 v181, v168, v51, v25\n\t" "v_fma_f32 v180, v168, v50, -v24\n\t" "v_fma_f32 v183, v170, v51, v27\n\t" \
   "v_fma_f32 v182, v170, v50, -v26\n\t" "v_cvt_pk_bf16_f32 v12, v177, v181\n\t" "v_cvt_pk_bf16_f32 v8, v176, v180\n\t" \
   "v_cvt_pk_bf16_f32 v188, v179, v183\n\t" "v_cvt_pk_bf16_f32 v184, v178, v182\n\t" "ds_read_b64_tr_b16 v[144:145], v16 offset:128\n\t" \
   "ds_read_b64_tr_b16 v[146:147], v16 offset:1184\n\t" "ds_read_b64_tr_b16 v[148:149], v16 offset:8576\n\t" \
   "ds_read_b64_tr_b16 v[150:151], v16 offset:9632\n\t" "s_waitcnt lgkmcnt(4)\n\t" "v_mfma_f32_16x16x32_bf16 v[168:171], v[136:139], v[32:35], 0\n\t" \
   "v_add_f32 v160, v160, v165\n\t" "v_sub_f32 v161, v161, v164\n\t" "v_mfma_f32_16x16x32_bf16 v[172:175], v[136:139], v[40:43], 0\n\t" \
   "v_add_f32 v162, v162, v167\n\t" "v_sub_f32 v163, v163, v166\n\t" "v_mfma_f32_16x16x32_bf16 v[168:171], v[140:143], v[36:39], v[168:171]\n\t" \
   "v_mul_f32 v24, v161, v53\n\t" "v_mul_f32 v25, v161, v52\n\t" "v_mfma_f32_16x16x32_bf16 v[172:175], v[140:143], v[44:47], v[172:175]\n\t" \
   "v_mul_f32 v26, v163, v53\n\t" "v_mul_f32 v27, v163, v52\n\t" "v_fma_f32 v177, v160, v53, v25\n\t" "v_fma_f32 v176, v160, v52, -v24\n\t" \
   "v_fma_f32 v179, v162, v53, v27\n\t" "v_fma_f32 v178, v162, v52, -v26\n\t" "ds_read_b64_tr_b16 v[152:153], v16 offset:136\n\t" \
   "ds_read_b64_tr_b16 v[154:155], v16 offset:1192\n\t" "ds_read_b64_tr_b16 v[156:157], v16 offset:8584\n\t" \
   "ds_read_b64_tr_b16 v[158:159], v16 offset:9640\n\t" "s_waitcnt lgkmcnt(4)\n\t" "v_mfma_f32_16x16x32_bf16 v[160:163], v[144:147], v[32:35], 0\n\t" \
   "v_add_f32 v168, v168, v173\n\t" "v_sub_f32 v169, v169, v172\n\t" "v_mfma_f32_16x16x32_bf16 v[164:167], v[144:147], v[40:43], 0\n\t" \
   "v_add_f32 v170, v170, v175\n\t" "v_sub_f32 v171, v171, v174\n\t" "v_mfma_f32_16x16x32_bf16 v[160:163], v[148:151], v[36:39], v[160:163]\n\t" \
   "v_mul_f32 v24, v169, v55\n\t" "v_mul_f32 v25, v169, v54\n\t" "v_mfma_f32_16x16x32_bf16 v[164:167], v[148:151], v[44:47], v[164:167]\n\t" \
   "v_mul_f32 v26, v171, v55\n\t" "v_mul_f32 v27, v171, v54\n\t" "v_fma_f32 v181, v168, v55, v25\n\t" "v_fma_f32 v180, v168, v54, -v24\n\t" \
   "v_fma_f32 v183, v170, v55, v27\n\t" "v_fma_f32 v182, v170, v54, -v26\n\t" "v_cvt_pk_bf16_f32 v13, v177, v181\n\t" \
   "v_cvt_pk_bf16_f32 v9, v176, v180\n\t" "v_cvt_pk_bf16_f32 v189, v179, v183\n\t" "v_cvt_pk_bf16_f32 v185, v178, v182\n\t" \
   "ds_read_b64_tr_b16 v[136:137], v16 offset:144\n\t" "ds_read_b64_tr_b16 v[138:139], v16 offset:1200\n\t" \
   "ds_read_b64_tr_b16 v[140:141], v16 offset:8592\n\t" "ds_read_b64_tr_b16 v[142:143], v16 offset:9648\n\t" "s_waitcnt lgkmcnt(4)\n\t" \
   "v_mfma_f32_16x16x32_bf16 v[168:171], v[152:155], v[32:35], 0\n\t" "v_add_f32 v160, v160, v165\n\t" "v_sub_f32 v161, v161, v164\n\t" \
   "v_mfma_f32_16x16x32_bf16 v[172:175], v[152:155], v[40:43], 0\n\t" "v_add_f32 v162, v162, v167\n\t" "v_sub_f32 v163, v163, v166\n\t" \
   "v_mfma_f32_16x16x32_bf16 v[168:171], v[156:159], v[36:39], v[168:171]\n\t" "v_mul_f32 v24, v161, v57\n\t" "v_mul_f32 v25, v161, v56\n\t" \
   "v_mfma_f32_16x16x32_bf16 v[172:175], v[156:159], v[44:47], v[172:175]\n\t" "v_mul_f32 v26, v163, v57\n\t" "v_mul_f32 v27, v163, v56\n\t" \
   "v_fma_f32 v177, v160, v57, v25\n\t" "v_fma_f32 v176, v160, v56, -v24\n\t" "v_fma_f32 v179, v162, v57, v27\n\t" \
   "v_fma_f32 v178, v162, v56, -v26\n\t" "ds_read_b64_tr_b16 v[144:145], v16 offset:152\n\t" "ds_read_b64_tr_b16 v[146:147], v16 offset:1208\n\t" \
   "ds_read_b64_tr_b16 v[148:149], v16 offset:8600\n\t" "ds_read_b64_tr_b16 v[150:151], v16 offset:9656\n\t" "s_waitcnt lgkmcnt(4)\n\t" \
   "v_mfma_f32_16x16x32_bf16 v[160:163], v[136:139], v[32:35], 0\n\t" "v_add_f32 v168, v168, v173\n\t" "v_sub_f32 v169, v169, v172\n\t" \
   "v_mfma_f32_16x16x32_bf16 v[164:167], v[136:139], v[40:43], 0\n\t" "v_add_f32 v170, v170, v175\n\t" "v_sub_f32 v171, v171, v174\n\t" \
   "v_mfma_f32_16x16x32_bf16 v[160:163], v[140:143], v[36:39], v[160:163]\n\t" "v_mul_f32 v24, v169, v59\n\t" "v_mul_f32 v25, v169, v58\n\t" \
   "v_mfma_f32_16x16x32_bf16 v[164:167], v[140:143], v[44:47], v[164:167]\n\t" "v_mul_f32 v26, v171, v59\n\t" "v_mul_f32 v27, v171, v58\n\t" \
   "v_fma_f32 v181, v168, v59, v25\n\t" "v_fma_f32 v180, v168, v58, -v24\n\t" "v_fma_f32 v183, v170, v59, v27\n\t" \
   "v_fma_f32 v182, v170, v58, -v26\n\t" "v_cvt_pk_bf16_f32 v14, v177, v181\n\t" "v_cvt_pk_bf16_f32 v10, v176, v180\n\t" \
   "v_cvt_pk_bf16_f32 v190, v179, v183\n\t" "v_cvt_pk_bf16_f32 v186, v178, v182\n\t" "s_nop 1\n\t" "s_waitcnt lgkmcnt(0)\n\t" \
   "v_mfma_f32_16x16x32_bf16 v[168:171], v[144:147], v[32:35], 0\n\t" "v_add_f32 v160, v160, v165\n\t" "v_sub_f32 v161, v161, v164\n\t" \
   "v_mfma_f32_16x16x32_bf16 v[172:175], v[144:147], v[40:43], 0\n\t" "v_add_f32 v162, v162, v167\n\t" "v_sub_f32 v163, v163, v166\n\t" \
   "v_mfma_f32_16x16x32_bf16 v[168:171], v[148:151], v[36:39], v[168:171]\n\t" "v_mul_f32 v24, v161, v61\n\t" "v_mul_f32 v25, v161, v60\n\t" \
   "v_mfma_f32_16x16x32_bf16 v[172:175], v[148:151], v[44:47], v[172:175]\n\t" "v_mul_f32 v26, v163, v61\n\t" "v_mul_f32 v27, v163, v60\n\t" \
   "v_fma_f32 v177, v160, v61, v25\n\t" "v_fma_f32 v176, v160, v60, -v24\n\t" "v_fma_f32 v179, v162, v61, v27\n\t" \
   "v_fma_f32 v178, v162, v60, -v26\n\t" "s_nop 1\n\t" "v_add_f32 v168, v168, v173\n\t" "v_sub_f32 v169, v169, v172\n\t" "v_mul_f32 v24, v169, v63\n\t" \
   "v_mul_f32 v25, v169, v62\n\t" "v_fma_f32 v181, v168, v63, v25\n\t" "v_fma_f32 v180, v168, v62, -v24\n\t" "v_cvt_pk_bf16_f32 v15, v177, v181\n\t" \
   "v_cvt_pk_bf16_f32 v11, v176, v180\n\t" "s_nop 1\n\t" "v_mfma_f32_16x16x32_bf16 v[160:163], v[64:67], v[8:11], 0\n\t" \
   "v_add_f32 v170, v170, v175\n\t" "v_sub_f32 v171, v171, v174\n\t" "v_mfma_f32_16x16x32_bf16 v[164:167], v[64:67], v[12:15], 0\n\t" \
   "v_mul_f32 v24, v171, v63\n\t" "v_mfma_f32_16x16x32_bf16 v[160:163], v[72:75], v[12:15], v[160:163]\n\t" "v_mul_f32 v25, v171, v62\n\t" \
   "v_mfma_f32_16x16x32_bf16 v[164:167], v[128:131], v[8:11], v[164:167]\n\t" "v_fma_f32 v183, v170, v63, v25\n\t" \
   "v_fma_f32 v182, v170, v62, -v24\n\t" "v_cvt_pk_bf16_f32 v191, v179, v183\n\t" "v_cvt_pk_bf16_f32 v187, v178, v182\n\t" "s_nop 4\n\t" \
   "v_mfma_f32_16x16x32_bf16 v[168:171], v[68:71], v[8:11], 0\n\t" "v_mul_f32 v24, v164, v97\n\t" "v_mul_f32 v25, v164, v96\n\t" \
   "v_mul_f32 v26, v165, v99\n\t" "v_mfma_f32_16x16x32_bf16 v[172:175], v[68:71], v[12:15], 0\n\t" "v_mul_f32 v27, v165, v98\n\t" \
   "v_mul_f32 v28, v166, v101\n\t" "v_mul_f32 v29, v166, v100\n\t" "v_mfma_f32_16x16x32_bf16 v[168:171], v[76:79], v[12:15], v[168:171]\n\t" \
   "v_mul_f32 v30, v167, v103\n\t" "v_mul_f32 v31, v167, v102\n\t" "v_fma_f32 v164, v160, v97, v25\n\t" \
   "v_mfma_f32_16x16x32_bf16 v[172:175], v[132:135], v[8:11], v[172:175]\n\t" "v_fma_f32 v160, v160, v96, -v24\n\t" \
   "v_fma_f32 v165, v161, v99, v27\n\t" "v_fma_f32 v161, v161, v98, -v26\n\t" "v_fma_f32 v166, v162, v101, v29\n\t" \
   "v_fma_f32 v162, v162, v100, -v28\n\t" "v_fma_f32 v167, v163, v103, v31\n\t" "v_fma_f32 v163, v163, v102, -v30\n\t" \
   "v_cvt_pk_bf16_f32 v140, v164, v165\n\t" "v_cvt_pk_bf16_f32 v136, v160, v161\n\t" "v_cvt_pk_bf16_f32 v141, v166, v167\n\t" \
   "v_cvt_pk_bf16_f32 v137, v162, v163\n\t" "v_mfma_f32_16x16x32_bf16 v[160:163], v[64:67], v[184:187], 0\n\t" "v_mul_f32 v24, v172, v105\n\t" \
   "v_mul_f32 v25, v172, v104\n\t" "v_mul_f32 v26, v173, v107\n\t" "v_mfma_f32_16x16x32_bf16 v[164:167], v[64:67], v[188:191], 0\n\t" \
   "v_mul_f32 v27, v173, v106\n\t" "v_mul_f32 v28, v174, v109\n\t" "v_mul_f32 v29, v174, v108\n\t" \
   "v_mfma_f32_16x16x32_bf16 v[160:163], v[72:75], v[188:191], v[160:163]\n\t" "v_mul_f32 v30, v175, v111\n\t" "v_mul_f32 v31, v175, v110\n\t" \
   "v_fma_f32 v172, v168, v105, v25\n\t" "v_mfma_f32_16x16x32_bf16 v[164:167], v[128:131], v[184:187], v[164:167]\n\t" \
   "v_fma_f32 v168, v168, v104, -v24\n\t" "v_fma_f32 v173, v169, v107, v27\n\t" "v_fma_f32 v169, v169, v106, -v26\n\t" \
   "v_fma_f32 v174, v170, v109, v29\n\t" "v_fma_f32 v170, v170, v108, -v28\n\t" "v_fma_f32 v175, v171, v111, v31\n\t" \
   "v_fma_f32 v171, v171, v110, -v30\n\t" "v_cvt_pk_bf16_f32 v142, v172, v173\n\t" "v_cvt_pk_bf16_f32 v138, v168, v169\n\t" \
   "v_cvt_pk_bf16_f32 v143, v174, v175\n\t" "v_cvt_pk_bf16_f32 v139, v170, v171\n\t" "v_mfma_f32_16x16x32_bf16 v[168:171], v[68:71], v[184:187], 0\n\t" \
   "v_mul_f32 v24, v164, v113\n\t" "v_mul_f32 v25, v164, v112\n\t" "v_mul_f32 v26, v165, v115\n\t" \
   "v_mfma_f32_16x16x32_bf16 v[172:175], v[68:71], v[188:191], 0\n\t" "v_mul_f32 v27, v165, v114\n\t" "v_mul_f32 v28, v166, v117\n\t" \
   "v_mul_f32 v29, v166, v116\n\t" "v_mfma_f32_16x16x32_bf16 v[168:171], v[76:79], v[188:191], v[168:171]\n\t" "v_mul_f32 v30, v167, v119\n\t" \
   "v_mul_f32 v31, v167, v118\n\t" "v_fma_f32 v164, v160, v113, v25\n\t" "v_mfma_f32_16x16x32_bf16 v[172:175], v[132:135], v[184:187], v[172:175]\n\t" \
   "v_fma_f32 v160, v160, v112, -v24\n\t" "v_fma_f32 v165, v161, v115, v27\n\t" "v_fma_f32 v161, v161, v114, -v26\n\t" \
   "v_fma_f32 v166, v162, v117, v29\n\t" "v_fma_f32 v162, v162, v116, -v28\n\t" "v_fma_f32 v167, v163, v119, v31\n\t" \
   "v_fma_f32 v163, v163, v118, -v30\n\t" "v_cvt_pk_bf16_f32 v148, v164, v165\n\t" "v_cvt_pk_bf16_f32 v144, v160, v161\n\t" \
   "v_cvt_pk_bf16_f32 v149, v166, v167\n\t" "v_cvt_pk_bf16_f32 v145, v162, v163\n\t" "v_mfma_f32_16x16x32_bf16 v[160:163], v[136:139], v[64:67], 0\n\t" \
   "v_mul_f32 v24, v172, v121\n\t" "v_mul_f32 v25, v172, v120\n\t" "v_mul_f32 v26, v173, v123\n\t" \
   "v_mfma_f32_16x16x32_bf16 v[164:167], v[140:143], v[64:67], 0\n\t" "v_mul_f32 v27, v173, v122\n\t" "v_mul_f32 v28, v174, v125\n\t" \
   "v_mul_f32 v29, v174, v124\n\t" "v_mfma_f32_16x16x32_bf16 v[160:163], v[140:143], v[128:131], v[160:163]\n\t" "v_mul_f32 v30, v175, v127\n\t" \
   "v_mul_f32 v31, v175, v126\n\t" "v_fma_f32 v172, v168, v121, v25\n\t" "v_mfma_f32_16x16x32_bf16 v[164:167], v[136:139], v[72:75], v[164:167]\n\t" \
   "v_fma_f32 v168, v168, v120, -v24\n\t" "v_fma_f32 v173, v169, v123, v27\n\t" "v_fma_f32 v169, v169, v122, -v26\n\t" \
   "v_fma_f32 v174, v170, v125, v29\n\t" "v_fma_f32 v170, v170, v124, -v28\n\t" "v_fma_f32 v175, v171, v127, v31\n\t" \
   "v_fma_f32 v171, v171, v126, -v30\n\t" "v_cvt_pk_bf16_f32 v150, v172, v173\n\t" "v_cvt_pk_bf16_f32 v146, v168, v169\n\t" \
   "v_cvt_pk_bf16_f32 v151, v174, v175\n\t" "v_cvt_pk_bf16_f32 v147, v170, v171\n\t" "v_mfma_f32_16x16x32_bf16 v[168:171], v[136:139], v[68:71], 0\n\t" \
   "v_mul_f32 v24, v164, v81\n\t" "v_mul_f32 v25, v164, v80\n\t" "v_mul_f32 v26, v165, v83\n\t" \
   "v_mfma_f32_16x16x32_bf16 v[172:175], v[140:143], v[68:71], 0\n\t" "v_mul_f32 v27, v165, v82\n\t" "v_mul_f32 v28, v166, v85\n\t" \
   "v_mul_f32 v29, v166, v84\n\t" "v_mfma_f32_16x16x32_bf16 v[168:171], v[140:143], v[132:135], v[168:171]\n\t" "v_mul_f32 v30, v167, v87\n\t" \
   "v_mul_f32 v31, v167, v86\n\t" "v_fma_f32 v164, v160, v81, v25\n\t" "v_mfma_f32_16x16x32_bf16 v[172:175], v[136:139], v[76:79], v[172:175]\n\t" \
   "v_fma_f32 v160, v160, v80, -v24\n\t" "v_fma_f32 v165, v161, v83, v27\n\t" "v_fma_f32 v161, v161, v82, -v26\n\t" \
   "v_fma_f32 v166, v162, v85, v29\n\t" "v_fma_f32 v162, v162, v84, -v28\n\t" "v_fma_f32 v167, v163, v87, v31\n\t" \
   "v_fma_f32 v163, v163, v86, -v30\n\t" "v_cvt_pk_bf16_f32 v178, v164, v165\n\t" "v_cvt_pk_bf16_f32 v176, v160, v161\n\t" \
   "v_cvt_pk_bf16_f32 v179, v166, v167\n\t" "v_cvt_pk_bf16_f32 v177, v162, v163\n\t" "ds_write_b64 v17, v[178:179] offset:128\n\t" \
   "ds_write_b64 v17, v[176:177] offset:0\n\t" "v_mfma_f32_16x16x32_bf16 v[160:163], v[144:147], v[64:67], 0\n\t" "v_mul_f32 v24, v172, v89\n\t" \
   "v_mul_f32 v25, v172, v88\n\t" "v_mul_f32 v26, v173, v91\n\t" "v_mfma_f32_16x16x32_bf16 v[164:167], v[148:151], v[64:67], 0\n\t" \
   "v_mul_f32 v27, v173, v90\n\t" "v_mul_f32 v28, v174, v93\n\t" "v_mul_f32 v29, v174, v92\n\t" \
   "v_mfma_f32_16x16x32_bf16 v[160:163], v[148:151], v[128:131], v[160:163]\n\t" "v_mul_f32 v30, v175, v95\n\t" "v_mul_f32 v31, v175, v94\n\t" \
   "v_fma_f32 v172, v168, v89, v25\n\t" "v_mfma_f32_16x16x32_bf16 v[164:167], v[144:147], v[72:75], v[164:167]\n\t" \
   "v_fma_f32 v168, v168, v88, -v24\n\t" "v_fma_f32 v173, v169, v91, v27\n\t" "v_fma_f32 v169, v169, v90, -v26\n\t" \
   "v_fma_f32 v174, v170, v93, v29\n\t" "v_fma_f32 v170, v170, v92, -v28\n\t" "v_fma_f32 v175, v171, v95, v31\n\t" \
   "v_fma_f32 v171, v171, v94, -v30\n\t" "v_cvt_pk_bf16_f32 v182, v172, v173\n\t" "v_cvt_pk_bf16_f32 v180, v168, v169\n\t" \
   "v_cvt_pk_bf16_f32 v183, v174, v175\n\t" "v_cvt_pk_bf16_f32 v181, v170, v171\n\t" "ds_write_b64 v17, v[182:183] offset:8384\n\t" \
   "ds_write_b64 v17, v[180:181] offset:8256\n\t" "v_mfma_f32_16x16x32_bf16 v[168:171], v[144:147], v[68:71], 0\n\t" "v_mul_f32 v24, v164, v81\n\t" \
   "v_mul_f32 v25, v164, v80\n\t" "v_mul_f32 v26, v165, v83\n\t" "v_mfma_f32_16x16x32_bf16 v[172:175], v[148:151], v[68:71], 0\n\t" \
   "v_mul_f32 v27, v165, v82\n\t" "v_mul_f32 v28, v166, v85\n\t" "v_mul_f32 v29, v166, v84\n\t" \
   "v_mfma_f32_16x16x32_bf16 v[168:171], v[148:151], v[132:135], v[168:171]\n\t" "v_mul_f32 v30, v167, v87\n\t" "v_mul_f32 v31, v167, v86\n\t" \
   "v_fma_f32 v164, v160, v81, v25\n\t" "v_mfma_f32_16x16x32_bf16 v[172:175], v[144:147], v[76:79], v[172:175]\n\t" \
   "v_fma_f32 v160, v160, v80, -v24\n\t" "v_fma_f32 v165, v161, v83, v27\n\t" "v_fma_f32 v161, v161, v82, -v26\n\t" \
   "v_fma_f32 v166, v162, v85, v29\n\t" "v_fma_f32 v162, v162, v84, -v28\n\t" "v_fma_f32 v167, v163, v87, v31\n\t" \
   "v_fma_f32 v163, v163, v86, -v30\n\t" "v_cvt_pk_bf16_f32 v178, v164, v165\n\t" "v_cvt_pk_bf16_f32 v176, v160, v161\n\t" \
   "v_cvt_pk_bf16_f32 v179, v166, v167\n\t" "v_cvt_pk_bf16_f32 v177, v162, v163\n\t" "ds_write_b64 v18, v[178:179] offset:128\n\t" \
   "ds_write_b64 v18, v[176:177] offset:0\n\t" "v_mul_f32 v24, v172, v89\n\t" "v_mul_f32 v25, v172, v88\n\t" "v_mul_f32 v26, v173, v91\n\t" \
   "v_mul_f32 v27, v173, v90\n\t" "v_mul_f32 v28, v174, v93\n\t" "v_mul_f32 v29, v174, v92\n\t" "v_mul_f32 v30, v175, v95\n\t" \
   "v_mul_f32 v31, v175, v94\n\t" "v_fma_f32 v172, v168, v89, v25\n\t" "v_fma_f32 v168, v168, v88, -v24\n\t" "v_fma_f32 v173, v169, v91, v27\n\t" \
   "v_fma_f32 v169, v169, v90, -v26\n\t" "v_fma_f32 v174, v170, v93, v29\n\t" "v_fma_f32 v170, v170, v92, -v28\n\t" \
   "v_fma_f32 v175, v171, v95, v31\n\t" "v_fma_f32 v171, v171, v94, -v30\n\t" "v_cvt_pk_bf16_f32 v182, v172, v173\n\t" \
   "v_cvt_pk_bf16_f32 v180, v168, v169\n\t" "v_cvt_pk_bf16_f32 v183, v174, v175\n\t" "v_cvt_pk_bf16_f32 v181, v170, v171\n\t" \
   "ds_write_b64 v18, v[182:183] offset:8384\n\t" "ds_write_b64 v18, v[180:181] offset:8256\n\t" "s_waitcnt lgkmcnt(0)\n\t" "s_barrier\n\t" \
   "ds_read_b128 v[136:139], v19 offset:0\n\t" "ds_read_b128 v[140:143], v20 offset:0\n\t" "ds_read_b128 v[144:147], v19 offset:2064\n\t" \
   "ds_read_b128 v[148:151], v20 offset:2064\n\t" "s_waitcnt lgkmcnt(2)\n\t" "v_mfma_f32_16x16x32_bf16 v[160:163], v[136:139], v[32:35], 0\n\t" \
   "ds_read_b128 v[152:155], v19 offset:4128\n\t" "ds_read_b128 v[156:159], v20 offset:4128\n\t" \
   "v_mfma_f32_16x16x32_bf16 v[164:167], v[136:139], v[40:43], 0\n\t" "v_mfma_f32_16x16x32_bf16 v[160:163], v[140:143], v[36:39], v[160:163]\n\t" \
   "v_mfma_f32_16x16x32_bf16 v[164:167], v[140:143], v[44:47], v[164:167]\n\t" "s_waitcnt lgkmcnt(2)\n\t" \
   "v_mfma_f32_16x16x32_bf16 v[168:171], v[144:147], v[32:35], 0\n\t" "ds_read_b128 v[136:139], v19 offset:6192\n\t" \
   "ds_read_b128 v[140:143], v20 offset:6192\n\t" "v_mfma_f32_16x16x32_bf16 v[172:175], v[144:147], v[40:43], 0\n\t" "v_sub_f32 v160, v160, v165\n\t" \
   "v_add_f32 v161, v161, v164\n\t" "v_sub_f32 v162, v162, v167\n\t" "v_mfma_f32_16x16x32_bf16 v[168:171], v[148:151], v[36:39], v[168:171]\n\t" \
   "v_add_f32 v163, v163, v166\n\t" "v_cvt_pk_bf16_f32 v8, v160, v161\n\t" "v_cvt_pk_bf16_f32 v9, v162, v163\n\t" \
   "v_mfma_f32_16x16x32_bf16 v[172:175], v[148:151], v[44:47], v[172:175]\n\t" "s_waitcnt lgkmcnt(2)\n\t" \
   "v_mfma_f32_16x16x32_bf16 v[160:163], v[152:155], v[32:35], 0\n\t" "ds_read_b128 v[144:147], v19 offset:8256\n\t" \
   "ds_read_b128 v[148:151], v20 offset:8256\n\t" "v_mfma_f32_16x16x32_bf16 v[164:167], v[152:155], v[40:43], 0\n\t" "v_sub_f32 v168, v168, v173\n\t" \
   "v_add_f32 v169, v169, v172\n\t" "v_sub_f32 v170, v170, v175\n\t" "v_mfma_f32_16x16x32_bf16 v[160:163], v[156:159], v[36:39], v[160:163]\n\t" \
   "v_add_f32 v171, v171, v174\n\t" "v_cvt_pk_bf16_f32 v10, v168, v169\n\t" "v_cvt_pk_bf16_f32 v11, v170, v171\n\t" \
   "v_mfma_f32_16x16x32_bf16 v[164:167], v[156:159], v[44:47], v[164:167]\n\t" "buffer_store_dwordx4 v[8:11], v21, %[rs], 0 offen offset:0 sc1\n\t" \
   "s_waitcnt lgkmcnt(2)\n\t" "v_mfma_f32_16x16x32_bf16 v[168:171], v[136:139], v[32:35], 0\n\t" "ds_read_b128 v[152:155], v19 offset:10320\n\t" \
   "ds_read_b128 v[156:159], v20 offset:10320\n\t" "v_mfma_f32_16x16x32_bf16 v[172:175], v[136:139], v[40:43], 0\n\t" "v_sub_f32 v160, v160, v165\n\t" \
   "v_add_f32 v161, v161, v164\n\t" "v_sub_f32 v162, v162, v167\n\t" "v_mfma_f32_16x16x32_bf16 v[168:171], v[140:143], v[36:39], v[168:171]\n\t" \
   "v_add_f32 v163, v163, v166\n\t" "v_cvt_pk_bf16_f32 v12, v160, v161\n\t" "v_cvt_pk_bf16_f32 v13, v162, v163\n\t" \
   "v_mfma_f32_16x16x32_bf16 v[172:175], v[140:143], v[44:47], v[172:175]\n\t" "s_waitcnt lgkmcnt(2)\n\t" \
   "v_mfma_f32_16x16x32_bf16 v[160:163], v[144:147], v[32:35], 0\n\t" "ds_read_b128 v[136:139], v19 offset:12384\n\t" \
   "ds_read_b128 v[140:143], v20 offset:12384\n\t" "v_mfma_f32_16x16x32_bf16 v[164:167], v[144:147], v[40:43], 0\n\t" "v_sub_f32 v168, v168, v173\n\t" \
   "v_add_f32 v169, v169, v172\n\t" "v_sub_f32 v170, v170, v175\n\t" "v_mfma_f32_16x16x32_bf16 v[160:163], v[148:151], v[36:39], v[160:163]\n\t" \
   "v_add_f32 v171, v171, v174\n\t" "v_cvt_pk_bf16_f32 v14, v168, v169\n\t" "v_cvt_pk_bf16_f32 v15, v170, v171\n\t" \
   "v_mfma_f32_16x16x32_bf16 v[164:167], v[148:151], v[44:47], v[164:167]\n\t" "buffer_store_dwordx4 v[12:15], v21, %[rs], 0 offen offset:64 sc1\n\t" \
   "s_waitcnt lgkmcnt(2)\n\t" "v_mfma_f32_16x16x32_bf16 v[168:171], v[152:155], v[32:35], 0\n\t" "ds_read_b128 v[144:147], v19 offset:14448\n\t" \
   "ds_read_b128 v[148:151], v20 offset:14448\n\t" "v_mfma_f32_16x16x32_bf16 v[172:175], v[152:155], v[40:43], 0\n\t" "v_sub_f32 v160, v160, v165\n\t" \
   "v_add_f32 v161, v161, v164\n\t" "v_sub_f32 v162, v162, v167\n\t" "v_mfma_f32_16x16x32_bf16 v[168:171], v[156:159], v[36:39], v[168:171]\n\t" \
   "v_add_f32 v163, v163, v166\n\t" "v_cvt_pk_bf16_f32 v8, v160, v161\n\t" "v_cvt_pk_bf16_f32 v9, v162, v163\n\t" \
   "v_mfma_f32_16x16x32_bf16 v[172:175], v[156:159], v[44:47], v[172:175]\n\t" "s_waitcnt lgkmcnt(2)\n\t" \
   "v_mfma_f32_16x16x32_bf16 v[160:163], v[136:139], v[32:35], 0\n\t" "s_nop 0\n\t" "s_nop 0\n\t" \
   "v_mfma_f32_16x16x32_bf16 v[164:167], v[136:139], v[40:43], 0\n\t" "v_sub_f32 v168, v168, v173\n\t" "v_add_f32 v169, v169, v172\n\t" \
   "v_sub_f32 v170, v170, v175\n\t" "v_mfma_f32_16x16x32_bf16 v[160:163], v[140:143], v[36:39], v[160:163]\n\t" "v_add_f32 v171, v171, v174\n\t" \
   "v_cvt_pk_bf16_f32 v10, v168, v169\n\t" "v_cvt_pk_bf16_f32 v11, v170, v171\n\t" \
   "v_mfma_f32_16x16x32_bf16 v[164:167], v[140:143], v[44:47], v[164:167]\n\t" "buffer_store_dwordx4 v[8:11], v21, %[rs], 0 offen offset:128 sc1\n\t" \
   "s_waitcnt lgkmcnt(0)\n\t" "v_mfma_f32_16x16x32_bf16 v[168:171], v[144:147], v[32:35], 0\n\t" "s_nop 0\n\t" "s_nop 0\n\t" \
   "v_mfma_f32_16x16x32_bf16 v[172:175], v[144:147], v[40:43], 0\n\t" "v_sub_f32 v160, v160, v165\n\t" "v_add_f32 v161, v161, v164\n\t" \
   "v_sub_f32 v162, v162, v167\n\t" "v_mfma_f32_16x16x32_bf16 v[168:171], v[148:151], v[36:39], v[168:171]\n\t" "v_add_f32 v163, v163, v166\n\t" \
   "v_cvt_pk_bf16_f32 v12, v160, v161\n\t" "v_cvt_pk_bf16_f32 v13, v162, v163\n\t" \
   "v_mfma_f32_16x16x32_bf16 v[172:175], v[148:151], v[44:47], v[172:175]\n\t" "s_nop 7\n\t" "v_sub_f32 v168, v168, v173\n\t" \
   "v_add_f32 v169, v169, v172\n\t" "v_sub_f32 v170, v170, v175\n\t" "v_add_f32 v171, v171, v174\n\t" "v_cvt_pk_bf16_f32 v14, v168, v169\n\t" \
   "v_cvt_pk_bf16_f32 v15, v170, v171\n\t" "buffer_store_dwordx4 v[12:15], v21, %[rs], 0 offen offset:192 sc1\n\t" "s_nop 1"
#define COL_CLOB "v8", "v9", "v10", "v11", "v12", "v13", "v14", "v15", "v24", "v25", "v26", "v27", "v28", "v29", "v30", "v31", "v136", "v137", "v138", "v139", "v140", "v141", "v142", "v143", "v144", "v145", "v146", "v147", "v148", "v149", "v150", "v151", "v152", "v153", "v154", "v155", "v156", "v157", "v158", "v159", "v160", "v161", "v162", "v163", "v164", "v165", "v166", "v167", "v168", "v169", "v170", "v171", "v172", "v173", "v174", "v175", "v176", "v177", "v178", "v179", "v180", "v181", "v182", "v183", "v184", "v185", "v186", "v187", "v188", "v189", "v190", "v191"
#define IM_ASM \
   "s_mov_b32 s40, 0\n\t" "buffer_load_dwordx4 a[0:3], %[o0], %[rs], s40 offen offset:0 sc1\n\t" "s_mov_b32 s41, 20480\n\t" \
   "buffer_load_dwordx4 a[80:83], %[o0], %[rs], s41 offen offset:0 sc1\n\t" "s_mov_b32 s42, 40960\n\t" \
   "buffer_load_dwordx4 a[160:163], %[o1], %[rs], s42 offen offset:0 sc1\n\t" "buffer_load_dwordx4 a[4:7], %[o0], %[rs], s40 offen offset:1024 sc1\n\t" \
   "buffer_load_dwordx4 a[84:87], %[o0], %[rs], s41 offen offset:1024 sc1\n\t" \
   "buffer_load_dwordx4 a[164:167], %[o1], %[rs], s42 offen offset:1024 sc1\n\t" \
   "buffer_load_dwordx4 a[8:11], %[o0], %[rs], s40 offen offset:2048 sc1\n\t" \
   "buffer_load_dwordx4 a[88:91], %[o0], %[rs], s41 offen offset:2048 sc1\n\t" \
   "buffer_load_dwordx4 a[168:171], %[o1], %[rs], s42 offen offset:2048 sc1\n\t" \
   "buffer_load_dwordx4 a[12:15], %[o0], %[rs], s40 offen offset:3072 sc1\n\t" \
   "buffer_load_dwordx4 a[92:95], %[o0], %[rs], s41 offen offset:3072 sc1\n\t" \
   "buffer_load_dwordx4 a[172:175], %[o1], %[rs], s42 offen offset:3072 sc1\n\t" "s_mov_b32 s40, 4096\n\t" \
   "buffer_load_dwordx4 a[16:19], %[o0], %[rs], s40 offen offset:0 sc1\n\t" "s_mov_b32 s41, 24576\n\t" \
   "buffer_load_dwordx4 a[96:99], %[o0], %[rs], s41 offen offset:0 sc1\n\t" "s_mov_b32 s42, 45056\n\t" \
   "buffer_load_dwordx4 a[176:179], %[o1], %[rs], s42 offen offset:0 sc1\n\t" \
   "buffer_load_dwordx4 a[20:23], %[o0], %[rs], s40 offen offset:1024 sc1\n\t" \
   "buffer_load_dwordx4 a[100:103], %[o0], %[rs], s41 offen offset:1024 sc1\n\t" \
   "buffer_load_dwordx4 a[180:183], %[o1], %[rs], s42 offen offset:1024 sc1\n\t" \
   "buffer_load_dwordx4 a[24:27], %[o0], %[rs], s40 offen offset:2048 sc1\n\t" \
   "buffer_load_dwordx4 a[104:107], %[o0], %[rs], s41 offen offset:2048 sc1\n\t" \
   "buffer_load_dwordx4 a[184:187], %[o1], %[rs], s42 offen offset:2048 sc1\n\t" \
   "buffer_load_dwordx4 a[28:31], %[o0], %[rs], s40 offen offset:3072 sc1\n\t" \
   "buffer_load_dwordx4 a[108:111], %[o0], %[rs], s41 offen offset:3072 sc1\n\t" \
   "buffer_load_dwordx4 a[188:191], %[o1], %[rs], s42 offen offset:3072 sc1\n\t" "s_mov_b32 s40, 8192\n\t" \
   "buffer_load_dwordx4 a[32:35], %[o0], %[rs], s40 offen offset:0 sc1\n\t" "s_mov_b32 s41, 28672\n\t" \
   "buffer_load_dwordx4 a[112:115], %[o0], %[rs], s41 offen offset:0 sc1\n\t" "s_mov_b32 s42, 49152\n\t" \
   "buffer_load_dwordx4 a[192:195], %[o1], %[rs], s42 offen offset:0 sc1\n\t" \
   "buffer_load_dwordx4 a[36:39], %[o0], %[rs], s40 offen offset:1024 sc1\n\t" \
   "buffer_load_dwordx4 a[116:119], %[o0], %[rs], s41 offen offset:1024 sc1\n\t" \
   "buffer_load_dwordx4 a[196:199], %[o1], %[rs], s42 offen offset:1024 sc1\n\t" \
   "buffer_load_dwordx4 a[40:43], %[o0], %[rs], s40 offen offset:2048 sc1\n\t" \
   "buffer_load_dwordx4 a[120:123], %[o0], %[rs], s41 offen offset:2048 sc1\n\t" \
   "buffer_load_dwordx4 a[200:203], %[o1], %[rs], s42 offen offset:2048 sc1\n\t" \
   "buffer_load_dwordx4 a[44:47], %[o0], %[rs], s40 offen offset:3072 sc1\n\t" \
   "buffer_load_dwordx4 a[124:127], %[o0], %[rs], s41 offen offset:3072 sc1\n\t" \
   "buffer_load_dwordx4 a[204:207], %[o1], %[rs], s42 offen offset:3072 sc1\n\t" "s_mov_b32 s40, 12288\n\t" \
   "buffer_load_dwordx4 a[48:51], %[o0], %[rs], s40 offen offset:0 sc1\n\t" "s_mov_b32 s41, 32768\n\t" \
   "buffer_load_dwordx4 a[128:131], %[o0], %[rs], s41 offen offset:0 sc1\n\t" "s_mov_b32 s42, 53248\n\t" \
   "buffer_load_dwordx4 a[208:211], %[o1], %[rs], s42 offen offset:0 sc1\n\t" \
   "buffer_load_dwordx4 a[52:55], %[o0], %[rs], s40 offen offset:1024 sc1\n\t" \
   "buffer_load_dwordx4 a[132:135], %[o0], %[rs], s41 offen offset:1024 sc1\n\t" \
   "buffer_load_dwordx4 a[212:215], %[o1], %[rs], s42 offen offset:1024 sc1\n\t" \
   "buffer_load_dwordx4 a[56:59], %[o0], %[rs], s40 offen offset:2048 sc1\n\t" \
   "buffer_load_dwordx4 a[136:139], %[o0], %[rs], s41 offen offset:2048 sc1\n\t" \
   "buffer_load_dwordx4 a[216:219], %[o1], %[rs], s42 offen offset:2048 sc1\n\t" \
   "buffer_load_dwordx4 a[60:63], %[o0], %[rs], s40 offen offset:3072 sc1\n\t" \
   "buffer_load_dwordx4 a[140:143], %[o0], %[rs], s41 offen offset:3072 sc1\n\t" \
   "buffer_load_dwordx4 a[220:223], %[o1], %[rs], s42 offen offset:3072 sc1\n\t" "s_mov_b32 s40, 16384\n\t" \
   "buffer_load_dwordx4 a[64:67], %[o0], %[rs], s40 offen offset:0 sc1\n\t" "s_mov_b32 s41, 36864\n\t" \
   "buffer_load_dwordx4 a[144:147], %[o0], %[rs], s41 offen offset:0 sc1\n\t" "s_mov_b32 s42, 57344\n\t" \
   "buffer_load_dwordx4 a[224:227], %[o1], %[rs], s42 offen offset:0 sc1\n\t" \
   "buffer_load_dwordx4 a[68:71], %[o0], %[rs], s40 offen offset:1024 sc1\n\t" \
   "buffer_load_dwordx4 a[148:151], %[o0], %[rs], s41 offen offset:1024 sc1\n\t" \
   "buffer_load_dwordx4 a[228:231], %[o1], %[rs], s42 offen offset:1024 sc1\n\t" \
   "buffer_load_dwordx4 a[72:75], %[o0], %[rs], s40 offen offset:2048 sc1\n\t" \
   "buffer_load_dwordx4 a[152:155], %[o0], %[rs], s41 offen offset:2048 sc1\n\t" \
   "buffer_load_dwordx4 a[232:235], %[o1], %[rs], s42 offen offset:2048 sc1\n\t" \
   "buffer_load_dwordx4 a[76:79], %[o0], %[rs], s40 offen offset:3072 sc1\n\t" \
   "buffer_load_dwordx4 a[156:159], %[o0], %[rs], s41 offen offset:3072 sc1\n\t" \
   "buffer_load_dwordx4 a[236:239], %[o1], %[rs], s42 offen offset:3072 sc1\n\t" "s_waitcnt vmcnt(0)"
#define IM_CLOB "s40", "s41", "s42"
#define ID_ASM \
   "s_waitcnt lgkmcnt(0)\n\t" "s_barrier\n\t" "ds_read_b128 v[32:35], %[la] offset:0\n\t" "ds_read_b128 v[36:39], %[la] offset:10240\n\t" \
   "ds_read_b128 v[40:43], %[la] offset:1024\n\t" "ds_read_b128 v[44:47], %[la] offset:11264\n\t" "ds_read_b128 v[48:51], %[la] offset:2048\n\t" \
   "ds_read_b128 v[52:55], %[la] offset:12288\n\t" "ds_read_b128 v[56:59], %[la] offset:3072\n\t" "ds_read_b128 v[60:63], %[la] offset:13312\n\t" \
   "ds_read_b128 v[64:67], %[la] offset:4096\n\t" "ds_read_b128 v[68:71], %[la] offset:14336\n\t" "ds_read_b128 v[72:75], %[la] offset:5120\n\t" \
   "ds_read_b128 v[76:79], %[la] offset:15360\n\t" "ds_read_b128 v[80:83], %[la] offset:6144\n\t" "ds_read_b128 v[84:87], %[la] offset:16384\n\t" \
   "ds_read_b128 v[88:91], %[la] offset:7168\n\t" "ds_read_b128 v[92:95], %[la] offset:17408\n\t" "ds_read_b128 v[96:99], %[la] offset:8192\n\t" \
   "ds_read_b128 v[100:103], %[la] offset:18432\n\t" "ds_read_b128 v[104:107], %[la] offset:9216\n\t" "ds_read_b128 v[108:111], %[la] offset:19456\n\t" \
   "v_lshlrev_b32 v152, 16, v136\n\t" "v_and_b32 v153, 0xffff0000, v136\n\t" "v_lshlrev_b32 v154, 16, v137\n\t" "v_and_b32 v155, 0xffff0000, v137\n\t" \
   "v_lshlrev_b32 v156, 16, v140\n\t" "v_and_b32 v157, 0xffff0000, v140\n\t" "v_lshlrev_b32 v158, 16, v141\n\t" "v_and_b32 v159, 0xffff0000, v141\n\t" \
   "v_lshlrev_b32 v160, 16, v138\n\t" "v_and_b32 v161, 0xffff0000, v138\n\t" "v_lshlrev_b32 v162, 16, v139\n\t" "v_and_b32 v163, 0xffff0000, v139\n\t" \
   "s_waitcnt lgkmcnt(15)\n\t" "v_mfma_f32_16x16x32_bf16 v[112:115], a[0:3], v[32:35], 0\n\t" "v_lshlrev_b32 v164, 16, v142\n\t" \
   "v_and_b32 v165, 0xffff0000, v142\n\t" "v_mfma_f32_16x16x32_bf16 v[116:119], a[4:7], v[36:39], 0\n\t" "v_lshlrev_b32 v166, 16, v143\n\t" \
   "v_and_b32 v167, 0xffff0000, v143\n\t" "v_mfma_f32_16x16x32_bf16 v[112:115], a[8:11], v[40:43], v[112:115]\n\t" "v_lshlrev_b32 v168, 16, v144\n\t" \
   "v_and_b32 v169, 0xffff0000, v144\n\t" "v_mfma_f32_16x16x32_bf16 v[116:119], a[12:15], v[44:47], v[116:119]\n\t" "v_lshlrev_b32 v170, 16, v145\n\t" \
   "v_and_b32 v171, 0xffff0000, v145\n\t" "v_mfma_f32_16x16x32_bf16 v[112:115], a[16:19], v[48:51], v[112:115]\n\t" "v_lshlrev_b32 v172, 16, v146\n\t" \
   "v_and_b32 v173, 0xffff0000, v146\n\t" "s_waitcnt lgkmcnt(14)\n\t" "v_mfma_f32_16x16x32_bf16 v[116:119], a[20:23], v[52:55], v[116:119]\n\t" \
   "v_lshlrev_b32 v174, 16, v147\n\t" "v_and_b32 v175, 0xffff0000, v147\n\t" "s_waitcnt lgkmcnt(13)\n\t" \
   "v_mfma_f32_16x16x32_bf16 v[112:115], a[24:27], v[56:59], v[112:115]\n\t" "s_waitcnt lgkmcnt(12)\n\t" \
   "v_mfma_f32_16x16x32_bf16 v[116:119], a[28:31], v[60:63], v[116:119]\n\t" "s_waitcnt lgkmcnt(11)\n\t" \
   "v_mfma_f32_16x16x32_bf16 v[112:115], a[32:35], v[64:67], v[112:115]\n\t" "s_waitcnt lgkmcnt(10)\n\t" \
   "v_mfma_f32_16x16x32_bf16 v[116:119], a[36:39], v[68:71], v[116:119]\n\t" "s_waitcnt lgkmcnt(9)\n\t" \
   "v_mfma_f32_16x16x32_bf16 v[112:115], a[40:43], v[72:75], v[112:115]\n\t" "s_waitcnt lgkmcnt(8)\n\t" \
   "v_mfma_f32_16x16x32_bf16 v[116:119], a[44:47], v[76:79], v[116:119]\n\t" "s_waitcnt lgkmcnt(7)\n\t" \
   "v_mfma_f32_16x16x32_bf16 v[112:115], a[48:51], v[80:83], v[112:115]\n\t" "s_waitcnt lgkmcnt(6)\n\t" \
   "v_mfma_f32_16x16x32_bf16 v[116:119], a[52:55], v[84:87], v[116:119]\n\t" "s_waitcnt lgkmcnt(5)\n\t" \
   "v_mfma_f32_16x16x32_bf16 v[112:115], a[56:59], v[88:91], v[112:115]\n\t" "s_waitcnt lgkmcnt(4)\n\t" \
   "v_mfma_f32_16x16x32_bf16 v[116:119], a[60:63], v[92:95], v[116:119]\n\t" "s_waitcnt lgkmcnt(3)\n\t" \
   "v_mfma_f32_16x16x32_bf16 v[112:115], a[64:67], v[96:99], v[112:115]\n\t" "s_waitcnt lgkmcnt(2)\n\t" \
   "v_mfma_f32_16x16x32_bf16 v[116:119], a[68:71], v[100:103], v[116:119]\n\t" "s_waitcnt lgkmcnt(1)\n\t" \
   "v_mfma_f32_16x16x32_bf16 v[112:115], a[72:75], v[104:107], v[112:115]\n\t" "s_waitcnt lgkmcnt(0)\n\t" \
   "v_mfma_f32_16x16x32_bf16 v[116:119], a[76:79], v[108:111], v[116:119]\n\t" "v_mfma_f32_16x16x32_bf16 v[120:123], a[80:83], v[32:35], 0\n\t" \
   "v_mfma_f32_16x16x32_bf16 v[124:127], a[84:87], v[36:39], 0\n\t" "v_mfma_f32_16x16x32_bf16 v[120:123], a[88:91], v[40:43], v[120:123]\n\t" \
   "s_nop 1\n\t" "v_add_f32 v176, v112, v116\n\t" "v_sub_f32 v180, v112, v116\n\t" \
   "v_mfma_f32_16x16x32_bf16 v[124:127], a[92:95], v[44:47], v[124:127]\n\t" "v_add_f32 v177, v113, v117\n\t" "v_sub_f32 v181, v113, v117\n\t" \
   "v_mfma_f32_16x16x32_bf16 v[120:123], a[96:99], v[48:51], v[120:123]\n\t" "v_add_f32 v178, v114, v118\n\t" "v_sub_f32 v182, v114, v118\n\t" \
   "v_mfma_f32_16x16x32_bf16 v[124:127], a[100:103], v[52:55], v[124:127]\n\t" "v_add_f32 v179, v115, v119\n\t" "v_sub_f32 v183, v115, v119\n\t" \
   "v_mfma_f32_16x16x32_bf16 v[120:123], a[104:107], v[56:59], v[120:123]\n\t" "v_cvt_pk_bf16_f32 v184, v176, v177\n\t" \
   "v_cvt_pk_bf16_f32 v185, v178, v179\n\t" "v_mfma_f32_16x16x32_bf16 v[124:127], a[108:111], v[60:63], v[124:127]\n\t" \
   "v_cvt_pk_bf16_f32 v186, v180, v181\n\t" "v_cvt_pk_bf16_f32 v187, v182, v183\n\t" \
   "v_mfma_f32_16x16x32_bf16 v[120:123], a[112:115], v[64:67], v[120:123]\n\t" "v_lshlrev_b32 v176, 16, v184\n\t" \
   "v_and_b32 v177, 0xffff0000, v184\n\t" "v_mfma_f32_16x16x32_bf16 v[124:127], a[116:119], v[68:71], v[124:127]\n\t" \
   "v_lshlrev_b32 v178, 16, v185\n\t" "v_and_b32 v179, 0xffff0000, v185\n\t" \
   "v_mfma_f32_16x16x32_bf16 v[120:123], a[120:123], v[72:75], v[120:123]\n\t" "v_lshlrev_b32 v180, 16, v186\n\t" \
   "v_and_b32 v181, 0xffff0000, v186\n\t" "v_mfma_f32_16x16x32_bf16 v[124:127], a[124:127], v[76:79], v[124:127]\n\t" \
   "v_lshlrev_b32 v182, 16, v187\n\t" "v_and_b32 v183, 0xffff0000, v187\n\t" \
   "v_mfma_f32_16x16x32_bf16 v[120:123], a[128:131], v[80:83], v[120:123]\n\t" "v_add_f32 v176, v176, v152\n\t" "v_add_f32 v177, v177, v153\n\t" \
   "v_mfma_f32_16x16x32_bf16 v[124:127], a[132:135], v[84:87], v[124:127]\n\t" "v_add_f32 v178, v178, v154\n\t" "v_add_f32 v179, v179, v155\n\t" \
   "v_mfma_f32_16x16x32_bf16 v[120:123], a[136:139], v[88:91], v[120:123]\n\t" "v_add_f32 v180, v180, v156\n\t" "v_add_f32 v181, v181, v157\n\t" \
   "v_mfma_f32_16x16x32_bf16 v[124:127], a[140:143], v[92:95], v[124:127]\n\t" "v_add_f32 v182, v182, v158\n\t" "v_add_f32 v183, v183, v159\n\t" \
   "v_mfma_f32_16x16x32_bf16 v[120:123], a[144:147], v[96:99], v[120:123]\n\t" "v_cvt_pk_bf16_f32 v136, v176, v177\n\t" \
   "v_cvt_pk_bf16_f32 v137, v178, v179\n\t" "v_mfma_f32_16x16x32_bf16 v[124:127], a[148:151], v[100:103], v[124:127]\n\t" \
   "v_cvt_pk_bf16_f32 v140, v180, v181\n\t" "v_cvt_pk_bf16_f32 v141, v182, v183\n\t" \
   "v_mfma_f32_16x16x32_bf16 v[120:123], a[152:155], v[104:107], v[120:123]\n\t" \
   "v_mfma_f32_16x16x32_bf16 v[124:127], a[156:159], v[108:111], v[124:127]\n\t" "v_mfma_f32_16x16x32_bf16 v[128:131], a[160:163], v[32:35], 0\n\t" \
   "v_mfma_f32_16x16x32_bf16 v[132:135], a[164:167], v[36:39], 0\n\t" "v_mfma_f32_16x16x32_bf16 v[128:131], a[168:171], v[40:43], v[128:131]\n\t" \
   "s_nop 1\n\t" "v_add_f32 v176, v120, v124\n\t" "v_sub_f32 v180, v120, v124\n\t" \
   "v_mfma_f32_16x16x32_bf16 v[132:135], a[172:175], v[44:47], v[132:135]\n\t" "v_add_f32 v177, v121, v125\n\t" "v_sub_f32 v181, v121, v125\n\t" \
   "v_mfma_f32_16x16x32_bf16 v[128:131], a[176:179], v[48:51], v[128:131]\n\t" "v_add_f32 v178, v122, v126\n\t" "v_sub_f32 v182, v122, v126\n\t" \
   "v_mfma_f32_16x16x32_bf16 v[132:135], a[180:183], v[52:55], v[132:135]\n\t" "v_add_f32 v179, v123, v127\n\t" "v_sub_f32 v183, v123, v127\n\t" \
   "v_mfma_f32_16x16x32_bf16 v[128:131], a[184:187], v[56:59], v[128:131]\n\t" "v_cvt_pk_bf16_f32 v184, v176, v177\n\t" \
   "v_cvt_pk_bf16_f32 v185, v178, v179\n\t" "v_mfma_f32_16x16x32_bf16 v[132:135], a[188:191], v[60:63], v[132:135]\n\t" \
   "v_cvt_pk_bf16_f32 v186, v180, v181\n\t" "v_cvt_pk_bf16_f32 v187, v182, v183\n\t" \
   "v_mfma_f32_16x16x32_bf16 v[128:131], a[192:195], v[64:67], v[128:131]\n\t" "v_lshlrev_b32 v176, 16, v184\n\t" \
   "v_and_b32 v177, 0xffff0000, v184\n\t" "v_mfma_f32_16x16x32_bf16 v[132:135], a[196:199], v[68:71], v[132:135]\n\t" \
   "v_lshlrev_b32 v178, 16, v185\n\t" "v_and_b32 v179, 0xffff0000, v185\n\t" \
   "v_mfma_f32_16x16x32_bf16 v[128:131], a[200:203], v[72:75], v[128:131]\n\t" "v_lshlrev_b32 v180, 16, v186\n\t" \
   "v_and_b32 v181, 0xffff0000, v186\n\t" "v_mfma_f32_16x16x32_bf16 v[132:135], a[204:207], v[76:79], v[132:135]\n\t" \
   "v_lshlrev_b32 v182, 16, v187\n\t" "v_and_b32 v183, 0xffff0000, v187\n\t" \
   "v_mfma_f32_16x16x32_bf16 v[128:131], a[208:211], v[80:83], v[128:131]\n\t" "v_add_f32 v176, v176, v160\n\t" "v_add_f32 v177, v177, v161\n\t" \
   "v_mfma_f32_16x16x32_bf16 v[132:135], a[212:215], v[84:87], v[132:135]\n\t" "v_add_f32 v178, v178, v162\n\t" "v_add_f32 v179, v179, v163\n\t" \
   "v_mfma_f32_16x16x32_bf16 v[128:131], a[216:219], v[88:91], v[128:131]\n\t" "v_add_f32 v180, v180, v164\n\t" "v_add_f32 v181, v181, v165\n\t" \
   "v_mfma_f32_16x16x32_bf16 v[132:135], a[220:223], v[92:95], v[132:135]\n\t" "v_add_f32 v182, v182, v166\n\t" "v_add_f32 v183, v183, v167\n\t" \
   "v_mfma_f32_16x16x32_bf16 v[128:131], a[224:227], v[96:99], v[128:131]\n\t" "v_cvt_pk_bf16_f32 v138, v176, v177\n\t" \
   "v_cvt_pk_bf16_f32 v139, v178, v179\n\t" "v_mfma_f32_16x16x32_bf16 v[132:135], a[228:231], v[100:103], v[132:135]\n\t" \
   "v_cvt_pk_bf16_f32 v142, v180, v181\n\t" "v_cvt_pk_bf16_f32 v143, v182, v183\n\t" \
   "v_mfma_f32_16x16x32_bf16 v[128:131], a[232:235], v[104:107], v[128:131]\n\t" \
   "v_mfma_f32_16x16x32_bf16 v[132:135], a[236:239], v[108:111], v[132:135]\n\t" "s_nop 7\n\t" "v_add_f32 v176, v128, v132\n\t" \
   "v_sub_f32 v180, v128, v132\n\t" "v_add_f32 v177, v129, v133\n\t" "v_sub_f32 v181, v129, v133\n\t" "v_add_f32 v178, v130, v134\n\t" \
   "v_sub_f32 v182, v130, v134\n\t" "v_add_f32 v179, v131, v135\n\t" "v_sub_f32 v183, v131, v135\n\t" "v_cvt_pk_bf16_f32 v184, v176, v177\n\t" \
   "v_cvt_pk_bf16_f32 v185, v178, v179\n\t" "v_cvt_pk_bf16_f32 v186, v180, v181\n\t" "v_cvt_pk_bf16_f32 v187, v182, v183\n\t" \
   "v_lshlrev_b32 v176, 16, v184\n\t" "v_and_b32 v177, 0xffff0000, v184\n\t" "v_lshlrev_b32 v178, 16, v185\n\t" "v_and_b32 v179, 0xffff0000, v185\n\t" \
   "v_lshlrev_b32 v180, 16, v186\n\t" "v_and_b32 v181, 0xffff0000, v186\n\t" "v_lshlrev_b32 v182, 16, v187\n\t" "v_and_b32 v183, 0xffff0000, v187\n\t" \
   "v_add_f32 v176, v176, v168\n\t" "v_add_f32 v177, v177, v169\n\t" "v_add_f32 v178, v178, v170\n\t" "v_add_f32 v179, v179, v171\n\t" \
   "v_add_f32 v180, v180, v172\n\t" "v_add_f32 v181, v181, v173\n\t" "v_add_f32 v182, v182, v174\n\t" "v_add_f32 v183, v183, v175\n\t" \
   "v_cvt_pk_bf16_f32 v144, v176, v177\n\t" "v_cvt_pk_bf16_f32 v145, v178, v179\n\t" "v_cvt_pk_bf16_f32 v146, v180, v181\n\t" \
   "v_cvt_pk_bf16_f32 v147, v182, v183\n\t" "s_nop 1"
#define ID_CLOB "v32", "v33", "v34", "v35", "v36", "v37", "v38", "v39", "v40", "v41", "v42", "v43", "v44", "v45", "v46", "v47", "v48", "v49", "v50", "v51", "v52", "v53", "v54", "v55", "v56", "v57", "v58", "v59", "v60", "v61", "v62", "v63", "v64", "v65", "v66", "v67", "v68", "v69", "v70", "v71", "v72", "v73", "v74", "v75", "v76", "v77", "v78", "v79", "v80", "v81", "v82", "v83", "v84", "v85", "v86", "v87", "v88", "v89", "v90", "v91", "v92", "v93", "v94", "v95", "v96", "v97", "v98", "v99", "v100", "v101", "v102", "v103", "v104", "v105", "v106", "v107", "v108", "v109", "v110", "v111", "v112", "v113", "v114", "v115", "v116", "v117", "v118", "v119", "v120", "v121", "v122", "v123", "v124", "v125", "v126", "v127", "v128", "v129", "v130", "v131", "v132", "v133", "v134", "v135", "v152", "v153", "v154", "v155", "v156", "v157", "v158", "v159", "v160", "v161", "v162", "v163", "v164", "v165", "v166", "v167", "v168", "v169", "v170", "v171", "v172", "v173", "v174", "v175", "v176", "v177", "v178", "v179", "v180", "v181", "v182", "v183", "v184", "v185", "v186", "v187"
#define OU_ASM \
   "s_add_u32 s40, %[so], 0\n\t" "buffer_load_dwordx4 a[0:3], %[o], %[rs], s40 offen offset:0 sc1\n\t" \
   "buffer_load_dwordx4 a[32:35], %[o], %[rs], s40 offen offset:1024 sc1\n\t" \
   "buffer_load_dwordx4 a[64:67], %[o], %[rs], s40 offen offset:2048 sc1\n\t" \
   "buffer_load_dwordx4 a[96:99], %[o], %[rs], s40 offen offset:3072 sc1\n\t" "s_add_u32 s40, %[so], 4096\n\t" \
   "buffer_load_dwordx4 a[128:131], %[o], %[rs], s40 offen offset:0 sc1\n\t" \
   "buffer_load_dwordx4 a[160:163], %[o], %[rs], s40 offen offset:1024 sc1\n\t" \
   "buffer_load_dwordx4 a[192:195], %[o], %[rs], s40 offen offset:2048 sc1\n\t" \
   "buffer_load_dwordx4 a[224:227], %[o], %[rs], s40 offen offset:3072 sc1\n\t" "s_add_u32 s40, %[so], 32768\n\t" \
   "buffer_load_dwordx4 a[4:7], %[o], %[rs], s40 offen offset:0 sc1\n\t" "buffer_load_dwordx4 a[36:39], %[o], %[rs], s40 offen offset:1024 sc1\n\t" \
   "buffer_load_dwordx4 a[68:71], %[o], %[rs], s40 offen offset:2048 sc1\n\t" \
   "buffer_load_dwordx4 a[100:103], %[o], %[rs], s40 offen offset:3072 sc1\n\t" "s_add_u32 s40, %[so], 36864\n\t" \
   "buffer_load_dwordx4 a[132:135], %[o], %[rs], s40 offen offset:0 sc1\n\t" \
   "buffer_load_dwordx4 a[164:167], %[o], %[rs], s40 offen offset:1024 sc1\n\t" \
   "buffer_load_dwordx4 a[196:199], %[o], %[rs], s40 offen offset:2048 sc1\n\t" \
   "buffer_load_dwordx4 a[228:231], %[o], %[rs], s40 offen offset:3072 sc1\n\t" "s_add_u32 s40, %[so], 65536\n\t" \
   "buffer_load_dwordx4 a[8:11], %[o], %[rs], s40 offen offset:0 sc1\n\t" "buffer_load_dwordx4 a[40:43], %[o], %[rs], s40 offen offset:1024 sc1\n\t" \
   "buffer_load_dwordx4 a[72:75], %[o], %[rs], s40 offen offset:2048 sc1\n\t" \
   "buffer_load_dwordx4 a[104:107], %[o], %[rs], s40 offen offset:3072 sc1\n\t" "s_add_u32 s40, %[so], 69632\n\t" \
   "buffer_load_dwordx4 a[136:139], %[o], %[rs], s40 offen offset:0 sc1\n\t" \
   "buffer_load_dwordx4 a[168:171], %[o], %[rs], s40 offen offset:1024 sc1\n\t" \
   "buffer_load_dwordx4 a[200:203], %[o], %[rs], s40 offen offset:2048 sc1\n\t" \
   "buffer_load_dwordx4 a[232:235], %[o], %[rs], s40 offen offset:3072 sc1\n\t" "s_add_u32 s40, %[so], 98304\n\t" \
   "buffer_load_dwordx4 a[12:15], %[o], %[rs], s40 offen offset:0 sc1\n\t" "buffer_load_dwordx4 a[44:47], %[o], %[rs], s40 offen offset:1024 sc1\n\t" \
   "buffer_load_dwordx4 a[76:79], %[o], %[rs], s40 offen offset:2048 sc1\n\t" \
   "buffer_load_dwordx4 a[108:111], %[o], %[rs], s40 offen offset:3072 sc1\n\t" "s_add_u32 s40, %[so], 102400\n\t" \
   "buffer_load_dwordx4 a[140:143], %[o], %[rs], s40 offen offset:0 sc1\n\t" \
   "buffer_load_dwordx4 a[172:175], %[o], %[rs], s40 offen offset:1024 sc1\n\t" \
   "buffer_load_dwordx4 a[204:207], %[o], %[rs], s40 offen offset:2048 sc1\n\t" \
   "buffer_load_dwordx4 a[236:239], %[o], %[rs], s40 offen offset:3072 sc1\n\t" "s_add_u32 s40, %[so], 131072\n\t" \
   "buffer_load_dwordx4 a[16:19], %[o], %[rs], s40 offen offset:0 sc1\n\t" "buffer_load_dwordx4 a[48:51], %[o], %[rs], s40 offen offset:1024 sc1\n\t" \
   "buffer_load_dwordx4 a[80:83], %[o], %[rs], s40 offen offset:2048 sc1\n\t" \
   "buffer_load_dwordx4 a[112:115], %[o], %[rs], s40 offen offset:3072 sc1\n\t" "s_add_u32 s40, %[so], 135168\n\t" \
   "buffer_load_dwordx4 a[144:147], %[o], %[rs], s40 offen offset:0 sc1\n\t" \
   "buffer_load_dwordx4 a[176:179], %[o], %[rs], s40 offen offset:1024 sc1\n\t" \
   "buffer_load_dwordx4 a[208:211], %[o], %[rs], s40 offen offset:2048 sc1\n\t" \
   "buffer_load_dwordx4 a[240:243], %[o], %[rs], s40 offen offset:3072 sc1\n\t" "s_add_u32 s40, %[so], 163840\n\t" \
   "buffer_load_dwordx4 a[20:23], %[o], %[rs], s40 offen offset:0 sc1\n\t" "buffer_load_dwordx4 a[52:55], %[o], %[rs], s40 offen offset:1024 sc1\n\t" \
   "buffer_load_dwordx4 a[84:87], %[o], %[rs], s40 offen offset:2048 sc1\n\t" \
   "buffer_load_dwordx4 a[116:119], %[o], %[rs], s40 offen offset:3072 sc1\n\t" "s_add_u32 s40, %[so], 167936\n\t" \
   "buffer_load_dwordx4 a[148:151], %[o], %[rs], s40 offen offset:0 sc1\n\t" \
   "buffer_load_dwordx4 a[180:183], %[o], %[rs], s40 offen offset:1024 sc1\n\t" \
   "buffer_load_dwordx4 a[212:215], %[o], %[rs], s40 offen offset:2048 sc1\n\t" \
   "buffer_load_dwordx4 a[244:247], %[o], %[rs], s40 offen offset:3072 sc1\n\t" "s_add_u32 s40, %[so], 196608\n\t" \
   "buffer_load_dwordx4 a[24:27], %[o], %[rs], s40 offen offset:0 sc1\n\t" "buffer_load_dwordx4 a[56:59], %[o], %[rs], s40 offen offset:1024 sc1\n\t" \
   "buffer_load_dwordx4 a[88:91], %[o], %[rs], s40 offen offset:2048 sc1\n\t" \
   "buffer_load_dwordx4 a[120:123], %[o], %[rs], s40 offen offset:3072 sc1\n\t" "s_add_u32 s40, %[so], 200704\n\t" \
   "buffer_load_dwordx4 a[152:155], %[o], %[rs], s40 offen offset:0 sc1\n\t" \
   "buffer_load_dwordx4 a[184:187], %[o], %[rs], s40 offen offset:1024 sc1\n\t" \
   "buffer_load_dwordx4 a[216:219], %[o], %[rs], s40 offen offset:2048 sc1\n\t" \
   "buffer_load_dwordx4 a[248:251], %[o], %[rs], s40 offen offset:3072 sc1\n\t" "s_add_u32 s40, %[so], 229376\n\t" \
   "buffer_load_dwordx4 a[28:31], %[o], %[rs], s40 offen offset:0 sc1\n\t" "buffer_load_dwordx4 a[60:63], %[o], %[rs], s40 offen offset:1024 sc1\n\t" \
   "buffer_load_dwordx4 a[92:95], %[o], %[rs], s40 offen offset:2048 sc1\n\t" \
   "buffer_load_dwordx4 a[124:127], %[o], %[rs], s40 offen offset:3072 sc1\n\t" "s_add_u32 s40, %[so], 233472\n\t" \
   "buffer_load_dwordx4 a[156:159], %[o], %[rs], s40 offen offset:0 sc1\n\t" \
   "buffer_load_dwordx4 a[188:191], %[o], %[rs], s40 offen offset:1024 sc1\n\t" \
   "buffer_load_dwordx4 a[220:223], %[o], %[rs], s40 offen offset:2048 sc1\n\t" \
   "buffer_load_dwordx4 a[252:255], %[o], %[rs], s40 offen offset:3072 sc1\n\t" "s_waitcnt vmcnt(0)"
#define OU_CLOB "s40"
#define OY_ASM \
   "s_add_u32 s40, %[sy], 0\n\t" "buffer_load_dwordx4 v[192:195], %[yo], %[rs], s40 offen offset:0 sc1\n\t" "s_add_u32 s40, %[sy], 16384\n\t" \
   "buffer_load_dwordx4 v[196:199], %[yo], %[rs], s40 offen offset:0 sc1\n\t" "s_add_u32 s40, %[sy], 32768\n\t" \
   "buffer_load_dwordx4 v[200:203], %[yo], %[rs], s40 offen offset:0 sc1\n\t" "s_add_u32 s40, %[sy], 49152\n\t" \
   "buffer_load_dwordx4 v[204:207], %[yo], %[rs], s40 offen offset:0 sc1\n\t" "s_add_u32 s40, %[sy], 65536\n\t" \
   "buffer_load_dwordx4 v[208:211], %[yo], %[rs], s40 offen offset:0 sc1\n\t" "s_add_u32 s40, %[sy], 81920\n\t" \
   "buffer_load_dwordx4 v[212:215], %[yo], %[rs], s40 offen offset:0 sc1\n\t" "s_add_u32 s40, %[sy], 98304\n\t" \
   "buffer_load_dwordx4 v[216:219], %[yo], %[rs], s40 offen offset:0 sc1\n\t" "s_add_u32 s40, %[sy], 114688\n\t" \
   "buffer_load_dwordx4 v[220:223], %[yo], %[rs], s40 offen offset:0 sc1\n\t" "s_add_u32 s40, %[sy], 0\n\t" \
   "buffer_load_dwordx4 v[224:227], %[yo], %[rs], s40 offen offset:1024 sc1\n\t" "s_add_u32 s40, %[sy], 16384\n\t" \
   "buffer_load_dwordx4 v[228:231], %[yo], %[rs], s40 offen offset:1024 sc1\n\t" "s_add_u32 s40, %[sy], 32768\n\t" \
   "buffer_load_dwordx4 v[232:235], %[yo], %[rs], s40 offen offset:1024 sc1\n\t" "s_add_u32 s40, %[sy], 49152\n\t" \
   "buffer_load_dwordx4 v[236:239], %[yo], %[rs], s40 offen offset:1024 sc1\n\t" "s_add_u32 s40, %[sy], 65536\n\t" \
   "buffer_load_dwordx4 v[240:243], %[yo], %[rs], s40 offen offset:1024 sc1\n\t" "s_add_u32 s40, %[sy], 81920\n\t" \
   "buffer_load_dwordx4 v[244:247], %[yo], %[rs], s40 offen offset:1024 sc1\n\t" "s_add_u32 s40, %[sy], 98304\n\t" \
   "buffer_load_dwordx4 v[248:251], %[yo], %[rs], s40 offen offset:1024 sc1\n\t" "s_add_u32 s40, %[sy], 114688\n\t" \
   "buffer_load_dwordx4 v[252:255], %[yo], %[rs], s40 offen offset:1024 sc1\n\t" "v_mov_b32 %[m], 0\n\t" "s_waitcnt vmcnt(15)\n\t" \
   "v_max3_u32 %[m], %[m], v192, v194\n\t" "s_waitcnt vmcnt(14)\n\t" "v_max3_u32 %[m], %[m], v196, v198\n\t" "s_waitcnt vmcnt(13)\n\t" \
   "v_max3_u32 %[m], %[m], v200, v202\n\t" "s_waitcnt vmcnt(12)\n\t" "v_max3_u32 %[m], %[m], v204, v206\n\t" "s_waitcnt vmcnt(11)\n\t" \
   "v_max3_u32 %[m], %[m], v208, v210\n\t" "s_waitcnt vmcnt(10)\n\t" "v_max3_u32 %[m], %[m], v212, v214\n\t" "s_waitcnt vmcnt(9)\n\t" \
   "v_max3_u32 %[m], %[m], v216, v218\n\t" "s_waitcnt vmcnt(8)\n\t" "v_max3_u32 %[m], %[m], v220, v222\n\t" "s_waitcnt vmcnt(7)\n\t" \
   "v_max3_u32 %[m], %[m], v224, v226\n\t" "s_waitcnt vmcnt(6)\n\t" "v_max3_u32 %[m], %[m], v228, v230\n\t" "s_waitcnt vmcnt(5)\n\t" \
   "v_max3_u32 %[m], %[m], v232, v234\n\t" "s_waitcnt vmcnt(4)\n\t" "v_max3_u32 %[m], %[m], v236, v238\n\t" "s_waitcnt vmcnt(3)\n\t" \
   "v_max3_u32 %[m], %[m], v240, v242\n\t" "s_waitcnt vmcnt(2)\n\t" "v_max3_u32 %[m], %[m], v244, v246\n\t" "s_waitcnt vmcnt(1)\n\t" \
   "v_max3_u32 %[m], %[m], v248, v250\n\t" "s_waitcnt vmcnt(0)\n\t" "v_max3_u32 %[m], %[m], v252, v254\n\t" "s_nop 0"
#define OY_CLOB "s40"
#define O1_ASM \
   "s_add_u32 s40, %[sp], 0\n\t" "buffer_load_dwordx4 v[112:115], %[po], %[rs], s40 offen offset:0 sc1\n\t" \
   "buffer_load_dwordx4 v[116:119], %[po], %[rs], s40 offen offset:2048 sc1\n\t" "s_add_u32 s40, %[sp], 4096\n\t" \
   "buffer_load_dwordx4 v[120:123], %[po], %[rs], s40 offen offset:0 sc1\n\t" \
   "buffer_load_dwordx4 v[124:127], %[po], %[rs], s40 offen offset:2048 sc1\n\t" "s_add_u32 s40, %[sp], 8192\n\t" \
   "buffer_load_dwordx4 v[128:131], %[po], %[rs], s40 offen offset:0 sc1\n\t" \
   "buffer_load_dwordx4 v[132:135], %[po], %[rs], s40 offen offset:2048 sc1\n\t" "s_add_u32 s40, %[sp], 12288\n\t" \
   "buffer_load_dwordx4 v[136:139], %[po], %[rs], s40 offen offset:0 sc1\n\t" \
   "buffer_load_dwordx4 v[140:143], %[po], %[rs], s40 offen offset:2048 sc1\n\t" \
   "buffer_load_dwordx4 v[144:147], %[mo], %[rs], %[sm] offen offset:0 sc1\n\t" \
   "buffer_load_dwordx4 v[148:151], %[mo], %[rs], %[sm] offen offset:2048 sc1\n\t" "v_mov_b32 v160, 16\n\t" \
   "buffer_atomic_smax %[at], %[ao], %[rs], 0 offen offset:4 sc1\n\t" "buffer_atomic_add v160, %[ao], %[rs], 0 offen sc1\n\t" "s_waitcnt vmcnt(11)\n\t" \
   "v_mfma_f32_16x16x32_bf16 v[32:35], a[0:3], v[112:115], 0\n\t" "v_mfma_f32_16x16x32_bf16 v[36:39], a[4:7], v[112:115], 0\n\t" \
   "v_mfma_f32_16x16x32_bf16 v[40:43], a[8:11], v[112:115], 0\n\t" "v_mfma_f32_16x16x32_bf16 v[44:47], a[12:15], v[112:115], 0\n\t" \
   "v_mfma_f32_16x16x32_bf16 v[48:51], a[16:19], v[112:115], 0\n\t" "v_mfma_f32_16x16x32_bf16 v[52:55], a[20:23], v[112:115], 0\n\t" \
   "v_mfma_f32_16x16x32_bf16 v[56:59], a[24:27], v[112:115], 0\n\t" "v_mfma_f32_16x16x32_bf16 v[60:63], a[28:31], v[112:115], 0\n\t" \
   "s_waitcnt vmcnt(10)\n\t" "v_mfma_f32_16x16x32_bf16 v[32:35], a[32:35], v[116:119], v[32:35]\n\t" \
   "v_mfma_f32_16x16x32_bf16 v[36:39], a[36:39], v[116:119], v[36:39]\n\t" "v_mfma_f32_16x16x32_bf16 v[40:43], a[40:43], v[116:119], v[40:43]\n\t" \
   "v_mfma_f32_16x16x32_bf16 v[44:47], a[44:47], v[116:119], v[44:47]\n\t" "v_mfma_f32_16x16x32_bf16 v[48:51], a[48:51], v[116:119], v[48:51]\n\t" \
   "v_mfma_f32_16x16x32_bf16 v[52:55], a[52:55], v[116:119], v[52:55]\n\t" "v_mfma_f32_16x16x32_bf16 v[56:59], a[56:59], v[116:119], v[56:59]\n\t" \
   "v_mfma_f32_16x16x32_bf16 v[60:63], a[60:63], v[116:119], v[60:63]\n\t" "s_waitcnt vmcnt(9)\n\t" \
   "v_mfma_f32_16x16x32_bf16 v[32:35], a[64:67], v[120:123], v[32:35]\n\t" "v_mfma_f32_16x16x32_bf16 v[36:39], a[68:71], v[120:123], v[36:39]\n\t" \
   "v_mfma_f32_16x16x32_bf16 v[40:43], a[72:75], v[120:123], v[40:43]\n\t" "v_mfma_f32_16x16x32_bf16 v[44:47], a[76:79], v[120:123], v[44:47]\n\t" \
   "v_mfma_f32_16x16x32_bf16 v[48:51], a[80:83], v[120:123], v[48:51]\n\t" "v_mfma_f32_16x16x32_bf16 v[52:55], a[84:87], v[120:123], v[52:55]\n\t" \
   "v_mfma_f32_16x16x32_bf16 v[56:59], a[88:91], v[120:123], v[56:59]\n\t" "v_mfma_f32_16x16x32_bf16 v[60:63], a[92:95], v[120:123], v[60:63]\n\t" \
   "s_waitcnt vmcnt(8)\n\t" "v_mfma_f32_16x16x32_bf16 v[32:35], a[96:99], v[124:127], v[32:35]\n\t" \
   "v_mfma_f32_16x16x32_bf16 v[36:39], a[100:103], v[124:127], v[36:39]\n\t" "v_mfma_f32_16x16x32_bf16 v[40:43], a[104:107], v[124:127], v[40:43]\n\t" \
   "v_mfma_f32_16x16x32_bf16 v[44:47], a[108:111], v[124:127], v[44:47]\n\t" "v_mfma_f32_16x16x32_bf16 v[48:51], a[112:115], v[124:127], v[48:51]\n\t" \
   "v_mfma_f32_16x16x32_bf16 v[52:55], a[116:119], v[124:127], v[52:55]\n\t" "v_mfma_f32_16x16x32_bf16 v[56:59], a[120:123], v[124:127], v[56:59]\n\t" \
   "v_mfma_f32_16x16x32_bf16 v[60:63], a[124:127], v[124:127], v[60:63]\n\t" "s_waitcnt vmcnt(7)\n\t" \
   "v_mfma_f32_16x16x32_bf16 v[32:35], a[128:131], v[128:131], v[32:35]\n\t" "v_mfma_f32_16x16x32_bf16 v[36:39], a[132:135], v[128:131], v[36:39]\n\t" \
   "v_mfma_f32_16x16x32_bf16 v[40:43], a[136:139], v[128:131], v[40:43]\n\t" "v_mfma_f32_16x16x32_bf16 v[44:47], a[140:143], v[128:131], v[44:47]\n\t" \
   "v_mfma_f32_16x16x32_bf16 v[48:51], a[144:147], v[128:131], v[48:51]\n\t" "v_mfma_f32_16x16x32_bf16 v[52:55], a[148:151], v[128:131], v[52:55]\n\t" \
   "v_mfma_f32_16x16x32_bf16 v[56:59], a[152:155], v[128:131], v[56:59]\n\t" "v_mfma_f32_16x16x32_bf16 v[60:63], a[156:159], v[128:131], v[60:63]\n\t" \
   "s_waitcnt vmcnt(6)\n\t" "v_mfma_f32_16x16x32_bf16 v[32:35], a[160:163], v[132:135], v[32:35]\n\t" \
   "v_mfma_f32_16x16x32_bf16 v[36:39], a[164:167], v[132:135], v[36:39]\n\t" "v_mfma_f32_16x16x32_bf16 v[40:43], a[168:171], v[132:135], v[40:43]\n\t" \
   "v_mfma_f32_16x16x32_bf16 v[44:47], a[172:175], v[132:135], v[44:47]\n\t" "v_mfma_f32_16x16x32_bf16 v[48:51], a[176:179], v[132:135], v[48:51]\n\t" \
   "v_mfma_f32_16x16x32_bf16 v[52:55], a[180:183], v[132:135], v[52:55]\n\t" "v_mfma_f32_16x16x32_bf16 v[56:59], a[184:187], v[132:135], v[56:59]\n\t" \
   "v_mfma_f32_16x16x32_bf16 v[60:63], a[188:191], v[132:135], v[60:63]\n\t" "s_waitcnt vmcnt(5)\n\t" \
   "v_mfma_f32_16x16x32_bf16 v[32:35], a[192:195], v[136:139], v[32:35]\n\t" "v_mfma_f32_16x16x32_bf16 v[36:39], a[196:199], v[136:139], v[36:39]\n\t" \
   "v_mfma_f32_16x16x32_bf16 v[40:43], a[200:203], v[136:139], v[40:43]\n\t" "v_mfma_f32_16x16x32_bf16 v[44:47], a[204:207], v[136:139], v[44:47]\n\t" \
   "v_mfma_f32_16x16x32_bf16 v[48:51], a[208:211], v[136:139], v[48:51]\n\t" "v_mfma_f32_16x16x32_bf16 v[52:55], a[212:215], v[136:139], v[52:55]\n\t" \
   "v_mfma_f32_16x16x32_bf16 v[56:59], a[216:219], v[136:139], v[56:59]\n\t" "v_mfma_f32_16x16x32_bf16 v[60:63], a[220:223], v[136:139], v[60:63]\n\t" \
   "s_waitcnt vmcnt(4)\n\t" "s_waitcnt vmcnt(2)\n\t" "v_mfma_f32_16x16x32_bf16 v[32:35], a[224:227], v[140:143], v[32:35]\n\t" \
   "v_swap_b32 v145, v146\n\t" "v_mfma_f32_16x16x32_bf16 v[36:39], a[228:231], v[140:143], v[36:39]\n\t" "v_swap_b32 v149, v150\n\t" \
   "v_mfma_f32_16x16x32_bf16 v[40:43], a[232:235], v[140:143], v[40:43]\n\t" "v_mfma_f32_16x16x32_bf16 v[44:47], a[236:239], v[140:143], v[44:47]\n\t" \
   "v_mfma_f32_16x16x32_bf16 v[48:51], a[240:243], v[140:143], v[48:51]\n\t" "v_mfma_f32_16x16x32_bf16 v[52:55], a[244:247], v[140:143], v[52:55]\n\t" \
   "v_mfma_f32_16x16x32_bf16 v[56:59], a[248:251], v[140:143], v[56:59]\n\t" "v_mfma_f32_16x16x32_bf16 v[60:63], a[252:255], v[140:143], v[60:63]\n\t" \
   "v_mfma_f32_16x16x32_bf16 v[32:35], v[192:195], v[144:147], v[32:35]\n\t" "v_mfma_f32_16x16x32_bf16 v[36:39], v[196:199], v[144:147], v[36:39]\n\t" \
   "v_mfma_f32_16x16x32_bf16 v[40:43], v[200:203], v[144:147], v[40:43]\n\t" "v_mfma_f32_16x16x32_bf16 v[44:47], v[204:207], v[144:147], v[44:47]\n\t" \
   "v_mfma_f32_16x16x32_bf16 v[48:51], v[208:211], v[144:147], v[48:51]\n\t" "v_mfma_f32_16x16x32_bf16 v[52:55], v[212:215], v[144:147], v[52:55]\n\t" \
   "v_mfma_f32_16x16x32_bf16 v[56:59], v[216:219], v[144:147], v[56:59]\n\t" "v_mfma_f32_16x16x32_bf16 v[60:63], v[220:223], v[144:147], v[60:63]\n\t" \
   "v_mfma_f32_16x16x32_bf16 v[32:35], v[224:227], v[148:151], v[32:35]\n\t" "v_mfma_f32_16x16x32_bf16 v[36:39], v[228:231], v[148:151], v[36:39]\n\t" \
   "v_mfma_f32_16x16x32_bf16 v[40:43], v[232:235], v[148:151], v[40:43]\n\t" "v_mfma_f32_16x16x32_bf16 v[44:47], v[236:239], v[148:151], v[44:47]\n\t" \
   "ds_write_b128 %[lw], v[32:35] offset:0\n\t" "v_mfma_f32_16x16x32_bf16 v[48:51], v[240:243], v[148:151], v[48:51]\n\t" \
   "ds_write_b128 %[lw], v[36:39] offset:1024\n\t" "v_mfma_f32_16x16x32_bf16 v[52:55], v[244:247], v[148:151], v[52:55]\n\t" \
   "ds_write_b128 %[lw], v[40:43] offset:2048\n\t" "v_mfma_f32_16x16x32_bf16 v[56:59], v[248:251], v[148:151], v[56:59]\n\t" \
   "ds_write_b128 %[lw], v[44:47] offset:3072\n\t" "v_mfma_f32_16x16x32_bf16 v[60:63], v[252:255], v[148:151], v[60:63]\n\t" \
   "ds_write_b128 %[lw], v[48:51] offset:4096\n\t" "s_nop 1\n\t" "s_nop 3\n\t" "ds_write_b128 %[lw], v[52:55] offset:5120\n\t" "s_nop 3\n\t" \
   "ds_write_b128 %[lw], v[56:59] offset:6144\n\t" "s_nop 3\n\t" "ds_write_b128 %[lw], v[60:63] offset:7168\n\t" "s_waitcnt lgkmcnt(0)\n\t" \
   "s_barrier\n\t" "ds_read_b128 v[112:115], %[lr] offset:0\n\t" "ds_read_b128 v[116:119], %[lr] offset:256\n\t" \
   "ds_read_b128 v[120:123], %[lr] offset:8192\n\t" "ds_read_b128 v[124:127], %[lr] offset:8448\n\t" "ds_read_b128 v[128:131], %[lr] offset:16384\n\t" \
   "ds_read_b128 v[132:135], %[lr] offset:16640\n\t" "ds_read_b128 v[136:139], %[lr] offset:24576\n\t" \
   "ds_read_b128 v[140:143], %[lr] offset:24832\n\t" "s_waitcnt lgkmcnt(4)\n\t" "v_add_f32 v112, v112, v120\n\t" "v_add_f32 v113, v113, v121\n\t" \
   "v_add_f32 v114, v114, v122\n\t" "v_add_f32 v115, v115, v123\n\t" "v_add_f32 v116, v116, v124\n\t" "v_add_f32 v117, v117, v125\n\t" \
   "v_add_f32 v118, v118, v126\n\t" "v_add_f32 v119, v119, v127\n\t" "s_waitcnt lgkmcnt(0)\n\t" "v_add_f32 v128, v128, v136\n\t" \
   "v_add_f32 v129, v129, v137\n\t" "v_add_f32 v130, v130, v138\n\t" "v_add_f32 v131, v131, v139\n\t" "v_add_f32 v132, v132, v140\n\t" \
   "v_add_f32 v133, v133, v141\n\t" "v_add_f32 v134, v134, v142\n\t" "v_add_f32 v135, v135, v143\n\t" "v_add_f32 v112, v112, v128\n\t" \
   "v_add_f32 v113, v113, v129\n\t" "v_add_f32 v114, v114, v130\n\t" "v_add_f32 v115, v115, v131\n\t" "v_add_f32 v116, v116, v132\n\t" \
   "v_add_f32 v117, v117, v133\n\t" "v_add_f32 v118, v118, v134\n\t" "v_add_f32 v119, v119, v135\n\t" "v_cvt_pk_bf16_f32 v96, v112, v113\n\t" \
   "v_cvt_pk_bf16_f32 v97, v114, v115\n\t" "v_cvt_pk_bf16_f32 v98, v116, v117\n\t" "v_cvt_pk_bf16_f32 v99, v118, v119\n\t" "s_nop 1"
#define O1_CLOB "v32", "v33", "v34", "v35", "v36", "v37", "v38", "v39", "v40", "v41", "v42", "v43", "v44", "v45", "v46", "v47", "v48", "v49", "v50", "v51", "v52", "v53", "v54", "v55", "v56", "v57", "v58", "v59", "v60", "v61", "v62", "v63", "v64", "v65", "v66", "v67", "v68", "v69", "v70", "v71", "v72", "v73", "v74", "v75", "v76", "v77", "v78", "v79", "v80", "v81", "v82", "v83", "v84", "v85", "v86", "v87", "v88", "v89", "v90", "v91", "v92", "v93", "v94", "v95", "v112", "v113", "v114", "v115", "v116", "v117", "v118", "v119", "v120", "v121", "v122", "v123", "v124", "v125", "v126", "v127", "v128", "v129", "v130", "v131", "v132", "v133", "v134", "v135", "v136", "v137", "v138", "v139", "v140", "v141", "v142", "v143", "v144", "v145", "v146", "v147", "v148", "v149", "v150", "v151", "v152", "v153", "v154", "v155", "v156", "v157", "v158", "v159", "v160", "v161", "v162", "v163", "v164", "v165", "v166", "v167", "v168", "v169", "v170", "v171", "v172", "v173", "v174", "v175", "v176", "v177", "v178", "v179", "v180", "v181", "v182", "v183", "v184", "v185", "v186", "v187", "v188", "v189", "v190", "v191", "s40"
#define O2_ASM \
   "s_add_u32 s40, %[sp], 0\n\t" "buffer_load_dwordx4 v[112:115], %[po], %[rs], s40 offen offset:0 sc1\n\t" \
   "buffer_load_dwordx4 v[116:119], %[po], %[rs], s40 offen offset:2048 sc1\n\t" "s_add_u32 s40, %[sp], 4096\n\t" \
   "buffer_load_dwordx4 v[120:123], %[po], %[rs], s40 offen offset:0 sc1\n\t" \
   "buffer_load_dwordx4 v[124:127], %[po], %[rs], s40 offen offset:2048 sc1\n\t" "s_add_u32 s40, %[sp], 8192\n\t" \
   "buffer_load_dwordx4 v[128:131], %[po], %[rs], s40 offen offset:0 sc1\n\t" \
   "buffer_load_dwordx4 v[132:135], %[po], %[rs], s40 offen offset:2048 sc1\n\t" "s_add_u32 s40, %[sp], 12288\n\t" \
   "buffer_load_dwordx4 v[136:139], %[po], %[rs], s40 offen offset:0 sc1\n\t" \
   "buffer_load_dwordx4 v[140:143], %[po], %[rs], s40 offen offset:2048 sc1\n\t" "s_add_u32 s40, %[sp], 0\n\t" \
   "buffer_load_dwordx4 v[152:155], %[po], %[rs], s40 offen offset:1024 sc1\n\t" \
   "buffer_load_dwordx4 v[156:159], %[po], %[rs], s40 offen offset:3072 sc1\n\t" "s_add_u32 s40, %[sp], 4096\n\t" \
   "buffer_load_dwordx4 v[160:163], %[po], %[rs], s40 offen offset:1024 sc1\n\t" \
   "buffer_load_dwordx4 v[164:167], %[po], %[rs], s40 offen offset:3072 sc1\n\t" "s_add_u32 s40, %[sp], 8192\n\t" \
   "buffer_load_dwordx4 v[168:171], %[po], %[rs], s40 offen offset:1024 sc1\n\t" \
   "buffer_load_dwordx4 v[172:175], %[po], %[rs], s40 offen offset:3072 sc1\n\t" "s_add_u32 s40, %[sp], 12288\n\t" \
   "buffer_load_dwordx4 v[176:179], %[po], %[rs], s40 offen offset:1024 sc1\n\t" \
   "buffer_load_dwordx4 v[180:183], %[po], %[rs], s40 offen offset:3072 sc1\n\t" \
   "buffer_load_dwordx4 v[144:147], %[mo], %[rs], %[sm] offen offset:0 sc1\n\t" \
   "buffer_load_dwordx4 v[148:151], %[mo], %[rs], %[sm] offen offset:2048 sc1\n\t" \
   "buffer_load_dwordx4 v[184:187], %[mo], %[rs], %[sm] offen offset:1024 sc1\n\t" \
   "buffer_load_dwordx4 v[188:191], %[mo], %[rs], %[sm] offen offset:3072 sc1\n\t" "s_add_u32 s40, %[sy], 0\n\t" \
   "buffer_load_dwordx4 v[192:195], %[yo], %[rs], s40 offen offset:0 sc1\n\t" "s_add_u32 s40, %[sy], 16384\n\t" \
   "buffer_load_dwordx4 v[196:199], %[yo], %[rs], s40 offen offset:0 sc1\n\t" "s_add_u32 s40, %[sy], 32768\n\t" \
   "buffer_load_dwordx4 v[200:203], %[yo], %[rs], s40 offen offset:0 sc1\n\t" "s_add_u32 s40, %[sy], 49152\n\t" \
   "buffer_load_dwordx4 v[204:207], %[yo], %[rs], s40 offen offset:0 sc1\n\t" "s_add_u32 s40, %[sy], 65536\n\t" \
   "buffer_load_dwordx4 v[208:211], %[yo], %[rs], s40 offen offset:0 sc1\n\t" "s_add_u32 s40, %[sy], 81920\n\t" \
   "buffer_load_dwordx4 v[212:215], %[yo], %[rs], s40 offen offset:0 sc1\n\t" "s_add_u32 s40, %[sy], 98304\n\t" \
   "buffer_load_dwordx4 v[216:219], %[yo], %[rs], s40 offen offset:0 sc1\n\t" "s_add_u32 s40, %[sy], 114688\n\t" \
   "buffer_load_dwordx4 v[220:223], %[yo], %[rs], s40 offen offset:0 sc1\n\t" "s_add_u32 s40, %[sy], 0\n\t" \
   "buffer_load_dwordx4 v[224:227], %[yo], %[rs], s40 offen offset:1024 sc1\n\t" "s_add_u32 s40, %[sy], 16384\n\t" \
   "buffer_load_dwordx4 v[228:231], %[yo], %[rs], s40 offen offset:1024 sc1\n\t" "s_add_u32 s40, %[sy], 32768\n\t" \
   "buffer_load_dwordx4 v[232:235], %[yo], %[rs], s40 offen offset:1024 sc1\n\t" "s_add_u32 s40, %[sy], 49152\n\t" \
   "buffer_load_dwordx4 v[236:239], %[yo], %[rs], s40 offen offset:1024 sc1\n\t" "s_add_u32 s40, %[sy], 65536\n\t" \
   "buffer_load_dwordx4 v[240:243], %[yo], %[rs], s40 offen offset:1024 sc1\n\t" "s_add_u32 s40, %[sy], 81920\n\t" \
   "buffer_load_dwordx4 v[244:247], %[yo], %[rs], s40 offen offset:1024 sc1\n\t" "s_add_u32 s40, %[sy], 98304\n\t" \
   "buffer_load_dwordx4 v[248:251], %[yo], %[rs], s40 offen offset:1024 sc1\n\t" "s_add_u32 s40, %[sy], 114688\n\t" \
   "buffer_load_dwordx4 v[252:255], %[yo], %[rs], s40 offen offset:1024 sc1\n\t" "s_waitcnt vmcnt(35)\n\t" \
   "v_mfma_f32_16x16x32_bf16 v[32:35], a[0:3], v[112:115], 0\n\t" "v_mfma_f32_16x16x32_bf16 v[36:39], a[4:7], v[112:115], 0\n\t" \
   "v_mfma_f32_16x16x32_bf16 v[40:43], a[8:11], v[112:115], 0\n\t" "v_mfma_f32_16x16x32_bf16 v[44:47], a[12:15], v[112:115], 0\n\t" \
   "v_mfma_f32_16x16x32_bf16 v[48:51], a[16:19], v[112:115], 0\n\t" "v_mfma_f32_16x16x32_bf16 v[52:55], a[20:23], v[112:115], 0\n\t" \
   "v_mfma_f32_16x16x32_bf16 v[56:59], a[24:27], v[112:115], 0\n\t" "v_mfma_f32_16x16x32_bf16 v[60:63], a[28:31], v[112:115], 0\n\t" \
   "s_waitcnt vmcnt(34)\n\t" "v_mfma_f32_16x16x32_bf16 v[32:35], a[32:35], v[116:119], v[32:35]\n\t" \
   "v_mfma_f32_16x16x32_bf16 v[36:39], a[36:39], v[116:119], v[36:39]\n\t" "v_mfma_f32_16x16x32_bf16 v[40:43], a[40:43], v[116:119], v[40:43]\n\t" \
   "v_mfma_f32_16x16x32_bf16 v[44:47], a[44:47], v[116:119], v[44:47]\n\t" "v_mfma_f32_16x16x32_bf16 v[48:51], a[48:51], v[116:119], v[48:51]\n\t" \
   "v_mfma_f32_16x16x32_bf16 v[52:55], a[52:55], v[116:119], v[52:55]\n\t" "v_mfma_f32_16x16x32_bf16 v[56:59], a[56:59], v[116:119], v[56:59]\n\t" \
   "v_mfma_f32_16x16x32_bf16 v[60:63], a[60:63], v[116:119], v[60:63]\n\t" "s_waitcnt vmcnt(33)\n\t" \
   "v_mfma_f32_16x16x32_bf16 v[32:35], a[64:67], v[120:123], v[32:35]\n\t" "v_mfma_f32_16x16x32_bf16 v[36:39], a[68:71], v[120:123], v[36:39]\n\t" \
   "v_mfma_f32_16x16x32_bf16 v[40:43], a[72:75], v[120:123], v[40:43]\n\t" "v_mfma_f32_16x16x32_bf16 v[44:47], a[76:79], v[120:123], v[44:47]\n\t" \
   "v_mfma_f32_16x16x32_bf16 v[48:51], a[80:83], v[120:123], v[48:51]\n\t" "v_mfma_f32_16x16x32_bf16 v[52:55], a[84:87], v[120:123], v[52:55]\n\t" \
   "v_mfma_f32_16x16x32_bf16 v[56:59], a[88:91], v[120:123], v[56:59]\n\t" "v_mfma_f32_16x16x32_bf16 v[60:63], a[92:95], v[120:123], v[60:63]\n\t" \
   "s_waitcnt vmcnt(32)\n\t" "v_mfma_f32_16x16x32_bf16 v[32:35], a[96:99], v[124:127], v[32:35]\n\t" \
   "v_mfma_f32_16x16x32_bf16 v[36:39], a[100:103], v[124:127], v[36:39]\n\t" "v_mfma_f32_16x16x32_bf16 v[40:43], a[104:107], v[124:127], v[40:43]\n\t" \
   "v_mfma_f32_16x16x32_bf16 v[44:47], a[108:111], v[124:127], v[44:47]\n\t" "v_mfma_f32_16x16x32_bf16 v[48:51], a[112:115], v[124:127], v[48:51]\n\t" \
   "v_mfma_f32_16x16x32_bf16 v[52:55], a[116:119], v[124:127], v[52:55]\n\t" "v_mfma_f32_16x16x32_bf16 v[56:59], a[120:123], v[124:127], v[56:59]\n\t" \
   "v_mfma_f32_16x16x32_bf16 v[60:63], a[124:127], v[124:127], v[60:63]\n\t" "s_waitcnt vmcnt(31)\n\t" \
   "v_mfma_f32_16x16x32_bf16 v[32:35], a[128:131], v[128:131], v[32:35]\n\t" "v_mfma_f32_16x16x32_bf16 v[36:39], a[132:135], v[128:131], v[36:39]\n\t" \
   "v_mfma_f32_16x16x32_bf16 v[40:43], a[136:139], v[128:131], v[40:43]\n\t" "v_mfma_f32_16x16x32_bf16 v[44:47], a[140:143], v[128:131], v[44:47]\n\t" \
   "v_mfma_f32_16x16x32_bf16 v[48:51], a[144:147], v[128:131], v[48:51]\n\t" "v_mfma_f32_16x16x32_bf16 v[52:55], a[148:151], v[128:131], v[52:55]\n\t" \
   "v_mfma_f32_16x16x32_bf16 v[56:59], a[152:155], v[128:131], v[56:59]\n\t" "v_mfma_f32_16x16x32_bf16 v[60:63], a[156:159], v[128:131], v[60:63]\n\t" \
   "s_waitcnt vmcnt(30)\n\t" "v_mfma_f32_16x16x32_bf16 v[32:35], a[160:163], v[132:135], v[32:35]\n\t" \
   "v_mfma_f32_16x16x32_bf16 v[36:39], a[164:167], v[132:135], v[36:39]\n\t" "v_mfma_f32_16x16x32_bf16 v[40:43], a[168:171], v[132:135], v[40:43]\n\t" \
   "v_mfma_f32_16x16x32_bf16 v[44:47], a[172:175], v[132:135], v[44:47]\n\t" "v_mfma_f32_16x16x32_bf16 v[48:51], a[176:179], v[132:135], v[48:51]\n\t" \
   "v_mfma_f32_16x16x32_bf16 v[52:55], a[180:183], v[132:135], v[52:55]\n\t" "v_mfma_f32_16x16x32_bf16 v[56:59], a[184:187], v[132:135], v[56:59]\n\t" \
   "v_mfma_f32_16x16x32_bf16 v[60:63], a[188:191], v[132:135], v[60:63]\n\t" "s_waitcnt vmcnt(29)\n\t" \
   "v_mfma_f32_16x16x32_bf16 v[32:35], a[192:195], v[136:139], v[32:35]\n\t" "v_mfma_f32_16x16x32_bf16 v[36:39], a[196:199], v[136:139], v[36:39]\n\t" \
   "v_mfma_f32_16x16x32_bf16 v[40:43], a[200:203], v[136:139], v[40:43]\n\t" "v_mfma_f32_16x16x32_bf16 v[44:47], a[204:207], v[136:139], v[44:47]\n\t" \
   "v_mfma_f32_16x16x32_bf16 v[48:51], a[208:211], v[136:139], v[48:51]\n\t" "v_mfma_f32_16x16x32_bf16 v[52:55], a[212:215], v[136:139], v[52:55]\n\t" \
   "v_mfma_f32_16x16x32_bf16 v[56:59], a[216:219], v[136:139], v[56:59]\n\t" "v_mfma_f32_16x16x32_bf16 v[60:63], a[220:223], v[136:139], v[60:63]\n\t" \
   "s_waitcnt vmcnt(28)\n\t" "v_mfma_f32_16x16x32_bf16 v[32:35], a[224:227], v[140:143], v[32:35]\n\t" \
   "v_mfma_f32_16x16x32_bf16 v[36:39], a[228:231], v[140:143], v[36:39]\n\t" "v_mfma_f32_16x16x32_bf16 v[40:43], a[232:235], v[140:143], v[40:43]\n\t" \
   "v_mfma_f32_16x16x32_bf16 v[44:47], a[236:239], v[140:143], v[44:47]\n\t" "v_mfma_f32_16x16x32_bf16 v[48:51], a[240:243], v[140:143], v[48:51]\n\t" \
   "v_mfma_f32_16x16x32_bf16 v[52:55], a[244:247], v[140:143], v[52:55]\n\t" "v_mfma_f32_16x16x32_bf16 v[56:59], a[248:251], v[140:143], v[56:59]\n\t" \
   "v_mfma_f32_16x16x32_bf16 v[60:63], a[252:255], v[140:143], v[60:63]\n\t" "s_waitcnt vmcnt(27)\n\t" \
   "v_mfma_f32_16x16x32_bf16 v[64:67], a[0:3], v[152:155], 0\n\t" "v_mfma_f32_16x16x32_bf16 v[68:71], a[4:7], v[152:155], 0\n\t" \
   "v_mfma_f32_16x16x32_bf16 v[72:75], a[8:11], v[152:155], 0\n\t" "v_mfma_f32_16x16x32_bf16 v[76:79], a[12:15], v[152:155], 0\n\t" \
   "v_mfma_f32_16x16x32_bf16 v[80:83], a[16:19], v[152:155], 0\n\t" "v_mfma_f32_16x16x32_bf16 v[84:87], a[20:23], v[152:155], 0\n\t" \
   "v_mfma_f32_16x16x32_bf16 v[88:91], a[24:27], v[152:155], 0\n\t" "v_mfma_f32_16x16x32_bf16 v[92:95], a[28:31], v[152:155], 0\n\t" \
   "s_waitcnt vmcnt(26)\n\t" "v_mfma_f32_16x16x32_bf16 v[64:67], a[32:35], v[156:159], v[64:67]\n\t" \
   "v_mfma_f32_16x16x32_bf16 v[68:71], a[36:39], v[156:159], v[68:71]\n\t" "v_mfma_f32_16x16x32_bf16 v[72:75], a[40:43], v[156:159], v[72:75]\n\t" \
   "v_mfma_f32_16x16x32_bf16 v[76:79], a[44:47], v[156:159], v[76:79]\n\t" "v_mfma_f32_16x16x32_bf16 v[80:83], a[48:51], v[156:159], v[80:83]\n\t" \
   "v_mfma_f32_16x16x32_bf16 v[84:87], a[52:55], v[156:159], v[84:87]\n\t" "v_mfma_f32_16x16x32_bf16 v[88:91], a[56:59], v[156:159], v[88:91]\n\t" \
   "v_mfma_f32_16x16x32_bf16 v[92:95], a[60:63], v[156:159], v[92:95]\n\t" "s_waitcnt vmcnt(25)\n\t" \
   "v_mfma_f32_16x16x32_bf16 v[64:67], a[64:67], v[160:163], v[64:67]\n\t" "v_mfma_f32_16x16x32_bf16 v[68:71], a[68:71], v[160:163], v[68:71]\n\t" \
   "v_mfma_f32_16x16x32_bf16 v[72:75], a[72:75], v[160:163], v[72:75]\n\t" "v_mfma_f32_16x16x32_bf16 v[76:79], a[76:79], v[160:163], v[76:79]\n\t" \
   "v_mfma_f32_16x16x32_bf16 v[80:83], a[80:83], v[160:163], v[80:83]\n\t" "v_mfma_f32_16x16x32_bf16 v[84:87], a[84:87], v[160:163], v[84:87]\n\t" \
   "v_mfma_f32_16x16x32_bf16 v[88:91], a[88:91], v[160:163], v[88:91]\n\t" "v_mfma_f32_16x16x32_bf16 v[92:95], a[92:95], v[160:163], v[92:95]\n\t" \
   "s_waitcnt vmcnt(24)\n\t" "v_mfma_f32_16x16x32_bf16 v[64:67], a[96:99], v[164:167], v[64:67]\n\t" \
   "v_mfma_f32_16x16x32_bf16 v[68:71], a[100:103], v[164:167], v[68:71]\n\t" "v_mfma_f32_16x16x32_bf16 v[72:75], a[104:107], v[164:167], v[72:75]\n\t" \
   "v_mfma_f32_16x16x32_bf16 v[76:79], a[108:111], v[164:167], v[76:79]\n\t" "v_mfma_f32_16x16x32_bf16 v[80:83], a[112:115], v[164:167], v[80:83]\n\t" \
   "v_mfma_f32_16x16x32_bf16 v[84:87], a[116:119], v[164:167], v[84:87]\n\t" "v_mfma_f32_16x16x32_bf16 v[88:91], a[120:123], v[164:167], v[88:91]\n\t" \
   "v_mfma_f32_16x16x32_bf16 v[92:95], a[124:127], v[164:167], v[92:95]\n\t" "s_waitcnt vmcnt(23)\n\t" \
   "v_mfma_f32_16x16x32_bf16 v[64:67], a[128:131], v[168:171], v[64:67]\n\t" "v_mfma_f32_16x16x32_bf16 v[68:71], a[132:135], v[168:171], v[68:71]\n\t" \
   "v_mfma_f32_16x16x32_bf16 v[72:75], a[136:139], v[168:171], v[72:75]\n\t" "v_mfma_f32_16x16x32_bf16 v[76:79], a[140:143], v[168:171], v[76:79]\n\t" \
   "v_mfma_f32_16x16x32_bf16 v[80:83], a[144:147], v[168:171], v[80:83]\n\t" "v_mfma_f32_16x16x32_bf16 v[84:87], a[148:151], v[168:171], v[84:87]\n\t" \
   "v_mfma_f32_16x16x32_bf16 v[88:91], a[152:155], v[168:171], v[88:91]\n\t" "v_mfma_f32_16x16x32_bf16 v[92:95], a[156:159], v[168:171], v[92:95]\n\t" \
   "s_waitcnt vmcnt(22)\n\t" "v_mfma_f32_16x16x32_bf16 v[64:67], a[160:163], v[172:175], v[64:67]\n\t" \
   "v_mfma_f32_16x16x32_bf16 v[68:71], a[164:167], v[172:175], v[68:71]\n\t" "v_mfma_f32_16x16x32_bf16 v[72:75], a[168:171], v[172:175], v[72:75]\n\t" \
   "v_mfma_f32_16x16x32_bf16 v[76:79], a[172:175], v[172:175], v[76:79]\n\t" "v_mfma_f32_16x16x32_bf16 v[80:83], a[176:179], v[172:175], v[80:83]\n\t" \
   "v_mfma_f32_16x16x32_bf16 v[84:87], a[180:183], v[172:175], v[84:87]\n\t" "v_mfma_f32_16x16x32_bf16 v[88:91], a[184:187], v[172:175], v[88:91]\n\t" \
   "v_mfma_f32_16x16x32_bf16 v[92:95], a[188:191], v[172:175], v[92:95]\n\t" "s_waitcnt vmcnt(21)\n\t" \
   "v_mfma_f32_16x16x32_bf16 v[64:67], a[192:195], v[176:179], v[64:67]\n\t" "v_mfma_f32_16x16x32_bf16 v[68:71], a[196:199], v[176:179], v[68:71]\n\t" \
   "v_mfma_f32_16x16x32_bf16 v[72:75], a[200:203], v[176:179], v[72:75]\n\t" "v_mfma_f32_16x16x32_bf16 v[76:79], a[204:207], v[176:179], v[76:79]\n\t" \
   "v_mfma_f32_16x16x32_bf16 v[80:83], a[208:211], v[176:179], v[80:83]\n\t" "v_mfma_f32_16x16x32_bf16 v[84:87], a[212:215], v[176:179], v[84:87]\n\t" \
   "v_mfma_f32_16x16x32_bf16 v[88:91], a[216:219], v[176:179], v[88:91]\n\t" "v_mfma_f32_16x16x32_bf16 v[92:95], a[220:223], v[176:179], v[92:95]\n\t" \
   "s_waitcnt vmcnt(20)\n\t" "s_waitcnt vmcnt(16)\n\t" "v_mfma_f32_16x16x32_bf16 v[64:67], a[224:227], v[180:183], v[64:67]\n\t" \
   "v_swap_b32 v145, v146\n\t" "v_mfma_f32_16x16x32_bf16 v[68:71], a[228:231], v[180:183], v[68:71]\n\t" "v_swap_b32 v149, v150\n\t" \
   "v_mfma_f32_16x16x32_bf16 v[72:75], a[232:235], v[180:183], v[72:75]\n\t" "v_swap_b32 v185, v186\n\t" \
   "v_mfma_f32_16x16x32_bf16 v[76:79], a[236:239], v[180:183], v[76:79]\n\t" "v_swap_b32 v189, v190\n\t" \
   "v_mfma_f32_16x16x32_bf16 v[80:83], a[240:243], v[180:183], v[80:83]\n\t" "v_mfma_f32_16x16x32_bf16 v[84:87], a[244:247], v[180:183], v[84:87]\n\t" \
   "v_mfma_f32_16x16x32_bf16 v[88:91], a[248:251], v[180:183], v[88:91]\n\t" "v_mfma_f32_16x16x32_bf16 v[92:95], a[252:255], v[180:183], v[92:95]\n\t" \
   "s_waitcnt vmcnt(15)\n\t" "v_mfma_f32_16x16x32_bf16 v[32:35], v[192:195], v[144:147], v[32:35]\n\t" "s_waitcnt vmcnt(14)\n\t" \
   "v_mfma_f32_16x16x32_bf16 v[36:39], v[196:199], v[144:147], v[36:39]\n\t" "s_waitcnt vmcnt(13)\n\t" \
   "v_mfma_f32_16x16x32_bf16 v[40:43], v[200:203], v[144:147], v[40:43]\n\t" "s_waitcnt vmcnt(12)\n\t" \
   "v_mfma_f32_16x16x32_bf16 v[44:47], v[204:207], v[144:147], v[44:47]\n\t" "s_waitcnt vmcnt(11)\n\t" \
   "v_mfma_f32_16x16x32_bf16 v[48:51], v[208:211], v[144:147], v[48:51]\n\t" "s_waitcnt vmcnt(10)\n\t" \
   "v_mfma_f32_16x16x32_bf16 v[52:55], v[212:215], v[144:147], v[52:55]\n\t" "s_waitcnt vmcnt(9)\n\t" \
   "v_mfma_f32_16x16x32_bf16 v[56:59], v[216:219], v[144:147], v[56:59]\n\t" "s_waitcnt vmcnt(8)\n\t" \
   "v_mfma_f32_16x16x32_bf16 v[60:63], v[220:223], v[144:147], v[60:63]\n\t" "s_waitcnt vmcnt(7)\n\t" \
   "v_mfma_f32_16x16x32_bf16 v[32:35], v[224:227], v[148:151], v[32:35]\n\t" "s_waitcnt vmcnt(6)\n\t" \
   "v_mfma_f32_16x16x32_bf16 v[36:39], v[228:231], v[148:151], v[36:39]\n\t" "s_waitcnt vmcnt(5)\n\t" \
   "v_mfma_f32_16x16x32_bf16 v[40:43], v[232:235], v[148:151], v[40:43]\n\t" "s_waitcnt vmcnt(4)\n\t" \
   "v_mfma_f32_16x16x32_bf16 v[44:47], v[236:239], v[148:151], v[44:47]\n\t" "s_waitcnt vmcnt(3)\n\t" \
   "v_mfma_f32_16x16x32_bf16 v[48:51], v[240:243], v[148:151], v[48:51]\n\t" "s_waitcnt vmcnt(2)\n\t" \
   "v_mfma_f32_16x16x32_bf16 v[52:55], v[244:247], v[148:151], v[52:55]\n\t" "s_waitcnt vmcnt(1)\n\t" \
   "v_mfma_f32_16x16x32_bf16 v[56:59], v[248:251], v[148:151], v[56:59]\n\t" "s_waitcnt vmcnt(0)\n\t" \
   "v_mfma_f32_16x16x32_bf16 v[60:63], v[252:255], v[148:151], v[60:63]\n\t" "v_mfma_f32_16x16x32_bf16 v[64:67], v[192:195], v[184:187], v[64:67]\n\t" \
   "v_mfma_f32_16x16x32_bf16 v[68:71], v[196:199], v[184:187], v[68:71]\n\t" "v_mfma_f32_16x16x32_bf16 v[72:75], v[200:203], v[184:187], v[72:75]\n\t" \
   "v_mfma_f32_16x16x32_bf16 v[76:79], v[204:207], v[184:187], v[76:79]\n\t" "v_mfma_f32_16x16x32_bf16 v[80:83], v[208:211], v[184:187], v[80:83]\n\t" \
   "v_mfma_f32_16x16x32_bf16 v[84:87], v[212:215], v[184:187], v[84:87]\n\t" "v_mfma_f32_16x16x32_bf16 v[88:91], v[216:219], v[184:187], v[88:91]\n\t" \
   "v_mfma_f32_16x16x32_bf16 v[92:95], v[220:223], v[184:187], v[92:95]\n\t" "v_mfma_f32_16x16x32_bf16 v[64:67], v[224:227], v[188:191], v[64:67]\n\t" \
   "ds_write_b128 %[lw], v[32:35] offset:0\n\t" "v_mfma_f32_16x16x32_bf16 v[68:71], v[228:231], v[188:191], v[68:71]\n\t" \
   "ds_write_b128 %[lw], v[36:39] offset:1024\n\t" "v_mfma_f32_16x16x32_bf16 v[72:75], v[232:235], v[188:191], v[72:75]\n\t" \
   "ds_write_b128 %[lw], v[40:43] offset:2048\n\t" "v_mfma_f32_16x16x32_bf16 v[76:79], v[236:239], v[188:191], v[76:79]\n\t" \
   "ds_write_b128 %[lw], v[44:47] offset:3072\n\t" "ds_write_b128 %[lw], v[64:67] offset:32768\n\t" \
   "v_mfma_f32_16x16x32_bf16 v[80:83], v[240:243], v[188:191], v[80:83]\n\t" "ds_write_b128 %[lw], v[48:51] offset:4096\n\t" \
   "ds_write_b128 %[lw], v[68:71] offset:33792\n\t" "v_mfma_f32_16x16x32_bf16 v[84:87], v[244:247], v[188:191], v[84:87]\n\t" \
   "ds_write_b128 %[lw], v[52:55] offset:5120\n\t" "ds_write_b128 %[lw], v[72:75] offset:34816\n\t" \
   "v_mfma_f32_16x16x32_bf16 v[88:91], v[248:251], v[188:191], v[88:91]\n\t" "ds_write_b128 %[lw], v[56:59] offset:6144\n\t" \
   "ds_write_b128 %[lw], v[76:79] offset:35840\n\t" "v_mfma_f32_16x16x32_bf16 v[92:95], v[252:255], v[188:191], v[92:95]\n\t" \
   "ds_write_b128 %[lw], v[60:63] offset:7168\n\t" "ds_write_b128 %[lw], v[80:83] offset:36864\n\t" "s_nop 1\n\t" "s_nop 3\n\t" \
   "ds_write_b128 %[lw], v[84:87] offset:37888\n\t" "s_nop 3\n\t" "ds_write_b128 %[lw], v[88:91] offset:38912\n\t" "s_nop 3\n\t" \
   "ds_write_b128 %[lw], v[92:95] offset:39936\n\t" "s_waitcnt lgkmcnt(0)\n\t" "s_barrier\n\t" "ds_read_b128 v[112:115], %[lr] offset:0\n\t" \
   "ds_read_b128 v[116:119], %[lr] offset:256\n\t" "ds_read_b128 v[120:123], %[lr] offset:8192\n\t" "ds_read_b128 v[124:127], %[lr] offset:8448\n\t" \
   "ds_read_b128 v[128:131], %[lr] offset:16384\n\t" "ds_read_b128 v[132:135], %[lr] offset:16640\n\t" \
   "ds_read_b128 v[136:139], %[lr] offset:24576\n\t" "ds_read_b128 v[140:143], %[lr] offset:24832\n\t" \
   "ds_read_b128 v[144:147], %[lr] offset:32768\n\t" "ds_read_b128 v[148:151], %[lr] offset:33024\n\t" \
   "ds_read_b128 v[152:155], %[lr] offset:40960\n\t" "ds_read_b128 v[156:159], %[lr] offset:41216\n\t" \
   "ds_read_b128 v[160:163], %[lr] offset:49152\n\t" "ds_read_b128 v[164:167], %[lr] offset:49408\n\t" \
   "ds_read_b128 v[168:171], %[lr] offset:57344\n\t" "ds_read_b128 v[172:175], %[lr] offset:57600\n\t" "s_waitcnt lgkmcnt(12)\n\t" \
   "v_add_f32 v112, v112, v120\n\t" "v_add_f32 v113, v113, v121\n\t" "v_add_f32 v114, v114, v122\n\t" "v_add_f32 v115, v115, v123\n\t" \
   "v_add_f32 v116, v116, v124\n\t" "v_add_f32 v117, v117, v125\n\t" "v_add_f32 v118, v118, v126\n\t" "v_add_f32 v119, v119, v127\n\t" \
   "s_waitcnt lgkmcnt(8)\n\t" "v_add_f32 v128, v128, v136\n\t" "v_add_f32 v129, v129, v137\n\t" "v_add_f32 v130, v130, v138\n\t" \
   "v_add_f32 v131, v131, v139\n\t" "v_add_f32 v132, v132, v140\n\t" "v_add_f32 v133, v133, v141\n\t" "v_add_f32 v134, v134, v142\n\t" \
   "v_add_f32 v135, v135, v143\n\t" "v_add_f32 v112, v112, v128\n\t" "v_add_f32 v113, v113, v129\n\t" "v_add_f32 v114, v114, v130\n\t" \
   "v_add_f32 v115, v115, v131\n\t" "v_add_f32 v116, v116, v132\n\t" "v_add_f32 v117, v117, v133\n\t" "v_add_f32 v118, v118, v134\n\t" \
   "v_add_f32 v119, v119, v135\n\t" "v_cvt_pk_bf16_f32 v96, v112, v113\n\t" "v_cvt_pk_bf16_f32 v97, v114, v115\n\t" \
   "v_cvt_pk_bf16_f32 v98, v116, v117\n\t" "v_cvt_pk_bf16_f32 v99, v118, v119\n\t" "s_waitcnt lgkmcnt(4)\n\t" "v_add_f32 v144, v144, v152\n\t" \
   "v_add_f32 v145, v145, v153\n\t" "v_add_f32 v146, v146, v154\n\t" "v_add_f32 v147, v147, v155\n\t" "v_add_f32 v148, v148, v156\n\t" \
   "v_add_f32 v149, v149, v157\n\t" "v_add_f32 v150, v150, v158\n\t" "v_add_f32 v151, v151, v159\n\t" "s_waitcnt lgkmcnt(0)\n\t" \
   "v_add_f32 v160, v160, v168\n\t" "v_add_f32 v161, v161, v169\n\t" "v_add_f32 v162, v162, v170\n\t" "v_add_f32 v163, v163, v171\n\t" \
   "v_add_f32 v164, v164, v172\n\t" "v_add_f32 v165, v165, v173\n\t" "v_add_f32 v166, v166, v174\n\t" "v_add_f32 v167, v167, v175\n\t" \
   "v_add_f32 v144, v144, v160\n\t" "v_add_f32 v145, v145, v161\n\t" "v_add_f32 v146, v146, v162\n\t" "v_add_f32 v147, v147, v163\n\t" \
   "v_add_f32 v148, v148, v164\n\t" "v_add_f32 v149, v149, v165\n\t" "v_add_f32 v150, v150, v166\n\t" "v_add_f32 v151, v151, v167\n\t" \
   "v_cvt_pk_bf16_f32 v100, v144, v145\n\t" "v_cvt_pk_bf16_f32 v101, v146, v147\n\t" "v_cvt_pk_bf16_f32 v102, v148, v149\n\t" \
   "v_cvt_pk_bf16_f32 v103, v150, v151\n\t" "s_nop 1"
#define O2_CLOB "v32", "v33", "v34", "v35", "v36", "v37", "v38", "v39", "v40", "v41", "v42", "v43", "v44", "v45", "v46", "v47", "v48", "v49", "v50", "v51", "v52", "v53", "v54", "v55", "v56", "v57", "v58", "v59", "v60", "v61", "v62", "v63", "v64", "v65", "v66", "v67", "v68", "v69", "v70", "v71", "v72", "v73", "v74", "v75", "v76", "v77", "v78", "v79", "v80", "v81", "v82", "v83", "v84", "v85", "v86", "v87", "v88", "v89", "v90", "v91", "v92", "v93", "v94", "v95", "v112", "v113", "v114", "v115", "v116", "v117", "v118", "v119", "v120", "v121", "v122", "v123", "v124", "v125", "v126", "v127", "v128", "v129", "v130", "v131", "v132", "v133", "v134", "v135", "v136", "v137", "v138", "v139", "v140", "v141", "v142", "v143", "v144", "v145", "v146", "v147", "v148", "v149", "v150", "v151", "v152", "v153", "v154", "v155", "v156", "v157", "v158", "v159", "v160", "v161", "v162", "v163", "v164", "v165", "v166", "v167", "v168", "v169", "v170", "v171", "v172", "v173", "v174", "v175", "v176", "v177", "v178", "v179", "v180", "v181", "v182", "v183", "v184", "v185", "v186", "v187", "v188", "v189", "v190", "v191", "s40"
#define TZ 430
#define TZM 450
constexpr int OOB = 0x7FFFFFF0;
#define TDC 190
__device__ __forceinline__ void when(unsigned tz) {
  const unsigned t = __builtin_amdgcn_readfirstlane(*(unsigned*)(dyn_lds + L_WHEN)) + tz;
  while ((int)(t - rtc()) > 0 && (int)(t - rtc()) < 1000) __builtin_amdgcn_s_sleep(1);
}
constexpr int COL_I2 = 64 * 264, COL_LDS = COL_I2 + 8 * 2064;
__device__ __forceinline__ void job_col(int wv, unsigned char* ws, rsrc_t rs, unsigned lds, int jb, int go, int clr, bool all) {
  const int lane = lane_id(), g = lane >> 4, r = lane & 15, j = wv * 64 + lane;
  const int xin = (int)OFF_X1T + (jb == 160 ? 0 : jb) * XS * 8 + j * 16;
  v4u x[4], wf[4];
#pragma unroll
  for (int q = 0; q < 4; ++q) stc(rs, (int)OFF_ZT + jb * ZS * 8 + q * 4096 + j * 16, v4u{0xFFFFFFFFu, 0xFFFFFFFFu, 0xFFFFFFFFu, 0xFFFFFFFFu});
#pragma unroll
  for (int q = 0; q < 4; ++q) {
    const int o = (int)OFF_WT + (jb == 160 ? 320 : 2 * jb + (q >> 1)) * S_ * 4 + ((wv * 2 + (q & 1)) * 64 + lane) * 16;
    wf[q] = ldc(rs, o);
  }
  v32u k0, k1, k2;
  v8u k3, ad = (unsigned)lane;
  {
    const int n = 16 * wv + r;
    cis8(k0, n * (512 * (g & 1) + 256 * (g >> 1)), n * 32, n * 128, (r & 1) ? 0x80008000u : 0u);
    cis8(k1, r * g * 256, r * 64, r * 1024, 0x80000000u);
#pragma unroll
    for (int q = 0; q < 8; ++q) {
      const int i = -n * sigma(g, q), k = (r + 16 * (q >> 2)) * (16 * wv + 4 * g + (q & 3));
      const float sc = 1.0f / ((float)S_ * (float)D_);
      k0[16 + 2 * q] = __builtin_bit_cast(unsigned, cosi(i)); k0[17 + 2 * q] = __builtin_bit_cast(unsigned, sini(i));
      k1[16 + 2 * q] = __builtin_bit_cast(unsigned, cosi(k) * sc); k1[17 + 2 * q] = __builtin_bit_cast(unsigned, sini(k) * sc);
      k3[q] = k1[8 + q] ^ 0x80008000u;
    }
    ad[0] = lds + 264 * (16 * (g & 1) + 8 * (g >> 1) + (r >> 2)) + 32 * (r & 3);
    ad[1] = lds + COL_I2 + (2 * (r >> 3) + (r & 1)) * 2064 + ((r >> 1) & 3) * 512 + (g & 1) * 8;
    ad[2] = ad[1] + 256 + (((4 * (wv >> 1) + 2 * (g >> 1) + (wv & 1)) ^ ((r & 6) + 1)) << 4);
    ad[1] += ((4 * (wv >> 1) + 2 * (g >> 1) + (wv & 1)) ^ (r & 6)) << 4;
    ad[3] = lds + COL_I2 + r * 128 + ((g ^ (r >> 1)) << 4);
    ad[4] = lds + COL_I2 + r * 128 + (((4 + g) ^ (r >> 1)) << 4);
    ad[5] = (int)OFF_ZT + (jb * ZS + 32 * (16 * wv + r) + 2 * g) * 8;
    asm volatile("" : "+{v[32:63]}"(k0), "+{v[64:95]}"(k1), "+{v[128:135]}"(k3), "+{v[16:23]}"(ad));
  }
  const unsigned sh = jb == 160 ? 16u : 0u, mk = jb == 0 ? 0xFFFFu : ~0u, d = lds + (j >> 4) * 264 + r * 16;
#pragma unroll
  for (int q = 0; q < 16; ++q) { k2[2 * q] = wf[q >> 2][q & 3] << 16; k2[2 * q + 1] = wf[q >> 2][q & 3] & 0xFFFF0000u; }
  asm volatile("" : "+{v[96:127]}"(k2));
  fan_signal<FSP>(wv, ws, clr, jb);
  fan_wait<64, 20>(wv, ws, go, jb);
  PH(all ? 27 : 47)
#pragma unroll
  for (int q = 0; q < 4; ++q) x[q] = ldc(rs, q == 3 && wv == 3 ? OOB : xin + q * 4096);
  if (all) fan_wait<FSP, NCOL, false>(wv, ws, clr, jb);
  if (wv == 3) {
    when(TDC);
    for (unsigned spins = 0; spins < (1u << 16); ++spins) {
      x[3] = ldc(rs, xin + 3 * 4096);
      if (__builtin_expect(!__any(max(x[3].x, x[3].z) == 0xFFFFFFFFu), 1)) break;
      __builtin_amdgcn_s_sleep(2);
      asm volatile("" ::: "memory");
    }
  }
#define CSTAMP(i)
  CSTAMP(0)
#pragma unroll
  for (int q = 0; q < 4; ++q) {
    LDS_V2(d + q * 16 * 264) = v2u{(x[q].x >> sh) & mk, x[q].y};
    LDS_V2(d + q * 16 * 264 + 8) = v2u{(x[q].z >> sh) & mk, x[q].w};
  }
  CSTAMP(1)
  PH(all ? 28 : 48)
  asm volatile(COL_ASM ::"{v[32:63]}"(k0), "{v[64:95]}"(k1), "{v[96:127]}"(k2), "{v[128:135]}"(k3), "{v[16:23]}"(ad), [rs] "s"(rs) : COL_CLOB, "memory");
  CSTAMP(2)
  PH(all ? 29 : 49)
  CSTAMP(3)
}
struct XRows { v4u p[2]; v2u q[2]; };
__device__ __forceinline__ int xr_off(int rt, int hf, int wv, int lane, bool third) {
  const int o = (int)OFF_XR + ((rt * KT + hf * 5) * 64 + (lane & 48) + 2 * (lane & 7) + ((lane >> 3) & 1)) * 16;
  return third ? (wv < 2 ? o + 4096 + wv * 8 : OOB) : o + wv * 1024;
}
__device__ __forceinline__ void idft_x(XRows& x, rsrc_t rs, int rt, int hf, int wv, int lane) {
#pragma unroll
  for (int m = 0; m < 2; ++m) {
    x.p[m] = __builtin_amdgcn_raw_buffer_load_b128(rs, xr_off(rt, hf, wv, lane, false), m * 10240, AUX_SC1);
    x.q[m] = __builtin_amdgcn_raw_buffer_load_b64(rs, xr_off(rt, hf, wv, lane, true), m * 10240, AUX_SC1);
  }
}
__device__ __forceinline__ void idft_stage(rsrc_t rs, v4u* lds, int rt, int wv, int lane) {
  const int o = (int)OFF_ZT + ((lane >> 3) * ZS + rt * 16 + 2 * (lane & 7)) * 8;
  const bool more = lane >> 4 == wv;
  int off[9];
  v4u t[9];
#pragma unroll
  for (int i = 0; i < 9; ++i) {
    off[i] = i < 4 ? o + (wv * 32 + i * 8) * ZS * 8 : (more && (i < 8 || lane < 8) ? o + (96 + i * 8) * ZS * 8 : OOB);
    t[i] = ldc(rs, off[i]);
  }
  for (unsigned spins = 0; spins < (1u << 16); ++spins) {
    unsigned m = 0;
#pragma unroll
    for (int i = 0; i < 9; ++i) m = max(m, max(t[i].x, t[i].z));
    if (__builtin_expect(!__any(m == 0xFFFFFFFFu), 1)) break;
    __builtin_amdgcn_s_sleep(2);
#pragma unroll
    for (int i = 0; i < 9; ++i) {
      const bool again = max(t[i].x, t[i].z) == 0xFFFFFFFFu;
      const v4u u = ldc(rs, again ? off[i] : OOB);
      if (again) t[i] = u;
    }
    asm volatile("" ::: "memory");
  }
  if (more && lane < 8) {
    t[0].x = (t[0].x & 0xFFFFu) | (t[8].x << 16);
    t[0].z = (t[0].z & 0xFFFFu) | (t[8].z << 16);
  }
  v4u* dst = lds + (lane >> 3) * 16 + (lane & 7);
#pragma unroll
  for (int c = 0; c < 4; ++c) dst[wv * 128 + (c & 1) * 640 + (c >> 1) * 8] = v4u{t[0][c], t[1][c], t[2][c], t[3][c]};
  if (more) {
#pragma unroll
    for (int c = 0; c < 4; ++c) dst[512 + (c & 1) * 640 + (c >> 1) * 8] = v4u{t[4][c], t[5][c], t[6][c], t[7][c]};
  }
}
#define A_IN(a) "{a[0:31]}"(a[0]), "{a[32:63]}"(a[1]), "{a[64:95]}"(a[2]), "{a[96:127]}"(a[3]), "{a[128:159]}"(a[4]), "{a[160:191]}"(a[5]), "{a[192:223]}"(a[6]), "{a[224:255]}"(a[7])
#define A_OUT(a) "={a[0:31]}"(a[0]), "={a[32:63]}"(a[1]), "={a[64:95]}"(a[2]), "={a[96:127]}"(a[3]), "={a[128:159]}"(a[4]), "={a[160:191]}"(a[5]), \
                 "={a[192:223]}"(a[6]), "={a[224:255]}"(a[7])
__device__ __forceinline__ void idft_mul(XRows& x, const v32u (&a)[8], unsigned la) {
  asm volatile(ID_ASM : "+{v[136:139]}"(x.p[0]), "+{v[140:143]}"(x.p[1]), "+{v[144:145]}"(x.q[0]), "+{v[146:147]}"(x.q[1])
               : A_IN(a), [la] "v"(la) : ID_CLOB, "memory");
}
__device__ __forceinline__ void idft_matrix(v32u (&a)[8], rsrc_t rs, int hf, int wv, int lane) {
  const int o = (int)OFF_INV + hf * 10 * NFK * 1024 + lane * 16;
  asm volatile(IM_ASM : A_OUT(a)
               : [o0] "v"(o + wv * 2 * NFK * 1024), [o1] "v"(wv < 2 ? o + (wv + 6) * NFK * 1024 : OOB), [rs] "s"(rs) : IM_CLOB, "memory");
}
constexpr int NFIRST = 40;
__device__ __forceinline__ void job_idft(int wv_, unsigned char* ws, rsrc_t rs, v4u* lds, int rt0, int hf, unsigned long long* dbg) {
  const int lane = lane_id(), wv = wv_;
  v32u a[8];
  idft_matrix(a, rs, hf, wv, lane);
  fan_wait<FSP, NCOL>(wv_, ws, fan_off(1, 0, 0), rt0 >> 2);
  fan_wait<64, 20>(wv_, ws, fan_off(0, 1, 0), rt0 >> 2);
#pragma unroll 1
  for (int rt = rt0; rt < RT; rt += 32) {
    XRows x;
    idft_x(x, rs, rt, hf, wv, lane);
    if (rt == rt0) { when(TZM); PH(30) }
    idft_stage(rs, lds, rt, wv, lane);
    if (rt == rt0) { PH(50) }
    idft_mul(x, a, lds_base() + lane * 16);
#pragma unroll
    for (int m = 0; m < 2; ++m) {
      __builtin_amdgcn_raw_buffer_store_b128(x.p[m], rs, xr_off(rt, hf, wv, lane, false), m * 10240, AUX_SC1);
      __builtin_amdgcn_raw_buffer_store_b64(x.q[m], rs, xr_off(rt, hf, wv, lane, true), m * 10240, AUX_SC1);
    }
    const f32x2 st = rows_sum(frag_stat(x.p[0]) + frag_stat(x.p[1]) + frag_stat(v4u{x.q[0].x, x.q[0].y, x.q[1].x, x.q[1].y}));
    f32x2* sp = (f32x2*)(lds + 5120) + (lane & 15);
    if (lane < 16) sp[wv * 16] = st;
    asm volatile("s_waitcnt lgkmcnt(0)\n\ts_barrier" ::: "memory");
    if (wv == 0) {
      const f32x2 s0 = sp[0] + sp[16], s1 = sp[32] + sp[48];
      const f32x4 sv = lane < 16 ? f32x4{s0.x, s0.y, s1.x, s1.y} : f32x4{0, 0, 0, 0};
      stc(rs, lane < 16 + hf * 16 ? (int)OFF_ST + (rt * 16 + 2 * (lane & 7) + ((lane >> 3) & 1)) * 48 + (hf + (lane >> 4)) * 16 : OOB, __builtin_bit_cast(v4u, sv));
    }
    if (rt == rt0) { PH(31) }
    if (rt < NFIRST / 2) fan_signal<64>(wv_, ws, fan_off(1, 1, 0), rt * 2 + hf);
    else dep_signal(wv_, ws, cnt_off(1, CNT_IDFT, rt >> 2));
    if (rt < NFIRST / 2) { PH(51) }
  }
}

constexpr int NFAST = 62, NEARLY = 61;
#define TYL 160
#define TG0 190
#define TG1 165
__device__ __forceinline__ void job_out(int wv_, unsigned char* ws, rsrc_t rs, unsigned long long ysel, int layer, int s, int cg, int qt) {
  const int lane = lane_id(), wv = wv_, g = lane >> 4, r = lane & 15, q0 = max(qt, 0);
  const int yo = wv * 2048 + lane * 16;
  const int sy = __builtin_amdgcn_readfirstlane(pool_base(ws) + P_Y + y_region(ysel, s) + cg * 131072);
  const int xo = (int)OFF_XR + (((2 * s + q0) * KT + cg * 4 + wv) * 64 + lane) * 16;
  const v4u x0 = ldc(rs, xo), x1 = ldc(rs, qt < 0 ? xo + KT * 1024 : OOB);
  v32u u[8], y0, y1;
  asm volatile(OU_ASM : A_OUT(u) : [o] "v"(wv * 8192 + lane * 16), [so] "s"((int)OFF_UF + (s * 40 + cg * 8) * 32768), [rs] "s"(rs) : OU_CLOB, "scc", "memory");
  const int sw = s < NEARLY ? s : s - 2;
  if (s > 1) prog_prewait(wv_, ws, prog_off(layer, sw - 2, (s + cg * 7) & 31));
  prog_wait(wv_, ws, prog_off(layer, sw, 0), s < PWL);
  v8u acc;
#define OM_OPS(q0) A_IN(u), [po] "v"(wv * 16384 + lane * 16), [mo] "v"(wv * 4096 + lane * 16), [sp] "s"((int)OFF_PXF + s * 65536 + q0 * 1024),         \
               [sm] "s"((int)OFF_PXM + s * 16384 + q0 * 1024), [lw] "v"(lds_base() + wv * 8192 + lane * 16), [lr] "v"(lds_base() + wv * 2048 + g * 512 + r * 16), [rs] "s"(rs)
  PHT(1)
  if (s >= NEARLY) {
    unsigned nn;
    asm volatile("v_mov_b32 %0, -1" : "=v"(nn) : "v"(x0.x), "v"(x1.x));
    stc(rs, xo, v4u{nn, nn, nn, nn});
    stc(rs, qt < 0 ? xo + KT * 1024 : OOB, v4u{nn, nn, nn, nn});
    __builtin_amdgcn_raw_buffer_store_b64(v2u{nn, nn}, rs, TIDX < (qt < 0 ? 32 : 16) ? (int)OFF_ST + ((s * 32 + q0 * 16 + lane) * 6 + cg) * 8 : OOB, 0, AUX_SC1);
    when(TYL);
    for (int i = 0; i < 2; ++i) {
      unsigned m;
      asm volatile(OY_ASM : "={v[192:223]}"(y0), "={v[224:255]}"(y1), [m] "=&v"(m) : [yo] "v"(yo), [sy] "s"(sy), [rs] "s"(rs) : OY_CLOB, "scc", "memory");
      if (i == 0) prog_wait(wv_, ws, prog_off(layer, s, 0), false);
      if (!__any(m == 0xFFFFFFFFu)) break;
    }
    PHT(0)
#pragma unroll 1
    for (int q = qt < 0 ? 1 : qt;; q = 0) {
      const int lane = lane_id(), g = lane >> 4, r = lane & 15;
      asm volatile(O1_ASM : "=&{v[96:103]}"(acc) : OM_OPS(q), "{v[192:223]}"(y0), "{v[224:255]}"(y1), [ao] "v"(wv * 64 + lane == 0 ? cnt_off(layer, CNT_OUT, s) : OOB),
                   [at] "v"(rtc() & 0x7FFFFFFFu) : O1_CLOB, "scc", "memory");
      if (q == q0) break;
      *(v4u*)(dyn_lds + 65536 + wv * 1024 + lane_id() * 16) = v4u{acc[0], acc[1], acc[2], acc[3]};
      asm volatile("s_barrier" ::: "memory");
    }
    const v4u second = *(const v4u*)(dyn_lds + 65536 + wv * 1024 + lane_id() * 16);
    acc[4] = second.x, acc[5] = second.y, acc[6] = second.z, acc[7] = second.w;
    PHV(32 + (s - 61) * 4, pht0)
  } else {
    asm volatile(O2_ASM : "=&{v[96:103]}"(acc), "=&{v[192:223]}"(y0), "=&{v[224:255]}"(y1) : OM_OPS(0), [yo] "v"(yo), [sy] "s"(sy) : O2_CLOB, "scc", "memory");
  }
  f32x2* sp = (f32x2*)(dyn_lds + 81920);
#pragma unroll
  for (int q = 0; q < 2; ++q) {
    if (q && qt >= 0) break;
    const v4u x = q ? x1 : x0;
    const auto s0 = __builtin_amdgcn_permlane16_swap(acc[4 * q], acc[4 * q + 2], false, false);
    const auto s1 = __builtin_amdgcn_permlane16_swap(acc[4 * q + 1], acc[4 * q + 3], false, false);
    v4u y;
    y.x = pk(lo_f(x.x) + lo_f(s0[0]), hi_f(x.x) + hi_f(s0[0]));
    y.y = pk(lo_f(x.y) + lo_f(s0[1]), hi_f(x.y) + hi_f(s0[1]));
    y.z = pk(lo_f(x.z) + lo_f(s1[0]), hi_f(x.z) + hi_f(s1[0]));
    y.w = pk(lo_f(x.w) + lo_f(s1[1]), hi_f(x.w) + hi_f(s1[1]));
    stc(rs, xo + q * KT * 1024, y);
    const f32x2 st = rows_sum(frag_stat(y));
    if (lane < 16) sp[(wv * 2 + q) * 16 + lane] = st;
  }
  PHT(2)
  asm volatile("s_waitcnt lgkmcnt(0)\n\ts_barrier" ::: "memory");
  if (wv == 0) {
    sp += lane & 31;
    __builtin_amdgcn_raw_buffer_store_b64(__builtin_bit_cast(v2u, (sp[0] + sp[32]) + (sp[64] + sp[96])), rs,
                                          lane < (qt < 0 ? 32 : 16) ? (int)OFF_ST + ((s * 32 + q0 * 16 + lane) * 6 + cg) * 8 : OOB, 0, AUX_SC1);
  }
  dep_signal(wv_, ws, cnt_off(layer, CNT_OUT, s), qt < 0 ? 2u : 1u);
  PHT(3)
  if (s > 60) { PHV(33 + (s - 61) * 4, pht1) PHV(34 + (s - 61) * 4, pht2) PHV(35 + (s - 61) * 4, pht3) }
}
constexpr int JOB_LDS = 4 * NFK * 1024;
constexpr int FAN_LAST = fan_off(0, 1, 0) + 20 * 64;
#define OAUX 0
__device__ __forceinline__ void job_last(int wv_, unsigned char* ws, u16* out, int jb, int rt, int hf) {
  const rsrc_t rs = ws_rsrc(ws), ro = __builtin_amdgcn_make_buffer_rsrc(out, 0, S_ * D_ * 2, 0x00020000);
  const int wv = wv_;
  v32u a[8];
  idft_matrix(a, rs, hf, wv, lane_id());
  PH(26)
  if (jb >= 0) {
    job_col(wv_, ws, rs, lds_base(), jb, FAN_LAST, fan_off(0, 0, 0), true);
    PH(13)
  } else {
    fan_wait<FSP, NCOL>(wv_, ws, fan_off(0, 0, 0), rt);
    fan_wait<64, 20>(wv_, ws, FAN_LAST, rt);
  }
  const int lane = lane_id();
  v4u* lds = (v4u*)dyn_lds + 3072;
  XRows x;
  if (rt < 2 * NEARLY) idft_x(x, rs, rt, hf, wv, lane);
  when(TZ);
  PHT(0)
  if (rt >= 2 * NEARLY) idft_x(x, rs, rt, hf, wv, lane);
  idft_stage(rs, lds, rt, wv, lane);
  if (rt >= 2 * NEARLY && __any(max(max(x.p[0].x, x.p[0].z), max(max(x.p[1].x, x.p[1].z), max(x.q[0].x, x.q[1].x))) == ~0u)) idft_x(x, rs, rt, hf, wv, lane);
  PHT(1)
  idft_mul(x, a, lds_base() + 49152 + lane * 16);
  const int o = ((rt * 16 + 2 * (lane & 7) + ((lane >> 3) & 1)) * D_ + hf * 160 + (lane >> 5) * 8) * 2;
#pragma unroll
  for (int m = 0; m < 3; ++m) {
    const v4u y = m < 2 ? x.p[m] : v4u{x.q[0].x, x.q[0].y, x.q[1].x, x.q[1].y};
    const auto s0 = __builtin_amdgcn_permlane16_swap(y.x, y.z, false, false), s1 = __builtin_amdgcn_permlane16_swap(y.y, y.w, false, false);
    __builtin_amdgcn_raw_buffer_store_b128(v4u{s0[0], s1[0], s0[1], s1[1]}, ro,
                                           m < 2 ? o + m * 640 + wv * 64 + (lane & 16) * 2 : (wv < 2 ? o + 256 + wv * 32 + (lane & 16) * 40 : OOB), 0, OAUX);
  }
  PH(15)
  PHV(24, pht0) PHV(25, pht1)
}

constexpr unsigned SPIN_CAP = 1u << 16;
#define NFILL 24
#define FSPARE
#define FLAG 4

#define VM_LD128(dst, off, so, IMM) asm volatile("buffer_load_dwordx4 %0, %1, %2, %3 offen offset:" #IMM " sc1" : "=v"(dst) : "v"(off), "s"(rs), "s"(so) : "memory")
#define VM_LD32(dst, off, so) asm volatile("buffer_load_dword %0, %1, %2, %3 offen sc1" : "=v"(dst) : "v"(off), "s"(rs), "s"(so) : "memory")
#define VM_ST32(val, off, so) asm volatile("buffer_store_dword %0, %1, %2, %3 offen sc1" ::"v"(val), "v"(off), "s"(rs), "s"(so) : "memory")
#define VM_ST128(val, off, so) asm volatile("buffer_store_dwordx4 %0, %1, %2, %3 offen sc1\n\ts_nop 1" ::"v"(val), "v"(off), "s"(rs), "s"(so) : "memory")
#define VM_WAIT(N) asm volatile("s_waitcnt vmcnt(" #N ")" ::: "memory")
#define VM_WAIT1(N, a) asm volatile("s_waitcnt vmcnt(" #N ")" : "+v"(a)::"memory")
__device__ __forceinline__ v4u make_rs(const void* p, unsigned bytes) {
  const unsigned long long a = (unsigned long long)p;
  v4u r;
  r.x = (unsigned)__builtin_amdgcn_readfirstlane((unsigned)a);
  r.y = (unsigned)__builtin_amdgcn_readfirstlane((unsigned)(a >> 32) & 0xFFFFu);
  r.z = bytes;
  r.w = 0x00020000u;
  return r;
}
struct Clk0 { unsigned r, c; };
__device__ __forceinline__ Clk0 clk_sample() {
  unsigned long long r, c;
  asm volatile("s_memrealtime %0\n\ts_memtime %1\n\ts_waitcnt lgkmcnt(0)" : "=s"(r), "=s"(c)::"memory");
  return Clk0{(unsigned)r, (unsigned)c};
}
__device__ __forceinline__ v4u clk_init(Clk0 a) {
  const Clk0 b = clk_sample();
  unsigned k = 1400u;
  if (b.r - a.r >= 100u) k = (unsigned)((float)(b.r - a.r) * 32768.f / (float)(b.c - a.c));
  k = (unsigned)__builtin_amdgcn_readfirstlane((int)min(max(k, 1200u), 3000u));
  asm volatile("" : "+s"(k));
  return v4u{b.r * 8u + 16u, b.c, k, 0u};
}
#define LDSBAR() asm volatile("s_waitcnt lgkmcnt(0)\n\ts_barrier" ::: "memory")
#define MEMPIN() asm volatile("" ::: "memory")

__device__ __forceinline__ int ladder(int x, int i) { return copy_off(((x + (i >> 1)) & 3) | ((i & 1) << 2)); }

struct RecTune { int m1, m2, dl1, rob, c2, vis, dlt, du; };

#define STAT(r)
__device__ __forceinline__ unsigned max4(v4u v) { return max(max(v.x, v.y), max(v.z, v.w)); }
template <int NAP = 6>
__device__ __forceinline__ unsigned flag_finish(const v4u rs, int off, unsigned cv) {
  for (unsigned spins = 0; __any(cv == 0xFFFFFFFFu) && spins < SPIN_CAP; ++spins) {
    __builtin_amdgcn_s_sleep(NAP);
    v4u t;
    VM_LD128(t, off, 0, 0);
    VM_WAIT1(0, t);
    cv = max4(t);
  }
  return cv;
}
struct Pool {
  unsigned long long sel, s3, ysel; int base;
  __device__ __forceinline__ int near(int s) const { return (int)((s3 >> s) & 1ull) * 16384; }
  __device__ __forceinline__ int region(int s) const { return base + s * REG + (int)((sel >> s) & 1ull) * 8192; }
};
constexpr int L_QT = 10240, L_KT = 12288, L_CL = 14336, L_KQ = 16384;
constexpr int L_T = 14336;
constexpr int L_M2 = L_KQ + 65536;
constexpr int REC_ST = L_M2 + 12288;
constexpr int REC_LDS = REC_ST + 4096 + 64;

#define RSTAMP(i)
#define YSTAMP(n)
#define RST_CLOB
#define P1TRACE
#define PVTRACE
#define FINTRACE
#define P1_ASM \
   ".p2align 6\n\t" "s_nop 0\n\t" "s_nop 0\n\t" "s_nop 0\n\t" "s_nop 0\n\t" "s_mov_b32 s64, -1\n\t" "s_mov_b32 s76, 0\n\t" "s_mov_b32 s77, 0\n\t" \
   "s_mov_b32 s71, 63\n\t" "s_mov_b32 s47, 1300\n\t" "s_mov_b32 s49, 0\n\t" "s_mov_b32 s51, 0\n\t" "s_mov_b32 s52, 0\n\t" "s_mov_b32 s70, 0\n\t" \
   "s_mov_b32 s69, 0\n\t" "v_cmp_gt_i32 s[20:21], 0, v126\n\t" "v_cmp_gt_i32 s[22:23], 1, v126\n\t" "v_cmp_gt_i32 s[24:25], 2, v126\n\t" \
   "v_cmp_gt_i32 s[26:27], 3, v126\n\t" "v_cmp_gt_i32 s[28:29], 16, v126\n\t" "v_cmp_gt_i32 s[30:31], 17, v126\n\t" \
   "v_cmp_gt_i32 s[32:33], 18, v126\n\t" "v_cmp_gt_i32 s[34:35], 19, v126\n\t" "s_waitcnt vmcnt(0)\n\t" "s_lshr_b64 s[72:73], %[sel], s71\n\t" \
   "s_lshl_b32 s65, s71, 19\n\t" "s_and_b32 s72, s72, 1\n\t" "s_add_u32 s65, s65, %[pool]\n\t" "s_lshl_b32 s72, s72, 13\n\t" \
   "s_add_u32 s65, s65, s72\n\t" "s_lshr_b64 s[72:73], %[s3], s71\n\t" "s_and_b32 s54, s72, 1\n\t" "s_lshl_b32 s54, s54, 14\n\t" \
   "s_memtime s[62:63]\n\t" "s_memrealtime s[60:61]\n\t" "40:\n\t" "s_cmp_eq_u32 s64, 0\n\t" "s_cbranch_scc1 95f\n\t" "96:\n\t" \
   "s_add_i32 s72, s64, 2\n\t" "s_min_i32 s72, s72, 63\n\t" "s_lshl_b32 s59, s72, 12\n\t" "s_lshl_b32 s79, s72, 13\n\t" \
   "buffer_load_dword v108, v139, %[rs], s65 offen sc1\n\t" "s_cmp_gt_u32 %[wv], 1\n\t" "s_waitcnt vmcnt(4)\n\t" "s_cbranch_scc1 44f\n\t" \
   "buffer_load_dword v15, v140, %[rs], s59 offen sc1\n\t" "ds_write_b128 v127, v[16:19] offset:0\n\t" "ds_write_b128 v127, v[20:23] offset:1024\n\t" \
   "ds_write_b128 v127, v[24:27] offset:2048\n\t" "ds_write_b128 v127, v[28:31] offset:3072\n\t" "s_waitcnt lgkmcnt(0)\n\t" \
   "s_sub_u32 s74, s62, s57\n\t" "s_mul_i32 s74, s74, s58\n\t" "s_lshr_b32 s74, s74, 12\n\t" "s_add_u32 s74, s74, s56\n\t" "s_lshl_b32 s60, s60, 3\n\t" \
   "s_sub_u32 s75, s74, s60\n\t" "s_cmp_lt_u32 s75, 32\n\t" "s_cbranch_scc1 38f\n\t" "s_cmp_lt_i32 s75, 0\n\t" "s_cselect_b32 s61, 0, 31\n\t" \
   "s_add_u32 s74, s60, s61\n\t" "s_sub_i32 s75, s61, s75\n\t" "s_add_i32 s75, s75, 1\n\t" "s_ashr_i32 s75, s75, 1\n\t" "s_min_i32 s75, s75, 8\n\t" \
   "s_max_i32 s75, s75, -8\n\t" "s_mov_b32 s56, s74\n\t" "s_mov_b32 s57, s62\n\t" "s_add_u32 s58, s58, s75\n\t" "38:\n\t" "s_min_u32 s72, s70, 1\n\t" \
   "s_and_b32 s71, s74, -4\n\t" "s_or_b32 s71, s71, s72\n\t" "v_mov_b32 v111, s71\n\t" "buffer_store_dword v111, v131, %[rs], s65 offen sc1\n\t" \
   P1TRACE "s_barrier\n\t" "ds_read_b128 v[160:163], v122 offset:0\n\t" "ds_read_b128 v[164:167], v122 offset:1024\n\t" \
   "ds_read_b128 v[168:171], v122 offset:2048\n\t" "ds_read_b128 v[172:175], v122 offset:3072\n\t" "ds_read_b128 v[176:179], v122 offset:4096\n\t" \
   "ds_read_b128 v[180:183], v122 offset:5120\n\t" "ds_read_b128 v[184:187], v122 offset:6144\n\t" "ds_read_b128 v[188:191], v122 offset:7168\n\t" \
   "s_cmp_lt_i32 s64, 1\n\t" "s_cselect_b32 s72, 0x7ffffff0, 0\n\t" "s_max_i32 s71, s64, 1\n\t" "v_or_b32 v109, s72, v143\n\t" \
   "s_lshl_b32 s71, s71, 12\n\t" "s_nop 0\n\t" "s_sub_u32 s71, s71, 4096\n\t" "buffer_store_dword v111, v109, %[rs], s71 offen sc1\n\t" \
   "s_add_u32 s48, s69, s47\n\t" "s_cmp_eq_u32 s70, 0\n\t" "s_cselect_b32 s48, s48, -1\n\t" "s_mov_b32 s66, s65\n\t" "s_add_i32 s64, s64, 1\n\t" \
   "s_and_b32 s71, s64, 63\n\t" "s_lshr_b64 s[72:73], %[sel], s71\n\t" "s_lshl_b32 s65, s71, 19\n\t" "s_and_b32 s72, s72, 1\n\t" \
   "s_add_u32 s65, s65, %[pool]\n\t" "s_lshl_b32 s72, s72, 13\n\t" "s_add_u32 s65, s65, s72\n\t" "s_xor_b32 s67, s54, %[lad0]\n\t" \
   "s_xor_b32 s68, s67, 0x4000\n\t" "s_add_u32 s67, s67, s66\n\t" "s_add_u32 s68, s68, s66\n\t" "s_lshr_b64 s[72:73], %[s3], s71\n\t" \
   "s_and_b32 s54, s72, 1\n\t" "s_lshl_b32 s54, s54, 14\n\t" "s_cmp_eq_u32 s64, 0\n\t" "s_cselect_b32 s70, 1, 0\n\t" "s_and_b32 s55, %[wv], 1\n\t" \
   "s_lshl_b32 s55, s55, 14\n\t" "s_xor_b32 s55, s55, s54\n\t" "s_xor_b32 s50, s55, 0x4000\n\t" "s_add_u32 s55, s55, s65\n\t" \
   "s_add_u32 s50, s50, s65\n\t" "s_sub_u32 s53, s64, 63\n\t" "s_mul_i32 s53, s53, s64\n\t" "v_mov_b32 v48, 0\n\t" "v_mov_b32 v49, 0\n\t" \
   "v_mov_b32 v50, 0\n\t" "v_mov_b32 v51, 0\n\t" "v_mov_b32 v52, 0\n\t" "v_mov_b32 v53, 0\n\t" "v_mov_b32 v54, 0\n\t" "v_mov_b32 v55, 0\n\t" \
   "v_mov_b32 v56, 0\n\t" "v_mov_b32 v57, 0\n\t" "v_mov_b32 v58, 0\n\t" "v_mov_b32 v59, 0\n\t" "v_mov_b32 v60, 0\n\t" "v_mov_b32 v61, 0\n\t" \
   "v_mov_b32 v62, 0\n\t" "v_mov_b32 v63, 0\n\t" "s_waitcnt vmcnt(2)\n\t" "v_cmp_eq_u32 vcc, -1, v15\n\t" "s_nop 0\n\t" "s_cbranch_vccnz 141f\n\t" \
   "41:\n\t" "s_branch 48f\n\t" "44:\n\t" "v_mfma_f32_16x16x32_bf16 v[32:35], v[160:163], v[16:19], 0\n\t" "s_max_i32 s51, s64, 0\n\t" \
   "s_lshl_b32 s52, s51, 16\n\t" "s_lshl_b32 s51, s51, 14\n\t" "v_mfma_f32_16x16x32_bf16 v[36:39], v[176:179], v[16:19], 0\n\t" \
   "s_cmp_lt_i32 s64, 0\n\t" "s_cselect_b32 s49, 0x7ffffff0, 0\n\t" "s_add_i32 s72, s64, 1\n\t" \
   "v_mfma_f32_16x16x32_bf16 v[32:35], v[164:167], v[20:23], v[32:35]\n\t" "s_min_i32 s72, s72, 63\n\t" "s_lshl_b32 s79, s72, 13\n\t" \
   "v_mfma_f32_16x16x32_bf16 v[36:39], v[180:183], v[20:23], v[36:39]\n\t" "v_mfma_f32_16x16x32_bf16 v[32:35], v[168:171], v[24:27], v[32:35]\n\t" \
   "s_waitcnt lgkmcnt(0)\n\t" "v_mfma_f32_16x16x32_bf16 v[36:39], v[184:187], v[24:27], v[36:39]\n\t" "s_sub_u32 s74, s62, s57\n\t" \
   "s_mul_i32 s74, s74, s58\n\t" "s_lshr_b32 s74, s74, 12\n\t" "v_mfma_f32_16x16x32_bf16 v[32:35], v[172:175], v[28:31], v[32:35]\n\t" \
   "s_add_u32 s74, s74, s56\n\t" "s_lshl_b32 s60, s60, 3\n\t" "s_sub_u32 s75, s74, s60\n\t" \
   "v_mfma_f32_16x16x32_bf16 v[36:39], v[188:191], v[28:31], v[36:39]\n\t" "s_cmp_lt_u32 s75, 32\n\t" "s_cbranch_scc1 39f\n\t" \
   "s_cmp_lt_i32 s75, 0\n\t" "s_cselect_b32 s61, 0, 31\n\t" "s_add_u32 s74, s60, s61\n\t" "s_sub_i32 s75, s61, s75\n\t" "s_add_i32 s75, s75, 1\n\t" \
   "s_ashr_i32 s75, s75, 1\n\t" "s_min_i32 s75, s75, 8\n\t" "s_max_i32 s75, s75, -8\n\t" "s_mov_b32 s56, s74\n\t" "s_mov_b32 s57, s62\n\t" \
   "s_add_u32 s58, s58, s75\n\t" "39:\n\t" "s_barrier\n\t" "v_mfma_f32_16x16x32_bf16 v[40:43], v[144:147], v[16:19], 0\n\t" "s_min_u32 s72, s70, 1\n\t" \
   "s_and_b32 s71, s74, -4\n\t" "s_or_b32 s71, s71, s72\n\t" "v_mov_b32 v111, s71\n\t" \
   "v_mfma_f32_16x16x32_bf16 v[40:43], v[148:151], v[20:23], v[40:43]\n\t" "buffer_store_dword v111, v131, %[rs], s65 offen sc1\n\t" P1TRACE \
   "v_mov_b32 v48, 0\n\t" "v_mov_b32 v49, 0\n\t" "v_mfma_f32_16x16x32_bf16 v[40:43], v[152:155], v[24:27], v[40:43]\n\t" "v_mov_b32 v50, 0\n\t" \
   "v_mov_b32 v51, 0\n\t" "v_mov_b32 v52, 0\n\t" "v_mov_b32 v53, 0\n\t" "v_mfma_f32_16x16x32_bf16 v[40:43], v[156:159], v[28:31], v[40:43]\n\t" \
   "v_mov_b32 v54, 0\n\t" "v_mov_b32 v55, 0\n\t" "v_cndmask_b32 v32, v32, v125, s[20:21]\n\t" "v_cndmask_b32 v33, v33, v125, s[22:23]\n\t" \
   "v_cndmask_b32 v34, v34, v125, s[24:25]\n\t" "v_cndmask_b32 v35, v35, v125, s[26:27]\n\t" "v_cndmask_b32 v36, v36, v125, s[28:29]\n\t" \
   "v_cndmask_b32 v37, v37, v125, s[30:31]\n\t" "v_cndmask_b32 v38, v38, v125, s[32:33]\n\t" "v_cndmask_b32 v39, v39, v125, s[34:35]\n\t" \
   "v_exp_f32 v64, v32\n\t" "s_add_u32 s48, s69, s47\n\t" "s_cmp_eq_u32 s70, 0\n\t" "s_cselect_b32 s48, s48, -1\n\t" "v_exp_f32 v65, v33\n\t" \
   "s_mov_b32 s66, s65\n\t" "s_add_i32 s64, s64, 1\n\t" "s_and_b32 s71, s64, 63\n\t" "v_exp_f32 v66, v34\n\t" "s_lshr_b64 s[72:73], %[sel], s71\n\t" \
   "s_lshl_b32 s65, s71, 19\n\t" "s_and_b32 s72, s72, 1\n\t" "v_exp_f32 v67, v35\n\t" "s_add_u32 s65, s65, %[pool]\n\t" "s_lshl_b32 s72, s72, 13\n\t" \
   "s_add_u32 s65, s65, s72\n\t" "v_exp_f32 v68, v36\n\t" "s_xor_b32 s67, s54, %[lad0]\n\t" "s_xor_b32 s68, s67, 0x4000\n\t" \
   "s_add_u32 s67, s67, s66\n\t" "v_exp_f32 v69, v37\n\t" "s_add_u32 s68, s68, s66\n\t" "s_lshr_b64 s[72:73], %[s3], s71\n\t" \
   "s_and_b32 s54, s72, 1\n\t" "v_exp_f32 v70, v38\n\t" "s_lshl_b32 s54, s54, 14\n\t" "s_cmp_eq_u32 s64, 0\n\t" "s_cselect_b32 s70, 1, 0\n\t" \
   "v_exp_f32 v71, v39\n\t" "s_and_b32 s55, %[wv], 1\n\t" "s_lshl_b32 s55, s55, 14\n\t" "s_xor_b32 s55, s55, s54\n\t" "v_exp_f32 v72, v40\n\t" \
   "s_xor_b32 s50, s55, 0x4000\n\t" "s_add_u32 s55, s55, s65\n\t" "s_add_u32 s50, s50, s65\n\t" "v_exp_f32 v73, v41\n\t" "s_sub_u32 s53, s64, 63\n\t" \
   "s_mul_i32 s53, s53, s64\n\t" "v_mov_b32 v56, 0\n\t" "v_exp_f32 v74, v42\n\t" "v_mov_b32 v57, 0\n\t" "v_mov_b32 v58, 0\n\t" "v_mov_b32 v59, 0\n\t" \
   "v_exp_f32 v75, v43\n\t" "v_mov_b32 v60, 0\n\t" "v_mov_b32 v61, 0\n\t" "v_mov_b32 v62, 0\n\t" "v_mov_b32 v63, 0\n\t" "v_add_f32 v80, v64, v65\n\t" \
   "v_add_f32 v81, v66, v67\n\t" "v_add_f32 v82, v68, v69\n\t" "v_add_f32 v83, v70, v71\n\t" "v_add_f32 v84, v72, v73\n\t" \
   "v_add_f32 v85, v74, v75\n\t" "v_add_f32 v80, v80, v81\n\t" "v_add_f32 v82, v82, v83\n\t" "v_add_f32 v84, v84, v85\n\t" \
   "v_add_f32 v80, v80, v82\n\t" "v_fma_f32 v82, v80, 2.0, v84\n\t" "v_fma_f32 v80, v80, 2.0, v84\n\t" "ds_read_b128 v[160:163], v122 offset:0\n\t" \
   "ds_read_b128 v[164:167], v122 offset:1024\n\t" "v_permlane16_swap_b32 v80, v82\n\t" "v_add_f32 v81, v80, v82\n\t" "v_add_f32 v80, v80, v82\n\t" \
   "ds_read_b128 v[168:171], v122 offset:2048\n\t" "ds_read_b128 v[172:175], v122 offset:3072\n\t" "v_permlane32_swap_b32 v80, v81\n\t" \
   "v_add_f32 v80, v80, v81\n\t" "v_rcp_f32 v90, v80\n\t" "v_add_u32 v81, 0xe0800000, v80\n\t" "v_add_f32 v90, v90, v90\n\t" \
   "v_cmp_lt_u32 vcc, 0x40000000, v81\n\t" "ds_read_b128 v[176:179], v122 offset:4096\n\t" "ds_read_b128 v[180:183], v122 offset:5120\n\t" \
   "ds_read_b128 v[184:187], v122 offset:6144\n\t" "ds_read_b128 v[188:191], v122 offset:7168\n\t" "s_cbranch_vccnz 74f\n\t" "75:\n\t" \
   "v_pk_mul_f32 v[64:65], v[64:65], v[90:91] op_sel_hi:[1,0]\n\t" "v_pk_mul_f32 v[66:67], v[66:67], v[90:91] op_sel_hi:[1,0]\n\t" \
   "v_pk_mul_f32 v[68:69], v[68:69], v[90:91] op_sel_hi:[1,0]\n\t" "v_pk_mul_f32 v[70:71], v[70:71], v[90:91] op_sel_hi:[1,0]\n\t" \
   "v_pk_mul_f32 v[72:73], v[72:73], v[90:91] op_sel_hi:[1,0]\n\t" "v_pk_mul_f32 v[74:75], v[74:75], v[90:91] op_sel_hi:[1,0]\n\t" \
   "v_cvt_pk_bf16_f32 v44, v64, v65\n\t" "v_cvt_pk_bf16_f32 v46, v66, v67\n\t" "v_cvt_pk_bf16_f32 v45, v68, v69\n\t" \
   "v_cvt_pk_bf16_f32 v47, v70, v71\n\t" "v_cvt_pk_bf16_f32 v32, v72, v73\n\t" "v_cvt_pk_bf16_f32 v33, v74, v75\n\t" "s_waitcnt vmcnt(1)\n\t" "48:\n\t" \
   "v_readlane_b32 s69, v108, 0\n\t" "v_readlane_b32 s71, v108, 1\n\t" "s_cmp_eq_u32 s64, 64\n\t" "s_cbranch_scc1 90f\n\t" "s_cmp_eq_u32 s69, -1\n\t" \
   "s_cbranch_scc1 63f\n\t" "s_cmp_eq_u32 s71, -1\n\t" "s_cselect_b32 s47, s47, s71\n\t" "50:\n\t" "s_add_u32 s73, s69, %[m1]\n\t" RSTAMP(5) "51:\n\t" \
   "s_memrealtime s[60:61]\n\t" "s_memtime s[62:63]\n\t" "s_waitcnt lgkmcnt(0)\n\t" "s_sub_u32 s74, s62, s57\n\t" "s_mul_i32 s74, s74, s58\n\t" \
   "s_lshr_b32 s74, s74, 12\n\t" "s_add_u32 s74, s74, s56\n\t" "s_lshl_b32 s60, s60, 3\n\t" "s_sub_u32 s75, s74, s60\n\t" "s_cmp_lt_u32 s75, 32\n\t" \
   "s_cbranch_scc1 52f\n\t" "s_cmp_lt_i32 s75, 0\n\t" "s_cselect_b32 s61, 0, 31\n\t" "s_add_u32 s74, s60, s61\n\t" "s_sub_i32 s75, s61, s75\n\t" \
   "s_add_i32 s75, s75, 1\n\t" "s_ashr_i32 s75, s75, 1\n\t" "s_min_i32 s75, s75, 8\n\t" "s_max_i32 s75, s75, -8\n\t" "s_mov_b32 s56, s74\n\t" \
   "s_mov_b32 s57, s62\n\t" "s_add_u32 s58, s58, s75\n\t" "52:\n\t" "s_sub_i32 s74, s73, s74\n\t" "s_cmp_le_i32 s74, 150\n\t" "s_cbranch_scc1 53f\n\t" \
   "s_cmp_lt_i32 s74, 260\n\t" "s_cbranch_scc1 51b\n\t" "s_sleep 2\n\t" "s_cmp_lt_i32 s74, 32000\n\t" "s_cbranch_scc1 51b\n\t" "s_mov_b32 s74, 0\n\t" \
   "53:\n\t" "s_sub_i32 s74, s74, 56\n\t" "s_max_i32 s74, s74, 0\n\t" "s_mul_i32 s74, s74, 23\n\t" "s_lshr_b32 s74, s74, 5\n\t" \
   "s_and_b32 s74, s74, -4\n\t" "s_getpc_b64 s[60:61]\n\t" "54:\n\t" "s_sub_u32 s74, 55f-54b, s74\n\t" "s_add_u32 s60, s60, s74\n\t" \
   "s_addc_u32 s61, s61, 0\n\t" "s_setpc_b64 s[60:61]\n\t" "s_nop 3\n\t" "s_nop 3\n\t" "s_nop 3\n\t" "s_nop 3\n\t" "s_nop 3\n\t" "s_nop 3\n\t" \
   "s_nop 3\n\t" "s_nop 3\n\t" "s_nop 3\n\t" "s_nop 3\n\t" "s_nop 3\n\t" "s_nop 3\n\t" "s_nop 3\n\t" "s_nop 3\n\t" "s_nop 3\n\t" "s_nop 3\n\t" \
   "s_nop 3\n\t" "s_nop 3\n\t" "s_nop 3\n\t" "s_nop 3\n\t" "s_nop 3\n\t" "s_nop 3\n\t" "s_nop 3\n\t" "s_nop 3\n\t" "55:\n\t" "20:\n\t" RSTAMP(4) \
   "buffer_load_dwordx4 v[64:67], v128, %[rs], s67 offen sc1\n\t" "buffer_load_dwordx4 v[68:71], v129, %[rs], s67 offen sc1\n\t" \
   "buffer_load_dwordx4 v[72:75], v130, %[rs], s67 offen sc1\n\t" "s_nop 7\n\t" "s_nop 7\n\t" "s_nop 7\n\t" "s_cmp_lg_u32 s70, 0\n\t" \
   "s_cbranch_scc1 27f\n\t" "buffer_load_dwordx4 v[16:19], v138, %[rs], s79 offen offset:0 sc1\n\t" \
   "buffer_load_dwordx4 v[20:23], v138, %[rs], s79 offen offset:1024 sc1\n\t" \
   "buffer_load_dwordx4 v[24:27], v138, %[rs], s79 offen offset:2048 sc1\n\t" \
   "buffer_load_dwordx4 v[28:31], v138, %[rs], s79 offen offset:3072 sc1\n\t" "v_or_b32 v108, s49, v141\n\t" "v_or_b32 v109, s49, v142\n\t" \
   "buffer_store_dwordx4 v[44:47], v108, %[rs], s52 offen sc1\n\t" "buffer_store_dwordx2 v[32:33], v109, %[rs], s51 offen sc1\n\t" "28:\n\t" RSTAMP(3) \
   "s_waitcnt vmcnt(8)\n\t" "ds_write_b128 v112, v[64:67]\n\t" "v_smfmac_f32_16x16x64_bf16 v[48:51], v[64:67], a[0:7], v134\n\t" \
   "s_waitcnt vmcnt(7)\n\t" "ds_write_b128 v113, v[68:71]\n\t" "s_waitcnt vmcnt(6)\n\t" "ds_write_b128 v114, v[72:75]\n\t" RSTAMP(7) "2:\n\t" \
   "v_smfmac_f32_16x16x64_bf16 v[52:55], v[64:67], a[64:71], v134\n\t" "v_max3_u32 v110, v64, v66, v68\n\t" "v_max3_u32 v111, v70, v72, v74\n\t" \
   "v_smfmac_f32_16x16x64_bf16 v[48:51], v[68:71], a[8:15], v134\n\t" "v_max_u32 v110, v110, v111\n\t" "v_cmp_eq_u32 vcc, -1, v110\n\t" \
   "v_smfmac_f32_16x16x64_bf16 v[52:55], v[68:71], a[72:79], v134\n\t" "s_cbranch_vccnz 8f\n\t" \
   "v_smfmac_f32_16x16x64_bf16 v[48:51], v[72:75], a[16:23], v134\n\t" "s_waitcnt lgkmcnt(0)\n\t" "s_barrier\n\t" RSTAMP(0) \
   "ds_read_b128 v[76:79], v115\n\t" "ds_read_b128 v[80:83], v116\n\t" "v_smfmac_f32_16x16x64_bf16 v[52:55], v[72:75], a[80:87], v134\n\t" \
   "ds_read_b128 v[84:87], v117\n\t" "ds_read_b128 v[88:91], v118\n\t" "v_smfmac_f32_16x16x64_bf16 v[56:59], v[64:67], a[128:135], v134\n\t" \
   "ds_read_b128 v[92:95], v119\n\t" "ds_read_b128 v[96:99], v120\n\t" "v_smfmac_f32_16x16x64_bf16 v[60:63], v[64:67], a[192:199], v134\n\t" \
   "ds_read_b128 v[100:103], v121\n\t" "v_smfmac_f32_16x16x64_bf16 v[56:59], v[68:71], a[136:143], v134\n\t" \
   "v_smfmac_f32_16x16x64_bf16 v[60:63], v[68:71], a[200:207], v134\n\t" "s_waitcnt lgkmcnt(6)\n\t" \
   "v_smfmac_f32_16x16x64_bf16 v[48:51], v[76:79], a[24:31], v134\n\t" "v_smfmac_f32_16x16x64_bf16 v[52:55], v[76:79], a[88:95], v134\n\t" \
   "s_waitcnt lgkmcnt(5)\n\t" "v_smfmac_f32_16x16x64_bf16 v[48:51], v[80:83], a[32:39], v134\n\t" \
   "v_smfmac_f32_16x16x64_bf16 v[52:55], v[80:83], a[96:103], v134\n\t" "s_waitcnt lgkmcnt(4)\n\t" \
   "v_smfmac_f32_16x16x64_bf16 v[48:51], v[84:87], a[40:47], v134\n\t" "v_smfmac_f32_16x16x64_bf16 v[52:55], v[84:87], a[104:111], v134\n\t" \
   "s_waitcnt lgkmcnt(3)\n\t" "v_smfmac_f32_16x16x64_bf16 v[48:51], v[88:91], a[48:55], v134\n\t" \
   "v_smfmac_f32_16x16x64_bf16 v[52:55], v[88:91], a[112:119], v134\n\t" "s_waitcnt lgkmcnt(2)\n\t" \
   "v_smfmac_f32_16x16x64_bf16 v[48:51], v[92:95], a[56:63], v134\n\t" "v_smfmac_f32_16x16x64_bf16 v[52:55], v[92:95], a[120:127], v134\n\t" \
   "s_waitcnt lgkmcnt(0)\n\t" "v_smfmac_f32_16x16x64_bf16 v[48:51], v[96:99], v[192:199], v134\n\t" \
   "v_smfmac_f32_16x16x64_bf16 v[48:51], v[100:103], v[200:207], v134\n\t" "v_smfmac_f32_16x16x64_bf16 v[52:55], v[96:99], v[208:215], v134\n\t" \
   "v_smfmac_f32_16x16x64_bf16 v[52:55], v[100:103], v[216:223], v134\n\t" "v_smfmac_f32_16x16x64_bf16 v[56:59], v[72:75], a[144:151], v134\n\t" \
   "v_add_f32 v48, v48, v49\n\t" "v_add_f32 v49, v50, v51\n\t" "v_smfmac_f32_16x16x64_bf16 v[60:63], v[72:75], a[208:215], v134\n\t" \
   "v_mul_f32 v48, 0x3e0293ee, v48\n\t" "v_mul_f32 v49, 0x3e0293ee, v49\n\t" "v_smfmac_f32_16x16x64_bf16 v[56:59], v[76:79], a[152:159], v134\n\t" \
   "v_add_f32 v52, v52, v53\n\t" "v_add_f32 v53, v54, v55\n\t" "v_smfmac_f32_16x16x64_bf16 v[56:59], v[80:83], a[160:167], v134\n\t" \
   "v_mul_f32 v52, 0x3e0293ee, v52\n\t" "v_mul_f32 v53, 0x3e0293ee, v53\n\t" "v_smfmac_f32_16x16x64_bf16 v[56:59], v[84:87], a[168:175], v134\n\t" \
   "v_cvt_pk_bf16_f32 v48, v48, v49\n\t" "ds_write_b32 v135, v48 offset:10240\n\t" \
   "v_smfmac_f32_16x16x64_bf16 v[56:59], v[88:91], a[176:183], v134\n\t" "v_cvt_pk_bf16_f32 v52, v52, v53\n\t" \
   "ds_write_b32 v135, v52 offset:10496\n\t" "v_smfmac_f32_16x16x64_bf16 v[56:59], v[92:95], a[184:191], v134\n\t" \
   "v_smfmac_f32_16x16x64_bf16 v[56:59], v[96:99], v[224:231], v134\n\t" "v_smfmac_f32_16x16x64_bf16 v[56:59], v[100:103], v[232:239], v134\n\t" \
   "s_waitcnt lgkmcnt(0)\n\t" "s_barrier\n\t" "ds_read_b64_tr_b16 v[64:65], v136 offset:10240\n\t" "ds_read_b64_tr_b16 v[66:67], v136 offset:10496\n\t" \
   "v_smfmac_f32_16x16x64_bf16 v[60:63], v[76:79], a[216:223], v134\n\t" "ds_read_b64_tr_b16 v[68:69], v136 offset:10752\n\t" \
   "v_smfmac_f32_16x16x64_bf16 v[60:63], v[80:83], a[224:231], v134\n\t" "ds_read_b64_tr_b16 v[70:71], v136 offset:11008\n\t" \
   "v_smfmac_f32_16x16x64_bf16 v[60:63], v[84:87], a[232:239], v134\n\t" "ds_read_b64_tr_b16 v[72:73], v136 offset:11264\n\t" \
   "v_smfmac_f32_16x16x64_bf16 v[60:63], v[88:91], a[240:247], v134\n\t" "ds_read_b64_tr_b16 v[74:75], v136 offset:11520\n\t" \
   "v_add_f32 v56, v56, v57\n\t" "v_add_f32 v57, v58, v59\n\t" "v_smfmac_f32_16x16x64_bf16 v[60:63], v[92:95], a[248:255], v134\n\t" \
   "ds_read_b64_tr_b16 v[104:105], v136 offset:11776\n\t" "ds_read_b64_tr_b16 v[106:107], v136 offset:12032\n\t" \
   "v_smfmac_f32_16x16x64_bf16 v[60:63], v[96:99], v[240:247], v134\n\t" "v_cvt_pk_bf16_f32 v56, v56, v57\n\t" \
   "ds_write_b32 v135, v56 offset:12288\n\t" "v_smfmac_f32_16x16x64_bf16 v[60:63], v[100:103], v[248:255], v134\n\t" RSTAMP(6) \
   "s_waitcnt lgkmcnt(7)\n\t" "v_mfma_f32_16x16x32_bf16 v[48:51], v[160:163], v[64:67], 0\n\t" \
   "v_mfma_f32_16x16x32_bf16 v[52:55], v[176:179], v[64:67], 0\n\t" "s_waitcnt lgkmcnt(3)\n\t" "s_nop 0\n\t" "v_add_f32 v60, v60, v61\n\t" \
   "v_add_f32 v61, v62, v63\n\t" "v_cvt_pk_bf16_f32 v60, v60, v61\n\t" "ds_write_b32 v135, v60 offset:12544\n\t" \
   "v_mfma_f32_16x16x32_bf16 v[48:51], v[164:167], v[68:71], v[48:51]\n\t" "v_mfma_f32_16x16x32_bf16 v[52:55], v[180:183], v[68:71], v[52:55]\n\t" \
   "v_mfma_f32_16x16x32_bf16 v[48:51], v[168:171], v[72:75], v[48:51]\n\t" "v_mfma_f32_16x16x32_bf16 v[52:55], v[184:187], v[72:75], v[52:55]\n\t" \
   "s_waitcnt lgkmcnt(2)\n\t" "v_mfma_f32_16x16x32_bf16 v[48:51], v[172:175], v[104:107], v[48:51]\n\t" \
   "v_mfma_f32_16x16x32_bf16 v[52:55], v[188:191], v[104:107], v[52:55]\n\t" "s_waitcnt lgkmcnt(0)\n\t" "s_barrier\n\t" \
   "ds_read_b64_tr_b16 v[144:145], v136 offset:12288\n\t" "ds_read_b64_tr_b16 v[146:147], v136 offset:12544\n\t" \
   "ds_read_b64_tr_b16 v[148:149], v136 offset:12800\n\t" "ds_read_b64_tr_b16 v[150:151], v136 offset:13056\n\t" \
   "ds_read_b64_tr_b16 v[152:153], v136 offset:13312\n\t" "ds_read_b64_tr_b16 v[154:155], v136 offset:13568\n\t" \
   "ds_read_b64_tr_b16 v[156:157], v136 offset:13824\n\t" "ds_read_b64_tr_b16 v[158:159], v136 offset:14080\n\t" "s_cmp_eq_u32 s53, 0\n\t" \
   "s_cbranch_scc1 91f\n\t" "93:\n\t" RSTAMP(1) "v_cndmask_b32 v48, v48, v52, %[hi8]\n\t" "v_cndmask_b32 v49, v49, v53, %[hi8]\n\t" \
   "v_cndmask_b32 v50, v50, v54, %[hi8]\n\t" "v_cndmask_b32 v51, v51, v55, %[hi8]\n\t" "v_exp_f32 v80, v48\n\t" "v_exp_f32 v81, v49\n\t" \
   "s_waitcnt lgkmcnt(6)\n\t" "v_mfma_f32_16x16x32_bf16 v[76:79], v[144:147], v[64:67], 0\n\t" "v_exp_f32 v82, v50\n\t" "v_exp_f32 v83, v51\n\t" \
   "s_waitcnt lgkmcnt(4)\n\t" "v_mfma_f32_16x16x32_bf16 v[76:79], v[148:151], v[68:71], v[76:79]\n\t" "v_add_f32 v96, v80, v81\n\t" \
   "v_add_f32 v103, v82, v83\n\t" "s_waitcnt lgkmcnt(2)\n\t" "v_mfma_f32_16x16x32_bf16 v[76:79], v[152:155], v[72:75], v[76:79]\n\t" \
   "v_add_f32 v94, v96, v103\n\t" "v_add_f32 v96, v96, v103\n\t" "s_waitcnt lgkmcnt(0)\n\t" \
   "v_mfma_f32_16x16x32_bf16 v[76:79], v[156:159], v[104:107], v[76:79]\n\t" "v_permlane32_swap_b32 v96, v94\n\t" "v_add_f32 v96, v96, v94\n\t" \
   "s_nop 1\n\t" "v_add_f32_dpp v96, v96, v96 row_ror:8 row_mask:0xf bank_mask:0xf\n\t" "s_nop 2\n\t" "v_exp_f32 v88, v76\n\t" "v_exp_f32 v89, v77\n\t" \
   "v_add_f32 v103, v96, v88\n\t" "v_exp_f32 v90, v78\n\t" "v_add_f32 v103, v103, v89\n\t" "v_exp_f32 v91, v79\n\t" "v_add_f32 v103, v103, v90\n\t" \
   "v_add_f32 v97, v103, v91\n\t" "v_add_f32 v103, v103, v91\n\t" "s_nop 1\n\t" "v_permlane16_swap_b32 v103, v97\n\t" "v_add_f32 v103, v103, v97\n\t" \
   "v_rcp_f32 v100, v103\n\t" "v_add_u32 v94, 0xe0800000, v103\n\t" "v_pk_mul_f32 v[80:81], v[80:81], v[100:101] op_sel_hi:[1,0]\n\t" \
   "v_cmp_lt_u32 vcc, 0x40000000, v94\n\t" "v_pk_mul_f32 v[82:83], v[82:83], v[100:101] op_sel_hi:[1,0]\n\t" "s_cbranch_vccnz 70f\n\t" \
   "v_cvt_pk_bf16_f32 v48, v80, v81\n\t" "v_cvt_pk_bf16_f32 v49, v82, v83\n\t" "buffer_store_dwordx2 v[48:49], v132, %[rs], s55 offen sc1\n\t" \
   "v_pk_mul_f32 v[88:89], v[88:89], v[100:101] op_sel_hi:[1,0]\n\t" "v_pk_mul_f32 v[90:91], v[90:91], v[100:101] op_sel_hi:[1,0]\n\t" \
   "v_cvt_pk_bf16_f32 v52, v88, v89\n\t" "v_cvt_pk_bf16_f32 v53, v90, v91\n\t" "71:\n\t" \
   "buffer_store_dwordx2 v[52:53], v133, %[rs], s65 offen sc1\n\t" "s_memtime s[62:63]\n\t" "s_memrealtime s[60:61]\n\t" \
   "buffer_store_dwordx2 v[48:49], v132, %[rs], s50 offen sc1\n\t" RSTAMP(2) "s_branch 40b\n\t" "70:\n\t" "v_max3_f32 v94, v48, v49, v50\n\t" \
   "v_max3_f32 v102, v76, v77, v78\n\t" "v_max3_f32 v94, v94, v51, v79\n\t" "v_max_f32 v92, v94, v102\n\t" "s_nop 1\n\t" \
   "v_max_f32_dpp v102, v92, v92 row_ror:8 row_mask:0xf bank_mask:0xf\n\t" "v_max_f32_dpp v94, v92, v92 row_ror:8 row_mask:0xf bank_mask:0xf\n\t" \
   "s_nop 1\n\t" "v_permlane32_swap_b32 v102, v94\n\t" "v_max_f32 v95, v102, v94\n\t" "v_max_f32 v102, v102, v94\n\t" "s_nop 1\n\t" \
   "v_permlane16_swap_b32 v102, v95\n\t" "v_max_f32 v102, v102, v95\n\t" "v_sub_f32 v80, v48, v102\n\t" "v_sub_f32 v88, v76, v102\n\t" \
   "v_sub_f32 v81, v49, v102\n\t" "v_sub_f32 v89, v77, v102\n\t" "v_sub_f32 v82, v50, v102\n\t" "v_sub_f32 v90, v78, v102\n\t" \
   "v_sub_f32 v83, v51, v102\n\t" "v_sub_f32 v91, v79, v102\n\t" "v_exp_f32 v80, v80\n\t" "v_exp_f32 v88, v88\n\t" "v_exp_f32 v81, v81\n\t" \
   "v_exp_f32 v89, v89\n\t" "v_exp_f32 v82, v82\n\t" "v_exp_f32 v90, v90\n\t" "v_exp_f32 v83, v83\n\t" "v_exp_f32 v91, v91\n\t" "s_nop 0\n\t" \
   "v_add_f32 v96, v80, v81\n\t" "v_add_f32 v103, v82, v83\n\t" "v_add_f32 v94, v96, v103\n\t" "v_add_f32 v96, v96, v103\n\t" "s_nop 1\n\t" \
   "v_permlane32_swap_b32 v96, v94\n\t" "v_add_f32 v96, v96, v94\n\t" "s_nop 1\n\t" \
   "v_add_f32_dpp v96, v96, v96 row_ror:8 row_mask:0xf bank_mask:0xf\n\t" "v_add_f32 v103, v96, v88\n\t" "v_add_f32 v103, v103, v89\n\t" \
   "v_add_f32 v103, v103, v90\n\t" "v_add_f32 v97, v103, v91\n\t" "v_add_f32 v103, v103, v91\n\t" "s_nop 1\n\t" "v_permlane16_swap_b32 v103, v97\n\t" \
   "v_add_f32 v103, v103, v97\n\t" "v_rcp_f32 v100, v103\n\t" "s_nop 0\n\t" "v_pk_mul_f32 v[80:81], v[80:81], v[100:101] op_sel_hi:[1,0]\n\t" \
   "v_pk_mul_f32 v[82:83], v[82:83], v[100:101] op_sel_hi:[1,0]\n\t" "v_pk_mul_f32 v[88:89], v[88:89], v[100:101] op_sel_hi:[1,0]\n\t" \
   "v_pk_mul_f32 v[90:91], v[90:91], v[100:101] op_sel_hi:[1,0]\n\t" "v_cvt_pk_bf16_f32 v48, v80, v81\n\t" "v_cvt_pk_bf16_f32 v49, v82, v83\n\t" \
   "v_cvt_pk_bf16_f32 v52, v88, v89\n\t" "v_cvt_pk_bf16_f32 v53, v90, v91\n\t" "v_min_u32 v48, -2, v48\n\t" "v_min_u32 v52, -2, v52\n\t" \
   "buffer_store_dwordx2 v[48:49], v132, %[rs], s55 offen sc1\n\t" "s_branch 71b\n\t" "74:\n\t" "v_max3_f32 v80, v32, v33, v34\n\t" \
   "v_max3_f32 v81, v35, v36, v37\n\t" "v_max3_f32 v82, v38, v39, v40\n\t" "v_max3_f32 v83, v41, v42, v43\n\t" "v_max3_f32 v80, v80, v81, v82\n\t" \
   "v_max_f32 v81, v80, v83\n\t" "v_max_f32 v80, v80, v83\n\t" "s_nop 1\n\t" "v_permlane16_swap_b32 v80, v81\n\t" "v_max_f32 v82, v80, v81\n\t" \
   "v_max_f32 v80, v80, v81\n\t" "s_nop 1\n\t" "v_permlane32_swap_b32 v80, v82\n\t" "v_max_f32 v80, v80, v82\n\t" "v_sub_f32 v64, v32, v80\n\t" \
   "v_sub_f32 v65, v33, v80\n\t" "v_sub_f32 v66, v34, v80\n\t" "v_sub_f32 v67, v35, v80\n\t" "v_sub_f32 v68, v36, v80\n\t" \
   "v_sub_f32 v69, v37, v80\n\t" "v_sub_f32 v70, v38, v80\n\t" "v_sub_f32 v71, v39, v80\n\t" "v_sub_f32 v72, v40, v80\n\t" \
   "v_sub_f32 v73, v41, v80\n\t" "v_sub_f32 v74, v42, v80\n\t" "v_sub_f32 v75, v43, v80\n\t" "v_exp_f32 v64, v64\n\t" "v_exp_f32 v65, v65\n\t" \
   "v_exp_f32 v66, v66\n\t" "v_exp_f32 v67, v67\n\t" "v_exp_f32 v68, v68\n\t" "v_exp_f32 v69, v69\n\t" "v_exp_f32 v70, v70\n\t" \
   "v_exp_f32 v71, v71\n\t" "v_exp_f32 v72, v72\n\t" "v_exp_f32 v73, v73\n\t" "v_exp_f32 v74, v74\n\t" "v_exp_f32 v75, v75\n\t" "s_nop 0\n\t" \
   "v_add_f32 v80, v64, v65\n\t" "v_add_f32 v81, v66, v67\n\t" "v_add_f32 v82, v68, v69\n\t" "v_add_f32 v83, v70, v71\n\t" \
   "v_add_f32 v84, v72, v73\n\t" "v_add_f32 v85, v74, v75\n\t" "v_add_f32 v80, v80, v81\n\t" "v_add_f32 v82, v82, v83\n\t" \
   "v_add_f32 v84, v84, v85\n\t" "v_add_f32 v80, v80, v82\n\t" "v_fma_f32 v82, v80, 2.0, v84\n\t" "v_fma_f32 v80, v80, 2.0, v84\n\t" "s_nop 0\n\t" \
   "s_nop 0\n\t" "v_permlane16_swap_b32 v80, v82\n\t" "v_add_f32 v81, v80, v82\n\t" "v_add_f32 v80, v80, v82\n\t" "s_nop 0\n\t" "s_nop 0\n\t" \
   "v_permlane32_swap_b32 v80, v81\n\t" "v_add_f32 v80, v80, v81\n\t" "v_rcp_f32 v90, v80\n\t" "s_nop 0\n\t" "v_add_f32 v90, v90, v90\n\t" \
   "s_branch 75b\n\t" "63:\n\t" "s_cmp_lg_u32 s64, 0\n\t" "s_cbranch_scc1 64f\n\t" "s_sub_u32 s69, s56, 0x10000\n\t" "s_and_b32 s69, s69, -2\n\t" \
   "s_branch 50b\n\t" "64:\n\t" "s_mov_b32 s69, s48\n\t" "s_cmp_lt_u32 s64, 3\n\t" "s_cselect_b32 s69, -1, s69\n\t" "s_cmp_lg_u32 s69, -1\n\t" \
   "s_cbranch_scc1 50b\n\t" "s_add_u32 s76, s76, 0x1000000\n\t" "s_mov_b32 s73, 0\n\t" "65:\n\t" "s_add_u32 s73, s73, 1\n\t" \
   "s_min_u32 s72, s73, 7\n\t" "s_mul_i32 s72, s72, 5\n\t" "s_add_u32 s72, s72, %[xs]\n\t" "s_and_b32 s72, s72, 7\n\t" "s_lshl_b32 s71, %[xs], 7\n\t" \
   "v_subrev_u32 v109, s71, v139\n\t" "s_lshl_b32 s72, s72, 7\n\t" "s_add_u32 s72, s72, s66\n\t" "s_cmp_lt_u32 s73, 7\n\t" "s_cbranch_scc1 66f\n\t" \
   "s_sleep 4\n\t" "66:\n\t" "buffer_load_dword v108, v109, %[rs], s72 offen sc1\n\t" "s_waitcnt vmcnt(0)\n\t" "v_readfirstlane_b32 s69, v108\n\t" \
   "s_cmp_gt_u32 s73, 0x10000\n\t" "s_cbranch_scc1 50b\n\t" "s_cmp_eq_u32 s69, -1\n\t" "s_cbranch_scc1 65b\n\t" "s_branch 50b\n\t" "8:\n\t" \
   "s_nop 7\n\t" "s_waitcnt vmcnt(6) lgkmcnt(0)\n\t" "s_add_u32 s70, s70, 1\n\t" "s_cmp_gt_u32 s70, 2\n\t" "s_cbranch_scc1 81f\n\t" \
   "s_lshl_b32 s71, s70, 3\n\t" "s_lshl_b32 s71, 1, s71\n\t" "s_add_u32 s76, s76, s71\n\t" "81:\n\t" "v_mov_b32 v48, 0\n\t" "v_mov_b32 v49, 0\n\t" \
   "v_mov_b32 v50, 0\n\t" "v_mov_b32 v51, 0\n\t" "v_mov_b32 v52, 0\n\t" "v_mov_b32 v53, 0\n\t" "v_mov_b32 v54, 0\n\t" "v_mov_b32 v55, 0\n\t" \
   "v_mov_b32 v56, 0\n\t" "v_mov_b32 v57, 0\n\t" "v_mov_b32 v58, 0\n\t" "v_mov_b32 v59, 0\n\t" "v_mov_b32 v60, 0\n\t" "v_mov_b32 v61, 0\n\t" \
   "v_mov_b32 v62, 0\n\t" "v_mov_b32 v63, 0\n\t" "s_min_u32 s71, s70, 7\n\t" "s_lshr_b32 s67, s71, 1\n\t" "s_add_u32 s67, s67, %[xs]\n\t" \
   "s_and_b32 s67, s67, 3\n\t" "s_lshl_b32 s67, s67, 11\n\t" "s_and_b32 s72, s71, 1\n\t" "s_lshl_b32 s72, s72, 14\n\t" "s_add_u32 s67, s67, s72\n\t" \
   "s_add_u32 s67, s67, s66\n\t" "s_cmp_eq_u32 s70, 1\n\t" "s_cselect_b32 s67, s68, s67\n\t" "s_cmp_lt_u32 s70, 7\n\t" "s_cbranch_scc1 20b\n\t" \
   "s_sleep 3\n\t" "s_cmp_lt_u32 s70, 0x20000\n\t" "s_cbranch_scc1 20b\n\t" "s_mov_b32 s67, %[esc]\n\t" "s_mov_b32 s68, %[esc]\n\t" "s_branch 20b\n\t" \
   "27:\n\t" "v_mov_b32 v98, 0x7ffffff0\n\t" "buffer_store_dword v98, v98, %[rs], 0 offen\n\t" "buffer_store_dword v98, v98, %[rs], 0 offen\n\t" \
   "buffer_store_dword v98, v98, %[rs], 0 offen\n\t" "buffer_store_dword v98, v98, %[rs], 0 offen\n\t" \
   "buffer_store_dword v98, v98, %[rs], 0 offen\n\t" "buffer_store_dword v98, v98, %[rs], 0 offen\n\t" "s_branch 28b\n\t" "141:\n\t" \
   "s_mov_b32 s74, 0\n\t" "s_cmp_eq_u32 s64, 0\n\t" "s_cbranch_scc1 41b\n\t" "42:\n\t" "buffer_load_dword v15, v140, %[rs], s59 offen sc1\n\t" \
   "s_add_u32 s74, s74, 1\n\t" "s_waitcnt vmcnt(0)\n\t" "v_cmp_eq_u32 vcc, -1, v15\n\t" "s_cmp_gt_u32 s74, 0x10000\n\t" "s_cbranch_scc1 41b\n\t" \
   "s_cbranch_vccz 41b\n\t" "s_sleep 2\n\t" "s_branch 42b\n\t" "95:\n\t" "s_movk_i32 s59, 0x1000\n\t" "s_branch 197f\n\t" "97:\n\t" \
   "buffer_load_dwordx4 v[16:19], v138, %[rs], s79 offen offset:0 sc1\n\t" "buffer_load_dwordx4 v[20:23], v138, %[rs], s79 offen offset:1024 sc1\n\t" \
   "buffer_load_dwordx4 v[24:27], v138, %[rs], s79 offen offset:2048 sc1\n\t" \
   "buffer_load_dwordx4 v[28:31], v138, %[rs], s79 offen offset:3072 sc1\n\t" "v_or_b32 v108, s49, v141\n\t" "v_or_b32 v109, s49, v142\n\t" \
   "buffer_store_dwordx4 v[44:47], v108, %[rs], s52 offen sc1\n\t" "buffer_store_dwordx2 v[32:33], v109, %[rs], s51 offen sc1\n\t" \
   "s_waitcnt vmcnt(0)\n\t" "s_branch 96b\n\t" "197:\n\t" "s_mov_b32 s74, 0\n\t" "98:\n\t" "buffer_load_dword v15, v140, %[rs], s59 offen sc1\n\t" \
   "s_add_u32 s74, s74, 1\n\t" "s_waitcnt vmcnt(0)\n\t" "v_cmp_eq_u32 vcc, -1, v15\n\t" "s_cmp_gt_u32 s74, 0x10000\n\t" "s_cbranch_scc1 97b\n\t" \
   "s_cbranch_vccz 97b\n\t" "s_sleep 2\n\t" "s_branch 98b\n\t" "91:\n\t" "s_waitcnt vmcnt(0)\n\t" "s_cmp_lg_u32 s64, 0\n\t" "s_cbranch_scc1 40b\n\t" \
   "s_mov_b32 s59, 0\n\t" "s_branch 145f\n\t" "45:\n\t" "v_mbcnt_lo_u32_b32 v98, -1, 0\n\t" "v_mbcnt_hi_u32_b32 v98, -1, v98\n\t" \
   "v_lshlrev_b32 v98, 4, v98\n\t" "s_add_u32 s72, %[kf0], 4096\n\t" "buffer_load_dwordx4 v[160:163], v98, %[rs], %[kf0] offen offset:0 sc1\n\t" \
   "buffer_load_dwordx4 v[164:167], v98, %[rs], %[kf0] offen offset:1024 sc1\n\t" \
   "buffer_load_dwordx4 v[168:171], v98, %[rs], %[kf0] offen offset:2048 sc1\n\t" \
   "buffer_load_dwordx4 v[172:175], v98, %[rs], %[kf0] offen offset:3072 sc1\n\t" \
   "buffer_load_dwordx4 v[176:179], v98, %[rs], s72 offen offset:0 sc1\n\t" "buffer_load_dwordx4 v[180:183], v98, %[rs], s72 offen offset:1024 sc1\n\t" \
   "buffer_load_dwordx4 v[184:187], v98, %[rs], s72 offen offset:2048 sc1\n\t" \
   "buffer_load_dwordx4 v[188:191], v98, %[rs], s72 offen offset:3072 sc1\n\t" "s_waitcnt vmcnt(0)\n\t" \
   "v_mfma_f32_16x16x32_bf16 v[48:51], v[160:163], v[64:67], 0\n\t" "v_mfma_f32_16x16x32_bf16 v[52:55], v[176:179], v[64:67], 0\n\t" \
   "v_mfma_f32_16x16x32_bf16 v[48:51], v[164:167], v[68:71], v[48:51]\n\t" "v_mfma_f32_16x16x32_bf16 v[52:55], v[180:183], v[68:71], v[52:55]\n\t" \
   "v_mfma_f32_16x16x32_bf16 v[48:51], v[168:171], v[72:75], v[48:51]\n\t" "v_mfma_f32_16x16x32_bf16 v[52:55], v[184:187], v[72:75], v[52:55]\n\t" \
   "v_mfma_f32_16x16x32_bf16 v[48:51], v[172:175], v[104:107], v[48:51]\n\t" "v_mfma_f32_16x16x32_bf16 v[52:55], v[188:191], v[104:107], v[52:55]\n\t" \
   "s_nop 7\n\t" "s_branch 93b\n\t" "145:\n\t" "s_mov_b32 s74, 0\n\t" "46:\n\t" "buffer_load_dword v15, v140, %[rs], s59 offen sc1\n\t" \
   "s_add_u32 s74, s74, 1\n\t" "s_waitcnt vmcnt(0)\n\t" "v_cmp_eq_u32 vcc, -1, v15\n\t" "s_cmp_gt_u32 s74, 0x10000\n\t" "s_cbranch_scc1 45b\n\t" \
   "s_cbranch_vccz 45b\n\t" "s_sleep 2\n\t" "s_branch 46b\n\t" "90:\n\t" "v_or_b32 v108, s49, v141\n\t" "v_or_b32 v109, s49, v142\n\t" \
   "buffer_store_dwordx4 v[44:47], v108, %[rs], s52 offen sc1\n\t" "buffer_store_dwordx2 v[32:33], v109, %[rs], s51 offen sc1\n\t" \
   "s_waitcnt vmcnt(0) lgkmcnt(0)"
#define P1_CLOB "v15", "v16", "v17", "v18", "v19", "v20", "v21", "v22", "v23", "v24", "v25", "v26", "v27", "v28", "v29", "v30", "v31", "v32", "v33", "v34", "v35", "v36", "v37", "v38", "v39", "v40", "v41", "v42", "v43", "v44", "v45", "v46", "v47", "v48", "v49", "v50", "v51", "v52", "v53", "v54", "v55", "v56", "v57", "v58", "v59", "v60", "v61", "v62", "v63", "v64", "v65", "v66", "v67", "v68", "v69", "v70", "v71", "v72", "v73", "v74", "v75", "v76", "v77", "v78", "v79", "v80", "v81", "v82", "v83", "v84", "v85", "v86", "v87", "v88", "v89", "v90", "v91", "v92", "v93", "v94", "v95", "v96", "v97", "v98", "v99", "v100", "v101", "v102", "v103", "v104", "v105", "v106", "v107", "v108", "v109", "v110", "v111", "v144", "v145", "v146", "v147", "v148", "v149", "v150", "v151", "v152", "v153", "v154", "v155", "v156", "v157", "v158", "v159", "v160", "v161", "v162", "v163", "v164", "v165", "v166", "v167", "v168", "v169", "v170", "v171", "v172", "v173", "v174", "v175", "v176", "v177", "v178", "v179", "v180", "v181", "v182", "v183", "v184", "v185", "v186", "v187", "v188", "v189", "v190", "v191", "s60", "s61", "s62", "s63", "s64", "s65", "s66", "s67", "s68", "s69", "s70", "s71", "s72", "s73", "s74", "s75", "s47", "s48", "s49", "s50", "s51", "s52", "s53", "s54", "s55", "s79", "s20", "s21", "s22", "s23", "s24", "s25", "s26", "s27", "s28", "s29", "s30", "s31", "s32", "s33", "s34", "s35"
#define PV_ASM \
   ".p2align 6\n\t" "s_nop 0\n\t" "s_nop 0\n\t" "s_nop 0\n\t" "s_nop 0\n\t" "s_nop 0\n\t" "s_nop 0\n\t" "s_nop 0\n\t" "s_nop 0\n\t" \
   "s_mov_b32 s64, -1\n\t" "s_mov_b32 s76, 0\n\t" "s_mov_b32 s77, 0\n\t" "s_mov_b32 s71, 63\n\t" "s_mov_b32 s47, 1300\n\t" "s_mov_b32 s70, 0\n\t" \
   "s_mov_b32 s69, 0\n\t" "s_lshr_b64 s[72:73], %[sel], s71\n\t" "s_lshl_b32 s65, s71, 19\n\t" "s_and_b32 s72, s72, 1\n\t" \
   "s_add_u32 s65, s65, %[pool]\n\t" "s_lshl_b32 s72, s72, 13\n\t" "s_add_u32 s65, s65, s72\n\t" "s_lshr_b64 s[72:73], %[s3], s71\n\t" \
   "s_and_b32 s54, s72, 1\n\t" "s_lshl_b32 s54, s54, 14\n\t" "40:\n\t" "s_cmp_lt_i32 s64, 0\n\t" "s_cselect_b32 s72, 0x7ffffff0, 0\n\t" \
   "s_max_i32 s71, s64, 0\n\t" "v_or_b32 v109, s72, v143\n\t" "s_lshl_b32 s71, s71, 12\n\t" "v_mov_b32 v110, 1\n\t" "s_cmp_eq_u32 s64, 63\n\t" \
   "s_cbranch_scc1 70f\n\t" "71:\n\t" "s_waitcnt vmcnt(0)\n\t" RSTAMP(3) "s_barrier\n\t" "buffer_load_dword v108, v139, %[rs], s65 offen sc1\n\t" \
   "buffer_store_dword v110, v109, %[rs], s71 offen sc1\n\t" PVTRACE "s_add_u32 s48, s69, s47\n\t" "s_cmp_eq_u32 s70, 0\n\t" \
   "s_cselect_b32 s48, s48, -1\n\t" "s_mov_b32 s66, s65\n\t" "s_add_i32 s64, s64, 1\n\t" "s_and_b32 s71, s64, 63\n\t" \
   "s_lshr_b64 s[72:73], %[sel], s71\n\t" "s_lshl_b32 s65, s71, 19\n\t" "s_and_b32 s72, s72, 1\n\t" "s_add_u32 s65, s65, %[pool]\n\t" \
   "s_lshl_b32 s72, s72, 13\n\t" "s_add_u32 s65, s65, s72\n\t" "s_xor_b32 s67, s54, %[lad0]\n\t" "s_xor_b32 s68, s67, 0x4000\n\t" \
   "s_add_u32 s67, s67, s66\n\t" "s_add_u32 s68, s68, s66\n\t" "s_lshr_b64 s[72:73], %[s3], s71\n\t" "s_and_b32 s54, s72, 1\n\t" \
   "s_lshl_b32 s54, s54, 14\n\t" "s_mov_b32 s70, 0\n\t" "s_lshr_b64 s[72:73], %[ysel], s71\n\t" "s_mul_i32 s73, s71, 43\n\t" \
   "s_mul_i32 s49, s71, 80\n\t" "s_and_b32 s72, s72, 1\n\t" "s_lshr_b32 s73, s73, 7\n\t" "s_add_u32 s49, s49, s72\n\t" \
   "s_lshl4_add_u32 s49, s73, s49\n\t" "s_lshl_b32 s49, s49, 13\n\t" "s_add_u32 s49, s49, %[yb]\n\t" "s_add_u32 s50, s49, 32768\n\t" \
   "s_add_u32 s51, s49, 65536\n\t" "s_add_u32 s52, s49, 98304\n\t" "s_add_u32 s53, s49, 131072\n\t" "v_mov_b32 v48, 0\n\t" "v_mov_b32 v49, 0\n\t" \
   "v_mov_b32 v50, 0\n\t" "v_mov_b32 v51, 0\n\t" "v_mov_b32 v52, 0\n\t" "v_mov_b32 v53, 0\n\t" "v_mov_b32 v54, 0\n\t" "v_mov_b32 v55, 0\n\t" \
   "v_mov_b32 v144, 0\n\t" "v_mov_b32 v145, 0\n\t" "v_mov_b32 v146, 0\n\t" "v_mov_b32 v147, 0\n\t" "v_mov_b32 v148, 0\n\t" "v_mov_b32 v149, 0\n\t" \
   "v_mov_b32 v150, 0\n\t" "v_mov_b32 v151, 0\n\t" "v_mov_b32 v152, 0\n\t" "v_mov_b32 v153, 0\n\t" "v_mov_b32 v154, 0\n\t" "v_mov_b32 v155, 0\n\t" \
   "v_mov_b32 v156, 0\n\t" "v_mov_b32 v157, 0\n\t" "v_mov_b32 v158, 0\n\t" "v_mov_b32 v159, 0\n\t" "v_mov_b32 v160, 0\n\t" "v_mov_b32 v161, 0\n\t" \
   "v_mov_b32 v162, 0\n\t" "v_mov_b32 v163, 0\n\t" "s_cmp_eq_u32 s64, 64\n\t" "s_cbranch_scc1 90f\n\t" "s_waitcnt vmcnt(1)\n\t" \
   "v_readlane_b32 s69, v108, 0\n\t" "v_readlane_b32 s71, v108, 1\n\t" "s_cmp_eq_u32 s69, -1\n\t" "s_cbranch_scc1 63f\n\t" "s_cmp_eq_u32 s71, -1\n\t" \
   "s_cselect_b32 s47, s47, s71\n\t" "50:\n\t" "s_add_u32 s73, s69, %[m1c]\n\t" RSTAMP(5) "51:\n\t" "s_memrealtime s[60:61]\n\t" \
   "s_memtime s[62:63]\n\t" "s_waitcnt lgkmcnt(0)\n\t" "s_sub_u32 s74, s62, s57\n\t" "s_mul_i32 s74, s74, s58\n\t" "s_lshr_b32 s74, s74, 12\n\t" \
   "s_add_u32 s74, s74, s56\n\t" "s_lshl_b32 s60, s60, 3\n\t" "s_sub_u32 s75, s74, s60\n\t" "s_cmp_lt_u32 s75, 32\n\t" "s_cbranch_scc1 52f\n\t" \
   "s_cmp_lt_i32 s75, 0\n\t" "s_cselect_b32 s61, 0, 31\n\t" "s_add_u32 s74, s60, s61\n\t" "s_sub_i32 s75, s61, s75\n\t" "s_add_i32 s75, s75, 1\n\t" \
   "s_ashr_i32 s75, s75, 1\n\t" "s_min_i32 s75, s75, 8\n\t" "s_max_i32 s75, s75, -8\n\t" "s_mov_b32 s56, s74\n\t" "s_mov_b32 s57, s62\n\t" \
   "s_add_u32 s58, s58, s75\n\t" "52:\n\t" "s_sub_i32 s74, s73, s74\n\t" "s_cmp_le_i32 s74, 150\n\t" "s_cbranch_scc1 53f\n\t" \
   "s_cmp_lt_i32 s74, 260\n\t" "s_cbranch_scc1 51b\n\t" "s_sleep 2\n\t" "s_cmp_lt_i32 s74, 32000\n\t" "s_cbranch_scc1 51b\n\t" "s_mov_b32 s74, 0\n\t" \
   "53:\n\t" "s_sub_i32 s74, s74, 56\n\t" "s_max_i32 s74, s74, 0\n\t" "s_mul_i32 s74, s74, 23\n\t" "s_lshr_b32 s74, s74, 5\n\t" \
   "s_and_b32 s74, s74, -4\n\t" "s_getpc_b64 s[60:61]\n\t" "54:\n\t" "s_sub_u32 s74, 55f-54b, s74\n\t" "s_add_u32 s60, s60, s74\n\t" \
   "s_addc_u32 s61, s61, 0\n\t" "s_setpc_b64 s[60:61]\n\t" "s_nop 3\n\t" "s_nop 3\n\t" "s_nop 3\n\t" "s_nop 3\n\t" "s_nop 3\n\t" "s_nop 3\n\t" \
   "s_nop 3\n\t" "s_nop 3\n\t" "s_nop 3\n\t" "s_nop 3\n\t" "s_nop 3\n\t" "s_nop 3\n\t" "s_nop 3\n\t" "s_nop 3\n\t" "s_nop 3\n\t" "s_nop 3\n\t" \
   "s_nop 3\n\t" "s_nop 3\n\t" "s_nop 3\n\t" "s_nop 3\n\t" "s_nop 3\n\t" "s_nop 3\n\t" "s_nop 3\n\t" "s_nop 3\n\t" "55:\n\t" "20:\n\t" RSTAMP(4) \
   "buffer_load_dwordx4 v[64:67], v128, %[rs], s67 offen sc1\n\t" "buffer_load_dwordx4 v[68:71], v129, %[rs], s67 offen sc1\n\t" \
   "buffer_load_dwordx4 v[72:75], v130, %[rs], s67 offen sc1\n\t" "s_waitcnt vmcnt(2)\n\t" "ds_write_b128 v112, v[64:67]\n\t" \
   "v_smfmac_f32_16x16x64_bf16 v[48:51], v[64:67], a[0:7], v134\n\t" "s_waitcnt vmcnt(1)\n\t" "ds_write_b128 v113, v[68:71]\n\t" \
   "s_waitcnt vmcnt(0)\n\t" "ds_write_b128 v114, v[72:75]\n\t" RSTAMP(7) "v_smfmac_f32_16x16x64_bf16 v[52:55], v[64:67], a[64:71], v134\n\t" \
   "v_max3_u32 v110, v64, v66, v68\n\t" "v_max3_u32 v111, v70, v72, v74\n\t" "v_smfmac_f32_16x16x64_bf16 v[48:51], v[68:71], a[8:15], v134\n\t" \
   "v_max_u32 v110, v110, v111\n\t" "v_cmp_eq_u32 vcc, -1, v110\n\t" "v_smfmac_f32_16x16x64_bf16 v[52:55], v[68:71], a[72:79], v134\n\t" \
   "s_cbranch_vccnz 8f\n\t" "s_waitcnt lgkmcnt(0)\n\t" "s_barrier\n\t" RSTAMP(0) "ds_read_b128 v[76:79], v115\n\t" "ds_read_b128 v[80:83], v116\n\t" \
   "ds_read_b128 v[84:87], v117\n\t" "v_smfmac_f32_16x16x64_bf16 v[48:51], v[72:75], a[16:23], v134\n\t" "ds_read_b128 v[88:91], v118\n\t" \
   "ds_read_b128 v[92:95], v119\n\t" "v_smfmac_f32_16x16x64_bf16 v[52:55], v[72:75], a[80:87], v134\n\t" "ds_read_b128 v[96:99], v120\n\t" \
   "ds_read_b128 v[100:103], v121\n\t" "s_waitcnt lgkmcnt(6)\n\t" "v_smfmac_f32_16x16x64_bf16 v[48:51], v[76:79], a[24:31], v134\n\t" \
   "v_smfmac_f32_16x16x64_bf16 v[52:55], v[76:79], a[88:95], v134\n\t" "s_waitcnt lgkmcnt(5)\n\t" \
   "v_smfmac_f32_16x16x64_bf16 v[48:51], v[80:83], a[32:39], v134\n\t" "v_smfmac_f32_16x16x64_bf16 v[52:55], v[80:83], a[96:103], v134\n\t" \
   "s_waitcnt lgkmcnt(4)\n\t" "v_smfmac_f32_16x16x64_bf16 v[48:51], v[84:87], a[40:47], v134\n\t" \
   "v_smfmac_f32_16x16x64_bf16 v[52:55], v[84:87], a[104:111], v134\n\t" "s_waitcnt lgkmcnt(3)\n\t" \
   "v_smfmac_f32_16x16x64_bf16 v[48:51], v[88:91], a[48:55], v134\n\t" "v_smfmac_f32_16x16x64_bf16 v[52:55], v[88:91], a[112:119], v134\n\t" \
   "s_waitcnt lgkmcnt(0)\n\t" "v_smfmac_f32_16x16x64_bf16 v[48:51], v[92:95], a[56:63], v134\n\t" \
   "v_smfmac_f32_16x16x64_bf16 v[48:51], v[96:99], v[192:199], v134\n\t" "v_smfmac_f32_16x16x64_bf16 v[48:51], v[100:103], v[200:207], v134\n\t" \
   "v_smfmac_f32_16x16x64_bf16 v[52:55], v[92:95], a[120:127], v134\n\t" "v_smfmac_f32_16x16x64_bf16 v[52:55], v[96:99], v[208:215], v134\n\t" \
   "v_smfmac_f32_16x16x64_bf16 v[52:55], v[100:103], v[216:223], v134\n\t" "v_add_f32 v48, v48, v49\n\t" "v_add_f32 v49, v50, v51\n\t" \
   "v_cvt_pk_bf16_f32 v48, v48, v49\n\t" "ds_write_b32 v135, v48 offset:10240\n\t" "s_nop 3\n\t" "v_add_f32 v52, v52, v53\n\t" \
   "v_add_f32 v53, v54, v55\n\t" "v_cvt_pk_bf16_f32 v52, v52, v53\n\t" "ds_write_b32 v135, v52 offset:10496\n\t" "s_waitcnt lgkmcnt(0)\n\t" \
   "s_barrier\n\t" RSTAMP(6) "ds_read_b64_tr_b16 v[16:17], v136 offset:10240\n\t" "ds_read_b64_tr_b16 v[18:19], v136 offset:10496\n\t" \
   "ds_read_b64_tr_b16 v[20:21], v136 offset:10752\n\t" "ds_read_b64_tr_b16 v[22:23], v136 offset:11008\n\t" \
   "ds_read_b64_tr_b16 v[24:25], v136 offset:11264\n\t" "ds_read_b64_tr_b16 v[26:27], v136 offset:11520\n\t" \
   "ds_read_b64_tr_b16 v[28:29], v136 offset:11776\n\t" "ds_read_b64_tr_b16 v[30:31], v136 offset:12032\n\t" "s_waitcnt lgkmcnt(6)\n\t" \
   "v_smfmac_f32_16x16x64_bf16 v[144:147], v[16:19], a[128:135], v131\n\t" "v_smfmac_f32_16x16x64_bf16 v[148:151], v[16:19], a[160:167], v131\n\t" \
   "v_smfmac_f32_16x16x64_bf16 v[152:155], v[16:19], a[192:199], v131\n\t" "v_smfmac_f32_16x16x64_bf16 v[156:159], v[16:19], a[224:231], v131\n\t" \
   "v_smfmac_f32_16x16x64_bf16 v[160:163], v[16:19], v[224:231], v131\n\t" "s_waitcnt lgkmcnt(0)\n\t" \
   "v_smfmac_f32_16x16x64_bf16 v[144:147], v[20:23], a[136:143], v131\n\t" "v_smfmac_f32_16x16x64_bf16 v[144:147], v[24:27], a[144:151], v131\n\t" \
   "v_smfmac_f32_16x16x64_bf16 v[144:147], v[28:31], a[152:159], v131\n\t" "v_smfmac_f32_16x16x64_bf16 v[148:151], v[20:23], a[168:175], v131\n\t" \
   "v_smfmac_f32_16x16x64_bf16 v[148:151], v[24:27], a[176:183], v131\n\t" "v_smfmac_f32_16x16x64_bf16 v[148:151], v[28:31], a[184:191], v131\n\t" \
   "v_smfmac_f32_16x16x64_bf16 v[152:155], v[20:23], a[200:207], v131\n\t" "v_cvt_pk_bf16_f32 v164, v144, v145\n\t" \
   "v_cvt_pk_bf16_f32 v165, v146, v147\n\t" "v_smfmac_f32_16x16x64_bf16 v[152:155], v[24:27], a[208:215], v131\n\t" "v_min_u32 v164, -2, v164\n\t" \
   "buffer_store_dwordx2 v[164:165], v132, %[rs], s49 offen sc1\n\t" "v_smfmac_f32_16x16x64_bf16 v[152:155], v[28:31], a[216:223], v131\n\t" \
   "v_smfmac_f32_16x16x64_bf16 v[156:159], v[20:23], a[232:239], v131\n\t" "v_cvt_pk_bf16_f32 v166, v148, v149\n\t" \
   "v_cvt_pk_bf16_f32 v167, v150, v151\n\t" "v_smfmac_f32_16x16x64_bf16 v[156:159], v[24:27], a[240:247], v131\n\t" "v_min_u32 v166, -2, v166\n\t" \
   "buffer_store_dwordx2 v[166:167], v132, %[rs], s50 offen sc1\n\t" "v_smfmac_f32_16x16x64_bf16 v[156:159], v[28:31], a[248:255], v131\n\t" \
   "v_smfmac_f32_16x16x64_bf16 v[160:163], v[20:23], v[232:239], v131\n\t" "v_cvt_pk_bf16_f32 v168, v152, v153\n\t" \
   "v_cvt_pk_bf16_f32 v169, v154, v155\n\t" "v_smfmac_f32_16x16x64_bf16 v[160:163], v[24:27], v[240:247], v131\n\t" "v_min_u32 v168, -2, v168\n\t" \
   "buffer_store_dwordx2 v[168:169], v132, %[rs], s51 offen sc1\n\t" "v_smfmac_f32_16x16x64_bf16 v[160:163], v[28:31], v[248:255], v131\n\t" \
   "v_cvt_pk_bf16_f32 v170, v156, v157\n\t" "v_cvt_pk_bf16_f32 v171, v158, v159\n\t" "v_min_u32 v170, -2, v170\n\t" \
   "buffer_store_dwordx2 v[170:171], v132, %[rs], s52 offen sc1\n\t" "s_nop 3\n\t" "v_cvt_pk_bf16_f32 v172, v160, v161\n\t" \
   "v_cvt_pk_bf16_f32 v173, v162, v163\n\t" "v_min_u32 v172, -2, v172\n\t" "buffer_store_dwordx2 v[172:173], v132, %[rs], s53 offen sc1\n\t" RSTAMP(2) \
   "s_branch 40b\n\t" "70:\n\t" "s_mov_b32 s46, 0\n\t" "s_lshr_b64 s[72:73], %[sel], s46\n\t" "s_lshl_b32 s45, s46, 19\n\t" "s_and_b32 s72, s72, 1\n\t" \
   "s_add_u32 s45, s45, %[pool]\n\t" "s_lshl_b32 s72, s72, 13\n\t" "s_add_u32 s45, s45, s72\n\t" "v_mov_b32 v76, -1\n\t" "v_mov_b32 v77, -1\n\t" \
   "v_mov_b32 v78, -1\n\t" "v_mov_b32 v79, -1\n\t" "buffer_store_dwordx4 v[76:79], v133, %[rs], s45 offen sc1\n\t" \
   "buffer_store_dwordx4 v[76:79], v133, %[rs], s45 offen offset:1024 sc1\n\t" "s_branch 71b\n\t" "63:\n\t" "s_cmp_lg_u32 s64, 0\n\t" \
   "s_cbranch_scc1 64f\n\t" "s_sub_u32 s69, s56, 0x10000\n\t" "s_and_b32 s69, s69, -2\n\t" "s_branch 50b\n\t" "64:\n\t" "s_mov_b32 s69, s48\n\t" \
   "s_cmp_lt_u32 s64, 3\n\t" "s_cselect_b32 s69, -1, s69\n\t" "s_cmp_lg_u32 s69, -1\n\t" "s_cbranch_scc1 50b\n\t" "s_add_u32 s76, s76, 0x1000000\n\t" \
   "s_mov_b32 s73, 0\n\t" "65:\n\t" "s_add_u32 s73, s73, 1\n\t" "s_sleep 6\n\t" "s_and_b32 s72, s73, 3\n\t" "s_add_u32 s72, s72, 8\n\t" \
   "s_sub_u32 s72, s72, %[xs]\n\t" "s_lshl_b32 s72, s72, 7\n\t" "s_add_u32 s72, s72, s66\n\t" "buffer_load_dword v108, v139, %[rs], s72 offen sc1\n\t" \
   "s_waitcnt vmcnt(0)\n\t" "v_readfirstlane_b32 s69, v108\n\t" "s_cmp_gt_u32 s73, 0x10000\n\t" "s_cbranch_scc1 50b\n\t" "s_cmp_eq_u32 s69, -1\n\t" \
   "s_cbranch_scc1 65b\n\t" "s_branch 50b\n\t" "8:\n\t" "s_nop 7\n\t" "s_waitcnt lgkmcnt(0)\n\t" "s_add_u32 s70, s70, 1\n\t" "s_cmp_gt_u32 s70, 2\n\t" \
   "s_cbranch_scc1 81f\n\t" "s_lshl_b32 s71, s70, 3\n\t" "s_lshl_b32 s71, 1, s71\n\t" "s_add_u32 s76, s76, s71\n\t" "81:\n\t" "v_mov_b32 v48, 0\n\t" \
   "v_mov_b32 v49, 0\n\t" "v_mov_b32 v50, 0\n\t" "v_mov_b32 v51, 0\n\t" "v_mov_b32 v52, 0\n\t" "v_mov_b32 v53, 0\n\t" "v_mov_b32 v54, 0\n\t" \
   "v_mov_b32 v55, 0\n\t" "s_add_u32 s71, s70, 1\n\t" "s_min_u32 s71, s71, 7\n\t" "s_lshr_b32 s67, s71, 1\n\t" "s_add_u32 s67, s67, %[xs]\n\t" \
   "s_and_b32 s67, s67, 3\n\t" "s_lshl_b32 s67, s67, 11\n\t" "s_and_b32 s72, s71, 1\n\t" "s_lshl_b32 s72, s72, 14\n\t" "s_add_u32 s67, s67, s72\n\t" \
   "s_add_u32 s67, s67, s66\n\t" "s_cmp_lt_u32 s70, 6\n\t" "s_cbranch_scc1 20b\n\t" "s_sleep 3\n\t" "s_cmp_lt_u32 s70, 0x20000\n\t" \
   "s_cbranch_scc1 20b\n\t" "s_mov_b32 s67, %[esc]\n\t" "s_branch 20b\n\t" "90:\n\t" "s_waitcnt vmcnt(0) lgkmcnt(0)"
#define PV_CLOB "v16", "v17", "v18", "v19", "v20", "v21", "v22", "v23", "v24", "v25", "v26", "v27", "v28", "v29", "v30", "v31", "v48", "v49", "v50", "v51", "v52", "v53", "v54", "v55", "v56", "v57", "v58", "v59", "v60", "v61", "v62", "v63", "v64", "v65", "v66", "v67", "v68", "v69", "v70", "v71", "v72", "v73", "v74", "v75", "v76", "v77", "v78", "v79", "v80", "v81", "v82", "v83", "v84", "v85", "v86", "v87", "v88", "v89", "v90", "v91", "v92", "v93", "v94", "v95", "v96", "v97", "v98", "v99", "v100", "v101", "v102", "v103", "v104", "v105", "v106", "v107", "v108", "v109", "v110", "v111", "v144", "v145", "v146", "v147", "v148", "v149", "v150", "v151", "v152", "v153", "v154", "v155", "v156", "v157", "v158", "v159", "v160", "v161", "v162", "v163", "v164", "v165", "v166", "v167", "v168", "v169", "v170", "v171", "v172", "v173", "v174", "v175", "s60", "s61", "s62", "s63", "s64", "s65", "s66", "s67", "s68", "s69", "s70", "s71", "s72", "s73", "s74", "s75", "s45", "s46", "s47", "s48", "s49", "s50", "s51", "s52", "s53", "s54"
#define WAIT_ASM \
   "11:\n\t" "s_memrealtime s[60:61]\n\t" "s_memtime s[62:63]\n\t" "s_waitcnt lgkmcnt(0)\n\t" "s_sub_u32 s74, s62, s57\n\t" \
   "s_mul_i32 s74, s74, s58\n\t" "s_lshr_b32 s74, s74, 12\n\t" "s_add_u32 s74, s74, s56\n\t" "s_lshl_b32 s60, s60, 3\n\t" "s_sub_u32 s75, s74, s60\n\t" \
   "s_cmp_lt_u32 s75, 32\n\t" "s_cbranch_scc1 12f\n\t" "s_cmp_lt_i32 s75, 0\n\t" "s_cselect_b32 s61, 0, 31\n\t" "s_add_u32 s74, s60, s61\n\t" \
   "s_sub_i32 s75, s61, s75\n\t" "s_add_i32 s75, s75, 1\n\t" "s_ashr_i32 s75, s75, 1\n\t" "s_min_i32 s75, s75, 8\n\t" "s_max_i32 s75, s75, -8\n\t" \
   "s_mov_b32 s56, s74\n\t" "s_mov_b32 s57, s62\n\t" "s_add_u32 s58, s58, s75\n\t" "12:\n\t" "s_sub_i32 s74, %[ta], s74\n\t" \
   "s_cmp_le_i32 s74, 150\n\t" "s_cbranch_scc1 13f\n\t" "s_cmp_lt_i32 s74, 260\n\t" "s_cbranch_scc1 11b\n\t" "s_sleep 2\n\t" \
   "s_cmp_lt_i32 s74, 32000\n\t" "s_cbranch_scc1 11b\n\t" "s_mov_b32 s74, 0\n\t" "13:\n\t" "s_sub_i32 s74, s74, 56\n\t" "s_max_i32 s74, s74, 0\n\t" \
   "s_mul_i32 s74, s74, 23\n\t" "s_lshr_b32 s74, s74, 5\n\t" "s_and_b32 s74, s74, -4\n\t" "s_getpc_b64 s[60:61]\n\t" "14:\n\t" \
   "s_sub_u32 s74, 15f-14b, s74\n\t" "s_add_u32 s60, s60, s74\n\t" "s_addc_u32 s61, s61, 0\n\t" "s_setpc_b64 s[60:61]\n\t" "s_nop 3\n\t" "s_nop 3\n\t" \
   "s_nop 3\n\t" "s_nop 3\n\t" "s_nop 3\n\t" "s_nop 3\n\t" "s_nop 3\n\t" "s_nop 3\n\t" "s_nop 3\n\t" "s_nop 3\n\t" "s_nop 3\n\t" "s_nop 3\n\t" \
   "s_nop 3\n\t" "s_nop 3\n\t" "s_nop 3\n\t" "s_nop 3\n\t" "s_nop 3\n\t" "s_nop 3\n\t" "s_nop 3\n\t" "s_nop 3\n\t" "s_nop 3\n\t" "s_nop 3\n\t" \
   "s_nop 3\n\t" "s_nop 3\n\t" "15:\n\t" ""
#define WAIT_CLOB "s60", "s61", "s62", "s63", "s74", "s75"
#define FIN_ASM \
   ".p2align 6\n\t" "s_nop 0\n\t" "s_nop 0\n\t" "s_nop 0\n\t" "s_nop 0\n\t" "s_nop 0\n\t" "s_nop 0\n\t" "s_nop 0\n\t" "s_nop 0\n\t" "s_nop 0\n\t" \
   "s_nop 0\n\t" "s_nop 0\n\t" "s_mov_b32 s40, 0\n\t" "s_mov_b32 s76, 0\n\t" "s_mov_b32 s77, 0\n\t" "s_sub_u32 s45, s56, 0x10000\n\t" \
   "v_mov_b32 v13, -2\n\t" "v_mov_b32 v11, -1\n\t" "30:\n\t" "s_lshr_b64 s[72:73], %[sel], s40\n\t" "s_lshl_b32 s41, s40, 19\n\t" \
   "s_and_b32 s72, s72, 1\n\t" "s_add_u32 s41, s41, %[pool]\n\t" "s_lshl_b32 s72, s72, 13\n\t" "s_add_u32 s41, s41, s72\n\t" \
   "s_lshr_b64 s[72:73], %[s3], s40\n\t" "s_and_b32 s47, s72, 1\n\t" "s_lshl_b32 s47, s47, 14\n\t" "s_mul_i32 s49, s40, 1310720\n\t" \
   "s_add_u32 s49, s49, %[su]\n\t" "s_lshr_b64 s[72:73], %[ysel], s40\n\t" "s_mul_i32 s73, s40, 43\n\t" "s_mul_i32 s50, s40, 80\n\t" \
   "s_and_b32 s72, s72, 1\n\t" "s_lshr_b32 s73, s73, 7\n\t" "s_add_u32 s50, s50, s72\n\t" "s_lshl4_add_u32 s50, s73, s50\n\t" \
   "s_lshl_b32 s50, s50, 13\n\t" "s_add_u32 s50, s50, %[yb]\n\t" "s_add_u32 s51, s50, 4096\n\t" "s_lshl_b32 s52, s40, 12\n\t" \
   "s_add_u32 s52, s52, %[uf]\n\t" "s_add_u32 s69, s45, %[tu]\n\t" "v_max3_u32 v15, v8, v9, v10\n\t" "v_max_u32 v15, v15, v11\n\t" \
   "v_cmp_eq_u32 vcc, -1, v15\n\t" "s_add_u32 s40, s40, 1\n\t" "s_cbranch_vccnz 95f\n\t" "96:\n\t" "11:\n\t" "s_memrealtime s[60:61]\n\t" \
   "s_memtime s[62:63]\n\t" "s_waitcnt lgkmcnt(0)\n\t" "s_sub_u32 s74, s62, s57\n\t" "s_mul_i32 s74, s74, s58\n\t" "s_lshr_b32 s74, s74, 12\n\t" \
   "s_add_u32 s74, s74, s56\n\t" "s_lshl_b32 s60, s60, 3\n\t" "s_sub_u32 s75, s74, s60\n\t" "s_cmp_lt_u32 s75, 32\n\t" "s_cbranch_scc1 12f\n\t" \
   "s_cmp_lt_i32 s75, 0\n\t" "s_cselect_b32 s61, 0, 31\n\t" "s_add_u32 s74, s60, s61\n\t" "s_sub_i32 s75, s61, s75\n\t" "s_add_i32 s75, s75, 1\n\t" \
   "s_ashr_i32 s75, s75, 1\n\t" "s_min_i32 s75, s75, 8\n\t" "s_max_i32 s75, s75, -8\n\t" "s_mov_b32 s56, s74\n\t" "s_mov_b32 s57, s62\n\t" \
   "s_add_u32 s58, s58, s75\n\t" "12:\n\t" "s_sub_i32 s74, s69, s74\n\t" "s_cmp_le_i32 s74, 150\n\t" "s_cbranch_scc1 13f\n\t" \
   "s_cmp_lt_i32 s74, 260\n\t" "s_cbranch_scc1 11b\n\t" "s_sleep 2\n\t" "s_cmp_lt_i32 s74, 32000\n\t" "s_cbranch_scc1 11b\n\t" "s_mov_b32 s74, 0\n\t" \
   "13:\n\t" "s_sub_i32 s74, s74, 56\n\t" "s_max_i32 s74, s74, 0\n\t" "s_mul_i32 s74, s74, 23\n\t" "s_lshr_b32 s74, s74, 5\n\t" \
   "s_and_b32 s74, s74, -4\n\t" "s_getpc_b64 s[60:61]\n\t" "14:\n\t" "s_sub_u32 s74, 15f-14b, s74\n\t" "s_add_u32 s60, s60, s74\n\t" \
   "s_addc_u32 s61, s61, 0\n\t" "s_setpc_b64 s[60:61]\n\t" "s_nop 3\n\t" "s_nop 3\n\t" "s_nop 3\n\t" "s_nop 3\n\t" "s_nop 3\n\t" "s_nop 3\n\t" \
   "s_nop 3\n\t" "s_nop 3\n\t" "s_nop 3\n\t" "s_nop 3\n\t" "s_nop 3\n\t" "s_nop 3\n\t" "s_nop 3\n\t" "s_nop 3\n\t" "s_nop 3\n\t" "s_nop 3\n\t" \
   "s_nop 3\n\t" "s_nop 3\n\t" "s_nop 3\n\t" "s_nop 3\n\t" "s_nop 3\n\t" "s_nop 3\n\t" "s_nop 3\n\t" "s_nop 3\n\t" "15:\n\t" RSTAMP(4) \
   "buffer_load_dwordx4 a[0:3], v16, %[rs], s49 offen offset:0 sc1\n\t" "s_nop 0\n\t" \
   "buffer_load_dwordx4 a[4:7], v16, %[rs], s49 offen offset:1024 sc1\n\t" "s_nop 0\n\t" \
   "buffer_load_dwordx4 a[8:11], v16, %[rs], s49 offen offset:2048 sc1\n\t" "s_nop 0\n\t" \
   "buffer_load_dwordx4 a[12:15], v16, %[rs], s49 offen offset:3072 sc1\n\t" "s_nop 0\n\t" "s_add_u32 s49, s49, 4096\n\t" \
   "buffer_load_dwordx4 a[16:19], v16, %[rs], s49 offen offset:0 sc1\n\t" "s_nop 0\n\t" \
   "buffer_load_dwordx4 a[20:23], v16, %[rs], s49 offen offset:1024 sc1\n\t" "s_nop 0\n\t" \
   "buffer_load_dwordx4 a[24:27], v16, %[rs], s49 offen offset:2048 sc1\n\t" "s_nop 0\n\t" \
   "buffer_load_dwordx4 a[28:31], v16, %[rs], s49 offen offset:3072 sc1\n\t" "s_nop 0\n\t" "s_add_u32 s49, s49, 4096\n\t" \
   "buffer_load_dwordx4 a[32:35], v16, %[rs], s49 offen offset:0 sc1\n\t" "s_nop 0\n\t" \
   "buffer_load_dwordx4 a[36:39], v16, %[rs], s49 offen offset:1024 sc1\n\t" "s_nop 0\n\t" \
   "buffer_load_dwordx4 a[40:43], v16, %[rs], s49 offen offset:2048 sc1\n\t" "s_nop 0\n\t" \
   "buffer_load_dwordx4 a[44:47], v16, %[rs], s49 offen offset:3072 sc1\n\t" "s_nop 0\n\t" "s_add_u32 s49, s49, 4096\n\t" \
   "buffer_load_dwordx4 a[48:51], v16, %[rs], s49 offen offset:0 sc1\n\t" "s_nop 0\n\t" \
   "buffer_load_dwordx4 a[52:55], v16, %[rs], s49 offen offset:1024 sc1\n\t" "s_nop 0\n\t" \
   "buffer_load_dwordx4 a[56:59], v16, %[rs], s49 offen offset:2048 sc1\n\t" "s_nop 0\n\t" \
   "buffer_load_dwordx4 a[60:63], v16, %[rs], s49 offen offset:3072 sc1\n\t" "s_nop 0\n\t" "s_add_u32 s49, s49, 4096\n\t" \
   "buffer_load_dwordx4 a[64:67], v16, %[rs], s49 offen offset:0 sc1\n\t" "s_nop 0\n\t" \
   "buffer_load_dwordx4 a[68:71], v16, %[rs], s49 offen offset:1024 sc1\n\t" "s_nop 0\n\t" \
   "buffer_load_dwordx4 a[72:75], v16, %[rs], s49 offen offset:2048 sc1\n\t" "s_nop 0\n\t" \
   "buffer_load_dwordx4 a[76:79], v16, %[rs], s49 offen offset:3072 sc1\n\t" "s_nop 0\n\t" "s_add_u32 s49, s49, 4096\n\t" \
   "buffer_load_dwordx4 a[80:83], v16, %[rs], s49 offen offset:0 sc1\n\t" "s_nop 0\n\t" \
   "buffer_load_dwordx4 a[84:87], v16, %[rs], s49 offen offset:1024 sc1\n\t" "s_nop 0\n\t" \
   "buffer_load_dwordx4 a[88:91], v16, %[rs], s49 offen offset:2048 sc1\n\t" "s_nop 0\n\t" \
   "buffer_load_dwordx4 a[92:95], v16, %[rs], s49 offen offset:3072 sc1\n\t" "s_nop 0\n\t" "s_add_u32 s49, s49, 4096\n\t" \
   "buffer_load_dwordx4 a[96:99], v16, %[rs], s49 offen offset:0 sc1\n\t" "s_nop 0\n\t" \
   "buffer_load_dwordx4 a[100:103], v16, %[rs], s49 offen offset:1024 sc1\n\t" "s_nop 0\n\t" \
   "buffer_load_dwordx4 a[104:107], v16, %[rs], s49 offen offset:2048 sc1\n\t" "s_nop 0\n\t" \
   "buffer_load_dwordx4 a[108:111], v16, %[rs], s49 offen offset:3072 sc1\n\t" "s_nop 0\n\t" "s_add_u32 s49, s49, 4096\n\t" \
   "buffer_load_dwordx4 a[112:115], v16, %[rs], s49 offen offset:0 sc1\n\t" "s_nop 0\n\t" \
   "buffer_load_dwordx4 a[116:119], v16, %[rs], s49 offen offset:1024 sc1\n\t" "s_nop 0\n\t" \
   "buffer_load_dwordx4 a[120:123], v16, %[rs], s49 offen offset:2048 sc1\n\t" "s_nop 0\n\t" \
   "buffer_load_dwordx4 a[124:127], v16, %[rs], s49 offen offset:3072 sc1\n\t" "s_nop 0\n\t" "s_add_u32 s52, s52, 4096\n\t" \
   "buffer_load_dwordx4 v[8:11], v19, %[rs], s52 offen sc1\n\t" RSTAMP(0) "s_mov_b32 s46, 0\n\t" "s_xor_b32 s64, s47, %[lad0]\n\t" \
   "s_and_b32 s65, s64, 0x4000\n\t" "s_xor_b32 s64, s64, s65\n\t" "v_or_b32 v224, s65, v16\n\t" "v_or_b32 v225, s65, v17\n\t" \
   "v_xor_b32 v226, 0x4000, v224\n\t" "v_xor_b32 v227, 0x4000, v225\n\t" "s_add_u32 s64, s64, s41\n\t" "v_xor_b32 v14, s47, v20\n\t" \
   "v_xor_b32 v15, 0x4000, v14\n\t" "s_add_u32 s65, s64, 32768\n\t" "s_add_u32 s66, s64, 65536\n\t" "s_add_u32 s67, s64, 98304\n\t" \
   "s_add_u32 s68, s64, 131072\n\t" "s_add_u32 s69, s64, 163840\n\t" "s_add_u32 s70, s64, 196608\n\t" "s_add_u32 s71, s64, 229376\n\t" \
   "s_add_u32 s72, s64, 262144\n\t" "s_add_u32 s73, s64, 294912\n\t" "s_add_u32 s53, s41, 4096\n\t" "v_mov_b32 v28, 0\n\t" "v_mov_b32 v29, 0\n\t" \
   "v_mov_b32 v30, 0\n\t" "v_mov_b32 v31, 0\n\t" "19:\n\t" "ds_read_b128 v[24:27], v23\n\t" "s_waitcnt lgkmcnt(0)\n\t" \
   "v_readfirstlane_b32 s74, v26\n\t" "v_readfirstlane_b32 s44, v24\n\t" "v_readfirstlane_b32 s45, v25\n\t" "s_sub_u32 s48, s44, 110\n\t" \
   "s_cmp_lt_u32 s74, s40\n\t" "s_cbranch_scc1 18f\n\t" RSTAMP(7) "21:\n\t" "s_memrealtime s[60:61]\n\t" "s_memtime s[62:63]\n\t" \
   "s_waitcnt lgkmcnt(0)\n\t" "s_sub_u32 s74, s62, s57\n\t" "s_mul_i32 s74, s74, s58\n\t" "s_lshr_b32 s74, s74, 12\n\t" "s_add_u32 s74, s74, s56\n\t" \
   "s_lshl_b32 s60, s60, 3\n\t" "s_sub_u32 s75, s74, s60\n\t" "s_cmp_lt_u32 s75, 32\n\t" "s_cbranch_scc1 22f\n\t" "s_cmp_lt_i32 s75, 0\n\t" \
   "s_cselect_b32 s61, 0, 31\n\t" "s_add_u32 s74, s60, s61\n\t" "s_sub_i32 s75, s61, s75\n\t" "s_add_i32 s75, s75, 1\n\t" "s_ashr_i32 s75, s75, 1\n\t" \
   "s_min_i32 s75, s75, 8\n\t" "s_max_i32 s75, s75, -8\n\t" "s_mov_b32 s56, s74\n\t" "s_mov_b32 s57, s62\n\t" "s_add_u32 s58, s58, s75\n\t" "22:\n\t" \
   "s_sub_i32 s74, s48, s74\n\t" "s_cmp_le_i32 s74, 150\n\t" "s_cbranch_scc1 23f\n\t" "s_cmp_lt_i32 s74, 260\n\t" "s_cbranch_scc1 21b\n\t" \
   "s_sleep 2\n\t" "s_cmp_lt_i32 s74, 32000\n\t" "s_cbranch_scc1 21b\n\t" "s_mov_b32 s74, 0\n\t" "23:\n\t" "s_sub_i32 s74, s74, 56\n\t" \
   "s_max_i32 s74, s74, 0\n\t" "s_mul_i32 s74, s74, 23\n\t" "s_lshr_b32 s74, s74, 5\n\t" "s_and_b32 s74, s74, -4\n\t" "s_getpc_b64 s[60:61]\n\t" \
   "24:\n\t" "s_sub_u32 s74, 25f-24b, s74\n\t" "s_add_u32 s60, s60, s74\n\t" "s_addc_u32 s61, s61, 0\n\t" "s_setpc_b64 s[60:61]\n\t" "s_nop 3\n\t" \
   "s_nop 3\n\t" "s_nop 3\n\t" "s_nop 3\n\t" "s_nop 3\n\t" "s_nop 3\n\t" "s_nop 3\n\t" "s_nop 3\n\t" "s_nop 3\n\t" "s_nop 3\n\t" "s_nop 3\n\t" \
   "s_nop 3\n\t" "s_nop 3\n\t" "s_nop 3\n\t" "s_nop 3\n\t" "s_nop 3\n\t" "s_nop 3\n\t" "s_nop 3\n\t" "s_nop 3\n\t" "s_nop 3\n\t" "s_nop 3\n\t" \
   "s_nop 3\n\t" "s_nop 3\n\t" "s_nop 3\n\t" "25:\n\t" "buffer_load_dwordx4 v[32:35], v16, %[rs], s50 offen offset:0 sc1\n\t" \
   "buffer_load_dwordx4 v[36:39], v16, %[rs], s50 offen offset:1024 sc1\n\t" "buffer_load_dwordx4 v[40:43], v16, %[rs], s50 offen offset:2048 sc1\n\t" \
   "buffer_load_dwordx4 v[44:47], v16, %[rs], s50 offen offset:3072 sc1\n\t" "buffer_load_dwordx4 v[48:51], v16, %[rs], s51 offen offset:0 sc1\n\t" \
   "buffer_load_dwordx4 v[52:55], v16, %[rs], s51 offen offset:1024 sc1\n\t" "buffer_load_dwordx4 v[56:59], v16, %[rs], s51 offen offset:2048 sc1\n\t" \
   "buffer_load_dwordx4 v[60:63], v16, %[rs], s51 offen offset:3072 sc1\n\t" "s_nop 3\n\t" "s_nop 3\n\t" "s_nop 3\n\t" "s_nop 3\n\t" "s_nop 3\n\t" \
   "s_nop 3\n\t" "s_nop 3\n\t" "s_nop 3\n\t" "s_nop 3\n\t" "s_nop 3\n\t" "s_nop 3\n\t" "s_nop 3\n\t" "s_nop 3\n\t" "s_nop 3\n\t" "s_nop 3\n\t" \
   "s_nop 3\n\t" RSTAMP(6) "buffer_load_dwordx4 v[64:67], v224, %[rs], s64 offen offset:0 sc1\n\t" \
   "buffer_load_dwordx4 v[68:71], v224, %[rs], s64 offen offset:1024 sc1\n\t" "buffer_load_dwordx4 v[72:75], v224, %[rs], s65 offen offset:0 sc1\n\t" \
   "buffer_load_dwordx4 v[76:79], v224, %[rs], s65 offen offset:1024 sc1\n\t" "buffer_load_dwordx4 v[80:83], v224, %[rs], s66 offen offset:0 sc1\n\t" \
   "buffer_load_dwordx4 v[84:87], v224, %[rs], s66 offen offset:1024 sc1\n\t" "buffer_load_dwordx4 v[88:91], v224, %[rs], s67 offen offset:0 sc1\n\t" \
   "buffer_load_dwordx4 v[92:95], v224, %[rs], s67 offen offset:1024 sc1\n\t" "buffer_load_dwordx4 v[96:99], v224, %[rs], s68 offen offset:0 sc1\n\t" \
   "buffer_load_dwordx4 v[100:103], v224, %[rs], s68 offen offset:1024 sc1\n\t" \
   "buffer_load_dwordx4 v[104:107], v224, %[rs], s69 offen offset:0 sc1\n\t" \
   "buffer_load_dwordx4 v[108:111], v224, %[rs], s69 offen offset:1024 sc1\n\t" \
   "buffer_load_dwordx4 v[112:115], v224, %[rs], s70 offen offset:0 sc1\n\t" \
   "buffer_load_dwordx4 v[116:119], v224, %[rs], s70 offen offset:1024 sc1\n\t" \
   "buffer_load_dwordx4 v[120:123], v224, %[rs], s71 offen offset:0 sc1\n\t" \
   "buffer_load_dwordx4 v[124:127], v224, %[rs], s71 offen offset:1024 sc1\n\t" \
   "buffer_load_dwordx2 v[128:129], v225, %[rs], s72 offen offset:0 sc1\n\t" \
   "buffer_load_dwordx2 v[130:131], v225, %[rs], s72 offen offset:128 sc1\n\t" \
   "buffer_load_dwordx2 v[132:133], v225, %[rs], s72 offen offset:1024 sc1\n\t" \
   "buffer_load_dwordx2 v[134:135], v225, %[rs], s72 offen offset:1152 sc1\n\t" \
   "buffer_load_dwordx2 v[136:137], v225, %[rs], s73 offen offset:0 sc1\n\t" \
   "buffer_load_dwordx2 v[138:139], v225, %[rs], s73 offen offset:128 sc1\n\t" \
   "buffer_load_dwordx2 v[140:141], v225, %[rs], s73 offen offset:1024 sc1\n\t" \
   "buffer_load_dwordx2 v[142:143], v225, %[rs], s73 offen offset:1152 sc1\n\t" YSTAMP(24) "s_waitcnt vmcnt(23)\n\t" RSTAMP(1) \
   "v_smfmac_f32_16x16x64_bf16 v[28:31], v[64:67], a[0:7], v22\n\t" "s_waitcnt vmcnt(22)\n\t" \
   "v_smfmac_f32_16x16x64_bf16 v[28:31], v[68:71], a[8:15], v22\n\t" "s_waitcnt vmcnt(21)\n\t" \
   "v_smfmac_f32_16x16x64_bf16 v[28:31], v[72:75], a[16:23], v22\n\t" "s_waitcnt vmcnt(20)\n\t" \
   "v_smfmac_f32_16x16x64_bf16 v[28:31], v[76:79], a[24:31], v22\n\t" "s_waitcnt vmcnt(19)\n\t" \
   "v_smfmac_f32_16x16x64_bf16 v[28:31], v[80:83], a[32:39], v22\n\t" "s_waitcnt vmcnt(18)\n\t" \
   "v_smfmac_f32_16x16x64_bf16 v[28:31], v[84:87], a[40:47], v22\n\t" "s_waitcnt vmcnt(17)\n\t" \
   "v_smfmac_f32_16x16x64_bf16 v[28:31], v[88:91], a[48:55], v22\n\t" "s_waitcnt vmcnt(16)\n\t" \
   "v_smfmac_f32_16x16x64_bf16 v[28:31], v[92:95], a[56:63], v22\n\t" "s_waitcnt vmcnt(15)\n\t" \
   "v_smfmac_f32_16x16x64_bf16 v[28:31], v[96:99], a[64:71], v22\n\t" "s_waitcnt vmcnt(14)\n\t" \
   "v_smfmac_f32_16x16x64_bf16 v[28:31], v[100:103], a[72:79], v22\n\t" "s_waitcnt vmcnt(13)\n\t" \
   "v_smfmac_f32_16x16x64_bf16 v[28:31], v[104:107], a[80:87], v22\n\t" "s_waitcnt vmcnt(12)\n\t" \
   "v_smfmac_f32_16x16x64_bf16 v[28:31], v[108:111], a[88:95], v22\n\t" "s_waitcnt vmcnt(11)\n\t" \
   "v_smfmac_f32_16x16x64_bf16 v[28:31], v[112:115], a[96:103], v22\n\t" "s_waitcnt vmcnt(10)\n\t" \
   "v_smfmac_f32_16x16x64_bf16 v[28:31], v[116:119], a[104:111], v22\n\t" "s_waitcnt vmcnt(9)\n\t" \
   "v_smfmac_f32_16x16x64_bf16 v[28:31], v[120:123], a[112:119], v22\n\t" "s_waitcnt vmcnt(8)\n\t" \
   "v_smfmac_f32_16x16x64_bf16 v[28:31], v[124:127], a[120:127], v22\n\t" "s_waitcnt vmcnt(6)\n\t" \
   "v_smfmac_f32_16x16x64_bf16 v[28:31], v[128:131], v[32:39], v22\n\t" "s_waitcnt vmcnt(4)\n\t" \
   "v_smfmac_f32_16x16x64_bf16 v[28:31], v[132:135], v[40:47], v22\n\t" "s_waitcnt vmcnt(2)\n\t" \
   "v_smfmac_f32_16x16x64_bf16 v[28:31], v[136:139], v[48:55], v22\n\t" "s_waitcnt vmcnt(0)\n\t" \
   "v_smfmac_f32_16x16x64_bf16 v[28:31], v[140:143], v[56:63], v22\n\t" "s_nop 7\n\t" "v_add_f32 v28, v28, v29\n\t" "v_add_f32 v30, v30, v31\n\t" \
   "v_cvt_pk_bf16_f32 v24, v28, v30\n\t" "v_cmp_u_f32 vcc, v28, v30\n\t" "s_nop 0\n\t" \
   "v_mov_b32_dpp v25, v24 quad_perm:[1,0,3,2] row_mask:0xf bank_mask:0xf\n\t" "s_cbranch_vccnz 8f\n\t" "2:\n\t" RSTAMP(2) \
   "v_perm_b32 v26, v25, v24, v18\n\t" "s_nop 1\n\t" "v_min_u32_dpp v24, v26, v13 quad_perm:[0,1,0,1] row_mask:0xf bank_mask:0xf\n\t" \
   "v_mov_b32_dpp v25, v26 quad_perm:[2,3,2,3] row_mask:0xf bank_mask:0xf\n\t" "buffer_store_dwordx2 v[24:25], v14, %[rs], s41 offen sc1\n\t" \
   "buffer_store_dwordx2 v[24:25], v14, %[rs], s53 offen sc1\n\t" "s_memtime s[62:63]\n\t" "s_memrealtime s[60:61]\n\t" \
   "buffer_store_dwordx2 v[24:25], v15, %[rs], s41 offen sc1\n\t" "buffer_store_dwordx2 v[24:25], v15, %[rs], s53 offen sc1\n\t" RSTAMP(3) \
   "s_waitcnt lgkmcnt(0)\n\t" "s_sub_u32 s74, s62, s57\n\t" "s_mul_i32 s74, s74, s58\n\t" "s_lshr_b32 s74, s74, 12\n\t" "s_add_u32 s74, s74, s56\n\t" \
   "s_lshl_b32 s60, s60, 3\n\t" "s_sub_u32 s75, s74, s60\n\t" "s_cmp_lt_u32 s75, 32\n\t" "s_cbranch_scc1 38f\n\t" "s_cmp_lt_i32 s75, 0\n\t" \
   "s_cselect_b32 s61, 0, 31\n\t" "s_add_u32 s74, s60, s61\n\t" "s_sub_i32 s75, s61, s75\n\t" "s_add_i32 s75, s75, 1\n\t" "s_ashr_i32 s75, s75, 1\n\t" \
   "s_min_i32 s75, s75, 8\n\t" "s_max_i32 s75, s75, -8\n\t" "s_mov_b32 s56, s74\n\t" "s_mov_b32 s57, s62\n\t" "s_add_u32 s58, s58, s75\n\t" "38:\n\t" \
   "s_min_u32 s75, s46, 1\n\t" "s_and_b32 s74, s74, -4\n\t" "s_or_b32 s74, s74, s75\n\t" "v_mov_b32 v26, s74\n\t" \
   "buffer_store_dword v26, v21, %[rs], s41 offen sc1\n\t" FINTRACE "v_xor_b32 v23, 32, v23\n\t" "s_cmp_lt_u32 s40, 63\n\t" "s_cbranch_scc1 30b\n\t" \
   "s_branch 90f\n\t" "18:\n\t" "s_sleep 2\n\t" "s_branch 19b\n\t" "8:\n\t" "s_mov_b32 s74, 0\n\t" "87:\n\t" "v_max3_u32 v12, v32, v34, v36\n\t" \
   "v_max3_u32 v12, v12, v38, v40\n\t" "v_max3_u32 v12, v12, v42, v44\n\t" "v_max3_u32 v12, v12, v46, v48\n\t" "v_max3_u32 v12, v12, v50, v52\n\t" \
   "v_max3_u32 v12, v12, v54, v56\n\t" "v_max3_u32 v12, v12, v58, v60\n\t" "v_max_u32 v12, v12, v62\n\t" "v_cmp_eq_u32 vcc, -1, v12\n\t" \
   "s_cmp_gt_u32 s74, 0x10000\n\t" "s_cbranch_scc1 86f\n\t" "s_cbranch_vccz 86f\n\t" "s_cmp_eq_u32 s74, 0\n\t" "s_cselect_b32 s75, 0x100, 0\n\t" \
   "s_cbranch_scc1 88f\n\t" "s_sleep 3\n\t" "88:\n\t" "buffer_load_dwordx4 v[32:35], v16, %[rs], s50 offen offset:0 sc1\n\t" \
   "buffer_load_dwordx4 v[36:39], v16, %[rs], s50 offen offset:1024 sc1\n\t" "buffer_load_dwordx4 v[40:43], v16, %[rs], s50 offen offset:2048 sc1\n\t" \
   "buffer_load_dwordx4 v[44:47], v16, %[rs], s50 offen offset:3072 sc1\n\t" "buffer_load_dwordx4 v[48:51], v16, %[rs], s51 offen offset:0 sc1\n\t" \
   "buffer_load_dwordx4 v[52:55], v16, %[rs], s51 offen offset:1024 sc1\n\t" "buffer_load_dwordx4 v[56:59], v16, %[rs], s51 offen offset:2048 sc1\n\t" \
   "buffer_load_dwordx4 v[60:63], v16, %[rs], s51 offen offset:3072 sc1\n\t" "s_add_u32 s76, s76, s75\n\t" "s_add_u32 s74, s74, 1\n\t" \
   "s_waitcnt vmcnt(0)\n\t" "s_branch 87b\n\t" "86:\n\t" "v_mov_b32 v27, 0\n\t" "v_mov_b32 v28, 0\n\t" "v_mov_b32 v29, 0\n\t" "v_mov_b32 v30, 0\n\t" \
   "v_mov_b32 v31, 0\n\t" "v_max3_u32 v27, v27, v64, v66\n\t" "v_max3_u32 v27, v27, v68, v70\n\t" "v_max3_u32 v27, v27, v72, v74\n\t" \
   "v_max3_u32 v27, v27, v76, v78\n\t" "v_max3_u32 v27, v27, v80, v82\n\t" "v_max3_u32 v27, v27, v84, v86\n\t" "v_max3_u32 v27, v27, v88, v90\n\t" \
   "v_max3_u32 v27, v27, v92, v94\n\t" "v_max3_u32 v27, v27, v96, v98\n\t" "v_max3_u32 v27, v27, v100, v102\n\t" "v_max3_u32 v27, v27, v104, v106\n\t" \
   "v_max3_u32 v27, v27, v108, v110\n\t" "v_max3_u32 v27, v27, v112, v114\n\t" "v_max3_u32 v27, v27, v116, v118\n\t" \
   "v_max3_u32 v27, v27, v120, v122\n\t" "v_max3_u32 v27, v27, v124, v126\n\t" "v_max3_u32 v27, v27, v128, v130\n\t" \
   "v_max3_u32 v27, v27, v132, v134\n\t" "v_max3_u32 v27, v27, v136, v138\n\t" "v_max3_u32 v27, v27, v140, v142\n\t" "v_cmp_eq_u32 vcc, -1, v27\n\t" \
   "s_nop 0\n\t" "s_cbranch_vccnz 85f\n\t" "v_smfmac_f32_16x16x64_bf16 v[28:31], v[64:67], a[0:7], v22\n\t" \
   "v_smfmac_f32_16x16x64_bf16 v[28:31], v[68:71], a[8:15], v22\n\t" "v_smfmac_f32_16x16x64_bf16 v[28:31], v[72:75], a[16:23], v22\n\t" \
   "v_smfmac_f32_16x16x64_bf16 v[28:31], v[76:79], a[24:31], v22\n\t" "v_smfmac_f32_16x16x64_bf16 v[28:31], v[80:83], a[32:39], v22\n\t" \
   "v_smfmac_f32_16x16x64_bf16 v[28:31], v[84:87], a[40:47], v22\n\t" "v_smfmac_f32_16x16x64_bf16 v[28:31], v[88:91], a[48:55], v22\n\t" \
   "v_smfmac_f32_16x16x64_bf16 v[28:31], v[92:95], a[56:63], v22\n\t" "v_smfmac_f32_16x16x64_bf16 v[28:31], v[96:99], a[64:71], v22\n\t" \
   "v_smfmac_f32_16x16x64_bf16 v[28:31], v[100:103], a[72:79], v22\n\t" "v_smfmac_f32_16x16x64_bf16 v[28:31], v[104:107], a[80:87], v22\n\t" \
   "v_smfmac_f32_16x16x64_bf16 v[28:31], v[108:111], a[88:95], v22\n\t" "v_smfmac_f32_16x16x64_bf16 v[28:31], v[112:115], a[96:103], v22\n\t" \
   "v_smfmac_f32_16x16x64_bf16 v[28:31], v[116:119], a[104:111], v22\n\t" "v_smfmac_f32_16x16x64_bf16 v[28:31], v[120:123], a[112:119], v22\n\t" \
   "v_smfmac_f32_16x16x64_bf16 v[28:31], v[124:127], a[120:127], v22\n\t" "v_smfmac_f32_16x16x64_bf16 v[28:31], v[128:131], v[32:39], v22\n\t" \
   "v_smfmac_f32_16x16x64_bf16 v[28:31], v[132:135], v[40:47], v22\n\t" "v_smfmac_f32_16x16x64_bf16 v[28:31], v[136:139], v[48:55], v22\n\t" \
   "v_smfmac_f32_16x16x64_bf16 v[28:31], v[140:143], v[56:63], v22\n\t" "84:\n\t" "v_cmp_eq_u32 vcc, -1, v27\n\t" "s_cmp_gt_u32 s46, 0x10000\n\t" \
   "s_cbranch_scc1 83f\n\t" "s_cbranch_vccz 83f\n\t" "85:\n\t" "s_add_u32 s46, s46, 1\n\t" "s_cmp_gt_u32 s46, 2\n\t" "s_cbranch_scc1 81f\n\t" \
   "s_lshl_b32 s74, s46, 3\n\t" "s_lshl_b32 s74, 1, s74\n\t" "s_add_u32 s77, s77, s74\n\t" "81:\n\t" "s_min_u32 s74, s46, 7\n\t" \
   "s_lshr_b32 s64, s74, 1\n\t" "s_add_u32 s64, s64, %[xs]\n\t" "s_and_b32 s64, s64, 3\n\t" "s_lshl_b32 s64, s64, 11\n\t" "s_and_b32 s75, s74, 1\n\t" \
   "s_lshl_b32 s75, s75, 14\n\t" "s_xor_b32 s75, s75, s47\n\t" "s_xor_b32 s75, s75, %[lad0]\n\t" "s_and_b32 s75, s75, 0x4000\n\t" \
   "s_add_u32 s64, s64, s75\n\t" "s_add_u32 s64, s64, s41\n\t" "s_add_u32 s65, s64, 32768\n\t" "s_add_u32 s66, s64, 65536\n\t" \
   "s_add_u32 s67, s64, 98304\n\t" "s_add_u32 s68, s64, 131072\n\t" "s_add_u32 s69, s64, 163840\n\t" "s_add_u32 s70, s64, 196608\n\t" \
   "s_add_u32 s71, s64, 229376\n\t" "s_add_u32 s72, s64, 262144\n\t" "s_add_u32 s73, s64, 294912\n\t" "v_mov_b32 v28, 0\n\t" "v_mov_b32 v29, 0\n\t" \
   "v_mov_b32 v30, 0\n\t" "v_mov_b32 v31, 0\n\t" "v_mov_b32 v27, 0\n\t" "s_cmp_lt_u32 s46, 7\n\t" "s_cbranch_scc1 82f\n\t" "s_sleep 3\n\t" "82:\n\t" \
   RSTAMP(6) "buffer_load_dwordx4 v[64:67], v16, %[rs], s64 offen offset:0 sc1\n\t" \
   "buffer_load_dwordx4 v[68:71], v16, %[rs], s64 offen offset:1024 sc1\n\t" "buffer_load_dwordx4 v[72:75], v16, %[rs], s65 offen offset:0 sc1\n\t" \
   "buffer_load_dwordx4 v[76:79], v16, %[rs], s65 offen offset:1024 sc1\n\t" "buffer_load_dwordx4 v[80:83], v16, %[rs], s66 offen offset:0 sc1\n\t" \
   "buffer_load_dwordx4 v[84:87], v16, %[rs], s66 offen offset:1024 sc1\n\t" "buffer_load_dwordx4 v[88:91], v16, %[rs], s67 offen offset:0 sc1\n\t" \
   "buffer_load_dwordx4 v[92:95], v16, %[rs], s67 offen offset:1024 sc1\n\t" "buffer_load_dwordx4 v[96:99], v16, %[rs], s68 offen offset:0 sc1\n\t" \
   "buffer_load_dwordx4 v[100:103], v16, %[rs], s68 offen offset:1024 sc1\n\t" "buffer_load_dwordx4 v[104:107], v16, %[rs], s69 offen offset:0 sc1\n\t" \
   "buffer_load_dwordx4 v[108:111], v16, %[rs], s69 offen offset:1024 sc1\n\t" "buffer_load_dwordx4 v[112:115], v16, %[rs], s70 offen offset:0 sc1\n\t" \
   "buffer_load_dwordx4 v[116:119], v16, %[rs], s70 offen offset:1024 sc1\n\t" "buffer_load_dwordx4 v[120:123], v16, %[rs], s71 offen offset:0 sc1\n\t" \
   "buffer_load_dwordx4 v[124:127], v16, %[rs], s71 offen offset:1024 sc1\n\t" "buffer_load_dwordx2 v[128:129], v17, %[rs], s72 offen offset:0 sc1\n\t" \
   "buffer_load_dwordx2 v[130:131], v17, %[rs], s72 offen offset:128 sc1\n\t" \
   "buffer_load_dwordx2 v[132:133], v17, %[rs], s72 offen offset:1024 sc1\n\t" \
   "buffer_load_dwordx2 v[134:135], v17, %[rs], s72 offen offset:1152 sc1\n\t" "buffer_load_dwordx2 v[136:137], v17, %[rs], s73 offen offset:0 sc1\n\t" \
   "buffer_load_dwordx2 v[138:139], v17, %[rs], s73 offen offset:128 sc1\n\t" \
   "buffer_load_dwordx2 v[140:141], v17, %[rs], s73 offen offset:1024 sc1\n\t" \
   "buffer_load_dwordx2 v[142:143], v17, %[rs], s73 offen offset:1152 sc1\n\t" "s_waitcnt vmcnt(23)\n\t" \
   "v_smfmac_f32_16x16x64_bf16 v[28:31], v[64:67], a[0:7], v22\n\t" "v_max3_u32 v27, v27, v64, v66\n\t" "s_waitcnt vmcnt(22)\n\t" \
   "v_smfmac_f32_16x16x64_bf16 v[28:31], v[68:71], a[8:15], v22\n\t" "v_max3_u32 v27, v27, v68, v70\n\t" "s_waitcnt vmcnt(21)\n\t" \
   "v_smfmac_f32_16x16x64_bf16 v[28:31], v[72:75], a[16:23], v22\n\t" "v_max3_u32 v27, v27, v72, v74\n\t" "s_waitcnt vmcnt(20)\n\t" \
   "v_smfmac_f32_16x16x64_bf16 v[28:31], v[76:79], a[24:31], v22\n\t" "v_max3_u32 v27, v27, v76, v78\n\t" "s_waitcnt vmcnt(19)\n\t" \
   "v_smfmac_f32_16x16x64_bf16 v[28:31], v[80:83], a[32:39], v22\n\t" "v_max3_u32 v27, v27, v80, v82\n\t" "s_waitcnt vmcnt(18)\n\t" \
   "v_smfmac_f32_16x16x64_bf16 v[28:31], v[84:87], a[40:47], v22\n\t" "v_max3_u32 v27, v27, v84, v86\n\t" "s_waitcnt vmcnt(17)\n\t" \
   "v_smfmac_f32_16x16x64_bf16 v[28:31], v[88:91], a[48:55], v22\n\t" "v_max3_u32 v27, v27, v88, v90\n\t" "s_waitcnt vmcnt(16)\n\t" \
   "v_smfmac_f32_16x16x64_bf16 v[28:31], v[92:95], a[56:63], v22\n\t" "v_max3_u32 v27, v27, v92, v94\n\t" "s_waitcnt vmcnt(15)\n\t" \
   "v_smfmac_f32_16x16x64_bf16 v[28:31], v[96:99], a[64:71], v22\n\t" "v_max3_u32 v27, v27, v96, v98\n\t" "s_waitcnt vmcnt(14)\n\t" \
   "v_smfmac_f32_16x16x64_bf16 v[28:31], v[100:103], a[72:79], v22\n\t" "v_max3_u32 v27, v27, v100, v102\n\t" "s_waitcnt vmcnt(13)\n\t" \
   "v_smfmac_f32_16x16x64_bf16 v[28:31], v[104:107], a[80:87], v22\n\t" "v_max3_u32 v27, v27, v104, v106\n\t" "s_waitcnt vmcnt(12)\n\t" \
   "v_smfmac_f32_16x16x64_bf16 v[28:31], v[108:111], a[88:95], v22\n\t" "v_max3_u32 v27, v27, v108, v110\n\t" "s_waitcnt vmcnt(11)\n\t" \
   "v_smfmac_f32_16x16x64_bf16 v[28:31], v[112:115], a[96:103], v22\n\t" "v_max3_u32 v27, v27, v112, v114\n\t" "s_waitcnt vmcnt(10)\n\t" \
   "v_smfmac_f32_16x16x64_bf16 v[28:31], v[116:119], a[104:111], v22\n\t" "v_max3_u32 v27, v27, v116, v118\n\t" "s_waitcnt vmcnt(9)\n\t" \
   "v_smfmac_f32_16x16x64_bf16 v[28:31], v[120:123], a[112:119], v22\n\t" "v_max3_u32 v27, v27, v120, v122\n\t" "s_waitcnt vmcnt(8)\n\t" \
   "v_smfmac_f32_16x16x64_bf16 v[28:31], v[124:127], a[120:127], v22\n\t" "v_max3_u32 v27, v27, v124, v126\n\t" "s_waitcnt vmcnt(6)\n\t" \
   "v_smfmac_f32_16x16x64_bf16 v[28:31], v[128:131], v[32:39], v22\n\t" "v_max3_u32 v27, v27, v128, v130\n\t" "s_waitcnt vmcnt(4)\n\t" \
   "v_smfmac_f32_16x16x64_bf16 v[28:31], v[132:135], v[40:47], v22\n\t" "v_max3_u32 v27, v27, v132, v134\n\t" "s_waitcnt vmcnt(2)\n\t" \
   "v_smfmac_f32_16x16x64_bf16 v[28:31], v[136:139], v[48:55], v22\n\t" "v_max3_u32 v27, v27, v136, v138\n\t" "s_waitcnt vmcnt(0)\n\t" \
   "v_smfmac_f32_16x16x64_bf16 v[28:31], v[140:143], v[56:63], v22\n\t" "v_max3_u32 v27, v27, v140, v142\n\t" "s_branch 84b\n\t" "83:\n\t" \
   "s_or_b32 s46, s46, 0x10000000\n\t" "s_nop 4\n\t" "v_add_f32 v28, v28, v29\n\t" "v_add_f32 v30, v30, v31\n\t" "v_cvt_pk_bf16_f32 v24, v28, v30\n\t" \
   "s_nop 1\n\t" "v_mov_b32_dpp v25, v24 quad_perm:[1,0,3,2] row_mask:0xf bank_mask:0xf\n\t" "s_branch 2b\n\t" "95:\n\t" "s_mov_b32 s74, 0\n\t" \
   "97:\n\t" "buffer_load_dwordx4 v[8:11], v19, %[rs], s52 offen sc1\n\t" "s_add_u32 s74, s74, 1\n\t" "s_waitcnt vmcnt(0)\n\t" \
   "v_max3_u32 v15, v8, v9, v10\n\t" "v_max_u32 v15, v15, v11\n\t" "v_cmp_eq_u32 vcc, -1, v15\n\t" "s_cmp_gt_u32 s74, 0x10000\n\t" \
   "s_cbranch_scc1 96b\n\t" "s_cbranch_vccz 96b\n\t" "s_sleep 2\n\t" "s_branch 97b\n\t" "90:\n\t" "s_waitcnt vmcnt(0) lgkmcnt(0)"
#define FIN_CLOB "v8", "v9", "v10", "v11", "v12", "v13", "v14", "v15", "v24", "v25", "v26", "v27", "v28", "v29", "v30", "v31", "v32", "v33", "v34", "v35", "v36", "v37", "v38", "v39", "v40", "v41", "v42", "v43", "v44", "v45", "v46", "v47", "v48", "v49", "v50", "v51", "v52", "v53", "v54", "v55", "v56", "v57", "v58", "v59", "v60", "v61", "v62", "v63", "v64", "v65", "v66", "v67", "v68", "v69", "v70", "v71", "v72", "v73", "v74", "v75", "v76", "v77", "v78", "v79", "v80", "v81", "v82", "v83", "v84", "v85", "v86", "v87", "v88", "v89", "v90", "v91", "v92", "v93", "v94", "v95", "v96", "v97", "v98", "v99", "v100", "v101", "v102", "v103", "v104", "v105", "v106", "v107", "v108", "v109", "v110", "v111", "v112", "v113", "v114", "v115", "v116", "v117", "v118", "v119", "v120", "v121", "v122", "v123", "v124", "v125", "v126", "v127", "v128", "v129", "v130", "v131", "v132", "v133", "v134", "v135", "v136", "v137", "v138", "v139", "v140", "v141", "v142", "v143", "v144", "v145", "v146", "v147", "v148", "v149", "v150", "v151", "v152", "v153", "v154", "v155", "v156", "v157", "v158", "v159", "v160", "v161", "v162", "v163", "v164", "v165", "v166", "v167", "v168", "v169", "v170", "v171", "v172", "v173", "v174", "v175", "v176", "v177", "v178", "v179", "v180", "v181", "v182", "v183", "v184", "v185", "v186", "v187", "v188", "v189", "v190", "v191", "v192", "v193", "v194", "v195", "v196", "v197", "v198", "v199", "v200", "v201", "v202", "v203", "v204", "v205", "v206", "v207", "v208", "v209", "v210", "v211", "v212", "v213", "v214", "v215", "v216", "v217", "v218", "v219", "v220", "v221", "v222", "v223", "v224", "v225", "v226", "v227", "s40", "s41", "s42", "s43", "s44", "s45", "s46", "s47", "s48", "s49", "s50", "s51", "s52", "s53", "s60", "s61", "s62", "s63", "s64", "s65", "s66", "s67", "s68", "s69", "s70", "s71", "s72", "s73", "s74", "s75", "a0", "a1", "a2", "a3", "a4", "a5", "a6", "a7", "a8", "a9", "a10", "a11", "a12", "a13", "a14", "a15", "a16", "a17", "a18", "a19", "a20", "a21", "a22", "a23", "a24", "a25", "a26", "a27", "a28", "a29", "a30", "a31", "a32", "a33", "a34", "a35", "a36", "a37", "a38", "a39", "a40", "a41", "a42", "a43", "a44", "a45", "a46", "a47", "a48", "a49", "a50", "a51", "a52", "a53", "a54", "a55", "a56", "a57", "a58", "a59", "a60", "a61", "a62", "a63", "a64", "a65", "a66", "a67", "a68", "a69", "a70", "a71", "a72", "a73", "a74", "a75", "a76", "a77", "a78", "a79", "a80", "a81", "a82", "a83", "a84", "a85", "a86", "a87", "a88", "a89", "a90", "a91", "a92", "a93", "a94", "a95", "a96", "a97", "a98", "a99", "a100", "a101", "a102", "a103", "a104", "a105", "a106", "a107", "a108", "a109", "a110", "a111", "a112", "a113", "a114", "a115", "a116", "a117", "a118", "a119", "a120", "a121", "a122", "a123", "a124", "a125", "a126", "a127"
__device__ __forceinline__ void put4(v32u& d, int i, v4u v) { d[4 * i] = v.x; d[4 * i + 1] = v.y; d[4 * i + 2] = v.z; d[4 * i + 3] = v.w; }
#define LDP_ASM \
   "buffer_load_dword v26, v113, %[rw], 0 offen sc1\n\t" "s_mov_b32 s41, 0\n\t" "buffer_load_dwordx4 a[0:3], v16, %[ra], s41 offen\n\t" \
   "s_mov_b32 s41, 131072\n\t" "buffer_load_dwordx4 a[64:67], v16, %[ra], s41 offen\n\t" "s_mov_b32 s41, 524288\n\t" \
   "buffer_load_dwordx4 a[8:11], v16, %[ra], s41 offen\n\t" "s_mov_b32 s41, 655360\n\t" "buffer_load_dwordx4 a[72:75], v16, %[ra], s41 offen\n\t" \
   "s_mov_b32 s41, 1048576\n\t" "buffer_load_dwordx4 a[16:19], v16, %[ra], s41 offen\n\t" "s_mov_b32 s41, 1179648\n\t" \
   "buffer_load_dwordx4 a[80:83], v16, %[ra], s41 offen\n\t" "s_mov_b32 s41, 1572864\n\t" "buffer_load_dwordx4 a[24:27], v16, %[ra], s41 offen\n\t" \
   "s_mov_b32 s41, 1703936\n\t" "buffer_load_dwordx4 a[88:91], v16, %[ra], s41 offen\n\t" "s_mov_b32 s41, 2097152\n\t" \
   "buffer_load_dwordx4 a[32:35], v16, %[ra], s41 offen\n\t" "s_mov_b32 s41, 2228224\n\t" "buffer_load_dwordx4 a[96:99], v16, %[ra], s41 offen\n\t" \
   "s_mov_b32 s41, 2621440\n\t" "buffer_load_dwordx4 a[40:43], v16, %[ra], s41 offen\n\t" "s_mov_b32 s41, 2752512\n\t" \
   "buffer_load_dwordx4 a[104:107], v16, %[ra], s41 offen\n\t" "s_mov_b32 s41, 3145728\n\t" "buffer_load_dwordx4 a[48:51], v16, %[ra], s41 offen\n\t" \
   "s_mov_b32 s41, 3276800\n\t" "buffer_load_dwordx4 a[112:115], v16, %[ra], s41 offen\n\t" "s_mov_b32 s41, 3670016\n\t" \
   "buffer_load_dwordx4 a[56:59], v16, %[ra], s41 offen\n\t" "s_mov_b32 s41, 3801088\n\t" "buffer_load_dwordx4 a[120:123], v16, %[ra], s41 offen\n\t" \
   "s_waitcnt vmcnt(16)\n\t" "v_add_u32 v26, 1, v26\n\t" "buffer_store_dword v26, v114, %[rw], 0 offen sc1\n\t" "ds_write_b32 v115, v26\n\t" LDSTAMP(0) \
   "s_waitcnt vmcnt(15)\n\t" LDSTAMP(1) "ds_write_b128 v20, a[0:3] offset:0\n\t" "s_mov_b32 s41, 4194304\n\t" \
   "buffer_load_dwordx4 v[192:195], v16, %[ra], s41 offen\n\t" "s_waitcnt vmcnt(15)\n\t" "ds_write_b128 v20, a[64:67] offset:4096\n\t" \
   "s_mov_b32 s41, 4325376\n\t" "buffer_load_dwordx4 v[208:211], v16, %[ra], s41 offen\n\t" "s_waitcnt vmcnt(15)\n\t" \
   "ds_write_b128 v20, a[8:11] offset:8192\n\t" "s_mov_b32 s41, 4718592\n\t" "buffer_load_dwordx4 v[200:203], v16, %[ra], s41 offen\n\t" \
   "s_waitcnt vmcnt(15)\n\t" "ds_write_b128 v20, a[72:75] offset:12288\n\t" "s_mov_b32 s41, 4849664\n\t" \
   "buffer_load_dwordx4 v[216:219], v16, %[ra], s41 offen\n\t" "s_waitcnt vmcnt(15)\n\t" "ds_write_b128 v20, a[16:19] offset:16384\n\t" \
   "s_mov_b32 s41, 0\n\t" "buffer_load_dwordx4 a[128:131], v16, %[rb], s41 offen\n\t" "s_waitcnt vmcnt(15)\n\t" \
   "ds_write_b128 v20, a[80:83] offset:20480\n\t" "s_mov_b32 s41, 131072\n\t" "buffer_load_dwordx4 a[192:195], v16, %[rb], s41 offen\n\t" \
   "s_waitcnt vmcnt(15)\n\t" "ds_write_b128 v20, a[24:27] offset:24576\n\t" "s_mov_b32 s41, 524288\n\t" \
   "buffer_load_dwordx4 a[136:139], v16, %[rb], s41 offen\n\t" "s_waitcnt vmcnt(15)\n\t" "ds_write_b128 v20, a[88:91] offset:28672\n\t" \
   "s_mov_b32 s41, 655360\n\t" "buffer_load_dwordx4 a[200:203], v16, %[rb], s41 offen\n\t" "s_waitcnt vmcnt(15)\n\t" \
   "ds_write_b128 v20, a[32:35] offset:32768\n\t" "s_mov_b32 s41, 1048576\n\t" "buffer_load_dwordx4 a[144:147], v16, %[rb], s41 offen\n\t" \
   "s_waitcnt vmcnt(15)\n\t" "ds_write_b128 v20, a[96:99] offset:36864\n\t" "s_mov_b32 s41, 1179648\n\t" \
   "buffer_load_dwordx4 a[208:211], v16, %[rb], s41 offen\n\t" "s_waitcnt vmcnt(15)\n\t" "ds_write_b128 v20, a[40:43] offset:40960\n\t" \
   "s_mov_b32 s41, 1572864\n\t" "buffer_load_dwordx4 a[152:155], v16, %[rb], s41 offen\n\t" "s_waitcnt vmcnt(15)\n\t" \
   "ds_write_b128 v20, a[104:107] offset:45056\n\t" "s_mov_b32 s41, 1703936\n\t" "buffer_load_dwordx4 a[216:219], v16, %[rb], s41 offen\n\t" \
   "s_waitcnt vmcnt(15)\n\t" "ds_write_b128 v20, a[48:51] offset:49152\n\t" "s_mov_b32 s41, 2097152\n\t" \
   "buffer_load_dwordx4 a[160:163], v16, %[rb], s41 offen\n\t" "s_waitcnt vmcnt(15)\n\t" "ds_write_b128 v20, a[112:115] offset:53248\n\t" \
   "s_mov_b32 s41, 2228224\n\t" "buffer_load_dwordx4 a[224:227], v16, %[rb], s41 offen\n\t" "s_waitcnt vmcnt(15)\n\t" \
   "ds_write_b128 v20, a[56:59] offset:57344\n\t" "s_mov_b32 s41, 2621440\n\t" "buffer_load_dwordx4 a[168:171], v16, %[rb], s41 offen\n\t" \
   "s_waitcnt vmcnt(15)\n\t" "ds_write_b128 v20, a[120:123] offset:61440\n\t" "s_mov_b32 s41, 2752512\n\t" \
   "buffer_load_dwordx4 a[232:235], v16, %[rb], s41 offen\n\t" "s_waitcnt vmcnt(15)\n\t" "ds_write_b128 v23, v[192:195] offset:0\n\t" \
   "s_mov_b32 s41, 3145728\n\t" "buffer_load_dwordx4 a[176:179], v16, %[rb], s41 offen\n\t" "s_waitcnt vmcnt(15)\n\t" \
   "ds_write_b128 v23, v[208:211] offset:4096\n\t" "s_mov_b32 s41, 3276800\n\t" "buffer_load_dwordx4 a[240:243], v16, %[rb], s41 offen\n\t" \
   "s_waitcnt vmcnt(15)\n\t" "ds_write_b128 v23, v[200:203] offset:8192\n\t" "s_mov_b32 s41, 3670016\n\t" \
   "buffer_load_dwordx4 a[184:187], v16, %[rb], s41 offen\n\t" "s_waitcnt vmcnt(15)\n\t" "ds_write_b128 v23, v[216:219] offset:12288\n\t" \
   "s_mov_b32 s41, 3801088\n\t" "buffer_load_dwordx4 a[248:251], v16, %[rb], s41 offen\n\t" "s_waitcnt lgkmcnt(0)\n\t" "s_barrier\n\t" LDSTAMP(2) \
   "s_lshl_b32 s40, %[p0], 13\n\t" "v_add_u32 v24, s40, v18\n\t" "v_add_u32 v25, s40, v19\n\t" "s_add_u32 s40, s40, 8192\n\t" \
   "s_cmp_eq_u32 s40, 81920\n\t" "s_cselect_b32 s40, 0, s40\n\t" "ds_read_b64_tr_b16 a[0:1], v24 offset:0\n\t" \
   "ds_read_b64_tr_b16 a[2:3], v24 offset:4096\n\t" "ds_read_b64_tr_b16 a[64:65], v25 offset:0\n\t" "ds_read_b64_tr_b16 a[66:67], v25 offset:4096\n\t" \
   "v_add_u32 v24, s40, v18\n\t" "v_add_u32 v25, s40, v19\n\t" "s_add_u32 s40, s40, 8192\n\t" "s_cmp_eq_u32 s40, 81920\n\t" \
   "s_cselect_b32 s40, 0, s40\n\t" "ds_read_b64_tr_b16 a[8:9], v24 offset:0\n\t" "ds_read_b64_tr_b16 a[10:11], v24 offset:4096\n\t" \
   "ds_read_b64_tr_b16 a[72:73], v25 offset:0\n\t" "ds_read_b64_tr_b16 a[74:75], v25 offset:4096\n\t" "v_add_u32 v24, s40, v18\n\t" \
   "v_add_u32 v25, s40, v19\n\t" "s_add_u32 s40, s40, 8192\n\t" "s_cmp_eq_u32 s40, 81920\n\t" "s_cselect_b32 s40, 0, s40\n\t" \
   "ds_read_b64_tr_b16 a[16:17], v24 offset:0\n\t" "ds_read_b64_tr_b16 a[18:19], v24 offset:4096\n\t" "ds_read_b64_tr_b16 a[80:81], v25 offset:0\n\t" \
   "ds_read_b64_tr_b16 a[82:83], v25 offset:4096\n\t" "v_add_u32 v24, s40, v18\n\t" "v_add_u32 v25, s40, v19\n\t" "s_add_u32 s40, s40, 8192\n\t" \
   "s_cmp_eq_u32 s40, 81920\n\t" "s_cselect_b32 s40, 0, s40\n\t" "ds_read_b64_tr_b16 a[24:25], v24 offset:0\n\t" \
   "ds_read_b64_tr_b16 a[26:27], v24 offset:4096\n\t" "ds_read_b64_tr_b16 a[88:89], v25 offset:0\n\t" \
   "ds_read_b64_tr_b16 a[90:91], v25 offset:4096\n\t" "v_add_u32 v24, s40, v18\n\t" "v_add_u32 v25, s40, v19\n\t" "s_add_u32 s40, s40, 8192\n\t" \
   "s_cmp_eq_u32 s40, 81920\n\t" "s_cselect_b32 s40, 0, s40\n\t" "ds_read_b64_tr_b16 a[32:33], v24 offset:0\n\t" \
   "ds_read_b64_tr_b16 a[34:35], v24 offset:4096\n\t" "ds_read_b64_tr_b16 a[96:97], v25 offset:0\n\t" \
   "ds_read_b64_tr_b16 a[98:99], v25 offset:4096\n\t" "v_add_u32 v24, s40, v18\n\t" "v_add_u32 v25, s40, v19\n\t" "s_add_u32 s40, s40, 8192\n\t" \
   "s_cmp_eq_u32 s40, 81920\n\t" "s_cselect_b32 s40, 0, s40\n\t" "ds_read_b64_tr_b16 a[40:41], v24 offset:0\n\t" \
   "ds_read_b64_tr_b16 a[42:43], v24 offset:4096\n\t" "ds_read_b64_tr_b16 a[104:105], v25 offset:0\n\t" \
   "ds_read_b64_tr_b16 a[106:107], v25 offset:4096\n\t" "v_add_u32 v24, s40, v18\n\t" "v_add_u32 v25, s40, v19\n\t" "s_add_u32 s40, s40, 8192\n\t" \
   "s_cmp_eq_u32 s40, 81920\n\t" "s_cselect_b32 s40, 0, s40\n\t" "ds_read_b64_tr_b16 a[48:49], v24 offset:0\n\t" \
   "ds_read_b64_tr_b16 a[50:51], v24 offset:4096\n\t" "ds_read_b64_tr_b16 a[112:113], v25 offset:0\n\t" \
   "ds_read_b64_tr_b16 a[114:115], v25 offset:4096\n\t" "v_add_u32 v24, s40, v18\n\t" "v_add_u32 v25, s40, v19\n\t" "s_add_u32 s40, s40, 8192\n\t" \
   "s_cmp_eq_u32 s40, 81920\n\t" "s_cselect_b32 s40, 0, s40\n\t" "ds_read_b64_tr_b16 a[56:57], v24 offset:0\n\t" \
   "ds_read_b64_tr_b16 a[58:59], v24 offset:4096\n\t" "ds_read_b64_tr_b16 a[120:121], v25 offset:0\n\t" \
   "ds_read_b64_tr_b16 a[122:123], v25 offset:4096\n\t" "v_add_u32 v24, s40, v18\n\t" "v_add_u32 v25, s40, v19\n\t" "s_add_u32 s40, s40, 8192\n\t" \
   "s_cmp_eq_u32 s40, 81920\n\t" "s_cselect_b32 s40, 0, s40\n\t" "ds_read_b64_tr_b16 v[192:193], v24 offset:0\n\t" \
   "ds_read_b64_tr_b16 v[194:195], v24 offset:4096\n\t" "ds_read_b64_tr_b16 v[208:209], v25 offset:0\n\t" \
   "ds_read_b64_tr_b16 v[210:211], v25 offset:4096\n\t" "v_add_u32 v24, s40, v18\n\t" "v_add_u32 v25, s40, v19\n\t" "s_add_u32 s40, s40, 8192\n\t" \
   "s_cmp_eq_u32 s40, 81920\n\t" "s_cselect_b32 s40, 0, s40\n\t" "ds_read_b64_tr_b16 v[200:201], v24 offset:0\n\t" \
   "ds_read_b64_tr_b16 v[202:203], v24 offset:4096\n\t" "ds_read_b64_tr_b16 v[216:217], v25 offset:0\n\t" \
   "ds_read_b64_tr_b16 v[218:219], v25 offset:4096\n\t" "s_waitcnt vmcnt(15)\n\t" "s_waitcnt lgkmcnt(0)\n\t" "s_barrier\n\t" \
   "ds_write_b128 v20, a[128:131] offset:0\n\t" "s_mov_b32 s41, 4194304\n\t" "buffer_load_dwordx4 v[224:227], v16, %[rb], s41 offen\n\t" \
   "s_waitcnt vmcnt(15)\n\t" "ds_write_b128 v20, a[192:195] offset:4096\n\t" "s_mov_b32 s41, 4325376\n\t" \
   "buffer_load_dwordx4 v[240:243], v16, %[rb], s41 offen\n\t" "s_waitcnt vmcnt(15)\n\t" "ds_write_b128 v20, a[136:139] offset:8192\n\t" \
   "s_mov_b32 s41, 4718592\n\t" "buffer_load_dwordx4 v[232:235], v16, %[rb], s41 offen\n\t" "s_waitcnt vmcnt(15)\n\t" \
   "ds_write_b128 v20, a[200:203] offset:12288\n\t" "s_mov_b32 s41, 4849664\n\t" "buffer_load_dwordx4 v[248:251], v16, %[rb], s41 offen\n\t" \
   "s_waitcnt vmcnt(15)\n\t" "ds_write_b128 v20, a[144:147] offset:16384\n\t" "s_mov_b32 s41, 262144\n\t" \
   "buffer_load_dwordx4 a[4:7], v16, %[ra], s41 offen\n\t" "s_waitcnt vmcnt(15)\n\t" "ds_write_b128 v20, a[208:211] offset:20480\n\t" \
   "s_mov_b32 s41, 393216\n\t" "buffer_load_dwordx4 a[68:71], v16, %[ra], s41 offen\n\t" "s_waitcnt vmcnt(15)\n\t" \
   "ds_write_b128 v20, a[152:155] offset:24576\n\t" "s_mov_b32 s41, 786432\n\t" "buffer_load_dwordx4 a[12:15], v16, %[ra], s41 offen\n\t" \
   "s_waitcnt vmcnt(15)\n\t" "ds_write_b128 v20, a[216:219] offset:28672\n\t" "s_mov_b32 s41, 917504\n\t" \
   "buffer_load_dwordx4 a[76:79], v16, %[ra], s41 offen\n\t" "s_waitcnt vmcnt(15)\n\t" "ds_write_b128 v20, a[160:163] offset:32768\n\t" \
   "s_mov_b32 s41, 1310720\n\t" "buffer_load_dwordx4 a[20:23], v16, %[ra], s41 offen\n\t" "s_waitcnt vmcnt(15)\n\t" \
   "ds_write_b128 v20, a[224:227] offset:36864\n\t" "s_mov_b32 s41, 1441792\n\t" "buffer_load_dwordx4 a[84:87], v16, %[ra], s41 offen\n\t" \
   "s_waitcnt vmcnt(15)\n\t" "ds_write_b128 v20, a[168:171] offset:40960\n\t" "s_mov_b32 s41, 1835008\n\t" \
   "buffer_load_dwordx4 a[28:31], v16, %[ra], s41 offen\n\t" "s_waitcnt vmcnt(15)\n\t" "ds_write_b128 v20, a[232:235] offset:45056\n\t" \
   "s_mov_b32 s41, 1966080\n\t" "buffer_load_dwordx4 a[92:95], v16, %[ra], s41 offen\n\t" "s_waitcnt vmcnt(15)\n\t" \
   "ds_write_b128 v20, a[176:179] offset:49152\n\t" "s_mov_b32 s41, 2359296\n\t" "buffer_load_dwordx4 a[36:39], v16, %[ra], s41 offen\n\t" \
   "s_waitcnt vmcnt(15)\n\t" "ds_write_b128 v20, a[240:243] offset:53248\n\t" "s_mov_b32 s41, 2490368\n\t" \
   "buffer_load_dwordx4 a[100:103], v16, %[ra], s41 offen\n\t" "s_waitcnt vmcnt(15)\n\t" "ds_write_b128 v20, a[184:187] offset:57344\n\t" \
   "s_mov_b32 s41, 2883584\n\t" "buffer_load_dwordx4 a[44:47], v16, %[ra], s41 offen\n\t" "s_waitcnt vmcnt(15)\n\t" \
   "ds_write_b128 v20, a[248:251] offset:61440\n\t" "s_mov_b32 s41, 3014656\n\t" "buffer_load_dwordx4 a[108:111], v16, %[ra], s41 offen\n\t" \
   "s_waitcnt vmcnt(15)\n\t" "ds_write_b128 v23, v[224:227] offset:0\n\t" "s_mov_b32 s41, 3407872\n\t" \
   "buffer_load_dwordx4 a[52:55], v16, %[ra], s41 offen\n\t" "s_waitcnt vmcnt(15)\n\t" "ds_write_b128 v23, v[240:243] offset:4096\n\t" \
   "s_mov_b32 s41, 3538944\n\t" "buffer_load_dwordx4 a[116:119], v16, %[ra], s41 offen\n\t" "s_waitcnt vmcnt(15)\n\t" \
   "ds_write_b128 v23, v[232:235] offset:8192\n\t" "s_mov_b32 s41, 3932160\n\t" "buffer_load_dwordx4 a[60:63], v16, %[ra], s41 offen\n\t" \
   "s_waitcnt vmcnt(15)\n\t" "ds_write_b128 v23, v[248:251] offset:12288\n\t" "s_mov_b32 s41, 4063232\n\t" \
   "buffer_load_dwordx4 a[124:127], v16, %[ra], s41 offen\n\t" "s_waitcnt lgkmcnt(0)\n\t" "s_barrier\n\t" LDSTAMP(3) "s_lshl_b32 s40, %[p0], 13\n\t" \
   "v_add_u32 v24, s40, v18\n\t" "v_add_u32 v25, s40, v19\n\t" "s_add_u32 s40, s40, 8192\n\t" "s_cmp_eq_u32 s40, 81920\n\t" \
   "s_cselect_b32 s40, 0, s40\n\t" "ds_read_b64_tr_b16 a[128:129], v24 offset:0\n\t" "ds_read_b64_tr_b16 a[130:131], v24 offset:4096\n\t" \
   "ds_read_b64_tr_b16 a[192:193], v25 offset:0\n\t" "ds_read_b64_tr_b16 a[194:195], v25 offset:4096\n\t" "v_add_u32 v24, s40, v18\n\t" \
   "v_add_u32 v25, s40, v19\n\t" "s_add_u32 s40, s40, 8192\n\t" "s_cmp_eq_u32 s40, 81920\n\t" "s_cselect_b32 s40, 0, s40\n\t" \
   "ds_read_b64_tr_b16 a[136:137], v24 offset:0\n\t" "ds_read_b64_tr_b16 a[138:139], v24 offset:4096\n\t" \
   "ds_read_b64_tr_b16 a[200:201], v25 offset:0\n\t" "ds_read_b64_tr_b16 a[202:203], v25 offset:4096\n\t" "v_add_u32 v24, s40, v18\n\t" \
   "v_add_u32 v25, s40, v19\n\t" "s_add_u32 s40, s40, 8192\n\t" "s_cmp_eq_u32 s40, 81920\n\t" "s_cselect_b32 s40, 0, s40\n\t" \
   "ds_read_b64_tr_b16 a[144:145], v24 offset:0\n\t" "ds_read_b64_tr_b16 a[146:147], v24 offset:4096\n\t" \
   "ds_read_b64_tr_b16 a[208:209], v25 offset:0\n\t" "ds_read_b64_tr_b16 a[210:211], v25 offset:4096\n\t" "v_add_u32 v24, s40, v18\n\t" \
   "v_add_u32 v25, s40, v19\n\t" "s_add_u32 s40, s40, 8192\n\t" "s_cmp_eq_u32 s40, 81920\n\t" "s_cselect_b32 s40, 0, s40\n\t" \
   "ds_read_b64_tr_b16 a[152:153], v24 offset:0\n\t" "ds_read_b64_tr_b16 a[154:155], v24 offset:4096\n\t" \
   "ds_read_b64_tr_b16 a[216:217], v25 offset:0\n\t" "ds_read_b64_tr_b16 a[218:219], v25 offset:4096\n\t" "v_add_u32 v24, s40, v18\n\t" \
   "v_add_u32 v25, s40, v19\n\t" "s_add_u32 s40, s40, 8192\n\t" "s_cmp_eq_u32 s40, 81920\n\t" "s_cselect_b32 s40, 0, s40\n\t" \
   "ds_read_b64_tr_b16 a[160:161], v24 offset:0\n\t" "ds_read_b64_tr_b16 a[162:163], v24 offset:4096\n\t" \
   "ds_read_b64_tr_b16 a[224:225], v25 offset:0\n\t" "ds_read_b64_tr_b16 a[226:227], v25 offset:4096\n\t" "v_add_u32 v24, s40, v18\n\t" \
   "v_add_u32 v25, s40, v19\n\t" "s_add_u32 s40, s40, 8192\n\t" "s_cmp_eq_u32 s40, 81920\n\t" "s_cselect_b32 s40, 0, s40\n\t" \
   "ds_read_b64_tr_b16 a[168:169], v24 offset:0\n\t" "ds_read_b64_tr_b16 a[170:171], v24 offset:4096\n\t" \
   "ds_read_b64_tr_b16 a[232:233], v25 offset:0\n\t" "ds_read_b64_tr_b16 a[234:235], v25 offset:4096\n\t" "v_add_u32 v24, s40, v18\n\t" \
   "v_add_u32 v25, s40, v19\n\t" "s_add_u32 s40, s40, 8192\n\t" "s_cmp_eq_u32 s40, 81920\n\t" "s_cselect_b32 s40, 0, s40\n\t" \
   "ds_read_b64_tr_b16 a[176:177], v24 offset:0\n\t" "ds_read_b64_tr_b16 a[178:179], v24 offset:4096\n\t" \
   "ds_read_b64_tr_b16 a[240:241], v25 offset:0\n\t" "ds_read_b64_tr_b16 a[242:243], v25 offset:4096\n\t" "v_add_u32 v24, s40, v18\n\t" \
   "v_add_u32 v25, s40, v19\n\t" "s_add_u32 s40, s40, 8192\n\t" "s_cmp_eq_u32 s40, 81920\n\t" "s_cselect_b32 s40, 0, s40\n\t" \
   "ds_read_b64_tr_b16 a[184:185], v24 offset:0\n\t" "ds_read_b64_tr_b16 a[186:187], v24 offset:4096\n\t" \
   "ds_read_b64_tr_b16 a[248:249], v25 offset:0\n\t" "ds_read_b64_tr_b16 a[250:251], v25 offset:4096\n\t" "v_add_u32 v24, s40, v18\n\t" \
   "v_add_u32 v25, s40, v19\n\t" "s_add_u32 s40, s40, 8192\n\t" "s_cmp_eq_u32 s40, 81920\n\t" "s_cselect_b32 s40, 0, s40\n\t" \
   "ds_read_b64_tr_b16 v[224:225], v24 offset:0\n\t" "ds_read_b64_tr_b16 v[226:227], v24 offset:4096\n\t" \
   "ds_read_b64_tr_b16 v[240:241], v25 offset:0\n\t" "ds_read_b64_tr_b16 v[242:243], v25 offset:4096\n\t" "v_add_u32 v24, s40, v18\n\t" \
   "v_add_u32 v25, s40, v19\n\t" "s_add_u32 s40, s40, 8192\n\t" "s_cmp_eq_u32 s40, 81920\n\t" "s_cselect_b32 s40, 0, s40\n\t" \
   "ds_read_b64_tr_b16 v[232:233], v24 offset:0\n\t" "ds_read_b64_tr_b16 v[234:235], v24 offset:4096\n\t" \
   "ds_read_b64_tr_b16 v[248:249], v25 offset:0\n\t" "ds_read_b64_tr_b16 v[250:251], v25 offset:4096\n\t" "s_waitcnt vmcnt(15)\n\t" \
   "s_waitcnt lgkmcnt(0)\n\t" "s_barrier\n\t" "ds_write_b128 v20, a[4:7] offset:0\n\t" "s_mov_b32 s41, 4456448\n\t" \
   "buffer_load_dwordx4 v[196:199], v16, %[ra], s41 offen\n\t" "s_waitcnt vmcnt(15)\n\t" "ds_write_b128 v20, a[68:71] offset:4096\n\t" \
   "s_mov_b32 s41, 4587520\n\t" "buffer_load_dwordx4 v[212:215], v16, %[ra], s41 offen\n\t" "s_waitcnt vmcnt(15)\n\t" \
   "ds_write_b128 v20, a[12:15] offset:8192\n\t" "s_mov_b32 s41, 4980736\n\t" "buffer_load_dwordx4 v[204:207], v16, %[ra], s41 offen\n\t" \
   "s_waitcnt vmcnt(15)\n\t" "ds_write_b128 v20, a[76:79] offset:12288\n\t" "s_mov_b32 s41, 5111808\n\t" \
   "buffer_load_dwordx4 v[220:223], v16, %[ra], s41 offen\n\t" "s_waitcnt vmcnt(15)\n\t" "ds_write_b128 v20, a[20:23] offset:16384\n\t" \
   "s_mov_b32 s41, 262144\n\t" "buffer_load_dwordx4 a[132:135], v16, %[rb], s41 offen\n\t" "s_waitcnt vmcnt(15)\n\t" \
   "ds_write_b128 v20, a[84:87] offset:20480\n\t" "s_mov_b32 s41, 393216\n\t" "buffer_load_dwordx4 a[196:199], v16, %[rb], s41 offen\n\t" \
   "s_waitcnt vmcnt(15)\n\t" "ds_write_b128 v20, a[28:31] offset:24576\n\t" "s_mov_b32 s41, 786432\n\t" \
   "buffer_load_dwordx4 a[140:143], v16, %[rb], s41 offen\n\t" "s_waitcnt vmcnt(15)\n\t" "ds_write_b128 v20, a[92:95] offset:28672\n\t" \
   "s_mov_b32 s41, 917504\n\t" "buffer_load_dwordx4 a[204:207], v16, %[rb], s41 offen\n\t" "s_waitcnt vmcnt(15)\n\t" \
   "ds_write_b128 v20, a[36:39] offset:32768\n\t" "s_mov_b32 s41, 1310720\n\t" "buffer_load_dwordx4 a[148:151], v16, %[rb], s41 offen\n\t" \
   "s_waitcnt vmcnt(15)\n\t" "ds_write_b128 v20, a[100:103] offset:36864\n\t" "s_mov_b32 s41, 1441792\n\t" \
   "buffer_load_dwordx4 a[212:215], v16, %[rb], s41 offen\n\t" "s_waitcnt vmcnt(15)\n\t" "ds_write_b128 v20, a[44:47] offset:40960\n\t" \
   "s_mov_b32 s41, 1835008\n\t" "buffer_load_dwordx4 a[156:159], v16, %[rb], s41 offen\n\t" "s_waitcnt vmcnt(15)\n\t" \
   "ds_write_b128 v20, a[108:111] offset:45056\n\t" "s_mov_b32 s41, 1966080\n\t" "buffer_load_dwordx4 a[220:223], v16, %[rb], s41 offen\n\t" \
   "s_waitcnt vmcnt(15)\n\t" "ds_write_b128 v20, a[52:55] offset:49152\n\t" "s_mov_b32 s41, 2359296\n\t" \
   "buffer_load_dwordx4 a[164:167], v16, %[rb], s41 offen\n\t" "s_waitcnt vmcnt(15)\n\t" "ds_write_b128 v20, a[116:119] offset:53248\n\t" \
   "s_mov_b32 s41, 2490368\n\t" "buffer_load_dwordx4 a[228:231], v16, %[rb], s41 offen\n\t" "s_waitcnt vmcnt(15)\n\t" \
   "ds_write_b128 v20, a[60:63] offset:57344\n\t" "s_mov_b32 s41, 2883584\n\t" "buffer_load_dwordx4 a[172:175], v16, %[rb], s41 offen\n\t" \
   "s_waitcnt vmcnt(15)\n\t" "ds_write_b128 v20, a[124:127] offset:61440\n\t" "s_mov_b32 s41, 3014656\n\t" \
   "buffer_load_dwordx4 a[236:239], v16, %[rb], s41 offen\n\t" "s_waitcnt vmcnt(15)\n\t" "ds_write_b128 v23, v[196:199] offset:0\n\t" \
   "s_mov_b32 s41, 3407872\n\t" "buffer_load_dwordx4 a[180:183], v16, %[rb], s41 offen\n\t" "s_waitcnt vmcnt(15)\n\t" \
   "ds_write_b128 v23, v[212:215] offset:4096\n\t" "s_mov_b32 s41, 3538944\n\t" "buffer_load_dwordx4 a[244:247], v16, %[rb], s41 offen\n\t" \
   "s_waitcnt vmcnt(15)\n\t" "ds_write_b128 v23, v[204:207] offset:8192\n\t" "s_mov_b32 s41, 3932160\n\t" \
   "buffer_load_dwordx4 a[188:191], v16, %[rb], s41 offen\n\t" "s_waitcnt vmcnt(15)\n\t" "ds_write_b128 v23, v[220:223] offset:12288\n\t" \
   "s_mov_b32 s41, 4063232\n\t" "buffer_load_dwordx4 a[252:255], v16, %[rb], s41 offen\n\t" "s_waitcnt lgkmcnt(0)\n\t" "s_barrier\n\t" LDSTAMP(4) \
   "s_lshl_b32 s40, %[p0], 13\n\t" "v_add_u32 v24, s40, v18\n\t" "v_add_u32 v25, s40, v19\n\t" "s_add_u32 s40, s40, 8192\n\t" \
   "s_cmp_eq_u32 s40, 81920\n\t" "s_cselect_b32 s40, 0, s40\n\t" "ds_read_b64_tr_b16 a[4:5], v24 offset:0\n\t" \
   "ds_read_b64_tr_b16 a[6:7], v24 offset:4096\n\t" "ds_read_b64_tr_b16 a[68:69], v25 offset:0\n\t" "ds_read_b64_tr_b16 a[70:71], v25 offset:4096\n\t" \
   "v_add_u32 v24, s40, v18\n\t" "v_add_u32 v25, s40, v19\n\t" "s_add_u32 s40, s40, 8192\n\t" "s_cmp_eq_u32 s40, 81920\n\t" \
   "s_cselect_b32 s40, 0, s40\n\t" "ds_read_b64_tr_b16 a[12:13], v24 offset:0\n\t" "ds_read_b64_tr_b16 a[14:15], v24 offset:4096\n\t" \
   "ds_read_b64_tr_b16 a[76:77], v25 offset:0\n\t" "ds_read_b64_tr_b16 a[78:79], v25 offset:4096\n\t" "v_add_u32 v24, s40, v18\n\t" \
   "v_add_u32 v25, s40, v19\n\t" "s_add_u32 s40, s40, 8192\n\t" "s_cmp_eq_u32 s40, 81920\n\t" "s_cselect_b32 s40, 0, s40\n\t" \
   "ds_read_b64_tr_b16 a[20:21], v24 offset:0\n\t" "ds_read_b64_tr_b16 a[22:23], v24 offset:4096\n\t" "ds_read_b64_tr_b16 a[84:85], v25 offset:0\n\t" \
   "ds_read_b64_tr_b16 a[86:87], v25 offset:4096\n\t" "v_add_u32 v24, s40, v18\n\t" "v_add_u32 v25, s40, v19\n\t" "s_add_u32 s40, s40, 8192\n\t" \
   "s_cmp_eq_u32 s40, 81920\n\t" "s_cselect_b32 s40, 0, s40\n\t" "ds_read_b64_tr_b16 a[28:29], v24 offset:0\n\t" \
   "ds_read_b64_tr_b16 a[30:31], v24 offset:4096\n\t" "ds_read_b64_tr_b16 a[92:93], v25 offset:0\n\t" \
   "ds_read_b64_tr_b16 a[94:95], v25 offset:4096\n\t" "v_add_u32 v24, s40, v18\n\t" "v_add_u32 v25, s40, v19\n\t" "s_add_u32 s40, s40, 8192\n\t" \
   "s_cmp_eq_u32 s40, 81920\n\t" "s_cselect_b32 s40, 0, s40\n\t" "ds_read_b64_tr_b16 a[36:37], v24 offset:0\n\t" \
   "ds_read_b64_tr_b16 a[38:39], v24 offset:4096\n\t" "ds_read_b64_tr_b16 a[100:101], v25 offset:0\n\t" \
   "ds_read_b64_tr_b16 a[102:103], v25 offset:4096\n\t" "v_add_u32 v24, s40, v18\n\t" "v_add_u32 v25, s40, v19\n\t" "s_add_u32 s40, s40, 8192\n\t" \
   "s_cmp_eq_u32 s40, 81920\n\t" "s_cselect_b32 s40, 0, s40\n\t" "ds_read_b64_tr_b16 a[44:45], v24 offset:0\n\t" \
   "ds_read_b64_tr_b16 a[46:47], v24 offset:4096\n\t" "ds_read_b64_tr_b16 a[108:109], v25 offset:0\n\t" \
   "ds_read_b64_tr_b16 a[110:111], v25 offset:4096\n\t" "v_add_u32 v24, s40, v18\n\t" "v_add_u32 v25, s40, v19\n\t" "s_add_u32 s40, s40, 8192\n\t" \
   "s_cmp_eq_u32 s40, 81920\n\t" "s_cselect_b32 s40, 0, s40\n\t" "ds_read_b64_tr_b16 a[52:53], v24 offset:0\n\t" \
   "ds_read_b64_tr_b16 a[54:55], v24 offset:4096\n\t" "ds_read_b64_tr_b16 a[116:117], v25 offset:0\n\t" \
   "ds_read_b64_tr_b16 a[118:119], v25 offset:4096\n\t" "v_add_u32 v24, s40, v18\n\t" "v_add_u32 v25, s40, v19\n\t" "s_add_u32 s40, s40, 8192\n\t" \
   "s_cmp_eq_u32 s40, 81920\n\t" "s_cselect_b32 s40, 0, s40\n\t" "ds_read_b64_tr_b16 a[60:61], v24 offset:0\n\t" \
   "ds_read_b64_tr_b16 a[62:63], v24 offset:4096\n\t" "ds_read_b64_tr_b16 a[124:125], v25 offset:0\n\t" \
   "ds_read_b64_tr_b16 a[126:127], v25 offset:4096\n\t" "v_add_u32 v24, s40, v18\n\t" "v_add_u32 v25, s40, v19\n\t" "s_add_u32 s40, s40, 8192\n\t" \
   "s_cmp_eq_u32 s40, 81920\n\t" "s_cselect_b32 s40, 0, s40\n\t" "ds_read_b64_tr_b16 v[196:197], v24 offset:0\n\t" \
   "ds_read_b64_tr_b16 v[198:199], v24 offset:4096\n\t" "ds_read_b64_tr_b16 v[212:213], v25 offset:0\n\t" \
   "ds_read_b64_tr_b16 v[214:215], v25 offset:4096\n\t" "v_add_u32 v24, s40, v18\n\t" "v_add_u32 v25, s40, v19\n\t" "s_add_u32 s40, s40, 8192\n\t" \
   "s_cmp_eq_u32 s40, 81920\n\t" "s_cselect_b32 s40, 0, s40\n\t" "ds_read_b64_tr_b16 v[204:205], v24 offset:0\n\t" \
   "ds_read_b64_tr_b16 v[206:207], v24 offset:4096\n\t" "ds_read_b64_tr_b16 v[220:221], v25 offset:0\n\t" \
   "ds_read_b64_tr_b16 v[222:223], v25 offset:4096\n\t" "s_waitcnt vmcnt(15)\n\t" "s_waitcnt lgkmcnt(0)\n\t" "s_barrier\n\t" \
   "ds_write_b128 v20, a[132:135] offset:0\n\t" "s_mov_b32 s41, 4456448\n\t" "buffer_load_dwordx4 v[228:231], v16, %[rb], s41 offen\n\t" \
   "s_waitcnt vmcnt(15)\n\t" "ds_write_b128 v20, a[196:199] offset:4096\n\t" "s_mov_b32 s41, 4587520\n\t" \
   "buffer_load_dwordx4 v[244:247], v16, %[rb], s41 offen\n\t" "s_waitcnt vmcnt(15)\n\t" "ds_write_b128 v20, a[140:143] offset:8192\n\t" \
   "s_mov_b32 s41, 4980736\n\t" "buffer_load_dwordx4 v[236:239], v16, %[rb], s41 offen\n\t" "s_waitcnt vmcnt(15)\n\t" \
   "ds_write_b128 v20, a[204:207] offset:12288\n\t" "s_mov_b32 s41, 5111808\n\t" "buffer_load_dwordx4 v[252:255], v16, %[rb], s41 offen\n\t" \
   "s_waitcnt vmcnt(15)\n\t" "ds_write_b128 v20, a[148:151] offset:16384\n\t" "s_waitcnt vmcnt(14)\n\t" \
   "ds_write_b128 v20, a[212:215] offset:20480\n\t" "s_waitcnt vmcnt(13)\n\t" "ds_write_b128 v20, a[156:159] offset:24576\n\t" \
   "s_waitcnt vmcnt(12)\n\t" "ds_write_b128 v20, a[220:223] offset:28672\n\t" "s_waitcnt vmcnt(11)\n\t" \
   "ds_write_b128 v20, a[164:167] offset:32768\n\t" "s_waitcnt vmcnt(10)\n\t" "ds_write_b128 v20, a[228:231] offset:36864\n\t" \
   "buffer_load_dwordx4 v[28:31], v112, %[rw], 0 offen sc1\n\t" "s_waitcnt vmcnt(10)\n\t" "ds_write_b128 v20, a[172:175] offset:40960\n\t" \
   "s_waitcnt vmcnt(9)\n\t" "ds_write_b128 v20, a[236:239] offset:45056\n\t" "s_waitcnt vmcnt(8)\n\t" "ds_write_b128 v20, a[180:183] offset:49152\n\t" \
   "s_waitcnt vmcnt(7)\n\t" "ds_write_b128 v20, a[244:247] offset:53248\n\t" "s_waitcnt vmcnt(6)\n\t" "ds_write_b128 v20, a[188:191] offset:57344\n\t" \
   "s_waitcnt vmcnt(5)\n\t" "ds_write_b128 v20, a[252:255] offset:61440\n\t" "s_waitcnt vmcnt(4)\n\t" "ds_write_b128 v23, v[228:231] offset:0\n\t" \
   "s_waitcnt vmcnt(3)\n\t" "ds_write_b128 v23, v[244:247] offset:4096\n\t" "s_waitcnt vmcnt(2)\n\t" "ds_write_b128 v23, v[236:239] offset:8192\n\t" \
   "s_waitcnt vmcnt(1)\n\t" "ds_write_b128 v23, v[252:255] offset:12288\n\t" "s_waitcnt lgkmcnt(0)\n\t" "s_barrier\n\t" LDSTAMP(5) \
   "s_lshl_b32 s40, %[p0], 13\n\t" "v_add_u32 v24, s40, v18\n\t" "v_add_u32 v25, s40, v19\n\t" "s_add_u32 s40, s40, 8192\n\t" \
   "s_cmp_eq_u32 s40, 81920\n\t" "s_cselect_b32 s40, 0, s40\n\t" "ds_read_b64_tr_b16 a[132:133], v24 offset:0\n\t" \
   "ds_read_b64_tr_b16 a[134:135], v24 offset:4096\n\t" "ds_read_b64_tr_b16 a[196:197], v25 offset:0\n\t" \
   "ds_read_b64_tr_b16 a[198:199], v25 offset:4096\n\t" "v_add_u32 v24, s40, v18\n\t" "v_add_u32 v25, s40, v19\n\t" "s_add_u32 s40, s40, 8192\n\t" \
   "s_cmp_eq_u32 s40, 81920\n\t" "s_cselect_b32 s40, 0, s40\n\t" "ds_read_b64_tr_b16 a[140:141], v24 offset:0\n\t" \
   "ds_read_b64_tr_b16 a[142:143], v24 offset:4096\n\t" "ds_read_b64_tr_b16 a[204:205], v25 offset:0\n\t" \
   "ds_read_b64_tr_b16 a[206:207], v25 offset:4096\n\t" "v_add_u32 v24, s40, v18\n\t" "v_add_u32 v25, s40, v19\n\t" "s_add_u32 s40, s40, 8192\n\t" \
   "s_cmp_eq_u32 s40, 81920\n\t" "s_cselect_b32 s40, 0, s40\n\t" "ds_read_b64_tr_b16 a[148:149], v24 offset:0\n\t" \
   "ds_read_b64_tr_b16 a[150:151], v24 offset:4096\n\t" "ds_read_b64_tr_b16 a[212:213], v25 offset:0\n\t" \
   "ds_read_b64_tr_b16 a[214:215], v25 offset:4096\n\t" "v_add_u32 v24, s40, v18\n\t" "v_add_u32 v25, s40, v19\n\t" "s_add_u32 s40, s40, 8192\n\t" \
   "s_cmp_eq_u32 s40, 81920\n\t" "s_cselect_b32 s40, 0, s40\n\t" "ds_read_b64_tr_b16 a[156:157], v24 offset:0\n\t" \
   "ds_read_b64_tr_b16 a[158:159], v24 offset:4096\n\t" "ds_read_b64_tr_b16 a[220:221], v25 offset:0\n\t" \
   "ds_read_b64_tr_b16 a[222:223], v25 offset:4096\n\t" "v_add_u32 v24, s40, v18\n\t" "v_add_u32 v25, s40, v19\n\t" "s_add_u32 s40, s40, 8192\n\t" \
   "s_cmp_eq_u32 s40, 81920\n\t" "s_cselect_b32 s40, 0, s40\n\t" "ds_read_b64_tr_b16 a[164:165], v24 offset:0\n\t" \
   "ds_read_b64_tr_b16 a[166:167], v24 offset:4096\n\t" "ds_read_b64_tr_b16 a[228:229], v25 offset:0\n\t" \
   "ds_read_b64_tr_b16 a[230:231], v25 offset:4096\n\t" "v_add_u32 v24, s40, v18\n\t" "v_add_u32 v25, s40, v19\n\t" "s_add_u32 s40, s40, 8192\n\t" \
   "s_cmp_eq_u32 s40, 81920\n\t" "s_cselect_b32 s40, 0, s40\n\t" "ds_read_b64_tr_b16 a[172:173], v24 offset:0\n\t" \
   "ds_read_b64_tr_b16 a[174:175], v24 offset:4096\n\t" "ds_read_b64_tr_b16 a[236:237], v25 offset:0\n\t" \
   "ds_read_b64_tr_b16 a[238:239], v25 offset:4096\n\t" "v_add_u32 v24, s40, v18\n\t" "v_add_u32 v25, s40, v19\n\t" "s_add_u32 s40, s40, 8192\n\t" \
   "s_cmp_eq_u32 s40, 81920\n\t" "s_cselect_b32 s40, 0, s40\n\t" "ds_read_b64_tr_b16 a[180:181], v24 offset:0\n\t" \
   "ds_read_b64_tr_b16 a[182:183], v24 offset:4096\n\t" "ds_read_b64_tr_b16 a[244:245], v25 offset:0\n\t" \
   "ds_read_b64_tr_b16 a[246:247], v25 offset:4096\n\t" "v_add_u32 v24, s40, v18\n\t" "v_add_u32 v25, s40, v19\n\t" "s_add_u32 s40, s40, 8192\n\t" \
   "s_cmp_eq_u32 s40, 81920\n\t" "s_cselect_b32 s40, 0, s40\n\t" "ds_read_b64_tr_b16 a[188:189], v24 offset:0\n\t" \
   "ds_read_b64_tr_b16 a[190:191], v24 offset:4096\n\t" "ds_read_b64_tr_b16 a[252:253], v25 offset:0\n\t" \
   "ds_read_b64_tr_b16 a[254:255], v25 offset:4096\n\t" "v_add_u32 v24, s40, v18\n\t" "v_add_u32 v25, s40, v19\n\t" "s_add_u32 s40, s40, 8192\n\t" \
   "s_cmp_eq_u32 s40, 81920\n\t" "s_cselect_b32 s40, 0, s40\n\t" "ds_read_b64_tr_b16 v[228:229], v24 offset:0\n\t" \
   "ds_read_b64_tr_b16 v[230:231], v24 offset:4096\n\t" "ds_read_b64_tr_b16 v[244:245], v25 offset:0\n\t" \
   "ds_read_b64_tr_b16 v[246:247], v25 offset:4096\n\t" "v_add_u32 v24, s40, v18\n\t" "v_add_u32 v25, s40, v19\n\t" "s_add_u32 s40, s40, 8192\n\t" \
   "s_cmp_eq_u32 s40, 81920\n\t" "s_cselect_b32 s40, 0, s40\n\t" "ds_read_b64_tr_b16 v[236:237], v24 offset:0\n\t" \
   "ds_read_b64_tr_b16 v[238:239], v24 offset:4096\n\t" "ds_read_b64_tr_b16 v[252:253], v25 offset:0\n\t" \
   "ds_read_b64_tr_b16 v[254:255], v25 offset:4096\n\t" "s_waitcnt vmcnt(0) lgkmcnt(0)\n\t" "s_barrier"
#define LDP_CLOB "v24", "v25", "v26", "v32", "v33", "v34", "v35", "v36", "v37", "v38", "v39", "v40", "v41", "v42", "v43", "v44", "v45", "v46", "v47", "v48", "v49", "v50", "v51", "v52", "v53", "v54", "v55", "v56", "v57", "v58", "v59", "v60", "v61", "v62", "v63", "v64", "v65", "v66", "v67", "v68", "v69", "v70", "v71", "v72", "v73", "v74", "v75", "v76", "v77", "v78", "v79", "v80", "v81", "v82", "v83", "v84", "v85", "v86", "v87", "v88", "v89", "v90", "v91", "v92", "v93", "v94", "v95", "v96", "v97", "v98", "v99", "v100", "v101", "v102", "v103", "v104", "v105", "v106", "v107", "v108", "v109", "v110", "v111", "v120", "v121", "v122", "v123", "s40", "s41"
#define LDK_ASM \
   "buffer_load_dword v26, v113, %[rw], 0 offen sc1\n\t" "s_mov_b32 s41, 0\n\t" "buffer_load_dwordx4 v[32:35], v116, %[rx], s41 offen\n\t" \
   "s_mov_b32 s41, 10240\n\t" "buffer_load_dwordx4 v[36:39], v116, %[rx], s41 offen\n\t" "s_mov_b32 s41, 128\n\t" \
   "buffer_load_dwordx4 v[40:43], v116, %[rx], s41 offen\n\t" "s_mov_b32 s41, 10368\n\t" "buffer_load_dwordx4 v[44:47], v116, %[rx], s41 offen\n\t" \
   "s_mov_b32 s41, 256\n\t" "buffer_load_dwordx4 v[48:51], v116, %[rx], s41 offen\n\t" "s_mov_b32 s41, 10496\n\t" \
   "buffer_load_dwordx4 v[52:55], v116, %[rx], s41 offen\n\t" "s_mov_b32 s41, 384\n\t" "buffer_load_dwordx4 v[56:59], v116, %[rx], s41 offen\n\t" \
   "s_mov_b32 s41, 10624\n\t" "buffer_load_dwordx4 v[60:63], v116, %[rx], s41 offen\n\t" "s_mov_b32 s41, 512\n\t" \
   "buffer_load_dwordx4 v[64:67], v116, %[rx], s41 offen\n\t" "s_mov_b32 s41, 10752\n\t" "buffer_load_dwordx4 v[68:71], v116, %[rx], s41 offen\n\t" \
   "s_mov_b32 s41, 640\n\t" "buffer_load_dwordx4 v[72:75], v116, %[rx], s41 offen\n\t" "s_mov_b32 s41, 10880\n\t" \
   "buffer_load_dwordx4 v[76:79], v116, %[rx], s41 offen\n\t" "s_mov_b32 s41, 768\n\t" "buffer_load_dwordx4 v[80:83], v116, %[rx], s41 offen\n\t" \
   "s_mov_b32 s41, 11008\n\t" "buffer_load_dwordx4 v[84:87], v116, %[rx], s41 offen\n\t" "s_mov_b32 s41, 896\n\t" \
   "buffer_load_dwordx4 v[88:91], v116, %[rx], s41 offen\n\t" "s_mov_b32 s41, 11136\n\t" "buffer_load_dwordx4 v[92:95], v116, %[rx], s41 offen\n\t" \
   "s_mov_b32 s40, 0x3f803f80\n\t" "v_mov_b32 v120, 0\n\t" "v_mov_b32 v121, 0\n\t" "v_mov_b32 v122, 0\n\t" "v_mov_b32 v123, 0\n\t" \
   "s_waitcnt vmcnt(16)\n\t" "v_add_u32 v26, 1, v26\n\t" "buffer_store_dword v26, v114, %[rw], 0 offen sc1\n\t" "ds_write_b32 v115, v26\n\t" LDSTAMP(0) \
   "s_waitcnt vmcnt(15)\n\t" LDSTAMP(1) "ds_write_b64 v117, v[32:33] offset:0\n\t" "ds_write_b64 v117, v[34:35] offset:256\n\t" \
   "s_mov_b32 s41, 1024\n\t" "buffer_load_dwordx4 v[96:99], v116, %[rx], s41 offen\n\t" "v_dot2c_f32_bf16 v120, s40, v32\n\t" \
   "v_dot2c_f32_bf16 v121, v32, v32\n\t" "v_dot2c_f32_bf16 v120, s40, v33\n\t" "v_dot2c_f32_bf16 v121, v33, v33\n\t" \
   "v_dot2c_f32_bf16 v120, s40, v34\n\t" "v_dot2c_f32_bf16 v121, v34, v34\n\t" "v_dot2c_f32_bf16 v120, s40, v35\n\t" \
   "v_dot2c_f32_bf16 v121, v35, v35\n\t" "s_waitcnt vmcnt(15)\n\t" "ds_write_b64 v117, v[36:37] offset:128\n\t" \
   "ds_write_b64 v117, v[38:39] offset:384\n\t" "s_mov_b32 s41, 11264\n\t" "buffer_load_dwordx4 v[100:103], v116, %[rx], s41 offen\n\t" \
   "v_dot2c_f32_bf16 v122, s40, v36\n\t" "v_dot2c_f32_bf16 v123, v36, v36\n\t" "v_dot2c_f32_bf16 v122, s40, v37\n\t" \
   "v_dot2c_f32_bf16 v123, v37, v37\n\t" "v_dot2c_f32_bf16 v122, s40, v38\n\t" "v_dot2c_f32_bf16 v123, v38, v38\n\t" \
   "v_dot2c_f32_bf16 v122, s40, v39\n\t" "v_dot2c_f32_bf16 v123, v39, v39\n\t" "s_waitcnt vmcnt(15)\n\t" "ds_write_b64 v117, v[40:41] offset:2048\n\t" \
   "ds_write_b64 v117, v[42:43] offset:2304\n\t" "s_mov_b32 s41, 1152\n\t" "buffer_load_dwordx4 v[104:107], v116, %[rx], s41 offen\n\t" \
   "v_dot2c_f32_bf16 v120, s40, v40\n\t" "v_dot2c_f32_bf16 v121, v40, v40\n\t" "v_dot2c_f32_bf16 v120, s40, v41\n\t" \
   "v_dot2c_f32_bf16 v121, v41, v41\n\t" "v_dot2c_f32_bf16 v120, s40, v42\n\t" "v_dot2c_f32_bf16 v121, v42, v42\n\t" \
   "v_dot2c_f32_bf16 v120, s40, v43\n\t" "v_dot2c_f32_bf16 v121, v43, v43\n\t" "s_waitcnt vmcnt(15)\n\t" "ds_write_b64 v117, v[44:45] offset:2176\n\t" \
   "ds_write_b64 v117, v[46:47] offset:2432\n\t" "s_mov_b32 s41, 11392\n\t" "buffer_load_dwordx4 v[108:111], v116, %[rx], s41 offen\n\t" \
   "v_dot2c_f32_bf16 v122, s40, v44\n\t" "v_dot2c_f32_bf16 v123, v44, v44\n\t" "v_dot2c_f32_bf16 v122, s40, v45\n\t" \
   "v_dot2c_f32_bf16 v123, v45, v45\n\t" "v_dot2c_f32_bf16 v122, s40, v46\n\t" "v_dot2c_f32_bf16 v123, v46, v46\n\t" \
   "v_dot2c_f32_bf16 v122, s40, v47\n\t" "v_dot2c_f32_bf16 v123, v47, v47\n\t" "s_waitcnt vmcnt(15)\n\t" "ds_write_b64 v117, v[48:49] offset:4096\n\t" \
   "ds_write_b64 v117, v[50:51] offset:4352\n\t" "s_mov_b32 s41, 0\n\t" "buffer_load_dwordx4 a[0:3], v16, %[ra], s41 offen\n\t" \
   "v_dot2c_f32_bf16 v120, s40, v48\n\t" "v_dot2c_f32_bf16 v121, v48, v48\n\t" "v_dot2c_f32_bf16 v120, s40, v49\n\t" \
   "v_dot2c_f32_bf16 v121, v49, v49\n\t" "v_dot2c_f32_bf16 v120, s40, v50\n\t" "v_dot2c_f32_bf16 v121, v50, v50\n\t" \
   "v_dot2c_f32_bf16 v120, s40, v51\n\t" "v_dot2c_f32_bf16 v121, v51, v51\n\t" "s_waitcnt vmcnt(15)\n\t" "ds_write_b64 v117, v[52:53] offset:4224\n\t" \
   "ds_write_b64 v117, v[54:55] offset:4480\n\t" "s_mov_b32 s41, 131072\n\t" "buffer_load_dwordx4 a[4:7], v16, %[ra], s41 offen\n\t" \
   "v_dot2c_f32_bf16 v122, s40, v52\n\t" "v_dot2c_f32_bf16 v123, v52, v52\n\t" "v_dot2c_f32_bf16 v122, s40, v53\n\t" \
   "v_dot2c_f32_bf16 v123, v53, v53\n\t" "v_dot2c_f32_bf16 v122, s40, v54\n\t" "v_dot2c_f32_bf16 v123, v54, v54\n\t" \
   "v_dot2c_f32_bf16 v122, s40, v55\n\t" "v_dot2c_f32_bf16 v123, v55, v55\n\t" "s_waitcnt vmcnt(15)\n\t" "ds_write_b64 v117, v[56:57] offset:6144\n\t" \
   "ds_write_b64 v117, v[58:59] offset:6400\n\t" "s_mov_b32 s41, 262144\n\t" "buffer_load_dwordx4 a[16:19], v16, %[ra], s41 offen\n\t" \
   "v_dot2c_f32_bf16 v120, s40, v56\n\t" "v_dot2c_f32_bf16 v121, v56, v56\n\t" "v_dot2c_f32_bf16 v120, s40, v57\n\t" \
   "v_dot2c_f32_bf16 v121, v57, v57\n\t" "v_dot2c_f32_bf16 v120, s40, v58\n\t" "v_dot2c_f32_bf16 v121, v58, v58\n\t" \
   "v_dot2c_f32_bf16 v120, s40, v59\n\t" "v_dot2c_f32_bf16 v121, v59, v59\n\t" "s_waitcnt vmcnt(15)\n\t" "ds_write_b64 v117, v[60:61] offset:6272\n\t" \
   "ds_write_b64 v117, v[62:63] offset:6528\n\t" "s_mov_b32 s41, 393216\n\t" "buffer_load_dwordx4 a[20:23], v16, %[ra], s41 offen\n\t" \
   "v_dot2c_f32_bf16 v122, s40, v60\n\t" "v_dot2c_f32_bf16 v123, v60, v60\n\t" "v_dot2c_f32_bf16 v122, s40, v61\n\t" \
   "v_dot2c_f32_bf16 v123, v61, v61\n\t" "v_dot2c_f32_bf16 v122, s40, v62\n\t" "v_dot2c_f32_bf16 v123, v62, v62\n\t" \
   "v_dot2c_f32_bf16 v122, s40, v63\n\t" "v_dot2c_f32_bf16 v123, v63, v63\n\t" "s_waitcnt vmcnt(15)\n\t" "ds_write_b64 v117, v[64:65] offset:8192\n\t" \
   "ds_write_b64 v117, v[66:67] offset:8448\n\t" "s_mov_b32 s41, 524288\n\t" "buffer_load_dwordx4 a[32:35], v16, %[ra], s41 offen\n\t" \
   "v_dot2c_f32_bf16 v120, s40, v64\n\t" "v_dot2c_f32_bf16 v121, v64, v64\n\t" "v_dot2c_f32_bf16 v120, s40, v65\n\t" \
   "v_dot2c_f32_bf16 v121, v65, v65\n\t" "v_dot2c_f32_bf16 v120, s40, v66\n\t" "v_dot2c_f32_bf16 v121, v66, v66\n\t" \
   "v_dot2c_f32_bf16 v120, s40, v67\n\t" "v_dot2c_f32_bf16 v121, v67, v67\n\t" "s_waitcnt vmcnt(15)\n\t" "ds_write_b64 v117, v[68:69] offset:8320\n\t" \
   "ds_write_b64 v117, v[70:71] offset:8576\n\t" "s_mov_b32 s41, 655360\n\t" "buffer_load_dwordx4 a[36:39], v16, %[ra], s41 offen\n\t" \
   "v_dot2c_f32_bf16 v122, s40, v68\n\t" "v_dot2c_f32_bf16 v123, v68, v68\n\t" "v_dot2c_f32_bf16 v122, s40, v69\n\t" \
   "v_dot2c_f32_bf16 v123, v69, v69\n\t" "v_dot2c_f32_bf16 v122, s40, v70\n\t" "v_dot2c_f32_bf16 v123, v70, v70\n\t" \
   "v_dot2c_f32_bf16 v122, s40, v71\n\t" "v_dot2c_f32_bf16 v123, v71, v71\n\t" "s_waitcnt vmcnt(15)\n\t" "ds_write_b64 v117, v[72:73] offset:10240\n\t" \
   "ds_write_b64 v117, v[74:75] offset:10496\n\t" "s_mov_b32 s41, 786432\n\t" "buffer_load_dwordx4 a[48:51], v16, %[ra], s41 offen\n\t" \
   "v_dot2c_f32_bf16 v120, s40, v72\n\t" "v_dot2c_f32_bf16 v121, v72, v72\n\t" "v_dot2c_f32_bf16 v120, s40, v73\n\t" \
   "v_dot2c_f32_bf16 v121, v73, v73\n\t" "v_dot2c_f32_bf16 v120, s40, v74\n\t" "v_dot2c_f32_bf16 v121, v74, v74\n\t" \
   "v_dot2c_f32_bf16 v120, s40, v75\n\t" "v_dot2c_f32_bf16 v121, v75, v75\n\t" "s_waitcnt vmcnt(15)\n\t" "ds_write_b64 v117, v[76:77] offset:10368\n\t" \
   "ds_write_b64 v117, v[78:79] offset:10624\n\t" "s_mov_b32 s41, 917504\n\t" "buffer_load_dwordx4 a[52:55], v16, %[ra], s41 offen\n\t" \
   "v_dot2c_f32_bf16 v122, s40, v76\n\t" "v_dot2c_f32_bf16 v123, v76, v76\n\t" "v_dot2c_f32_bf16 v122, s40, v77\n\t" \
   "v_dot2c_f32_bf16 v123, v77, v77\n\t" "v_dot2c_f32_bf16 v122, s40, v78\n\t" "v_dot2c_f32_bf16 v123, v78, v78\n\t" \
   "v_dot2c_f32_bf16 v122, s40, v79\n\t" "v_dot2c_f32_bf16 v123, v79, v79\n\t" "s_waitcnt vmcnt(15)\n\t" "ds_write_b64 v117, v[80:81] offset:12288\n\t" \
   "ds_write_b64 v117, v[82:83] offset:12544\n\t" "s_mov_b32 s41, 1048576\n\t" "buffer_load_dwordx4 a[64:67], v16, %[ra], s41 offen\n\t" \
   "v_dot2c_f32_bf16 v120, s40, v80\n\t" "v_dot2c_f32_bf16 v121, v80, v80\n\t" "v_dot2c_f32_bf16 v120, s40, v81\n\t" \
   "v_dot2c_f32_bf16 v121, v81, v81\n\t" "v_dot2c_f32_bf16 v120, s40, v82\n\t" "v_dot2c_f32_bf16 v121, v82, v82\n\t" \
   "v_dot2c_f32_bf16 v120, s40, v83\n\t" "v_dot2c_f32_bf16 v121, v83, v83\n\t" "s_waitcnt vmcnt(15)\n\t" "ds_write_b64 v117, v[84:85] offset:12416\n\t" \
   "ds_write_b64 v117, v[86:87] offset:12672\n\t" "s_mov_b32 s41, 1179648\n\t" "buffer_load_dwordx4 a[68:71], v16, %[ra], s41 offen\n\t" \
   "v_dot2c_f32_bf16 v122, s40, v84\n\t" "v_dot2c_f32_bf16 v123, v84, v84\n\t" "v_dot2c_f32_bf16 v122, s40, v85\n\t" \
   "v_dot2c_f32_bf16 v123, v85, v85\n\t" "v_dot2c_f32_bf16 v122, s40, v86\n\t" "v_dot2c_f32_bf16 v123, v86, v86\n\t" \
   "v_dot2c_f32_bf16 v122, s40, v87\n\t" "v_dot2c_f32_bf16 v123, v87, v87\n\t" "s_waitcnt vmcnt(15)\n\t" "ds_write_b64 v117, v[88:89] offset:14336\n\t" \
   "ds_write_b64 v117, v[90:91] offset:14592\n\t" "s_mov_b32 s41, 0\n\t" "buffer_load_dwordx4 a[8:11], v16, %[rb], s41 offen\n\t" \
   "v_dot2c_f32_bf16 v120, s40, v88\n\t" "v_dot2c_f32_bf16 v121, v88, v88\n\t" "v_dot2c_f32_bf16 v120, s40, v89\n\t" \
   "v_dot2c_f32_bf16 v121, v89, v89\n\t" "v_dot2c_f32_bf16 v120, s40, v90\n\t" "v_dot2c_f32_bf16 v121, v90, v90\n\t" \
   "v_dot2c_f32_bf16 v120, s40, v91\n\t" "v_dot2c_f32_bf16 v121, v91, v91\n\t" "s_waitcnt vmcnt(15)\n\t" "ds_write_b64 v117, v[92:93] offset:14464\n\t" \
   "ds_write_b64 v117, v[94:95] offset:14720\n\t" "s_mov_b32 s41, 131072\n\t" "buffer_load_dwordx4 a[12:15], v16, %[rb], s41 offen\n\t" \
   "v_dot2c_f32_bf16 v122, s40, v92\n\t" "v_dot2c_f32_bf16 v123, v92, v92\n\t" "v_dot2c_f32_bf16 v122, s40, v93\n\t" \
   "v_dot2c_f32_bf16 v123, v93, v93\n\t" "v_dot2c_f32_bf16 v122, s40, v94\n\t" "v_dot2c_f32_bf16 v123, v94, v94\n\t" \
   "v_dot2c_f32_bf16 v122, s40, v95\n\t" "v_dot2c_f32_bf16 v123, v95, v95\n\t" "s_waitcnt vmcnt(15)\n\t" "ds_write_b64 v117, v[96:97] offset:16384\n\t" \
   "ds_write_b64 v117, v[98:99] offset:16640\n\t" "s_mov_b32 s41, 262144\n\t" "buffer_load_dwordx4 a[24:27], v16, %[rb], s41 offen\n\t" \
   "v_dot2c_f32_bf16 v120, s40, v96\n\t" "v_dot2c_f32_bf16 v121, v96, v96\n\t" "v_dot2c_f32_bf16 v120, s40, v97\n\t" \
   "v_dot2c_f32_bf16 v121, v97, v97\n\t" "v_dot2c_f32_bf16 v120, s40, v98\n\t" "v_dot2c_f32_bf16 v121, v98, v98\n\t" \
   "v_dot2c_f32_bf16 v120, s40, v99\n\t" "v_dot2c_f32_bf16 v121, v99, v99\n\t" "s_waitcnt vmcnt(15)\n\t" \
   "ds_write_b64 v117, v[100:101] offset:16512\n\t" "ds_write_b64 v117, v[102:103] offset:16768\n\t" "s_mov_b32 s41, 393216\n\t" \
   "buffer_load_dwordx4 a[28:31], v16, %[rb], s41 offen\n\t" "v_dot2c_f32_bf16 v122, s40, v100\n\t" "v_dot2c_f32_bf16 v123, v100, v100\n\t" \
   "v_dot2c_f32_bf16 v122, s40, v101\n\t" "v_dot2c_f32_bf16 v123, v101, v101\n\t" "v_dot2c_f32_bf16 v122, s40, v102\n\t" \
   "v_dot2c_f32_bf16 v123, v102, v102\n\t" "v_dot2c_f32_bf16 v122, s40, v103\n\t" "v_dot2c_f32_bf16 v123, v103, v103\n\t" "s_waitcnt vmcnt(15)\n\t" \
   "ds_write_b64 v117, v[104:105] offset:18432\n\t" "ds_write_b64 v117, v[106:107] offset:18688\n\t" "s_mov_b32 s41, 524288\n\t" \
   "buffer_load_dwordx4 a[40:43], v16, %[rb], s41 offen\n\t" "v_dot2c_f32_bf16 v120, s40, v104\n\t" "v_dot2c_f32_bf16 v121, v104, v104\n\t" \
   "v_dot2c_f32_bf16 v120, s40, v105\n\t" "v_dot2c_f32_bf16 v121, v105, v105\n\t" "v_dot2c_f32_bf16 v120, s40, v106\n\t" \
   "v_dot2c_f32_bf16 v121, v106, v106\n\t" "v_dot2c_f32_bf16 v120, s40, v107\n\t" "v_dot2c_f32_bf16 v121, v107, v107\n\t" "s_waitcnt vmcnt(15)\n\t" \
   "ds_write_b64 v117, v[108:109] offset:18560\n\t" "ds_write_b64 v117, v[110:111] offset:18816\n\t" "s_mov_b32 s41, 655360\n\t" \
   "buffer_load_dwordx4 a[44:47], v16, %[rb], s41 offen\n\t" "v_dot2c_f32_bf16 v122, s40, v108\n\t" "v_dot2c_f32_bf16 v123, v108, v108\n\t" \
   "v_dot2c_f32_bf16 v122, s40, v109\n\t" "v_dot2c_f32_bf16 v123, v109, v109\n\t" "v_dot2c_f32_bf16 v122, s40, v110\n\t" \
   "v_dot2c_f32_bf16 v123, v110, v110\n\t" "v_dot2c_f32_bf16 v122, s40, v111\n\t" "v_dot2c_f32_bf16 v123, v111, v111\n\t" "s_nop 3\n\t" \
   "v_add_f32_dpp v120, v120, v120 quad_perm:[1,0,3,2] row_mask:0xf bank_mask:0xf\n\t" \
   "v_add_f32_dpp v121, v121, v121 quad_perm:[1,0,3,2] row_mask:0xf bank_mask:0xf\n\t" \
   "v_add_f32_dpp v122, v122, v122 quad_perm:[1,0,3,2] row_mask:0xf bank_mask:0xf\n\t" \
   "v_add_f32_dpp v123, v123, v123 quad_perm:[1,0,3,2] row_mask:0xf bank_mask:0xf\n\t" \
   "v_add_f32_dpp v120, v120, v120 quad_perm:[2,3,0,1] row_mask:0xf bank_mask:0xf\n\t" \
   "v_add_f32_dpp v121, v121, v121 quad_perm:[2,3,0,1] row_mask:0xf bank_mask:0xf\n\t" \
   "v_add_f32_dpp v122, v122, v122 quad_perm:[2,3,0,1] row_mask:0xf bank_mask:0xf\n\t" \
   "v_add_f32_dpp v123, v123, v123 quad_perm:[2,3,0,1] row_mask:0xf bank_mask:0xf\n\t" \
   "v_add_f32_dpp v120, v120, v120 row_half_mirror row_mask:0xf bank_mask:0xf\n\t" \
   "v_add_f32_dpp v121, v121, v121 row_half_mirror row_mask:0xf bank_mask:0xf\n\t" \
   "v_add_f32_dpp v122, v122, v122 row_half_mirror row_mask:0xf bank_mask:0xf\n\t" \
   "v_add_f32_dpp v123, v123, v123 row_half_mirror row_mask:0xf bank_mask:0xf\n\t" "v_and_b32 v120, v119, v120\n\t" "v_and_b32 v121, v119, v121\n\t" \
   "v_and_b32 v122, v119, v122\n\t" "v_and_b32 v123, v119, v123\n\t" "ds_write_b64 v118, v[120:121]\n\t" "ds_write_b64 v118, v[122:123] offset:384\n\t" \
   "s_waitcnt vmcnt(15)\n\t" "ds_write_b128 v20, a[0:3] offset:0\n\t" "s_mov_b32 s41, 786432\n\t" \
   "buffer_load_dwordx4 a[56:59], v16, %[rb], s41 offen\n\t" "s_waitcnt vmcnt(15)\n\t" "ds_write_b128 v20, a[4:7] offset:4096\n\t" \
   "s_mov_b32 s41, 917504\n\t" "buffer_load_dwordx4 a[60:63], v16, %[rb], s41 offen\n\t" "s_waitcnt vmcnt(15)\n\t" \
   "ds_write_b128 v20, a[16:19] offset:8192\n\t" "s_mov_b32 s41, 1048576\n\t" "buffer_load_dwordx4 a[72:75], v16, %[rb], s41 offen\n\t" \
   "s_waitcnt vmcnt(15)\n\t" "ds_write_b128 v20, a[20:23] offset:12288\n\t" "s_mov_b32 s41, 1179648\n\t" \
   "buffer_load_dwordx4 a[76:79], v16, %[rb], s41 offen\n\t" "s_waitcnt vmcnt(15)\n\t" "ds_write_b128 v20, a[32:35] offset:16384\n\t" \
   "s_mov_b32 s41, 1310720\n\t" "buffer_load_dwordx4 a[80:83], v16, %[ra], s41 offen\n\t" "s_waitcnt vmcnt(15)\n\t" \
   "ds_write_b128 v20, a[36:39] offset:20480\n\t" "s_mov_b32 s41, 1441792\n\t" "buffer_load_dwordx4 a[84:87], v16, %[ra], s41 offen\n\t" \
   "s_waitcnt vmcnt(15)\n\t" "ds_write_b128 v20, a[48:51] offset:24576\n\t" "s_mov_b32 s41, 1572864\n\t" \
   "buffer_load_dwordx4 a[96:99], v16, %[ra], s41 offen\n\t" "s_waitcnt vmcnt(15)\n\t" "ds_write_b128 v20, a[52:55] offset:28672\n\t" \
   "s_mov_b32 s41, 1703936\n\t" "buffer_load_dwordx4 a[100:103], v16, %[ra], s41 offen\n\t" "s_waitcnt vmcnt(15)\n\t" \
   "ds_write_b128 v20, a[64:67] offset:32768\n\t" "s_mov_b32 s41, 1835008\n\t" "buffer_load_dwordx4 a[112:115], v16, %[ra], s41 offen\n\t" \
   "s_waitcnt vmcnt(15)\n\t" "ds_write_b128 v20, a[68:71] offset:36864\n\t" "s_mov_b32 s41, 1966080\n\t" \
   "buffer_load_dwordx4 a[116:119], v16, %[ra], s41 offen\n\t" "s_waitcnt lgkmcnt(0)\n\t" "s_barrier\n\t" LDSTAMP(2) \
   "ds_read_b64_tr_b16 a[0:1], v18 offset:0\n\t" "ds_read_b64_tr_b16 a[2:3], v18 offset:4096\n\t" "ds_read_b64_tr_b16 a[4:5], v19 offset:0\n\t" \
   "ds_read_b64_tr_b16 a[6:7], v19 offset:4096\n\t" "ds_read_b64_tr_b16 a[16:17], v18 offset:8192\n\t" \
   "ds_read_b64_tr_b16 a[18:19], v18 offset:12288\n\t" "ds_read_b64_tr_b16 a[20:21], v19 offset:8192\n\t" \
   "ds_read_b64_tr_b16 a[22:23], v19 offset:12288\n\t" "ds_read_b64_tr_b16 a[32:33], v18 offset:16384\n\t" \
   "ds_read_b64_tr_b16 a[34:35], v18 offset:20480\n\t" "ds_read_b64_tr_b16 a[36:37], v19 offset:16384\n\t" \
   "ds_read_b64_tr_b16 a[38:39], v19 offset:20480\n\t" "ds_read_b64_tr_b16 a[48:49], v18 offset:24576\n\t" \
   "ds_read_b64_tr_b16 a[50:51], v18 offset:28672\n\t" "ds_read_b64_tr_b16 a[52:53], v19 offset:24576\n\t" \
   "ds_read_b64_tr_b16 a[54:55], v19 offset:28672\n\t" "ds_read_b64_tr_b16 a[64:65], v18 offset:32768\n\t" \
   "ds_read_b64_tr_b16 a[66:67], v18 offset:36864\n\t" "ds_read_b64_tr_b16 a[68:69], v19 offset:32768\n\t" \
   "ds_read_b64_tr_b16 a[70:71], v19 offset:36864\n\t" "s_waitcnt vmcnt(15)\n\t" "s_waitcnt lgkmcnt(0)\n\t" "s_barrier\n\t" \
   "v_mov_b32 v64, 0x3f803f80\n\t" "v_mov_b32 v65, 0x3f803f80\n\t" "v_mov_b32 v66, 0x3f803f80\n\t" "v_mov_b32 v67, 0x3f803f80\n\t" \
   "ds_write_b128 v20, a[8:11] offset:0\n\t" "s_mov_b32 s41, 2097152\n\t" "buffer_load_dwordx4 a[128:131], v16, %[ra], s41 offen\n\t" \
   "v_mfma_f32_32x32x16_bf16 v[32:47], a[0:3], v[64:67], 0\n\t" "s_waitcnt vmcnt(15)\n\t" "ds_write_b128 v20, a[12:15] offset:4096\n\t" \
   "s_mov_b32 s41, 2228224\n\t" "buffer_load_dwordx4 a[132:135], v16, %[ra], s41 offen\n\t" \
   "v_mfma_f32_32x32x16_bf16 v[32:47], a[4:7], v[64:67], v[32:47]\n\t" "s_waitcnt vmcnt(15)\n\t" "ds_write_b128 v20, a[24:27] offset:8192\n\t" \
   "s_mov_b32 s41, 2359296\n\t" "buffer_load_dwordx4 a[144:147], v16, %[ra], s41 offen\n\t" \
   "v_mfma_f32_32x32x16_bf16 v[32:47], a[16:19], v[64:67], v[32:47]\n\t" "s_waitcnt vmcnt(15)\n\t" "ds_write_b128 v20, a[28:31] offset:12288\n\t" \
   "s_mov_b32 s41, 2490368\n\t" "buffer_load_dwordx4 a[148:151], v16, %[ra], s41 offen\n\t" \
   "v_mfma_f32_32x32x16_bf16 v[32:47], a[20:23], v[64:67], v[32:47]\n\t" "s_waitcnt vmcnt(15)\n\t" "ds_write_b128 v20, a[40:43] offset:16384\n\t" \
   "s_mov_b32 s41, 1310720\n\t" "buffer_load_dwordx4 a[88:91], v16, %[rb], s41 offen\n\t" \
   "v_mfma_f32_32x32x16_bf16 v[32:47], a[32:35], v[64:67], v[32:47]\n\t" "s_waitcnt vmcnt(15)\n\t" "ds_write_b128 v20, a[44:47] offset:20480\n\t" \
   "s_mov_b32 s41, 1441792\n\t" "buffer_load_dwordx4 a[92:95], v16, %[rb], s41 offen\n\t" \
   "v_mfma_f32_32x32x16_bf16 v[32:47], a[36:39], v[64:67], v[32:47]\n\t" "s_waitcnt vmcnt(15)\n\t" "ds_write_b128 v20, a[56:59] offset:24576\n\t" \
   "s_mov_b32 s41, 1572864\n\t" "buffer_load_dwordx4 a[104:107], v16, %[rb], s41 offen\n\t" \
   "v_mfma_f32_32x32x16_bf16 v[32:47], a[48:51], v[64:67], v[32:47]\n\t" "s_waitcnt vmcnt(15)\n\t" "ds_write_b128 v20, a[60:63] offset:28672\n\t" \
   "s_mov_b32 s41, 1703936\n\t" "buffer_load_dwordx4 a[108:111], v16, %[rb], s41 offen\n\t" \
   "v_mfma_f32_32x32x16_bf16 v[32:47], a[52:55], v[64:67], v[32:47]\n\t" "s_waitcnt vmcnt(15)\n\t" "ds_write_b128 v20, a[72:75] offset:32768\n\t" \
   "s_mov_b32 s41, 1835008\n\t" "buffer_load_dwordx4 a[120:123], v16, %[rb], s41 offen\n\t" \
   "v_mfma_f32_32x32x16_bf16 v[32:47], a[64:67], v[64:67], v[32:47]\n\t" "s_waitcnt vmcnt(15)\n\t" "ds_write_b128 v20, a[76:79] offset:36864\n\t" \
   "s_mov_b32 s41, 1966080\n\t" "buffer_load_dwordx4 a[124:127], v16, %[rb], s41 offen\n\t" \
   "v_mfma_f32_32x32x16_bf16 v[32:47], a[68:71], v[64:67], v[32:47]\n\t" "s_waitcnt lgkmcnt(0)\n\t" "s_barrier\n\t" LDSTAMP(3) \
   "ds_read_b64_tr_b16 a[8:9], v18 offset:0\n\t" "ds_read_b64_tr_b16 a[10:11], v18 offset:4096\n\t" "ds_read_b64_tr_b16 a[12:13], v19 offset:0\n\t" \
   "ds_read_b64_tr_b16 a[14:15], v19 offset:4096\n\t" "ds_read_b64_tr_b16 a[24:25], v18 offset:8192\n\t" \
   "ds_read_b64_tr_b16 a[26:27], v18 offset:12288\n\t" "ds_read_b64_tr_b16 a[28:29], v19 offset:8192\n\t" \
   "ds_read_b64_tr_b16 a[30:31], v19 offset:12288\n\t" "ds_read_b64_tr_b16 a[40:41], v18 offset:16384\n\t" \
   "ds_read_b64_tr_b16 a[42:43], v18 offset:20480\n\t" "ds_read_b64_tr_b16 a[44:45], v19 offset:16384\n\t" \
   "ds_read_b64_tr_b16 a[46:47], v19 offset:20480\n\t" "ds_read_b64_tr_b16 a[56:57], v18 offset:24576\n\t" \
   "ds_read_b64_tr_b16 a[58:59], v18 offset:28672\n\t" "ds_read_b64_tr_b16 a[60:61], v19 offset:24576\n\t" \
   "ds_read_b64_tr_b16 a[62:63], v19 offset:28672\n\t" "ds_read_b64_tr_b16 a[72:73], v18 offset:32768\n\t" \
   "ds_read_b64_tr_b16 a[74:75], v18 offset:36864\n\t" "ds_read_b64_tr_b16 a[76:77], v19 offset:32768\n\t" \
   "ds_read_b64_tr_b16 a[78:79], v19 offset:36864\n\t" "s_waitcnt vmcnt(15)\n\t" "s_waitcnt lgkmcnt(0)\n\t" "s_barrier\n\t" \
   "ds_write_b128 v20, a[80:83] offset:0\n\t" "s_mov_b32 s41, 2097152\n\t" "buffer_load_dwordx4 a[136:139], v16, %[rb], s41 offen\n\t" \
   "v_mfma_f32_32x32x16_bf16 v[48:63], a[8:11], v[64:67], 0\n\t" "s_waitcnt vmcnt(15)\n\t" "ds_write_b128 v20, a[84:87] offset:4096\n\t" \
   "s_mov_b32 s41, 2228224\n\t" "buffer_load_dwordx4 a[140:143], v16, %[rb], s41 offen\n\t" \
   "v_mfma_f32_32x32x16_bf16 v[48:63], a[12:15], v[64:67], v[48:63]\n\t" "s_waitcnt vmcnt(15)\n\t" "ds_write_b128 v20, a[96:99] offset:8192\n\t" \
   "s_mov_b32 s41, 2359296\n\t" "buffer_load_dwordx4 a[152:155], v16, %[rb], s41 offen\n\t" \
   "v_mfma_f32_32x32x16_bf16 v[48:63], a[24:27], v[64:67], v[48:63]\n\t" "s_waitcnt vmcnt(15)\n\t" "ds_write_b128 v20, a[100:103] offset:12288\n\t" \
   "s_mov_b32 s41, 2490368\n\t" "buffer_load_dwordx4 a[156:159], v16, %[rb], s41 offen\n\t" \
   "v_mfma_f32_32x32x16_bf16 v[48:63], a[28:31], v[64:67], v[48:63]\n\t" "s_waitcnt vmcnt(15)\n\t" "ds_write_b128 v20, a[112:115] offset:16384\n\t" \
   "s_mov_b32 s41, 2621440\n\t" "buffer_load_dwordx4 a[160:163], v16, %[ra], s41 offen\n\t" \
   "v_mfma_f32_32x32x16_bf16 v[48:63], a[40:43], v[64:67], v[48:63]\n\t" "s_waitcnt vmcnt(15)\n\t" "ds_write_b128 v20, a[116:119] offset:20480\n\t" \
   "s_mov_b32 s41, 2752512\n\t" "buffer_load_dwordx4 a[164:167], v16, %[ra], s41 offen\n\t" \
   "v_mfma_f32_32x32x16_bf16 v[48:63], a[44:47], v[64:67], v[48:63]\n\t" "s_waitcnt vmcnt(15)\n\t" "ds_write_b128 v20, a[128:131] offset:24576\n\t" \
   "s_mov_b32 s41, 2883584\n\t" "buffer_load_dwordx4 a[176:179], v16, %[ra], s41 offen\n\t" \
   "v_mfma_f32_32x32x16_bf16 v[48:63], a[56:59], v[64:67], v[48:63]\n\t" "s_waitcnt vmcnt(15)\n\t" "ds_write_b128 v20, a[132:135] offset:28672\n\t" \
   "s_mov_b32 s41, 3014656\n\t" "buffer_load_dwordx4 a[180:183], v16, %[ra], s41 offen\n\t" \
   "v_mfma_f32_32x32x16_bf16 v[48:63], a[60:63], v[64:67], v[48:63]\n\t" "s_waitcnt vmcnt(15)\n\t" "ds_write_b128 v20, a[144:147] offset:32768\n\t" \
   "s_mov_b32 s41, 3145728\n\t" "buffer_load_dwordx4 a[192:195], v16, %[ra], s41 offen\n\t" \
   "v_mfma_f32_32x32x16_bf16 v[48:63], a[72:75], v[64:67], v[48:63]\n\t" "s_waitcnt vmcnt(15)\n\t" "ds_write_b128 v20, a[148:151] offset:36864\n\t" \
   "s_mov_b32 s41, 3276800\n\t" "buffer_load_dwordx4 a[196:199], v16, %[ra], s41 offen\n\t" \
   "v_mfma_f32_32x32x16_bf16 v[48:63], a[76:79], v[64:67], v[48:63]\n\t" "s_waitcnt lgkmcnt(0)\n\t" "s_barrier\n\t" LDSTAMP(4) \
   "ds_read_b64_tr_b16 a[80:81], v18 offset:0\n\t" "ds_read_b64_tr_b16 a[82:83], v18 offset:4096\n\t" "ds_read_b64_tr_b16 a[84:85], v19 offset:0\n\t" \
   "ds_read_b64_tr_b16 a[86:87], v19 offset:4096\n\t" "ds_read_b64_tr_b16 a[96:97], v18 offset:8192\n\t" \
   "ds_read_b64_tr_b16 a[98:99], v18 offset:12288\n\t" "ds_read_b64_tr_b16 a[100:101], v19 offset:8192\n\t" \
   "ds_read_b64_tr_b16 a[102:103], v19 offset:12288\n\t" "ds_read_b64_tr_b16 a[112:113], v18 offset:16384\n\t" \
   "ds_read_b64_tr_b16 a[114:115], v18 offset:20480\n\t" "ds_read_b64_tr_b16 a[116:117], v19 offset:16384\n\t" \
   "ds_read_b64_tr_b16 a[118:119], v19 offset:20480\n\t" "ds_read_b64_tr_b16 a[128:129], v18 offset:24576\n\t" \
   "ds_read_b64_tr_b16 a[130:131], v18 offset:28672\n\t" "ds_read_b64_tr_b16 a[132:133], v19 offset:24576\n\t" \
   "ds_read_b64_tr_b16 a[134:135], v19 offset:28672\n\t" "ds_read_b64_tr_b16 a[144:145], v18 offset:32768\n\t" \
   "ds_read_b64_tr_b16 a[146:147], v18 offset:36864\n\t" "ds_read_b64_tr_b16 a[148:149], v19 offset:32768\n\t" \
   "ds_read_b64_tr_b16 a[150:151], v19 offset:36864\n\t" "s_waitcnt vmcnt(15)\n\t" "s_waitcnt lgkmcnt(0)\n\t" "s_barrier\n\t" \
   "ds_write_b128 v20, a[88:91] offset:0\n\t" "s_mov_b32 s41, 3407872\n\t" "buffer_load_dwordx4 a[208:211], v16, %[ra], s41 offen\n\t" \
   "v_mfma_f32_32x32x16_bf16 v[32:47], a[80:83], v[64:67], v[32:47]\n\t" "s_waitcnt vmcnt(15)\n\t" "ds_write_b128 v20, a[92:95] offset:4096\n\t" \
   "s_mov_b32 s41, 3538944\n\t" "buffer_load_dwordx4 a[212:215], v16, %[ra], s41 offen\n\t" \
   "v_mfma_f32_32x32x16_bf16 v[32:47], a[84:87], v[64:67], v[32:47]\n\t" "s_waitcnt vmcnt(15)\n\t" "ds_write_b128 v20, a[104:107] offset:8192\n\t" \
   "s_mov_b32 s41, 3670016\n\t" "buffer_load_dwordx4 a[224:227], v16, %[ra], s41 offen\n\t" \
   "v_mfma_f32_32x32x16_bf16 v[32:47], a[96:99], v[64:67], v[32:47]\n\t" "s_waitcnt vmcnt(15)\n\t" "ds_write_b128 v20, a[108:111] offset:12288\n\t" \
   "s_mov_b32 s41, 3801088\n\t" "buffer_load_dwordx4 a[228:231], v16, %[ra], s41 offen\n\t" \
   "v_mfma_f32_32x32x16_bf16 v[32:47], a[100:103], v[64:67], v[32:47]\n\t" "s_waitcnt vmcnt(15)\n\t" "ds_write_b128 v20, a[120:123] offset:16384\n\t" \
   "s_mov_b32 s41, 2621440\n\t" "buffer_load_dwordx4 a[168:171], v16, %[rb], s41 offen\n\t" \
   "v_mfma_f32_32x32x16_bf16 v[32:47], a[112:115], v[64:67], v[32:47]\n\t" "s_waitcnt vmcnt(15)\n\t" "ds_write_b128 v20, a[124:127] offset:20480\n\t" \
   "s_mov_b32 s41, 2752512\n\t" "buffer_load_dwordx4 a[172:175], v16, %[rb], s41 offen\n\t" \
   "v_mfma_f32_32x32x16_bf16 v[32:47], a[116:119], v[64:67], v[32:47]\n\t" "s_waitcnt vmcnt(15)\n\t" "ds_write_b128 v20, a[136:139] offset:24576\n\t" \
   "s_mov_b32 s41, 2883584\n\t" "buffer_load_dwordx4 a[184:187], v16, %[rb], s41 offen\n\t" \
   "v_mfma_f32_32x32x16_bf16 v[32:47], a[128:131], v[64:67], v[32:47]\n\t" "s_waitcnt vmcnt(15)\n\t" "ds_write_b128 v20, a[140:143] offset:28672\n\t" \
   "s_mov_b32 s41, 3014656\n\t" "buffer_load_dwordx4 a[188:191], v16, %[rb], s41 offen\n\t" \
   "v_mfma_f32_32x32x16_bf16 v[32:47], a[132:135], v[64:67], v[32:47]\n\t" "s_waitcnt vmcnt(15)\n\t" "ds_write_b128 v20, a[152:155] offset:32768\n\t" \
   "s_mov_b32 s41, 3145728\n\t" "buffer_load_dwordx4 a[200:203], v16, %[rb], s41 offen\n\t" \
   "v_mfma_f32_32x32x16_bf16 v[32:47], a[144:147], v[64:67], v[32:47]\n\t" "s_waitcnt vmcnt(15)\n\t" "ds_write_b128 v20, a[156:159] offset:36864\n\t" \
   "s_mov_b32 s41, 3276800\n\t" "buffer_load_dwordx4 a[204:207], v16, %[rb], s41 offen\n\t" \
   "v_mfma_f32_32x32x16_bf16 v[32:47], a[148:151], v[64:67], v[32:47]\n\t" "s_waitcnt lgkmcnt(0)\n\t" "s_barrier\n\t" LDSTAMP(5) \
   "ds_read_b64_tr_b16 a[88:89], v18 offset:0\n\t" "ds_read_b64_tr_b16 a[90:91], v18 offset:4096\n\t" "ds_read_b64_tr_b16 a[92:93], v19 offset:0\n\t" \
   "ds_read_b64_tr_b16 a[94:95], v19 offset:4096\n\t" "ds_read_b64_tr_b16 a[104:105], v18 offset:8192\n\t" \
   "ds_read_b64_tr_b16 a[106:107], v18 offset:12288\n\t" "ds_read_b64_tr_b16 a[108:109], v19 offset:8192\n\t" \
   "ds_read_b64_tr_b16 a[110:111], v19 offset:12288\n\t" "ds_read_b64_tr_b16 a[120:121], v18 offset:16384\n\t" \
   "ds_read_b64_tr_b16 a[122:123], v18 offset:20480\n\t" "ds_read_b64_tr_b16 a[124:125], v19 offset:16384\n\t" \
   "ds_read_b64_tr_b16 a[126:127], v19 offset:20480\n\t" "ds_read_b64_tr_b16 a[136:137], v18 offset:24576\n\t" \
   "ds_read_b64_tr_b16 a[138:139], v18 offset:28672\n\t" "ds_read_b64_tr_b16 a[140:141], v19 offset:24576\n\t" \
   "ds_read_b64_tr_b16 a[142:143], v19 offset:28672\n\t" "ds_read_b64_tr_b16 a[152:153], v18 offset:32768\n\t" \
   "ds_read_b64_tr_b16 a[154:155], v18 offset:36864\n\t" "ds_read_b64_tr_b16 a[156:157], v19 offset:32768\n\t" \
   "ds_read_b64_tr_b16 a[158:159], v19 offset:36864\n\t" "s_waitcnt vmcnt(15)\n\t" "s_waitcnt lgkmcnt(0)\n\t" "s_barrier\n\t" \
   "ds_write_b128 v20, a[160:163] offset:0\n\t" "s_mov_b32 s41, 3407872\n\t" "buffer_load_dwordx4 a[216:219], v16, %[rb], s41 offen\n\t" \
   "v_mfma_f32_32x32x16_bf16 v[48:63], a[88:91], v[64:67], v[48:63]\n\t" "s_waitcnt vmcnt(15)\n\t" "ds_write_b128 v20, a[164:167] offset:4096\n\t" \
   "s_mov_b32 s41, 3538944\n\t" "buffer_load_dwordx4 a[220:223], v16, %[rb], s41 offen\n\t" \
   "v_mfma_f32_32x32x16_bf16 v[48:63], a[92:95], v[64:67], v[48:63]\n\t" "s_waitcnt vmcnt(15)\n\t" "ds_write_b128 v20, a[176:179] offset:8192\n\t" \
   "s_mov_b32 s41, 3670016\n\t" "buffer_load_dwordx4 a[232:235], v16, %[rb], s41 offen\n\t" \
   "v_mfma_f32_32x32x16_bf16 v[48:63], a[104:107], v[64:67], v[48:63]\n\t" "s_waitcnt vmcnt(15)\n\t" "ds_write_b128 v20, a[180:183] offset:12288\n\t" \
   "s_mov_b32 s41, 3801088\n\t" "buffer_load_dwordx4 a[236:239], v16, %[rb], s41 offen\n\t" \
   "v_mfma_f32_32x32x16_bf16 v[48:63], a[108:111], v[64:67], v[48:63]\n\t" "s_waitcnt vmcnt(15)\n\t" "ds_write_b128 v20, a[192:195] offset:16384\n\t" \
   "s_mov_b32 s41, 3932160\n\t" "buffer_load_dwordx4 a[240:243], v16, %[ra], s41 offen\n\t" \
   "v_mfma_f32_32x32x16_bf16 v[48:63], a[120:123], v[64:67], v[48:63]\n\t" "s_waitcnt vmcnt(15)\n\t" "ds_write_b128 v20, a[196:199] offset:20480\n\t" \
   "s_mov_b32 s41, 4063232\n\t" "buffer_load_dwordx4 a[244:247], v16, %[ra], s41 offen\n\t" \
   "v_mfma_f32_32x32x16_bf16 v[48:63], a[124:127], v[64:67], v[48:63]\n\t" "s_waitcnt vmcnt(15)\n\t" "ds_write_b128 v20, a[208:211] offset:24576\n\t" \
   "s_mov_b32 s41, 4194304\n\t" "buffer_load_dwordx4 v[192:195], v16, %[ra], s41 offen\n\t" \
   "v_mfma_f32_32x32x16_bf16 v[48:63], a[136:139], v[64:67], v[48:63]\n\t" "s_waitcnt vmcnt(15)\n\t" "ds_write_b128 v20, a[212:215] offset:28672\n\t" \
   "s_mov_b32 s41, 4325376\n\t" "buffer_load_dwordx4 v[196:199], v16, %[ra], s41 offen\n\t" \
   "v_mfma_f32_32x32x16_bf16 v[48:63], a[140:143], v[64:67], v[48:63]\n\t" "s_waitcnt vmcnt(15)\n\t" "ds_write_b128 v20, a[224:227] offset:32768\n\t" \
   "s_mov_b32 s41, 4456448\n\t" "buffer_load_dwordx4 v[208:211], v16, %[ra], s41 offen\n\t" \
   "v_mfma_f32_32x32x16_bf16 v[48:63], a[152:155], v[64:67], v[48:63]\n\t" "s_waitcnt vmcnt(15)\n\t" "ds_write_b128 v20, a[228:231] offset:36864\n\t" \
   "s_mov_b32 s41, 4587520\n\t" "buffer_load_dwordx4 v[212:215], v16, %[ra], s41 offen\n\t" \
   "v_mfma_f32_32x32x16_bf16 v[48:63], a[156:159], v[64:67], v[48:63]\n\t" "s_waitcnt lgkmcnt(0)\n\t" "s_barrier\n\t" LDSTAMP(6) \
   "ds_read_b64_tr_b16 a[160:161], v18 offset:0\n\t" "ds_read_b64_tr_b16 a[162:163], v18 offset:4096\n\t" \
   "ds_read_b64_tr_b16 a[164:165], v19 offset:0\n\t" "ds_read_b64_tr_b16 a[166:167], v19 offset:4096\n\t" \
   "ds_read_b64_tr_b16 a[176:177], v18 offset:8192\n\t" "ds_read_b64_tr_b16 a[178:179], v18 offset:12288\n\t" \
   "ds_read_b64_tr_b16 a[180:181], v19 offset:8192\n\t" "ds_read_b64_tr_b16 a[182:183], v19 offset:12288\n\t" \
   "ds_read_b64_tr_b16 a[192:193], v18 offset:16384\n\t" "ds_read_b64_tr_b16 a[194:195], v18 offset:20480\n\t" \
   "ds_read_b64_tr_b16 a[196:197], v19 offset:16384\n\t" "ds_read_b64_tr_b16 a[198:199], v19 offset:20480\n\t" \
   "ds_read_b64_tr_b16 a[208:209], v18 offset:24576\n\t" "ds_read_b64_tr_b16 a[210:211], v18 offset:28672\n\t" \
   "ds_read_b64_tr_b16 a[212:213], v19 offset:24576\n\t" "ds_read_b64_tr_b16 a[214:215], v19 offset:28672\n\t" \
   "ds_read_b64_tr_b16 a[224:225], v18 offset:32768\n\t" "ds_read_b64_tr_b16 a[226:227], v18 offset:36864\n\t" \
   "ds_read_b64_tr_b16 a[228:229], v19 offset:32768\n\t" "ds_read_b64_tr_b16 a[230:231], v19 offset:36864\n\t" "s_waitcnt vmcnt(15)\n\t" \
   "s_waitcnt lgkmcnt(0)\n\t" "s_barrier\n\t" "ds_write_b128 v20, a[168:171] offset:0\n\t" "s_mov_b32 s41, 4718592\n\t" \
   "buffer_load_dwordx4 v[224:227], v16, %[ra], s41 offen\n\t" "v_mfma_f32_32x32x16_bf16 v[32:47], a[160:163], v[64:67], v[32:47]\n\t" \
   "s_waitcnt vmcnt(15)\n\t" "ds_write_b128 v20, a[172:175] offset:4096\n\t" "s_mov_b32 s41, 4849664\n\t" \
   "buffer_load_dwordx4 v[228:231], v16, %[ra], s41 offen\n\t" "v_mfma_f32_32x32x16_bf16 v[32:47], a[164:167], v[64:67], v[32:47]\n\t" \
   "s_waitcnt vmcnt(15)\n\t" "ds_write_b128 v20, a[184:187] offset:8192\n\t" "s_mov_b32 s41, 4980736\n\t" \
   "buffer_load_dwordx4 v[240:243], v16, %[ra], s41 offen\n\t" "v_mfma_f32_32x32x16_bf16 v[32:47], a[176:179], v[64:67], v[32:47]\n\t" \
   "s_waitcnt vmcnt(15)\n\t" "ds_write_b128 v20, a[188:191] offset:12288\n\t" "s_mov_b32 s41, 5111808\n\t" \
   "buffer_load_dwordx4 v[244:247], v16, %[ra], s41 offen\n\t" "v_mfma_f32_32x32x16_bf16 v[32:47], a[180:183], v[64:67], v[32:47]\n\t" \
   "s_waitcnt vmcnt(15)\n\t" "ds_write_b128 v20, a[200:203] offset:16384\n\t" "s_mov_b32 s41, 3932160\n\t" \
   "buffer_load_dwordx4 a[248:251], v16, %[rb], s41 offen\n\t" "v_mfma_f32_32x32x16_bf16 v[32:47], a[192:195], v[64:67], v[32:47]\n\t" \
   "s_waitcnt vmcnt(15)\n\t" "ds_write_b128 v20, a[204:207] offset:20480\n\t" "s_mov_b32 s41, 4063232\n\t" \
   "buffer_load_dwordx4 a[252:255], v16, %[rb], s41 offen\n\t" "v_mfma_f32_32x32x16_bf16 v[32:47], a[196:199], v[64:67], v[32:47]\n\t" \
   "s_waitcnt vmcnt(15)\n\t" "ds_write_b128 v20, a[216:219] offset:24576\n\t" "s_mov_b32 s41, 4194304\n\t" \
   "buffer_load_dwordx4 v[200:203], v16, %[rb], s41 offen\n\t" "v_mfma_f32_32x32x16_bf16 v[32:47], a[208:211], v[64:67], v[32:47]\n\t" \
   "s_waitcnt vmcnt(15)\n\t" "ds_write_b128 v20, a[220:223] offset:28672\n\t" "s_mov_b32 s41, 4325376\n\t" \
   "buffer_load_dwordx4 v[204:207], v16, %[rb], s41 offen\n\t" "v_mfma_f32_32x32x16_bf16 v[32:47], a[212:215], v[64:67], v[32:47]\n\t" \
   "s_waitcnt vmcnt(15)\n\t" "ds_write_b128 v20, a[232:235] offset:32768\n\t" "s_mov_b32 s41, 4456448\n\t" \
   "buffer_load_dwordx4 v[216:219], v16, %[rb], s41 offen\n\t" "v_mfma_f32_32x32x16_bf16 v[32:47], a[224:227], v[64:67], v[32:47]\n\t" \
   "s_waitcnt vmcnt(15)\n\t" "ds_write_b128 v20, a[236:239] offset:36864\n\t" "s_mov_b32 s41, 4587520\n\t" \
   "buffer_load_dwordx4 v[220:223], v16, %[rb], s41 offen\n\t" "v_mfma_f32_32x32x16_bf16 v[32:47], a[228:231], v[64:67], v[32:47]\n\t" \
   "s_waitcnt lgkmcnt(0)\n\t" "s_barrier\n\t" LDSTAMP(7) "ds_read_b64_tr_b16 a[168:169], v18 offset:0\n\t" \
   "ds_read_b64_tr_b16 a[170:171], v18 offset:4096\n\t" "ds_read_b64_tr_b16 a[172:173], v19 offset:0\n\t" \
   "ds_read_b64_tr_b16 a[174:175], v19 offset:4096\n\t" "ds_read_b64_tr_b16 a[184:185], v18 offset:8192\n\t" \
   "ds_read_b64_tr_b16 a[186:187], v18 offset:12288\n\t" "ds_read_b64_tr_b16 a[188:189], v19 offset:8192\n\t" \
   "ds_read_b64_tr_b16 a[190:191], v19 offset:12288\n\t" "ds_read_b64_tr_b16 a[200:201], v18 offset:16384\n\t" \
   "ds_read_b64_tr_b16 a[202:203], v18 offset:20480\n\t" "ds_read_b64_tr_b16 a[204:205], v19 offset:16384\n\t" \
   "ds_read_b64_tr_b16 a[206:207], v19 offset:20480\n\t" "ds_read_b64_tr_b16 a[216:217], v18 offset:24576\n\t" \
   "ds_read_b64_tr_b16 a[218:219], v18 offset:28672\n\t" "ds_read_b64_tr_b16 a[220:221], v19 offset:24576\n\t" \
   "ds_read_b64_tr_b16 a[222:223], v19 offset:28672\n\t" "ds_read_b64_tr_b16 a[232:233], v18 offset:32768\n\t" \
   "ds_read_b64_tr_b16 a[234:235], v18 offset:36864\n\t" "ds_read_b64_tr_b16 a[236:237], v19 offset:32768\n\t" \
   "ds_read_b64_tr_b16 a[238:239], v19 offset:36864\n\t" "s_waitcnt vmcnt(15)\n\t" "s_waitcnt lgkmcnt(0)\n\t" "s_barrier\n\t" \
   "ds_write_b128 v20, a[240:243] offset:0\n\t" "s_mov_b32 s41, 4718592\n\t" "buffer_load_dwordx4 v[232:235], v16, %[rb], s41 offen\n\t" \
   "v_mfma_f32_32x32x16_bf16 v[48:63], a[168:171], v[64:67], v[48:63]\n\t" "s_waitcnt vmcnt(15)\n\t" "ds_write_b128 v20, a[244:247] offset:4096\n\t" \
   "s_mov_b32 s41, 4849664\n\t" "buffer_load_dwordx4 v[236:239], v16, %[rb], s41 offen\n\t" \
   "v_mfma_f32_32x32x16_bf16 v[48:63], a[172:175], v[64:67], v[48:63]\n\t" "s_waitcnt vmcnt(15)\n\t" "ds_write_b128 v20, v[192:195] offset:8192\n\t" \
   "s_mov_b32 s41, 4980736\n\t" "buffer_load_dwordx4 v[248:251], v16, %[rb], s41 offen\n\t" \
   "v_mfma_f32_32x32x16_bf16 v[48:63], a[184:187], v[64:67], v[48:63]\n\t" "s_waitcnt vmcnt(15)\n\t" "ds_write_b128 v20, v[196:199] offset:12288\n\t" \
   "s_mov_b32 s41, 5111808\n\t" "buffer_load_dwordx4 v[252:255], v16, %[rb], s41 offen\n\t" \
   "v_mfma_f32_32x32x16_bf16 v[48:63], a[188:191], v[64:67], v[48:63]\n\t" "s_waitcnt vmcnt(15)\n\t" "ds_write_b128 v20, v[208:211] offset:16384\n\t" \
   "v_mfma_f32_32x32x16_bf16 v[48:63], a[200:203], v[64:67], v[48:63]\n\t" "s_waitcnt vmcnt(14)\n\t" "ds_write_b128 v20, v[212:215] offset:20480\n\t" \
   "v_mfma_f32_32x32x16_bf16 v[48:63], a[204:207], v[64:67], v[48:63]\n\t" "s_waitcnt vmcnt(13)\n\t" "ds_write_b128 v20, v[224:227] offset:24576\n\t" \
   "v_mfma_f32_32x32x16_bf16 v[48:63], a[216:219], v[64:67], v[48:63]\n\t" "s_waitcnt vmcnt(12)\n\t" "ds_write_b128 v20, v[228:231] offset:28672\n\t" \
   "v_mfma_f32_32x32x16_bf16 v[48:63], a[220:223], v[64:67], v[48:63]\n\t" "s_waitcnt vmcnt(11)\n\t" "ds_write_b128 v20, v[240:243] offset:32768\n\t" \
   "v_mfma_f32_32x32x16_bf16 v[48:63], a[232:235], v[64:67], v[48:63]\n\t" "s_waitcnt vmcnt(10)\n\t" "ds_write_b128 v20, v[244:247] offset:36864\n\t" \
   "v_mfma_f32_32x32x16_bf16 v[48:63], a[236:239], v[64:67], v[48:63]\n\t" "buffer_load_dwordx4 v[28:31], v112, %[rw], 0 offen sc1\n\t" \
   "s_waitcnt lgkmcnt(0)\n\t" "s_barrier\n\t" LDSTAMP(8) "ds_read_b64_tr_b16 a[240:241], v18 offset:0\n\t" \
   "ds_read_b64_tr_b16 a[242:243], v18 offset:4096\n\t" "ds_read_b64_tr_b16 a[244:245], v19 offset:0\n\t" \
   "ds_read_b64_tr_b16 a[246:247], v19 offset:4096\n\t" "ds_read_b64_tr_b16 v[192:193], v18 offset:8192\n\t" \
   "ds_read_b64_tr_b16 v[194:195], v18 offset:12288\n\t" "ds_read_b64_tr_b16 v[196:197], v19 offset:8192\n\t" \
   "ds_read_b64_tr_b16 v[198:199], v19 offset:12288\n\t" "ds_read_b64_tr_b16 v[208:209], v18 offset:16384\n\t" \
   "ds_read_b64_tr_b16 v[210:211], v18 offset:20480\n\t" "ds_read_b64_tr_b16 v[212:213], v19 offset:16384\n\t" \
   "ds_read_b64_tr_b16 v[214:215], v19 offset:20480\n\t" "ds_read_b64_tr_b16 v[224:225], v18 offset:24576\n\t" \
   "ds_read_b64_tr_b16 v[226:227], v18 offset:28672\n\t" "ds_read_b64_tr_b16 v[228:229], v19 offset:24576\n\t" \
   "ds_read_b64_tr_b16 v[230:231], v19 offset:28672\n\t" "ds_read_b64_tr_b16 v[240:241], v18 offset:32768\n\t" \
   "ds_read_b64_tr_b16 v[242:243], v18 offset:36864\n\t" "ds_read_b64_tr_b16 v[244:245], v19 offset:32768\n\t" \
   "ds_read_b64_tr_b16 v[246:247], v19 offset:36864\n\t" "s_waitcnt vmcnt(10)\n\t" "s_waitcnt lgkmcnt(0)\n\t" "s_barrier\n\t" \
   "ds_write_b128 v20, a[248:251] offset:0\n\t" "v_mfma_f32_32x32x16_bf16 v[32:47], a[240:243], v[64:67], v[32:47]\n\t" "s_waitcnt vmcnt(9)\n\t" \
   "ds_write_b128 v20, a[252:255] offset:4096\n\t" "v_mfma_f32_32x32x16_bf16 v[32:47], a[244:247], v[64:67], v[32:47]\n\t" "s_waitcnt vmcnt(8)\n\t" \
   "ds_write_b128 v20, v[200:203] offset:8192\n\t" "v_mfma_f32_32x32x16_bf16 v[32:47], v[192:195], v[64:67], v[32:47]\n\t" "s_waitcnt vmcnt(7)\n\t" \
   "ds_write_b128 v20, v[204:207] offset:12288\n\t" "v_mfma_f32_32x32x16_bf16 v[32:47], v[196:199], v[64:67], v[32:47]\n\t" "s_waitcnt vmcnt(6)\n\t" \
   "ds_write_b128 v20, v[216:219] offset:16384\n\t" "v_mfma_f32_32x32x16_bf16 v[32:47], v[208:211], v[64:67], v[32:47]\n\t" "s_waitcnt vmcnt(5)\n\t" \
   "ds_write_b128 v20, v[220:223] offset:20480\n\t" "v_mfma_f32_32x32x16_bf16 v[32:47], v[212:215], v[64:67], v[32:47]\n\t" "s_waitcnt vmcnt(4)\n\t" \
   "ds_write_b128 v20, v[232:235] offset:24576\n\t" "v_mfma_f32_32x32x16_bf16 v[32:47], v[224:227], v[64:67], v[32:47]\n\t" "s_waitcnt vmcnt(3)\n\t" \
   "ds_write_b128 v20, v[236:239] offset:28672\n\t" "v_mfma_f32_32x32x16_bf16 v[32:47], v[228:231], v[64:67], v[32:47]\n\t" "s_waitcnt vmcnt(2)\n\t" \
   "ds_write_b128 v20, v[248:251] offset:32768\n\t" "v_mfma_f32_32x32x16_bf16 v[32:47], v[240:243], v[64:67], v[32:47]\n\t" "s_waitcnt vmcnt(1)\n\t" \
   "ds_write_b128 v20, v[252:255] offset:36864\n\t" "v_mfma_f32_32x32x16_bf16 v[32:47], v[244:247], v[64:67], v[32:47]\n\t" "s_waitcnt lgkmcnt(0)\n\t" \
   "s_barrier\n\t" LDSTAMP(9) "ds_read_b64_tr_b16 a[248:249], v18 offset:0\n\t" "ds_read_b64_tr_b16 a[250:251], v18 offset:4096\n\t" \
   "ds_read_b64_tr_b16 a[252:253], v19 offset:0\n\t" "ds_read_b64_tr_b16 a[254:255], v19 offset:4096\n\t" \
   "ds_read_b64_tr_b16 v[200:201], v18 offset:8192\n\t" "ds_read_b64_tr_b16 v[202:203], v18 offset:12288\n\t" \
   "ds_read_b64_tr_b16 v[204:205], v19 offset:8192\n\t" "ds_read_b64_tr_b16 v[206:207], v19 offset:12288\n\t" \
   "ds_read_b64_tr_b16 v[216:217], v18 offset:16384\n\t" "ds_read_b64_tr_b16 v[218:219], v18 offset:20480\n\t" \
   "ds_read_b64_tr_b16 v[220:221], v19 offset:16384\n\t" "ds_read_b64_tr_b16 v[222:223], v19 offset:20480\n\t" \
   "ds_read_b64_tr_b16 v[232:233], v18 offset:24576\n\t" "ds_read_b64_tr_b16 v[234:235], v18 offset:28672\n\t" \
   "ds_read_b64_tr_b16 v[236:237], v19 offset:24576\n\t" "ds_read_b64_tr_b16 v[238:239], v19 offset:28672\n\t" \
   "ds_read_b64_tr_b16 v[248:249], v18 offset:32768\n\t" "ds_read_b64_tr_b16 v[250:251], v18 offset:36864\n\t" \
   "ds_read_b64_tr_b16 v[252:253], v19 offset:32768\n\t" "ds_read_b64_tr_b16 v[254:255], v19 offset:36864\n\t" "s_waitcnt vmcnt(0) lgkmcnt(0)\n\t" \
   "v_mfma_f32_32x32x16_bf16 v[48:63], a[248:251], v[64:67], v[48:63]\n\t" "v_mfma_f32_32x32x16_bf16 v[48:63], a[252:255], v[64:67], v[48:63]\n\t" \
   "v_mfma_f32_32x32x16_bf16 v[48:63], v[200:203], v[64:67], v[48:63]\n\t" "v_mfma_f32_32x32x16_bf16 v[48:63], v[204:207], v[64:67], v[48:63]\n\t" \
   "v_mfma_f32_32x32x16_bf16 v[48:63], v[216:219], v[64:67], v[48:63]\n\t" "v_mfma_f32_32x32x16_bf16 v[48:63], v[220:223], v[64:67], v[48:63]\n\t" \
   "v_mfma_f32_32x32x16_bf16 v[48:63], v[232:235], v[64:67], v[48:63]\n\t" "v_mfma_f32_32x32x16_bf16 v[48:63], v[236:239], v[64:67], v[48:63]\n\t" \
   "v_mfma_f32_32x32x16_bf16 v[48:63], v[248:251], v[64:67], v[48:63]\n\t" "v_mfma_f32_32x32x16_bf16 v[48:63], v[252:255], v[64:67], v[48:63]\n\t" \
   "s_mov_b32 exec_lo, 1\n\t" "s_mov_b32 exec_hi, 1\n\t" "s_nop 7\n\t" "s_nop 3\n\t" "ds_write_b128 v27, v[32:35] offset:0\n\t" \
   "ds_write_b128 v27, v[36:39] offset:16\n\t" "ds_write_b128 v27, v[40:43] offset:32\n\t" "ds_write_b128 v27, v[44:47] offset:48\n\t" \
   "ds_write_b128 v27, v[48:51] offset:64\n\t" "ds_write_b128 v27, v[52:55] offset:80\n\t" "ds_write_b128 v27, v[56:59] offset:96\n\t" \
   "ds_write_b128 v27, v[60:63] offset:112\n\t" "s_mov_b64 exec, -1\n\t" "s_waitcnt lgkmcnt(0)\n\t" "s_barrier"
#define LDK_CLOB "v24", "v25", "v26", "v32", "v33", "v34", "v35", "v36", "v37", "v38", "v39", "v40", "v41", "v42", "v43", "v44", "v45", "v46", "v47", "v48", "v49", "v50", "v51", "v52", "v53", "v54", "v55", "v56", "v57", "v58", "v59", "v60", "v61", "v62", "v63", "v64", "v65", "v66", "v67", "v68", "v69", "v70", "v71", "v72", "v73", "v74", "v75", "v76", "v77", "v78", "v79", "v80", "v81", "v82", "v83", "v84", "v85", "v86", "v87", "v88", "v89", "v90", "v91", "v92", "v93", "v94", "v95", "v96", "v97", "v98", "v99", "v100", "v101", "v102", "v103", "v104", "v105", "v106", "v107", "v108", "v109", "v110", "v111", "v120", "v121", "v122", "v123", "s40", "s41"
#define LDV_ASM \
   "buffer_load_dword v26, v113, %[rw], 0 offen sc1\n\t" "s_mov_b32 s41, 0\n\t" "buffer_load_dwordx4 v[32:35], v116, %[rx], s41 offen\n\t" \
   "s_mov_b32 s41, 10240\n\t" "buffer_load_dwordx4 v[36:39], v116, %[rx], s41 offen\n\t" "s_mov_b32 s41, 128\n\t" \
   "buffer_load_dwordx4 v[40:43], v116, %[rx], s41 offen\n\t" "s_mov_b32 s41, 10368\n\t" "buffer_load_dwordx4 v[44:47], v116, %[rx], s41 offen\n\t" \
   "s_mov_b32 s41, 256\n\t" "buffer_load_dwordx4 v[48:51], v116, %[rx], s41 offen\n\t" "s_mov_b32 s41, 10496\n\t" \
   "buffer_load_dwordx4 v[52:55], v116, %[rx], s41 offen\n\t" "s_mov_b32 s41, 384\n\t" "buffer_load_dwordx4 v[56:59], v116, %[rx], s41 offen\n\t" \
   "s_mov_b32 s41, 10624\n\t" "buffer_load_dwordx4 v[60:63], v116, %[rx], s41 offen\n\t" "s_mov_b32 s41, 512\n\t" \
   "buffer_load_dwordx4 v[64:67], v116, %[rx], s41 offen\n\t" "s_mov_b32 s41, 10752\n\t" "buffer_load_dwordx4 v[68:71], v116, %[rx], s41 offen\n\t" \
   "s_mov_b32 s41, 640\n\t" "buffer_load_dwordx4 v[72:75], v116, %[rx], s41 offen\n\t" "s_mov_b32 s41, 10880\n\t" \
   "buffer_load_dwordx4 v[76:79], v116, %[rx], s41 offen\n\t" "s_mov_b32 s41, 768\n\t" "buffer_load_dwordx4 v[80:83], v116, %[rx], s41 offen\n\t" \
   "s_mov_b32 s41, 11008\n\t" "buffer_load_dwordx4 v[84:87], v116, %[rx], s41 offen\n\t" "s_mov_b32 s41, 896\n\t" \
   "buffer_load_dwordx4 v[88:91], v116, %[rx], s41 offen\n\t" "s_mov_b32 s41, 11136\n\t" "buffer_load_dwordx4 v[92:95], v116, %[rx], s41 offen\n\t" \
   "s_mov_b32 s40, 0x3f803f80\n\t" "v_mov_b32 v120, 0\n\t" "v_mov_b32 v121, 0\n\t" "v_mov_b32 v122, 0\n\t" "v_mov_b32 v123, 0\n\t" \
   "s_waitcnt vmcnt(16)\n\t" "v_add_u32 v26, 1, v26\n\t" "buffer_store_dword v26, v114, %[rw], 0 offen sc1\n\t" "ds_write_b32 v115, v26\n\t" LDSTAMP(0) \
   "s_waitcnt vmcnt(15)\n\t" LDSTAMP(1) "ds_write_b64 v117, v[32:33] offset:0\n\t" "ds_write_b64 v117, v[34:35] offset:256\n\t" \
   "s_mov_b32 s41, 1024\n\t" "buffer_load_dwordx4 v[96:99], v116, %[rx], s41 offen\n\t" "v_dot2c_f32_bf16 v120, s40, v32\n\t" \
   "v_dot2c_f32_bf16 v121, v32, v32\n\t" "v_dot2c_f32_bf16 v120, s40, v33\n\t" "v_dot2c_f32_bf16 v121, v33, v33\n\t" \
   "v_dot2c_f32_bf16 v120, s40, v34\n\t" "v_dot2c_f32_bf16 v121, v34, v34\n\t" "v_dot2c_f32_bf16 v120, s40, v35\n\t" \
   "v_dot2c_f32_bf16 v121, v35, v35\n\t" "s_waitcnt vmcnt(15)\n\t" "ds_write_b64 v117, v[36:37] offset:128\n\t" \
   "ds_write_b64 v117, v[38:39] offset:384\n\t" "s_mov_b32 s41, 11264\n\t" "buffer_load_dwordx4 v[100:103], v116, %[rx], s41 offen\n\t" \
   "v_dot2c_f32_bf16 v122, s40, v36\n\t" "v_dot2c_f32_bf16 v123, v36, v36\n\t" "v_dot2c_f32_bf16 v122, s40, v37\n\t" \
   "v_dot2c_f32_bf16 v123, v37, v37\n\t" "v_dot2c_f32_bf16 v122, s40, v38\n\t" "v_dot2c_f32_bf16 v123, v38, v38\n\t" \
   "v_dot2c_f32_bf16 v122, s40, v39\n\t" "v_dot2c_f32_bf16 v123, v39, v39\n\t" "s_waitcnt vmcnt(15)\n\t" "ds_write_b64 v117, v[40:41] offset:2048\n\t" \
   "ds_write_b64 v117, v[42:43] offset:2304\n\t" "s_mov_b32 s41, 1152\n\t" "buffer_load_dwordx4 v[104:107], v116, %[rx], s41 offen\n\t" \
   "v_dot2c_f32_bf16 v120, s40, v40\n\t" "v_dot2c_f32_bf16 v121, v40, v40\n\t" "v_dot2c_f32_bf16 v120, s40, v41\n\t" \
   "v_dot2c_f32_bf16 v121, v41, v41\n\t" "v_dot2c_f32_bf16 v120, s40, v42\n\t" "v_dot2c_f32_bf16 v121, v42, v42\n\t" \
   "v_dot2c_f32_bf16 v120, s40, v43\n\t" "v_dot2c_f32_bf16 v121, v43, v43\n\t" "s_waitcnt vmcnt(15)\n\t" "ds_write_b64 v117, v[44:45] offset:2176\n\t" \
   "ds_write_b64 v117, v[46:47] offset:2432\n\t" "s_mov_b32 s41, 11392\n\t" "buffer_load_dwordx4 v[108:111], v116, %[rx], s41 offen\n\t" \
   "v_dot2c_f32_bf16 v122, s40, v44\n\t" "v_dot2c_f32_bf16 v123, v44, v44\n\t" "v_dot2c_f32_bf16 v122, s40, v45\n\t" \
   "v_dot2c_f32_bf16 v123, v45, v45\n\t" "v_dot2c_f32_bf16 v122, s40, v46\n\t" "v_dot2c_f32_bf16 v123, v46, v46\n\t" \
   "v_dot2c_f32_bf16 v122, s40, v47\n\t" "v_dot2c_f32_bf16 v123, v47, v47\n\t" "s_waitcnt vmcnt(15)\n\t" "ds_write_b64 v117, v[48:49] offset:4096\n\t" \
   "ds_write_b64 v117, v[50:51] offset:4352\n\t" "s_mov_b32 s41, 0\n\t" "buffer_load_dwordx4 a[0:3], v16, %[ra], s41 offen\n\t" \
   "v_dot2c_f32_bf16 v120, s40, v48\n\t" "v_dot2c_f32_bf16 v121, v48, v48\n\t" "v_dot2c_f32_bf16 v120, s40, v49\n\t" \
   "v_dot2c_f32_bf16 v121, v49, v49\n\t" "v_dot2c_f32_bf16 v120, s40, v50\n\t" "v_dot2c_f32_bf16 v121, v50, v50\n\t" \
   "v_dot2c_f32_bf16 v120, s40, v51\n\t" "v_dot2c_f32_bf16 v121, v51, v51\n\t" "s_waitcnt vmcnt(15)\n\t" "ds_write_b64 v117, v[52:53] offset:4224\n\t" \
   "ds_write_b64 v117, v[54:55] offset:4480\n\t" "s_mov_b32 s41, 131072\n\t" "buffer_load_dwordx4 a[4:7], v16, %[ra], s41 offen\n\t" \
   "v_dot2c_f32_bf16 v122, s40, v52\n\t" "v_dot2c_f32_bf16 v123, v52, v52\n\t" "v_dot2c_f32_bf16 v122, s40, v53\n\t" \
   "v_dot2c_f32_bf16 v123, v53, v53\n\t" "v_dot2c_f32_bf16 v122, s40, v54\n\t" "v_dot2c_f32_bf16 v123, v54, v54\n\t" \
   "v_dot2c_f32_bf16 v122, s40, v55\n\t" "v_dot2c_f32_bf16 v123, v55, v55\n\t" "s_waitcnt vmcnt(15)\n\t" "ds_write_b64 v117, v[56:57] offset:6144\n\t" \
   "ds_write_b64 v117, v[58:59] offset:6400\n\t" "s_mov_b32 s41, 262144\n\t" "buffer_load_dwordx4 a[8:11], v16, %[ra], s41 offen\n\t" \
   "v_dot2c_f32_bf16 v120, s40, v56\n\t" "v_dot2c_f32_bf16 v121, v56, v56\n\t" "v_dot2c_f32_bf16 v120, s40, v57\n\t" \
   "v_dot2c_f32_bf16 v121, v57, v57\n\t" "v_dot2c_f32_bf16 v120, s40, v58\n\t" "v_dot2c_f32_bf16 v121, v58, v58\n\t" \
   "v_dot2c_f32_bf16 v120, s40, v59\n\t" "v_dot2c_f32_bf16 v121, v59, v59\n\t" "s_waitcnt vmcnt(15)\n\t" "ds_write_b64 v117, v[60:61] offset:6272\n\t" \
   "ds_write_b64 v117, v[62:63] offset:6528\n\t" "s_mov_b32 s41, 393216\n\t" "buffer_load_dwordx4 a[12:15], v16, %[ra], s41 offen\n\t" \
   "v_dot2c_f32_bf16 v122, s40, v60\n\t" "v_dot2c_f32_bf16 v123, v60, v60\n\t" "v_dot2c_f32_bf16 v122, s40, v61\n\t" \
   "v_dot2c_f32_bf16 v123, v61, v61\n\t" "v_dot2c_f32_bf16 v122, s40, v62\n\t" "v_dot2c_f32_bf16 v123, v62, v62\n\t" \
   "v_dot2c_f32_bf16 v122, s40, v63\n\t" "v_dot2c_f32_bf16 v123, v63, v63\n\t" "s_waitcnt vmcnt(15)\n\t" "ds_write_b64 v117, v[64:65] offset:8192\n\t" \
   "ds_write_b64 v117, v[66:67] offset:8448\n\t" "s_mov_b32 s41, 524288\n\t" "buffer_load_dwordx4 a[16:19], v16, %[ra], s41 offen\n\t" \
   "v_dot2c_f32_bf16 v120, s40, v64\n\t" "v_dot2c_f32_bf16 v121, v64, v64\n\t" "v_dot2c_f32_bf16 v120, s40, v65\n\t" \
   "v_dot2c_f32_bf16 v121, v65, v65\n\t" "v_dot2c_f32_bf16 v120, s40, v66\n\t" "v_dot2c_f32_bf16 v121, v66, v66\n\t" \
   "v_dot2c_f32_bf16 v120, s40, v67\n\t" "v_dot2c_f32_bf16 v121, v67, v67\n\t" "s_waitcnt vmcnt(15)\n\t" "ds_write_b64 v117, v[68:69] offset:8320\n\t" \
   "ds_write_b64 v117, v[70:71] offset:8576\n\t" "s_mov_b32 s41, 655360\n\t" "buffer_load_dwordx4 a[20:23], v16, %[ra], s41 offen\n\t" \
   "v_dot2c_f32_bf16 v122, s40, v68\n\t" "v_dot2c_f32_bf16 v123, v68, v68\n\t" "v_dot2c_f32_bf16 v122, s40, v69\n\t" \
   "v_dot2c_f32_bf16 v123, v69, v69\n\t" "v_dot2c_f32_bf16 v122, s40, v70\n\t" "v_dot2c_f32_bf16 v123, v70, v70\n\t" \
   "v_dot2c_f32_bf16 v122, s40, v71\n\t" "v_dot2c_f32_bf16 v123, v71, v71\n\t" "s_waitcnt vmcnt(15)\n\t" "ds_write_b64 v117, v[72:73] offset:10240\n\t" \
   "ds_write_b64 v117, v[74:75] offset:10496\n\t" "s_mov_b32 s41, 786432\n\t" "buffer_load_dwordx4 a[24:27], v16, %[ra], s41 offen\n\t" \
   "v_dot2c_f32_bf16 v120, s40, v72\n\t" "v_dot2c_f32_bf16 v121, v72, v72\n\t" "v_dot2c_f32_bf16 v120, s40, v73\n\t" \
   "v_dot2c_f32_bf16 v121, v73, v73\n\t" "v_dot2c_f32_bf16 v120, s40, v74\n\t" "v_dot2c_f32_bf16 v121, v74, v74\n\t" \
   "v_dot2c_f32_bf16 v120, s40, v75\n\t" "v_dot2c_f32_bf16 v121, v75, v75\n\t" "s_waitcnt vmcnt(15)\n\t" "ds_write_b64 v117, v[76:77] offset:10368\n\t" \
   "ds_write_b64 v117, v[78:79] offset:10624\n\t" "s_mov_b32 s41, 917504\n\t" "buffer_load_dwordx4 a[28:31], v16, %[ra], s41 offen\n\t" \
   "v_dot2c_f32_bf16 v122, s40, v76\n\t" "v_dot2c_f32_bf16 v123, v76, v76\n\t" "v_dot2c_f32_bf16 v122, s40, v77\n\t" \
   "v_dot2c_f32_bf16 v123, v77, v77\n\t" "v_dot2c_f32_bf16 v122, s40, v78\n\t" "v_dot2c_f32_bf16 v123, v78, v78\n\t" \
   "v_dot2c_f32_bf16 v122, s40, v79\n\t" "v_dot2c_f32_bf16 v123, v79, v79\n\t" "s_waitcnt vmcnt(15)\n\t" "ds_write_b64 v117, v[80:81] offset:12288\n\t" \
   "ds_write_b64 v117, v[82:83] offset:12544\n\t" "s_mov_b32 s41, 1048576\n\t" "buffer_load_dwordx4 a[32:35], v16, %[ra], s41 offen\n\t" \
   "v_dot2c_f32_bf16 v120, s40, v80\n\t" "v_dot2c_f32_bf16 v121, v80, v80\n\t" "v_dot2c_f32_bf16 v120, s40, v81\n\t" \
   "v_dot2c_f32_bf16 v121, v81, v81\n\t" "v_dot2c_f32_bf16 v120, s40, v82\n\t" "v_dot2c_f32_bf16 v121, v82, v82\n\t" \
   "v_dot2c_f32_bf16 v120, s40, v83\n\t" "v_dot2c_f32_bf16 v121, v83, v83\n\t" "s_waitcnt vmcnt(15)\n\t" "ds_write_b64 v117, v[84:85] offset:12416\n\t" \
   "ds_write_b64 v117, v[86:87] offset:12672\n\t" "s_mov_b32 s41, 1179648\n\t" "buffer_load_dwordx4 a[36:39], v16, %[ra], s41 offen\n\t" \
   "v_dot2c_f32_bf16 v122, s40, v84\n\t" "v_dot2c_f32_bf16 v123, v84, v84\n\t" "v_dot2c_f32_bf16 v122, s40, v85\n\t" \
   "v_dot2c_f32_bf16 v123, v85, v85\n\t" "v_dot2c_f32_bf16 v122, s40, v86\n\t" "v_dot2c_f32_bf16 v123, v86, v86\n\t" \
   "v_dot2c_f32_bf16 v122, s40, v87\n\t" "v_dot2c_f32_bf16 v123, v87, v87\n\t" "s_waitcnt vmcnt(15)\n\t" "ds_write_b64 v117, v[88:89] offset:14336\n\t" \
   "ds_write_b64 v117, v[90:91] offset:14592\n\t" "s_mov_b32 s41, 1310720\n\t" "buffer_load_dwordx4 a[40:43], v16, %[ra], s41 offen\n\t" \
   "v_dot2c_f32_bf16 v120, s40, v88\n\t" "v_dot2c_f32_bf16 v121, v88, v88\n\t" "v_dot2c_f32_bf16 v120, s40, v89\n\t" \
   "v_dot2c_f32_bf16 v121, v89, v89\n\t" "v_dot2c_f32_bf16 v120, s40, v90\n\t" "v_dot2c_f32_bf16 v121, v90, v90\n\t" \
   "v_dot2c_f32_bf16 v120, s40, v91\n\t" "v_dot2c_f32_bf16 v121, v91, v91\n\t" "s_waitcnt vmcnt(15)\n\t" "ds_write_b64 v117, v[92:93] offset:14464\n\t" \
   "ds_write_b64 v117, v[94:95] offset:14720\n\t" "s_mov_b32 s41, 1441792\n\t" "buffer_load_dwordx4 a[44:47], v16, %[ra], s41 offen\n\t" \
   "v_dot2c_f32_bf16 v122, s40, v92\n\t" "v_dot2c_f32_bf16 v123, v92, v92\n\t" "v_dot2c_f32_bf16 v122, s40, v93\n\t" \
   "v_dot2c_f32_bf16 v123, v93, v93\n\t" "v_dot2c_f32_bf16 v122, s40, v94\n\t" "v_dot2c_f32_bf16 v123, v94, v94\n\t" \
   "v_dot2c_f32_bf16 v122, s40, v95\n\t" "v_dot2c_f32_bf16 v123, v95, v95\n\t" "s_waitcnt vmcnt(15)\n\t" "ds_write_b64 v117, v[96:97] offset:16384\n\t" \
   "ds_write_b64 v117, v[98:99] offset:16640\n\t" "s_mov_b32 s41, 1572864\n\t" "buffer_load_dwordx4 a[48:51], v16, %[ra], s41 offen\n\t" \
   "v_dot2c_f32_bf16 v120, s40, v96\n\t" "v_dot2c_f32_bf16 v121, v96, v96\n\t" "v_dot2c_f32_bf16 v120, s40, v97\n\t" \
   "v_dot2c_f32_bf16 v121, v97, v97\n\t" "v_dot2c_f32_bf16 v120, s40, v98\n\t" "v_dot2c_f32_bf16 v121, v98, v98\n\t" \
   "v_dot2c_f32_bf16 v120, s40, v99\n\t" "v_dot2c_f32_bf16 v121, v99, v99\n\t" "s_waitcnt vmcnt(15)\n\t" \
   "ds_write_b64 v117, v[100:101] offset:16512\n\t" "ds_write_b64 v117, v[102:103] offset:16768\n\t" "s_mov_b32 s41, 1703936\n\t" \
   "buffer_load_dwordx4 a[52:55], v16, %[ra], s41 offen\n\t" "v_dot2c_f32_bf16 v122, s40, v100\n\t" "v_dot2c_f32_bf16 v123, v100, v100\n\t" \
   "v_dot2c_f32_bf16 v122, s40, v101\n\t" "v_dot2c_f32_bf16 v123, v101, v101\n\t" "v_dot2c_f32_bf16 v122, s40, v102\n\t" \
   "v_dot2c_f32_bf16 v123, v102, v102\n\t" "v_dot2c_f32_bf16 v122, s40, v103\n\t" "v_dot2c_f32_bf16 v123, v103, v103\n\t" "s_waitcnt vmcnt(15)\n\t" \
   "ds_write_b64 v117, v[104:105] offset:18432\n\t" "ds_write_b64 v117, v[106:107] offset:18688\n\t" "s_mov_b32 s41, 1835008\n\t" \
   "buffer_load_dwordx4 a[56:59], v16, %[ra], s41 offen\n\t" "v_dot2c_f32_bf16 v120, s40, v104\n\t" "v_dot2c_f32_bf16 v121, v104, v104\n\t" \
   "v_dot2c_f32_bf16 v120, s40, v105\n\t" "v_dot2c_f32_bf16 v121, v105, v105\n\t" "v_dot2c_f32_bf16 v120, s40, v106\n\t" \
   "v_dot2c_f32_bf16 v121, v106, v106\n\t" "v_dot2c_f32_bf16 v120, s40, v107\n\t" "v_dot2c_f32_bf16 v121, v107, v107\n\t" "s_waitcnt vmcnt(15)\n\t" \
   "ds_write_b64 v117, v[108:109] offset:18560\n\t" "ds_write_b64 v117, v[110:111] offset:18816\n\t" "s_mov_b32 s41, 1966080\n\t" \
   "buffer_load_dwordx4 a[60:63], v16, %[ra], s41 offen\n\t" "v_dot2c_f32_bf16 v122, s40, v108\n\t" "v_dot2c_f32_bf16 v123, v108, v108\n\t" \
   "v_dot2c_f32_bf16 v122, s40, v109\n\t" "v_dot2c_f32_bf16 v123, v109, v109\n\t" "v_dot2c_f32_bf16 v122, s40, v110\n\t" \
   "v_dot2c_f32_bf16 v123, v110, v110\n\t" "v_dot2c_f32_bf16 v122, s40, v111\n\t" "v_dot2c_f32_bf16 v123, v111, v111\n\t" "s_nop 3\n\t" \
   "v_add_f32_dpp v120, v120, v120 quad_perm:[1,0,3,2] row_mask:0xf bank_mask:0xf\n\t" \
   "v_add_f32_dpp v121, v121, v121 quad_perm:[1,0,3,2] row_mask:0xf bank_mask:0xf\n\t" \
   "v_add_f32_dpp v122, v122, v122 quad_perm:[1,0,3,2] row_mask:0xf bank_mask:0xf\n\t" \
   "v_add_f32_dpp v123, v123, v123 quad_perm:[1,0,3,2] row_mask:0xf bank_mask:0xf\n\t" \
   "v_add_f32_dpp v120, v120, v120 quad_perm:[2,3,0,1] row_mask:0xf bank_mask:0xf\n\t" \
   "v_add_f32_dpp v121, v121, v121 quad_perm:[2,3,0,1] row_mask:0xf bank_mask:0xf\n\t" \
   "v_add_f32_dpp v122, v122, v122 quad_perm:[2,3,0,1] row_mask:0xf bank_mask:0xf\n\t" \
   "v_add_f32_dpp v123, v123, v123 quad_perm:[2,3,0,1] row_mask:0xf bank_mask:0xf\n\t" \
   "v_add_f32_dpp v120, v120, v120 row_half_mirror row_mask:0xf bank_mask:0xf\n\t" \
   "v_add_f32_dpp v121, v121, v121 row_half_mirror row_mask:0xf bank_mask:0xf\n\t" \
   "v_add_f32_dpp v122, v122, v122 row_half_mirror row_mask:0xf bank_mask:0xf\n\t" \
   "v_add_f32_dpp v123, v123, v123 row_half_mirror row_mask:0xf bank_mask:0xf\n\t" "v_and_b32 v120, v119, v120\n\t" "v_and_b32 v121, v119, v121\n\t" \
   "v_and_b32 v122, v119, v122\n\t" "v_and_b32 v123, v119, v123\n\t" "ds_write_b64 v118, v[120:121]\n\t" "ds_write_b64 v118, v[122:123] offset:384\n\t" \
   "s_waitcnt vmcnt(15)\n\t" "ds_write_b128 v20, a[0:3] offset:0\n\t" "s_mov_b32 s41, 2097152\n\t" \
   "buffer_load_dwordx4 a[64:67], v16, %[ra], s41 offen\n\t" "s_waitcnt vmcnt(15)\n\t" "ds_write_b128 v20, a[4:7] offset:4096\n\t" \
   "s_mov_b32 s41, 2228224\n\t" "buffer_load_dwordx4 a[68:71], v16, %[ra], s41 offen\n\t" "s_waitcnt vmcnt(15)\n\t" \
   "ds_write_b128 v20, a[8:11] offset:8192\n\t" "s_mov_b32 s41, 2359296\n\t" "buffer_load_dwordx4 a[72:75], v16, %[ra], s41 offen\n\t" \
   "s_waitcnt vmcnt(15)\n\t" "ds_write_b128 v20, a[12:15] offset:12288\n\t" "s_mov_b32 s41, 2490368\n\t" \
   "buffer_load_dwordx4 a[76:79], v16, %[ra], s41 offen\n\t" "s_waitcnt vmcnt(15)\n\t" "ds_write_b128 v20, a[16:19] offset:16384\n\t" \
   "s_mov_b32 s41, 2621440\n\t" "buffer_load_dwordx4 a[80:83], v16, %[ra], s41 offen\n\t" "s_waitcnt vmcnt(15)\n\t" \
   "ds_write_b128 v20, a[20:23] offset:20480\n\t" "s_mov_b32 s41, 2752512\n\t" "buffer_load_dwordx4 a[84:87], v16, %[ra], s41 offen\n\t" \
   "s_waitcnt vmcnt(15)\n\t" "ds_write_b128 v20, a[24:27] offset:24576\n\t" "s_mov_b32 s41, 2883584\n\t" \
   "buffer_load_dwordx4 a[88:91], v16, %[ra], s41 offen\n\t" "s_waitcnt vmcnt(15)\n\t" "ds_write_b128 v20, a[28:31] offset:28672\n\t" \
   "s_mov_b32 s41, 3014656\n\t" "buffer_load_dwordx4 a[92:95], v16, %[ra], s41 offen\n\t" "s_waitcnt vmcnt(15)\n\t" \
   "ds_write_b128 v20, a[32:35] offset:32768\n\t" "s_mov_b32 s41, 3145728\n\t" "buffer_load_dwordx4 a[96:99], v16, %[ra], s41 offen\n\t" \
   "s_waitcnt vmcnt(15)\n\t" "ds_write_b128 v20, a[36:39] offset:36864\n\t" "s_mov_b32 s41, 3276800\n\t" \
   "buffer_load_dwordx4 a[100:103], v16, %[ra], s41 offen\n\t" "s_waitcnt lgkmcnt(0)\n\t" "s_barrier\n\t" LDSTAMP(2) \
   "ds_read_b64_tr_b16 a[0:1], v18 offset:0\n\t" "ds_read_b64_tr_b16 a[2:3], v18 offset:4096\n\t" "ds_read_b64_tr_b16 a[4:5], v19 offset:0\n\t" \
   "ds_read_b64_tr_b16 a[6:7], v19 offset:4096\n\t" "ds_read_b64_tr_b16 a[8:9], v18 offset:8192\n\t" \
   "ds_read_b64_tr_b16 a[10:11], v18 offset:12288\n\t" "ds_read_b64_tr_b16 a[12:13], v19 offset:8192\n\t" \
   "ds_read_b64_tr_b16 a[14:15], v19 offset:12288\n\t" "ds_read_b64_tr_b16 a[16:17], v18 offset:16384\n\t" \
   "ds_read_b64_tr_b16 a[18:19], v18 offset:20480\n\t" "ds_read_b64_tr_b16 a[20:21], v19 offset:16384\n\t" \
   "ds_read_b64_tr_b16 a[22:23], v19 offset:20480\n\t" "ds_read_b64_tr_b16 a[24:25], v18 offset:24576\n\t" \
   "ds_read_b64_tr_b16 a[26:27], v18 offset:28672\n\t" "ds_read_b64_tr_b16 a[28:29], v19 offset:24576\n\t" \
   "ds_read_b64_tr_b16 a[30:31], v19 offset:28672\n\t" "ds_read_b64_tr_b16 a[32:33], v18 offset:32768\n\t" \
   "ds_read_b64_tr_b16 a[34:35], v18 offset:36864\n\t" "ds_read_b64_tr_b16 a[36:37], v19 offset:32768\n\t" \
   "ds_read_b64_tr_b16 a[38:39], v19 offset:36864\n\t" "s_waitcnt vmcnt(15)\n\t" "s_waitcnt lgkmcnt(0)\n\t" "s_barrier\n\t" \
   "v_mov_b32 v64, 0x3f803f80\n\t" "v_mov_b32 v65, 0x3f803f80\n\t" "v_mov_b32 v66, 0x3f803f80\n\t" "v_mov_b32 v67, 0x3f803f80\n\t" \
   "ds_write_b128 v20, a[40:43] offset:0\n\t" "s_mov_b32 s41, 3407872\n\t" "buffer_load_dwordx4 a[104:107], v16, %[ra], s41 offen\n\t" \
   "v_mfma_f32_32x32x16_bf16 v[32:47], a[0:3], v[64:67], 0\n\t" "s_waitcnt vmcnt(15)\n\t" "ds_write_b128 v20, a[44:47] offset:4096\n\t" \
   "s_mov_b32 s41, 3538944\n\t" "buffer_load_dwordx4 a[108:111], v16, %[ra], s41 offen\n\t" \
   "v_mfma_f32_32x32x16_bf16 v[32:47], a[4:7], v[64:67], v[32:47]\n\t" "s_waitcnt vmcnt(15)\n\t" "ds_write_b128 v20, a[48:51] offset:8192\n\t" \
   "s_mov_b32 s41, 3670016\n\t" "buffer_load_dwordx4 a[112:115], v16, %[ra], s41 offen\n\t" \
   "v_mfma_f32_32x32x16_bf16 v[32:47], a[8:11], v[64:67], v[32:47]\n\t" "s_waitcnt vmcnt(15)\n\t" "ds_write_b128 v20, a[52:55] offset:12288\n\t" \
   "s_mov_b32 s41, 3801088\n\t" "buffer_load_dwordx4 a[116:119], v16, %[ra], s41 offen\n\t" \
   "v_mfma_f32_32x32x16_bf16 v[32:47], a[12:15], v[64:67], v[32:47]\n\t" "s_waitcnt vmcnt(15)\n\t" "ds_write_b128 v20, a[56:59] offset:16384\n\t" \
   "s_mov_b32 s41, 3932160\n\t" "buffer_load_dwordx4 a[120:123], v16, %[ra], s41 offen\n\t" \
   "v_mfma_f32_32x32x16_bf16 v[32:47], a[16:19], v[64:67], v[32:47]\n\t" "s_waitcnt vmcnt(15)\n\t" "ds_write_b128 v20, a[60:63] offset:20480\n\t" \
   "s_mov_b32 s41, 4063232\n\t" "buffer_load_dwordx4 a[124:127], v16, %[ra], s41 offen\n\t" \
   "v_mfma_f32_32x32x16_bf16 v[32:47], a[20:23], v[64:67], v[32:47]\n\t" "s_waitcnt vmcnt(15)\n\t" "ds_write_b128 v20, a[64:67] offset:24576\n\t" \
   "s_mov_b32 s41, 4194304\n\t" "buffer_load_dwordx4 a[128:131], v16, %[ra], s41 offen\n\t" \
   "v_mfma_f32_32x32x16_bf16 v[32:47], a[24:27], v[64:67], v[32:47]\n\t" "s_waitcnt vmcnt(15)\n\t" "ds_write_b128 v20, a[68:71] offset:28672\n\t" \
   "s_mov_b32 s41, 4325376\n\t" "buffer_load_dwordx4 a[132:135], v16, %[ra], s41 offen\n\t" \
   "v_mfma_f32_32x32x16_bf16 v[32:47], a[28:31], v[64:67], v[32:47]\n\t" "s_waitcnt vmcnt(15)\n\t" "ds_write_b128 v20, a[72:75] offset:32768\n\t" \
   "s_mov_b32 s41, 4456448\n\t" "buffer_load_dwordx4 a[136:139], v16, %[ra], s41 offen\n\t" \
   "v_mfma_f32_32x32x16_bf16 v[32:47], a[32:35], v[64:67], v[32:47]\n\t" "s_waitcnt vmcnt(15)\n\t" "ds_write_b128 v20, a[76:79] offset:36864\n\t" \
   "s_mov_b32 s41, 4587520\n\t" "buffer_load_dwordx4 a[140:143], v16, %[ra], s41 offen\n\t" \
   "v_mfma_f32_32x32x16_bf16 v[32:47], a[36:39], v[64:67], v[32:47]\n\t" "s_waitcnt lgkmcnt(0)\n\t" "s_barrier\n\t" LDSTAMP(3) \
   "ds_read_b64_tr_b16 a[40:41], v18 offset:0\n\t" "ds_read_b64_tr_b16 a[42:43], v18 offset:4096\n\t" "ds_read_b64_tr_b16 a[44:45], v19 offset:0\n\t" \
   "ds_read_b64_tr_b16 a[46:47], v19 offset:4096\n\t" "ds_read_b64_tr_b16 a[48:49], v18 offset:8192\n\t" \
   "ds_read_b64_tr_b16 a[50:51], v18 offset:12288\n\t" "ds_read_b64_tr_b16 a[52:53], v19 offset:8192\n\t" \
   "ds_read_b64_tr_b16 a[54:55], v19 offset:12288\n\t" "ds_read_b64_tr_b16 a[56:57], v18 offset:16384\n\t" \
   "ds_read_b64_tr_b16 a[58:59], v18 offset:20480\n\t" "ds_read_b64_tr_b16 a[60:61], v19 offset:16384\n\t" \
   "ds_read_b64_tr_b16 a[62:63], v19 offset:20480\n\t" "ds_read_b64_tr_b16 a[64:65], v18 offset:24576\n\t" \
   "ds_read_b64_tr_b16 a[66:67], v18 offset:28672\n\t" "ds_read_b64_tr_b16 a[68:69], v19 offset:24576\n\t" \
   "ds_read_b64_tr_b16 a[70:71], v19 offset:28672\n\t" "ds_read_b64_tr_b16 a[72:73], v18 offset:32768\n\t" \
   "ds_read_b64_tr_b16 a[74:75], v18 offset:36864\n\t" "ds_read_b64_tr_b16 a[76:77], v19 offset:32768\n\t" \
   "ds_read_b64_tr_b16 a[78:79], v19 offset:36864\n\t" "s_waitcnt vmcnt(15)\n\t" "s_waitcnt lgkmcnt(0)\n\t" "s_barrier\n\t" \
   "ds_write_b128 v20, a[80:83] offset:0\n\t" "s_mov_b32 s41, 4718592\n\t" "buffer_load_dwordx4 a[144:147], v16, %[ra], s41 offen\n\t" \
   "v_mfma_f32_32x32x16_bf16 v[32:47], a[40:43], v[64:67], v[32:47]\n\t" "s_waitcnt vmcnt(15)\n\t" "ds_write_b128 v20, a[84:87] offset:4096\n\t" \
   "s_mov_b32 s41, 4849664\n\t" "buffer_load_dwordx4 a[148:151], v16, %[ra], s41 offen\n\t" \
   "v_mfma_f32_32x32x16_bf16 v[32:47], a[44:47], v[64:67], v[32:47]\n\t" "s_waitcnt vmcnt(15)\n\t" "ds_write_b128 v20, a[88:91] offset:8192\n\t" \
   "s_mov_b32 s41, 4980736\n\t" "buffer_load_dwordx4 a[152:155], v16, %[ra], s41 offen\n\t" \
   "v_mfma_f32_32x32x16_bf16 v[32:47], a[48:51], v[64:67], v[32:47]\n\t" "s_waitcnt vmcnt(15)\n\t" "ds_write_b128 v20, a[92:95] offset:12288\n\t" \
   "s_mov_b32 s41, 5111808\n\t" "buffer_load_dwordx4 a[156:159], v16, %[ra], s41 offen\n\t" \
   "v_mfma_f32_32x32x16_bf16 v[32:47], a[52:55], v[64:67], v[32:47]\n\t" "s_waitcnt vmcnt(15)\n\t" "ds_write_b128 v20, a[96:99] offset:16384\n\t" \
   "s_mov_b32 s41, 0\n\t" "buffer_load_dwordx4 a[160:163], v17, %[rb], s41 offen\n\t" \
   "v_mfma_f32_32x32x16_bf16 v[32:47], a[56:59], v[64:67], v[32:47]\n\t" "s_waitcnt vmcnt(15)\n\t" "ds_write_b128 v20, a[100:103] offset:20480\n\t" \
   "s_mov_b32 s41, 256\n\t" "buffer_load_dwordx4 a[176:179], v17, %[rb], s41 offen\n\t" \
   "v_mfma_f32_32x32x16_bf16 v[32:47], a[60:63], v[64:67], v[32:47]\n\t" "s_waitcnt vmcnt(15)\n\t" "ds_write_b128 v20, a[104:107] offset:24576\n\t" \
   "s_mov_b32 s41, 512\n\t" "buffer_load_dwordx4 a[192:195], v17, %[rb], s41 offen\n\t" \
   "v_mfma_f32_32x32x16_bf16 v[32:47], a[64:67], v[64:67], v[32:47]\n\t" "s_waitcnt vmcnt(15)\n\t" "ds_write_b128 v20, a[108:111] offset:28672\n\t" \
   "s_mov_b32 s41, 768\n\t" "buffer_load_dwordx4 a[208:211], v17, %[rb], s41 offen\n\t" \
   "v_mfma_f32_32x32x16_bf16 v[32:47], a[68:71], v[64:67], v[32:47]\n\t" "s_waitcnt vmcnt(15)\n\t" "ds_write_b128 v20, a[112:115] offset:32768\n\t" \
   "s_mov_b32 s41, 1024\n\t" "buffer_load_dwordx4 a[224:227], v17, %[rb], s41 offen\n\t" \
   "v_mfma_f32_32x32x16_bf16 v[32:47], a[72:75], v[64:67], v[32:47]\n\t" "s_waitcnt vmcnt(15)\n\t" "ds_write_b128 v20, a[116:119] offset:36864\n\t" \
   "s_mov_b32 s41, 20480\n\t" "buffer_load_dwordx4 a[240:243], v17, %[rb], s41 offen\n\t" \
   "v_mfma_f32_32x32x16_bf16 v[32:47], a[76:79], v[64:67], v[32:47]\n\t" "s_waitcnt lgkmcnt(0)\n\t" "s_barrier\n\t" LDSTAMP(4) \
   "ds_read_b64_tr_b16 a[80:81], v18 offset:0\n\t" "ds_read_b64_tr_b16 a[82:83], v18 offset:4096\n\t" "ds_read_b64_tr_b16 a[84:85], v19 offset:0\n\t" \
   "ds_read_b64_tr_b16 a[86:87], v19 offset:4096\n\t" "ds_read_b64_tr_b16 a[88:89], v18 offset:8192\n\t" \
   "ds_read_b64_tr_b16 a[90:91], v18 offset:12288\n\t" "ds_read_b64_tr_b16 a[92:93], v19 offset:8192\n\t" \
   "ds_read_b64_tr_b16 a[94:95], v19 offset:12288\n\t" "ds_read_b64_tr_b16 a[96:97], v18 offset:16384\n\t" \
   "ds_read_b64_tr_b16 a[98:99], v18 offset:20480\n\t" "ds_read_b64_tr_b16 a[100:101], v19 offset:16384\n\t" \
   "ds_read_b64_tr_b16 a[102:103], v19 offset:20480\n\t" "ds_read_b64_tr_b16 a[104:105], v18 offset:24576\n\t" \
   "ds_read_b64_tr_b16 a[106:107], v18 offset:28672\n\t" "ds_read_b64_tr_b16 a[108:109], v19 offset:24576\n\t" \
   "ds_read_b64_tr_b16 a[110:111], v19 offset:28672\n\t" "ds_read_b64_tr_b16 a[112:113], v18 offset:32768\n\t" \
   "ds_read_b64_tr_b16 a[114:115], v18 offset:36864\n\t" "ds_read_b64_tr_b16 a[116:117], v19 offset:32768\n\t" \
   "ds_read_b64_tr_b16 a[118:119], v19 offset:36864\n\t" "s_waitcnt vmcnt(15)\n\t" "s_waitcnt lgkmcnt(0)\n\t" "s_barrier\n\t" \
   "ds_write_b128 v20, a[120:123] offset:0\n\t" "s_mov_b32 s41, 20736\n\t" "buffer_load_dwordx4 v[192:195], v17, %[rb], s41 offen\n\t" \
   "v_mfma_f32_32x32x16_bf16 v[32:47], a[80:83], v[64:67], v[32:47]\n\t" "s_waitcnt vmcnt(15)\n\t" "ds_write_b128 v20, a[124:127] offset:4096\n\t" \
   "s_mov_b32 s41, 20992\n\t" "buffer_load_dwordx4 v[208:211], v17, %[rb], s41 offen\n\t" \
   "v_mfma_f32_32x32x16_bf16 v[32:47], a[84:87], v[64:67], v[32:47]\n\t" "s_waitcnt vmcnt(15)\n\t" "ds_write_b128 v20, a[128:131] offset:8192\n\t" \
   "s_mov_b32 s41, 21248\n\t" "buffer_load_dwordx4 v[224:227], v17, %[rb], s41 offen\n\t" \
   "v_mfma_f32_32x32x16_bf16 v[32:47], a[88:91], v[64:67], v[32:47]\n\t" "s_waitcnt vmcnt(15)\n\t" "ds_write_b128 v20, a[132:135] offset:12288\n\t" \
   "s_mov_b32 s41, 21504\n\t" "buffer_load_dwordx4 v[240:243], v17, %[rb], s41 offen\n\t" \
   "v_mfma_f32_32x32x16_bf16 v[32:47], a[92:95], v[64:67], v[32:47]\n\t" "s_waitcnt vmcnt(15)\n\t" "ds_write_b128 v20, a[136:139] offset:16384\n\t" \
   "s_mov_b32 s41, 40960\n\t" "buffer_load_dwordx4 a[164:167], v17, %[rb], s41 offen\n\t" \
   "v_mfma_f32_32x32x16_bf16 v[32:47], a[96:99], v[64:67], v[32:47]\n\t" "s_waitcnt vmcnt(15)\n\t" "ds_write_b128 v20, a[140:143] offset:20480\n\t" \
   "s_mov_b32 s41, 41216\n\t" "buffer_load_dwordx4 a[180:183], v17, %[rb], s41 offen\n\t" \
   "v_mfma_f32_32x32x16_bf16 v[32:47], a[100:103], v[64:67], v[32:47]\n\t" "s_waitcnt vmcnt(15)\n\t" "ds_write_b128 v20, a[144:147] offset:24576\n\t" \
   "s_mov_b32 s41, 41472\n\t" "buffer_load_dwordx4 a[196:199], v17, %[rb], s41 offen\n\t" \
   "v_mfma_f32_32x32x16_bf16 v[32:47], a[104:107], v[64:67], v[32:47]\n\t" "s_waitcnt vmcnt(15)\n\t" "ds_write_b128 v20, a[148:151] offset:28672\n\t" \
   "s_mov_b32 s41, 41728\n\t" "buffer_load_dwordx4 a[212:215], v17, %[rb], s41 offen\n\t" \
   "v_mfma_f32_32x32x16_bf16 v[32:47], a[108:111], v[64:67], v[32:47]\n\t" "s_waitcnt vmcnt(15)\n\t" "ds_write_b128 v20, a[152:155] offset:32768\n\t" \
   "s_mov_b32 s41, 41984\n\t" "buffer_load_dwordx4 a[228:231], v17, %[rb], s41 offen\n\t" \
   "v_mfma_f32_32x32x16_bf16 v[32:47], a[112:115], v[64:67], v[32:47]\n\t" "s_waitcnt vmcnt(15)\n\t" "ds_write_b128 v20, a[156:159] offset:36864\n\t" \
   "s_mov_b32 s41, 61440\n\t" "buffer_load_dwordx4 a[244:247], v17, %[rb], s41 offen\n\t" \
   "v_mfma_f32_32x32x16_bf16 v[32:47], a[116:119], v[64:67], v[32:47]\n\t" "s_waitcnt lgkmcnt(0)\n\t" "s_barrier\n\t" LDSTAMP(5) \
   "ds_read_b64_tr_b16 a[120:121], v18 offset:0\n\t" "ds_read_b64_tr_b16 a[122:123], v18 offset:4096\n\t" \
   "ds_read_b64_tr_b16 a[124:125], v19 offset:0\n\t" "ds_read_b64_tr_b16 a[126:127], v19 offset:4096\n\t" \
   "ds_read_b64_tr_b16 a[128:129], v18 offset:8192\n\t" "ds_read_b64_tr_b16 a[130:131], v18 offset:12288\n\t" \
   "ds_read_b64_tr_b16 a[132:133], v19 offset:8192\n\t" "ds_read_b64_tr_b16 a[134:135], v19 offset:12288\n\t" \
   "ds_read_b64_tr_b16 a[136:137], v18 offset:16384\n\t" "ds_read_b64_tr_b16 a[138:139], v18 offset:20480\n\t" \
   "ds_read_b64_tr_b16 a[140:141], v19 offset:16384\n\t" "ds_read_b64_tr_b16 a[142:143], v19 offset:20480\n\t" \
   "ds_read_b64_tr_b16 a[144:145], v18 offset:24576\n\t" "ds_read_b64_tr_b16 a[146:147], v18 offset:28672\n\t" \
   "ds_read_b64_tr_b16 a[148:149], v19 offset:24576\n\t" "ds_read_b64_tr_b16 a[150:151], v19 offset:28672\n\t" \
   "ds_read_b64_tr_b16 a[152:153], v18 offset:32768\n\t" "ds_read_b64_tr_b16 a[154:155], v18 offset:36864\n\t" \
   "ds_read_b64_tr_b16 a[156:157], v19 offset:32768\n\t" "ds_read_b64_tr_b16 a[158:159], v19 offset:36864\n\t" "s_waitcnt vmcnt(15)\n\t" \
   "s_waitcnt lgkmcnt(0)\n\t" "s_barrier\n\t" "ds_write_b32 v21, a160 offset:0\n\t" "ds_write_b32 v21, a161 offset:21120\n\t" \
   "ds_write_b32 v21, a162 offset:10560\n\t" "ds_write_b32 v21, a163 offset:31680\n\t" "s_mov_b32 s41, 61696\n\t" \
   "buffer_load_dwordx4 v[196:199], v17, %[rb], s41 offen\n\t" "v_mfma_f32_32x32x16_bf16 v[32:47], a[120:123], v[64:67], v[32:47]\n\t" \
   "s_waitcnt vmcnt(15)\n\t" "ds_write_b32 v21, a176 offset:64\n\t" "ds_write_b32 v21, a177 offset:21184\n\t" "ds_write_b32 v21, a178 offset:10624\n\t" \
   "ds_write_b32 v21, a179 offset:31744\n\t" "s_mov_b32 s41, 61952\n\t" "buffer_load_dwordx4 v[212:215], v17, %[rb], s41 offen\n\t" \
   "v_mfma_f32_32x32x16_bf16 v[32:47], a[124:127], v[64:67], v[32:47]\n\t" "s_waitcnt vmcnt(15)\n\t" "ds_write_b32 v21, a192 offset:128\n\t" \
   "ds_write_b32 v21, a193 offset:21248\n\t" "ds_write_b32 v21, a194 offset:10688\n\t" "ds_write_b32 v21, a195 offset:31808\n\t" \
   "s_mov_b32 s41, 62208\n\t" "buffer_load_dwordx4 v[228:231], v17, %[rb], s41 offen\n\t" \
   "v_mfma_f32_32x32x16_bf16 v[32:47], a[128:131], v[64:67], v[32:47]\n\t" "s_waitcnt vmcnt(15)\n\t" "ds_write_b32 v21, a208 offset:192\n\t" \
   "ds_write_b32 v21, a209 offset:21312\n\t" "ds_write_b32 v21, a210 offset:10752\n\t" "ds_write_b32 v21, a211 offset:31872\n\t" \
   "s_mov_b32 s41, 62464\n\t" "buffer_load_dwordx4 v[244:247], v17, %[rb], s41 offen\n\t" \
   "v_mfma_f32_32x32x16_bf16 v[32:47], a[132:135], v[64:67], v[32:47]\n\t" "s_waitcnt vmcnt(15)\n\t" "ds_write_b32 v21, a224 offset:256\n\t" \
   "ds_write_b32 v21, a225 offset:21376\n\t" "ds_write_b32 v21, a226 offset:10816\n\t" "ds_write_b32 v21, a227 offset:31936\n\t" \
   "s_mov_b32 s41, 81920\n\t" "buffer_load_dwordx4 a[168:171], v17, %[rb], s41 offen\n\t" \
   "v_mfma_f32_32x32x16_bf16 v[32:47], a[136:139], v[64:67], v[32:47]\n\t" "s_waitcnt vmcnt(15)\n\t" "ds_write_b32 v21, a240 offset:5248\n\t" \
   "ds_write_b32 v21, a241 offset:26368\n\t" "ds_write_b32 v21, a242 offset:15808\n\t" "ds_write_b32 v21, a243 offset:36928\n\t" \
   "s_mov_b32 s41, 82176\n\t" "buffer_load_dwordx4 a[184:187], v17, %[rb], s41 offen\n\t" \
   "v_mfma_f32_32x32x16_bf16 v[32:47], a[140:143], v[64:67], v[32:47]\n\t" "s_waitcnt vmcnt(15)\n\t" "ds_write_b32 v21, v192 offset:5312\n\t" \
   "ds_write_b32 v21, v193 offset:26432\n\t" "ds_write_b32 v21, v194 offset:15872\n\t" "ds_write_b32 v21, v195 offset:36992\n\t" \
   "s_mov_b32 s41, 82432\n\t" "buffer_load_dwordx4 a[200:203], v17, %[rb], s41 offen\n\t" \
   "v_mfma_f32_32x32x16_bf16 v[32:47], a[144:147], v[64:67], v[32:47]\n\t" "s_waitcnt vmcnt(15)\n\t" "ds_write_b32 v21, v208 offset:5376\n\t" \
   "ds_write_b32 v21, v209 offset:26496\n\t" "ds_write_b32 v21, v210 offset:15936\n\t" "ds_write_b32 v21, v211 offset:37056\n\t" \
   "s_mov_b32 s41, 82688\n\t" "buffer_load_dwordx4 a[216:219], v17, %[rb], s41 offen\n\t" \
   "v_mfma_f32_32x32x16_bf16 v[32:47], a[148:151], v[64:67], v[32:47]\n\t" "s_waitcnt vmcnt(15)\n\t" "ds_write_b32 v21, v224 offset:5440\n\t" \
   "ds_write_b32 v21, v225 offset:26560\n\t" "ds_write_b32 v21, v226 offset:16000\n\t" "ds_write_b32 v21, v227 offset:37120\n\t" \
   "s_mov_b32 s41, 82944\n\t" "buffer_load_dwordx4 a[232:235], v17, %[rb], s41 offen\n\t" \
   "v_mfma_f32_32x32x16_bf16 v[32:47], a[152:155], v[64:67], v[32:47]\n\t" "s_waitcnt vmcnt(15)\n\t" "ds_write_b32 v21, v240 offset:5504\n\t" \
   "ds_write_b32 v21, v241 offset:26624\n\t" "ds_write_b32 v21, v242 offset:16064\n\t" "ds_write_b32 v21, v243 offset:37184\n\t" \
   "s_mov_b32 s41, 102400\n\t" "buffer_load_dwordx4 a[248:251], v17, %[rb], s41 offen\n\t" \
   "v_mfma_f32_32x32x16_bf16 v[32:47], a[156:159], v[64:67], v[32:47]\n\t" "s_waitcnt lgkmcnt(0)\n\t" "s_barrier\n\t" LDSTAMP(6) \
   "ds_read_b64_tr_b16 a[160:161], v22 offset:0\n\t" "ds_read_b64_tr_b16 a[162:163], v22 offset:2624\n\t" \
   "ds_read_b64_tr_b16 a[176:177], v22 offset:5248\n\t" "ds_read_b64_tr_b16 a[178:179], v22 offset:7872\n\t" \
   "ds_read_b64_tr_b16 a[192:193], v22 offset:16\n\t" "ds_read_b64_tr_b16 a[194:195], v22 offset:2640\n\t" \
   "ds_read_b64_tr_b16 a[208:209], v22 offset:5264\n\t" "ds_read_b64_tr_b16 a[210:211], v22 offset:7888\n\t" \
   "ds_read_b64_tr_b16 a[224:225], v22 offset:32\n\t" "ds_read_b64_tr_b16 a[226:227], v22 offset:2656\n\t" \
   "ds_read_b64_tr_b16 a[240:241], v22 offset:5280\n\t" "ds_read_b64_tr_b16 a[242:243], v22 offset:7904\n\t" \
   "ds_read_b64_tr_b16 v[192:193], v22 offset:48\n\t" "ds_read_b64_tr_b16 v[194:195], v22 offset:2672\n\t" \
   "ds_read_b64_tr_b16 v[208:209], v22 offset:5296\n\t" "ds_read_b64_tr_b16 v[210:211], v22 offset:7920\n\t" \
   "ds_read_b64_tr_b16 v[224:225], v22 offset:64\n\t" "ds_read_b64_tr_b16 v[226:227], v22 offset:2688\n\t" \
   "ds_read_b64_tr_b16 v[240:241], v22 offset:5312\n\t" "ds_read_b64_tr_b16 v[242:243], v22 offset:7936\n\t" "s_waitcnt vmcnt(15)\n\t" \
   "s_waitcnt lgkmcnt(0)\n\t" "s_barrier\n\t" "ds_write_b32 v21, a164 offset:0\n\t" "ds_write_b32 v21, a165 offset:21120\n\t" \
   "ds_write_b32 v21, a166 offset:10560\n\t" "ds_write_b32 v21, a167 offset:31680\n\t" "s_mov_b32 s41, 102656\n\t" \
   "buffer_load_dwordx4 v[200:203], v17, %[rb], s41 offen\n\t" "s_waitcnt vmcnt(15)\n\t" "ds_write_b32 v21, a180 offset:64\n\t" \
   "ds_write_b32 v21, a181 offset:21184\n\t" "ds_write_b32 v21, a182 offset:10624\n\t" "ds_write_b32 v21, a183 offset:31744\n\t" \
   "s_mov_b32 s41, 102912\n\t" "buffer_load_dwordx4 v[216:219], v17, %[rb], s41 offen\n\t" "s_waitcnt vmcnt(15)\n\t" \
   "ds_write_b32 v21, a196 offset:128\n\t" "ds_write_b32 v21, a197 offset:21248\n\t" "ds_write_b32 v21, a198 offset:10688\n\t" \
   "ds_write_b32 v21, a199 offset:31808\n\t" "s_mov_b32 s41, 103168\n\t" "buffer_load_dwordx4 v[232:235], v17, %[rb], s41 offen\n\t" \
   "s_waitcnt vmcnt(15)\n\t" "ds_write_b32 v21, a212 offset:192\n\t" "ds_write_b32 v21, a213 offset:21312\n\t" \
   "ds_write_b32 v21, a214 offset:10752\n\t" "ds_write_b32 v21, a215 offset:31872\n\t" "s_mov_b32 s41, 103424\n\t" \
   "buffer_load_dwordx4 v[248:251], v17, %[rb], s41 offen\n\t" "s_waitcnt vmcnt(15)\n\t" "ds_write_b32 v21, a228 offset:256\n\t" \
   "ds_write_b32 v21, a229 offset:21376\n\t" "ds_write_b32 v21, a230 offset:10816\n\t" "ds_write_b32 v21, a231 offset:31936\n\t" \
   "s_mov_b32 s41, 122880\n\t" "buffer_load_dwordx4 a[172:175], v17, %[rb], s41 offen\n\t" "s_waitcnt vmcnt(15)\n\t" \
   "ds_write_b32 v21, a244 offset:5248\n\t" "ds_write_b32 v21, a245 offset:26368\n\t" "ds_write_b32 v21, a246 offset:15808\n\t" \
   "ds_write_b32 v21, a247 offset:36928\n\t" "s_mov_b32 s41, 123136\n\t" "buffer_load_dwordx4 a[188:191], v17, %[rb], s41 offen\n\t" \
   "s_waitcnt vmcnt(15)\n\t" "ds_write_b32 v21, v196 offset:5312\n\t" "ds_write_b32 v21, v197 offset:26432\n\t" \
   "ds_write_b32 v21, v198 offset:15872\n\t" "ds_write_b32 v21, v199 offset:36992\n\t" "s_mov_b32 s41, 123392\n\t" \
   "buffer_load_dwordx4 a[204:207], v17, %[rb], s41 offen\n\t" "s_waitcnt vmcnt(15)\n\t" "ds_write_b32 v21, v212 offset:5376\n\t" \
   "ds_write_b32 v21, v213 offset:26496\n\t" "ds_write_b32 v21, v214 offset:15936\n\t" "ds_write_b32 v21, v215 offset:37056\n\t" \
   "s_mov_b32 s41, 123648\n\t" "buffer_load_dwordx4 a[220:223], v17, %[rb], s41 offen\n\t" "s_waitcnt vmcnt(15)\n\t" \
   "ds_write_b32 v21, v228 offset:5440\n\t" "ds_write_b32 v21, v229 offset:26560\n\t" "ds_write_b32 v21, v230 offset:16000\n\t" \
   "ds_write_b32 v21, v231 offset:37120\n\t" "s_mov_b32 s41, 123904\n\t" "buffer_load_dwordx4 a[236:239], v17, %[rb], s41 offen\n\t" \
   "s_waitcnt vmcnt(15)\n\t" "ds_write_b32 v21, v244 offset:5504\n\t" "ds_write_b32 v21, v245 offset:26624\n\t" \
   "ds_write_b32 v21, v246 offset:16064\n\t" "ds_write_b32 v21, v247 offset:37184\n\t" "s_mov_b32 s41, 143360\n\t" \
   "buffer_load_dwordx4 a[252:255], v17, %[rb], s41 offen\n\t" "s_waitcnt lgkmcnt(0)\n\t" "s_barrier\n\t" LDSTAMP(7) \
   "ds_read_b64_tr_b16 a[164:165], v22 offset:0\n\t" "ds_read_b64_tr_b16 a[166:167], v22 offset:2624\n\t" \
   "ds_read_b64_tr_b16 a[180:181], v22 offset:5248\n\t" "ds_read_b64_tr_b16 a[182:183], v22 offset:7872\n\t" \
   "ds_read_b64_tr_b16 a[196:197], v22 offset:16\n\t" "ds_read_b64_tr_b16 a[198:199], v22 offset:2640\n\t" \
   "ds_read_b64_tr_b16 a[212:213], v22 offset:5264\n\t" "ds_read_b64_tr_b16 a[214:215], v22 offset:7888\n\t" \
   "ds_read_b64_tr_b16 a[228:229], v22 offset:32\n\t" "ds_read_b64_tr_b16 a[230:231], v22 offset:2656\n\t" \
   "ds_read_b64_tr_b16 a[244:245], v22 offset:5280\n\t" "ds_read_b64_tr_b16 a[246:247], v22 offset:7904\n\t" \
   "ds_read_b64_tr_b16 v[196:197], v22 offset:48\n\t" "ds_read_b64_tr_b16 v[198:199], v22 offset:2672\n\t" \
   "ds_read_b64_tr_b16 v[212:213], v22 offset:5296\n\t" "ds_read_b64_tr_b16 v[214:215], v22 offset:7920\n\t" \
   "ds_read_b64_tr_b16 v[228:229], v22 offset:64\n\t" "ds_read_b64_tr_b16 v[230:231], v22 offset:2688\n\t" \
   "ds_read_b64_tr_b16 v[244:245], v22 offset:5312\n\t" "ds_read_b64_tr_b16 v[246:247], v22 offset:7936\n\t" "s_waitcnt vmcnt(15)\n\t" \
   "s_waitcnt lgkmcnt(0)\n\t" "s_barrier\n\t" "ds_write_b32 v21, a168 offset:0\n\t" "ds_write_b32 v21, a169 offset:21120\n\t" \
   "ds_write_b32 v21, a170 offset:10560\n\t" "ds_write_b32 v21, a171 offset:31680\n\t" "s_mov_b32 s41, 143616\n\t" \
   "buffer_load_dwordx4 v[204:207], v17, %[rb], s41 offen\n\t" "s_waitcnt vmcnt(15)\n\t" "ds_write_b32 v21, a184 offset:64\n\t" \
   "ds_write_b32 v21, a185 offset:21184\n\t" "ds_write_b32 v21, a186 offset:10624\n\t" "ds_write_b32 v21, a187 offset:31744\n\t" \
   "s_mov_b32 s41, 143872\n\t" "buffer_load_dwordx4 v[220:223], v17, %[rb], s41 offen\n\t" "s_waitcnt vmcnt(15)\n\t" \
   "ds_write_b32 v21, a200 offset:128\n\t" "ds_write_b32 v21, a201 offset:21248\n\t" "ds_write_b32 v21, a202 offset:10688\n\t" \
   "ds_write_b32 v21, a203 offset:31808\n\t" "s_mov_b32 s41, 144128\n\t" "buffer_load_dwordx4 v[236:239], v17, %[rb], s41 offen\n\t" \
   "s_waitcnt vmcnt(15)\n\t" "ds_write_b32 v21, a216 offset:192\n\t" "ds_write_b32 v21, a217 offset:21312\n\t" \
   "ds_write_b32 v21, a218 offset:10752\n\t" "ds_write_b32 v21, a219 offset:31872\n\t" "s_mov_b32 s41, 144384\n\t" \
   "buffer_load_dwordx4 v[252:255], v17, %[rb], s41 offen\n\t" "s_waitcnt vmcnt(15)\n\t" "ds_write_b32 v21, a232 offset:256\n\t" \
   "ds_write_b32 v21, a233 offset:21376\n\t" "ds_write_b32 v21, a234 offset:10816\n\t" "ds_write_b32 v21, a235 offset:31936\n\t" \
   "s_waitcnt vmcnt(14)\n\t" "ds_write_b32 v21, a248 offset:5248\n\t" "ds_write_b32 v21, a249 offset:26368\n\t" \
   "ds_write_b32 v21, a250 offset:15808\n\t" "ds_write_b32 v21, a251 offset:36928\n\t" "s_waitcnt vmcnt(13)\n\t" \
   "ds_write_b32 v21, v200 offset:5312\n\t" "ds_write_b32 v21, v201 offset:26432\n\t" "ds_write_b32 v21, v202 offset:15872\n\t" \
   "ds_write_b32 v21, v203 offset:36992\n\t" "s_waitcnt vmcnt(12)\n\t" "ds_write_b32 v21, v216 offset:5376\n\t" \
   "ds_write_b32 v21, v217 offset:26496\n\t" "ds_write_b32 v21, v218 offset:15936\n\t" "ds_write_b32 v21, v219 offset:37056\n\t" \
   "s_waitcnt vmcnt(11)\n\t" "ds_write_b32 v21, v232 offset:5440\n\t" "ds_write_b32 v21, v233 offset:26560\n\t" \
   "ds_write_b32 v21, v234 offset:16000\n\t" "ds_write_b32 v21, v235 offset:37120\n\t" "s_waitcnt vmcnt(10)\n\t" \
   "ds_write_b32 v21, v248 offset:5504\n\t" "ds_write_b32 v21, v249 offset:26624\n\t" "ds_write_b32 v21, v250 offset:16064\n\t" \
   "ds_write_b32 v21, v251 offset:37184\n\t" "buffer_load_dwordx4 v[28:31], v112, %[rw], 0 offen sc1\n\t" "s_waitcnt lgkmcnt(0)\n\t" "s_barrier\n\t" \
   LDSTAMP(8) "ds_read_b64_tr_b16 a[168:169], v22 offset:0\n\t" "ds_read_b64_tr_b16 a[170:171], v22 offset:2624\n\t" \
   "ds_read_b64_tr_b16 a[184:185], v22 offset:5248\n\t" "ds_read_b64_tr_b16 a[186:187], v22 offset:7872\n\t" \
   "ds_read_b64_tr_b16 a[200:201], v22 offset:16\n\t" "ds_read_b64_tr_b16 a[202:203], v22 offset:2640\n\t" \
   "ds_read_b64_tr_b16 a[216:217], v22 offset:5264\n\t" "ds_read_b64_tr_b16 a[218:219], v22 offset:7888\n\t" \
   "ds_read_b64_tr_b16 a[232:233], v22 offset:32\n\t" "ds_read_b64_tr_b16 a[234:235], v22 offset:2656\n\t" \
   "ds_read_b64_tr_b16 a[248:249], v22 offset:5280\n\t" "ds_read_b64_tr_b16 a[250:251], v22 offset:7904\n\t" \
   "ds_read_b64_tr_b16 v[200:201], v22 offset:48\n\t" "ds_read_b64_tr_b16 v[202:203], v22 offset:2672\n\t" \
   "ds_read_b64_tr_b16 v[216:217], v22 offset:5296\n\t" "ds_read_b64_tr_b16 v[218:219], v22 offset:7920\n\t" \
   "ds_read_b64_tr_b16 v[232:233], v22 offset:64\n\t" "ds_read_b64_tr_b16 v[234:235], v22 offset:2688\n\t" \
   "ds_read_b64_tr_b16 v[248:249], v22 offset:5312\n\t" "ds_read_b64_tr_b16 v[250:251], v22 offset:7936\n\t" "s_waitcnt vmcnt(10)\n\t" \
   "s_waitcnt lgkmcnt(0)\n\t" "s_barrier\n\t" "ds_write_b32 v21, a172 offset:0\n\t" "ds_write_b32 v21, a173 offset:21120\n\t" \
   "ds_write_b32 v21, a174 offset:10560\n\t" "ds_write_b32 v21, a175 offset:31680\n\t" "s_waitcnt vmcnt(9)\n\t" "ds_write_b32 v21, a188 offset:64\n\t" \
   "ds_write_b32 v21, a189 offset:21184\n\t" "ds_write_b32 v21, a190 offset:10624\n\t" "ds_write_b32 v21, a191 offset:31744\n\t" \
   "s_waitcnt vmcnt(8)\n\t" "ds_write_b32 v21, a204 offset:128\n\t" "ds_write_b32 v21, a205 offset:21248\n\t" "ds_write_b32 v21, a206 offset:10688\n\t" \
   "ds_write_b32 v21, a207 offset:31808\n\t" "s_waitcnt vmcnt(7)\n\t" "ds_write_b32 v21, a220 offset:192\n\t" "ds_write_b32 v21, a221 offset:21312\n\t" \
   "ds_write_b32 v21, a222 offset:10752\n\t" "ds_write_b32 v21, a223 offset:31872\n\t" "s_waitcnt vmcnt(6)\n\t" "ds_write_b32 v21, a236 offset:256\n\t" \
   "ds_write_b32 v21, a237 offset:21376\n\t" "ds_write_b32 v21, a238 offset:10816\n\t" "ds_write_b32 v21, a239 offset:31936\n\t" \
   "s_waitcnt vmcnt(5)\n\t" "ds_write_b32 v21, a252 offset:5248\n\t" "ds_write_b32 v21, a253 offset:26368\n\t" \
   "ds_write_b32 v21, a254 offset:15808\n\t" "ds_write_b32 v21, a255 offset:36928\n\t" "s_waitcnt vmcnt(4)\n\t" \
   "ds_write_b32 v21, v204 offset:5312\n\t" "ds_write_b32 v21, v205 offset:26432\n\t" "ds_write_b32 v21, v206 offset:15872\n\t" \
   "ds_write_b32 v21, v207 offset:36992\n\t" "s_waitcnt vmcnt(3)\n\t" "ds_write_b32 v21, v220 offset:5376\n\t" \
   "ds_write_b32 v21, v221 offset:26496\n\t" "ds_write_b32 v21, v222 offset:15936\n\t" "ds_write_b32 v21, v223 offset:37056\n\t" \
   "s_waitcnt vmcnt(2)\n\t" "ds_write_b32 v21, v236 offset:5440\n\t" "ds_write_b32 v21, v237 offset:26560\n\t" \
   "ds_write_b32 v21, v238 offset:16000\n\t" "ds_write_b32 v21, v239 offset:37120\n\t" "s_waitcnt vmcnt(1)\n\t" \
   "ds_write_b32 v21, v252 offset:5504\n\t" "ds_write_b32 v21, v253 offset:26624\n\t" "ds_write_b32 v21, v254 offset:16064\n\t" \
   "ds_write_b32 v21, v255 offset:37184\n\t" "s_waitcnt lgkmcnt(0)\n\t" "s_barrier\n\t" LDSTAMP(9) "ds_read_b64_tr_b16 a[172:173], v22 offset:0\n\t" \
   "ds_read_b64_tr_b16 a[174:175], v22 offset:2624\n\t" "ds_read_b64_tr_b16 a[188:189], v22 offset:5248\n\t" \
   "ds_read_b64_tr_b16 a[190:191], v22 offset:7872\n\t" "ds_read_b64_tr_b16 a[204:205], v22 offset:16\n\t" \
   "ds_read_b64_tr_b16 a[206:207], v22 offset:2640\n\t" "ds_read_b64_tr_b16 a[220:221], v22 offset:5264\n\t" \
   "ds_read_b64_tr_b16 a[222:223], v22 offset:7888\n\t" "ds_read_b64_tr_b16 a[236:237], v22 offset:32\n\t" \
   "ds_read_b64_tr_b16 a[238:239], v22 offset:2656\n\t" "ds_read_b64_tr_b16 a[252:253], v22 offset:5280\n\t" \
   "ds_read_b64_tr_b16 a[254:255], v22 offset:7904\n\t" "ds_read_b64_tr_b16 v[204:205], v22 offset:48\n\t" \
   "ds_read_b64_tr_b16 v[206:207], v22 offset:2672\n\t" "ds_read_b64_tr_b16 v[220:221], v22 offset:5296\n\t" \
   "ds_read_b64_tr_b16 v[222:223], v22 offset:7920\n\t" "ds_read_b64_tr_b16 v[236:237], v22 offset:64\n\t" \
   "ds_read_b64_tr_b16 v[238:239], v22 offset:2688\n\t" "ds_read_b64_tr_b16 v[252:253], v22 offset:5312\n\t" \
   "ds_read_b64_tr_b16 v[254:255], v22 offset:7936\n\t" "s_waitcnt vmcnt(0) lgkmcnt(0)\n\t" "s_mov_b32 exec_lo, 1\n\t" "s_mov_b32 exec_hi, 1\n\t" \
   "s_nop 7\n\t" "s_nop 3\n\t" "ds_write_b128 v27, v[32:35] offset:0\n\t" "ds_write_b128 v27, v[36:39] offset:16\n\t" \
   "ds_write_b128 v27, v[40:43] offset:32\n\t" "ds_write_b128 v27, v[44:47] offset:48\n\t" "ds_write_b128 v27, v[48:51] offset:64\n\t" \
   "ds_write_b128 v27, v[52:55] offset:80\n\t" "ds_write_b128 v27, v[56:59] offset:96\n\t" "ds_write_b128 v27, v[60:63] offset:112\n\t" \
   "s_mov_b64 exec, -1\n\t" "s_waitcnt lgkmcnt(0)\n\t" "s_barrier"
#define LDV_CLOB "v24", "v25", "v26", "v32", "v33", "v34", "v35", "v36", "v37", "v38", "v39", "v40", "v41", "v42", "v43", "v44", "v45", "v46", "v47", "v48", "v49", "v50", "v51", "v52", "v53", "v54", "v55", "v56", "v57", "v58", "v59", "v60", "v61", "v62", "v63", "v64", "v65", "v66", "v67", "v68", "v69", "v70", "v71", "v72", "v73", "v74", "v75", "v76", "v77", "v78", "v79", "v80", "v81", "v82", "v83", "v84", "v85", "v86", "v87", "v88", "v89", "v90", "v91", "v92", "v93", "v94", "v95", "v96", "v97", "v98", "v99", "v100", "v101", "v102", "v103", "v104", "v105", "v106", "v107", "v108", "v109", "v110", "v111", "v120", "v121", "v122", "v123", "s40", "s41"
#define LDY_ASM \
   "buffer_load_dword v26, v113, %[rw], 0 offen sc1\n\t" "s_mov_b32 s41, 0\n\t" "buffer_load_dwordx4 a[0:3], v16, %[ra], s41 offen\n\t" \
   "s_mov_b32 s41, 131072\n\t" "buffer_load_dwordx4 a[64:67], v16, %[ra], s41 offen\n\t" "s_mov_b32 s41, 524288\n\t" \
   "buffer_load_dwordx4 a[8:11], v16, %[ra], s41 offen\n\t" "s_mov_b32 s41, 655360\n\t" "buffer_load_dwordx4 a[72:75], v16, %[ra], s41 offen\n\t" \
   "s_mov_b32 s41, 1048576\n\t" "buffer_load_dwordx4 a[16:19], v16, %[ra], s41 offen\n\t" "s_mov_b32 s41, 1179648\n\t" \
   "buffer_load_dwordx4 a[80:83], v16, %[ra], s41 offen\n\t" "s_mov_b32 s41, 1572864\n\t" "buffer_load_dwordx4 a[24:27], v16, %[ra], s41 offen\n\t" \
   "s_mov_b32 s41, 1703936\n\t" "buffer_load_dwordx4 a[88:91], v16, %[ra], s41 offen\n\t" "s_mov_b32 s41, 2097152\n\t" \
   "buffer_load_dwordx4 a[32:35], v16, %[ra], s41 offen\n\t" "s_mov_b32 s41, 2228224\n\t" "buffer_load_dwordx4 a[96:99], v16, %[ra], s41 offen\n\t" \
   "s_mov_b32 s41, 2621440\n\t" "buffer_load_dwordx4 a[40:43], v16, %[ra], s41 offen\n\t" "s_mov_b32 s41, 2752512\n\t" \
   "buffer_load_dwordx4 a[104:107], v16, %[ra], s41 offen\n\t" "s_mov_b32 s41, 3145728\n\t" "buffer_load_dwordx4 a[48:51], v16, %[ra], s41 offen\n\t" \
   "s_mov_b32 s41, 3276800\n\t" "buffer_load_dwordx4 a[112:115], v16, %[ra], s41 offen\n\t" "s_mov_b32 s41, 3670016\n\t" \
   "buffer_load_dwordx4 a[56:59], v16, %[ra], s41 offen\n\t" "s_mov_b32 s41, 3801088\n\t" "buffer_load_dwordx4 a[120:123], v16, %[ra], s41 offen\n\t" \
   "s_waitcnt vmcnt(16)\n\t" "v_add_u32 v26, 1, v26\n\t" "buffer_store_dword v26, v114, %[rw], 0 offen sc1\n\t" "ds_write_b32 v115, v26\n\t" LDSTAMP(0) \
   "s_waitcnt vmcnt(15)\n\t" LDSTAMP(1) "ds_write_b128 v20, a[0:3] offset:0\n\t" "s_mov_b32 s41, 4194304\n\t" \
   "buffer_load_dwordx4 v[192:195], v16, %[ra], s41 offen\n\t" "s_waitcnt vmcnt(15)\n\t" "ds_write_b128 v20, a[64:67] offset:4096\n\t" \
   "s_mov_b32 s41, 4325376\n\t" "buffer_load_dwordx4 v[208:211], v16, %[ra], s41 offen\n\t" "s_waitcnt vmcnt(15)\n\t" \
   "ds_write_b128 v20, a[8:11] offset:8192\n\t" "s_mov_b32 s41, 4718592\n\t" "buffer_load_dwordx4 v[200:203], v16, %[ra], s41 offen\n\t" \
   "s_waitcnt vmcnt(15)\n\t" "ds_write_b128 v20, a[72:75] offset:12288\n\t" "s_mov_b32 s41, 4849664\n\t" \
   "buffer_load_dwordx4 v[216:219], v16, %[ra], s41 offen\n\t" "s_waitcnt vmcnt(15)\n\t" "ds_write_b128 v20, a[16:19] offset:16384\n\t" \
   "s_mov_b32 s41, 262144\n\t" "buffer_load_dwordx4 a[4:7], v16, %[ra], s41 offen\n\t" "s_waitcnt vmcnt(15)\n\t" \
   "ds_write_b128 v20, a[80:83] offset:20480\n\t" "s_mov_b32 s41, 393216\n\t" "buffer_load_dwordx4 a[68:71], v16, %[ra], s41 offen\n\t" \
   "s_waitcnt vmcnt(15)\n\t" "ds_write_b128 v20, a[24:27] offset:24576\n\t" "s_mov_b32 s41, 786432\n\t" \
   "buffer_load_dwordx4 a[12:15], v16, %[ra], s41 offen\n\t" "s_waitcnt vmcnt(15)\n\t" "ds_write_b128 v20, a[88:91] offset:28672\n\t" \
   "s_mov_b32 s41, 917504\n\t" "buffer_load_dwordx4 a[76:79], v16, %[ra], s41 offen\n\t" "s_waitcnt vmcnt(15)\n\t" \
   "ds_write_b128 v20, a[32:35] offset:32768\n\t" "s_mov_b32 s41, 1310720\n\t" "buffer_load_dwordx4 a[20:23], v16, %[ra], s41 offen\n\t" \
   "s_waitcnt vmcnt(15)\n\t" "ds_write_b128 v20, a[96:99] offset:36864\n\t" "s_mov_b32 s41, 1441792\n\t" \
   "buffer_load_dwordx4 a[84:87], v16, %[ra], s41 offen\n\t" "s_waitcnt vmcnt(15)\n\t" "ds_write_b128 v20, a[40:43] offset:40960\n\t" \
   "s_mov_b32 s41, 1835008\n\t" "buffer_load_dwordx4 a[28:31], v16, %[ra], s41 offen\n\t" "s_waitcnt vmcnt(15)\n\t" \
   "ds_write_b128 v20, a[104:107] offset:45056\n\t" "s_mov_b32 s41, 1966080\n\t" "buffer_load_dwordx4 a[92:95], v16, %[ra], s41 offen\n\t" \
   "s_waitcnt vmcnt(15)\n\t" "ds_write_b128 v20, a[48:51] offset:49152\n\t" "s_mov_b32 s41, 2359296\n\t" \
   "buffer_load_dwordx4 a[36:39], v16, %[ra], s41 offen\n\t" "s_waitcnt vmcnt(15)\n\t" "ds_write_b128 v20, a[112:115] offset:53248\n\t" \
   "s_mov_b32 s41, 2490368\n\t" "buffer_load_dwordx4 a[100:103], v16, %[ra], s41 offen\n\t" "s_waitcnt vmcnt(15)\n\t" \
   "ds_write_b128 v20, a[56:59] offset:57344\n\t" "s_mov_b32 s41, 2883584\n\t" "buffer_load_dwordx4 a[44:47], v16, %[ra], s41 offen\n\t" \
   "s_waitcnt vmcnt(15)\n\t" "ds_write_b128 v20, a[120:123] offset:61440\n\t" "s_mov_b32 s41, 3014656\n\t" \
   "buffer_load_dwordx4 a[108:111], v16, %[ra], s41 offen\n\t" "s_waitcnt vmcnt(15)\n\t" "ds_write_b128 v23, v[192:195] offset:0\n\t" \
   "s_mov_b32 s41, 3407872\n\t" "buffer_load_dwordx4 a[52:55], v16, %[ra], s41 offen\n\t" "s_waitcnt vmcnt(15)\n\t" \
   "ds_write_b128 v23, v[208:211] offset:4096\n\t" "s_mov_b32 s41, 3538944\n\t" "buffer_load_dwordx4 a[116:119], v16, %[ra], s41 offen\n\t" \
   "s_waitcnt vmcnt(15)\n\t" "ds_write_b128 v23, v[200:203] offset:8192\n\t" "s_mov_b32 s41, 3932160\n\t" \
   "buffer_load_dwordx4 a[60:63], v16, %[ra], s41 offen\n\t" "s_waitcnt vmcnt(15)\n\t" "ds_write_b128 v23, v[216:219] offset:12288\n\t" \
   "s_mov_b32 s41, 4063232\n\t" "buffer_load_dwordx4 a[124:127], v16, %[ra], s41 offen\n\t" "s_waitcnt lgkmcnt(0)\n\t" "s_barrier\n\t" LDSTAMP(2) \
   "s_lshl_b32 s40, %[p0], 13\n\t" "v_add_u32 v24, s40, v18\n\t" "v_add_u32 v25, s40, v19\n\t" "s_add_u32 s40, s40, 8192\n\t" \
   "s_cmp_eq_u32 s40, 81920\n\t" "s_cselect_b32 s40, 0, s40\n\t" "ds_read_b64_tr_b16 a[0:1], v24 offset:0\n\t" \
   "ds_read_b64_tr_b16 a[2:3], v24 offset:4096\n\t" "ds_read_b64_tr_b16 a[64:65], v25 offset:0\n\t" "ds_read_b64_tr_b16 a[66:67], v25 offset:4096\n\t" \
   "v_add_u32 v24, s40, v18\n\t" "v_add_u32 v25, s40, v19\n\t" "s_add_u32 s40, s40, 8192\n\t" "s_cmp_eq_u32 s40, 81920\n\t" \
   "s_cselect_b32 s40, 0, s40\n\t" "ds_read_b64_tr_b16 a[8:9], v24 offset:0\n\t" "ds_read_b64_tr_b16 a[10:11], v24 offset:4096\n\t" \
   "ds_read_b64_tr_b16 a[72:73], v25 offset:0\n\t" "ds_read_b64_tr_b16 a[74:75], v25 offset:4096\n\t" "v_add_u32 v24, s40, v18\n\t" \
   "v_add_u32 v25, s40, v19\n\t" "s_add_u32 s40, s40, 8192\n\t" "s_cmp_eq_u32 s40, 81920\n\t" "s_cselect_b32 s40, 0, s40\n\t" \
   "ds_read_b64_tr_b16 a[16:17], v24 offset:0\n\t" "ds_read_b64_tr_b16 a[18:19], v24 offset:4096\n\t" "ds_read_b64_tr_b16 a[80:81], v25 offset:0\n\t" \
   "ds_read_b64_tr_b16 a[82:83], v25 offset:4096\n\t" "v_add_u32 v24, s40, v18\n\t" "v_add_u32 v25, s40, v19\n\t" "s_add_u32 s40, s40, 8192\n\t" \
   "s_cmp_eq_u32 s40, 81920\n\t" "s_cselect_b32 s40, 0, s40\n\t" "ds_read_b64_tr_b16 a[24:25], v24 offset:0\n\t" \
   "ds_read_b64_tr_b16 a[26:27], v24 offset:4096\n\t" "ds_read_b64_tr_b16 a[88:89], v25 offset:0\n\t" \
   "ds_read_b64_tr_b16 a[90:91], v25 offset:4096\n\t" "v_add_u32 v24, s40, v18\n\t" "v_add_u32 v25, s40, v19\n\t" "s_add_u32 s40, s40, 8192\n\t" \
   "s_cmp_eq_u32 s40, 81920\n\t" "s_cselect_b32 s40, 0, s40\n\t" "ds_read_b64_tr_b16 a[32:33], v24 offset:0\n\t" \
   "ds_read_b64_tr_b16 a[34:35], v24 offset:4096\n\t" "ds_read_b64_tr_b16 a[96:97], v25 offset:0\n\t" \
   "ds_read_b64_tr_b16 a[98:99], v25 offset:4096\n\t" "v_add_u32 v24, s40, v18\n\t" "v_add_u32 v25, s40, v19\n\t" "s_add_u32 s40, s40, 8192\n\t" \
   "s_cmp_eq_u32 s40, 81920\n\t" "s_cselect_b32 s40, 0, s40\n\t" "ds_read_b64_tr_b16 a[40:41], v24 offset:0\n\t" \
   "ds_read_b64_tr_b16 a[42:43], v24 offset:4096\n\t" "ds_read_b64_tr_b16 a[104:105], v25 offset:0\n\t" \
   "ds_read_b64_tr_b16 a[106:107], v25 offset:4096\n\t" "v_add_u32 v24, s40, v18\n\t" "v_add_u32 v25, s40, v19\n\t" "s_add_u32 s40, s40, 8192\n\t" \
   "s_cmp_eq_u32 s40, 81920\n\t" "s_cselect_b32 s40, 0, s40\n\t" "ds_read_b64_tr_b16 a[48:49], v24 offset:0\n\t" \
   "ds_read_b64_tr_b16 a[50:51], v24 offset:4096\n\t" "ds_read_b64_tr_b16 a[112:113], v25 offset:0\n\t" \
   "ds_read_b64_tr_b16 a[114:115], v25 offset:4096\n\t" "v_add_u32 v24, s40, v18\n\t" "v_add_u32 v25, s40, v19\n\t" "s_add_u32 s40, s40, 8192\n\t" \
   "s_cmp_eq_u32 s40, 81920\n\t" "s_cselect_b32 s40, 0, s40\n\t" "ds_read_b64_tr_b16 a[56:57], v24 offset:0\n\t" \
   "ds_read_b64_tr_b16 a[58:59], v24 offset:4096\n\t" "ds_read_b64_tr_b16 a[120:121], v25 offset:0\n\t" \
   "ds_read_b64_tr_b16 a[122:123], v25 offset:4096\n\t" "v_add_u32 v24, s40, v18\n\t" "v_add_u32 v25, s40, v19\n\t" "s_add_u32 s40, s40, 8192\n\t" \
   "s_cmp_eq_u32 s40, 81920\n\t" "s_cselect_b32 s40, 0, s40\n\t" "ds_read_b64_tr_b16 v[192:193], v24 offset:0\n\t" \
   "ds_read_b64_tr_b16 v[194:195], v24 offset:4096\n\t" "ds_read_b64_tr_b16 v[208:209], v25 offset:0\n\t" \
   "ds_read_b64_tr_b16 v[210:211], v25 offset:4096\n\t" "v_add_u32 v24, s40, v18\n\t" "v_add_u32 v25, s40, v19\n\t" "s_add_u32 s40, s40, 8192\n\t" \
   "s_cmp_eq_u32 s40, 81920\n\t" "s_cselect_b32 s40, 0, s40\n\t" "ds_read_b64_tr_b16 v[200:201], v24 offset:0\n\t" \
   "ds_read_b64_tr_b16 v[202:203], v24 offset:4096\n\t" "ds_read_b64_tr_b16 v[216:217], v25 offset:0\n\t" \
   "ds_read_b64_tr_b16 v[218:219], v25 offset:4096\n\t" "s_waitcnt vmcnt(15)\n\t" "s_waitcnt lgkmcnt(0)\n\t" "s_barrier\n\t" \
   "ds_write_b128 v20, a[4:7] offset:0\n\t" "s_mov_b32 s41, 4456448\n\t" "buffer_load_dwordx4 v[196:199], v16, %[ra], s41 offen\n\t" \
   "s_waitcnt vmcnt(15)\n\t" "ds_write_b128 v20, a[68:71] offset:4096\n\t" "s_mov_b32 s41, 4587520\n\t" \
   "buffer_load_dwordx4 v[212:215], v16, %[ra], s41 offen\n\t" "s_waitcnt vmcnt(15)\n\t" "ds_write_b128 v20, a[12:15] offset:8192\n\t" \
   "s_mov_b32 s41, 4980736\n\t" "buffer_load_dwordx4 v[204:207], v16, %[ra], s41 offen\n\t" "s_waitcnt vmcnt(15)\n\t" \
   "ds_write_b128 v20, a[76:79] offset:12288\n\t" "s_mov_b32 s41, 5111808\n\t" "buffer_load_dwordx4 v[220:223], v16, %[ra], s41 offen\n\t" \
   "s_waitcnt vmcnt(15)\n\t" "ds_write_b128 v20, a[20:23] offset:16384\n\t" "s_mov_b32 s41, 0\n\t" \
   "buffer_load_dwordx4 a[128:131], v17, %[rb], s41 offen\n\t" "s_waitcnt vmcnt(15)\n\t" "ds_write_b128 v20, a[84:87] offset:20480\n\t" \
   "s_mov_b32 s41, 256\n\t" "buffer_load_dwordx4 a[132:135], v17, %[rb], s41 offen\n\t" "s_waitcnt vmcnt(15)\n\t" \
   "ds_write_b128 v20, a[28:31] offset:24576\n\t" "s_mov_b32 s41, 512\n\t" "buffer_load_dwordx4 a[160:163], v17, %[rb], s41 offen\n\t" \
   "s_waitcnt vmcnt(15)\n\t" "ds_write_b128 v20, a[92:95] offset:28672\n\t" "s_mov_b32 s41, 768\n\t" \
   "buffer_load_dwordx4 a[164:167], v17, %[rb], s41 offen\n\t" "s_waitcnt vmcnt(15)\n\t" "ds_write_b128 v20, a[36:39] offset:32768\n\t" \
   "s_mov_b32 s41, 1024\n\t" "buffer_load_dwordx4 a[192:195], v17, %[rb], s41 offen\n\t" "s_waitcnt vmcnt(15)\n\t" \
   "ds_write_b128 v20, a[100:103] offset:36864\n\t" "s_mov_b32 s41, 20480\n\t" "buffer_load_dwordx4 a[196:199], v17, %[rb], s41 offen\n\t" \
   "s_waitcnt vmcnt(15)\n\t" "ds_write_b128 v20, a[44:47] offset:40960\n\t" "s_mov_b32 s41, 20736\n\t" \
   "buffer_load_dwordx4 a[224:227], v17, %[rb], s41 offen\n\t" "s_waitcnt vmcnt(15)\n\t" "ds_write_b128 v20, a[108:111] offset:45056\n\t" \
   "s_mov_b32 s41, 20992\n\t" "buffer_load_dwordx4 a[228:231], v17, %[rb], s41 offen\n\t" "s_waitcnt vmcnt(15)\n\t" \
   "ds_write_b128 v20, a[52:55] offset:49152\n\t" "s_mov_b32 s41, 21248\n\t" "buffer_load_dwordx4 v[224:227], v17, %[rb], s41 offen\n\t" \
   "s_waitcnt vmcnt(15)\n\t" "ds_write_b128 v20, a[116:119] offset:53248\n\t" "s_mov_b32 s41, 21504\n\t" \
   "buffer_load_dwordx4 v[228:231], v17, %[rb], s41 offen\n\t" "s_waitcnt vmcnt(15)\n\t" "ds_write_b128 v20, a[60:63] offset:57344\n\t" \
   "s_mov_b32 s41, 40960\n\t" "buffer_load_dwordx4 a[136:139], v17, %[rb], s41 offen\n\t" "s_waitcnt vmcnt(15)\n\t" \
   "ds_write_b128 v20, a[124:127] offset:61440\n\t" "s_mov_b32 s41, 41216\n\t" "buffer_load_dwordx4 a[140:143], v17, %[rb], s41 offen\n\t" \
   "s_waitcnt vmcnt(15)\n\t" "ds_write_b128 v23, v[196:199] offset:0\n\t" "s_mov_b32 s41, 41472\n\t" \
   "buffer_load_dwordx4 a[168:171], v17, %[rb], s41 offen\n\t" "s_waitcnt vmcnt(15)\n\t" "ds_write_b128 v23, v[212:215] offset:4096\n\t" \
   "s_mov_b32 s41, 41728\n\t" "buffer_load_dwordx4 a[172:175], v17, %[rb], s41 offen\n\t" "s_waitcnt vmcnt(15)\n\t" \
   "ds_write_b128 v23, v[204:207] offset:8192\n\t" "s_mov_b32 s41, 41984\n\t" "buffer_load_dwordx4 a[200:203], v17, %[rb], s41 offen\n\t" \
   "s_waitcnt vmcnt(15)\n\t" "ds_write_b128 v23, v[220:223] offset:12288\n\t" "s_mov_b32 s41, 61440\n\t" \
   "buffer_load_dwordx4 a[204:207], v17, %[rb], s41 offen\n\t" "s_waitcnt lgkmcnt(0)\n\t" "s_barrier\n\t" LDSTAMP(3) "s_lshl_b32 s40, %[p0], 13\n\t" \
   "v_add_u32 v24, s40, v18\n\t" "v_add_u32 v25, s40, v19\n\t" "s_add_u32 s40, s40, 8192\n\t" "s_cmp_eq_u32 s40, 81920\n\t" \
   "s_cselect_b32 s40, 0, s40\n\t" "ds_read_b64_tr_b16 a[4:5], v24 offset:0\n\t" "ds_read_b64_tr_b16 a[6:7], v24 offset:4096\n\t" \
   "ds_read_b64_tr_b16 a[68:69], v25 offset:0\n\t" "ds_read_b64_tr_b16 a[70:71], v25 offset:4096\n\t" "v_add_u32 v24, s40, v18\n\t" \
   "v_add_u32 v25, s40, v19\n\t" "s_add_u32 s40, s40, 8192\n\t" "s_cmp_eq_u32 s40, 81920\n\t" "s_cselect_b32 s40, 0, s40\n\t" \
   "ds_read_b64_tr_b16 a[12:13], v24 offset:0\n\t" "ds_read_b64_tr_b16 a[14:15], v24 offset:4096\n\t" "ds_read_b64_tr_b16 a[76:77], v25 offset:0\n\t" \
   "ds_read_b64_tr_b16 a[78:79], v25 offset:4096\n\t" "v_add_u32 v24, s40, v18\n\t" "v_add_u32 v25, s40, v19\n\t" "s_add_u32 s40, s40, 8192\n\t" \
   "s_cmp_eq_u32 s40, 81920\n\t" "s_cselect_b32 s40, 0, s40\n\t" "ds_read_b64_tr_b16 a[20:21], v24 offset:0\n\t" \
   "ds_read_b64_tr_b16 a[22:23], v24 offset:4096\n\t" "ds_read_b64_tr_b16 a[84:85], v25 offset:0\n\t" \
   "ds_read_b64_tr_b16 a[86:87], v25 offset:4096\n\t" "v_add_u32 v24, s40, v18\n\t" "v_add_u32 v25, s40, v19\n\t" "s_add_u32 s40, s40, 8192\n\t" \
   "s_cmp_eq_u32 s40, 81920\n\t" "s_cselect_b32 s40, 0, s40\n\t" "ds_read_b64_tr_b16 a[28:29], v24 offset:0\n\t" \
   "ds_read_b64_tr_b16 a[30:31], v24 offset:4096\n\t" "ds_read_b64_tr_b16 a[92:93], v25 offset:0\n\t" \
   "ds_read_b64_tr_b16 a[94:95], v25 offset:4096\n\t" "v_add_u32 v24, s40, v18\n\t" "v_add_u32 v25, s40, v19\n\t" "s_add_u32 s40, s40, 8192\n\t" \
   "s_cmp_eq_u32 s40, 81920\n\t" "s_cselect_b32 s40, 0, s40\n\t" "ds_read_b64_tr_b16 a[36:37], v24 offset:0\n\t" \
   "ds_read_b64_tr_b16 a[38:39], v24 offset:4096\n\t" "ds_read_b64_tr_b16 a[100:101], v25 offset:0\n\t" \
   "ds_read_b64_tr_b16 a[102:103], v25 offset:4096\n\t" "v_add_u32 v24, s40, v18\n\t" "v_add_u32 v25, s40, v19\n\t" "s_add_u32 s40, s40, 8192\n\t" \
   "s_cmp_eq_u32 s40, 81920\n\t" "s_cselect_b32 s40, 0, s40\n\t" "ds_read_b64_tr_b16 a[44:45], v24 offset:0\n\t" \
   "ds_read_b64_tr_b16 a[46:47], v24 offset:4096\n\t" "ds_read_b64_tr_b16 a[108:109], v25 offset:0\n\t" \
   "ds_read_b64_tr_b16 a[110:111], v25 offset:4096\n\t" "v_add_u32 v24, s40, v18\n\t" "v_add_u32 v25, s40, v19\n\t" "s_add_u32 s40, s40, 8192\n\t" \
   "s_cmp_eq_u32 s40, 81920\n\t" "s_cselect_b32 s40, 0, s40\n\t" "ds_read_b64_tr_b16 a[52:53], v24 offset:0\n\t" \
   "ds_read_b64_tr_b16 a[54:55], v24 offset:4096\n\t" "ds_read_b64_tr_b16 a[116:117], v25 offset:0\n\t" \
   "ds_read_b64_tr_b16 a[118:119], v25 offset:4096\n\t" "v_add_u32 v24, s40, v18\n\t" "v_add_u32 v25, s40, v19\n\t" "s_add_u32 s40, s40, 8192\n\t" \
   "s_cmp_eq_u32 s40, 81920\n\t" "s_cselect_b32 s40, 0, s40\n\t" "ds_read_b64_tr_b16 a[60:61], v24 offset:0\n\t" \
   "ds_read_b64_tr_b16 a[62:63], v24 offset:4096\n\t" "ds_read_b64_tr_b16 a[124:125], v25 offset:0\n\t" \
   "ds_read_b64_tr_b16 a[126:127], v25 offset:4096\n\t" "v_add_u32 v24, s40, v18\n\t" "v_add_u32 v25, s40, v19\n\t" "s_add_u32 s40, s40, 8192\n\t" \
   "s_cmp_eq_u32 s40, 81920\n\t" "s_cselect_b32 s40, 0, s40\n\t" "ds_read_b64_tr_b16 v[196:197], v24 offset:0\n\t" \
   "ds_read_b64_tr_b16 v[198:199], v24 offset:4096\n\t" "ds_read_b64_tr_b16 v[212:213], v25 offset:0\n\t" \
   "ds_read_b64_tr_b16 v[214:215], v25 offset:4096\n\t" "v_add_u32 v24, s40, v18\n\t" "v_add_u32 v25, s40, v19\n\t" "s_add_u32 s40, s40, 8192\n\t" \
   "s_cmp_eq_u32 s40, 81920\n\t" "s_cselect_b32 s40, 0, s40\n\t" "ds_read_b64_tr_b16 v[204:205], v24 offset:0\n\t" \
   "ds_read_b64_tr_b16 v[206:207], v24 offset:4096\n\t" "ds_read_b64_tr_b16 v[220:221], v25 offset:0\n\t" \
   "ds_read_b64_tr_b16 v[222:223], v25 offset:4096\n\t" "s_waitcnt vmcnt(15)\n\t" "s_waitcnt lgkmcnt(0)\n\t" "s_barrier\n\t" \
   "ds_write_b32 v21, a128 offset:0\n\t" "ds_write_b32 v21, a129 offset:21120\n\t" "ds_write_b32 v21, a130 offset:10560\n\t" \
   "ds_write_b32 v21, a131 offset:31680\n\t" "s_mov_b32 s41, 61696\n\t" "buffer_load_dwordx4 a[232:235], v17, %[rb], s41 offen\n\t" \
   "s_waitcnt vmcnt(15)\n\t" "ds_write_b32 v21, a132 offset:64\n\t" "ds_write_b32 v21, a133 offset:21184\n\t" "ds_write_b32 v21, a134 offset:10624\n\t" \
   "ds_write_b32 v21, a135 offset:31744\n\t" "s_mov_b32 s41, 61952\n\t" "buffer_load_dwordx4 a[236:239], v17, %[rb], s41 offen\n\t" \
   "s_waitcnt vmcnt(15)\n\t" "ds_write_b32 v21, a160 offset:128\n\t" "ds_write_b32 v21, a161 offset:21248\n\t" \
   "ds_write_b32 v21, a162 offset:10688\n\t" "ds_write_b32 v21, a163 offset:31808\n\t" "s_mov_b32 s41, 62208\n\t" \
   "buffer_load_dwordx4 v[232:235], v17, %[rb], s41 offen\n\t" "s_waitcnt vmcnt(15)\n\t" "ds_write_b32 v21, a164 offset:192\n\t" \
   "ds_write_b32 v21, a165 offset:21312\n\t" "ds_write_b32 v21, a166 offset:10752\n\t" "ds_write_b32 v21, a167 offset:31872\n\t" \
   "s_mov_b32 s41, 62464\n\t" "buffer_load_dwordx4 v[236:239], v17, %[rb], s41 offen\n\t" "s_waitcnt vmcnt(15)\n\t" \
   "ds_write_b32 v21, a192 offset:256\n\t" "ds_write_b32 v21, a193 offset:21376\n\t" "ds_write_b32 v21, a194 offset:10816\n\t" \
   "ds_write_b32 v21, a195 offset:31936\n\t" "s_mov_b32 s41, 81920\n\t" "buffer_load_dwordx4 a[144:147], v17, %[rb], s41 offen\n\t" \
   "s_waitcnt vmcnt(15)\n\t" "ds_write_b32 v21, a196 offset:5248\n\t" "ds_write_b32 v21, a197 offset:26368\n\t" \
   "ds_write_b32 v21, a198 offset:15808\n\t" "ds_write_b32 v21, a199 offset:36928\n\t" "s_mov_b32 s41, 82176\n\t" \
   "buffer_load_dwordx4 a[148:151], v17, %[rb], s41 offen\n\t" "s_waitcnt vmcnt(15)\n\t" "ds_write_b32 v21, a224 offset:5312\n\t" \
   "ds_write_b32 v21, a225 offset:26432\n\t" "ds_write_b32 v21, a226 offset:15872\n\t" "ds_write_b32 v21, a227 offset:36992\n\t" \
   "s_mov_b32 s41, 82432\n\t" "buffer_load_dwordx4 a[176:179], v17, %[rb], s41 offen\n\t" "s_waitcnt vmcnt(15)\n\t" \
   "ds_write_b32 v21, a228 offset:5376\n\t" "ds_write_b32 v21, a229 offset:26496\n\t" "ds_write_b32 v21, a230 offset:15936\n\t" \
   "ds_write_b32 v21, a231 offset:37056\n\t" "s_mov_b32 s41, 82688\n\t" "buffer_load_dwordx4 a[180:183], v17, %[rb], s41 offen\n\t" \
   "s_waitcnt vmcnt(15)\n\t" "ds_write_b32 v21, v224 offset:5440\n\t" "ds_write_b32 v21, v225 offset:26560\n\t" \
   "ds_write_b32 v21, v226 offset:16000\n\t" "ds_write_b32 v21, v227 offset:37120\n\t" "s_mov_b32 s41, 82944\n\t" \
   "buffer_load_dwordx4 a[208:211], v17, %[rb], s41 offen\n\t" "s_waitcnt vmcnt(15)\n\t" "ds_write_b32 v21, v228 offset:5504\n\t" \
   "ds_write_b32 v21, v229 offset:26624\n\t" "ds_write_b32 v21, v230 offset:16064\n\t" "ds_write_b32 v21, v231 offset:37184\n\t" \
   "s_mov_b32 s41, 102400\n\t" "buffer_load_dwordx4 a[212:215], v17, %[rb], s41 offen\n\t" "s_waitcnt lgkmcnt(0)\n\t" "s_barrier\n\t" LDSTAMP(4) \
   "ds_read_b64_tr_b16 a[128:129], v22 offset:0\n\t" "ds_read_b64_tr_b16 a[130:131], v22 offset:656\n\t" \
   "ds_read_b64_tr_b16 a[132:133], v22 offset:2624\n\t" "ds_read_b64_tr_b16 a[134:135], v22 offset:3280\n\t" \
   "ds_read_b64_tr_b16 a[160:161], v22 offset:16\n\t" "ds_read_b64_tr_b16 a[162:163], v22 offset:672\n\t" \
   "ds_read_b64_tr_b16 a[164:165], v22 offset:2640\n\t" "ds_read_b64_tr_b16 a[166:167], v22 offset:3296\n\t" \
   "ds_read_b64_tr_b16 a[192:193], v22 offset:32\n\t" "ds_read_b64_tr_b16 a[194:195], v22 offset:688\n\t" \
   "ds_read_b64_tr_b16 a[196:197], v22 offset:2656\n\t" "ds_read_b64_tr_b16 a[198:199], v22 offset:3312\n\t" \
   "ds_read_b64_tr_b16 a[224:225], v22 offset:48\n\t" "ds_read_b64_tr_b16 a[226:227], v22 offset:704\n\t" \
   "ds_read_b64_tr_b16 a[228:229], v22 offset:2672\n\t" "ds_read_b64_tr_b16 a[230:231], v22 offset:3328\n\t" \
   "ds_read_b64_tr_b16 v[224:225], v22 offset:64\n\t" "ds_read_b64_tr_b16 v[226:227], v22 offset:720\n\t" \
   "ds_read_b64_tr_b16 v[228:229], v22 offset:2688\n\t" "ds_read_b64_tr_b16 v[230:231], v22 offset:3344\n\t" "s_waitcnt vmcnt(15)\n\t" \
   "s_waitcnt lgkmcnt(0)\n\t" "s_barrier\n\t" "ds_write_b32 v21, a136 offset:0\n\t" "ds_write_b32 v21, a137 offset:21120\n\t" \
   "ds_write_b32 v21, a138 offset:10560\n\t" "ds_write_b32 v21, a139 offset:31680\n\t" "s_mov_b32 s41, 102656\n\t" \
   "buffer_load_dwordx4 a[240:243], v17, %[rb], s41 offen\n\t" "s_waitcnt vmcnt(15)\n\t" "ds_write_b32 v21, a140 offset:64\n\t" \
   "ds_write_b32 v21, a141 offset:21184\n\t" "ds_write_b32 v21, a142 offset:10624\n\t" "ds_write_b32 v21, a143 offset:31744\n\t" \
   "s_mov_b32 s41, 102912\n\t" "buffer_load_dwordx4 a[244:247], v17, %[rb], s41 offen\n\t" "s_waitcnt vmcnt(15)\n\t" \
   "ds_write_b32 v21, a168 offset:128\n\t" "ds_write_b32 v21, a169 offset:21248\n\t" "ds_write_b32 v21, a170 offset:10688\n\t" \
   "ds_write_b32 v21, a171 offset:31808\n\t" "s_mov_b32 s41, 103168\n\t" "buffer_load_dwordx4 v[240:243], v17, %[rb], s41 offen\n\t" \
   "s_waitcnt vmcnt(15)\n\t" "ds_write_b32 v21, a172 offset:192\n\t" "ds_write_b32 v21, a173 offset:21312\n\t" \
   "ds_write_b32 v21, a174 offset:10752\n\t" "ds_write_b32 v21, a175 offset:31872\n\t" "s_mov_b32 s41, 103424\n\t" \
   "buffer_load_dwordx4 v[244:247], v17, %[rb], s41 offen\n\t" "s_waitcnt vmcnt(15)\n\t" "ds_write_b32 v21, a200 offset:256\n\t" \
   "ds_write_b32 v21, a201 offset:21376\n\t" "ds_write_b32 v21, a202 offset:10816\n\t" "ds_write_b32 v21, a203 offset:31936\n\t" \
   "s_mov_b32 s41, 122880\n\t" "buffer_load_dwordx4 a[152:155], v17, %[rb], s41 offen\n\t" "s_waitcnt vmcnt(15)\n\t" \
   "ds_write_b32 v21, a204 offset:5248\n\t" "ds_write_b32 v21, a205 offset:26368\n\t" "ds_write_b32 v21, a206 offset:15808\n\t" \
   "ds_write_b32 v21, a207 offset:36928\n\t" "s_mov_b32 s41, 123136\n\t" "buffer_load_dwordx4 a[156:159], v17, %[rb], s41 offen\n\t" \
   "s_waitcnt vmcnt(15)\n\t" "ds_write_b32 v21, a232 offset:5312\n\t" "ds_write_b32 v21, a233 offset:26432\n\t" \
   "ds_write_b32 v21, a234 offset:15872\n\t" "ds_write_b32 v21, a235 offset:36992\n\t" "s_mov_b32 s41, 123392\n\t" \
   "buffer_load_dwordx4 a[184:187], v17, %[rb], s41 offen\n\t" "s_waitcnt vmcnt(15)\n\t" "ds_write_b32 v21, a236 offset:5376\n\t" \
   "ds_write_b32 v21, a237 offset:26496\n\t" "ds_write_b32 v21, a238 offset:15936\n\t" "ds_write_b32 v21, a239 offset:37056\n\t" \
   "s_mov_b32 s41, 123648\n\t" "buffer_load_dwordx4 a[188:191], v17, %[rb], s41 offen\n\t" "s_waitcnt vmcnt(15)\n\t" \
   "ds_write_b32 v21, v232 offset:5440\n\t" "ds_write_b32 v21, v233 offset:26560\n\t" "ds_write_b32 v21, v234 offset:16000\n\t" \
   "ds_write_b32 v21, v235 offset:37120\n\t" "s_mov_b32 s41, 123904\n\t" "buffer_load_dwordx4 a[216:219], v17, %[rb], s41 offen\n\t" \
   "s_waitcnt vmcnt(15)\n\t" "ds_write_b32 v21, v236 offset:5504\n\t" "ds_write_b32 v21, v237 offset:26624\n\t" \
   "ds_write_b32 v21, v238 offset:16064\n\t" "ds_write_b32 v21, v239 offset:37184\n\t" "s_mov_b32 s41, 143360\n\t" \
   "buffer_load_dwordx4 a[220:223], v17, %[rb], s41 offen\n\t" "s_waitcnt lgkmcnt(0)\n\t" "s_barrier\n\t" LDSTAMP(5) \
   "ds_read_b64_tr_b16 a[136:137], v22 offset:0\n\t" "ds_read_b64_tr_b16 a[138:139], v22 offset:656\n\t" \
   "ds_read_b64_tr_b16 a[140:141], v22 offset:2624\n\t" "ds_read_b64_tr_b16 a[142:143], v22 offset:3280\n\t" \
   "ds_read_b64_tr_b16 a[168:169], v22 offset:16\n\t" "ds_read_b64_tr_b16 a[170:171], v22 offset:672\n\t" \
   "ds_read_b64_tr_b16 a[172:173], v22 offset:2640\n\t" "ds_read_b64_tr_b16 a[174:175], v22 offset:3296\n\t" \
   "ds_read_b64_tr_b16 a[200:201], v22 offset:32\n\t" "ds_read_b64_tr_b16 a[202:203], v22 offset:688\n\t" \
   "ds_read_b64_tr_b16 a[204:205], v22 offset:2656\n\t" "ds_read_b64_tr_b16 a[206:207], v22 offset:3312\n\t" \
   "ds_read_b64_tr_b16 a[232:233], v22 offset:48\n\t" "ds_read_b64_tr_b16 a[234:235], v22 offset:704\n\t" \
   "ds_read_b64_tr_b16 a[236:237], v22 offset:2672\n\t" "ds_read_b64_tr_b16 a[238:239], v22 offset:3328\n\t" \
   "ds_read_b64_tr_b16 v[232:233], v22 offset:64\n\t" "ds_read_b64_tr_b16 v[234:235], v22 offset:720\n\t" \
   "ds_read_b64_tr_b16 v[236:237], v22 offset:2688\n\t" "ds_read_b64_tr_b16 v[238:239], v22 offset:3344\n\t" "s_waitcnt vmcnt(15)\n\t" \
   "s_waitcnt lgkmcnt(0)\n\t" "s_barrier\n\t" "ds_write_b32 v21, a144 offset:0\n\t" "ds_write_b32 v21, a145 offset:21120\n\t" \
   "ds_write_b32 v21, a146 offset:10560\n\t" "ds_write_b32 v21, a147 offset:31680\n\t" "s_mov_b32 s41, 143616\n\t" \
   "buffer_load_dwordx4 a[248:251], v17, %[rb], s41 offen\n\t" "s_waitcnt vmcnt(15)\n\t" "ds_write_b32 v21, a148 offset:64\n\t" \
   "ds_write_b32 v21, a149 offset:21184\n\t" "ds_write_b32 v21, a150 offset:10624\n\t" "ds_write_b32 v21, a151 offset:31744\n\t" \
   "s_mov_b32 s41, 143872\n\t" "buffer_load_dwordx4 a[252:255], v17, %[rb], s41 offen\n\t" "s_waitcnt vmcnt(15)\n\t" \
   "ds_write_b32 v21, a176 offset:128\n\t" "ds_write_b32 v21, a177 offset:21248\n\t" "ds_write_b32 v21, a178 offset:10688\n\t" \
   "ds_write_b32 v21, a179 offset:31808\n\t" "s_mov_b32 s41, 144128\n\t" "buffer_load_dwordx4 v[248:251], v17, %[rb], s41 offen\n\t" \
   "s_waitcnt vmcnt(15)\n\t" "ds_write_b32 v21, a180 offset:192\n\t" "ds_write_b32 v21, a181 offset:21312\n\t" \
   "ds_write_b32 v21, a182 offset:10752\n\t" "ds_write_b32 v21, a183 offset:31872\n\t" "s_mov_b32 s41, 144384\n\t" \
   "buffer_load_dwordx4 v[252:255], v17, %[rb], s41 offen\n\t" "s_waitcnt vmcnt(15)\n\t" "ds_write_b32 v21, a208 offset:256\n\t" \
   "ds_write_b32 v21, a209 offset:21376\n\t" "ds_write_b32 v21, a210 offset:10816\n\t" "ds_write_b32 v21, a211 offset:31936\n\t" \
   "s_waitcnt vmcnt(14)\n\t" "ds_write_b32 v21, a212 offset:5248\n\t" "ds_write_b32 v21, a213 offset:26368\n\t" \
   "ds_write_b32 v21, a214 offset:15808\n\t" "ds_write_b32 v21, a215 offset:36928\n\t" "s_waitcnt vmcnt(13)\n\t" \
   "ds_write_b32 v21, a240 offset:5312\n\t" "ds_write_b32 v21, a241 offset:26432\n\t" "ds_write_b32 v21, a242 offset:15872\n\t" \
   "ds_write_b32 v21, a243 offset:36992\n\t" "s_waitcnt vmcnt(12)\n\t" "ds_write_b32 v21, a244 offset:5376\n\t" \
   "ds_write_b32 v21, a245 offset:26496\n\t" "ds_write_b32 v21, a246 offset:15936\n\t" "ds_write_b32 v21, a247 offset:37056\n\t" \
   "s_waitcnt vmcnt(11)\n\t" "ds_write_b32 v21, v240 offset:5440\n\t" "ds_write_b32 v21, v241 offset:26560\n\t" \
   "ds_write_b32 v21, v242 offset:16000\n\t" "ds_write_b32 v21, v243 offset:37120\n\t" "s_waitcnt vmcnt(10)\n\t" \
   "ds_write_b32 v21, v244 offset:5504\n\t" "ds_write_b32 v21, v245 offset:26624\n\t" "ds_write_b32 v21, v246 offset:16064\n\t" \
   "ds_write_b32 v21, v247 offset:37184\n\t" "buffer_load_dwordx4 v[28:31], v112, %[rw], 0 offen sc1\n\t" "s_waitcnt lgkmcnt(0)\n\t" "s_barrier\n\t" \
   LDSTAMP(6) "ds_read_b64_tr_b16 a[144:145], v22 offset:0\n\t" "ds_read_b64_tr_b16 a[146:147], v22 offset:656\n\t" \
   "ds_read_b64_tr_b16 a[148:149], v22 offset:2624\n\t" "ds_read_b64_tr_b16 a[150:151], v22 offset:3280\n\t" \
   "ds_read_b64_tr_b16 a[176:177], v22 offset:16\n\t" "ds_read_b64_tr_b16 a[178:179], v22 offset:672\n\t" \
   "ds_read_b64_tr_b16 a[180:181], v22 offset:2640\n\t" "ds_read_b64_tr_b16 a[182:183], v22 offset:3296\n\t" \
   "ds_read_b64_tr_b16 a[208:209], v22 offset:32\n\t" "ds_read_b64_tr_b16 a[210:211], v22 offset:688\n\t" \
   "ds_read_b64_tr_b16 a[212:213], v22 offset:2656\n\t" "ds_read_b64_tr_b16 a[214:215], v22 offset:3312\n\t" \
   "ds_read_b64_tr_b16 a[240:241], v22 offset:48\n\t" "ds_read_b64_tr_b16 a[242:243], v22 offset:704\n\t" \
   "ds_read_b64_tr_b16 a[244:245], v22 offset:2672\n\t" "ds_read_b64_tr_b16 a[246:247], v22 offset:3328\n\t" \
   "ds_read_b64_tr_b16 v[240:241], v22 offset:64\n\t" "ds_read_b64_tr_b16 v[242:243], v22 offset:720\n\t" \
   "ds_read_b64_tr_b16 v[244:245], v22 offset:2688\n\t" "ds_read_b64_tr_b16 v[246:247], v22 offset:3344\n\t" "s_waitcnt vmcnt(10)\n\t" \
   "s_waitcnt lgkmcnt(0)\n\t" "s_barrier\n\t" "ds_write_b32 v21, a152 offset:0\n\t" "ds_write_b32 v21, a153 offset:21120\n\t" \
   "ds_write_b32 v21, a154 offset:10560\n\t" "ds_write_b32 v21, a155 offset:31680\n\t" "s_waitcnt vmcnt(9)\n\t" "ds_write_b32 v21, a156 offset:64\n\t" \
   "ds_write_b32 v21, a157 offset:21184\n\t" "ds_write_b32 v21, a158 offset:10624\n\t" "ds_write_b32 v21, a159 offset:31744\n\t" \
   "s_waitcnt vmcnt(8)\n\t" "ds_write_b32 v21, a184 offset:128\n\t" "ds_write_b32 v21, a185 offset:21248\n\t" "ds_write_b32 v21, a186 offset:10688\n\t" \
   "ds_write_b32 v21, a187 offset:31808\n\t" "s_waitcnt vmcnt(7)\n\t" "ds_write_b32 v21, a188 offset:192\n\t" "ds_write_b32 v21, a189 offset:21312\n\t" \
   "ds_write_b32 v21, a190 offset:10752\n\t" "ds_write_b32 v21, a191 offset:31872\n\t" "s_waitcnt vmcnt(6)\n\t" "ds_write_b32 v21, a216 offset:256\n\t" \
   "ds_write_b32 v21, a217 offset:21376\n\t" "ds_write_b32 v21, a218 offset:10816\n\t" "ds_write_b32 v21, a219 offset:31936\n\t" \
   "s_waitcnt vmcnt(5)\n\t" "ds_write_b32 v21, a220 offset:5248\n\t" "ds_write_b32 v21, a221 offset:26368\n\t" \
   "ds_write_b32 v21, a222 offset:15808\n\t" "ds_write_b32 v21, a223 offset:36928\n\t" "s_waitcnt vmcnt(4)\n\t" \
   "ds_write_b32 v21, a248 offset:5312\n\t" "ds_write_b32 v21, a249 offset:26432\n\t" "ds_write_b32 v21, a250 offset:15872\n\t" \
   "ds_write_b32 v21, a251 offset:36992\n\t" "s_waitcnt vmcnt(3)\n\t" "ds_write_b32 v21, a252 offset:5376\n\t" \
   "ds_write_b32 v21, a253 offset:26496\n\t" "ds_write_b32 v21, a254 offset:15936\n\t" "ds_write_b32 v21, a255 offset:37056\n\t" \
   "s_waitcnt vmcnt(2)\n\t" "ds_write_b32 v21, v248 offset:5440\n\t" "ds_write_b32 v21, v249 offset:26560\n\t" \
   "ds_write_b32 v21, v250 offset:16000\n\t" "ds_write_b32 v21, v251 offset:37120\n\t" "s_waitcnt vmcnt(1)\n\t" \
   "ds_write_b32 v21, v252 offset:5504\n\t" "ds_write_b32 v21, v253 offset:26624\n\t" "ds_write_b32 v21, v254 offset:16064\n\t" \
   "ds_write_b32 v21, v255 offset:37184\n\t" "s_waitcnt lgkmcnt(0)\n\t" "s_barrier\n\t" LDSTAMP(7) "ds_read_b64_tr_b16 a[152:153], v22 offset:0\n\t" \
   "ds_read_b64_tr_b16 a[154:155], v22 offset:656\n\t" "ds_read_b64_tr_b16 a[156:157], v22 offset:2624\n\t" \
   "ds_read_b64_tr_b16 a[158:159], v22 offset:3280\n\t" "ds_read_b64_tr_b16 a[184:185], v22 offset:16\n\t" \
   "ds_read_b64_tr_b16 a[186:187], v22 offset:672\n\t" "ds_read_b64_tr_b16 a[188:189], v22 offset:2640\n\t" \
   "ds_read_b64_tr_b16 a[190:191], v22 offset:3296\n\t" "ds_read_b64_tr_b16 a[216:217], v22 offset:32\n\t" \
   "ds_read_b64_tr_b16 a[218:219], v22 offset:688\n\t" "ds_read_b64_tr_b16 a[220:221], v22 offset:2656\n\t" \
   "ds_read_b64_tr_b16 a[222:223], v22 offset:3312\n\t" "ds_read_b64_tr_b16 a[248:249], v22 offset:48\n\t" \
   "ds_read_b64_tr_b16 a[250:251], v22 offset:704\n\t" "ds_read_b64_tr_b16 a[252:253], v22 offset:2672\n\t" \
   "ds_read_b64_tr_b16 a[254:255], v22 offset:3328\n\t" "ds_read_b64_tr_b16 v[248:249], v22 offset:64\n\t" \
   "ds_read_b64_tr_b16 v[250:251], v22 offset:720\n\t" "ds_read_b64_tr_b16 v[252:253], v22 offset:2688\n\t" \
   "ds_read_b64_tr_b16 v[254:255], v22 offset:3344\n\t" "s_waitcnt vmcnt(0) lgkmcnt(0)\n\t" "s_barrier"
#define LDY_CLOB "v24", "v25", "v26", "v32", "v33", "v34", "v35", "v36", "v37", "v38", "v39", "v40", "v41", "v42", "v43", "v44", "v45", "v46", "v47", "v48", "v49", "v50", "v51", "v52", "v53", "v54", "v55", "v56", "v57", "v58", "v59", "v60", "v61", "v62", "v63", "v64", "v65", "v66", "v67", "v68", "v69", "v70", "v71", "v72", "v73", "v74", "v75", "v76", "v77", "v78", "v79", "v80", "v81", "v82", "v83", "v84", "v85", "v86", "v87", "v88", "v89", "v90", "v91", "v92", "v93", "v94", "v95", "v96", "v97", "v98", "v99", "v100", "v101", "v102", "v103", "v104", "v105", "v106", "v107", "v108", "v109", "v110", "v111", "v120", "v121", "v122", "v123", "s40", "s41"
#define W_OPS "{a[0:31]}"(w[0]), "{a[32:63]}"(w[1]), "{a[64:95]}"(w[2]), "{a[96:127]}"(w[3]), "{a[128:159]}"(w[4]), "{a[160:191]}"(w[5]), \
              "{a[192:223]}"(w[6]), "{a[224:255]}"(w[7]), "{v[192:223]}"(w[8]), "{v[224:255]}"(w[9])

__device__ __forceinline__ int mem_lane(int lane) { return lane * 16; }
__device__ __forceinline__ int pair_of(int g) { return (g & 1) * 2 + (g >> 1); }

__device__ __forceinline__ void rec_p1(int wv_, const v32u (&w)[10], unsigned char* ws, const Pool& pool, int layer, int h, int xs, RecTune tn, unsigned long long* stamps) {
  const Clk0 c0 = clk_sample();
  const unsigned lds0 = (unsigned)(uintptr_t)(__attribute__((address_space(3))) unsigned char*)dyn_lds;
  const int lane = lane_id(), wv = wv_, g = lane >> 4, r = lane & 15;
  const v4u rs = make_rs(ws, (unsigned)OFF_END);
  const int p0 = wv < 3 ? 3 * wv : 8;
  v16u ad, la = (unsigned)lane;
  const int kqo = (int)(wv < 2 ? OFF_KF : OFF_QF) + ((h * RT + (wv & 1)) * 4 * 64 + lane) * 16;
  const int progo = (wv == 0 && lane == 0) ? prog_off(layer, 0, h) : OOB;
  {
    const int p2 = wv < 3 ? p0 + 2 : 0;
    ad[0] = W_MEM + (p0 >> 1) * WIN + (p0 & 1) * 1024 + mem_lane(lane);
    ad[1] = W_MEM + ((p0 + 1) >> 1) * WIN + ((p0 + 1) & 1) * 1024 + mem_lane(lane);
    ad[2] = W_MEM + (p2 >> 1) * WIN + (p2 & 1) * 1024 + mem_lane(lane);
    ad[3] = lane == 0 ? W_MISC + h * 64 + wv * 4 : OOB;
    ad[4] = copy_off(wv) + (h >> 2) * WIN + (h & 3) * 512 + (g >> 1) * 256 + (2 * (r & 7) + (r >> 3)) * 16 + (g & 1) * 8;
    ad[5] = g < 2 ? copy_off(wv + 4 * (r >> 3)) + (8 + (h >> 4)) * WIN + ((h >> 3) & 1) * 1024 + (((h >> 1) & 3) * 16 + (h & 1) * 8 + (r & 7)) * 16 + g * 8 : OOB;
    ad[6] = (r & 1) ? 0xEEEEu : 0x4444u;
    ad[7] = lds0 + (32 * wv + r) * 16 + g * 4;
    ad[8] = lds0 + (4 * g + (r >> 2)) * 16 + (r & 1) * 8;
    ad[9] = lds0 + L_M2 + wv * 3072 + lane * 16;
    ad[10] = kqo;
    ad[11] = M_TM + xs * 128 + (lane & 1) * 12;
    ad[12] = kqf_off(layer, 0, h) + (lane & (h < NFILL ? 7 : 3)) * 4;
    ad[13] = wv > 1 ? (int)OFF_PXF + ((h * 2 + (wv & 1)) * 64 + lane) * 16 : OOB;
    ad[14] = (g < 2 && wv > 1) ? (int)OFF_PXM + (((h >> 2) * 2 + (wv & 1)) * 64 + (h & 3) * 16 + r) * 16 + g * 8 : OOB;
    ad[15] = progo;
#pragma unroll
    for (int i = 0; i < 10; ++i) { int p = p0 + i; p = p >= 10 ? p - 10 : p; la[i] = lds0 + p * 1024 + lane * 16; }
    la[10] = lds0 + L_KQ + lane * 16;
    la[12] = lds0 + REC_ST + wv * 8;
    asm volatile("v_mov_b32 %0, 0xceabfb8f" : "=v"(la[13]));
    la[14] = 16 * (wv & 1) + r - 4 * g;
    la[15] = lds0 + L_KQ + (wv & 1) * 4096 + lane * 16;
  }
  __builtin_amdgcn_s_waitcnt(0x0F70);
  if (layer) __builtin_amdgcn_s_sleep(64);
  v4u clk = clk_init(c0);
  unsigned long long stat;
  asm volatile(P1_ASM
               : "=&{s[76:77]}"(stat), "+{s[56:59]}"(clk)
               : W_OPS, "{v[128:143]}"(ad), "{v[112:127]}"(la), [rs] "s"(rs), [sel] "s"(pool.sel), [pool] "s"(pool.base), [xs] "s"(xs),
                 [lad0] "s"(ladder(xs, xs & 1)), [s3] "s"(pool.s3), [m1] "s"(tn.m1), [l2s] "s"(lds0 + L_M2 + wv * 3072),
                 [hi8] "s"(0xFF00FF00FF00FF00ull), [wv] "s"(wv), [kf0] "s"((int)OFF_KF + h * RT * 4096),
                 [esc] "s"(pool.base + (NSEG - 1) * REG)
               : P1_CLOB RST_CLOB, "vcc", "scc", "m0", "memory");
  LDSBAR();
  {
    const unsigned one = 1u;
    const int so = (NSEG - 1) * 4096;
    VM_ST32(one, progo, so);
    VM_WAIT(0);
  }
}

__device__ __forceinline__ void rec_pv(int wv_, const v32u (&w)[10], unsigned char* ws, const Pool& pool, int layer, int h, int xs, RecTune tn, unsigned long long* stamps) {
  const Clk0 c0 = clk_sample();
  const unsigned lds0 = (unsigned)(uintptr_t)(__attribute__((address_space(3))) unsigned char*)dyn_lds;
  const int lane = lane_id(), wv = wv_, g = lane >> 4, r = lane & 15;
  const v4u rs = make_rs(ws, (unsigned)OFF_END);
  const int p0 = wv < 3 ? 3 * wv : 8, p2 = wv < 3 ? p0 + 2 : 0;
  v16u ad = (unsigned)lane, la = (unsigned)lane;
  ad[0] = W_MEM + (p0 >> 1) * WIN + (p0 & 1) * 1024 + mem_lane(lane);
  ad[1] = W_MEM + ((p0 + 1) >> 1) * WIN + ((p0 + 1) & 1) * 1024 + mem_lane(lane);
  ad[2] = W_MEM + (p2 >> 1) * WIN + (p2 & 1) * 1024 + mem_lane(lane);
  ad[3] = (r & 8) ? 0xEEEEu : 0x4444u;
  ad[4] = (wv * 10 + (g >> 1)) * 16384 + h * 256 + r * 16 + (g & 1) * 8;
  ad[5] = h * 16384 + wv * 2048 + lane * 16;
  ad[6] = (r & 1) ? 0xEEEEu : 0x4444u;
  ad[7] = lds0 + (32 * wv + r) * 16 + pair_of(g) * 4;
  ad[8] = lds0 + (4 * g + (r >> 2)) * 16 + (r & 1) * 8;
  ad[11] = M_TM + (4 + xs) * 128 + (lane & 1) * 12;
  ad[15] = (wv == 0 && lane == 0) ? prog_off(layer, 0, 32 + h) : OOB;
#pragma unroll
  for (int i = 0; i < 10; ++i) { int p = p0 + i; p = p >= 10 ? p - 10 : p; la[i] = lds0 + p * 1024 + lane * 16; }
  la[12] = lds0 + REC_ST + wv * 8;
  __builtin_amdgcn_s_waitcnt(0x0F70);
  if (layer) __builtin_amdgcn_s_sleep(64);
  v4u clk = clk_init(c0);
  unsigned long long stat;
  asm volatile(PV_ASM
               : "=&{s[76:77]}"(stat), "+{s[56:59]}"(clk)
               : W_OPS, "{v[128:143]}"(ad), "{v[112:127]}"(la), [rs] "s"(rs), [sel] "s"(pool.sel), [pool] "s"(pool.base), [xs] "s"(xs),
                 [lad0] "s"(ladder(xs, xs & 1)), [s3] "s"(pool.s3), [m1c] "s"(tn.m1 + tn.c2), [esc] "s"(pool.base + (NSEG - 1) * REG),
                 [ysel] "s"(pool.ysel), [yb] "s"(pool.base + P_Y)
               : PV_CLOB RST_CLOB, "vcc", "scc", "memory");
}

#define TSTART 5300
#define TSTART1 TSTART
#define TSTARU 3600
#define TD1 900u
#define TR2 400u
#define TEMA 1
#define TEMB 2
#define TBUP 48
#define TBDN 6
#define TLATE 520
#define TSEC 50
__device__ __forceinline__ unsigned track_dur(unsigned e, int m, int rob, int s) {
  const int lo = (int)e - (s < 8 ? 64 : (rob & 255)), hi = (int)e + (rob >> 8);
  if (s >= 8) m = (int)e + ((m - (int)e + (m > (int)e ? (1 << TEMA) - 1 : 0)) >> (m > (int)e ? TEMA : TEMB));
  return (unsigned)min(max(m, lo), hi);
}
#define TTS 200
#define TTU 240
__device__ __forceinline__ void rec_sched(unsigned char* ws, const Pool& pool, int layer, int c16, int xs, RecTune tn, unsigned long long* stamps) {
  const Clk0 c0 = clk_sample();
  const unsigned lds0 = (unsigned)(uintptr_t)(__attribute__((address_space(3))) unsigned char*)dyn_lds;
  const int lane = lane_id();
  const v4u rs = make_rs(ws, (unsigned)OFF_END);
  unsigned long long stat = 0;
  const int sm = M_STAMP + min(lane, 39) * 64, sp = W_MISC + (lane & 31) * 64;
  const int yo = M_TM + xs * 128, oc = (c16 == 0 && lane < 16) ? M_TM + lane * 128 : OOB;
  const unsigned cv = flag_finish<2>(rs, kqf_off(layer, 0, lane), 0xFFFFFFFFu);
  v4u clk = clk_init(c0);
  PH(3 + layer * 6)
  unsigned r2a = TR2, d1a = TD1, dd = TD1 + tn.m2, tp = 0, tm = 0, anchor = clk.x - dd;
  unsigned rr = (unsigned)min(max(wave_imax((int)(cv * 8u - clk.x) + (lane < 32 ? (layer ? TSTART1 : TSTART) : TSTARU)), 300), 4000);
  int bonus = 0;
  bool clean = false;
#pragma unroll 1
  for (int s = 0; s < NSEG - 1; ++s) {
    const int rgm = pool.region((s + NSEG - 1) & (NSEG - 1)), rg = pool.region(s);
    const unsigned slot = lds0 + L_T + (s & 1) * 32, ao = anchor;
    unsigned b2 = 1u;
    v4u b1 = {1u, 1u, 1u, 1u};
    if (s > 0) {
      asm volatile(WAIT_ASM : "+{s[56:59]}"(clk) : [ta] "s"(tm + TTS) : WAIT_CLOB, "scc", "memory");
      VM_LD32(b2, sm, rgm);
      VM_LD128(b1, sp, rgm, 0);
      asm volatile("s_waitcnt vmcnt(0)" : "+v"(b2), "+v"(b1)::"memory");
      anchor = tm;
      if (__builtin_expect(__any(b2 == 0xFFFFFFFFu || (int)(b2 - (tm + tn.m1 - tn.vis)) > 0), 0)) {
        STAT(3);
        for (unsigned spins = 0; __any(b2 == 0xFFFFFFFFu) && spins < SPIN_CAP; ++spins) {
          __builtin_amdgcn_s_sleep(2);
          VM_LD32(b2, sm, rgm);
          VM_WAIT1(0, b2);
        }
        const int d = wave_imax((int)(b2 - tm)) + tn.vis - tn.m1;
        anchor = tm + (d <= 0 ? 0 : (d <= tn.dlt ? tn.dlt + TSEC : d + TLATE));
      }
    }
    const unsigned tpo = tp, tmo = tm;
    tp = anchor + dd;
    tm = nosent(tp + rr);
    LDS_V4(slot) = v4u{tp, tm, (unsigned)(s + 1), anchor};
    if (anchor != tmo && s > 2) bonus = TBUP; else bonus = max(bonus - TBDN, 0);
    dd = d1a + tn.m2 + bonus;
    rr = r2a + bonus;
    {
      const v4u wq = v4u{tm, dd, rr, dd + rr};
      VM_ST128(wq, oc, rg);
    }
    if (s > 1 && !__any((b2 & 1u) != 0u)) r2a = track_dur(r2a, wave_imax((int)(b2 - tpo)), tn.rob, s);
    if (s > 1 && clean && !__any(((b1.x | b1.y | b1.z | b1.w) & 1u) != 0u))
      d1a = track_dur(d1a, wave_imax(max(max((int)(b1.x - ao), (int)(b1.y - ao)), max((int)(b1.z - ao), (int)(b1.w - ao)))), tn.rob, s);
    clean = anchor == tmo;
    while ((int)(tp + 40u - rtc() * 8u) > 0 && (int)(tp - rtc() * 8u) < 32000) __builtin_amdgcn_s_sleep(1);
    v4u lw;
    VM_LD128(lw, yo, rg, 0);
    VM_WAIT1(0, lw);
    const unsigned l0 = (unsigned)__builtin_amdgcn_readfirstlane(lw.x), l1 = (unsigned)__builtin_amdgcn_readfirstlane(lw.y),
                   l2 = (unsigned)__builtin_amdgcn_readfirstlane(lw.z);
    if (max(max(l0, l1), l2) != 0xFFFFFFFFu) { tm = l0; dd = l1; rr = l2; }
  }
}

__device__ __forceinline__ void rec_fin(unsigned char* ws, const Pool& pool, int layer, int c16, int xs, RecTune tn, unsigned long long* stamps) {
  const Clk0 c0 = clk_sample();
  const unsigned lds0 = (unsigned)(uintptr_t)(__attribute__((address_space(3))) unsigned char*)dyn_lds;
  const int lane = lane_id(), g = lane >> 4, r = lane & 15;
  const v4u rs = make_rs(ws, (unsigned)OFF_END);
  v8u ad;
  ad[0] = lane * 16;
  ad[1] = g * 256 + r * 8;
  ad[2] = (r & 1) ? 0x03020706u : 0x05040100u;
  ad[3] = (lane & 31) * 64;
  ad[4] = W_MEM + copy_off(((r >> 1) & 1) * 5) + (c16 >> 3) * WIN + (c16 & 7) * 256 + (2 * (2 * g + (r & 1)) + (r >> 3)) * 16 + ((r >> 2) & 1) * 8;
  ad[5] = lane == 0 ? M_STAMP + c16 * 64 : OOB;
  ad[6] = (r & 1) ? 0xEEEEu : 0x4444u;
  ad[7] = lds0 + L_T;
  __builtin_amdgcn_s_sleep(64);
  v4u clk = clk_init(c0);
  unsigned long long stat;
  asm volatile(FIN_ASM
               : "=&{s[76:77]}"(stat), "+{s[56:59]}"(clk), "+{v[16:23]}"(ad)
               : [rs] "s"(rs), [sel] "s"(pool.sel), [pool] "s"(pool.base), [xs] "s"(xs), [lad0] "s"(ladder(xs, xs & 1)), [s3] "s"(pool.s3),
                 [su] "s"((int)OFF_UF + c16 * 32768), [yb] "s"(pool.base + P_Y + c16 * 16384), [ysel] "s"(pool.ysel), [tu] "s"(TTU),
                 [stl] "s"(lds0 + REC_ST - 32), [uf] "s"(kqf_off(layer, 0, 32))
               : FIN_CLOB RST_CLOB, "vcc", "scc", "memory");
}

constexpr int PJ_BUF = 43 * 1024, PJ_X = 16384, PJ_LDS = PJ_X + 3 * PJ_BUF;
#define KQ_ASM \
   ".p2align 6\n\t" "s_nop 0\n\t" "s_mov_b32 s44, %[rdy]\n\t" "s_mov_b32 s45, 0\n\t" "s_mov_b32 s46, 0\n\t" "s_cmp_eq_u32 s44, -1\n\t" \
   "s_cbranch_scc1 68f\n\t" "s_mul_i32 s52, s45, 1536\n\t" "s_mul_i32 s51, s45, 40960\n\t" "s_add_u32 m0, s46, %[ldx]\n\t" "s_mov_b32 s53, s46\n\t" \
   "buffer_load_dwordx4 v16, %[rs], s51 offen offset:0 sc1 lds\n\t" "buffer_load_dwordx4 v16, %[rs], s51 offen offset:1024 sc1 lds\n\t" \
   "buffer_load_dwordx4 v16, %[rs], s51 offen offset:2048 sc1 lds\n\t" "buffer_load_dwordx4 v16, %[rs], s51 offen offset:3072 sc1 lds\n\t" \
   "s_add_u32 m0, m0, 4096\n\t" "s_add_u32 s51, s51, 4096\n\t" "buffer_load_dwordx4 v16, %[rs], s51 offen offset:0 sc1 lds\n\t" \
   "buffer_load_dwordx4 v16, %[rs], s51 offen offset:1024 sc1 lds\n\t" "buffer_load_dwordx4 v16, %[rs], s51 offen offset:2048 sc1 lds\n\t" \
   "buffer_load_dwordx4 v16, %[rs], s51 offen offset:3072 sc1 lds\n\t" "s_add_u32 m0, m0, 4096\n\t" "s_add_u32 s51, s51, 4096\n\t" \
   "buffer_load_dwordx4 v16, %[rs], s51 offen offset:0 sc1 lds\n\t" "buffer_load_dwordx4 v16, %[rs], s51 offen offset:1024 sc1 lds\n\t" \
   "s_add_u32 m0, s53, %[ld1]\n\t" "s_nop 0\n\t" "buffer_load_dwordx4 v17, %[rs], s52 offen sc1 lds\n\t" "s_mov_b32 s45, 1\n\t" \
   "s_mov_b32 s46, 44032\n\t" "s_mul_i32 s52, s45, 1536\n\t" "s_mul_i32 s51, s45, 40960\n\t" "s_add_u32 m0, s46, %[ldx]\n\t" "s_mov_b32 s53, s46\n\t" \
   "buffer_load_dwordx4 v16, %[rs], s51 offen offset:0 sc1 lds\n\t" "buffer_load_dwordx4 v16, %[rs], s51 offen offset:1024 sc1 lds\n\t" \
   "buffer_load_dwordx4 v16, %[rs], s51 offen offset:2048 sc1 lds\n\t" "buffer_load_dwordx4 v16, %[rs], s51 offen offset:3072 sc1 lds\n\t" \
   "s_add_u32 m0, m0, 4096\n\t" "s_add_u32 s51, s51, 4096\n\t" "buffer_load_dwordx4 v16, %[rs], s51 offen offset:0 sc1 lds\n\t" \
   "buffer_load_dwordx4 v16, %[rs], s51 offen offset:1024 sc1 lds\n\t" "buffer_load_dwordx4 v16, %[rs], s51 offen offset:2048 sc1 lds\n\t" \
   "buffer_load_dwordx4 v16, %[rs], s51 offen offset:3072 sc1 lds\n\t" "s_add_u32 m0, m0, 4096\n\t" "s_add_u32 s51, s51, 4096\n\t" \
   "buffer_load_dwordx4 v16, %[rs], s51 offen offset:0 sc1 lds\n\t" "buffer_load_dwordx4 v16, %[rs], s51 offen offset:1024 sc1 lds\n\t" \
   "s_add_u32 m0, s53, %[ld1]\n\t" "s_nop 0\n\t" "buffer_load_dwordx4 v17, %[rs], s52 offen sc1 lds\n\t" "68:\n\t" "v_mov_b32 v138, 0\n\t" \
   "v_mov_b32 v13, %[flw]\n\t" "v_and_b32 v12, -16, v13\n\t" "ds_write_b32 v13, v138\n\t" "ds_read_b128 v[32:35], v26 offset:0\n\t" \
   "ds_read_b128 v[36:39], v26 offset:16\n\t" "ds_read_b128 v[40:43], v26 offset:32\n\t" "ds_read_b128 v[44:47], v26 offset:48\n\t" \
   "ds_read_b128 v[48:51], v26 offset:64\n\t" "ds_read_b128 v[52:55], v26 offset:80\n\t" "ds_read_b128 v[56:59], v26 offset:96\n\t" \
   "ds_read_b128 v[60:63], v26 offset:112\n\t" "s_mov_b32 s40, 0\n\t" "s_mov_b32 s41, 0\n\t" "s_mov_b32 s42, 0\n\t" "s_mov_b32 s43, 44032\n\t" \
   "v_mov_b32 v28, v22\n\t" "v_mov_b32 v29, v23\n\t" "s_mov_b32 s45, 2\n\t" "s_mov_b32 s46, 88064\n\t" "s_waitcnt vmcnt(11) lgkmcnt(0)\n\t" \
   "s_barrier\n\t" "s_memrealtime s[54:55]\n\t" "v_mov_b32 v146, -1\n\t" "s_cmp_eq_u32 s44, -1\n\t" "s_cbranch_scc0 172f\n\t" "72:\n\t" \
   "ds_read_b128 v[160:163], v28 offset:0\n\t" "ds_read_b128 v[164:167], v28 offset:512\n\t" "ds_read_b128 v[168:171], v28 offset:1024\n\t" \
   "ds_read_b128 v[172:175], v28 offset:1536\n\t" "s_waitcnt lgkmcnt(0)\n\t" "v_mov_b32 v31, s54\n\t" "v_min_u32 v31, -2, v31\n\t" "1:\n\t" \
   "v_mov_b32 v26, v28\n\t" "v_mov_b32 v27, v29\n\t" "s_waitcnt lgkmcnt(2)\n\t" "v_mfma_f32_32x32x16_bf16 v[64:79], a[0:3], v[160:163], 0\n\t" \
   "ds_read_b128 v[176:179], v26 offset:2048\n\t" "v_mfma_f32_32x32x16_bf16 v[80:95], a[8:11], v[160:163], 0\n\t" "v_mul_f32 v11, 0x3e0293ee, v10\n\t" \
   "s_lshl_b32 s47, s41, 13\n\t" "s_sub_i32 s47, s47, 8192\n\t" "s_max_i32 s47, s47, 0\n\t" "s_cmp_eq_u32 s40, 0\n\t" \
   "s_cselect_b32 s48, 0x7ffffff0, 0\n\t" "v_mfma_f32_32x32x16_bf16 v[64:79], a[4:7], v[164:167], v[64:79]\n\t" \
   "ds_read_b128 v[180:183], v26 offset:2560\n\t" "v_or_b32 v30, s48, v21\n\t" "v_fmac_f32 v96, v32, v8\n\t" "v_fmac_f32 v97, v33, v8\n\t" \
   "v_fmac_f32 v98, v34, v8\n\t" "v_mfma_f32_32x32x16_bf16 v[80:95], a[12:15], v[164:167], v[80:95]\n\t" "v_fmac_f32 v99, v35, v8\n\t" \
   "v_fmac_f32 v100, v36, v8\n\t" "v_fmac_f32 v101, v37, v8\n\t" "v_fmac_f32 v102, v38, v8\n\t" "v_fmac_f32 v103, v39, v8\n\t" \
   "s_waitcnt lgkmcnt(2)\n\t" "v_mfma_f32_32x32x16_bf16 v[64:79], a[16:19], v[168:171], v[64:79]\n\t" "ds_read_b128 v[184:187], v26 offset:3072\n\t" \
   "v_fmac_f32 v104, v40, v8\n\t" "v_fmac_f32 v105, v41, v8\n\t" "v_fmac_f32 v106, v42, v8\n\t" "v_fmac_f32 v107, v43, v8\n\t" \
   "v_mfma_f32_32x32x16_bf16 v[80:95], a[24:27], v[168:171], v[80:95]\n\t" "v_fmac_f32 v108, v44, v8\n\t" "v_fmac_f32 v109, v45, v8\n\t" \
   "v_fmac_f32 v110, v46, v8\n\t" "v_fmac_f32 v111, v47, v8\n\t" "v_mul_f32 v96, v96, v11\n\t" "v_mul_f32 v97, v97, v11\n\t" \
   "v_mfma_f32_32x32x16_bf16 v[64:79], a[20:23], v[172:175], v[64:79]\n\t" "ds_read_b128 v[188:191], v26 offset:3584\n\t" "v_mul_f32 v98, v98, v11\n\t" \
   "v_mul_f32 v99, v99, v11\n\t" "v_mul_f32 v100, v100, v11\n\t" "v_mul_f32 v101, v101, v11\n\t" \
   "v_mfma_f32_32x32x16_bf16 v[80:95], a[28:31], v[172:175], v[80:95]\n\t" "v_mul_f32 v102, v102, v11\n\t" "v_mul_f32 v103, v103, v11\n\t" \
   "v_mul_f32 v104, v104, v11\n\t" "v_mul_f32 v105, v105, v11\n\t" "v_mul_f32 v106, v106, v11\n\t" "s_waitcnt lgkmcnt(2)\n\t" \
   "v_mfma_f32_32x32x16_bf16 v[64:79], a[32:35], v[176:179], v[64:79]\n\t" "ds_read_b128 v[160:163], v26 offset:4096\n\t" \
   "v_mul_f32 v107, v107, v11\n\t" "v_mul_f32 v108, v108, v11\n\t" "v_mul_f32 v109, v109, v11\n\t" "v_mul_f32 v110, v110, v11\n\t" \
   "v_mfma_f32_32x32x16_bf16 v[80:95], a[40:43], v[176:179], v[80:95]\n\t" "v_mul_f32 v111, v111, v11\n\t" "s_add_u32 s48, s47, %[qf]\n\t" \
   "v_cvt_pk_bf16_f32 v96, v96, v97\n\t" "v_cvt_pk_bf16_f32 v97, v98, v99\n\t" "v_cvt_pk_bf16_f32 v98, v104, v105\n\t" \
   "v_cvt_pk_bf16_f32 v99, v106, v107\n\t" "v_mfma_f32_32x32x16_bf16 v[64:79], a[36:39], v[180:183], v[64:79]\n\t" \
   "ds_read_b128 v[164:167], v26 offset:4608\n\t" "v_mfma_f32_32x32x16_bf16 v[80:95], a[44:47], v[180:183], v[80:95]\n\t" \
   "buffer_store_dwordx4 v[96:99], v30, %[rs], s48 offen offset:0 sc1\n\t" "s_waitcnt lgkmcnt(2)\n\t" \
   "v_mfma_f32_32x32x16_bf16 v[64:79], a[48:51], v[184:187], v[64:79]\n\t" "ds_read_b128 v[168:171], v26 offset:5120\n\t" \
   "v_cvt_pk_bf16_f32 v100, v100, v101\n\t" "v_cvt_pk_bf16_f32 v101, v102, v103\n\t" "v_cvt_pk_bf16_f32 v102, v108, v109\n\t" \
   "v_cvt_pk_bf16_f32 v103, v110, v111\n\t" "v_mfma_f32_32x32x16_bf16 v[80:95], a[56:59], v[184:187], v[80:95]\n\t" \
   "buffer_store_dwordx4 v[100:103], v30, %[rs], s48 offen offset:512 sc1\n\t" "v_fmac_f32 v112, v48, v8\n\t" \
   "v_mfma_f32_32x32x16_bf16 v[64:79], a[52:55], v[188:191], v[64:79]\n\t" "ds_read_b128 v[172:175], v26 offset:5632\n\t" \
   "v_fmac_f32 v113, v49, v8\n\t" "v_fmac_f32 v114, v50, v8\n\t" "v_fmac_f32 v115, v51, v8\n\t" "v_fmac_f32 v116, v52, v8\n\t" \
   "v_mfma_f32_32x32x16_bf16 v[80:95], a[60:63], v[188:191], v[80:95]\n\t" "v_fmac_f32 v117, v53, v8\n\t" "v_fmac_f32 v118, v54, v8\n\t" \
   "v_fmac_f32 v119, v55, v8\n\t" "v_fmac_f32 v120, v56, v8\n\t" "v_fmac_f32 v121, v57, v8\n\t" "s_waitcnt lgkmcnt(2)\n\t" \
   "v_mfma_f32_32x32x16_bf16 v[64:79], a[64:67], v[160:163], v[64:79]\n\t" "ds_read_b128 v[176:179], v26 offset:6144\n\t" \
   "v_fmac_f32 v122, v58, v8\n\t" "v_fmac_f32 v123, v59, v8\n\t" "v_fmac_f32 v124, v60, v8\n\t" "v_fmac_f32 v125, v61, v8\n\t" \
   "v_mfma_f32_32x32x16_bf16 v[80:95], a[72:75], v[160:163], v[80:95]\n\t" "v_fmac_f32 v126, v62, v8\n\t" "v_fmac_f32 v127, v63, v8\n\t" \
   "v_mul_f32 v112, v112, v10\n\t" "v_mul_f32 v113, v113, v10\n\t" "v_mul_f32 v114, v114, v10\n\t" "v_mul_f32 v115, v115, v10\n\t" \
   "v_mfma_f32_32x32x16_bf16 v[64:79], a[68:71], v[164:167], v[64:79]\n\t" "ds_read_b128 v[180:183], v26 offset:6656\n\t" \
   "v_mul_f32 v116, v116, v10\n\t" "v_mul_f32 v117, v117, v10\n\t" "v_mul_f32 v118, v118, v10\n\t" "v_mul_f32 v119, v119, v10\n\t" \
   "v_mfma_f32_32x32x16_bf16 v[80:95], a[76:79], v[164:167], v[80:95]\n\t" "v_mul_f32 v120, v120, v10\n\t" "v_mul_f32 v121, v121, v10\n\t" \
   "v_mul_f32 v122, v122, v10\n\t" "v_mul_f32 v123, v123, v10\n\t" "v_mul_f32 v124, v124, v10\n\t" "s_waitcnt lgkmcnt(2)\n\t" \
   "v_mfma_f32_32x32x16_bf16 v[64:79], a[80:83], v[168:171], v[64:79]\n\t" "ds_read_b128 v[184:187], v26 offset:7168\n\t" \
   "v_mul_f32 v125, v125, v10\n\t" "v_mul_f32 v126, v126, v10\n\t" "v_mul_f32 v127, v127, v10\n\t" "s_add_u32 s48, s47, %[kf]\n\t" \
   "v_mfma_f32_32x32x16_bf16 v[80:95], a[88:91], v[168:171], v[80:95]\n\t" "v_cvt_pk_bf16_f32 v112, v112, v113\n\t" \
   "v_cvt_pk_bf16_f32 v113, v114, v115\n\t" "v_cvt_pk_bf16_f32 v114, v120, v121\n\t" "v_cvt_pk_bf16_f32 v115, v122, v123\n\t" \
   "ds_read_b128 v[128:131], v27 offset:0\n\t" "v_mfma_f32_32x32x16_bf16 v[64:79], a[84:87], v[172:175], v[64:79]\n\t" \
   "ds_read_b128 v[188:191], v26 offset:7680\n\t" "ds_read_b128 v[132:135], v27 offset:16\n\t" "ds_read_b64 v[136:137], v27 offset:32\n\t" \
   "v_mfma_f32_32x32x16_bf16 v[80:95], a[92:95], v[172:175], v[80:95]\n\t" "buffer_store_dwordx4 v[112:115], v30, %[rs], s48 offen offset:0 sc1\n\t" \
   "s_waitcnt lgkmcnt(5)\n\t" "v_mfma_f32_32x32x16_bf16 v[64:79], a[96:99], v[176:179], v[64:79]\n\t" "ds_read_b128 v[160:163], v26 offset:8192\n\t" \
   "v_cvt_pk_bf16_f32 v116, v116, v117\n\t" "v_cvt_pk_bf16_f32 v117, v118, v119\n\t" "v_cvt_pk_bf16_f32 v118, v124, v125\n\t" \
   "v_cvt_pk_bf16_f32 v119, v126, v127\n\t" "v_mfma_f32_32x32x16_bf16 v[80:95], a[104:107], v[176:179], v[80:95]\n\t" \
   "buffer_store_dwordx4 v[116:119], v30, %[rs], s48 offen offset:512 sc1\n\t" \
   "v_mfma_f32_32x32x16_bf16 v[64:79], a[100:103], v[180:183], v[64:79]\n\t" "ds_read_b128 v[164:167], v26 offset:8704\n\t" \
   "v_add_u32 v28, s43, v22\n\t" "v_add_u32 v29, s43, v23\n\t" "v_mfma_f32_32x32x16_bf16 v[80:95], a[108:111], v[180:183], v[80:95]\n\t" \
   "s_waitcnt lgkmcnt(4)\n\t" "v_mfma_f32_32x32x16_bf16 v[64:79], a[112:115], v[184:187], v[64:79]\n\t" "ds_read_b128 v[168:171], v26 offset:9216\n\t" \
   "v_mfma_f32_32x32x16_bf16 v[80:95], a[120:123], v[184:187], v[80:95]\n\t" "ds_read_b128 v[140:143], v12\n\t" "s_waitcnt lgkmcnt(4)\n\t" \
   "v_add_f32 v128, v128, v130\n\t" "v_add_f32 v129, v129, v131\n\t" "v_add_f32 v132, v132, v134\n\t" "v_add_f32 v133, v133, v135\n\t" \
   "v_mfma_f32_32x32x16_bf16 v[64:79], a[116:119], v[188:191], v[64:79]\n\t" "ds_read_b128 v[172:175], v26 offset:9728\n\t" \
   "v_add_f32 v128, v128, v136\n\t" "v_add_f32 v129, v129, v137\n\t" "v_add_f32 v128, v128, v132\n\t" "v_add_f32 v129, v129, v133\n\t" \
   "v_mfma_f32_32x32x16_bf16 v[80:95], a[124:127], v[188:191], v[80:95]\n\t" "v_mul_f32 v128, 0xbacccccd, v128\n\t" \
   "v_mul_f32 v129, 0x3acccccd, v129\n\t" "v_fma_f32 v129, -v128, v128, v129\n\t" "v_max_f32 v129, 0, v129\n\t" \
   "v_cvt_pk_bf16_f32 v129, v129, v128\n\t" "s_waitcnt lgkmcnt(3)\n\t" "v_mfma_f32_32x32x16_bf16 v[64:79], a[128:131], v[160:163], v[64:79]\n\t" \
   "ds_read_b128 v[176:179], v26 offset:10240\n\t" "s_waitcnt lgkmcnt(2)\n\t" "v_min3_u32 v140, v140, v141, v142\n\t" "v_min_u32 v140, v140, v143\n\t" \
   "v_cmp_ge_u32 vcc, v140, v138\n\t" "v_mfma_f32_32x32x16_bf16 v[80:95], a[136:139], v[160:163], v[80:95]\n\t" "s_cbranch_vccz 150f\n\t" "50:\n\t" \
   "s_mul_i32 s52, s45, 1536\n\t" "s_mul_i32 s51, s45, 40960\n\t" "s_add_u32 m0, s46, %[ldx]\n\t" "s_mov_b32 s53, s46\n\t" \
   "v_mfma_f32_32x32x16_bf16 v[64:79], a[132:135], v[164:167], v[64:79]\n\t" "ds_read_b128 v[180:183], v26 offset:10752\n\t" \
   "buffer_load_dwordx4 v16, %[rs], s51 offen offset:0 sc1 lds\n\t" "v_and_b32 v8, 0xffff0000, v129\n\t" \
   "v_mfma_f32_32x32x16_bf16 v[80:95], a[140:143], v[164:167], v[80:95]\n\t" "v_lshlrev_b32 v129, 16, v129\n\t" "v_add_f32 v129, 0x3727c5ac, v129\n\t" \
   "v_cvt_pk_bf16_f32 v129, v129, v129\n\t" "v_lshlrev_b32 v129, 16, v129\n\t" "s_waitcnt lgkmcnt(2)\n\t" \
   "v_mfma_f32_32x32x16_bf16 v[64:79], a[144:147], v[168:171], v[64:79]\n\t" "ds_read_b128 v[184:187], v26 offset:11264\n\t" \
   "v_sqrt_f32 v129, v129\n\t" "s_nop 0\n\t" "v_cvt_pk_bf16_f32 v129, v129, v129\n\t" \
   "v_mfma_f32_32x32x16_bf16 v[80:95], a[152:155], v[168:171], v[80:95]\n\t" "buffer_load_dwordx4 v16, %[rs], s51 offen offset:1024 sc1 lds\n\t" \
   "v_lshlrev_b32 v129, 16, v129\n\t" "v_rcp_f32 v10, v129\n\t" "v_mfma_f32_32x32x16_bf16 v[64:79], a[148:151], v[172:175], v[64:79]\n\t" \
   "ds_read_b128 v[188:191], v26 offset:11776\n\t" "v_mfma_f32_32x32x16_bf16 v[80:95], a[156:159], v[172:175], v[80:95]\n\t" "s_waitcnt lgkmcnt(2)\n\t" \
   "v_mfma_f32_32x32x16_bf16 v[64:79], a[160:163], v[176:179], v[64:79]\n\t" "ds_read_b128 v[160:163], v26 offset:12288\n\t" \
   "buffer_load_dwordx4 v16, %[rs], s51 offen offset:2048 sc1 lds\n\t" "v_mfma_f32_32x32x16_bf16 v[80:95], a[168:171], v[176:179], v[80:95]\n\t" \
   "v_mfma_f32_32x32x16_bf16 v[64:79], a[164:167], v[180:183], v[64:79]\n\t" "ds_read_b128 v[164:167], v26 offset:12800\n\t" \
   "v_mfma_f32_32x32x16_bf16 v[80:95], a[172:175], v[180:183], v[80:95]\n\t" "buffer_load_dwordx4 v16, %[rs], s51 offen offset:3072 sc1 lds\n\t" \
   "s_add_u32 m0, m0, 4096\n\t" "s_add_u32 s51, s51, 4096\n\t" "s_waitcnt lgkmcnt(2)\n\t" \
   "v_mfma_f32_32x32x16_bf16 v[64:79], a[176:179], v[184:187], v[64:79]\n\t" "ds_read_b128 v[168:171], v26 offset:13312\n\t" "s_waitcnt vmcnt(8)\n\t" \
   "v_add_u32 v138, 1, v138\n\t" "ds_write_b32 v13, v138\n\t" "v_mfma_f32_32x32x16_bf16 v[80:95], a[184:187], v[184:187], v[80:95]\n\t" \
   "v_mfma_f32_32x32x16_bf16 v[64:79], a[180:183], v[188:191], v[64:79]\n\t" "ds_read_b128 v[172:175], v26 offset:13824\n\t" \
   "buffer_load_dwordx4 v16, %[rs], s51 offen offset:0 sc1 lds\n\t" "v_mfma_f32_32x32x16_bf16 v[80:95], a[188:191], v[188:191], v[80:95]\n\t" \
   "s_waitcnt lgkmcnt(3)\n\t" "v_mfma_f32_32x32x16_bf16 v[64:79], a[192:195], v[160:163], v[64:79]\n\t" "ds_read_b128 v[176:179], v26 offset:14336\n\t" \
   "v_mfma_f32_32x32x16_bf16 v[80:95], a[200:203], v[160:163], v[80:95]\n\t" "buffer_load_dwordx4 v16, %[rs], s51 offen offset:1024 sc1 lds\n\t" \
   "v_mfma_f32_32x32x16_bf16 v[64:79], a[196:199], v[164:167], v[64:79]\n\t" "ds_read_b128 v[180:183], v26 offset:14848\n\t" \
   "v_mfma_f32_32x32x16_bf16 v[80:95], a[204:207], v[164:167], v[80:95]\n\t" "s_waitcnt lgkmcnt(2)\n\t" \
   "v_mfma_f32_32x32x16_bf16 v[64:79], a[208:211], v[168:171], v[64:79]\n\t" "ds_read_b128 v[184:187], v26 offset:15360\n\t" \
   "buffer_load_dwordx4 v16, %[rs], s51 offen offset:2048 sc1 lds\n\t" "v_mfma_f32_32x32x16_bf16 v[80:95], a[216:219], v[168:171], v[80:95]\n\t" \
   "v_mfma_f32_32x32x16_bf16 v[64:79], a[212:215], v[172:175], v[64:79]\n\t" "ds_read_b128 v[188:191], v26 offset:15872\n\t" \
   "v_mfma_f32_32x32x16_bf16 v[80:95], a[220:223], v[172:175], v[80:95]\n\t" "buffer_load_dwordx4 v16, %[rs], s51 offen offset:3072 sc1 lds\n\t" \
   "s_add_u32 m0, m0, 4096\n\t" "s_add_u32 s51, s51, 4096\n\t" "s_waitcnt lgkmcnt(2)\n\t" \
   "v_mfma_f32_32x32x16_bf16 v[64:79], a[224:227], v[176:179], v[64:79]\n\t" "ds_read_b128 v[160:163], v26 offset:16384\n\t" \
   "v_mfma_f32_32x32x16_bf16 v[80:95], a[232:235], v[176:179], v[80:95]\n\t" "v_mfma_f32_32x32x16_bf16 v[64:79], a[228:231], v[180:183], v[64:79]\n\t" \
   "ds_read_b128 v[164:167], v26 offset:16896\n\t" "buffer_load_dwordx4 v16, %[rs], s51 offen offset:0 sc1 lds\n\t" \
   "v_mfma_f32_32x32x16_bf16 v[80:95], a[236:239], v[180:183], v[80:95]\n\t" "s_waitcnt lgkmcnt(2)\n\t" \
   "v_mfma_f32_32x32x16_bf16 v[64:79], a[240:243], v[184:187], v[64:79]\n\t" "ds_read_b128 v[168:171], v26 offset:17408\n\t" "s_cmp_lt_i32 s40, 1\n\t" \
   "s_cselect_b32 s49, 0x7ffffff0, 0\n\t" "v_or_b32 v9, s49, v20\n\t" "s_sub_i32 s49, s41, 1\n\t" \
   "v_mfma_f32_32x32x16_bf16 v[80:95], a[248:251], v[184:187], v[80:95]\n\t" "s_max_i32 s49, s49, 0\n\t" "s_lshl_b32 s49, s49, 12\n\t" \
   "s_waitcnt vmcnt(9)\n\t" "buffer_store_dword v31, v9, %[rs], s49 offen sc1\n\t" \
   "v_mfma_f32_32x32x16_bf16 v[64:79], a[244:247], v[188:191], v[64:79]\n\t" "ds_read_b128 v[172:175], v26 offset:17920\n\t" \
   "v_mfma_f32_32x32x16_bf16 v[80:95], a[252:255], v[188:191], v[80:95]\n\t" "buffer_load_dwordx4 v16, %[rs], s51 offen offset:1024 sc1 lds\n\t" \
   "s_add_u32 m0, s53, %[ld1]\n\t" "s_nop 0\n\t" "s_waitcnt lgkmcnt(2)\n\t" "v_mfma_f32_32x32x16_bf16 v[64:79], v[192:195], v[160:163], v[64:79]\n\t" \
   "ds_read_b128 v[176:179], v26 offset:18432\n\t" "buffer_load_dwordx4 v17, %[rs], s52 offen sc1 lds\n\t" "ds_read_b128 v[140:143], v12\n\t" \
   "v_mfma_f32_32x32x16_bf16 v[80:95], v[200:203], v[160:163], v[80:95]\n\t" "v_mfma_f32_32x32x16_bf16 v[64:79], v[196:199], v[164:167], v[64:79]\n\t" \
   "ds_read_b128 v[180:183], v26 offset:18944\n\t" "v_mfma_f32_32x32x16_bf16 v[80:95], v[204:207], v[164:167], v[80:95]\n\t" "s_waitcnt lgkmcnt(3)\n\t" \
   "v_mfma_f32_32x32x16_bf16 v[64:79], v[208:211], v[168:171], v[64:79]\n\t" "ds_read_b128 v[184:187], v26 offset:19456\n\t" "s_waitcnt lgkmcnt(2)\n\t" \
   "v_min3_u32 v140, v140, v141, v142\n\t" "v_min_u32 v140, v140, v143\n\t" "v_cmp_ge_u32 vcc, v140, v138\n\t" \
   "v_mfma_f32_32x32x16_bf16 v[80:95], v[216:219], v[168:171], v[80:95]\n\t" "s_cbranch_vccz 152f\n\t" "52:\n\t" \
   "v_mfma_f32_32x32x16_bf16 v[64:79], v[212:215], v[172:175], v[64:79]\n\t" "ds_read_b128 v[188:191], v26 offset:19968\n\t" \
   "v_mfma_f32_32x32x16_bf16 v[80:95], v[220:223], v[172:175], v[80:95]\n\t" "v_add_u32 v138, 1, v138\n\t" "ds_write_b32 v13, v138\n\t" \
   "s_waitcnt lgkmcnt(3)\n\t" "v_mfma_f32_32x32x16_bf16 v[64:79], v[224:227], v[176:179], v[64:79]\n\t" "ds_read_b128 v[160:163], v28 offset:0\n\t" \
   "s_add_u32 s45, s40, 3\n\t" "s_min_u32 s45, s45, 63\n\t" "s_mov_b32 s46, s42\n\t" \
   "v_mfma_f32_32x32x16_bf16 v[80:95], v[232:235], v[176:179], v[80:95]\n\t" "s_cmp_eq_u32 s44, -1\n\t" "s_cbranch_scc0 174f\n\t" "74:\n\t" \
   "s_mov_b32 s42, s43\n\t" "s_add_u32 s43, s43, 44032\n\t" "v_mfma_f32_32x32x16_bf16 v[64:79], v[228:231], v[180:183], v[64:79]\n\t" \
   "ds_read_b128 v[164:167], v28 offset:512\n\t" "s_cmp_eq_u32 s43, 132096\n\t" "s_cselect_b32 s43, 0, s43\n\t" "s_add_u32 s41, s41, 1\n\t" \
   "s_add_u32 s40, s40, 1\n\t" "v_mfma_f32_32x32x16_bf16 v[80:95], v[236:239], v[180:183], v[80:95]\n\t" "s_waitcnt lgkmcnt(3)\n\t" \
   "v_mfma_f32_32x32x16_bf16 v[64:79], v[240:243], v[184:187], v[64:79]\n\t" "ds_read_b128 v[168:171], v28 offset:1024\n\t" \
   "v_mfma_f32_32x32x16_bf16 v[80:95], v[248:251], v[184:187], v[80:95]\n\t" "v_mfma_f32_32x32x16_bf16 v[64:79], v[244:247], v[188:191], v[64:79]\n\t" \
   "ds_read_b128 v[172:175], v28 offset:1536\n\t" "v_mfma_f32_32x32x16_bf16 v[80:95], v[252:255], v[188:191], v[80:95]\n\t" "v_mov_b32 v26, v28\n\t" \
   "v_mov_b32 v27, v29\n\t" "s_waitcnt lgkmcnt(2)\n\t" "v_mfma_f32_32x32x16_bf16 v[96:111], a[0:3], v[160:163], 0\n\t" \
   "ds_read_b128 v[176:179], v26 offset:2048\n\t" "v_mfma_f32_32x32x16_bf16 v[112:127], a[8:11], v[160:163], 0\n\t" \
   "v_mul_f32 v11, 0x3e0293ee, v10\n\t" "s_lshl_b32 s47, s41, 13\n\t" "s_sub_i32 s47, s47, 8192\n\t" "v_fmac_f32 v64, v32, v8\n\t" \
   "v_fmac_f32 v65, v33, v8\n\t" "v_fmac_f32 v66, v34, v8\n\t" "v_mfma_f32_32x32x16_bf16 v[96:111], a[4:7], v[164:167], v[96:111]\n\t" \
   "ds_read_b128 v[180:183], v26 offset:2560\n\t" "v_fmac_f32 v67, v35, v8\n\t" "v_fmac_f32 v68, v36, v8\n\t" "v_fmac_f32 v69, v37, v8\n\t" \
   "v_fmac_f32 v70, v38, v8\n\t" "v_mfma_f32_32x32x16_bf16 v[112:127], a[12:15], v[164:167], v[112:127]\n\t" "v_fmac_f32 v71, v39, v8\n\t" \
   "v_fmac_f32 v72, v40, v8\n\t" "v_fmac_f32 v73, v41, v8\n\t" "v_fmac_f32 v74, v42, v8\n\t" "v_fmac_f32 v75, v43, v8\n\t" "s_waitcnt lgkmcnt(2)\n\t" \
   "v_mfma_f32_32x32x16_bf16 v[96:111], a[16:19], v[168:171], v[96:111]\n\t" "ds_read_b128 v[184:187], v26 offset:3072\n\t" \
   "v_fmac_f32 v76, v44, v8\n\t" "v_fmac_f32 v77, v45, v8\n\t" "v_fmac_f32 v78, v46, v8\n\t" "v_fmac_f32 v79, v47, v8\n\t" \
   "v_mfma_f32_32x32x16_bf16 v[112:127], a[24:27], v[168:171], v[112:127]\n\t" "v_mul_f32 v64, v64, v11\n\t" "v_mul_f32 v65, v65, v11\n\t" \
   "v_mul_f32 v66, v66, v11\n\t" "v_mul_f32 v67, v67, v11\n\t" "v_mul_f32 v68, v68, v11\n\t" "v_mul_f32 v69, v69, v11\n\t" \
   "v_mfma_f32_32x32x16_bf16 v[96:111], a[20:23], v[172:175], v[96:111]\n\t" "ds_read_b128 v[188:191], v26 offset:3584\n\t" \
   "v_mul_f32 v70, v70, v11\n\t" "v_mul_f32 v71, v71, v11\n\t" "v_mul_f32 v72, v72, v11\n\t" "v_mul_f32 v73, v73, v11\n\t" \
   "v_mfma_f32_32x32x16_bf16 v[112:127], a[28:31], v[172:175], v[112:127]\n\t" "v_mul_f32 v74, v74, v11\n\t" "v_mul_f32 v75, v75, v11\n\t" \
   "v_mul_f32 v76, v76, v11\n\t" "v_mul_f32 v77, v77, v11\n\t" "v_mul_f32 v78, v78, v11\n\t" "s_waitcnt lgkmcnt(2)\n\t" \
   "v_mfma_f32_32x32x16_bf16 v[96:111], a[32:35], v[176:179], v[96:111]\n\t" "ds_read_b128 v[160:163], v26 offset:4096\n\t" \
   "v_mul_f32 v79, v79, v11\n\t" "s_add_u32 s48, s47, %[qf]\n\t" "v_cvt_pk_bf16_f32 v64, v64, v65\n\t" "v_cvt_pk_bf16_f32 v65, v66, v67\n\t" \
   "v_mfma_f32_32x32x16_bf16 v[112:127], a[40:43], v[176:179], v[112:127]\n\t" "v_cvt_pk_bf16_f32 v66, v72, v73\n\t" \
   "v_cvt_pk_bf16_f32 v67, v74, v75\n\t" "v_mfma_f32_32x32x16_bf16 v[96:111], a[36:39], v[180:183], v[96:111]\n\t" \
   "ds_read_b128 v[164:167], v26 offset:4608\n\t" "v_mfma_f32_32x32x16_bf16 v[112:127], a[44:47], v[180:183], v[112:127]\n\t" \
   "buffer_store_dwordx4 v[64:67], v21, %[rs], s48 offen offset:0 sc1\n\t" "s_waitcnt lgkmcnt(2)\n\t" \
   "v_mfma_f32_32x32x16_bf16 v[96:111], a[48:51], v[184:187], v[96:111]\n\t" "ds_read_b128 v[168:171], v26 offset:5120\n\t" \
   "v_cvt_pk_bf16_f32 v68, v68, v69\n\t" "v_cvt_pk_bf16_f32 v69, v70, v71\n\t" "v_cvt_pk_bf16_f32 v70, v76, v77\n\t" \
   "v_cvt_pk_bf16_f32 v71, v78, v79\n\t" "v_mfma_f32_32x32x16_bf16 v[112:127], a[56:59], v[184:187], v[112:127]\n\t" \
   "buffer_store_dwordx4 v[68:71], v21, %[rs], s48 offen offset:512 sc1\n\t" "v_fmac_f32 v80, v48, v8\n\t" \
   "v_mfma_f32_32x32x16_bf16 v[96:111], a[52:55], v[188:191], v[96:111]\n\t" "ds_read_b128 v[172:175], v26 offset:5632\n\t" \
   "v_fmac_f32 v81, v49, v8\n\t" "v_fmac_f32 v82, v50, v8\n\t" "v_fmac_f32 v83, v51, v8\n\t" "v_fmac_f32 v84, v52, v8\n\t" \
   "v_mfma_f32_32x32x16_bf16 v[112:127], a[60:63], v[188:191], v[112:127]\n\t" "v_fmac_f32 v85, v53, v8\n\t" "v_fmac_f32 v86, v54, v8\n\t" \
   "v_fmac_f32 v87, v55, v8\n\t" "v_fmac_f32 v88, v56, v8\n\t" "v_fmac_f32 v89, v57, v8\n\t" "s_waitcnt lgkmcnt(2)\n\t" \
   "v_mfma_f32_32x32x16_bf16 v[96:111], a[64:67], v[160:163], v[96:111]\n\t" "ds_read_b128 v[176:179], v26 offset:6144\n\t" \
   "v_fmac_f32 v90, v58, v8\n\t" "v_fmac_f32 v91, v59, v8\n\t" "v_fmac_f32 v92, v60, v8\n\t" "v_fmac_f32 v93, v61, v8\n\t" \
   "v_mfma_f32_32x32x16_bf16 v[112:127], a[72:75], v[160:163], v[112:127]\n\t" "v_fmac_f32 v94, v62, v8\n\t" "v_fmac_f32 v95, v63, v8\n\t" \
   "v_mul_f32 v80, v80, v10\n\t" "v_mul_f32 v81, v81, v10\n\t" "v_mul_f32 v82, v82, v10\n\t" "v_mul_f32 v83, v83, v10\n\t" \
   "v_mfma_f32_32x32x16_bf16 v[96:111], a[68:71], v[164:167], v[96:111]\n\t" "ds_read_b128 v[180:183], v26 offset:6656\n\t" \
   "v_mul_f32 v84, v84, v10\n\t" "v_mul_f32 v85, v85, v10\n\t" "v_mul_f32 v86, v86, v10\n\t" "v_mul_f32 v87, v87, v10\n\t" \
   "v_mfma_f32_32x32x16_bf16 v[112:127], a[76:79], v[164:167], v[112:127]\n\t" "v_mul_f32 v88, v88, v10\n\t" "v_mul_f32 v89, v89, v10\n\t" \
   "v_mul_f32 v90, v90, v10\n\t" "v_mul_f32 v91, v91, v10\n\t" "v_mul_f32 v92, v92, v10\n\t" "s_waitcnt lgkmcnt(2)\n\t" \
   "v_mfma_f32_32x32x16_bf16 v[96:111], a[80:83], v[168:171], v[96:111]\n\t" "ds_read_b128 v[184:187], v26 offset:7168\n\t" \
   "v_mul_f32 v93, v93, v10\n\t" "v_mul_f32 v94, v94, v10\n\t" "v_mul_f32 v95, v95, v10\n\t" "s_add_u32 s48, s47, %[kf]\n\t" \
   "v_mfma_f32_32x32x16_bf16 v[112:127], a[88:91], v[168:171], v[112:127]\n\t" "v_cvt_pk_bf16_f32 v80, v80, v81\n\t" \
   "v_cvt_pk_bf16_f32 v81, v82, v83\n\t" "v_cvt_pk_bf16_f32 v82, v88, v89\n\t" "v_cvt_pk_bf16_f32 v83, v90, v91\n\t" \
   "ds_read_b128 v[128:131], v27 offset:0\n\t" "v_mfma_f32_32x32x16_bf16 v[96:111], a[84:87], v[172:175], v[96:111]\n\t" \
   "ds_read_b128 v[188:191], v26 offset:7680\n\t" "ds_read_b128 v[132:135], v27 offset:16\n\t" "ds_read_b64 v[136:137], v27 offset:32\n\t" \
   "v_mfma_f32_32x32x16_bf16 v[112:127], a[92:95], v[172:175], v[112:127]\n\t" "buffer_store_dwordx4 v[80:83], v21, %[rs], s48 offen offset:0 sc1\n\t" \
   "s_waitcnt lgkmcnt(5)\n\t" "v_mfma_f32_32x32x16_bf16 v[96:111], a[96:99], v[176:179], v[96:111]\n\t" "ds_read_b128 v[160:163], v26 offset:8192\n\t" \
   "v_cvt_pk_bf16_f32 v84, v84, v85\n\t" "v_cvt_pk_bf16_f32 v85, v86, v87\n\t" "v_cvt_pk_bf16_f32 v86, v92, v93\n\t" \
   "v_cvt_pk_bf16_f32 v87, v94, v95\n\t" "v_mfma_f32_32x32x16_bf16 v[112:127], a[104:107], v[176:179], v[112:127]\n\t" \
   "buffer_store_dwordx4 v[84:87], v21, %[rs], s48 offen offset:512 sc1\n\t" \
   "v_mfma_f32_32x32x16_bf16 v[96:111], a[100:103], v[180:183], v[96:111]\n\t" "ds_read_b128 v[164:167], v26 offset:8704\n\t" \
   "v_add_u32 v28, s43, v22\n\t" "v_add_u32 v29, s43, v23\n\t" "v_mfma_f32_32x32x16_bf16 v[112:127], a[108:111], v[180:183], v[112:127]\n\t" \
   "s_waitcnt lgkmcnt(4)\n\t" "v_mfma_f32_32x32x16_bf16 v[96:111], a[112:115], v[184:187], v[96:111]\n\t" \
   "ds_read_b128 v[168:171], v26 offset:9216\n\t" "v_mfma_f32_32x32x16_bf16 v[112:127], a[120:123], v[184:187], v[112:127]\n\t" \
   "ds_read_b128 v[140:143], v12\n\t" "s_waitcnt lgkmcnt(4)\n\t" "v_add_f32 v128, v128, v130\n\t" "v_add_f32 v129, v129, v131\n\t" \
   "v_add_f32 v132, v132, v134\n\t" "v_add_f32 v133, v133, v135\n\t" "v_mfma_f32_32x32x16_bf16 v[96:111], a[116:119], v[188:191], v[96:111]\n\t" \
   "ds_read_b128 v[172:175], v26 offset:9728\n\t" "v_add_f32 v128, v128, v136\n\t" "v_add_f32 v129, v129, v137\n\t" "v_add_f32 v128, v128, v132\n\t" \
   "v_add_f32 v129, v129, v133\n\t" "v_mfma_f32_32x32x16_bf16 v[112:127], a[124:127], v[188:191], v[112:127]\n\t" \
   "v_mul_f32 v128, 0xbacccccd, v128\n\t" "v_mul_f32 v129, 0x3acccccd, v129\n\t" "v_fma_f32 v129, -v128, v128, v129\n\t" "v_max_f32 v129, 0, v129\n\t" \
   "v_cvt_pk_bf16_f32 v129, v129, v128\n\t" "s_waitcnt lgkmcnt(3)\n\t" "v_mfma_f32_32x32x16_bf16 v[96:111], a[128:131], v[160:163], v[96:111]\n\t" \
   "ds_read_b128 v[176:179], v26 offset:10240\n\t" "s_waitcnt lgkmcnt(2)\n\t" "v_min3_u32 v140, v140, v141, v142\n\t" "v_min_u32 v140, v140, v143\n\t" \
   "v_cmp_ge_u32 vcc, v140, v138\n\t" "v_mfma_f32_32x32x16_bf16 v[112:127], a[136:139], v[160:163], v[112:127]\n\t" "s_cbranch_vccz 151f\n\t" "51:\n\t" \
   "s_mul_i32 s52, s45, 1536\n\t" "s_mul_i32 s51, s45, 40960\n\t" "s_add_u32 m0, s46, %[ldx]\n\t" "s_mov_b32 s53, s46\n\t" \
   "v_mfma_f32_32x32x16_bf16 v[96:111], a[132:135], v[164:167], v[96:111]\n\t" "ds_read_b128 v[180:183], v26 offset:10752\n\t" \
   "buffer_load_dwordx4 v16, %[rs], s51 offen offset:0 sc1 lds\n\t" "v_and_b32 v8, 0xffff0000, v129\n\t" \
   "v_mfma_f32_32x32x16_bf16 v[112:127], a[140:143], v[164:167], v[112:127]\n\t" "v_lshlrev_b32 v129, 16, v129\n\t" \
   "v_add_f32 v129, 0x3727c5ac, v129\n\t" "v_cvt_pk_bf16_f32 v129, v129, v129\n\t" "v_lshlrev_b32 v129, 16, v129\n\t" "s_waitcnt lgkmcnt(2)\n\t" \
   "v_mfma_f32_32x32x16_bf16 v[96:111], a[144:147], v[168:171], v[96:111]\n\t" "ds_read_b128 v[184:187], v26 offset:11264\n\t" \
   "v_sqrt_f32 v129, v129\n\t" "s_nop 0\n\t" "v_cvt_pk_bf16_f32 v129, v129, v129\n\t" \
   "v_mfma_f32_32x32x16_bf16 v[112:127], a[152:155], v[168:171], v[112:127]\n\t" "buffer_load_dwordx4 v16, %[rs], s51 offen offset:1024 sc1 lds\n\t" \
   "v_lshlrev_b32 v129, 16, v129\n\t" "v_rcp_f32 v10, v129\n\t" "v_mfma_f32_32x32x16_bf16 v[96:111], a[148:151], v[172:175], v[96:111]\n\t" \
   "ds_read_b128 v[188:191], v26 offset:11776\n\t" "v_mfma_f32_32x32x16_bf16 v[112:127], a[156:159], v[172:175], v[112:127]\n\t" \
   "s_waitcnt lgkmcnt(2)\n\t" "v_mfma_f32_32x32x16_bf16 v[96:111], a[160:163], v[176:179], v[96:111]\n\t" \
   "ds_read_b128 v[160:163], v26 offset:12288\n\t" "buffer_load_dwordx4 v16, %[rs], s51 offen offset:2048 sc1 lds\n\t" \
   "v_mfma_f32_32x32x16_bf16 v[112:127], a[168:171], v[176:179], v[112:127]\n\t" \
   "v_mfma_f32_32x32x16_bf16 v[96:111], a[164:167], v[180:183], v[96:111]\n\t" "ds_read_b128 v[164:167], v26 offset:12800\n\t" \
   "v_mfma_f32_32x32x16_bf16 v[112:127], a[172:175], v[180:183], v[112:127]\n\t" "buffer_load_dwordx4 v16, %[rs], s51 offen offset:3072 sc1 lds\n\t" \
   "s_add_u32 m0, m0, 4096\n\t" "s_add_u32 s51, s51, 4096\n\t" "s_waitcnt lgkmcnt(2)\n\t" \
   "v_mfma_f32_32x32x16_bf16 v[96:111], a[176:179], v[184:187], v[96:111]\n\t" "ds_read_b128 v[168:171], v26 offset:13312\n\t" "s_waitcnt vmcnt(8)\n\t" \
   "v_add_u32 v138, 1, v138\n\t" "ds_write_b32 v13, v138\n\t" "v_mfma_f32_32x32x16_bf16 v[112:127], a[184:187], v[184:187], v[112:127]\n\t" \
   "v_mfma_f32_32x32x16_bf16 v[96:111], a[180:183], v[188:191], v[96:111]\n\t" "ds_read_b128 v[172:175], v26 offset:13824\n\t" \
   "buffer_load_dwordx4 v16, %[rs], s51 offen offset:0 sc1 lds\n\t" "v_mfma_f32_32x32x16_bf16 v[112:127], a[188:191], v[188:191], v[112:127]\n\t" \
   "s_waitcnt lgkmcnt(3)\n\t" "v_mfma_f32_32x32x16_bf16 v[96:111], a[192:195], v[160:163], v[96:111]\n\t" \
   "ds_read_b128 v[176:179], v26 offset:14336\n\t" "v_mfma_f32_32x32x16_bf16 v[112:127], a[200:203], v[160:163], v[112:127]\n\t" \
   "buffer_load_dwordx4 v16, %[rs], s51 offen offset:1024 sc1 lds\n\t" "v_mfma_f32_32x32x16_bf16 v[96:111], a[196:199], v[164:167], v[96:111]\n\t" \
   "ds_read_b128 v[180:183], v26 offset:14848\n\t" "v_mfma_f32_32x32x16_bf16 v[112:127], a[204:207], v[164:167], v[112:127]\n\t" \
   "s_waitcnt lgkmcnt(2)\n\t" "v_mfma_f32_32x32x16_bf16 v[96:111], a[208:211], v[168:171], v[96:111]\n\t" \
   "ds_read_b128 v[184:187], v26 offset:15360\n\t" "buffer_load_dwordx4 v16, %[rs], s51 offen offset:2048 sc1 lds\n\t" \
   "v_mfma_f32_32x32x16_bf16 v[112:127], a[216:219], v[168:171], v[112:127]\n\t" \
   "v_mfma_f32_32x32x16_bf16 v[96:111], a[212:215], v[172:175], v[96:111]\n\t" "ds_read_b128 v[188:191], v26 offset:15872\n\t" \
   "v_mfma_f32_32x32x16_bf16 v[112:127], a[220:223], v[172:175], v[112:127]\n\t" "buffer_load_dwordx4 v16, %[rs], s51 offen offset:3072 sc1 lds\n\t" \
   "s_add_u32 m0, m0, 4096\n\t" "s_add_u32 s51, s51, 4096\n\t" "s_waitcnt lgkmcnt(2)\n\t" \
   "v_mfma_f32_32x32x16_bf16 v[96:111], a[224:227], v[176:179], v[96:111]\n\t" "ds_read_b128 v[160:163], v26 offset:16384\n\t" \
   "v_mfma_f32_32x32x16_bf16 v[112:127], a[232:235], v[176:179], v[112:127]\n\t" \
   "v_mfma_f32_32x32x16_bf16 v[96:111], a[228:231], v[180:183], v[96:111]\n\t" "ds_read_b128 v[164:167], v26 offset:16896\n\t" \
   "buffer_load_dwordx4 v16, %[rs], s51 offen offset:0 sc1 lds\n\t" "v_mfma_f32_32x32x16_bf16 v[112:127], a[236:239], v[180:183], v[112:127]\n\t" \
   "s_waitcnt lgkmcnt(2)\n\t" "v_mfma_f32_32x32x16_bf16 v[96:111], a[240:243], v[184:187], v[96:111]\n\t" \
   "ds_read_b128 v[168:171], v26 offset:17408\n\t" "s_cmp_lt_i32 s40, 1\n\t" "s_cselect_b32 s49, 0x7ffffff0, 0\n\t" "v_or_b32 v9, s49, v20\n\t" \
   "s_sub_i32 s49, s41, 1\n\t" "v_mfma_f32_32x32x16_bf16 v[112:127], a[248:251], v[184:187], v[112:127]\n\t" "s_max_i32 s49, s49, 0\n\t" \
   "s_lshl_b32 s49, s49, 12\n\t" "s_waitcnt vmcnt(9)\n\t" "buffer_store_dword v31, v9, %[rs], s49 offen sc1\n\t" \
   "v_mfma_f32_32x32x16_bf16 v[96:111], a[244:247], v[188:191], v[96:111]\n\t" "ds_read_b128 v[172:175], v26 offset:17920\n\t" \
   "v_mfma_f32_32x32x16_bf16 v[112:127], a[252:255], v[188:191], v[112:127]\n\t" "buffer_load_dwordx4 v16, %[rs], s51 offen offset:1024 sc1 lds\n\t" \
   "s_add_u32 m0, s53, %[ld1]\n\t" "s_nop 0\n\t" "s_waitcnt lgkmcnt(2)\n\t" "v_mfma_f32_32x32x16_bf16 v[96:111], v[192:195], v[160:163], v[96:111]\n\t" \
   "ds_read_b128 v[176:179], v26 offset:18432\n\t" "buffer_load_dwordx4 v17, %[rs], s52 offen sc1 lds\n\t" "ds_read_b128 v[140:143], v12\n\t" \
   "v_mfma_f32_32x32x16_bf16 v[112:127], v[200:203], v[160:163], v[112:127]\n\t" \
   "v_mfma_f32_32x32x16_bf16 v[96:111], v[196:199], v[164:167], v[96:111]\n\t" "ds_read_b128 v[180:183], v26 offset:18944\n\t" \
   "v_mfma_f32_32x32x16_bf16 v[112:127], v[204:207], v[164:167], v[112:127]\n\t" "s_waitcnt lgkmcnt(3)\n\t" \
   "v_mfma_f32_32x32x16_bf16 v[96:111], v[208:211], v[168:171], v[96:111]\n\t" "ds_read_b128 v[184:187], v26 offset:19456\n\t" \
   "s_waitcnt lgkmcnt(2)\n\t" "v_min3_u32 v140, v140, v141, v142\n\t" "v_min_u32 v140, v140, v143\n\t" "v_cmp_ge_u32 vcc, v140, v138\n\t" \
   "v_mfma_f32_32x32x16_bf16 v[112:127], v[216:219], v[168:171], v[112:127]\n\t" "s_cbranch_vccz 153f\n\t" "53:\n\t" \
   "v_mfma_f32_32x32x16_bf16 v[96:111], v[212:215], v[172:175], v[96:111]\n\t" "ds_read_b128 v[188:191], v26 offset:19968\n\t" \
   "v_mfma_f32_32x32x16_bf16 v[112:127], v[220:223], v[172:175], v[112:127]\n\t" "v_add_u32 v138, 1, v138\n\t" "ds_write_b32 v13, v138\n\t" \
   "s_waitcnt lgkmcnt(3)\n\t" "v_mfma_f32_32x32x16_bf16 v[96:111], v[224:227], v[176:179], v[96:111]\n\t" "ds_read_b128 v[160:163], v28 offset:0\n\t" \
   "s_add_u32 s45, s40, 3\n\t" "s_min_u32 s45, s45, 63\n\t" "s_mov_b32 s46, s42\n\t" \
   "v_mfma_f32_32x32x16_bf16 v[112:127], v[232:235], v[176:179], v[112:127]\n\t" "s_cmp_eq_u32 s44, -1\n\t" "s_cbranch_scc0 178f\n\t" "78:\n\t" \
   "s_mov_b32 s42, s43\n\t" "s_add_u32 s43, s43, 44032\n\t" "v_mfma_f32_32x32x16_bf16 v[96:111], v[228:231], v[180:183], v[96:111]\n\t" \
   "ds_read_b128 v[164:167], v28 offset:512\n\t" "s_cmp_eq_u32 s43, 132096\n\t" "s_cselect_b32 s43, 0, s43\n\t" "s_add_u32 s41, s41, 1\n\t" \
   "s_add_u32 s40, s40, 1\n\t" "v_mfma_f32_32x32x16_bf16 v[112:127], v[236:239], v[180:183], v[112:127]\n\t" "s_waitcnt lgkmcnt(3)\n\t" \
   "v_mfma_f32_32x32x16_bf16 v[96:111], v[240:243], v[184:187], v[96:111]\n\t" "ds_read_b128 v[168:171], v28 offset:1024\n\t" \
   "v_mfma_f32_32x32x16_bf16 v[112:127], v[248:251], v[184:187], v[112:127]\n\t" \
   "v_mfma_f32_32x32x16_bf16 v[96:111], v[244:247], v[188:191], v[96:111]\n\t" "ds_read_b128 v[172:175], v28 offset:1536\n\t" \
   "v_mfma_f32_32x32x16_bf16 v[112:127], v[252:255], v[188:191], v[112:127]\n\t" "s_cmp_lt_u32 s40, 64\n\t" "s_cbranch_scc1 1b\n\t" "s_nop 7\n\t" \
   "v_mul_f32 v11, 0x3e0293ee, v10\n\t" "s_lshl_b32 s47, s41, 13\n\t" "s_sub_i32 s47, s47, 8192\n\t" "v_fmac_f32 v96, v32, v8\n\t" \
   "v_fmac_f32 v97, v33, v8\n\t" "v_fmac_f32 v98, v34, v8\n\t" "v_fmac_f32 v99, v35, v8\n\t" "v_fmac_f32 v100, v36, v8\n\t" \
   "v_fmac_f32 v101, v37, v8\n\t" "v_fmac_f32 v102, v38, v8\n\t" "v_fmac_f32 v103, v39, v8\n\t" "v_fmac_f32 v104, v40, v8\n\t" \
   "v_fmac_f32 v105, v41, v8\n\t" "v_fmac_f32 v106, v42, v8\n\t" "v_fmac_f32 v107, v43, v8\n\t" "v_fmac_f32 v108, v44, v8\n\t" \
   "v_fmac_f32 v109, v45, v8\n\t" "v_fmac_f32 v110, v46, v8\n\t" "v_fmac_f32 v111, v47, v8\n\t" "v_mul_f32 v96, v96, v11\n\t" \
   "v_mul_f32 v97, v97, v11\n\t" "v_mul_f32 v98, v98, v11\n\t" "v_mul_f32 v99, v99, v11\n\t" "v_mul_f32 v100, v100, v11\n\t" \
   "v_mul_f32 v101, v101, v11\n\t" "v_mul_f32 v102, v102, v11\n\t" "v_mul_f32 v103, v103, v11\n\t" "v_mul_f32 v104, v104, v11\n\t" \
   "v_mul_f32 v105, v105, v11\n\t" "v_mul_f32 v106, v106, v11\n\t" "v_mul_f32 v107, v107, v11\n\t" "v_mul_f32 v108, v108, v11\n\t" \
   "v_mul_f32 v109, v109, v11\n\t" "v_mul_f32 v110, v110, v11\n\t" "v_mul_f32 v111, v111, v11\n\t" "s_add_u32 s48, s47, %[qf]\n\t" \
   "v_cvt_pk_bf16_f32 v96, v96, v97\n\t" "v_cvt_pk_bf16_f32 v97, v98, v99\n\t" "v_cvt_pk_bf16_f32 v98, v104, v105\n\t" \
   "v_cvt_pk_bf16_f32 v99, v106, v107\n\t" "buffer_store_dwordx4 v[96:99], v21, %[rs], s48 offen offset:0 sc1\n\t" \
   "v_cvt_pk_bf16_f32 v100, v100, v101\n\t" "v_cvt_pk_bf16_f32 v101, v102, v103\n\t" "v_cvt_pk_bf16_f32 v102, v108, v109\n\t" \
   "v_cvt_pk_bf16_f32 v103, v110, v111\n\t" "buffer_store_dwordx4 v[100:103], v21, %[rs], s48 offen offset:512 sc1\n\t" "v_fmac_f32 v112, v48, v8\n\t" \
   "v_fmac_f32 v113, v49, v8\n\t" "v_fmac_f32 v114, v50, v8\n\t" "v_fmac_f32 v115, v51, v8\n\t" "v_fmac_f32 v116, v52, v8\n\t" \
   "v_fmac_f32 v117, v53, v8\n\t" "v_fmac_f32 v118, v54, v8\n\t" "v_fmac_f32 v119, v55, v8\n\t" "v_fmac_f32 v120, v56, v8\n\t" \
   "v_fmac_f32 v121, v57, v8\n\t" "v_fmac_f32 v122, v58, v8\n\t" "v_fmac_f32 v123, v59, v8\n\t" "v_fmac_f32 v124, v60, v8\n\t" \
   "v_fmac_f32 v125, v61, v8\n\t" "v_fmac_f32 v126, v62, v8\n\t" "v_fmac_f32 v127, v63, v8\n\t" "v_mul_f32 v112, v112, v10\n\t" \
   "v_mul_f32 v113, v113, v10\n\t" "v_mul_f32 v114, v114, v10\n\t" "v_mul_f32 v115, v115, v10\n\t" "v_mul_f32 v116, v116, v10\n\t" \
   "v_mul_f32 v117, v117, v10\n\t" "v_mul_f32 v118, v118, v10\n\t" "v_mul_f32 v119, v119, v10\n\t" "v_mul_f32 v120, v120, v10\n\t" \
   "v_mul_f32 v121, v121, v10\n\t" "v_mul_f32 v122, v122, v10\n\t" "v_mul_f32 v123, v123, v10\n\t" "v_mul_f32 v124, v124, v10\n\t" \
   "v_mul_f32 v125, v125, v10\n\t" "v_mul_f32 v126, v126, v10\n\t" "v_mul_f32 v127, v127, v10\n\t" "s_add_u32 s48, s47, %[kf]\n\t" \
   "v_cvt_pk_bf16_f32 v112, v112, v113\n\t" "v_cvt_pk_bf16_f32 v113, v114, v115\n\t" "v_cvt_pk_bf16_f32 v114, v120, v121\n\t" \
   "v_cvt_pk_bf16_f32 v115, v122, v123\n\t" "buffer_store_dwordx4 v[112:115], v21, %[rs], s48 offen offset:0 sc1\n\t" \
   "v_cvt_pk_bf16_f32 v116, v116, v117\n\t" "v_cvt_pk_bf16_f32 v117, v118, v119\n\t" "v_cvt_pk_bf16_f32 v118, v124, v125\n\t" \
   "v_cvt_pk_bf16_f32 v119, v126, v127\n\t" "buffer_store_dwordx4 v[116:119], v21, %[rs], s48 offen offset:512 sc1\n\t" \
   "s_waitcnt vmcnt(0) lgkmcnt(0)\n\t" "s_cmp_lt_i32 s40, 1\n\t" "s_cselect_b32 s47, 0x7ffffff0, 0\n\t" "v_or_b32 v30, s47, v20\n\t" \
   "s_sub_i32 s47, s41, 1\n\t" "s_max_i32 s47, s47, 0\n\t" "s_lshl_b32 s47, s47, 12\n\t" "buffer_store_dword v31, v30, %[rs], s47 offen sc1\n\t" \
   "s_branch 9f\n\t" "172:\n\t" "v_cmp_lt_i32 vcc, 6, v146\n\t" "s_lshr_b32 s48, s45, 1\n\t" "s_or_b32 s44, s44, vcc_lo\n\t" "s_cmp_lt_u32 s40, 4\n\t" \
   "s_cbranch_scc1 272f\n\t" "buffer_load_dword v146, v19, %[rs], 0 offen sc1\n\t" "272:\n\t" "s_bitcmp1_b32 s44, s48\n\t" "s_cbranch_scc1 72b\n\t" \
   "s_mov_b32 s47, 0\n\t" "buffer_load_dword v30, v19, %[rs], 0 offen sc1\n\t" "s_waitcnt vmcnt(0)\n\t" "v_add_u32 v30, 1, v30\n\t" \
   "v_cmp_lt_u32 vcc, 7, v30\n\t" "s_or_b32 s44, s44, vcc_lo\n\t" "s_bitcmp1_b32 s44, s48\n\t" "s_cbranch_scc1 72b\n\t" "s_bitset1_b32 s44, s48\n\t" \
   "s_lshl_b32 s48, s48, 7\n\t" "s_add_u32 s48, s48, %[cnt]\n\t" "73:\n\t" "s_sleep 10\n\t" "buffer_load_dword v30, off, %[rs], s48 sc1\n\t" \
   "s_add_u32 s47, s47, 1\n\t" "s_waitcnt vmcnt(0)\n\t" "v_add_u32 v30, 1, v30\n\t" "v_cmp_lt_u32 vcc, 7, v30\n\t" "s_cmp_gt_u32 s47, 0x20000\n\t" \
   "s_cbranch_scc1 72b\n\t" "s_cbranch_vccz 73b\n\t" "s_branch 72b\n\t" "174:\n\t" "v_cmp_lt_i32 vcc, 6, v146\n\t" "s_lshr_b32 s48, s45, 1\n\t" \
   "s_or_b32 s44, s44, vcc_lo\n\t" "s_cmp_lt_u32 s40, 4\n\t" "s_cbranch_scc1 274f\n\t" "buffer_load_dword v146, v19, %[rs], 0 offen sc1\n\t" "274:\n\t" \
   "s_bitcmp1_b32 s44, s48\n\t" "s_cbranch_scc1 74b\n\t" "s_mov_b32 s47, 0\n\t" "buffer_load_dword v30, v19, %[rs], 0 offen sc1\n\t" \
   "s_waitcnt vmcnt(0)\n\t" "v_add_u32 v30, 1, v30\n\t" "v_cmp_lt_u32 vcc, 7, v30\n\t" "s_or_b32 s44, s44, vcc_lo\n\t" "s_bitcmp1_b32 s44, s48\n\t" \
   "s_cbranch_scc1 74b\n\t" "s_bitset1_b32 s44, s48\n\t" "s_lshl_b32 s48, s48, 7\n\t" "s_add_u32 s48, s48, %[cnt]\n\t" "75:\n\t" "s_sleep 10\n\t" \
   "buffer_load_dword v30, off, %[rs], s48 sc1\n\t" "s_add_u32 s47, s47, 1\n\t" "s_waitcnt vmcnt(0)\n\t" "v_add_u32 v30, 1, v30\n\t" \
   "v_cmp_lt_u32 vcc, 7, v30\n\t" "s_cmp_gt_u32 s47, 0x20000\n\t" "s_cbranch_scc1 74b\n\t" "s_cbranch_vccz 75b\n\t" "s_branch 74b\n\t" "178:\n\t" \
   "v_cmp_lt_i32 vcc, 6, v146\n\t" "s_lshr_b32 s48, s45, 1\n\t" "s_or_b32 s44, s44, vcc_lo\n\t" "s_cmp_lt_u32 s40, 4\n\t" "s_cbranch_scc1 278f\n\t" \
   "buffer_load_dword v146, v19, %[rs], 0 offen sc1\n\t" "278:\n\t" "s_bitcmp1_b32 s44, s48\n\t" "s_cbranch_scc1 78b\n\t" "s_mov_b32 s47, 0\n\t" \
   "buffer_load_dword v30, v19, %[rs], 0 offen sc1\n\t" "s_waitcnt vmcnt(0)\n\t" "v_add_u32 v30, 1, v30\n\t" "v_cmp_lt_u32 vcc, 7, v30\n\t" \
   "s_or_b32 s44, s44, vcc_lo\n\t" "s_bitcmp1_b32 s44, s48\n\t" "s_cbranch_scc1 78b\n\t" "s_bitset1_b32 s44, s48\n\t" "s_lshl_b32 s48, s48, 7\n\t" \
   "s_add_u32 s48, s48, %[cnt]\n\t" "79:\n\t" "s_sleep 10\n\t" "buffer_load_dword v30, off, %[rs], s48 sc1\n\t" "s_add_u32 s47, s47, 1\n\t" \
   "s_waitcnt vmcnt(0)\n\t" "v_add_u32 v30, 1, v30\n\t" "v_cmp_lt_u32 vcc, 7, v30\n\t" "s_cmp_gt_u32 s47, 0x20000\n\t" "s_cbranch_scc1 78b\n\t" \
   "s_cbranch_vccz 79b\n\t" "s_branch 78b\n\t" "150:\n\t" "ds_read_b128 v[140:143], v12\n\t" "s_waitcnt lgkmcnt(0)\n\t" \
   "v_min3_u32 v140, v140, v141, v142\n\t" "v_min_u32 v140, v140, v143\n\t" "v_cmp_ge_u32 vcc, v140, v138\n\t" "s_cbranch_vccz 150b\n\t" \
   "s_branch 50b\n\t" "152:\n\t" "ds_read_b128 v[140:143], v12\n\t" "s_waitcnt lgkmcnt(0)\n\t" "v_min3_u32 v140, v140, v141, v142\n\t" \
   "v_min_u32 v140, v140, v143\n\t" "v_cmp_ge_u32 vcc, v140, v138\n\t" "s_cbranch_vccz 152b\n\t" "s_branch 52b\n\t" "151:\n\t" \
   "ds_read_b128 v[140:143], v12\n\t" "s_waitcnt lgkmcnt(0)\n\t" "v_min3_u32 v140, v140, v141, v142\n\t" "v_min_u32 v140, v140, v143\n\t" \
   "v_cmp_ge_u32 vcc, v140, v138\n\t" "s_cbranch_vccz 151b\n\t" "s_branch 51b\n\t" "153:\n\t" "ds_read_b128 v[140:143], v12\n\t" \
   "s_waitcnt lgkmcnt(0)\n\t" "v_min3_u32 v140, v140, v141, v142\n\t" "v_min_u32 v140, v140, v143\n\t" "v_cmp_ge_u32 vcc, v140, v138\n\t" \
   "s_cbranch_vccz 153b\n\t" "s_branch 53b\n\t" "9:\n\t" "s_waitcnt vmcnt(0)"
#define KQ_CLOB "v8", "v9", "v10", "v11", "v12", "v13", "v14", "v15", "v32", "v33", "v34", "v35", "v36", "v37", "v38", "v39", "v40", "v41", "v42", "v43", "v44", "v45", "v46", "v47", "v48", "v49", "v50", "v51", "v52", "v53", "v54", "v55", "v56", "v57", "v58", "v59", "v60", "v61", "v62", "v63", "v64", "v65", "v66", "v67", "v68", "v69", "v70", "v71", "v72", "v73", "v74", "v75", "v76", "v77", "v78", "v79", "v80", "v81", "v82", "v83", "v84", "v85", "v86", "v87", "v88", "v89", "v90", "v91", "v92", "v93", "v94", "v95", "v96", "v97", "v98", "v99", "v100", "v101", "v102", "v103", "v104", "v105", "v106", "v107", "v108", "v109", "v110", "v111", "v112", "v113", "v114", "v115", "v116", "v117", "v118", "v119", "v120", "v121", "v122", "v123", "v124", "v125", "v126", "v127", "v128", "v129", "v130", "v131", "v132", "v133", "v134", "v135", "v136", "v137", "v138", "v139", "v140", "v141", "v142", "v143", "v144", "v145", "v146", "v147", "v148", "v149", "v150", "v151", "v160", "v161", "v162", "v163", "v164", "v165", "v166", "v167", "v168", "v169", "v170", "v171", "v172", "v173", "v174", "v175", "v176", "v177", "v178", "v179", "v180", "v181", "v182", "v183", "v184", "v185", "v186", "v187", "v188", "v189", "v190", "v191", "s40", "s41", "s42", "s43", "s44", "s45", "s46", "s47", "s48", "s49", "s50", "s51", "s52", "s53", "s54", "s55", "s60", "s61"
#define VU_ASM \
   ".p2align 6\n\t" "s_nop 0\n\t" "s_nop 0\n\t" "s_nop 0\n\t" "s_nop 0\n\t" "s_nop 0\n\t" "s_nop 0\n\t" "s_nop 0\n\t" "s_mov_b32 s44, %[rdy]\n\t" \
   "s_mov_b32 s45, 0\n\t" "s_mov_b32 s46, 0\n\t" "s_cmp_eq_u32 s44, -1\n\t" "s_cbranch_scc1 68f\n\t" "s_mul_i32 s52, s45, 1536\n\t" \
   "s_mul_i32 s51, s45, 40960\n\t" "s_add_u32 m0, s46, %[ldx]\n\t" "s_mov_b32 s53, s46\n\t" \
   "buffer_load_dwordx4 v16, %[rs], s51 offen offset:0 sc1 lds\n\t" "buffer_load_dwordx4 v16, %[rs], s51 offen offset:1024 sc1 lds\n\t" \
   "buffer_load_dwordx4 v16, %[rs], s51 offen offset:2048 sc1 lds\n\t" "buffer_load_dwordx4 v16, %[rs], s51 offen offset:3072 sc1 lds\n\t" \
   "s_add_u32 m0, m0, 4096\n\t" "s_add_u32 s51, s51, 4096\n\t" "buffer_load_dwordx4 v16, %[rs], s51 offen offset:0 sc1 lds\n\t" \
   "buffer_load_dwordx4 v16, %[rs], s51 offen offset:1024 sc1 lds\n\t" "buffer_load_dwordx4 v16, %[rs], s51 offen offset:2048 sc1 lds\n\t" \
   "buffer_load_dwordx4 v16, %[rs], s51 offen offset:3072 sc1 lds\n\t" "s_add_u32 m0, m0, 4096\n\t" "s_add_u32 s51, s51, 4096\n\t" \
   "buffer_load_dwordx4 v16, %[rs], s51 offen offset:0 sc1 lds\n\t" "buffer_load_dwordx4 v16, %[rs], s51 offen offset:1024 sc1 lds\n\t" \
   "s_add_u32 m0, s53, %[ld1]\n\t" "s_nop 0\n\t" "buffer_load_dwordx4 v17, %[rs], s52 offen sc1 lds\n\t" "s_mov_b32 s45, 1\n\t" \
   "s_mov_b32 s46, 44032\n\t" "s_mul_i32 s52, s45, 1536\n\t" "s_mul_i32 s51, s45, 40960\n\t" "s_add_u32 m0, s46, %[ldx]\n\t" "s_mov_b32 s53, s46\n\t" \
   "buffer_load_dwordx4 v16, %[rs], s51 offen offset:0 sc1 lds\n\t" "buffer_load_dwordx4 v16, %[rs], s51 offen offset:1024 sc1 lds\n\t" \
   "buffer_load_dwordx4 v16, %[rs], s51 offen offset:2048 sc1 lds\n\t" "buffer_load_dwordx4 v16, %[rs], s51 offen offset:3072 sc1 lds\n\t" \
   "s_add_u32 m0, m0, 4096\n\t" "s_add_u32 s51, s51, 4096\n\t" "buffer_load_dwordx4 v16, %[rs], s51 offen offset:0 sc1 lds\n\t" \
   "buffer_load_dwordx4 v16, %[rs], s51 offen offset:1024 sc1 lds\n\t" "buffer_load_dwordx4 v16, %[rs], s51 offen offset:2048 sc1 lds\n\t" \
   "buffer_load_dwordx4 v16, %[rs], s51 offen offset:3072 sc1 lds\n\t" "s_add_u32 m0, m0, 4096\n\t" "s_add_u32 s51, s51, 4096\n\t" \
   "buffer_load_dwordx4 v16, %[rs], s51 offen offset:0 sc1 lds\n\t" "buffer_load_dwordx4 v16, %[rs], s51 offen offset:1024 sc1 lds\n\t" \
   "s_add_u32 m0, s53, %[ld1]\n\t" "s_nop 0\n\t" "buffer_load_dwordx4 v17, %[rs], s52 offen sc1 lds\n\t" "68:\n\t" "v_mov_b32 v138, 0\n\t" \
   "v_mov_b32 v13, %[flw]\n\t" "v_and_b32 v12, -16, v13\n\t" "ds_write_b32 v13, v138\n\t" "ds_read_b128 v[32:35], v26 offset:0\n\t" \
   "ds_read_b128 v[36:39], v26 offset:16\n\t" "ds_read_b128 v[40:43], v26 offset:32\n\t" "ds_read_b128 v[44:47], v26 offset:48\n\t" \
   "s_mov_b32 s40, 0\n\t" "s_mov_b32 s41, 0\n\t" "s_mov_b32 s42, 0\n\t" "s_mov_b32 s43, 44032\n\t" "v_mov_b32 v28, v22\n\t" "v_mov_b32 v29, v23\n\t" \
   "s_mov_b32 s45, 2\n\t" "s_mov_b32 s46, 88064\n\t" "s_waitcnt vmcnt(11) lgkmcnt(0)\n\t" "s_barrier\n\t" "v_mov_b32 v14, v24\n\t" \
   "v_mov_b32 v15, v25\n\t" "v_mov_b32 v25, -1\n\t" "s_cmp_eq_u32 s44, -1\n\t" "s_cbranch_scc0 172f\n\t" "72:\n\t" \
   "ds_read_b128 v[160:163], v28 offset:0\n\t" "ds_read_b128 v[164:167], v28 offset:512\n\t" "ds_read_b128 v[168:171], v28 offset:1024\n\t" \
   "ds_read_b128 v[172:175], v28 offset:1536\n\t" "ds_read_b128 v[176:179], v28 offset:2048\n\t" "ds_read_b128 v[180:183], v28 offset:2560\n\t" \
   "ds_read_b128 v[184:187], v28 offset:3072\n\t" "v_mov_b32 v18, 0x7ffffff0\n\t" "s_mov_b32 s49, 0\n\t" "5:\n\t" "v_mov_b32 v26, v28\n\t" \
   "v_mov_b32 v27, v29\n\t" "s_waitcnt lgkmcnt(5)\n\t" "v_mfma_f32_32x32x16_bf16 v[48:63], a[0:3], v[160:163], 0\n\t" \
   "ds_read_b128 v[188:191], v26 offset:3584\n\t" "s_mul_i32 s52, s45, 1536\n\t" "s_mul_i32 s51, s45, 40960\n\t" "s_add_u32 m0, s46, %[ldx]\n\t" \
   "s_mov_b32 s53, s46\n\t" "v_mfma_f32_32x32x16_bf16 v[48:63], a[4:7], v[164:167], v[48:63]\n\t" "ds_read_b128 v[160:163], v26 offset:4096\n\t" \
   "ds_read_b128 v[128:131], v27 offset:0\n\t" "ds_read_b128 v[132:135], v27 offset:16\n\t" "s_waitcnt lgkmcnt(7)\n\t" \
   "v_mfma_f32_32x32x16_bf16 v[48:63], a[8:11], v[168:171], v[48:63]\n\t" "ds_read_b128 v[164:167], v26 offset:4608\n\t" \
   "ds_read_b64 v[136:137], v27 offset:32\n\t" "v_add_u32 v28, s43, v22\n\t" "v_add_u32 v29, s43, v23\n\t" \
   "v_mfma_f32_32x32x16_bf16 v[48:63], a[12:15], v[172:175], v[48:63]\n\t" "ds_read_b128 v[168:171], v26 offset:5120\n\t" \
   "buffer_load_dwordx4 v16, %[rs], s51 offen offset:0 sc1 lds\n\t" "s_waitcnt lgkmcnt(8)\n\t" \
   "v_mfma_f32_32x32x16_bf16 v[48:63], a[16:19], v[176:179], v[48:63]\n\t" "ds_read_b128 v[172:175], v26 offset:5632\n\t" \
   "v_mfma_f32_32x32x16_bf16 v[48:63], a[20:23], v[180:183], v[48:63]\n\t" "ds_read_b128 v[176:179], v26 offset:6144\n\t" "s_waitcnt lgkmcnt(3)\n\t" \
   "v_add_f32 v128, v128, v130\n\t" "v_add_f32 v129, v129, v131\n\t" "v_add_f32 v132, v132, v134\n\t" \
   "v_mfma_f32_32x32x16_bf16 v[48:63], a[24:27], v[184:187], v[48:63]\n\t" "ds_read_b128 v[180:183], v26 offset:6656\n\t" \
   "buffer_load_dwordx4 v16, %[rs], s51 offen offset:1024 sc1 lds\n\t" "v_add_f32 v133, v133, v135\n\t" \
   "v_mfma_f32_32x32x16_bf16 v[48:63], a[28:31], v[188:191], v[48:63]\n\t" "ds_read_b128 v[184:187], v26 offset:7168\n\t" \
   "v_add_f32 v128, v128, v136\n\t" "v_add_f32 v129, v129, v137\n\t" "v_add_f32 v128, v128, v132\n\t" \
   "v_mfma_f32_32x32x16_bf16 v[48:63], a[32:35], v[160:163], v[48:63]\n\t" "ds_read_b128 v[188:191], v26 offset:7680\n\t" \
   "v_add_f32 v129, v129, v133\n\t" "v_mul_f32 v128, 0xbacccccd, v128\n\t" "v_mul_f32 v129, 0x3acccccd, v129\n\t" \
   "v_fma_f32 v129, -v128, v128, v129\n\t" "v_mfma_f32_32x32x16_bf16 v[48:63], a[36:39], v[164:167], v[48:63]\n\t" \
   "ds_read_b128 v[160:163], v26 offset:8192\n\t" "buffer_load_dwordx4 v16, %[rs], s51 offen offset:2048 sc1 lds\n\t" "s_waitcnt lgkmcnt(5)\n\t" \
   "v_mfma_f32_32x32x16_bf16 v[48:63], a[40:43], v[168:171], v[48:63]\n\t" "ds_read_b128 v[164:167], v26 offset:8704\n\t" "v_max_f32 v129, 0, v129\n\t" \
   "v_cvt_pk_bf16_f32 v129, v129, v128\n\t" "v_and_b32 v8, 0xffff0000, v129\n\t" "v_lshlrev_b32 v129, 16, v129\n\t" \
   "v_mfma_f32_32x32x16_bf16 v[48:63], a[44:47], v[172:175], v[48:63]\n\t" "ds_read_b128 v[168:171], v26 offset:9216\n\t" \
   "v_add_f32 v129, 0x3727c5ac, v129\n\t" "v_cvt_pk_bf16_f32 v129, v129, v129\n\t" "v_lshlrev_b32 v129, 16, v129\n\t" "s_waitcnt lgkmcnt(5)\n\t" \
   "v_mfma_f32_32x32x16_bf16 v[48:63], a[48:51], v[176:179], v[48:63]\n\t" "ds_read_b128 v[172:175], v26 offset:9728\n\t" "v_sqrt_f32 v129, v129\n\t" \
   "s_nop 0\n\t" "v_cvt_pk_bf16_f32 v129, v129, v129\n\t" "v_mfma_f32_32x32x16_bf16 v[48:63], a[52:55], v[180:183], v[48:63]\n\t" \
   "ds_read_b128 v[176:179], v26 offset:10240\n\t" "v_lshlrev_b32 v129, 16, v129\n\t" "v_rcp_f32 v10, v129\n\t" "s_waitcnt lgkmcnt(5)\n\t" \
   "v_mfma_f32_32x32x16_bf16 v[48:63], a[56:59], v[184:187], v[48:63]\n\t" "ds_read_b128 v[180:183], v26 offset:10752\n\t" \
   "buffer_load_dwordx4 v16, %[rs], s51 offen offset:3072 sc1 lds\n\t" "s_add_u32 m0, m0, 4096\n\t" "s_add_u32 s51, s51, 4096\n\t" \
   "v_mfma_f32_32x32x16_bf16 v[48:63], a[60:63], v[188:191], v[48:63]\n\t" "ds_read_b128 v[184:187], v26 offset:11264\n\t" \
   "buffer_load_dwordx4 v16, %[rs], s51 offen offset:0 sc1 lds\n\t" "s_waitcnt lgkmcnt(5)\n\t" \
   "v_mfma_f32_32x32x16_bf16 v[48:63], a[64:67], v[160:163], v[48:63]\n\t" "ds_read_b128 v[188:191], v26 offset:11776\n\t" \
   "v_mfma_f32_32x32x16_bf16 v[48:63], a[68:71], v[164:167], v[48:63]\n\t" "ds_read_b128 v[160:163], v26 offset:12288\n\t" "s_waitcnt lgkmcnt(5)\n\t" \
   "v_mfma_f32_32x32x16_bf16 v[48:63], a[72:75], v[168:171], v[48:63]\n\t" "ds_read_b128 v[164:167], v26 offset:12800\n\t" \
   "buffer_load_dwordx4 v16, %[rs], s51 offen offset:1024 sc1 lds\n\t" "v_mfma_f32_32x32x16_bf16 v[48:63], a[76:79], v[172:175], v[48:63]\n\t" \
   "ds_read_b128 v[168:171], v26 offset:13312\n\t" "s_waitcnt lgkmcnt(5)\n\t" "v_mfma_f32_32x32x16_bf16 v[48:63], a[80:83], v[176:179], v[48:63]\n\t" \
   "ds_read_b128 v[172:175], v26 offset:13824\n\t" "v_mfma_f32_32x32x16_bf16 v[48:63], a[84:87], v[180:183], v[48:63]\n\t" \
   "ds_read_b128 v[176:179], v26 offset:14336\n\t" "buffer_load_dwordx4 v16, %[rs], s51 offen offset:2048 sc1 lds\n\t" "s_waitcnt lgkmcnt(5)\n\t" \
   "v_mfma_f32_32x32x16_bf16 v[48:63], a[88:91], v[184:187], v[48:63]\n\t" "ds_read_b128 v[180:183], v26 offset:14848\n\t" \
   "v_mfma_f32_32x32x16_bf16 v[48:63], a[92:95], v[188:191], v[48:63]\n\t" "ds_read_b128 v[184:187], v26 offset:15360\n\t" "s_waitcnt lgkmcnt(5)\n\t" \
   "v_mfma_f32_32x32x16_bf16 v[48:63], a[96:99], v[160:163], v[48:63]\n\t" "ds_read_b128 v[188:191], v26 offset:15872\n\t" \
   "v_mfma_f32_32x32x16_bf16 v[48:63], a[100:103], v[164:167], v[48:63]\n\t" "ds_read_b128 v[160:163], v26 offset:16384\n\t" "s_waitcnt lgkmcnt(5)\n\t" \
   "v_mfma_f32_32x32x16_bf16 v[48:63], a[104:107], v[168:171], v[48:63]\n\t" "ds_read_b128 v[164:167], v26 offset:16896\n\t" \
   "buffer_load_dwordx4 v16, %[rs], s51 offen offset:3072 sc1 lds\n\t" "s_add_u32 m0, m0, 4096\n\t" "s_add_u32 s51, s51, 4096\n\t" \
   "v_mfma_f32_32x32x16_bf16 v[48:63], a[108:111], v[172:175], v[48:63]\n\t" "ds_read_b128 v[168:171], v26 offset:17408\n\t" \
   "buffer_load_dwordx4 v16, %[rs], s51 offen offset:0 sc1 lds\n\t" "s_waitcnt lgkmcnt(5)\n\t" \
   "v_mfma_f32_32x32x16_bf16 v[48:63], a[112:115], v[176:179], v[48:63]\n\t" "ds_read_b128 v[172:175], v26 offset:17920\n\t" "s_cmp_lt_i32 s40, 1\n\t" \
   "s_cselect_b32 s47, 0x7ffffff0, 0\n\t" "v_or_b32 v30, s47, v20\n\t" "s_sub_i32 s47, s41, 1\n\t" \
   "v_mfma_f32_32x32x16_bf16 v[48:63], a[116:119], v[180:183], v[48:63]\n\t" "ds_read_b128 v[176:179], v26 offset:18432\n\t" \
   "s_max_i32 s47, s47, 0\n\t" "s_lshl_b32 s47, s47, 12\n\t" "s_waitcnt vmcnt(9)\n\t" "s_waitcnt lgkmcnt(5)\n\t" \
   "v_mfma_f32_32x32x16_bf16 v[48:63], a[120:123], v[184:187], v[48:63]\n\t" "ds_read_b128 v[180:183], v26 offset:18944\n\t" \
   "buffer_store_dword v31, v30, %[rs], s47 offen sc1\n\t" "v_mfma_f32_32x32x16_bf16 v[48:63], a[124:127], v[188:191], v[48:63]\n\t" \
   "ds_read_b128 v[184:187], v26 offset:19456\n\t" "s_waitcnt lgkmcnt(5)\n\t" "v_mfma_f32_32x32x16_bf16 v[48:63], a[128:131], v[160:163], v[48:63]\n\t" \
   "ds_read_b128 v[188:191], v26 offset:19968\n\t" "buffer_load_dwordx4 v16, %[rs], s51 offen offset:1024 sc1 lds\n\t" "s_add_u32 m0, s53, %[ld1]\n\t" \
   "s_nop 0\n\t" "v_mfma_f32_32x32x16_bf16 v[48:63], a[132:135], v[164:167], v[48:63]\n\t" "buffer_load_dwordx4 v17, %[rs], s52 offen sc1 lds\n\t" \
   "s_waitcnt lgkmcnt(4)\n\t" "v_mfma_f32_32x32x16_bf16 v[48:63], a[136:139], v[168:171], v[48:63]\n\t" \
   "v_mfma_f32_32x32x16_bf16 v[48:63], a[140:143], v[172:175], v[48:63]\n\t" "s_waitcnt lgkmcnt(2)\n\t" \
   "v_mfma_f32_32x32x16_bf16 v[48:63], a[144:147], v[176:179], v[48:63]\n\t" "v_mfma_f32_32x32x16_bf16 v[48:63], a[148:151], v[180:183], v[48:63]\n\t" \
   "s_waitcnt lgkmcnt(0)\n\t" "v_mfma_f32_32x32x16_bf16 v[48:63], a[152:155], v[184:187], v[48:63]\n\t" \
   "v_mfma_f32_32x32x16_bf16 v[48:63], a[156:159], v[188:191], v[48:63]\n\t" "s_memrealtime s[54:55]\n\t" "s_nop 7\n\t" "s_nop 2\n\t" \
   "v_fmac_f32 v48, v32, v8\n\t" "v_fmac_f32 v49, v33, v8\n\t" "v_fmac_f32 v50, v34, v8\n\t" "v_fmac_f32 v51, v35, v8\n\t" \
   "v_fmac_f32 v52, v36, v8\n\t" "v_fmac_f32 v53, v37, v8\n\t" "v_fmac_f32 v54, v38, v8\n\t" "v_fmac_f32 v55, v39, v8\n\t" \
   "v_fmac_f32 v56, v40, v8\n\t" "v_fmac_f32 v57, v41, v8\n\t" "v_fmac_f32 v58, v42, v8\n\t" "v_fmac_f32 v59, v43, v8\n\t" \
   "v_fmac_f32 v60, v44, v8\n\t" "v_fmac_f32 v61, v45, v8\n\t" "v_fmac_f32 v62, v46, v8\n\t" "v_fmac_f32 v63, v47, v8\n\t" \
   "v_mul_f32 v48, v48, v10\n\t" "v_mul_f32 v49, v49, v10\n\t" "v_mul_f32 v50, v50, v10\n\t" "v_mul_f32 v51, v51, v10\n\t" \
   "v_mul_f32 v52, v52, v10\n\t" "v_mul_f32 v53, v53, v10\n\t" "v_mul_f32 v54, v54, v10\n\t" "v_mul_f32 v55, v55, v10\n\t" \
   "v_mul_f32 v56, v56, v10\n\t" "v_mul_f32 v57, v57, v10\n\t" "v_mul_f32 v58, v58, v10\n\t" "v_mul_f32 v59, v59, v10\n\t" \
   "v_mul_f32 v60, v60, v10\n\t" "v_mul_f32 v61, v61, v10\n\t" "v_mul_f32 v62, v62, v10\n\t" "v_mul_f32 v63, v63, v10\n\t" \
   "v_cvt_pk_bf16_f32 v48, v48, v49\n\t" "v_cvt_pk_bf16_f32 v49, v50, v51\n\t" "v_cvt_pk_bf16_f32 v50, v52, v53\n\t" \
   "v_cvt_pk_bf16_f32 v51, v54, v55\n\t" "ds_write_b128 v14, v[48:51] offset:0\n\t" "v_cvt_pk_bf16_f32 v56, v56, v57\n\t" \
   "v_cvt_pk_bf16_f32 v57, v58, v59\n\t" "v_cvt_pk_bf16_f32 v58, v60, v61\n\t" "v_cvt_pk_bf16_f32 v59, v62, v63\n\t" \
   "ds_write_b128 v14, v[56:59] offset:1024\n\t" "s_waitcnt vmcnt(12) lgkmcnt(0)\n\t" "s_barrier\n\t" "s_mov_b32 s42, s43\n\t" \
   "s_add_u32 s43, s43, 44032\n\t" "s_cmp_eq_u32 s43, 132096\n\t" "s_cselect_b32 s43, 0, s43\n\t" "s_add_u32 s41, s41, 1\n\t" \
   "s_add_u32 s40, s40, 1\n\t" "v_xor_b32 v14, 0x2000, v14\n\t" "v_xor_b32 v15, 0x2000, v15\n\t" "s_cmp_eq_u32 s40, 1\n\t" "s_cbranch_scc0 6f\n\t" \
   "ds_read_b128 v[96:99], v15 offset:0\n\t" "ds_read_b128 v[100:103], v15 offset:1024\n\t" "ds_read_b128 v[104:107], v15 offset:2048\n\t" \
   "ds_read_b128 v[108:111], v15 offset:3072\n\t" "ds_read_b128 v[112:115], v15 offset:4096\n\t" "ds_read_b128 v[116:119], v15 offset:5120\n\t" \
   "ds_read_b128 v[120:123], v15 offset:6144\n\t" "ds_read_b128 v[124:127], v15 offset:7168\n\t" "s_sub_i32 s49, s41, 1\n\t" \
   "s_mul_i32 s49, s49, 1310720\n\t" "v_mov_b32 v31, s54\n\t" "v_min_u32 v31, -2, v31\n\t" "s_mov_b32 s45, 3\n\t" "s_mov_b32 s46, 0\n\t" \
   "s_waitcnt lgkmcnt(6)\n\t" "v_mfma_f32_32x32x16_bf16 v[64:79], v[96:99], a[160:163], 0\n\t" \
   "v_mfma_f32_32x32x16_bf16 v[64:79], v[100:103], a[176:179], v[64:79]\n\t" "s_waitcnt lgkmcnt(4)\n\t" \
   "v_mfma_f32_32x32x16_bf16 v[64:79], v[104:107], a[164:167], v[64:79]\n\t" "v_mfma_f32_32x32x16_bf16 v[64:79], v[108:111], a[180:183], v[64:79]\n\t" \
   "s_waitcnt lgkmcnt(2)\n\t" "v_mfma_f32_32x32x16_bf16 v[64:79], v[112:115], a[168:171], v[64:79]\n\t" \
   "v_mfma_f32_32x32x16_bf16 v[64:79], v[116:119], a[184:187], v[64:79]\n\t" "s_waitcnt lgkmcnt(0)\n\t" \
   "v_mfma_f32_32x32x16_bf16 v[64:79], v[120:123], a[172:175], v[64:79]\n\t" "v_mfma_f32_32x32x16_bf16 v[64:79], v[124:127], a[188:191], v[64:79]\n\t" \
   "v_mfma_f32_32x32x16_bf16 v[80:95], v[96:99], a[192:195], 0\n\t" "v_mfma_f32_32x32x16_bf16 v[80:95], v[100:103], a[208:211], v[80:95]\n\t" \
   "s_add_u32 s50, s49, 0\n\t" "v_cvt_pk_bf16_f32 v64, v64, v65\n\t" "v_cvt_pk_bf16_f32 v65, v72, v73\n\t" "v_cvt_pk_bf16_f32 v66, v66, v67\n\t" \
   "v_cvt_pk_bf16_f32 v67, v74, v75\n\t" "v_mfma_f32_32x32x16_bf16 v[80:95], v[104:107], a[196:199], v[80:95]\n\t" \
   "buffer_store_dwordx4 v[64:67], v21, %[rs], s50 offen offset:0 sc1\n\t" "v_cvt_pk_bf16_f32 v68, v68, v69\n\t" \
   "v_mfma_f32_32x32x16_bf16 v[80:95], v[108:111], a[212:215], v[80:95]\n\t" "v_cvt_pk_bf16_f32 v69, v76, v77\n\t" \
   "v_cvt_pk_bf16_f32 v70, v70, v71\n\t" "v_cvt_pk_bf16_f32 v71, v78, v79\n\t" \
   "v_mfma_f32_32x32x16_bf16 v[80:95], v[112:115], a[200:203], v[80:95]\n\t" "buffer_store_dwordx4 v[68:71], v21, %[rs], s50 offen offset:512 sc1\n\t" \
   "v_mfma_f32_32x32x16_bf16 v[80:95], v[116:119], a[216:219], v[80:95]\n\t" "v_mfma_f32_32x32x16_bf16 v[80:95], v[120:123], a[204:207], v[80:95]\n\t" \
   "v_mfma_f32_32x32x16_bf16 v[80:95], v[124:127], a[220:223], v[80:95]\n\t" "v_mfma_f32_32x32x16_bf16 v[64:79], v[96:99], a[224:227], 0\n\t" \
   "v_mfma_f32_32x32x16_bf16 v[64:79], v[100:103], a[240:243], v[64:79]\n\t" "s_add_u32 s50, s49, 65536\n\t" "v_cvt_pk_bf16_f32 v80, v80, v81\n\t" \
   "v_cvt_pk_bf16_f32 v81, v88, v89\n\t" "v_cvt_pk_bf16_f32 v82, v82, v83\n\t" "v_cvt_pk_bf16_f32 v83, v90, v91\n\t" \
   "v_mfma_f32_32x32x16_bf16 v[64:79], v[104:107], a[228:231], v[64:79]\n\t" "buffer_store_dwordx4 v[80:83], v21, %[rs], s50 offen offset:0 sc1\n\t" \
   "v_cvt_pk_bf16_f32 v84, v84, v85\n\t" "v_mfma_f32_32x32x16_bf16 v[64:79], v[108:111], a[244:247], v[64:79]\n\t" \
   "v_cvt_pk_bf16_f32 v85, v92, v93\n\t" "v_cvt_pk_bf16_f32 v86, v86, v87\n\t" "v_cvt_pk_bf16_f32 v87, v94, v95\n\t" \
   "v_mfma_f32_32x32x16_bf16 v[64:79], v[112:115], a[232:235], v[64:79]\n\t" "buffer_store_dwordx4 v[84:87], v21, %[rs], s50 offen offset:512 sc1\n\t" \
   "v_mfma_f32_32x32x16_bf16 v[64:79], v[116:119], a[248:251], v[64:79]\n\t" "ds_read_b128 v[160:163], v28 offset:0\n\t" \
   "ds_read_b128 v[164:167], v28 offset:512\n\t" "ds_read_b128 v[168:171], v28 offset:1024\n\t" "ds_read_b128 v[172:175], v28 offset:1536\n\t" \
   "v_mfma_f32_32x32x16_bf16 v[64:79], v[120:123], a[236:239], v[64:79]\n\t" "ds_read_b128 v[176:179], v28 offset:2048\n\t" \
   "ds_read_b128 v[180:183], v28 offset:2560\n\t" "ds_read_b128 v[184:187], v28 offset:3072\n\t" \
   "v_mfma_f32_32x32x16_bf16 v[64:79], v[124:127], a[252:255], v[64:79]\n\t" "v_mfma_f32_32x32x16_bf16 v[80:95], v[96:99], v[192:195], 0\n\t" \
   "v_mfma_f32_32x32x16_bf16 v[80:95], v[100:103], v[208:211], v[80:95]\n\t" "s_add_u32 s50, s49, 131072\n\t" "v_cvt_pk_bf16_f32 v64, v64, v65\n\t" \
   "v_cvt_pk_bf16_f32 v65, v72, v73\n\t" "v_cvt_pk_bf16_f32 v66, v66, v67\n\t" "v_cvt_pk_bf16_f32 v67, v74, v75\n\t" \
   "v_mfma_f32_32x32x16_bf16 v[80:95], v[104:107], v[196:199], v[80:95]\n\t" "buffer_store_dwordx4 v[64:67], v21, %[rs], s50 offen offset:0 sc1\n\t" \
   "v_cvt_pk_bf16_f32 v68, v68, v69\n\t" "v_mfma_f32_32x32x16_bf16 v[80:95], v[108:111], v[212:215], v[80:95]\n\t" \
   "v_cvt_pk_bf16_f32 v69, v76, v77\n\t" "v_cvt_pk_bf16_f32 v70, v70, v71\n\t" "v_cvt_pk_bf16_f32 v71, v78, v79\n\t" \
   "v_mfma_f32_32x32x16_bf16 v[80:95], v[112:115], v[200:203], v[80:95]\n\t" "buffer_store_dwordx4 v[68:71], v21, %[rs], s50 offen offset:512 sc1\n\t" \
   "v_mfma_f32_32x32x16_bf16 v[80:95], v[116:119], v[216:219], v[80:95]\n\t" "v_mfma_f32_32x32x16_bf16 v[80:95], v[120:123], v[204:207], v[80:95]\n\t" \
   "v_mfma_f32_32x32x16_bf16 v[80:95], v[124:127], v[220:223], v[80:95]\n\t" "v_mfma_f32_32x32x16_bf16 v[140:155], v[96:99], v[224:227], 0\n\t" \
   "v_mfma_f32_32x32x16_bf16 v[140:155], v[100:103], v[240:243], v[140:155]\n\t" "s_add_u32 s50, s49, 196608\n\t" "v_cvt_pk_bf16_f32 v80, v80, v81\n\t" \
   "v_cvt_pk_bf16_f32 v81, v88, v89\n\t" "v_cvt_pk_bf16_f32 v82, v82, v83\n\t" "v_cvt_pk_bf16_f32 v83, v90, v91\n\t" \
   "v_mfma_f32_32x32x16_bf16 v[140:155], v[104:107], v[228:231], v[140:155]\n\t" \
   "buffer_store_dwordx4 v[80:83], v21, %[rs], s50 offen offset:0 sc1\n\t" "v_cvt_pk_bf16_f32 v84, v84, v85\n\t" \
   "v_mfma_f32_32x32x16_bf16 v[140:155], v[108:111], v[244:247], v[140:155]\n\t" "v_cvt_pk_bf16_f32 v85, v92, v93\n\t" \
   "v_cvt_pk_bf16_f32 v86, v86, v87\n\t" "v_cvt_pk_bf16_f32 v87, v94, v95\n\t" \
   "v_mfma_f32_32x32x16_bf16 v[140:155], v[112:115], v[232:235], v[140:155]\n\t" \
   "buffer_store_dwordx4 v[84:87], v21, %[rs], s50 offen offset:512 sc1\n\t" \
   "v_mfma_f32_32x32x16_bf16 v[140:155], v[116:119], v[248:251], v[140:155]\n\t" \
   "v_mfma_f32_32x32x16_bf16 v[140:155], v[120:123], v[236:239], v[140:155]\n\t" \
   "v_mfma_f32_32x32x16_bf16 v[140:155], v[124:127], v[252:255], v[140:155]\n\t" "s_nop 7\n\t" "s_nop 2\n\t" "s_add_u32 s50, s49, 262144\n\t" \
   "v_cvt_pk_bf16_f32 v140, v140, v141\n\t" "v_cvt_pk_bf16_f32 v141, v148, v149\n\t" "v_cvt_pk_bf16_f32 v142, v142, v143\n\t" \
   "v_cvt_pk_bf16_f32 v143, v150, v151\n\t" "buffer_store_dwordx4 v[140:143], v21, %[rs], s50 offen offset:0 sc1\n\t" \
   "v_cvt_pk_bf16_f32 v144, v144, v145\n\t" "v_cvt_pk_bf16_f32 v145, v152, v153\n\t" "v_cvt_pk_bf16_f32 v146, v146, v147\n\t" \
   "v_cvt_pk_bf16_f32 v147, v154, v155\n\t" "buffer_store_dwordx4 v[144:147], v21, %[rs], s50 offen offset:512 sc1\n\t" "s_branch 5b\n\t" "6:\n\t" \
   "ds_read_b128 v[160:163], v28 offset:0\n\t" "ds_read_b128 v[164:167], v28 offset:512\n\t" "ds_read_b128 v[168:171], v28 offset:1024\n\t" \
   "ds_read_b128 v[172:175], v28 offset:1536\n\t" "ds_read_b128 v[176:179], v28 offset:2048\n\t" "ds_read_b128 v[180:183], v28 offset:2560\n\t" \
   "ds_read_b128 v[184:187], v28 offset:3072\n\t" "s_mov_b32 s45, 4\n\t" "s_mov_b32 s46, 44032\n\t" "s_cmp_eq_u32 s44, -1\n\t" \
   "s_cbranch_scc0 176f\n\t" "76:\n\t" "1:\n\t" "v_mov_b32 v26, v28\n\t" "v_mov_b32 v27, v29\n\t" "s_waitcnt lgkmcnt(5)\n\t" \
   "v_mfma_f32_32x32x16_bf16 v[48:63], a[0:3], v[160:163], 0\n\t" "ds_read_b128 v[188:191], v26 offset:3584\n\t" "ds_read_b128 v[156:159], v12\n\t" \
   "v_mfma_f32_32x32x16_bf16 v[48:63], a[4:7], v[164:167], v[48:63]\n\t" "ds_read_b128 v[160:163], v26 offset:4096\n\t" \
   "s_add_u32 s50, s49, 262144\n\t" "v_cvt_pk_bf16_f32 v140, v140, v141\n\t" "v_cvt_pk_bf16_f32 v141, v148, v149\n\t" "s_waitcnt lgkmcnt(6)\n\t" \
   "v_mfma_f32_32x32x16_bf16 v[48:63], a[8:11], v[168:171], v[48:63]\n\t" "ds_read_b128 v[164:167], v26 offset:4608\n\t" "s_waitcnt lgkmcnt(2)\n\t" \
   "v_min3_u32 v156, v156, v157, v158\n\t" "v_min_u32 v156, v156, v159\n\t" "v_cmp_ge_u32 vcc, v156, v138\n\t" \
   "v_mfma_f32_32x32x16_bf16 v[48:63], a[12:15], v[172:175], v[48:63]\n\t" "ds_read_b128 v[168:171], v26 offset:5120\n\t" "s_cbranch_vccz 150f\n\t" \
   "50:\n\t" "ds_read_b128 v[96:99], v15 offset:0\n\t" "v_mfma_f32_32x32x16_bf16 v[48:63], a[16:19], v[176:179], v[48:63]\n\t" \
   "ds_read_b128 v[172:175], v26 offset:5632\n\t" "ds_read_b128 v[100:103], v15 offset:1024\n\t" "ds_read_b128 v[104:107], v15 offset:2048\n\t" \
   "ds_read_b128 v[108:111], v15 offset:3072\n\t" "v_mfma_f32_32x32x16_bf16 v[48:63], a[20:23], v[180:183], v[48:63]\n\t" \
   "ds_read_b128 v[176:179], v26 offset:6144\n\t" "ds_read_b128 v[112:115], v15 offset:4096\n\t" "ds_read_b128 v[116:119], v15 offset:5120\n\t" \
   "v_mfma_f32_32x32x16_bf16 v[48:63], a[24:27], v[184:187], v[48:63]\n\t" "ds_read_b128 v[180:183], v26 offset:6656\n\t" \
   "ds_read_b128 v[120:123], v15 offset:6144\n\t" "ds_read_b128 v[124:127], v15 offset:7168\n\t" "s_mul_i32 s52, s45, 1536\n\t" \
   "s_mul_i32 s51, s45, 40960\n\t" "s_add_u32 m0, s46, %[ldx]\n\t" "s_mov_b32 s53, s46\n\t" \
   "v_mfma_f32_32x32x16_bf16 v[48:63], a[28:31], v[188:191], v[48:63]\n\t" "ds_read_b128 v[184:187], v26 offset:7168\n\t" \
   "v_cvt_pk_bf16_f32 v142, v142, v143\n\t" "v_cvt_pk_bf16_f32 v143, v150, v151\n\t" "s_waitcnt lgkmcnt(9)\n\t" \
   "v_mfma_f32_32x32x16_bf16 v[64:79], v[96:99], a[160:163], 0\n\t" "buffer_store_dwordx4 v[140:143], v18, %[rs], s50 offen offset:0 sc1\n\t" \
   "v_mfma_f32_32x32x16_bf16 v[48:63], a[32:35], v[160:163], v[48:63]\n\t" "ds_read_b128 v[188:191], v26 offset:7680\n\t" \
   "v_cvt_pk_bf16_f32 v144, v144, v145\n\t" "v_cvt_pk_bf16_f32 v145, v152, v153\n\t" "v_cvt_pk_bf16_f32 v146, v146, v147\n\t" \
   "v_cvt_pk_bf16_f32 v147, v154, v155\n\t" "v_mfma_f32_32x32x16_bf16 v[48:63], a[36:39], v[164:167], v[48:63]\n\t" \
   "ds_read_b128 v[160:163], v26 offset:8192\n\t" "v_add_u32 v28, s43, v22\n\t" "v_add_u32 v29, s43, v23\n\t" \
   "v_mfma_f32_32x32x16_bf16 v[64:79], v[100:103], a[176:179], v[64:79]\n\t" \
   "buffer_store_dwordx4 v[144:147], v18, %[rs], s50 offen offset:512 sc1\n\t" "s_sub_i32 s49, s41, 1\n\t" "s_mul_i32 s49, s49, 1310720\n\t" \
   "v_mfma_f32_32x32x16_bf16 v[48:63], a[40:43], v[168:171], v[48:63]\n\t" "ds_read_b128 v[164:167], v26 offset:8704\n\t" \
   "ds_read_b128 v[128:131], v27 offset:0\n\t" "buffer_load_dwordx4 v16, %[rs], s51 offen offset:0 sc1 lds\n\t" \
   "v_mfma_f32_32x32x16_bf16 v[48:63], a[44:47], v[172:175], v[48:63]\n\t" "ds_read_b128 v[168:171], v26 offset:9216\n\t" \
   "ds_read_b128 v[132:135], v27 offset:16\n\t" "ds_read_b64 v[136:137], v27 offset:32\n\t" "s_waitcnt lgkmcnt(14)\n\t" \
   "v_mfma_f32_32x32x16_bf16 v[64:79], v[104:107], a[164:167], v[64:79]\n\t" "s_waitcnt lgkmcnt(10)\n\t" \
   "v_mfma_f32_32x32x16_bf16 v[48:63], a[48:51], v[176:179], v[48:63]\n\t" "ds_read_b128 v[172:175], v26 offset:9728\n\t" \
   "v_mfma_f32_32x32x16_bf16 v[48:63], a[52:55], v[180:183], v[48:63]\n\t" "ds_read_b128 v[176:179], v26 offset:10240\n\t" \
   "buffer_load_dwordx4 v16, %[rs], s51 offen offset:1024 sc1 lds\n\t" "v_mfma_f32_32x32x16_bf16 v[64:79], v[108:111], a[180:183], v[64:79]\n\t" \
   "s_waitcnt lgkmcnt(2)\n\t" "v_add_f32 v128, v128, v130\n\t" "v_add_f32 v129, v129, v131\n\t" "v_add_f32 v132, v132, v134\n\t" \
   "v_add_f32 v133, v133, v135\n\t" "v_mfma_f32_32x32x16_bf16 v[48:63], a[56:59], v[184:187], v[48:63]\n\t" \
   "ds_read_b128 v[180:183], v26 offset:10752\n\t" "v_add_f32 v128, v128, v136\n\t" "v_add_f32 v129, v129, v137\n\t" "v_add_f32 v128, v128, v132\n\t" \
   "v_add_f32 v129, v129, v133\n\t" "v_mfma_f32_32x32x16_bf16 v[48:63], a[60:63], v[188:191], v[48:63]\n\t" \
   "ds_read_b128 v[184:187], v26 offset:11264\n\t" "v_mul_f32 v128, 0xbacccccd, v128\n\t" "v_mul_f32 v129, 0x3acccccd, v129\n\t" \
   "v_fma_f32 v129, -v128, v128, v129\n\t" "v_max_f32 v129, 0, v129\n\t" "v_mfma_f32_32x32x16_bf16 v[64:79], v[112:115], a[168:171], v[64:79]\n\t" \
   "buffer_load_dwordx4 v16, %[rs], s51 offen offset:2048 sc1 lds\n\t" "v_cvt_pk_bf16_f32 v129, v129, v128\n\t" "v_and_b32 v8, 0xffff0000, v129\n\t" \
   "v_mfma_f32_32x32x16_bf16 v[48:63], a[64:67], v[160:163], v[48:63]\n\t" "ds_read_b128 v[188:191], v26 offset:11776\n\t" \
   "v_lshlrev_b32 v129, 16, v129\n\t" "v_add_f32 v129, 0x3727c5ac, v129\n\t" "v_cvt_pk_bf16_f32 v129, v129, v129\n\t" \
   "v_lshlrev_b32 v129, 16, v129\n\t" "v_mfma_f32_32x32x16_bf16 v[48:63], a[68:71], v[164:167], v[48:63]\n\t" \
   "ds_read_b128 v[160:163], v26 offset:12288\n\t" "v_sqrt_f32 v129, v129\n\t" "s_nop 0\n\t" "v_cvt_pk_bf16_f32 v129, v129, v129\n\t" \
   "v_mfma_f32_32x32x16_bf16 v[64:79], v[116:119], a[184:187], v[64:79]\n\t" "v_lshlrev_b32 v129, 16, v129\n\t" "v_rcp_f32 v10, v129\n\t" \
   "s_waitcnt lgkmcnt(5)\n\t" "v_mfma_f32_32x32x16_bf16 v[48:63], a[72:75], v[168:171], v[48:63]\n\t" "ds_read_b128 v[164:167], v26 offset:12800\n\t" \
   "v_mfma_f32_32x32x16_bf16 v[48:63], a[76:79], v[172:175], v[48:63]\n\t" "ds_read_b128 v[168:171], v26 offset:13312\n\t" \
   "v_mfma_f32_32x32x16_bf16 v[64:79], v[120:123], a[172:175], v[64:79]\n\t" "buffer_load_dwordx4 v16, %[rs], s51 offen offset:3072 sc1 lds\n\t" \
   "s_add_u32 m0, m0, 4096\n\t" "s_add_u32 s51, s51, 4096\n\t" "s_waitcnt lgkmcnt(5)\n\t" \
   "v_mfma_f32_32x32x16_bf16 v[48:63], a[80:83], v[176:179], v[48:63]\n\t" "ds_read_b128 v[172:175], v26 offset:13824\n\t" \
   "v_mfma_f32_32x32x16_bf16 v[48:63], a[84:87], v[180:183], v[48:63]\n\t" "ds_read_b128 v[176:179], v26 offset:14336\n\t" \
   "buffer_load_dwordx4 v16, %[rs], s51 offen offset:0 sc1 lds\n\t" "v_mfma_f32_32x32x16_bf16 v[64:79], v[124:127], a[188:191], v[64:79]\n\t" \
   "s_waitcnt lgkmcnt(5)\n\t" "v_mfma_f32_32x32x16_bf16 v[48:63], a[88:91], v[184:187], v[48:63]\n\t" "ds_read_b128 v[180:183], v26 offset:14848\n\t" \
   "v_mfma_f32_32x32x16_bf16 v[48:63], a[92:95], v[188:191], v[48:63]\n\t" "ds_read_b128 v[184:187], v26 offset:15360\n\t" "s_add_u32 s50, s49, 0\n\t" \
   "v_cvt_pk_bf16_f32 v64, v64, v65\n\t" "v_cvt_pk_bf16_f32 v65, v72, v73\n\t" "v_cvt_pk_bf16_f32 v66, v66, v67\n\t" \
   "v_mfma_f32_32x32x16_bf16 v[80:95], v[96:99], a[192:195], 0\n\t" "v_cvt_pk_bf16_f32 v67, v74, v75\n\t" \
   "buffer_load_dwordx4 v16, %[rs], s51 offen offset:1024 sc1 lds\n\t" "s_waitcnt lgkmcnt(5)\n\t" \
   "v_mfma_f32_32x32x16_bf16 v[48:63], a[96:99], v[160:163], v[48:63]\n\t" "ds_read_b128 v[188:191], v26 offset:15872\n\t" \
   "v_mfma_f32_32x32x16_bf16 v[48:63], a[100:103], v[164:167], v[48:63]\n\t" "ds_read_b128 v[160:163], v26 offset:16384\n\t" \
   "v_mfma_f32_32x32x16_bf16 v[80:95], v[100:103], a[208:211], v[80:95]\n\t" "buffer_store_dwordx4 v[64:67], v21, %[rs], s50 offen offset:0 sc1\n\t" \
   "s_waitcnt lgkmcnt(5)\n\t" "v_mfma_f32_32x32x16_bf16 v[48:63], a[104:107], v[168:171], v[48:63]\n\t" "ds_read_b128 v[164:167], v26 offset:16896\n\t" \
   "v_cvt_pk_bf16_f32 v68, v68, v69\n\t" "v_cvt_pk_bf16_f32 v69, v76, v77\n\t" "v_cvt_pk_bf16_f32 v70, v70, v71\n\t" \
   "v_cvt_pk_bf16_f32 v71, v78, v79\n\t" "v_mfma_f32_32x32x16_bf16 v[48:63], a[108:111], v[172:175], v[48:63]\n\t" \
   "ds_read_b128 v[168:171], v26 offset:17408\n\t" "s_waitcnt vmcnt(9)\n\t" "v_add_u32 v138, 1, v138\n\t" "ds_write_b32 v13, v138\n\t" \
   "v_mfma_f32_32x32x16_bf16 v[80:95], v[104:107], a[196:199], v[80:95]\n\t" "buffer_store_dwordx4 v[68:71], v21, %[rs], s50 offen offset:512 sc1\n\t" \
   "s_waitcnt lgkmcnt(6)\n\t" "v_mfma_f32_32x32x16_bf16 v[48:63], a[112:115], v[176:179], v[48:63]\n\t" "ds_read_b128 v[172:175], v26 offset:17920\n\t" \
   "buffer_load_dwordx4 v16, %[rs], s51 offen offset:2048 sc1 lds\n\t" "v_mfma_f32_32x32x16_bf16 v[48:63], a[116:119], v[180:183], v[48:63]\n\t" \
   "ds_read_b128 v[176:179], v26 offset:18432\n\t" "v_mfma_f32_32x32x16_bf16 v[80:95], v[108:111], a[212:215], v[80:95]\n\t" \
   "buffer_load_dwordx4 v16, %[rs], s51 offen offset:3072 sc1 lds\n\t" "s_add_u32 m0, m0, 4096\n\t" "s_add_u32 s51, s51, 4096\n\t" \
   "s_waitcnt lgkmcnt(6)\n\t" "v_mfma_f32_32x32x16_bf16 v[48:63], a[120:123], v[184:187], v[48:63]\n\t" "ds_read_b128 v[180:183], v26 offset:18944\n\t" \
   "v_mfma_f32_32x32x16_bf16 v[48:63], a[124:127], v[188:191], v[48:63]\n\t" "ds_read_b128 v[184:187], v26 offset:19456\n\t" \
   "v_mfma_f32_32x32x16_bf16 v[80:95], v[112:115], a[200:203], v[80:95]\n\t" "buffer_load_dwordx4 v16, %[rs], s51 offen offset:0 sc1 lds\n\t" \
   "s_waitcnt lgkmcnt(6)\n\t" "v_mfma_f32_32x32x16_bf16 v[48:63], a[128:131], v[160:163], v[48:63]\n\t" "ds_read_b128 v[188:191], v26 offset:19968\n\t" \
   "v_mfma_f32_32x32x16_bf16 v[48:63], a[132:135], v[164:167], v[48:63]\n\t" "v_mfma_f32_32x32x16_bf16 v[80:95], v[116:119], a[216:219], v[80:95]\n\t" \
   "s_waitcnt lgkmcnt(4)\n\t" "v_mfma_f32_32x32x16_bf16 v[48:63], a[136:139], v[168:171], v[48:63]\n\t" "s_cmp_lt_i32 s40, 2\n\t" \
   "s_cselect_b32 s47, 0x7ffffff0, 0\n\t" "v_or_b32 v30, s47, v20\n\t" "s_sub_i32 s47, s41, 2\n\t" "s_max_i32 s47, s47, 0\n\t" \
   "s_lshl_b32 s47, s47, 12\n\t" "v_mfma_f32_32x32x16_bf16 v[48:63], a[140:143], v[172:175], v[48:63]\n\t" "s_waitcnt vmcnt(11)\n\t" \
   "buffer_load_dwordx4 v16, %[rs], s51 offen offset:1024 sc1 lds\n\t" "s_add_u32 m0, s53, %[ld1]\n\t" "s_nop 0\n\t" \
   "v_mfma_f32_32x32x16_bf16 v[80:95], v[120:123], a[204:207], v[80:95]\n\t" "buffer_store_dword v31, v30, %[rs], s47 offen sc1\n\t" \
   "s_waitcnt lgkmcnt(2)\n\t" "v_mfma_f32_32x32x16_bf16 v[48:63], a[144:147], v[176:179], v[48:63]\n\t" "ds_read_b128 v[156:159], v12\n\t" \
   "v_mfma_f32_32x32x16_bf16 v[48:63], a[148:151], v[180:183], v[48:63]\n\t" "buffer_load_dwordx4 v17, %[rs], s52 offen sc1 lds\n\t" \
   "v_mfma_f32_32x32x16_bf16 v[80:95], v[124:127], a[220:223], v[80:95]\n\t" "s_waitcnt lgkmcnt(1)\n\t" \
   "v_mfma_f32_32x32x16_bf16 v[48:63], a[152:155], v[184:187], v[48:63]\n\t" "s_waitcnt lgkmcnt(0)\n\t" "v_min3_u32 v156, v156, v157, v158\n\t" \
   "v_min_u32 v156, v156, v159\n\t" "v_cmp_ge_u32 vcc, v156, v138\n\t" "s_cbranch_vccz 152f\n\t" "52:\n\t" \
   "v_mfma_f32_32x32x16_bf16 v[48:63], a[156:159], v[188:191], v[48:63]\n\t" "s_add_u32 s50, s49, 65536\n\t" "v_cvt_pk_bf16_f32 v80, v80, v81\n\t" \
   "v_cvt_pk_bf16_f32 v81, v88, v89\n\t" "v_cvt_pk_bf16_f32 v82, v82, v83\n\t" "v_cvt_pk_bf16_f32 v83, v90, v91\n\t" \
   "v_mfma_f32_32x32x16_bf16 v[64:79], v[96:99], a[224:227], 0\n\t" "buffer_store_dwordx4 v[80:83], v21, %[rs], s50 offen offset:0 sc1\n\t" \
   "v_cvt_pk_bf16_f32 v84, v84, v85\n\t" "v_mfma_f32_32x32x16_bf16 v[64:79], v[100:103], a[240:243], v[64:79]\n\t" "v_fmac_f32 v48, v32, v8\n\t" \
   "v_fmac_f32 v49, v33, v8\n\t" "v_fmac_f32 v50, v34, v8\n\t" "v_fmac_f32 v51, v35, v8\n\t" "v_fmac_f32 v52, v36, v8\n\t" \
   "v_fmac_f32 v53, v37, v8\n\t" "v_mfma_f32_32x32x16_bf16 v[64:79], v[104:107], a[228:231], v[64:79]\n\t" "v_fmac_f32 v54, v38, v8\n\t" \
   "v_fmac_f32 v55, v39, v8\n\t" "v_fmac_f32 v56, v40, v8\n\t" "v_fmac_f32 v57, v41, v8\n\t" "v_fmac_f32 v58, v42, v8\n\t" \
   "v_fmac_f32 v59, v43, v8\n\t" "v_mfma_f32_32x32x16_bf16 v[64:79], v[108:111], a[244:247], v[64:79]\n\t" "v_fmac_f32 v60, v44, v8\n\t" \
   "v_fmac_f32 v61, v45, v8\n\t" "v_fmac_f32 v62, v46, v8\n\t" "v_fmac_f32 v63, v47, v8\n\t" "v_mul_f32 v48, v48, v10\n\t" \
   "v_mul_f32 v49, v49, v10\n\t" "v_mfma_f32_32x32x16_bf16 v[64:79], v[112:115], a[232:235], v[64:79]\n\t" "v_mul_f32 v50, v50, v10\n\t" \
   "v_mul_f32 v51, v51, v10\n\t" "v_mul_f32 v52, v52, v10\n\t" "v_mul_f32 v53, v53, v10\n\t" "v_mul_f32 v54, v54, v10\n\t" \
   "v_mul_f32 v55, v55, v10\n\t" "v_mfma_f32_32x32x16_bf16 v[64:79], v[116:119], a[248:251], v[64:79]\n\t" "v_mul_f32 v56, v56, v10\n\t" \
   "v_mul_f32 v57, v57, v10\n\t" "v_mul_f32 v58, v58, v10\n\t" "v_mul_f32 v59, v59, v10\n\t" "v_mul_f32 v60, v60, v10\n\t" \
   "v_mul_f32 v61, v61, v10\n\t" "v_mfma_f32_32x32x16_bf16 v[64:79], v[120:123], a[236:239], v[64:79]\n\t" "v_cvt_pk_bf16_f32 v85, v92, v93\n\t" \
   "v_cvt_pk_bf16_f32 v86, v86, v87\n\t" "v_cvt_pk_bf16_f32 v87, v94, v95\n\t" \
   "buffer_store_dwordx4 v[84:87], v21, %[rs], s50 offen offset:512 sc1\n\t" "v_mfma_f32_32x32x16_bf16 v[64:79], v[124:127], a[252:255], v[64:79]\n\t" \
   "v_mul_f32 v62, v62, v10\n\t" "v_mul_f32 v63, v63, v10\n\t" "v_cvt_pk_bf16_f32 v48, v48, v49\n\t" "v_cvt_pk_bf16_f32 v49, v50, v51\n\t" \
   "v_cvt_pk_bf16_f32 v50, v52, v53\n\t" "v_cvt_pk_bf16_f32 v51, v54, v55\n\t" "v_mfma_f32_32x32x16_bf16 v[80:95], v[96:99], v[192:195], 0\n\t" \
   "ds_write_b128 v14, v[48:51] offset:0\n\t" "v_cvt_pk_bf16_f32 v56, v56, v57\n\t" "v_cvt_pk_bf16_f32 v57, v58, v59\n\t" \
   "v_mfma_f32_32x32x16_bf16 v[80:95], v[100:103], v[208:211], v[80:95]\n\t" "v_cvt_pk_bf16_f32 v58, v60, v61\n\t" \
   "v_cvt_pk_bf16_f32 v59, v62, v63\n\t" "ds_write_b128 v14, v[56:59] offset:1024\n\t" "v_add_u32 v138, 1, v138\n\t" "ds_write_b32 v13, v138\n\t" \
   "v_mfma_f32_32x32x16_bf16 v[80:95], v[104:107], v[196:199], v[80:95]\n\t" "s_add_u32 s50, s49, 131072\n\t" "v_cvt_pk_bf16_f32 v64, v64, v65\n\t" \
   "v_cvt_pk_bf16_f32 v65, v72, v73\n\t" "v_cvt_pk_bf16_f32 v66, v66, v67\n\t" "v_cvt_pk_bf16_f32 v67, v74, v75\n\t" "s_add_u32 s45, s40, 3\n\t" \
   "v_mfma_f32_32x32x16_bf16 v[80:95], v[108:111], v[212:215], v[80:95]\n\t" "buffer_store_dwordx4 v[64:67], v21, %[rs], s50 offen offset:0 sc1\n\t" \
   "v_cvt_pk_bf16_f32 v68, v68, v69\n\t" "v_mfma_f32_32x32x16_bf16 v[80:95], v[112:115], v[200:203], v[80:95]\n\t" \
   "v_cvt_pk_bf16_f32 v69, v76, v77\n\t" "v_cvt_pk_bf16_f32 v70, v70, v71\n\t" "v_cvt_pk_bf16_f32 v71, v78, v79\n\t" "s_min_u32 s45, s45, 63\n\t" \
   "s_mov_b32 s46, s42\n\t" "v_mfma_f32_32x32x16_bf16 v[80:95], v[116:119], v[216:219], v[80:95]\n\t" \
   "buffer_store_dwordx4 v[68:71], v21, %[rs], s50 offen offset:512 sc1\n\t" "v_mfma_f32_32x32x16_bf16 v[80:95], v[120:123], v[204:207], v[80:95]\n\t" \
   "s_cmp_eq_u32 s44, -1\n\t" "s_cbranch_scc0 174f\n\t" "74:\n\t" "ds_read_b128 v[160:163], v28 offset:0\n\t" \
   "ds_read_b128 v[164:167], v28 offset:512\n\t" "v_mfma_f32_32x32x16_bf16 v[80:95], v[124:127], v[220:223], v[80:95]\n\t" "s_mov_b32 s42, s43\n\t" \
   "ds_read_b128 v[168:171], v28 offset:1024\n\t" "s_add_u32 s43, s43, 44032\n\t" "s_cmp_eq_u32 s43, 132096\n\t" "s_cselect_b32 s43, 0, s43\n\t" \
   "v_mfma_f32_32x32x16_bf16 v[140:155], v[96:99], v[224:227], 0\n\t" "ds_read_b128 v[172:175], v28 offset:1536\n\t" \
   "ds_read_b128 v[176:179], v28 offset:2048\n\t" "s_add_u32 s41, s41, 1\n\t" "s_add_u32 s40, s40, 1\n\t" "v_xor_b32 v14, 0x2000, v14\n\t" \
   "v_mfma_f32_32x32x16_bf16 v[140:155], v[100:103], v[240:243], v[140:155]\n\t" "s_add_u32 s50, s49, 196608\n\t" "v_cvt_pk_bf16_f32 v80, v80, v81\n\t" \
   "v_cvt_pk_bf16_f32 v81, v88, v89\n\t" "v_cvt_pk_bf16_f32 v82, v82, v83\n\t" "v_cvt_pk_bf16_f32 v83, v90, v91\n\t" "v_xor_b32 v15, 0x2000, v15\n\t" \
   "v_mfma_f32_32x32x16_bf16 v[140:155], v[104:107], v[228:231], v[140:155]\n\t" \
   "buffer_store_dwordx4 v[80:83], v21, %[rs], s50 offen offset:0 sc1\n\t" "v_cvt_pk_bf16_f32 v84, v84, v85\n\t" \
   "v_mfma_f32_32x32x16_bf16 v[140:155], v[108:111], v[244:247], v[140:155]\n\t" "v_cvt_pk_bf16_f32 v85, v92, v93\n\t" \
   "v_cvt_pk_bf16_f32 v86, v86, v87\n\t" "v_cvt_pk_bf16_f32 v87, v94, v95\n\t" "ds_read_b128 v[180:183], v28 offset:2560\n\t" \
   "ds_read_b128 v[184:187], v28 offset:3072\n\t" "v_mfma_f32_32x32x16_bf16 v[140:155], v[112:115], v[232:235], v[140:155]\n\t" \
   "buffer_store_dwordx4 v[84:87], v21, %[rs], s50 offen offset:512 sc1\n\t" "v_mov_b32 v18, v21\n\t" \
   "v_mfma_f32_32x32x16_bf16 v[140:155], v[116:119], v[248:251], v[140:155]\n\t" \
   "v_mfma_f32_32x32x16_bf16 v[140:155], v[120:123], v[236:239], v[140:155]\n\t" \
   "v_mfma_f32_32x32x16_bf16 v[140:155], v[124:127], v[252:255], v[140:155]\n\t" "s_cmp_lt_u32 s40, 64\n\t" "s_cbranch_scc1 1b\n\t" "s_nop 7\n\t" \
   "s_nop 2\n\t" "s_waitcnt lgkmcnt(0)\n\t" "s_barrier\n\t" "s_add_u32 s50, s49, 262144\n\t" "v_cvt_pk_bf16_f32 v140, v140, v141\n\t" \
   "v_cvt_pk_bf16_f32 v141, v148, v149\n\t" "v_cvt_pk_bf16_f32 v142, v142, v143\n\t" "v_cvt_pk_bf16_f32 v143, v150, v151\n\t" \
   "buffer_store_dwordx4 v[140:143], v21, %[rs], s50 offen offset:0 sc1\n\t" "v_cvt_pk_bf16_f32 v144, v144, v145\n\t" \
   "v_cvt_pk_bf16_f32 v145, v152, v153\n\t" "v_cvt_pk_bf16_f32 v146, v146, v147\n\t" "v_cvt_pk_bf16_f32 v147, v154, v155\n\t" \
   "buffer_store_dwordx4 v[144:147], v21, %[rs], s50 offen offset:512 sc1\n\t" "s_sub_i32 s49, s41, 1\n\t" "s_mul_i32 s49, s49, 1310720\n\t" \
   "ds_read_b128 v[96:99], v15 offset:0\n\t" "ds_read_b128 v[100:103], v15 offset:1024\n\t" "ds_read_b128 v[104:107], v15 offset:2048\n\t" \
   "ds_read_b128 v[108:111], v15 offset:3072\n\t" "ds_read_b128 v[112:115], v15 offset:4096\n\t" "ds_read_b128 v[116:119], v15 offset:5120\n\t" \
   "ds_read_b128 v[120:123], v15 offset:6144\n\t" "ds_read_b128 v[124:127], v15 offset:7168\n\t" "s_waitcnt lgkmcnt(6)\n\t" \
   "v_mfma_f32_32x32x16_bf16 v[64:79], v[96:99], a[160:163], 0\n\t" "v_mfma_f32_32x32x16_bf16 v[64:79], v[100:103], a[176:179], v[64:79]\n\t" \
   "s_waitcnt lgkmcnt(4)\n\t" "v_mfma_f32_32x32x16_bf16 v[64:79], v[104:107], a[164:167], v[64:79]\n\t" \
   "v_mfma_f32_32x32x16_bf16 v[64:79], v[108:111], a[180:183], v[64:79]\n\t" "s_waitcnt lgkmcnt(2)\n\t" \
   "v_mfma_f32_32x32x16_bf16 v[64:79], v[112:115], a[168:171], v[64:79]\n\t" "v_mfma_f32_32x32x16_bf16 v[64:79], v[116:119], a[184:187], v[64:79]\n\t" \
   "s_waitcnt lgkmcnt(0)\n\t" "v_mfma_f32_32x32x16_bf16 v[64:79], v[120:123], a[172:175], v[64:79]\n\t" \
   "v_mfma_f32_32x32x16_bf16 v[64:79], v[124:127], a[188:191], v[64:79]\n\t" "v_mfma_f32_32x32x16_bf16 v[80:95], v[96:99], a[192:195], 0\n\t" \
   "v_mfma_f32_32x32x16_bf16 v[80:95], v[100:103], a[208:211], v[80:95]\n\t" "s_add_u32 s50, s49, 0\n\t" "v_cvt_pk_bf16_f32 v64, v64, v65\n\t" \
   "v_cvt_pk_bf16_f32 v65, v72, v73\n\t" "v_cvt_pk_bf16_f32 v66, v66, v67\n\t" "v_cvt_pk_bf16_f32 v67, v74, v75\n\t" \
   "v_mfma_f32_32x32x16_bf16 v[80:95], v[104:107], a[196:199], v[80:95]\n\t" "buffer_store_dwordx4 v[64:67], v21, %[rs], s50 offen offset:0 sc1\n\t" \
   "v_cvt_pk_bf16_f32 v68, v68, v69\n\t" "v_mfma_f32_32x32x16_bf16 v[80:95], v[108:111], a[212:215], v[80:95]\n\t" \
   "v_cvt_pk_bf16_f32 v69, v76, v77\n\t" "v_cvt_pk_bf16_f32 v70, v70, v71\n\t" "v_cvt_pk_bf16_f32 v71, v78, v79\n\t" \
   "v_mfma_f32_32x32x16_bf16 v[80:95], v[112:115], a[200:203], v[80:95]\n\t" "buffer_store_dwordx4 v[68:71], v21, %[rs], s50 offen offset:512 sc1\n\t" \
   "v_mfma_f32_32x32x16_bf16 v[80:95], v[116:119], a[216:219], v[80:95]\n\t" "v_mfma_f32_32x32x16_bf16 v[80:95], v[120:123], a[204:207], v[80:95]\n\t" \
   "v_mfma_f32_32x32x16_bf16 v[80:95], v[124:127], a[220:223], v[80:95]\n\t" "v_mfma_f32_32x32x16_bf16 v[64:79], v[96:99], a[224:227], 0\n\t" \
   "v_mfma_f32_32x32x16_bf16 v[64:79], v[100:103], a[240:243], v[64:79]\n\t" "s_add_u32 s50, s49, 65536\n\t" "v_cvt_pk_bf16_f32 v80, v80, v81\n\t" \
   "v_cvt_pk_bf16_f32 v81, v88, v89\n\t" "v_cvt_pk_bf16_f32 v82, v82, v83\n\t" "v_cvt_pk_bf16_f32 v83, v90, v91\n\t" \
   "v_mfma_f32_32x32x16_bf16 v[64:79], v[104:107], a[228:231], v[64:79]\n\t" "buffer_store_dwordx4 v[80:83], v21, %[rs], s50 offen offset:0 sc1\n\t" \
   "v_cvt_pk_bf16_f32 v84, v84, v85\n\t" "v_mfma_f32_32x32x16_bf16 v[64:79], v[108:111], a[244:247], v[64:79]\n\t" \
   "v_cvt_pk_bf16_f32 v85, v92, v93\n\t" "v_cvt_pk_bf16_f32 v86, v86, v87\n\t" "v_cvt_pk_bf16_f32 v87, v94, v95\n\t" \
   "v_mfma_f32_32x32x16_bf16 v[64:79], v[112:115], a[232:235], v[64:79]\n\t" "buffer_store_dwordx4 v[84:87], v21, %[rs], s50 offen offset:512 sc1\n\t" \
   "v_mfma_f32_32x32x16_bf16 v[64:79], v[116:119], a[248:251], v[64:79]\n\t" "v_mfma_f32_32x32x16_bf16 v[64:79], v[120:123], a[236:239], v[64:79]\n\t" \
   "v_mfma_f32_32x32x16_bf16 v[64:79], v[124:127], a[252:255], v[64:79]\n\t" "v_mfma_f32_32x32x16_bf16 v[80:95], v[96:99], v[192:195], 0\n\t" \
   "v_mfma_f32_32x32x16_bf16 v[80:95], v[100:103], v[208:211], v[80:95]\n\t" "s_add_u32 s50, s49, 131072\n\t" "v_cvt_pk_bf16_f32 v64, v64, v65\n\t" \
   "v_cvt_pk_bf16_f32 v65, v72, v73\n\t" "v_cvt_pk_bf16_f32 v66, v66, v67\n\t" "v_cvt_pk_bf16_f32 v67, v74, v75\n\t" \
   "v_mfma_f32_32x32x16_bf16 v[80:95], v[104:107], v[196:199], v[80:95]\n\t" "buffer_store_dwordx4 v[64:67], v21, %[rs], s50 offen offset:0 sc1\n\t" \
   "v_cvt_pk_bf16_f32 v68, v68, v69\n\t" "v_mfma_f32_32x32x16_bf16 v[80:95], v[108:111], v[212:215], v[80:95]\n\t" \
   "v_cvt_pk_bf16_f32 v69, v76, v77\n\t" "v_cvt_pk_bf16_f32 v70, v70, v71\n\t" "v_cvt_pk_bf16_f32 v71, v78, v79\n\t" \
   "v_mfma_f32_32x32x16_bf16 v[80:95], v[112:115], v[200:203], v[80:95]\n\t" "buffer_store_dwordx4 v[68:71], v21, %[rs], s50 offen offset:512 sc1\n\t" \
   "v_mfma_f32_32x32x16_bf16 v[80:95], v[116:119], v[216:219], v[80:95]\n\t" "v_mfma_f32_32x32x16_bf16 v[80:95], v[120:123], v[204:207], v[80:95]\n\t" \
   "v_mfma_f32_32x32x16_bf16 v[80:95], v[124:127], v[220:223], v[80:95]\n\t" "v_mfma_f32_32x32x16_bf16 v[140:155], v[96:99], v[224:227], 0\n\t" \
   "v_mfma_f32_32x32x16_bf16 v[140:155], v[100:103], v[240:243], v[140:155]\n\t" "s_add_u32 s50, s49, 196608\n\t" "v_cvt_pk_bf16_f32 v80, v80, v81\n\t" \
   "v_cvt_pk_bf16_f32 v81, v88, v89\n\t" "v_cvt_pk_bf16_f32 v82, v82, v83\n\t" "v_cvt_pk_bf16_f32 v83, v90, v91\n\t" \
   "v_mfma_f32_32x32x16_bf16 v[140:155], v[104:107], v[228:231], v[140:155]\n\t" \
   "buffer_store_dwordx4 v[80:83], v21, %[rs], s50 offen offset:0 sc1\n\t" "v_cvt_pk_bf16_f32 v84, v84, v85\n\t" \
   "v_mfma_f32_32x32x16_bf16 v[140:155], v[108:111], v[244:247], v[140:155]\n\t" "v_cvt_pk_bf16_f32 v85, v92, v93\n\t" \
   "v_cvt_pk_bf16_f32 v86, v86, v87\n\t" "v_cvt_pk_bf16_f32 v87, v94, v95\n\t" \
   "v_mfma_f32_32x32x16_bf16 v[140:155], v[112:115], v[232:235], v[140:155]\n\t" \
   "buffer_store_dwordx4 v[84:87], v21, %[rs], s50 offen offset:512 sc1\n\t" \
   "v_mfma_f32_32x32x16_bf16 v[140:155], v[116:119], v[248:251], v[140:155]\n\t" \
   "v_mfma_f32_32x32x16_bf16 v[140:155], v[120:123], v[236:239], v[140:155]\n\t" \
   "v_mfma_f32_32x32x16_bf16 v[140:155], v[124:127], v[252:255], v[140:155]\n\t" "s_nop 7\n\t" "s_nop 2\n\t" "s_add_u32 s50, s49, 262144\n\t" \
   "v_cvt_pk_bf16_f32 v140, v140, v141\n\t" "v_cvt_pk_bf16_f32 v141, v148, v149\n\t" "v_cvt_pk_bf16_f32 v142, v142, v143\n\t" \
   "v_cvt_pk_bf16_f32 v143, v150, v151\n\t" "buffer_store_dwordx4 v[140:143], v21, %[rs], s50 offen offset:0 sc1\n\t" \
   "v_cvt_pk_bf16_f32 v144, v144, v145\n\t" "v_cvt_pk_bf16_f32 v145, v152, v153\n\t" "v_cvt_pk_bf16_f32 v146, v146, v147\n\t" \
   "v_cvt_pk_bf16_f32 v147, v154, v155\n\t" "buffer_store_dwordx4 v[144:147], v21, %[rs], s50 offen offset:512 sc1\n\t" "s_waitcnt vmcnt(0)\n\t" \
   "s_cmp_lt_i32 s40, 2\n\t" "s_cselect_b32 s47, 0x7ffffff0, 0\n\t" "v_or_b32 v30, s47, v20\n\t" "s_sub_i32 s47, s41, 2\n\t" \
   "s_max_i32 s47, s47, 0\n\t" "s_lshl_b32 s47, s47, 12\n\t" "buffer_store_dword v31, v30, %[rs], s47 offen sc1\n\t" "s_cmp_lt_i32 s40, 1\n\t" \
   "s_cselect_b32 s47, 0x7ffffff0, 0\n\t" "v_or_b32 v30, s47, v20\n\t" "s_sub_i32 s47, s41, 1\n\t" "s_max_i32 s47, s47, 0\n\t" \
   "s_lshl_b32 s47, s47, 12\n\t" "buffer_store_dword v31, v30, %[rs], s47 offen sc1\n\t" "s_branch 9f\n\t" "172:\n\t" "v_cmp_lt_i32 vcc, 6, v25\n\t" \
   "s_lshr_b32 s48, s45, 1\n\t" "s_or_b32 s44, s44, vcc_lo\n\t" "s_cmp_lt_u32 s40, 4\n\t" "s_cbranch_scc1 272f\n\t" \
   "buffer_load_dword v25, v19, %[rs], 0 offen sc1\n\t" "272:\n\t" "s_bitcmp1_b32 s44, s48\n\t" "s_cbranch_scc1 72b\n\t" "s_mov_b32 s47, 0\n\t" \
   "buffer_load_dword v30, v19, %[rs], 0 offen sc1\n\t" "s_waitcnt vmcnt(0)\n\t" "v_add_u32 v30, 1, v30\n\t" "v_cmp_lt_u32 vcc, 7, v30\n\t" \
   "s_or_b32 s44, s44, vcc_lo\n\t" "s_bitcmp1_b32 s44, s48\n\t" "s_cbranch_scc1 72b\n\t" "s_bitset1_b32 s44, s48\n\t" "s_lshl_b32 s48, s48, 7\n\t" \
   "s_add_u32 s48, s48, %[cnt]\n\t" "73:\n\t" "s_sleep 10\n\t" "buffer_load_dword v30, off, %[rs], s48 sc1\n\t" "s_add_u32 s47, s47, 1\n\t" \
   "s_waitcnt vmcnt(0)\n\t" "v_add_u32 v30, 1, v30\n\t" "v_cmp_lt_u32 vcc, 7, v30\n\t" "s_cmp_gt_u32 s47, 0x20000\n\t" "s_cbranch_scc1 72b\n\t" \
   "s_cbranch_vccz 73b\n\t" "s_branch 72b\n\t" "176:\n\t" "v_cmp_lt_i32 vcc, 6, v25\n\t" "s_lshr_b32 s48, s45, 1\n\t" "s_or_b32 s44, s44, vcc_lo\n\t" \
   "s_cmp_lt_u32 s40, 4\n\t" "s_cbranch_scc1 276f\n\t" "buffer_load_dword v25, v19, %[rs], 0 offen sc1\n\t" "276:\n\t" "s_bitcmp1_b32 s44, s48\n\t" \
   "s_cbranch_scc1 76b\n\t" "s_mov_b32 s47, 0\n\t" "buffer_load_dword v30, v19, %[rs], 0 offen sc1\n\t" "s_waitcnt vmcnt(0)\n\t" \
   "v_add_u32 v30, 1, v30\n\t" "v_cmp_lt_u32 vcc, 7, v30\n\t" "s_or_b32 s44, s44, vcc_lo\n\t" "s_bitcmp1_b32 s44, s48\n\t" "s_cbranch_scc1 76b\n\t" \
   "s_bitset1_b32 s44, s48\n\t" "s_lshl_b32 s48, s48, 7\n\t" "s_add_u32 s48, s48, %[cnt]\n\t" "77:\n\t" "s_sleep 10\n\t" \
   "buffer_load_dword v30, off, %[rs], s48 sc1\n\t" "s_add_u32 s47, s47, 1\n\t" "s_waitcnt vmcnt(0)\n\t" "v_add_u32 v30, 1, v30\n\t" \
   "v_cmp_lt_u32 vcc, 7, v30\n\t" "s_cmp_gt_u32 s47, 0x20000\n\t" "s_cbranch_scc1 76b\n\t" "s_cbranch_vccz 77b\n\t" "s_branch 76b\n\t" "174:\n\t" \
   "v_cmp_lt_i32 vcc, 6, v25\n\t" "s_lshr_b32 s48, s45, 1\n\t" "s_or_b32 s44, s44, vcc_lo\n\t" "s_cmp_lt_u32 s40, 4\n\t" "s_cbranch_scc1 274f\n\t" \
   "buffer_load_dword v25, v19, %[rs], 0 offen sc1\n\t" "274:\n\t" "s_bitcmp1_b32 s44, s48\n\t" "s_cbranch_scc1 74b\n\t" "s_mov_b32 s47, 0\n\t" \
   "buffer_load_dword v30, v19, %[rs], 0 offen sc1\n\t" "s_waitcnt vmcnt(0)\n\t" "v_add_u32 v30, 1, v30\n\t" "v_cmp_lt_u32 vcc, 7, v30\n\t" \
   "s_or_b32 s44, s44, vcc_lo\n\t" "s_bitcmp1_b32 s44, s48\n\t" "s_cbranch_scc1 74b\n\t" "s_bitset1_b32 s44, s48\n\t" "s_lshl_b32 s48, s48, 7\n\t" \
   "s_add_u32 s48, s48, %[cnt]\n\t" "75:\n\t" "s_sleep 10\n\t" "buffer_load_dword v30, off, %[rs], s48 sc1\n\t" "s_add_u32 s47, s47, 1\n\t" \
   "s_waitcnt vmcnt(0)\n\t" "v_add_u32 v30, 1, v30\n\t" "v_cmp_lt_u32 vcc, 7, v30\n\t" "s_cmp_gt_u32 s47, 0x20000\n\t" "s_cbranch_scc1 74b\n\t" \
   "s_cbranch_vccz 75b\n\t" "s_branch 74b\n\t" "150:\n\t" "ds_read_b128 v[156:159], v12\n\t" "s_waitcnt lgkmcnt(0)\n\t" \
   "v_min3_u32 v156, v156, v157, v158\n\t" "v_min_u32 v156, v156, v159\n\t" "v_cmp_ge_u32 vcc, v156, v138\n\t" "s_cbranch_vccz 150b\n\t" \
   "s_branch 50b\n\t" "152:\n\t" "ds_read_b128 v[156:159], v12\n\t" "s_waitcnt lgkmcnt(0)\n\t" "v_min3_u32 v156, v156, v157, v158\n\t" \
   "v_min_u32 v156, v156, v159\n\t" "v_cmp_ge_u32 vcc, v156, v138\n\t" "s_cbranch_vccz 152b\n\t" "s_branch 52b\n\t" "9:\n\t" "s_waitcnt vmcnt(0)"
#define VU_CLOB "v8", "v9", "v10", "v11", "v12", "v13", "v14", "v15", "v32", "v33", "v34", "v35", "v36", "v37", "v38", "v39", "v40", "v41", "v42", "v43", "v44", "v45", "v46", "v47", "v48", "v49", "v50", "v51", "v52", "v53", "v54", "v55", "v56", "v57", "v58", "v59", "v60", "v61", "v62", "v63", "v64", "v65", "v66", "v67", "v68", "v69", "v70", "v71", "v72", "v73", "v74", "v75", "v76", "v77", "v78", "v79", "v80", "v81", "v82", "v83", "v84", "v85", "v86", "v87", "v88", "v89", "v90", "v91", "v92", "v93", "v94", "v95", "v96", "v97", "v98", "v99", "v100", "v101", "v102", "v103", "v104", "v105", "v106", "v107", "v108", "v109", "v110", "v111", "v112", "v113", "v114", "v115", "v116", "v117", "v118", "v119", "v120", "v121", "v122", "v123", "v124", "v125", "v126", "v127", "v128", "v129", "v130", "v131", "v132", "v133", "v134", "v135", "v136", "v137", "v138", "v139", "v140", "v141", "v142", "v143", "v144", "v145", "v146", "v147", "v148", "v149", "v150", "v151", "v152", "v153", "v154", "v155", "v156", "v157", "v158", "v159", "v160", "v161", "v162", "v163", "v164", "v165", "v166", "v167", "v168", "v169", "v170", "v171", "v172", "v173", "v174", "v175", "v176", "v177", "v178", "v179", "v180", "v181", "v182", "v183", "v184", "v185", "v186", "v187", "v188", "v189", "v190", "v191", "s40", "s41", "s42", "s43", "s44", "s45", "s46", "s47", "s48", "s49", "s50", "s51", "s52", "s53", "s54", "s55"
#define DF_ASM \
   "v_mov_b32 v80, 0x3f803f80\n\t" "v_mov_b32 v81, 0x3f803f80\n\t" "v_mov_b32 v82, 0x3f803f80\n\t" "v_mov_b32 v83, 0x3f803f80\n\t" \
   "s_mov_b32 s45, %[set]\n\t" "s_mov_b32 s46, 0\n\t" "v_mfma_f32_16x16x32_bf16 v[32:35], a[0:3], v[80:83], 0\n\t" \
   "v_mfma_f32_16x16x32_bf16 v[32:35], a[4:7], v[80:83], v[32:35]\n\t" "v_mfma_f32_16x16x32_bf16 v[32:35], a[8:11], v[80:83], v[32:35]\n\t" \
   "v_mfma_f32_16x16x32_bf16 v[32:35], a[12:15], v[80:83], v[32:35]\n\t" "v_mfma_f32_16x16x32_bf16 v[32:35], a[16:19], v[80:83], v[32:35]\n\t" \
   "v_mfma_f32_16x16x32_bf16 v[32:35], a[20:23], v[80:83], v[32:35]\n\t" "v_mfma_f32_16x16x32_bf16 v[32:35], a[24:27], v[80:83], v[32:35]\n\t" \
   "v_mfma_f32_16x16x32_bf16 v[32:35], a[28:31], v[80:83], v[32:35]\n\t" "v_mfma_f32_16x16x32_bf16 v[32:35], a[32:35], v[80:83], v[32:35]\n\t" \
   "v_mfma_f32_16x16x32_bf16 v[32:35], a[36:39], v[80:83], v[32:35]\n\t" "v_mfma_f32_16x16x32_bf16 v[32:35], a[40:43], v[80:83], v[32:35]\n\t" \
   "v_mfma_f32_16x16x32_bf16 v[32:35], a[44:47], v[80:83], v[32:35]\n\t" "v_mfma_f32_16x16x32_bf16 v[32:35], a[48:51], v[80:83], v[32:35]\n\t" \
   "v_mfma_f32_16x16x32_bf16 v[32:35], a[52:55], v[80:83], v[32:35]\n\t" "v_mfma_f32_16x16x32_bf16 v[32:35], a[56:59], v[80:83], v[32:35]\n\t" \
   "v_mfma_f32_16x16x32_bf16 v[32:35], a[60:63], v[80:83], v[32:35]\n\t" "v_mfma_f32_16x16x32_bf16 v[32:35], a[64:67], v[80:83], v[32:35]\n\t" \
   "v_mfma_f32_16x16x32_bf16 v[32:35], a[68:71], v[80:83], v[32:35]\n\t" "v_mfma_f32_16x16x32_bf16 v[32:35], a[72:75], v[80:83], v[32:35]\n\t" \
   "v_mfma_f32_16x16x32_bf16 v[32:35], a[76:79], v[80:83], v[32:35]\n\t" "v_mfma_f32_16x16x32_bf16 v[36:39], a[80:83], v[80:83], 0\n\t" \
   "v_mfma_f32_16x16x32_bf16 v[36:39], a[84:87], v[80:83], v[36:39]\n\t" "v_mfma_f32_16x16x32_bf16 v[36:39], a[88:91], v[80:83], v[36:39]\n\t" \
   "v_mfma_f32_16x16x32_bf16 v[36:39], a[92:95], v[80:83], v[36:39]\n\t" "v_mfma_f32_16x16x32_bf16 v[36:39], a[96:99], v[80:83], v[36:39]\n\t" \
   "v_mfma_f32_16x16x32_bf16 v[36:39], a[100:103], v[80:83], v[36:39]\n\t" "v_mfma_f32_16x16x32_bf16 v[36:39], a[104:107], v[80:83], v[36:39]\n\t" \
   "v_mfma_f32_16x16x32_bf16 v[36:39], a[108:111], v[80:83], v[36:39]\n\t" "v_mfma_f32_16x16x32_bf16 v[36:39], a[112:115], v[80:83], v[36:39]\n\t" \
   "v_mfma_f32_16x16x32_bf16 v[36:39], a[116:119], v[80:83], v[36:39]\n\t" "v_mfma_f32_16x16x32_bf16 v[36:39], a[120:123], v[80:83], v[36:39]\n\t" \
   "v_mfma_f32_16x16x32_bf16 v[36:39], a[124:127], v[80:83], v[36:39]\n\t" "v_mfma_f32_16x16x32_bf16 v[36:39], a[128:131], v[80:83], v[36:39]\n\t" \
   "v_mfma_f32_16x16x32_bf16 v[36:39], a[132:135], v[80:83], v[36:39]\n\t" "v_mfma_f32_16x16x32_bf16 v[36:39], a[136:139], v[80:83], v[36:39]\n\t" \
   "v_mfma_f32_16x16x32_bf16 v[36:39], a[140:143], v[80:83], v[36:39]\n\t" "v_mfma_f32_16x16x32_bf16 v[36:39], a[144:147], v[80:83], v[36:39]\n\t" \
   "v_mfma_f32_16x16x32_bf16 v[36:39], a[148:151], v[80:83], v[36:39]\n\t" "v_mfma_f32_16x16x32_bf16 v[36:39], a[152:155], v[80:83], v[36:39]\n\t" \
   "v_mfma_f32_16x16x32_bf16 v[36:39], a[156:159], v[80:83], v[36:39]\n\t" "s_add_u32 s48, s45, 60\n\t" "s_lshl_b32 s48, s48, 8\n\t" \
   "s_add_u32 s47, s48, %[xs4]\n\t" "v_mov_b32 v64, -1\n\t" "v_mov_b32 v65, -1\n\t" "v_mov_b32 v72, 0x7ffffff0\n\t" \
   "buffer_store_dwordx2 v[64:65], v21, %[rs], s48 offen offset:0 sc1\n\t" "buffer_store_dwordx2 v[64:65], v21, %[rs], s47 offen offset:0 sc1\n\t" \
   "buffer_store_dwordx2 v[64:65], v21, %[rs], s48 offen offset:128 sc1\n\t" "buffer_store_dwordx2 v[64:65], v21, %[rs], s47 offen offset:128 sc1\n\t" \
   "1:\n\t" "s_cmp_lt_u32 s45, 61\n\t" "s_cselect_b32 s40, 10, 160\n\t" "s_cselect_b64 s[42:43], 0, -1\n\t" "5:\n\t" "s_lshl_b32 s48, s45, 7\n\t" \
   "s_mov_b32 s47, 0\n\t" "s_mov_b32 s41, 0\n\t" "s_cmp_lg_u32 %[ldx], %[ld0]\n\t" "s_cbranch_scc1 3f\n\t" "2:\n\t" \
   "buffer_load_dwordx2 v[64:65], v19, %[rs], s48 offen sc1\n\t" "s_add_u32 s47, s47, 1\n\t" "s_waitcnt vmcnt(0)\n\t" \
   "v_readfirstlane_b32 s44, v64\n\t" "v_readfirstlane_b32 s49, v65\n\t" "s_add_u32 s44, s44, 1\n\t" "s_cmp_ge_u32 s44, s40\n\t" \
   "s_cbranch_scc1 6f\n\t" "s_cmp_gt_u32 s47, 0x20000\n\t" "s_cbranch_scc1 3f\n\t" "s_cmp_lt_u32 s45, 56\n\t" "s_cbranch_scc1 4f\n\t" "s_sleep 3\n\t" \
   "s_cmp_lg_u32 s44, 0\n\t" "s_cbranch_scc1 2b\n\t" "s_sleep 8\n\t" "s_branch 2b\n\t" "4:\n\t" "s_sleep 60\n\t" "s_branch 2b\n\t" "6:\n\t" \
   "s_cmp_eq_u32 s40, 160\n\t" "s_cbranch_scc0 3f\n\t" "7:\n\t" "s_memrealtime s[54:55]\n\t" "s_waitcnt lgkmcnt(0)\n\t" "s_sub_u32 s50, s54, s49\n\t" \
   "s_and_b32 s50, s50, 0x7fffffff\n\t" "s_cmp_lt_u32 s50, %[tg]\n\t" "s_cbranch_scc1 7b\n\t" "3:\n\t" "s_waitcnt vmcnt(0)\n\t" "s_barrier\n\t" \
   "s_memrealtime s[54:55]\n\t" "s_mul_i32 s52, s45, 1536\n\t" "s_mul_i32 s51, s45, 40960\n\t" "s_add_u32 m0, s46, %[ldx]\n\t" "s_mov_b32 s53, s46\n\t" \
   "buffer_load_dwordx4 v16, %[rs], s51 offen offset:0 sc1 lds\n\t" "buffer_load_dwordx4 v16, %[rs], s51 offen offset:1024 sc1 lds\n\t" \
   "buffer_load_dwordx4 v16, %[rs], s51 offen offset:2048 sc1 lds\n\t" "buffer_load_dwordx4 v16, %[rs], s51 offen offset:3072 sc1 lds\n\t" \
   "s_add_u32 m0, m0, 4096\n\t" "s_add_u32 s51, s51, 4096\n\t" "buffer_load_dwordx4 v16, %[rs], s51 offen offset:0 sc1 lds\n\t" \
   "buffer_load_dwordx4 v16, %[rs], s51 offen offset:1024 sc1 lds\n\t" "buffer_load_dwordx4 v16, %[rs], s51 offen offset:2048 sc1 lds\n\t" \
   "buffer_load_dwordx4 v16, %[rs], s51 offen offset:3072 sc1 lds\n\t" "s_add_u32 m0, m0, 4096\n\t" "s_add_u32 s51, s51, 4096\n\t" \
   "buffer_load_dwordx4 v16, %[rs], s51 offen offset:0 sc1 lds\n\t" "buffer_load_dwordx4 v16, %[rs], s51 offen offset:1024 sc1 lds\n\t" \
   "s_add_u32 m0, s53, %[ld1]\n\t" "s_nop 0\n\t" "buffer_load_dwordx4 v17, %[rs], s52 offen sc1 lds\n\t" "s_cmp_lt_u32 s45, 60\n\t" \
   "s_cselect_b32 s47, 0x7ffffff0, 0\n\t" "v_or_b32 v30, s47, v20\n\t" "s_waitcnt lgkmcnt(0)\n\t" "v_mov_b32 v31, s54\n\t" "v_min_u32 v31, -2, v31\n\t" \
   "buffer_store_dword v31, v30, %[rs], 0 offen sc1\n\t" "v_add_u32 v26, s46, v22\n\t" "v_add_u32 v27, s46, v23\n\t" "s_waitcnt vmcnt(0)\n\t" \
   "s_barrier\n\t" "ds_read_b128 v[80:83], v26 offset:0\n\t" "ds_read_b128 v[84:87], v26 offset:1024\n\t" "ds_read_b128 v[88:91], v26 offset:2048\n\t" \
   "ds_read_b128 v[92:95], v26 offset:3072\n\t" "ds_read_b128 v[96:99], v26 offset:4096\n\t" "ds_read_b128 v[100:103], v26 offset:5120\n\t" \
   "ds_read_b128 v[104:107], v26 offset:6144\n\t" "s_waitcnt lgkmcnt(5)\n\t" "v_mfma_f32_16x16x32_bf16 v[52:55], a[80:83], v[80:83], 0\n\t" \
   "ds_read_b128 v[112:115], v27 offset:0\n\t" "v_mfma_f32_16x16x32_bf16 v[48:51], a[0:3], v[80:83], 0\n\t" \
   "ds_read_b128 v[108:111], v26 offset:7168\n\t" "ds_read_b128 v[116:119], v27 offset:16\n\t" \
   "v_mfma_f32_16x16x32_bf16 v[52:55], a[84:87], v[84:87], v[52:55]\n\t" "ds_read_b64 v[120:121], v27 offset:32\n\t" \
   "v_mfma_f32_16x16x32_bf16 v[48:51], a[4:7], v[84:87], v[48:51]\n\t" "ds_read_b128 v[80:83], v26 offset:8192\n\t" "s_waitcnt lgkmcnt(8)\n\t" \
   "v_mfma_f32_16x16x32_bf16 v[52:55], a[88:91], v[88:91], v[52:55]\n\t" "ds_read_b128 v[122:125], v27 offset:768\n\t" \
   "v_mfma_f32_16x16x32_bf16 v[48:51], a[8:11], v[88:91], v[48:51]\n\t" "ds_read_b128 v[84:87], v26 offset:9216\n\t" \
   "ds_read_b128 v[126:129], v27 offset:784\n\t" "v_mfma_f32_16x16x32_bf16 v[52:55], a[92:95], v[92:95], v[52:55]\n\t" \
   "v_mfma_f32_16x16x32_bf16 v[48:51], a[12:15], v[92:95], v[48:51]\n\t" "ds_read_b128 v[88:91], v26 offset:10240\n\t" \
   "ds_read_b64 v[130:131], v27 offset:800\n\t" "s_waitcnt lgkmcnt(11)\n\t" "v_mfma_f32_16x16x32_bf16 v[52:55], a[96:99], v[96:99], v[52:55]\n\t" \
   "s_lshl_b32 s48, s45, 8\n\t" "v_mfma_f32_16x16x32_bf16 v[48:51], a[16:19], v[96:99], v[48:51]\n\t" "ds_read_b128 v[92:95], v26 offset:11264\n\t" \
   "s_add_u32 s47, s48, %[xs4]\n\t" "v_mfma_f32_16x16x32_bf16 v[52:55], a[100:103], v[100:103], v[52:55]\n\t" \
   "v_mfma_f32_16x16x32_bf16 v[48:51], a[20:23], v[100:103], v[48:51]\n\t" "ds_read_b128 v[96:99], v26 offset:12288\n\t" "s_waitcnt lgkmcnt(8)\n\t" \
   "v_add_f32 v112, v112, v114\n\t" "v_mfma_f32_16x16x32_bf16 v[52:55], a[104:107], v[104:107], v[52:55]\n\t" "v_add_f32 v113, v113, v115\n\t" \
   "v_mfma_f32_16x16x32_bf16 v[48:51], a[24:27], v[104:107], v[48:51]\n\t" "ds_read_b128 v[100:103], v26 offset:13312\n\t" \
   "v_mfma_f32_16x16x32_bf16 v[52:55], a[108:111], v[108:111], v[52:55]\n\t" "v_add_f32 v116, v116, v118\n\t" \
   "v_mfma_f32_16x16x32_bf16 v[48:51], a[28:31], v[108:111], v[48:51]\n\t" "ds_read_b128 v[104:107], v26 offset:14336\n\t" \
   "v_add_f32 v117, v117, v119\n\t" "s_waitcnt lgkmcnt(7)\n\t" "v_mfma_f32_16x16x32_bf16 v[52:55], a[112:115], v[80:83], v[52:55]\n\t" \
   "v_mfma_f32_16x16x32_bf16 v[48:51], a[32:35], v[80:83], v[48:51]\n\t" "ds_read_b128 v[108:111], v26 offset:15360\n\t" \
   "v_add_f32 v112, v112, v120\n\t" "v_mfma_f32_16x16x32_bf16 v[52:55], a[116:119], v[84:87], v[52:55]\n\t" "v_add_f32 v113, v113, v121\n\t" \
   "v_mfma_f32_16x16x32_bf16 v[48:51], a[36:39], v[84:87], v[48:51]\n\t" "ds_read_b128 v[80:83], v26 offset:16384\n\t" "v_add_f32 v112, v112, v116\n\t" \
   "s_waitcnt lgkmcnt(5)\n\t" "v_mfma_f32_16x16x32_bf16 v[52:55], a[120:123], v[88:91], v[52:55]\n\t" \
   "v_mfma_f32_16x16x32_bf16 v[48:51], a[40:43], v[88:91], v[48:51]\n\t" "ds_read_b128 v[84:87], v26 offset:17408\n\t" "v_add_f32 v113, v113, v117\n\t" \
   "v_mfma_f32_16x16x32_bf16 v[52:55], a[124:127], v[92:95], v[52:55]\n\t" "v_mul_f32 v112, 0xbacccccd, v112\n\t" \
   "v_mfma_f32_16x16x32_bf16 v[48:51], a[44:47], v[92:95], v[48:51]\n\t" "ds_read_b128 v[88:91], v26 offset:18432\n\t" "s_waitcnt lgkmcnt(5)\n\t" \
   "v_mfma_f32_16x16x32_bf16 v[52:55], a[128:131], v[96:99], v[52:55]\n\t" "v_mul_f32 v113, 0x3acccccd, v113\n\t" \
   "v_mfma_f32_16x16x32_bf16 v[48:51], a[48:51], v[96:99], v[48:51]\n\t" "ds_read_b128 v[92:95], v26 offset:19456\n\t" \
   "v_fma_f32 v113, -v112, v112, v113\n\t" "v_mfma_f32_16x16x32_bf16 v[52:55], a[132:135], v[100:103], v[52:55]\n\t" "v_max_f32 v113, 0, v113\n\t" \
   "v_mfma_f32_16x16x32_bf16 v[48:51], a[52:55], v[100:103], v[48:51]\n\t" "ds_read_b128 v[96:99], v26 offset:20480\n\t" "s_waitcnt lgkmcnt(5)\n\t" \
   "v_mfma_f32_16x16x32_bf16 v[52:55], a[136:139], v[104:107], v[52:55]\n\t" "v_cvt_pk_bf16_f32 v113, v113, v112\n\t" \
   "v_mfma_f32_16x16x32_bf16 v[48:51], a[56:59], v[104:107], v[48:51]\n\t" "ds_read_b128 v[100:103], v26 offset:21504\n\t" \
   "v_and_b32 v8, 0xffff0000, v113\n\t" "v_mfma_f32_16x16x32_bf16 v[52:55], a[140:143], v[108:111], v[52:55]\n\t" \
   "v_mfma_f32_16x16x32_bf16 v[48:51], a[60:63], v[108:111], v[48:51]\n\t" "ds_read_b128 v[104:107], v26 offset:22528\n\t" \
   "v_lshlrev_b32 v113, 16, v113\n\t" "s_waitcnt lgkmcnt(5)\n\t" "v_mfma_f32_16x16x32_bf16 v[52:55], a[144:147], v[80:83], v[52:55]\n\t" \
   "v_add_f32 v113, 0x3727c5ac, v113\n\t" "v_mfma_f32_16x16x32_bf16 v[48:51], a[64:67], v[80:83], v[48:51]\n\t" \
   "ds_read_b128 v[108:111], v26 offset:23552\n\t" "v_mfma_f32_16x16x32_bf16 v[52:55], a[148:151], v[84:87], v[52:55]\n\t" \
   "v_cvt_pk_bf16_f32 v113, v113, v113\n\t" "v_mfma_f32_16x16x32_bf16 v[48:51], a[68:71], v[84:87], v[48:51]\n\t" \
   "ds_read_b128 v[80:83], v26 offset:24576\n\t" "v_lshlrev_b32 v113, 16, v113\n\t" "s_waitcnt lgkmcnt(5)\n\t" \
   "v_mfma_f32_16x16x32_bf16 v[52:55], a[152:155], v[88:91], v[52:55]\n\t" "v_sqrt_f32 v113, v113\n\t" \
   "v_mfma_f32_16x16x32_bf16 v[48:51], a[72:75], v[88:91], v[48:51]\n\t" "ds_read_b128 v[84:87], v26 offset:25600\n\t" \
   "v_mfma_f32_16x16x32_bf16 v[52:55], a[156:159], v[92:95], v[52:55]\n\t" "s_nop 0\n\t" \
   "v_mfma_f32_16x16x32_bf16 v[48:51], a[76:79], v[92:95], v[48:51]\n\t" "ds_read_b128 v[88:91], v26 offset:26624\n\t" \
   "v_cvt_pk_bf16_f32 v113, v113, v113\n\t" "s_waitcnt lgkmcnt(5)\n\t" "v_mfma_f32_16x16x32_bf16 v[60:63], a[80:83], v[96:99], 0\n\t" \
   "v_mfma_f32_16x16x32_bf16 v[56:59], a[0:3], v[96:99], 0\n\t" "ds_read_b128 v[92:95], v26 offset:27648\n\t" "v_lshlrev_b32 v113, 16, v113\n\t" \
   "v_mfma_f32_16x16x32_bf16 v[60:63], a[84:87], v[100:103], v[60:63]\n\t" "v_rcp_f32 v10, v113\n\t" \
   "v_mfma_f32_16x16x32_bf16 v[56:59], a[4:7], v[100:103], v[56:59]\n\t" "ds_read_b128 v[96:99], v26 offset:28672\n\t" "s_waitcnt lgkmcnt(5)\n\t" \
   "v_mfma_f32_16x16x32_bf16 v[60:63], a[88:91], v[104:107], v[60:63]\n\t" "v_fmac_f32 v48, v32, v8\n\t" "v_fmac_f32 v49, v33, v8\n\t" \
   "v_mfma_f32_16x16x32_bf16 v[56:59], a[8:11], v[104:107], v[56:59]\n\t" "ds_read_b128 v[100:103], v26 offset:29696\n\t" "v_fmac_f32 v50, v34, v8\n\t" \
   "v_mfma_f32_16x16x32_bf16 v[60:63], a[92:95], v[108:111], v[60:63]\n\t" "v_fmac_f32 v51, v35, v8\n\t" \
   "v_mfma_f32_16x16x32_bf16 v[56:59], a[12:15], v[108:111], v[56:59]\n\t" "ds_read_b128 v[104:107], v26 offset:30720\n\t" \
   "v_fmac_f32 v52, v36, v8\n\t" "v_fmac_f32 v53, v37, v8\n\t" "s_waitcnt lgkmcnt(5)\n\t" \
   "v_mfma_f32_16x16x32_bf16 v[60:63], a[96:99], v[80:83], v[60:63]\n\t" "v_fmac_f32 v54, v38, v8\n\t" \
   "v_mfma_f32_16x16x32_bf16 v[56:59], a[16:19], v[80:83], v[56:59]\n\t" "ds_read_b128 v[108:111], v26 offset:31744\n\t" "v_fmac_f32 v55, v39, v8\n\t" \
   "v_mfma_f32_16x16x32_bf16 v[60:63], a[100:103], v[84:87], v[60:63]\n\t" "v_mul_f32 v48, v48, v10\n\t" \
   "v_mfma_f32_16x16x32_bf16 v[56:59], a[20:23], v[84:87], v[56:59]\n\t" "ds_read_b128 v[80:83], v26 offset:32768\n\t" "v_mul_f32 v49, v49, v10\n\t" \
   "v_mul_f32 v50, v50, v10\n\t" "s_waitcnt lgkmcnt(5)\n\t" "v_mfma_f32_16x16x32_bf16 v[60:63], a[104:107], v[88:91], v[60:63]\n\t" \
   "v_mul_f32 v51, v51, v10\n\t" "v_mfma_f32_16x16x32_bf16 v[56:59], a[24:27], v[88:91], v[56:59]\n\t" "ds_read_b128 v[84:87], v26 offset:33792\n\t" \
   "v_mul_f32 v52, v52, v10\n\t" "v_mfma_f32_16x16x32_bf16 v[60:63], a[108:111], v[92:95], v[60:63]\n\t" "v_mul_f32 v53, v53, v10\n\t" \
   "v_mul_f32 v54, v54, v10\n\t" "v_mfma_f32_16x16x32_bf16 v[56:59], a[28:31], v[92:95], v[56:59]\n\t" "ds_read_b128 v[88:91], v26 offset:34816\n\t" \
   "v_mul_f32 v55, v55, v10\n\t" "s_waitcnt lgkmcnt(5)\n\t" "v_mfma_f32_16x16x32_bf16 v[60:63], a[112:115], v[96:99], v[60:63]\n\t" \
   "v_cmp_u_f32 vcc, v48, v48\n\t" "s_and_b64 vcc, vcc, s[42:43]\n\t" "s_or_b32 s41, s41, vcc_lo\n\t" "v_cndmask_b32 v30, v21, v72, vcc\n\t" \
   "v_mfma_f32_16x16x32_bf16 v[56:59], a[32:35], v[96:99], v[56:59]\n\t" "ds_read_b128 v[92:95], v26 offset:35840\n\t" \
   "v_cvt_pk_bf16_f32 v64, v48, v49\n\t" "v_mfma_f32_16x16x32_bf16 v[60:63], a[116:119], v[100:103], v[60:63]\n\t" \
   "v_cvt_pk_bf16_f32 v65, v50, v51\n\t" "v_cvt_pk_bf16_f32 v66, v52, v53\n\t" "v_mfma_f32_16x16x32_bf16 v[56:59], a[36:39], v[100:103], v[56:59]\n\t" \
   "ds_read_b128 v[96:99], v26 offset:36864\n\t" "v_cvt_pk_bf16_f32 v67, v54, v55\n\t" "s_waitcnt lgkmcnt(5)\n\t" \
   "v_mfma_f32_16x16x32_bf16 v[60:63], a[120:123], v[104:107], v[60:63]\n\t" "buffer_store_dwordx2 v[64:65], v30, %[rs], s48 offen offset:0 sc1\n\t" \
   "v_mfma_f32_16x16x32_bf16 v[56:59], a[40:43], v[104:107], v[56:59]\n\t" "ds_read_b128 v[100:103], v26 offset:37888\n\t" \
   "buffer_store_dwordx2 v[66:67], v30, %[rs], s47 offen offset:0 sc1\n\t" "v_mfma_f32_16x16x32_bf16 v[60:63], a[124:127], v[108:111], v[60:63]\n\t" \
   "v_add_f32 v122, v122, v124\n\t" "v_add_f32 v123, v123, v125\n\t" "v_mfma_f32_16x16x32_bf16 v[56:59], a[44:47], v[108:111], v[56:59]\n\t" \
   "ds_read_b128 v[104:107], v26 offset:38912\n\t" "v_add_f32 v126, v126, v128\n\t" "s_waitcnt lgkmcnt(5)\n\t" \
   "v_mfma_f32_16x16x32_bf16 v[60:63], a[128:131], v[80:83], v[60:63]\n\t" "v_add_f32 v127, v127, v129\n\t" \
   "v_mfma_f32_16x16x32_bf16 v[56:59], a[48:51], v[80:83], v[56:59]\n\t" "ds_read_b128 v[108:111], v26 offset:39936\n\t" \
   "v_add_f32 v122, v122, v130\n\t" "v_add_f32 v123, v123, v131\n\t" "v_mfma_f32_16x16x32_bf16 v[60:63], a[132:135], v[84:87], v[60:63]\n\t" \
   "v_add_f32 v122, v122, v126\n\t" "v_mfma_f32_16x16x32_bf16 v[56:59], a[52:55], v[84:87], v[56:59]\n\t" "v_add_f32 v123, v123, v127\n\t" \
   "s_waitcnt lgkmcnt(4)\n\t" "v_mfma_f32_16x16x32_bf16 v[60:63], a[136:139], v[88:91], v[60:63]\n\t" "v_mul_f32 v122, 0xbacccccd, v122\n\t" \
   "v_mfma_f32_16x16x32_bf16 v[56:59], a[56:59], v[88:91], v[56:59]\n\t" "v_mul_f32 v123, 0x3acccccd, v123\n\t" "v_fma_f32 v123, -v122, v122, v123\n\t" \
   "v_mfma_f32_16x16x32_bf16 v[60:63], a[140:143], v[92:95], v[60:63]\n\t" "v_max_f32 v123, 0, v123\n\t" \
   "v_mfma_f32_16x16x32_bf16 v[56:59], a[60:63], v[92:95], v[56:59]\n\t" "v_cvt_pk_bf16_f32 v123, v123, v122\n\t" "s_waitcnt lgkmcnt(2)\n\t" \
   "v_mfma_f32_16x16x32_bf16 v[60:63], a[144:147], v[96:99], v[60:63]\n\t" "v_and_b32 v12, 0xffff0000, v123\n\t" "v_lshlrev_b32 v123, 16, v123\n\t" \
   "v_mfma_f32_16x16x32_bf16 v[56:59], a[64:67], v[96:99], v[56:59]\n\t" "v_add_f32 v123, 0x3727c5ac, v123\n\t" \
   "v_mfma_f32_16x16x32_bf16 v[60:63], a[148:151], v[100:103], v[60:63]\n\t" "v_cvt_pk_bf16_f32 v123, v123, v123\n\t" \
   "v_mfma_f32_16x16x32_bf16 v[56:59], a[68:71], v[100:103], v[56:59]\n\t" "v_lshlrev_b32 v123, 16, v123\n\t" "s_waitcnt lgkmcnt(0)\n\t" \
   "v_mfma_f32_16x16x32_bf16 v[60:63], a[152:155], v[104:107], v[60:63]\n\t" "v_sqrt_f32 v123, v123\n\t" "s_nop 0\n\t" \
   "v_mfma_f32_16x16x32_bf16 v[56:59], a[72:75], v[104:107], v[56:59]\n\t" "v_cvt_pk_bf16_f32 v123, v123, v123\n\t" \
   "v_mfma_f32_16x16x32_bf16 v[60:63], a[156:159], v[108:111], v[60:63]\n\t" "v_lshlrev_b32 v123, 16, v123\n\t" \
   "v_mfma_f32_16x16x32_bf16 v[56:59], a[76:79], v[108:111], v[56:59]\n\t" "v_rcp_f32 v132, v123\n\t" "s_xor_b32 s46, s46, 44032\n\t" "s_nop 6\n\t" \
   "v_fmac_f32 v56, v32, v12\n\t" "v_fmac_f32 v57, v33, v12\n\t" "v_fmac_f32 v58, v34, v12\n\t" "v_fmac_f32 v59, v35, v12\n\t" \
   "v_fmac_f32 v60, v36, v12\n\t" "v_fmac_f32 v61, v37, v12\n\t" "v_fmac_f32 v62, v38, v12\n\t" "v_fmac_f32 v63, v39, v12\n\t" \
   "v_mul_f32 v56, v56, v132\n\t" "v_mul_f32 v57, v57, v132\n\t" "v_mul_f32 v58, v58, v132\n\t" "v_mul_f32 v59, v59, v132\n\t" \
   "v_mul_f32 v60, v60, v132\n\t" "v_mul_f32 v61, v61, v132\n\t" "v_mul_f32 v62, v62, v132\n\t" "v_mul_f32 v63, v63, v132\n\t" \
   "v_cmp_u_f32 vcc, v56, v56\n\t" "s_and_b64 vcc, vcc, s[42:43]\n\t" "s_or_b32 s41, s41, vcc_lo\n\t" "v_cndmask_b32 v30, v21, v72, vcc\n\t" \
   "v_cvt_pk_bf16_f32 v68, v56, v57\n\t" "v_cvt_pk_bf16_f32 v69, v58, v59\n\t" "v_cvt_pk_bf16_f32 v70, v60, v61\n\t" \
   "v_cvt_pk_bf16_f32 v71, v62, v63\n\t" "buffer_store_dwordx2 v[68:69], v30, %[rs], s48 offen offset:128 sc1\n\t" \
   "buffer_store_dwordx2 v[70:71], v30, %[rs], s47 offen offset:128 sc1\n\t" "s_mov_b32 s40, 170\n\t" "s_mov_b64 s[42:43], 0\n\t" \
   "s_cmp_lg_u32 s41, 0\n\t" "s_cbranch_scc1 5b\n\t" "s_add_u32 s45, s45, 4\n\t" "s_cmp_lt_u32 s45, 64\n\t" "s_cbranch_scc1 1b\n\t" \
   "s_waitcnt vmcnt(0) lgkmcnt(0)"
#define DF_CLOB "v8", "v9", "v10", "v11", "v12", "v13", "v14", "v15", "v32", "v33", "v34", "v35", "v36", "v37", "v38", "v39", "v40", "v41", "v42", "v43", "v44", "v45", "v46", "v47", "v48", "v49", "v50", "v51", "v52", "v53", "v54", "v55", "v56", "v57", "v58", "v59", "v60", "v61", "v62", "v63", "v64", "v65", "v66", "v67", "v68", "v69", "v70", "v71", "v72", "v73", "v74", "v75", "v76", "v77", "v78", "v79", "v80", "v81", "v82", "v83", "v84", "v85", "v86", "v87", "v88", "v89", "v90", "v91", "v92", "v93", "v94", "v95", "v96", "v97", "v98", "v99", "v100", "v101", "v102", "v103", "v104", "v105", "v106", "v107", "v108", "v109", "v110", "v111", "v112", "v113", "v114", "v115", "v116", "v117", "v118", "v119", "v120", "v121", "v122", "v123", "v124", "v125", "v126", "v127", "v128", "v129", "v130", "v131", "v132", "v133", "v134", "v135", "s40", "s41", "s42", "s43", "s44", "s45", "s46", "s47", "s48", "s49", "s50", "s51", "s52", "s53", "s54", "s55"
template <bool VU>
__device__ __forceinline__ void job_proj(int wv_, const v32u (&w)[10], unsigned char* ws, const Pool& pool, int layer, int h) {
  const unsigned lds0 = (unsigned)(uintptr_t)(__attribute__((address_space(3))) unsigned char*)dyn_lds;
  const int lane = lane_id(), wv = wv_, g = lane >> 4, r = lane & 15;
  const v4u rs = make_rs(ws, (unsigned)OFF_END);
  v16u ad = (unsigned)lane;
  ad[0] = (int)OFF_XR + wv * 10240 + lane * 16;
  ad[1] = wv * 64 + lane < 96 ? (int)OFF_ST + wv * 1024 + lane * 16 : OOB;
  ad[3] = cnt_off(layer, CNT_IDFT, lane & 31);
  ad[4] = lane == 0 ? kqf_off(layer, 0, (VU ? 32 : 0) + h) + wv * 4 : OOB;
  const int q = (g >> 1) * 256 + r * 16;
  ad[5] = (VU ? (int)OFF_UF + (wv * 320 + h) * 1024 + (g & 1) * 32768 : (h * RT * 4 + wv) * 1024 + (g & 1) * 4096) + q;
  ad[6] = lds0 + PJ_X + (g & 1) * 20480 + q;
  ad[7] = lds0 + PJ_X + 40960 + (lane & 31) * 48;
  ad[8] = lds0 + wv * 2048 + lane * 16;
  ad[9] = lds0 + 8192 + lane * 16;
  ad[10] = lds0 + PJ_LDS + 1024 + wv * 256 + (lane >> 5) * 128;
  if (layer > 0) fan_wait<64, NFIRST>(wv_, ws, fan_off(1, 1, 0), h);
  PH(2 + layer * 6)
#define PJ_DBG
#define PJ_OPS                                                                                                                      \
  : W_OPS, [rs] "s"(rs), [rdy] "s"((layer - 1) | ((1 << NFIRST / 8) - 1)), [ldx] "s"(lds0 + PJ_X + wv * 10240),                        \
    [ld1] "s"(lds0 + PJ_X + 40960 + (wv < 2 ? wv : 2) * 1024), [qf] "s"((int)OFF_QF), [kf] "s"((int)OFF_KF), [cnt] "s"(cnt_off(layer, CNT_IDFT, 0)), \
    [flw] "s"(lds0 + PJ_LDS + 512 + wv * 4) PJ_DBG
  if constexpr (VU) asm volatile(VU_ASM : "+{v[16:31]}"(ad) PJ_OPS : VU_CLOB, "vcc", "scc", "m0", "memory");
  else asm volatile(KQ_ASM : "+{v[16:31]}"(ad) PJ_OPS : KQ_CLOB, "vcc", "scc", "m0", "memory");
  LDSBAR();
}

__device__ __forceinline__ unsigned dft_idx(int f, int d) { return (unsigned)((f == 1 ? 320 : f >> 1) * d + (((f & 1) && f != 1) ? 160 : 0)) % 640u; }
__device__ __forceinline__ float cos640(unsigned i) { return __builtin_amdgcn_cosf((float)i * (1.f / 640)); }
__device__ __forceinline__ void job_dft(int wv_, unsigned char* ws, int layer, int fg, int set) {
  const unsigned lds0 = (unsigned)(uintptr_t)(__attribute__((address_space(3))) unsigned char*)dyn_lds;
  const int lane = lane_id(), wv = wv_, g = lane >> 4, r = lane & 15;
  const v4u rs = make_rs(ws, (unsigned)OFF_END);
  v32u w[5];
#pragma unroll
  for (int t = 0; t < 2; ++t) {
    const int f = 16 * (fg * 8 + wv * 2 + t) + r;
    const unsigned step = dft_idx(f & ~(f != 1), 32);
#pragma unroll
    for (int q = 0; q < 4; ++q) {
      unsigned m0 = dft_idx(f, sigma(g, 2 * q)), m1 = dft_idx(f, sigma(g, 2 * q + 1));
#pragma unroll
      for (int kt = 0; kt < KT; ++kt) {
        const int i = t * KT + kt;
        w[i >> 3][(i & 7) * 4 + q] = pk(cos640(m0), cos640(m1));
        m0 += step; m0 = min(m0, m0 - 640u); m1 += step; m1 = min(m1, m1 - 640u);
      }
    }
  }
  v16u ad = (unsigned)lane;
  ad[0] = (int)OFF_XR + wv * 10240 + lane * 16;
  ad[1] = wv * 64 + lane < 96 ? (int)OFF_ST + wv * 1024 + lane * 16 : OOB;
  ad[3] = cnt_off(layer, CNT_OUT, 0);
  ad[4] = wv == 0 && lane < FNG ? fan_off(0, 1, 0) + lane * FAN_G + (layer * 20 + set * 5 + fg) * 64 : OOB;
  ad[5] = (int)OFF_X1T + (((fg * 8 + wv * 2) * 4 + g) * XS + r) * 8;
  ad[6] = lds0 + lane * 16;
  ad[7] = lds0 + 40960 + r * 48;
  asm volatile(DF_ASM
               : "+{v[16:31]}"(ad)
               : "{a[0:31]}"(w[0]), "{a[32:63]}"(w[1]), "{a[64:95]}"(w[2]), "{a[96:127]}"(w[3]), "{a[128:159]}"(w[4]), [rs] "s"(rs), [set] "s"(set), [xs4] "s"(4 * XS * 8), [tg] "s"(TG0 + (TG1 - TG0) * layer),
                 [ldx] "s"(lds0 + wv * 10240), [ld0] "s"(lds0), [ld1] "s"(lds0 + 40960 + (wv < 2 ? wv : 2) * 1024)
               : DF_CLOB, "vcc", "scc", "m0", "memory");
}

constexpr int NOUT = 44, NLATE = 108 - NFILL;
constexpr int L_HS = PJ_LDS, MAIN_LDS = L_HS + 2048;
static_assert(MAIN_LDS >= JOB_LDS && MAIN_LDS >= REC_LDS && MAIN_LDS <= 160 * 1024, "LDS");
struct Go { Pool pool; int rot; };
__device__ __forceinline__ void st32(rsrc_t rs, int off, unsigned v) { __builtin_amdgcn_raw_buffer_store_b32(v, rs, off, 0, AUX_SC1); }
__device__ __forceinline__ unsigned hello_load(rsrc_t rs, int b) { return __builtin_amdgcn_raw_buffer_load_b32(rs, HS_GO + b * 16, 0, AUX_SC1); }
__device__ __forceinline__ void hello(int wv, rsrc_t rs, int b, unsigned t) {
  if (wv == 0) {
    st32(rs, lane_id() == 0 ? HS_HI + b * 8 : OOB, t + 1u);
    LDS_U32(lds_base() + L_HS) = t + 1u;
  }
}
__device__ __forceinline__ int go_off(int b) {
  const int l = lane_id() & 3;
  return l == 0 ? HS_GO + b * 16 : (l == 3 ? HS_ASK + (b & 127) * 16 : HS_PAY + (b & 31) * 32 + l * 16 - 16);
}
__device__ __forceinline__ Go wait_go(int wv, unsigned char* ws, int b, v4u rep, v4u m) {
  const v4u rs = make_rs(ws, (unsigned)OFF_END);
  const int l = lane_id();
  const unsigned a = __builtin_amdgcn_readfirstlane(LDS_U32(lds_base() + L_HS));
  for (unsigned spins = 0; spins < (1u << 18); ++spins) {
    if (spins || b >= 128) {
      VM_LD128(m, go_off(b), 0, 0);
      VM_WAIT1(0, m);
    }
    const unsigned n = __builtin_amdgcn_readlane(m.y, 0);
    if (__builtin_amdgcn_readlane(m.x, 0) == a && __builtin_amdgcn_readlane(m.x, 1) == n && __builtin_amdgcn_readlane(m.x, 2) == n) break;
    const unsigned t = (wv & 2) ? ((wv & 1) ? m.w : m.z) : ((wv & 1) ? m.y : m.x);
    const bool again = b >= 128 && l == 3 && t != rep.x;
    rep.x = t;
    VM_ST128(rep, again ? HS_CAL - 128 * 64 + b * 64 + wv * 16 : OOB, 0);
    if (spins) __builtin_amdgcn_s_sleep(6);
  }
  VM_ST32(m.z, (wv == 0 && l == 0) ? HS_HI + b * 8 + 4 : OOB, 0);
  if (b >= 128) { PH(1) }
  const auto u64 = [](unsigned lo, int i, unsigned hi, int k) {
    return (unsigned)__builtin_amdgcn_readlane(lo, i) | (unsigned long long)(unsigned)__builtin_amdgcn_readlane(hi, k) << 32;
  };
  Go g;
  g.pool.sel = u64(m.y, 1, m.z, 1); g.pool.s3 = u64(m.w, 1, m.w, 2); g.pool.ysel = u64(m.y, 2, m.z, 2);
  g.pool.base = __builtin_amdgcn_readfirstlane(pool_base(ws));
  g.rot = __builtin_amdgcn_readlane(m.w, 0) & 7;
  return g;
}
#define LDSTAMP(n)
#define LDX
#define LDC
__device__ __forceinline__ v4u load_rows(v32u (&w)[10], int kind, const u16* A, const u16* B, const u16* x, int h, int wv, unsigned slot, unsigned char* ws, int b, bool first) {
  unsigned tr;
  const int l = lane_id(), i = l & 15, g = l >> 4;
  const v4u ra = make_rs(A, 640u * 8192u), rb = make_rs(B, 640u * 8192u), rs = make_rs(ws, (unsigned)OFF_END), rx = make_rs(x, S_ * D_ * 2);
  v8u ad, ex;
  const bool pj = kind == 1 || kind == 2;
  const int wr = 4 * wv + g, rr = 4 * (pj ? g >> 1 : g) + (i >> 2), gt = pj ? g & 1 : 0;
  ad[0] = wr * 8192 + h * 256 + i * 16;
  ad[1] = (h * 128 + wr) * 1280 + i * 16;
  ad[2] = slot + rr * 256 + ((2 * wv + gt) ^ (rr & 7)) * 32 + (i & 3) * 8;
  ad[3] = pj ? ad[2] + 2048u : ad[2] ^ 32u;
  ad[4] = slot + wr * 256 + ((i >> 1) ^ (wr & 7)) * 32 + (i & 1) * 16;
  ad[5] = slot + wr * 328 + ((i >> 2) * 2 + (i & 1)) * 8 + ((i >> 1) & 1) * 4;
  ad[6] = slot + (kind == 3 ? (16 * (g & 1) + 4 * (g >> 1) + ((i >> 2) & 1)) * 328 + (i >> 3) * 8 : rr * 328 + gt * 8) + (i & 3) * 10560 + wv * 80;
  ad[7] = ad[4] + 65536u;
  const unsigned im = lds_base() + PJ_X + (wv >> 1) * PJ_BUF;
  const bool hi = first && wv == 0;
  ex[0] = go_off(b);
  ex[1] = hi ? HS_GO + b * 16 : OOB;
  ex[2] = hi && l == 0 ? HS_HI + b * 8 : OOB;
  ex[3] = lds_base() + L_HS + (hi ? 0 : 4);
  ex[4] = (wv * 16 + (l >> 3)) * 1280 + (l & 7) * 16;
  ex[5] = im + ((wv & 1) * KT + ((l >> 2) & 1)) * 1024 + ((l & 1) * 32 + (l >> 3)) * 16 + (l & 2) * 4;
  ex[6] = im + 40960 + ((wv & 1) * 16 + (l >> 3)) * 48 + min(l & 7, 5) * 8;
  ex[7] = (l & 7) ? 0u : ~0u;
  v4u m;
#define LD_OPS                                                                                                                                       \
  : "={a[0:31]}"(w[0]), "={a[32:63]}"(w[1]), "={a[64:95]}"(w[2]), "={a[96:127]}"(w[3]), "={a[128:159]}"(w[4]), "={a[160:191]}"(w[5]),                \
    "={a[192:223]}"(w[6]), "={a[224:255]}"(w[7]), "={v[192:223]}"(w[8]), "={v[224:255]}"(w[9]), "={v[28:31]}"(m) LDX                                 \
  : "{v[16:23]}"(ad), "{v[112:119]}"(ex), [ra] "s"(ra), [rb] "s"(rb), [rw] "s"(rs), [rx] "s"(rx), [p0] "s"(wv < 3 ? 3 * wv : 8),                     \
    "{v27}"(lds_base() + L_HS + 1024 + wv * 256 + (l >> 5) * 128)                                                                                   \
  : LDP_CLOB LDC, "scc", "memory"
  if (kind == 0) asm volatile(LDP_ASM LD_OPS);
  else if (kind == 1) asm volatile(LDK_ASM LD_OPS);
  else if (kind == 2) asm volatile(LDV_ASM LD_OPS);
  else asm volatile(LDY_ASM LD_OPS);
  return m;
}
__device__ __forceinline__ v4u prep_jobs(int wv, unsigned char* ws, rsrc_t rs, const u16* __restrict__ x, const u16* __restrict__ mem0, int j) {
  const int lane = lane_id(), g = lane >> 4, r = lane & 15, tid = wv * 64 + lane, pb = pool_base(ws);
  const int xc = (int)xcc_id(), u = (j >> 3) * 4 + (xc & 3), page = xc < 4 ? u >> 2 : 16 + u % NYP;
  const int cb = pb + page * PAGE + M_CAL + (xc < 4 ? wv * REG + xc * 512 : 3 * REG - u / NYP * WIN + wv * 512);
  stc(rs, lane < 32 ? cb + (lane >> 3) * 8192 + (lane & 7) * 16 : OOB, v4u{1u, 2u, 3u, 4u});
  const unsigned char* p = ws + __builtin_amdgcn_readfirstlane(cb);
  unsigned t0, t1, t2, t3, t4;
  asm volatile("s_load_dword %0, %4, 0x80\n\ts_load_dword %1, %4, 0x2080\n\ts_load_dword %2, %4, 0x4080\n\ts_load_dword %3, %4, 0x6080"
               : "={s40}"(t0), "={s41}"(t1), "={s42}"(t2), "={s43}"(t3) : "s"(p) : "memory");
  const unsigned tg = hello_load(rs, 128 + j);
  v2u c[5][2];
  unsigned t[4];
#pragma unroll
  for (int q = 0; q < 5; ++q) {
    const u16* xr = x + (size_t)(j * 16 + r) * D_ + 32 * (5 * wv + q) + 4 * g;
    c[q][0] = *(const v2u*)xr; c[q][1] = *(const v2u*)(xr + 16);
  }
#pragma unroll
  for (int k = 0; k < 4; ++k) t[k] = *(const unsigned*)(mem0 + (size_t)((tid >> 1) & 7) * D_ + colmap((min(j >> 3, 4) * 8 + ((tid >> 4) & 7)) * 2 + (tid & 1), 2 * k));
  __builtin_amdgcn_sched_barrier(0);
  const v4u ones = {0xFFFFFFFFu, 0xFFFFFFFFu, 0xFFFFFFFFu, 0xFFFFFFFFu};
  const int o = (j * 256 + tid) * 16;
  const bool cal = (o & 0x79800) == 0x79800;
  stc(rs, (int)OFF_HO + o, ones);
  stc(rs, o + REG < (int)HO_FIRST ? (int)OFF_HO + REG + o : OOB, ones);
  stc(rs, (cal || (o >= W_MEM && o < W_MISC)) ? OOB : pb + (NSEG - 1) * REG + o, ones);
  stc(rs, cal ? OOB : pb + o, ones);
  stc(rs, pb + P_Y + o, ones);
  stc(rs, o < 8 * 16384 ? pb + P_Y + REG + o : OOB, ones);
  hello(wv, rs, 128 + j, tg);
  asm volatile("" ::"v"(tg));
  __builtin_amdgcn_sched_barrier(0);
#define CAL_T(r, o) "s_load_dword " r ", %5, " o "\n\ts_waitcnt lgkmcnt(0)\n\ts_memtime s[46:47]\n\ts_waitcnt lgkmcnt(0)\n\ts_mov_b32 " r ", s46\n\t"
  asm volatile(CAL_T("%0", "0x80") CAL_T("%1", "0") CAL_T("%2", "0x2000") CAL_T("%3", "0x4000") CAL_T("%4", "0x6000")
               : "+{s40}"(t0), "+{s41}"(t1), "+{s42}"(t2), "+{s43}"(t3), "={s44}"(t4) : "s"(p) : "s46", "s47", "memory");
  PH(16)
  __builtin_amdgcn_sched_barrier(0);
  f32x2 st = {0.f, 0.f};
#pragma unroll
  for (int q = 0; q < 5; ++q) {
    const v4u f = v4u{c[q][0].x, c[q][0].y, c[q][1].x, c[q][1].y};
    stc(rs, (int)OFF_XR + ((j * KT + 5 * wv + q) * 64 + lane) * 16, f);
    st += frag_stat(f);
  }
  stc(rs, j < 40 ? pb + (NSEG - 1) * REG + W_MEM + o : OOB, v4u{nosent(t[0]), nosent(t[1]), nosent(t[2]), nosent(t[3])});
  st = rows_sum(st);
  __builtin_amdgcn_raw_buffer_store_b64(__builtin_bit_cast(v2u, g ? f32x2{0.f, 0.f} : st), rs,
                                        (g == 0 || (g == 1 && wv == 0)) ? (int)OFF_ST + ((j * 16 + r) * 6 + (g ? 4 : wv)) * 8 : OOB, 0, AUX_SC1);
  const unsigned ask = __builtin_amdgcn_raw_buffer_load_b32(rs, HS_ASK + j * 16 + wv * 4, 0, AUX_SC1);
  const unsigned b0[4] = {min(t1 - t0, 9999u), min(t2 - t1, 9999u), min(t3 - t2, 9999u), min(t4 - t3, 9999u)}, sh = ((xc ^ wv) & 1) * 16;
  const v4u rep = {ask, (unsigned)page, b0[0] << sh | b0[2] << (16 - sh), b0[1] << sh | b0[3] << (16 - sh)};
  asm volatile("s_waitcnt vmcnt(0)" ::: "memory");
  stc(rs, lane == 0 ? HS_CAL + j * 64 + wv * 16 : OOB, rep);
  LDSBAR();
  PH(18)
  return rep;
}
__device__ __forceinline__ int wave_isum(int v) {
  asm volatile("s_nop 1\n\tv_add_u32_dpp %0, %0, %0 quad_perm:[1,0,3,2] row_mask:0xf bank_mask:0xf\n\t"
               "s_nop 1\n\tv_add_u32_dpp %0, %0, %0 quad_perm:[2,3,0,1] row_mask:0xf bank_mask:0xf\n\t"
               "s_nop 1\n\tv_add_u32_dpp %0, %0, %0 row_half_mirror row_mask:0xf bank_mask:0xf\n\t"
               "s_nop 1\n\tv_add_u32_dpp %0, %0, %0 row_mirror row_mask:0xf bank_mask:0xf\n\ts_nop 0"
               : "+v"(v));
  return (__builtin_amdgcn_readlane(v, 0) + __builtin_amdgcn_readlane(v, 16)) + (__builtin_amdgcn_readlane(v, 32) + __builtin_amdgcn_readlane(v, 48));
}
__device__ __forceinline__ Go lead(int wv, unsigned char* ws, const u16* x, const u16* mem0, int rot) {
  const v4u rs = make_rs(ws, (unsigned)OFF_END);
  const int t = wv * 64 + lane_id();
  int* acc = (int*)(dyn_lds + L_HS + 128);
#define VM_LD64(dst, off) asm volatile("buffer_load_dwordx2 %0, %1, %2, 0 offen sc1" : "=v"(dst) : "v"(off), "s"(rs) : "memory")
  v4u c0, c1;
  v2u hi;
  unsigned old, n = 0;
  VM_LD128(c0, HS_CAL + t * 32, 0, 0);
  VM_LD128(c1, HS_CAL + t * 32, 0, 16);
  VM_LD64(hi, HS_HI + t * 8);
  VM_LD32(old, HS_PAY + (t & 63) * 16, 0);
  asm volatile("s_waitcnt vmcnt(0)" : "+v"(c0), "+v"(c1), "+v"(hi), "+v"(old)::"memory");
  const unsigned r0 = c0.x + 1u, r1 = c1.x + 1u, q = hi.y + 1u;
  __builtin_amdgcn_raw_buffer_store_b64(v2u{r0, r1}, ws_rsrc(ws), HS_ASK + t * 8, 0, AUX_SC1);
  while (__any(old == n)) ++n;
  if (t < 192) acc[t] = 9999;
  prep_jobs(wv, ws, ws_rsrc(ws), x, mem0, 127);
  for (unsigned spins = 0; spins < (1u << 18); ++spins) {
    VM_LD128(c0, HS_CAL + t * 32, 0, 0);
    VM_LD128(c1, HS_CAL + t * 32, 0, 16);
    VM_LD64(hi, HS_HI + t * 8);
    asm volatile("s_waitcnt vmcnt(0)" : "+v"(c0), "+v"(c1), "+v"(hi)::"memory");
    if (!__any(t < 254 && (c0.x != r0 || c1.x != r1))) break;
    __builtin_amdgcn_s_sleep(2);
  }
  if (t < 254) {
    int *a0 = acc + min(c0.y, 47u) * 4, *a1 = acc + min(c1.y, 47u) * 4;
    atomicMin(a0, c0.z & 0xFFFF); atomicMin(a0 + 1, c0.z >> 16); atomicMin(a0 + 2, c0.w & 0xFFFF); atomicMin(a0 + 3, c0.w >> 16);
    atomicMin(a1, c1.z & 0xFFFF); atomicMin(a1 + 1, c1.z >> 16); atomicMin(a1 + 2, c1.w & 0xFFFF); atomicMin(a1 + 3, c1.w >> 16);
  }
  __syncthreads();
  PH(19)
  const int s = lane_id(), fx = (s & 4) ? -1 : 1, fy = ((s * 43 >> 7) & 1) ? -1 : 1;
  const v4i u = ((const v4i*)acc)[s >> 2], v = ((const v4i*)acc)[16 + (s * 43 >> 7)];
  int ax = min(u.x, u.y) - min(u.z, u.w), yx = min(v.z, v.w) - min(v.x, v.y);
  const int n0 = ax > 0 ? u.z : u.x, n1 = ax > 0 ? u.w : u.y, lu = min(n0, n1), lv = min(min(v.x, v.y), min(v.z, v.w)), near = 110 - wave_imax(-min(lu, lv));
  const int sa = (lu < near && abs(ax) > 60) ? (ax > 0 ? fx : -fx) : 0, sy = (lv < near && abs(yx) > 60) ? (yx > 0 ? fy : -fy) : 0;
  const int g = wave_isum(sa + sy), m = wave_isum(abs(sa) + abs(sy));
  const bool with = sa * g > 0;
  const int bs = wave_imax(with ? -n1 : -9999) - wave_imax(with ? -n0 : -9999);
  int b = n0 - n1;
  if (abs(g) * 8 > m * 7) {
    if (sa * g >= 0) { ax = g * fx; b = bs; }
    if (sy == 0) yx = g * fy;
  }
  Go go;
  go.pool.sel = __ballot(ax > 0);
  go.pool.s3 = __ballot(b > 0) ^ 0xAAAAAAAAAAAAAAAAull;
  go.pool.ysel = __ballot(yx > 0);
  go.pool.base = __builtin_amdgcn_readfirstlane(pool_base(ws));
  go.rot = rot;
  PH(20)
  {
    const unsigned long long v = (s & 1) ? go.pool.ysel : go.pool.sel;
    VM_ST128((v4u{n, (unsigned)v, (unsigned)(v >> 32), (unsigned)(go.pool.s3 >> (s & 1) * 32)}), wv == 0 ? HS_PAY + s * 16 : OOB, 0);
  }
  for (unsigned spins = 0; spins < (1u << 18); ++spins) {
    VM_ST128((v4u{hi.x, n, q, (unsigned)rot}), HS_GO + t * 16, 0);
    const unsigned sent = hi.x;
    do {
      __builtin_amdgcn_s_sleep(8);
      VM_LD64(hi, HS_HI + t * 8);
      VM_WAIT1(0, hi);
    } while (!__any(hi.x != sent) && __any(t < 255 && hi.y != q) && ++spins < (1u << 18));
    if (!__any(t < 255 && hi.y != q)) break;
  }
  return go;
}
__device__ __forceinline__ void late_jobs(int wv_, unsigned char* ws, rsrc_t rs, const u16* __restrict__ wr, const u16* __restrict__ wi, int j) {
  const int lane = lane_id(), wv = wv_, g = lane >> 4, r = lane & 15;
  unsigned* t32 = (unsigned*)dyn_lds;
  for (int o = (int)HO_FIRST + (j * 256 + wv * 64 + lane) * 16; o < 2 * (int)HO_LAYER; o += NLATE * 4096)
    stc(rs, (int)OFF_HO + o, v4u{0xFFFFFFFFu, 0xFFFFFFFFu, 0xFFFFFFFFu, 0xFFFFFFFFu});
#pragma unroll 1
  for (int fi = j * 4 + wv; fi < 20 * NFK; fi += NLATE * 4) {
    v4u v;
#pragma unroll
    for (int q = 0; q < 4; ++q) {
      const int e = fi % NFK, d = 16 * (fi / NFK) + r, f = 4 * (32 * (e >> 2) + 8 * q + 2 * (e & 2) + g) + 2 * (e & 1);
      const float sc = f ? 2.f : 1.f;
      v[q] = pk(cos640(dft_idx(f, d)) * sc, cos640(dft_idx(f + 1, d)) * sc);
    }
    stc(rs, (int)OFF_INV + (fi * 64 + lane) * 16, v);
  }
#pragma unroll 1
  for (int b = j; b < 32 * 6; b += NLATE) {
    const int m0 = (b & 31) * 64, k0 = (b >> 5) * 64, n0 = (b & 24) * 2 + (b & 7) * 256;
#pragma unroll
    for (int q = 0; q < 16; ++q) {
      const size_t i = (size_t)(n0 + q + 64 * wv) * 321 + min(k0 + lane, 320);
      t32[(q * 4 + wv) * 65 + lane] = (unsigned)wr[i] | ((unsigned)wi[i] << 16);
    }
    __syncthreads();
#pragma unroll
    for (int q = 0; q < 16; ++q) {
      const int k = q * 4 + wv;
      st32(rs, k0 + k < 321 ? (int)OFF_WT + ((k0 + k) * S_ + m0 + lane) * 4 : OOB, t32[lane * 65 + k]);
    }
    __syncthreads();
  }
  dep_signal(wv_, ws, cnt_off(0, CNT_IDFT, 0));
}
__device__ __forceinline__ void late_wait(int wv_, unsigned char* ws) {
  if (TIDX == 0) {
    for (unsigned spins = 0; spins < (1u << 17); ++spins) {
      if (__hip_atomic_load((gu32*)(ws + cnt_off(0, CNT_IDFT, 0)), __ATOMIC_RELAXED, __HIP_MEMORY_SCOPE_AGENT) == NLATE - 1) break;
      __builtin_amdgcn_s_sleep(16);
    }
  }
  __syncthreads();
}
#define FPACE 1200
#define FILLK 12
#define STR_(x) #x
#define STR(x) STR_(x)
__device__ __forceinline__ void fill_run(int wv, unsigned char* ws, const Pool& pool, int f, int n0, int n1) {
  const v4u rs = make_rs(ws, (unsigned)OFF_END), ones = {~0u, ~0u, ~0u, ~0u};
  const int lane = lane_id();
  unsigned due = rtc() * 8u + (unsigned)(f * 4 + wv) * (FPACE / 576);
  int fo = OOB;
#pragma unroll 1
  for (int n = n0; n <= n1; ++n) {
    const int t = n & 63, rg = pool.region(t), yr = pool.base + P_Y + y_region(pool.ysel, t) - 32 * 16384;
    if (n == NSEG) { PH(44) late_wait(wv, ws); }
    if (n == NSEG + FILLK + 2) { PH(45) }
    if (n == 127) { PH(46) }
    if ((n == NSEG + 1 || n == NSEG + FILLK + 1) && n < n1) {
      unsigned c;
      for (unsigned spins = 0; spins < SPIN_CAP; ++spins) {
        VM_LD32(c, cnt_off(0, CNT_OUT, min(lane, t > 1 ? NSEG - 1 : FILLK + 1)), 0);
        VM_WAIT1(0, c);
        if (!__any((c & 15u) != 9u)) break;
        __builtin_amdgcn_s_sleep(32);
      }
    }
#pragma unroll 1
    for (int k = 0; k < 144 / NFILL; ++k) {
      const int i = f * 4 + wv + 4 * NFILL * k;
      const bool on = t && n < n1 && (t < NSEG - 1 || i < 160 || i >= 240);
      const int so = (i < 256 ? rg : yr) + (i >> 3) * 16384 + (i & 7) * 1024;
      if (on) {
        unsigned now;
        while ((int)(due - (now = rtc() * 8u)) > 0) __builtin_amdgcn_s_sleep(1);
        due = max((int)(due - now), -FPACE * NFILL / 144) + now + FPACE * NFILL / 144;
      }
      VM_ST128(ones, on ? lane * 16 : OOB, so);
      if (k == FLAG - 1) {
        asm volatile("s_waitcnt vmcnt(" STR(FLAG) ")" ::: "memory");
        VM_ST32(1u, fo, 0);
      }
    }
    fo = t && lane < (t == NSEG - 1 ? 2 : 1) ? kqf_off(n >> 6, t - 1 + lane, f) + 16 + wv * 4 : OOB;
  }
}
#define TROB (24 * 256 + 16)
#define TC2 30
#define TVIS 32
#define TDLT 170
#define TM1 88
#define SPARE_US 200
#define SPARE_NAP 127
#define TM2 60
__global__ void __launch_bounds__(256, 1) k_main(unsigned char* ws, const u16* x, const u16* mem0, const u16* Wq, const u16* Wk, const u16* Wv, const u16* Wo,
                                                const u16* wr, const u16* wi, u16* out, long nlayer
                                                ) {
  v4u* smem = (v4u*)dyn_lds;
  const int wv_ = __builtin_amdgcn_readfirstlane(threadIdx.x >> 6);
  const RecTune tn = {TM1, TM2, 4, TROB, TC2, TVIS, TDLT, 1};
  unsigned long long* dbg = nullptr;
#define RSTAMP_(i)
  const rsrc_t rs = ws_rsrc(ws);
  PH(0)
  const int b = blockIdx.x, tt = b >> 3;
  const unsigned t00 = rtc();
  int rot = (int)(xcc_id() - b) & 7, lpl, ljb = -1;
  if (tt < 16) {
    v32u w[10];
    Go go;
    const unsigned lds0 = lds_base();
#pragma unroll 1
    for (int it = 0;; ++it) {
      const int p = (b + rot) & 7, h = (p & 3) * 8 + (tt & 7);
      const bool v = (tt & 8) != 0;
      const v4u m = load_rows(w, p < 4 ? (v ? 3 : 0) : (v ? 2 : 1), v ? Wv : Wq, v ? Wo : Wk, x, h, wv_, lds0 + (p < 4 ? 0 : PJ_X + 2 * PJ_BUF), ws, b, it == 0);
      PH(21 + (p >> 2))
      go = wait_go(wv_, ws, b, m, m);
      if (go.rot == rot || it) break;
      rot = go.rot;
    }
    Pool pool = go.pool;
    const int pl = (b + rot) & 7, role = pl < 4 ? (tt & 8) * 4 + pl * 8 + (tt & 7) : pl * 32 + tt;
    const int hd = (pl - 4) * 8 + (tt & 7);
#pragma unroll 1
    for (int layer = 0; layer < nlayer; ++layer) {
      int ro = role, xs = pl, wq = wv_, h2 = hd | (tt & 8) << 8;
      asm volatile("" : "+s"(ro), "+s"(xs), "+s"(wq), "+s"(h2), "+s"(ws), "+s"(pool.base));
      if (layer > 0) {
        const int jb = ro < 64 ? ro + 57 : (xs - 4) * 16 + (ro & 31);
        late_wait(wq, ws);
        if (ro < 64 || jb < 57) {
          job_col(wq, ws, ws_rsrc(ws), lds_base(), jb, fan_off(0, 1, 0), fan_off(1, 0, 0), false);
          __syncthreads();
          RSTAMP_(5)
          PH(7)
        } else fan_wait<64, 20>(wq, ws, fan_off(0, 1, 0), jb);
      }
      if (ro < 32) { rec_p1(wq, w, ws, pool, layer, ro, xs, tn, dbg); PH(4 + layer * 6) }
      else if (ro < 64) rec_pv(wq, w, ws, pool, layer, ro - 32, xs, tn, dbg);
      else {
        if (h2 >> 8) job_proj<true>(wq, w, ws, pool, layer, h2 & 31);
        else job_proj<false>(wq, w, ws, pool, layer, h2 & 31);
        RSTAMP_(2)
      }
    }
    lpl = pl;
    ljb = role < 64 ? role + 57 : (pl - 4) * 16 + (role & 31);
    ljb = role < 32 ? -1 : ljb + (role >= 64 && ljb >= 57);
  } else {
  const Go go = b == 255 ? lead(wv_, ws, x, mem0, rot) : wait_go(wv_, ws, b, prep_jobs(wv_, ws, rs, x, mem0, b - 128), v4u{0u, 0u, 0u, 0u});
  Pool pool = go.pool;
  const int pl = (b + go.rot) & 7;
  const int role = pl < 4 ? (tt < 26 ? 64 + pl * 10 + tt - 16 : 104 + pl * 6 + tt - 26) : pl * 32 + tt;
  if (tt < (pl < 4 ? 32 - NFILL / 4 : 27)) late_jobs(wv_, ws, rs, wr, wi, pl < 4 ? 44 + pl * (16 - NFILL / 4) + tt - 16 : (pl - 4) * 11 + tt - 16);
  else if (pl < 4) fill_run(wv_, ws, pool, (31 - tt) * 4 + pl, 1, (int)nlayer * NSEG);
  PH(23)
  lpl = pl;
  if (role >= 104 && role < 128) {
    while (rtc() - t00 < 100u * SPARE_US) __builtin_amdgcn_s_sleep(127);
    late_wait(wv_, ws);
    prog_prewait(wv_, ws, kqf_off((int)nlayer - 1, NSEG - 1, ((pl + 1) & 3) * 8 + tt - 26));
    __syncthreads();
    __builtin_amdgcn_s_sleep(SPARE_NAP);
    ljb = role - 39;
  } else if (role >= 64 && role < 104) {
#pragma unroll 1
    for (int layer = 0; layer < nlayer; ++layer) {
      if (layer > 0) {
        late_wait(wv_, ws);
        job_col(wv_, ws, rs, lds_base(), role + 57, fan_off(0, 1, 0), fan_off(1, 0, 0), false);
        __syncthreads();
        PH(7)
      }
      ((unsigned*)(dyn_lds + L_T))[lane_id() & 15] = 0u;
      __syncthreads();
      if (wv_ == 3) rec_fin(ws, pool, layer, role - 64, pl, tn, dbg);
      else if (wv_ == 0) rec_sched(ws, pool, layer, role - 64, pl, tn, dbg);
      __syncthreads();
    }
    ljb = role + 57;
  } else {
#pragma unroll 1
  for (int layer = 0; layer < nlayer; ++layer) {
    int wq = wv_;
    asm volatile("" : "+s"(wq));
    if (layer > 0) late_wait(wq, ws);
    if (layer > 0) job_idft(wq, ws, rs, smem, ((tt - 16) >> 1) * 4 + pl - 4, tt & 1, dbg + 60000 + role * 8);
    RSTAMP_(2)
    __syncthreads();
    if (tt >= 27) {
      job_dft(wq, ws, layer, tt - 27, pl - 4);
      __syncthreads();
      if (layer) { PH(14) }
    } else {
#pragma unroll 1
      for (int j = (tt - 16) * 4 + pl - 4; j < 640 - NFAST * 5; j += NOUT) {
        const int u = j / 5 - NFAST, s = u < 0 ? j / 5 : NFAST + (u >> 1);
        flags32_wait(wq, ws, kqf_off(layer, s, 32));
        job_out(wq, ws, rs, pool.ysel, layer, s, j % 5, u < 0 ? -1 : u & 1);
      }
    }
    RSTAMP_(3)
    PH((tt >= 27 ? 6 : 5) + layer * 6)
  }
  if (role == 4 * 32 + 23) ljb = 57;
  }
  }
  int wq = wv_, tl = tt;
  asm volatile("" : "+s"(wq), "+s"(ljb), "+s"(lpl), "+s"(tl), "+s"(ws));
  job_last(wq, ws, out, ljb, lpl * 16 + (tl >> 1), tl & 1);
}

extern "C" void kernel_launch(void* const* d_in, const int* in_sizes, int n_in, void* d_out, int out_size, void* d_ws, size_t ws_size,
                              hipStream_t stream) {
  const u16* x = (const u16*)d_in[0];
  const u16* mem0 = (const u16*)d_in[1];
  const u16* Wq = (const u16*)d_in[2];
  const u16* Wk = (const u16*)d_in[3];
  const u16* Wv = (const u16*)d_in[4];
  const u16* Wo = (const u16*)d_in[5];
  const u16* wr = (const u16*)d_in[8];
  const u16* wi = (const u16*)d_in[9];
  unsigned char* ws = (unsigned char*)d_ws;
  u16* out = (u16*)d_out;
  (void)hipFuncSetAttribute((const void*)k_main, hipFuncAttributeMaxDynamicSharedMemorySize, MAIN_LDS);
  k_main<<<256, 256, MAIN_LDS, stream>>>(ws, x, mem0, Wq, Wk, Wv, Wo, wr, wi, out, 2);
}
```

```cpp
#include <hip/hip_runtime.h>
#include <hip/hip_bf16.h>
#include <stdint.h>

typedef unsigned v4u __attribute__((ext_vector_type(4)));
typedef unsigned v2u __attribute__((ext_vector_type(2)));
typedef float f32x4 __attribute__((ext_vector_type(4)));
typedef float f32x2 __attribute__((ext_vector_type(2)));
typedef __bf16 bf16x8 __attribute__((ext_vector_type(8)));
typedef __bf16 bf16x2 __attribute__((ext_vector_type(2)));
typedef unsigned short u16;

constexpr int S_ = 2048, D_ = 640, NSEG = 64, KT = 20, RT = 128;
constexpr int NFT = 40;
constexpr int NFK = 20;
constexpr int NCOL = 161;
constexpr int WIN = 32768, REG = 16 * WIN;
constexpr int W_MEM = 10 * WIN, W_MISC = 15 * WIN;
__host__ __device__ constexpr int copy_off(int c) { return (c & 3) * 2048 + (c >> 2) * 16384; }
constexpr int M_STAMP = W_MISC + copy_off(4), M_TM = M_STAMP + 40 * 64, M_CAL = W_MISC + copy_off(3);
constexpr int PAGE = 4 * REG, NYP = 22, P_Y = NSEG * REG;
constexpr size_t SZ_POOL = (size_t)P_Y + (size_t)(NYP + 1) * PAGE;

constexpr size_t OFF_CNT = 0;
constexpr int CNT_IDFT = 0, CNT_OUT = 1;
constexpr size_t OFF_PROG = OFF_CNT + (size_t)3 * 32 * 128;
constexpr size_t OFF_KQF = OFF_PROG + (size_t)NSEG * 4096;
#define FNAP 6
#define FSP 16
#define FNG 32
constexpr int FAN_G = (161 * FSP + 127) / 128 * 128 > 3840 ? (161 * FSP + 127) / 128 * 128 : 3840;
constexpr size_t OFF_FAN = OFF_KQF + (size_t)NSEG * 4096;
constexpr size_t HO_LAYER = OFF_FAN + (size_t)2 * FNG * FAN_G;
constexpr size_t OFF_HO = SZ_POOL;
constexpr size_t HO_FIRST = OFF_FAN + (size_t)FNG * FAN_G;
constexpr size_t OFF_HS = OFF_HO + 2 * HO_LAYER;
constexpr int HS_GO = (int)OFF_HS, HS_PAY = HS_GO + 4096, HS_ASK = HS_PAY + 1024, HS_HI = HS_ASK + 2048, HS_CAL = HS_HI + 2048;
__host__ __device__ constexpr int prog_off(int layer, int rb, int slot) { return (int)(OFF_HO + layer * HO_LAYER + OFF_PROG) + (rb * 64 + slot) * 64; }
__host__ __device__ constexpr int kqf_off(int layer, int sg, int h) { return (int)(OFF_HO + layer * HO_LAYER + OFF_KQF) + (sg * 64 + h) * 64; }
__host__ __device__ constexpr int cnt_off(int layer, int kind, int rb) { return (int)(OFF_HO + layer * HO_LAYER + OFF_CNT) + (kind * 32 + rb) * 128; }
__host__ __device__ constexpr int fan_off(int layer, int set, int i) { return (int)(OFF_HO + layer * HO_LAYER + OFF_FAN) + ((set ^ layer ^ 1) * FNG + i) * FAN_G; }
constexpr size_t OFF_XR = (OFF_HS + 65536 + 4095) / 4096 * 4096;
constexpr size_t OFF_ST = OFF_XR + (size_t)RT * KT * 1024;
constexpr size_t OFF_QF = OFF_ST + (size_t)S_ * 48;
constexpr size_t OFF_KF = OFF_QF + (size_t)32 * RT * 4 * 1024;
constexpr size_t OFF_UF = OFF_KF + (size_t)32 * RT * 4 * 1024;
constexpr size_t OFF_PXF = OFF_UF + (size_t)NSEG * 80 * 16 * 1024;
constexpr size_t OFF_PXM = OFF_PXF + (size_t)NSEG * 32 * 2 * 1024;
constexpr size_t OFF_INV = OFF_PXM + (size_t)NSEG * 8 * 2 * 1024;
constexpr size_t OFF_X1T = OFF_INV + (size_t)20 * NFK * 1024;
#define ZPAD 32
#define XPAD 32
constexpr int XS = S_ + XPAD, ZS = S_ + ZPAD;
constexpr size_t OFF_ZT = OFF_X1T + (size_t)160 * XS * 8;
constexpr size_t OFF_WT = OFF_ZT + (size_t)161 * ZS * 8;
constexpr size_t OFF_DBG = OFF_WT + (size_t)321 * S_ * 4;
constexpr size_t OFF_END = OFF_DBG + (size_t)1048576;
static_assert(OFF_END <= ((size_t)256 << 20), "workspace too large");

constexpr int AUX_SC1 = 16;
#define PH(k)
#define PHV(k, v)
#define PHT(n)

__device__ __forceinline__ f32x4 mfma(v4u a, v4u b, f32x4 c) {
  return __builtin_amdgcn_mfma_f32_16x16x32_bf16(__builtin_bit_cast(bf16x8, a), __builtin_bit_cast(bf16x8, b), c, 0, 0, 0);
}
__device__ __forceinline__ unsigned pk(float a, float b) {
  f32x2 v = {a, b};
  return __builtin_bit_cast(unsigned, __builtin_convertvector(v, bf16x2));
}
__device__ __forceinline__ float lo_f(unsigned u) { return __builtin_bit_cast(float, u << 16); }
__device__ __forceinline__ float hi_f(unsigned u) { return __builtin_bit_cast(float, u & 0xFFFF0000u); }
__device__ __forceinline__ float bfr(float x) { return lo_f(pk(x, x)); }
__device__ __forceinline__ v4u pk8(f32x4 a, f32x4 b) { return v4u{pk(a.x, a.y), pk(a.z, a.w), pk(b.x, b.y), pk(b.z, b.w)}; }
__device__ __forceinline__ int sigma(int g, int jj) { return 16 * (jj >> 2) + 4 * g + (jj & 3); }
__device__ __forceinline__ int colmap(int c8, int jj) {
  const int p = c8 >> 3, ga = (c8 >> 1) & 3, half = c8 & 1;
  return 32 * (2 * p + (ga >> 1)) + 16 * ((jj >> 1) & 1) + 4 * (2 * (ga & 1) + (jj >> 2)) + 2 * half + (jj & 1);
}
__device__ __forceinline__ int pool_base(const void* ws) { return (int)((0u - (unsigned)(uintptr_t)ws) & (unsigned)(PAGE - 1)); }
__device__ __forceinline__ unsigned xcc_id() { return (unsigned)__builtin_amdgcn_s_getreg(20 | (3 << 11)) & 7u; }
__device__ __forceinline__ float row_sum(float v) {
  float w;
  asm volatile("v_mov_b32 %1, %0\n\ts_nop 1\n\tv_permlane16_swap_b32 %0, %1\n\ts_nop 1\n\tv_add_f32 %0, %0, %1\n\t"
      "v_mov_b32 %1, %0\n\ts_nop 1\n\tv_permlane32_swap_b32 %0, %1\n\ts_nop 1\n\tv_add_f32 %0, %0, %1" : "+v"(v), "=&v"(w));
  return v;
}
__device__ __forceinline__ unsigned nosent(unsigned x) { return x < 0xFFFFFFFEu ? x : 0xFFFFFFFEu; }

__device__ __forceinline__ f32x2 frag_stat(v4u c) {
  const float v[8] = {lo_f(c.x), hi_f(c.x), lo_f(c.y), hi_f(c.y), lo_f(c.z), hi_f(c.z), lo_f(c.w), hi_f(c.w)};
  float q = 0.f;
#pragma unroll
  for (int j = 0; j < 8; ++j) q = __builtin_fmaf(v[j], v[j], q);
  return f32x2{((v[0] + v[1]) + (v[2] + v[3])) + ((v[4] + v[5]) + (v[6] + v[7])), q};
}
__device__ __forceinline__ f32x2 rows_sum(f32x2 a) {
  return f32x2{row_sum(a.x), row_sum(a.y)};
}

typedef int v4i __attribute__((ext_vector_type(4)));
__device__ __forceinline__ int y_region(unsigned long long sel, int s) { return (s * 80 + (s * 43 >> 7) * 16 + (int)((sel >> s) & 1ull)) * 8192; }

__device__ __forceinline__ int lane_id() {
  int l;
  asm volatile("v_mbcnt_lo_u32_b32 %0, -1, 0\n\tv_mbcnt_hi_u32_b32 %0, -1, %0" : "=v"(l));
  return l;
}
#define TIDX (wv_ * 64 + lane_id())
typedef __amdgpu_buffer_rsrc_t rsrc_t;
__device__ __forceinline__ rsrc_t ws_rsrc(unsigned char* ws) { return __builtin_amdgcn_make_buffer_rsrc(ws, 0, (int)OFF_END, 0x00020000); }
__device__ __forceinline__ v4u ldc(rsrc_t rs, int off) { return __builtin_amdgcn_raw_buffer_load_b128(rs, off, 0, AUX_SC1); }
__device__ __forceinline__ void stc(rsrc_t rs, int off, v4u v) { __builtin_amdgcn_raw_buffer_store_b128(v, rs, off, 0, AUX_SC1); }

extern __shared__ __attribute__((aligned(16))) unsigned char dyn_lds[];
__device__ __forceinline__ unsigned lds_base() { return (unsigned)(uintptr_t)(__attribute__((address_space(3))) unsigned char*)dyn_lds; }
__device__ __forceinline__ unsigned rtc() { return (unsigned)__builtin_amdgcn_s_memrealtime(); }
__device__ __forceinline__ int wave_imax(int v) {
  asm volatile("s_nop 1\n\tv_max_i32_dpp %0, %0, %0 quad_perm:[1,0,3,2] row_mask:0xf bank_mask:0xf\n\t"
               "s_nop 1\n\tv_max_i32_dpp %0, %0, %0 quad_perm:[2,3,0,1] row_mask:0xf bank_mask:0xf\n\t"
               "s_nop 1\n\tv_max_i32_dpp %0, %0, %0 row_half_mirror row_mask:0xf bank_mask:0xf\n\t"
               "s_nop 1\n\tv_max_i32_dpp %0, %0, %0 row_mirror row_mask:0xf bank_mask:0xf\n\ts_nop 0"
               : "+v"(v));
  const int a = __builtin_amdgcn_readlane(v, 0), b = __builtin_amdgcn_readlane(v, 16), c = __builtin_amdgcn_readlane(v, 32),
            d = __builtin_amdgcn_readlane(v, 48);
  return max(max(a, b), max(c, d));
}

#define PWN 40
#define PWL 52
typedef __attribute__((address_space(1))) unsigned gu32;
__device__ __forceinline__ void prog_prewait(int wv_, unsigned char* ws, int off) {
  if (TIDX == 0) {
    gu32* p = (gu32*)(ws + off);
    for (unsigned spins = 0; spins < (1u << 16); ++spins) {
      if (__hip_atomic_load(p, __ATOMIC_RELAXED, __HIP_MEMORY_SCOPE_AGENT) != 0xFFFFFFFFu) break;
      __builtin_amdgcn_s_sleep(48);
    }
  }
}
constexpr int L_WHEN = 90112;
__device__ __forceinline__ void prog_wait(int wv_, unsigned char* ws, int off, bool lazy) {
  if (wv_ == 0) {
    const int l = lane_id();
    gu32* p0 = (gu32*)(ws + off + l * 64);
    unsigned a;
    for (unsigned spins = 0; spins < (1u << 17); ++spins) {
      a = __hip_atomic_load(p0, __ATOMIC_RELAXED, __HIP_MEMORY_SCOPE_AGENT);
      if (!__any(a == 0xFFFFFFFFu)) break;
      __builtin_amdgcn_s_sleep(8);
      if (lazy) __builtin_amdgcn_s_sleep(PWN);
    }
    if (l == 0) *(unsigned*)(dyn_lds + L_WHEN) = rtc() + ((int)(a - rtc() * 8u) >> 3);
  }
  __syncthreads();
}
__device__ __forceinline__ void flags32_wait(int wv_, unsigned char* ws, int off) {
  if (wv_ == 0) {
    const int l = lane_id();
    __attribute__((address_space(1))) unsigned long long* p0 = (__attribute__((address_space(1))) unsigned long long*)(ws + off + (l & 31) * 64 + (l >> 5) * 8);
    for (unsigned spins = 0; spins < (1u << 17); ++spins) {
      const unsigned long long v = __hip_atomic_load(p0, __ATOMIC_RELAXED, __HIP_MEMORY_SCOPE_AGENT);
      if (!__any(max((unsigned)v, (unsigned)(v >> 32)) == 0xFFFFFFFFu)) break;
      __builtin_amdgcn_s_sleep(16);
    }
  }
  __syncthreads();
}
__device__ __forceinline__ void dep_signal(int wv_, unsigned char* ws, int off, unsigned inc = 1u) {
  asm volatile("s_waitcnt vmcnt(0)" ::: "memory");
  __syncthreads();
  if (TIDX == 0) __hip_atomic_fetch_add((gu32*)(ws + off), inc, __ATOMIC_RELAXED, __HIP_MEMORY_SCOPE_AGENT);
}

template <int SP> __device__ __forceinline__ void fan_signal(int wv_, unsigned char* ws, int fan, int i) {
  asm volatile("s_waitcnt vmcnt(0)" ::: "memory");
  __syncthreads();
  const int l = lane_id();
  if (wv_ == 0 && l < FNG) __hip_atomic_store((gu32*)(ws + fan + l * FAN_G + i * SP), nosent(rtc()), __ATOMIC_RELAXED, __HIP_MEMORY_SCOPE_AGENT);
}
template <int SP, int N, bool BAR = true> __device__ __forceinline__ void fan_wait(int wv_, unsigned char* ws, int fan, int salt) {
  if (wv_ * 64 < N) {
    gu32* p = (gu32*)(ws + fan + (((int)xcc_id() * (FNG / 8) + (salt & (FNG / 8 - 1))) * FAN_G + min(TIDX, N - 1) * SP));
    unsigned v;
    for (unsigned spins = 0; spins < (1u << 17); ++spins) {
      v = __hip_atomic_load(p, __ATOMIC_RELAXED, __HIP_MEMORY_SCOPE_AGENT);
      const unsigned long long m = __ballot(v == 0xFFFFFFFFu);
      if (m == 0) break;
      __builtin_amdgcn_s_sleep(N == NCOL ? 24 : FNAP);
      if (N == 20 && (m & 0x3FF)) __builtin_amdgcn_s_sleep(40);
    }
    if (N == 20) {
      const unsigned t = rtc();
      *(unsigned*)(dyn_lds + L_WHEN) = t + (unsigned)wave_imax((int)(v - t));
    }
  }
  if (BAR) __syncthreads();
}

typedef unsigned v8u __attribute__((ext_vector_type(8)));
typedef unsigned v16u __attribute__((ext_vector_type(16)));
typedef unsigned v32u __attribute__((ext_vector_type(32)));
__device__ __forceinline__ float cosi(int i) { return __builtin_amdgcn_cosf((float)i * (1.f / 2048)); }
__device__ __forceinline__ float sini(int i) { return __builtin_amdgcn_sinf((float)i * (1.f / 2048)); }
__device__ __forceinline__ void cis8(v32u& d, int x0, int dx, int d4, unsigned m) {
#pragma unroll
  for (int q = 0; q < 4; ++q) {
    const int x = x0 + dx * 2 * (q & 1) + d4 * (q >> 1);
    d[q] = pk(cosi(x), cosi(x + dx)); d[4 + q] = d[q] ^ m;
    d[8 + q] = pk(sini(x), sini(x + dx)); d[12 + q] = d[8 + q] ^ m;
  }
}
#define LDS_V4(a) (*(__attribute__((address_space(3))) v4u*)(uintptr_t)(a))
#define LDS_U32(a) (*(__attribute__((address_space(3))) unsigned*)(uintptr_t)(a))
#define LDS_V2(a) (*(__attribute__((address_space(3))) v2u*)(uintptr_t)(a))
#define COL_ASM \
   "s_waitcnt lgkmcnt(0)\n\t" "s_barrier\n\t" "ds_read_b64_tr_b16 v[136:137], v16 offset:0\n\t" "ds_read_b64_tr_b16 v[138:139], v16 offset:1056\n\t" \
   "ds_read_b64_tr_b16 v[140:141], v16 offset:8448\n\t" "ds_read_b64_tr_b16 v[142:143], v16 offset:9504\n\t" \
   "ds_read_b64_tr_b16 v[144:145], v16 offset:8\n\t" "ds_read_b64_tr_b16 v[146:147], v16 offset:1064\n\t" \
   "ds_read_b64_tr_b16 v[148:149], v16 offset:8456\n\t" "ds_read_b64_tr_b16 v[150:151], v16 offset:9512\n\t" "s_waitcnt lgkmcnt(4)\n\t" \
   "v_mfma_f32_16x16x32_bf16 v[160:163], v[136:139], v[32:35], 0\n\t" "v_mfma_f32_16x16x32_bf16 v[164:167], v[136:139], v[40:43], 0\n\t" \
   "v_mfma_f32_16x16x32_bf16 v[160:163], v[140:143], v[36:39], v[160:163]\n\t" \
   "v_mfma_f32_16x16x32_bf16 v[164:167], v[140:143], v[44:47], v[164:167]\n\t" "ds_read_b64_tr_b16 v[152:153], v16 offset:16\n\t" \
   "ds_read_b64_tr_b16 v[154:155], v16 offset:1072\n\t" "ds_read_b64_tr_b16 v[156:157], v16 offset:8464\n\t" \
   "ds_read_b64_tr_b16 v[158:159], v16 offset:9520\n\t" "s_nop 3\n\t" "s_waitcnt lgkmcnt(4)\n\t" \
   "v_mfma_f32_16x16x32_bf16 v[168:171], v[144:147], v[32:35], 0\n\t" "v_add_f32 v160, v160, v165\n\t" "v_sub_f32 v161, v161, v164\n\t" \
   "v_mfma_f32_16x16x32_bf16 v[172:175], v[144:147], v[40:43], 0\n\t" "v_add_f32 v162, v162, v167\n\t" "v_sub_f32 v163, v163, v166\n\t" \
   "v_mfma_f32_16x16x32_bf16 v[168:171], v[148:151], v[36:39], v[168:171]\n\t" "v_mul_f32 v24, v161, v49\n\t" "v_mul_f32 v25, v161, v48\n\t" \
   "v_mfma_f32_16x16x32_bf16 v[172:175], v[148:151], v[44:47], v[172:175]\n\t" "v_mul_f32 v26, v163, v49\n\t" "v_mul_f32 v27, v163, v48\n\t" \
   "v_fma_f32 v177, v160, v49, v25\n\t" "v_fma_f32 v176, v160, v48, -v24\n\t" "v_fma_f32 v179, v162, v49, v27\n\t" \
   "v_fma_f32 v178, v162, v48, -v26\n\t" "ds_read_b64_tr_b16 v[136:137], v16 offset:24\n\t" "ds_read_b64_tr_b16 v[138:139], v16 offset:1080\n\t" \
   "ds_read_b64_tr_b16 v[140:141], v16 offset:8472\n\t" "ds_read_b64_tr_b16 v[142:143], v16 offset:9528\n\t" "s_waitcnt lgkmcnt(4)\n\t" \
   "v_mfma_f32_16x16x32_bf16 v[160:163], v[152:155], v[32:35], 0\n\t" "v_add_f32 v168, v168, v173\n\t" "v_sub_f32 v169, v169, v172\n\t" \
   "v_mfma_f32_16x16x32_bf16 v[164:167], v[152:155], v[40:43], 0\n\t" "v_add_f32 v170, v170, v175\n\t" "v_sub_f32 v171, v171, v174\n\t" \
   "v_mfma_f32_16x16x32_bf16 v[160:163], v[156:159], v[36:39], v[160:163]\n\t" "v_mul_f32 v24, v169, v51\n\t" "v_mul_f32 v25, v169, v50\n\t" \
   "v_mfma_f32_16x16x32_bf16 v[164:167], v[156:159], v[44:47], v[164:167]\n\t" "v_mul_f32 v26, v171, v51\n\t" "v_mul_f32 v27, v171, v50\n\t" \
   "v_fma_f32 v181, v168, v51, v25\n\t" "v_fma_f32 v180, v168, v50, -v24\n\t" "v_fma_f32 v183, v170, v51, v27\n\t" \
   "v_fma_f32 v182, v170, v50, -v26\n\t" "v_cvt_pk_bf16_f32 v12, v177, v181\n\t" "v_cvt_pk_bf16_f32 v8, v176, v180\n\t" \
   "v_cvt_pk_bf16_f32 v188, v179, v183\n\t" "v_cvt_pk_bf16_f32 v184, v178, v182\n\t" "ds_read_b64_tr_b16 v[144:145], v16 offset:128\n\t" \
   "ds_read_b64_tr_b16 v[146:147], v16 offset:1184\n\t" "ds_read_b64_tr_b16 v[148:149], v16 offset:8576\n\t" \
   "ds_read_b64_tr_b16 v[150:151], v16 offset:9632\n\t" "s_waitcnt lgkmcnt(4)\n\t" "v_mfma_f32_16x16x32_bf16 v[168:171], v[136:139], v[32:35], 0\n\t" \
   "v_add_f32 v160, v160, v165\n\t" "v_sub_f32 v161, v161, v164\n\t" "v_mfma_f32_16x16x32_bf16 v[172:175], v[136:139], v[40:43], 0\n\t" \
   "v_add_f32 v162, v162, v167\n\t" "v_sub_f32 v163, v163, v166\n\t" "v_mfma_f32_16x16x32_bf16 v[168:171], v[140:143], v[36:39], v[168:171]\n\t" \
   "v_mul_f32 v24, v161, v53\n\t" "v_mul_f32 v25, v161, v52\n\t" "v_mfma_f32_16x16x32_bf16 v[172:175], v[140:143], v[44:47], v[172:175]\n\t" \
   "v_mul_f32 v26, v163, v53\n\t" "v_mul_f32 v27, v163, v52\n\t" "v_fma_f32 v177, v160, v53, v25\n\t" "v_fma_f32 v176, v160, v52, -v24\n\t" \
   "v_fma_f32 v179, v162, v53, v27\n\t" "v_fma_f32 v178, v162, v52, -v26\n\t" "ds_read_b64_tr_b16 v[152:153], v16 offset:136\n\t" \
   "ds_read_b64_tr_b16 v[154:155], v16 offset:1192\n\t" "ds_read_b64_tr_b16 v[156:157], v16 offset:8584\n\t" \
   "ds_read_b64_tr_b16 v[158:159], v16 offset:9640\n\t" "s_waitcnt lgkmcnt(4)\n\t" "v_mfma_f32_16x16x32_bf16 v[160:163], v[144:147], v[32:35], 0\n\t" \
   "v_add_f32 v168, v168, v173\n\t" "v_sub_f32 v169, v169, v172\n\t" "v_mfma_f32_16x16x32_bf16 v[164:167], v[144:147], v[40:43], 0\n\t" \
   "v_add_f32 v170, v170, v175\n\t" "v_sub_f32 v171, v171, v174\n\t" "v_mfma_f32_16x16x32_bf16 v[160:163], v[148:151], v[36:39], v[160:163]\n\t" \
   "v_mul_f32 v24, v169, v55\n\t" "v_mul_f32 v25, v169, v54\n\t" "v_mfma_f32_16x16x32_bf16 v[164:167], v[148:151], v[44:47], v[164:167]\n\t" \
   "v_mul_f32 v26, v171, v55\n\t" "v_mul_f32 v27, v171, v54\n\t" "v_fma_f32 v181, v168, v55, v25\n\t" "v_fma_f32 v180, v168, v54, -v24\n\t" \
   "v_fma_f32 v183, v170, v55, v27\n\t" "v_fma_f32 v182, v170, v54, -v26\n\t" "v_cvt_pk_bf16_f32 v13, v177, v181\n\t" \
   "v_cvt_pk_bf16_f32 v9, v176, v180\n\t" "v_cvt_pk_bf16_f32 v189, v179, v183\n\t" "v_cvt_pk_bf16_f32 v185, v178, v182\n\t" \
   "ds_read_b64_tr_b16 v[136:137], v16 offset:144\n\t" "ds_read_b64_tr_b16 v[138:139], v16 offset:1200\n\t" \
   "ds_read_b64_tr_b16 v[140:141], v16 offset:8592\n\t" "ds_read_b64_tr_b16 v[142:143], v16 offset:9648\n\t" "s_waitcnt lgkmcnt(4)\n\t" \
   "v_mfma_f32_16x16x32_bf16 v[168:171], v[152:155], v[32:35], 0\n\t" "v_add_f32 v160, v160, v165\n\t" "v_sub_f32 v161, v161, v164\n\t" \
   "v_mfma_f32_16x16x32_bf16 v[172:175], v[152:155], v[40:43], 0\n\t" "v_add_f32 v162, v162, v167\n\t" "v_sub_f32 v163, v163, v166\n\t" \
   "v_mfma_f32_16x16x32_bf16 v[168:171], v[156:159], v[36:39], v[168:171]\n\t" "v_mul_f32 v24, v161, v57\n\t" "v_mul_f32 v25, v161, v56\n\t" \
   "v_mfma_f32_16x16x32_bf16 v[172:175], v[156:159], v[44:47], v[172:175]\n\t" "v_mul_f32 v26, v163, v57\n\t" "v_mul_f32 v27, v163, v56\n\t" \
   "v_fma_f32 v177, v160, v57, v25\n\t" "v_fma_f32 v176, v160, v56, -v24\n\t" "v_fma_f32 v179, v162, v57, v27\n\t" \
   "v_fma_f32 v178, v162, v56, -v26\n\t" "ds_read_b64_tr_b16 v[144:145], v16 offset:152\n\t" "ds_read_b64_tr_b16 v[146:147], v16 offset:1208\n\t" \
   "ds_read_b64_tr_b16 v[148:149], v16 offset:8600\n\t" "ds_read_b64_tr_b16 v[150:151], v16 offset:9656\n\t" "s_waitcnt lgkmcnt(4)\n\t" \
   "v_mfma_f32_16x16x32_bf16 v[160:163], v[136:139], v[32:35], 0\n\t" "v_add_f32 v168, v168, v173\n\t" "v_sub_f32 v169, v169, v172\n\t" \
   "v_mfma_f32_16x16x32_bf16 v[164:167], v[136:139], v[40:43], 0\n\t" "v_add_f32 v170, v170, v175\n\t" "v_sub_f32 v171, v171, v174\n\t" \
   "v_mfma_f32_16x16x32_bf16 v[160:163], v[140:143], v[36:39], v[160:163]\n\t" "v_mul_f32 v24, v169, v59\n\t" "v_mul_f32 v25, v169, v58\n\t" \
   "v_mfma_f32_16x16x32_bf16 v[164:167], v[140:143], v[44:47], v[164:167]\n\t" "v_mul_f32 v26, v171, v59\n\t" "v_mul_f32 v27, v171, v58\n\t" \
   "v_fma_f32 v181, v168, v59, v25\n\t" "v_fma_f32 v180, v168, v58, -v24\n\t" "v_fma_f32 v183, v170, v59, v27\n\t" \
   "v_fma_f32 v182, v170, v58, -v26\n\t" "v_cvt_pk_bf16_f32 v14, v177, v181\n\t" "v_cvt_pk_bf16_f32 v10, v176, v180\n\t" \
   "v_cvt_pk_bf16_f32 v190, v179, v183\n\t" "v_cvt_pk_bf16_f32 v186, v178, v182\n\t" "s_nop 1\n\t" "s_waitcnt lgkmcnt(0)\n\t" \
   "v_mfma_f32_16x16x32_bf16 v[168:171], v[144:147], v[32:35], 0\n\t" "v_add_f32 v160, v160, v165\n\t" "v_sub_f32 v161, v161, v164\n\t" \
   "v_mfma_f32_16x16x32_bf16 v[172:175], v[144:147], v[40:43], 0\n\t" "v_add_f32 v162, v162, v167\n\t" "v_sub_f32 v163, v163, v166\n\t" \
   "v_mfma_f32_16x16x32_bf16 v[168:171], v[148:151], v[36:39], v[168:171]\n\t" "v_mul_f32 v24, v161, v61\n\t" "v_mul_f32 v25, v161, v60\n\t" \
   "v_mfma_f32_16x16x32_bf16 v[172:175], v[148:151], v[44:47], v[172:175]\n\t" "v_mul_f32 v26, v163, v61\n\t" "v_mul_f32 v27, v163, v60\n\t" \
   "v_fma_f32 v177, v160, v61, v25\n\t" "v_fma_f32 v176, v160, v60, -v24\n\t" "v_fma_f32 v179, v162, v61, v27\n\t" \
   "v_fma_f32 v178, v162, v60, -v26\n\t" "s_nop 1\n\t" "v_add_f32 v168, v168, v173\n\t" "v_sub_f32 v169, v169, v172\n\t" "v_mul_f32 v24, v169, v63\n\t" \
   "v_mul_f32 v25, v169, v62\n\t" "v_fma_f32 v181, v168, v63, v25\n\t" "v_fma_f32 v180, v168, v62, -v24\n\t" "v_cvt_pk_bf16_f32 v15, v177, v181\n\t" \
   "v_cvt_pk_bf16_f32 v11, v176, v180\n\t" "s_nop 1\n\t" "v_mfma_f32_16x16x32_bf16 v[160:163], v[64:67], v[8:11], 0\n\t" \
   "v_add_f32 v170, v170, v175\n\t" "v_sub_f32 v171, v171, v174\n\t" "v_mfma_f32_16x16x32_bf16 v[164:167], v[64:67], v[12:15], 0\n\t" \
   "v_mul_f32 v24, v171, v63\n\t" "v_mfma_f32_16x16x32_bf16 v[160:163], v[72:75], v[12:15], v[160:163]\n\t" "v_mul_f32 v25, v171, v62\n\t" \
   "v_mfma_f32_16x16x32_bf16 v[164:167], v[128:131], v[8:11], v[164:167]\n\t" "v_fma_f32 v183, v170, v63, v25\n\t" \
   "v_fma_f32 v182, v170, v62, -v24\n\t" "v_cvt_pk_bf16_f32 v191, v179, v183\n\t" "v_cvt_pk_bf16_f32 v187, v178, v182\n\t" "s_nop 4\n\t" \
   "v_mfma_f32_16x16x32_bf16 v[168:171], v[68:71], v[8:11], 0\n\t" "v_mul_f32 v24, v164, v97\n\t" "v_mul_f32 v25, v164, v96\n\t" \
   "v_mul_f32 v26, v165, v99\n\t" "v_mfma_f32_16x16x32_bf16 v[172:175], v[68:71], v[12:15], 0\n\t" "v_mul_f32 v27, v165, v98\n\t" \
   "v_mul_f32 v28, v166, v101\n\t" "v_mul_f32 v29, v166, v100\n\t" "v_mfma_f32_16x16x32_bf16 v[168:171], v[76:79], v[12:15], v[168:171]\n\t" \
   "v_mul_f32 v30, v167, v103\n\t" "v_mul_f32 v31, v167, v102\n\t" "v_fma_f32 v164, v160, v97, v25\n\t" \
   "v_mfma_f32_16x16x32_bf16 v[172:175], v[132:135], v[8:11], v[172:175]\n\t" "v_fma_f32 v160, v160, v96, -v24\n\t" \
   "v_fma_f32 v165, v161, v99, v27\n\t" "v_fma_f32 v161, v161, v98, -v26\n\t" "v_fma_f32 v166, v162, v101, v29\n\t" \
   "v_fma_f32 v162, v162, v100, -v28\n\t" "v_fma_f32 v167, v163, v103, v31\n\t" "v_fma_f32 v163, v163, v102, -v30\n\t" \
   "v_cvt_pk_bf16_f32 v140, v164, v165\n\t" "v_cvt_pk_bf16_f32 v136, v160, v161\n\t" "v_cvt_pk_bf16_f32 v141, v166, v167\n\t" \
   "v_cvt_pk_bf16_f32 v137, v162, v163\n\t" "v_mfma_f32_16x16x32_bf16 v[160:163], v[64:67], v[184:187], 0\n\t" "v_mul_f32 v24, v172, v105\n\t" \
   "v_mul_f32 v25, v172, v104\n\t" "v_mul_f32 v26, v173, v107\n\t" "v_mfma_f32_16x16x32_bf16 v[164:167], v[64:67], v[188:191], 0\n\t" \
   "v_mul_f32 v27, v173, v106\n\t" "v_mul_f32 v28, v174, v109\n\t" "v_mul_f32 v29, v174, v108\n\t" \
   "v_mfma_f32_16x16x32_bf16 v[160:163], v[72:75], v[188:191], v[160:163]\n\t" "v_mul_f32 v30, v175, v111\n\t" "v_mul_f32 v31, v175, v110\n\t" \
   "v_fma_f32 v172, v168, v105, v25\n\t" "v_mfma_f32_16x16x32_bf16 v[164:167], v[128:131], v[184:187], v[164:167]\n\t" \
   "v_fma_f32 v168, v168, v104, -v24\n\t" "v_fma_f32 v173, v169, v107, v27\n\t" "v_fma_f32 v169, v169, v106, -v26\n\t" \
   "v_fma_f32 v174, v170, v109, v29\n\t" "v_fma_f32 v170, v170, v108, -v28\n\t" "v_fma_f32 v175, v171, v111, v31\n\t" \
   "v_fma_f32 v171, v171, v110, -v30\n\t" "v_cvt_pk_bf16_f32 v142, v172, v173\n\t" "v_cvt_pk_bf16_f32 v138, v168, v169\n\t" \
   "v_cvt_pk_bf16_f32 v143, v174, v175\n\t" "v_cvt_pk_bf16_f32 v139, v170, v171\n\t" "v_mfma_f32_16x16x32_bf16 v[168:171], v[68:71], v[184:187], 0\n\t" \
   "v_mul_f32 v24, v164, v113\n\t" "v_mul_f32 v25, v164, v112\n\t" "v_mul_f32 v26, v165, v115\n\t" \
   "v_mfma_f32_16x16x32_bf16 v[172:175], v[68:71], v[188:191], 0\n\t" "v_mul_f32 v27, v165, v114\n\t" "v_mul_f32 v28, v166, v117\n\t" \
   "v_mul_f32 v29, v166, v116\n\t" "v_mfma_f32_16x16x32_bf16 v[168:171], v[76:79], v[188:191], v[168:171]\n\t" "v_mul_f32 v30, v167, v119\n\t" \
   "v_mul_f32 v31, v167, v118\n\t" "v_fma_f32 v164, v160, v113, v25\n\t" "v_mfma_f32_16x16x32_bf16 v[172:175], v[132:135], v[184:187], v[172:175]\n\t" \
   "v_fma_f32 v160, v160, v112, -v24\n\t" "v_fma_f32 v165, v161, v115, v27\n\t" "v_fma_f32 v161, v161, v114, -v26\n\t" \
   "v_fma_f32 v166, v162, v117, v29\n\t" "v_fma_f32 v162, v162, v116, -v28\n\t" "v_fma_f32 v167, v163, v119, v31\n\t" \
   "v_fma_f32 v163, v163, v118, -v30\n\t" "v_cvt_pk_bf16_f32 v148, v164, v165\n\t" "v_cvt_pk_bf16_f32 v144, v160, v161\n\t" \
   "v_cvt_pk_bf16_f32 v149, v166, v167\n\t" "v_cvt_pk_bf16_f32 v145, v162, v163\n\t" "v_mfma_f32_16x16x32_bf16 v[160:163], v[136:139], v[64:67], 0\n\t" \
   "v_mul_f32 v24, v172, v121\n\t" "v_mul_f32 v25, v172, v120\n\t" "v_mul_f32 v26, v173, v123\n\t" \
   "v_mfma_f32_16x16x32_bf16 v[164:167], v[140:143], v[64:67], 0\n\t" "v_mul_f32 v27, v173, v122\n\t" "v_mul_f32 v28, v174, v125\n\t" \
   "v_mul_f32 v29, v174, v124\n\t" "v_mfma_f32_16x16x32_bf16 v[160:163], v[140:143], v[128:131], v[160:163]\n\t" "v_mul_f32 v30, v175, v127\n\t" \
   "v_mul_f32 v31, v175, v126\n\t" "v_fma_f32 v172, v168, v121, v25\n\t" "v_mfma_f32_16x16x32_bf16 v[164:167], v[136:139], v[72:75], v[164:167]\n\t" \
   "v_fma_f32 v168, v168, v120, -v24\n\t" "v_fma_f32 v173, v169, v123, v27\n\t" "v_fma_f32 v169, v169, v122, -v26\n\t" \
   "v_fma_f32 v174, v170, v125, v29\n\t" "v_fma_f32 v170, v170, v124, -v28\n\t" "v_fma_f32 v175, v171, v127, v31\n\t" \
   "v_fma_f32 v171, v171, v126, -v30\n\t" "v_cvt_pk_bf16_f32 v150, v172, v173\n\t" "v_cvt_pk_bf16_f32 v146, v168, v169\n\t" \
   "v_cvt_pk_bf16_f32 v151, v174, v175\n\t" "v_cvt_pk_bf16_f32 v147, v170, v171\n\t" "v_mfma_f32_16x16x32_bf16 v[168:171], v[136:139], v[68:71], 0\n\t" \
   "v_mul_f32 v24, v164, v81\n\t" "v_mul_f32 v25, v164, v80\n\t" "v_mul_f32 v26, v165, v83\n\t" \
   "v_mfma_f32_16x16x32_bf16 v[172:175], v[140:143], v[68:71], 0\n\t" "v_mul_f32 v27, v165, v82\n\t" "v_mul_f32 v28, v166, v85\n\t" \
   "v_mul_f32 v29, v166, v84\n\t" "v_mfma_f32_16x16x32_bf16 v[168:171], v[140:143], v[132:135], v[168:171]\n\t" "v_mul_f32 v30, v167, v87\n\t" \
   "v_mul_f32 v31, v167, v86\n\t" "v_fma_f32 v164, v160, v81, v25\n\t" "v_mfma_f32_16x16x32_bf16 v[172:175], v[136:139], v[76:79], v[172:175]\n\t" \
   "v_fma_f32 v160, v160, v80, -v24\n\t" "v_fma_f32 v165, v161, v83, v27\n\t" "v_fma_f32 v161, v161, v82, -v26\n\t" \
   "v_fma_f32 v166, v162, v85, v29\n\t" "v_fma_f32 v162, v162, v84, -v28\n\t" "v_fma_f32 v167, v163, v87, v31\n\t" \
   "v_fma_f32 v163, v163, v86, -v30\n\t" "v_cvt_pk_bf16_f32 v178, v164, v165\n\t" "v_cvt_pk_bf16_f32 v176, v160, v161\n\t" \
   "v_cvt_pk_bf16_f32 v179, v166, v167\n\t" "v_cvt_pk_bf16_f32 v177, v162, v163\n\t" "ds_write_b64 v17, v[178:179] offset:128\n\t" \
   "ds_write_b64 v17, v[176:177] offset:0\n\t" "v_mfma_f32_16x16x32_bf16 v[160:163], v[144:147], v[64:67], 0\n\t" "v_mul_f32 v24, v172, v89\n\t" \
   "v_mul_f32 v25, v172, v88\n\t" "v_mul_f32 v26, v173, v91\n\t" "v_mfma_f32_16x16x32_bf16 v[164:167], v[148:151], v[64:67], 0\n\t" \
   "v_mul_f32 v27, v173, v90\n\t" "v_mul_f32 v28, v174, v93\n\t" "v_mul_f32 v29, v174, v92\n\t" \
   "v_mfma_f32_16x16x32_bf16 v[160:163], v[148:151], v[128:131], v[160:163]\n\t" "v_mul_f32 v30, v175, v95\n\t" "v_mul_f32 v31, v175, v94\n\t" \
   "v_fma_f32 v172, v168, v89, v25\n\t" "v_mfma_f32_16x16x32_bf16 v[164:167], v[144:147], v[72:75], v[164:167]\n\t" \
   "v_fma_f32 v168, v168, v88, -v24\n\t" "v_fma_f32 v173, v169, v91, v27\n\t" "v_fma_f32 v169, v169, v90, -v26\n\t" \
   "v_fma_f32 v174, v170, v93, v29\n\t" "v_fma_f32 v170, v170, v92, -v28\n\t" "v_fma_f32 v175, v171, v95, v31\n\t" \
   "v_fma_f32 v171, v171, v94, -v30\n\t" "v_cvt_pk_bf16_f32 v182, v172, v173\n\t" "v_cvt_pk_bf16_f32 v180, v168, v169\n\t" \
   "v_cvt_pk_bf16_f32 v183, v174, v175\n\t" "v_cvt_pk_bf16_f32 v181, v170, v171\n\t" "ds_write_b64 v17, v[182:183] offset:8384\n\t" \
   "ds_write_b64 v17, v[180:181] offset:8256\n\t" "v_mfma_f32_16x16x32_bf16 v[168:171], v[144:147], v[68:71], 0\n\t" "v_mul_f32 v24, v164, v81\n\t" \
   "v_mul_f32 v25, v164, v80\n\t" "v_mul_f32 v26, v165, v83\n\t" "v_mfma_f32_16x16x32_bf16 v[172:175], v[148:151], v[68:71], 0\n\t" \
   "v_mul_f32 v27, v165, v82\n\t" "v_mul_f32 v28, v166, v85\n\t" "v_mul_f32 v29, v166, v84\n\t" \
   "v_mfma_f32_16x16x32_bf16 v[168:171], v[148:151], v[132:135], v[168:171]\n\t" "v_mul_f32 v30, v167, v87\n\t" "v_mul_f32 v31, v167, v86\n\t" \
   "v_fma_f32 v164, v160, v81, v25\n\t" "v_mfma_f32_16x16x32_bf16 v[172:175], v[144:147], v[76:79], v[172:175]\n\t" \
   "v_fma_f32 v160, v160, v80, -v24\n\t" "v_fma_f32 v165, v161, v83, v27\n\t" "v_fma_f32 v161, v161, v82, -v26\n\t" \
   "v_fma_f32 v166, v162, v85, v29\n\t" "v_fma_f32 v162, v162, v84, -v28\n\t" "v_fma_f32 v167, v163, v87, v31\n\t" \
   "v_fma_f32 v163, v163, v86, -v30\n\t" "v_cvt_pk_bf16_f32 v178, v164, v165\n\t" "v_cvt_pk_bf16_f32 v176, v160, v161\n\t" \
   "v_cvt_pk_bf16_f32 v179, v166, v167\n\t" "v_cvt_pk_bf16_f32 v177, v162, v163\n\t" "ds_write_b64 v18, v[178:179] offset:128\n\t" \
   "ds_write_b64 v18, v[176:177] offset:0\n\t" "v_mul_f32 v24, v172, v89\n\t" "v_mul_f32 v25, v172, v88\n\t" "v_mul_f32 v26, v173, v91\n\t" \
   "v_mul_f32 v27, v173, v90\n\t" "v_mul_f32 v28, v174, v93\n\t" "v_mul_f32 v29, v174, v92\n\t" "v_mul_f32 v30, v175, v95\n\t" \
   "v_mul_f32 v31, v175, v94\n\t" "v_fma_f32 v172, v168, v89, v25\n\t" "v_fma_f32 v168, v168, v88, -v24\n\t" "v_fma_f32 v173, v169, v91, v27\n\t" \
   "v_fma_f32 v169, v169, v90, -v26\n\t" "v_fma_f32 v174, v170, v93, v29\n\t" "v_fma_f32 v170, v170, v92, -v28\n\t" \
   "v_fma_f32 v175, v171, v95, v31\n\t" "v_fma_f32 v171, v171, v94, -v30\n\t" "v_cvt_pk_bf16_f32 v182, v172, v173\n\t" \
   "v_cvt_pk_bf16_f32 v180, v168, v169\n\t" "v_cvt_pk_bf16_f32 v183, v174, v175\n\t" "v_cvt_pk_bf16_f32 v181, v170, v171\n\t" \
   "ds_write_b64 v18, v[182:183] offset:8384\n\t" "ds_write_b64 v18, v[180:181] offset:8256\n\t" "s_waitcnt lgkmcnt(0)\n\t" "s_barrier\n\t" \
   "ds_read_b128 v[136:139], v19 offset:0\n\t" "ds_read_b128 v[140:143], v20 offset:0\n\t" "ds_read_b128 v[144:147], v19 offset:2064\n\t" \
   "ds_read_b128 v[148:151], v20 offset:2064\n\t" "s_waitcnt lgkmcnt(2)\n\t" "v_mfma_f32_16x16x32_bf16 v[160:163], v[136:139], v[32:35], 0\n\t" \
   "ds_read_b128 v[152:155], v19 offset:4128\n\t" "ds_read_b128 v[156:159], v20 offset:4128\n\t" \
   "v_mfma_f32_16x16x32_bf16 v[164:167], v[136:139], v[40:43], 0\n\t" "v_mfma_f32_16x16x32_bf16 v[160:163], v[140:143], v[36:39], v[160:163]\n\t" \
   "v_mfma_f32_16x16x32_bf16 v[164:167], v[140:143], v[44:47], v[164:167]\n\t" "s_waitcnt lgkmcnt(2)\n\t" \
   "v_mfma_f32_16x16x32_bf16 v[168:171], v[144:147], v[32:35], 0\n\t" "ds_read_b128 v[136:139], v19 offset:6192\n\t" \
   "ds_read_b128 v[140:143], v20 offset:6192\n\t" "v_mfma_f32_16x16x32_bf16 v[172:175], v[144:147], v[40:43], 0\n\t" "v_sub_f32 v160, v160, v165\n\t" \
   "v_add_f32 v161, v161, v164\n\t" "v_sub_f32 v162, v162, v167\n\t" "v_mfma_f32_16x16x32_bf16 v[168:171], v[148:151], v[36:39], v[168:171]\n\t" \
   "v_add_f32 v163, v163, v166\n\t" "v_cvt_pk_bf16_f32 v8, v160, v161\n\t" "v_cvt_pk_bf16_f32 v9, v162, v163\n\t" \
   "v_mfma_f32_16x16x32_bf16 v[172:175], v[148:151], v[44:47], v[172:175]\n\t" "s_waitcnt lgkmcnt(2)\n\t" \
   "v_mfma_f32_16x16x32_bf16 v[160:163], v[152:155], v[32:35], 0\n\t" "ds_read_b128 v[144:147], v19 offset:8256\n\t" \
   "ds_read_b128 v[148:151], v20 offset:8256\n\t" "v_mfma_f32_16x16x32_bf16 v[164:167], v[152:155], v[40:43], 0\n\t" "v_sub_f32 v168, v168, v173\n\t" \
   "v_add_f32 v169, v169, v172\n\t" "v_sub_f32 v170, v170, v175\n\t" "v_mfma_f32_16x16x32_bf16 v[160:163], v[156:159], v[36:39], v[160:163]\n\t" \
   "v_add_f32 v171, v171, v174\n\t" "v_cvt_pk_bf16_f32 v10, v168, v169\n\t" "v_cvt_pk_bf16_f32 v11, v170, v171\n\t" \
   "v_mfma_f32_16x16x32_bf16 v[164:167], v[156:159], v[44:47], v[164:167]\n\t" "buffer_store_dwordx4 v[8:11], v21, %[rs], 0 offen offset:0 sc1\n\t" \
   "s_waitcnt lgkmcnt(2)\n\t" "v_mfma_f32_16x16x32_bf16 v[168:171], v[136:139], v[32:35], 0\n\t" "ds_read_b128 v[152:155], v19 offset:10320\n\t" \
   "ds_read_b128 v[156:159], v20 offset:10320\n\t" "v_mfma_f32_16x16x32_bf16 v[172:175], v[136:139], v[40:43], 0\n\t" "v_sub_f32 v160, v160, v165\n\t" \
   "v_add_f32 v161, v161, v164\n\t" "v_sub_f32 v162, v162, v167\n\t" "v_mfma_f32_16x16x32_bf16 v[168:171], v[140:143], v[36:39], v[168:171]\n\t" \
   "v_add_f32 v163, v163, v166\n\t" "v_cvt_pk_bf16_f32 v12, v160, v161\n\t" "v_cvt_pk_bf16_f32 v13, v162, v163\n\t" \
   "v_mfma_f32_16x16x32_bf16 v[172:175], v[140:143], v[44:47], v[172:175]\n\t" "s_waitcnt lgkmcnt(2)\n\t" \
   "v_mfma_f32_16x16x32_bf16 v[160:163], v[144:147], v[32:35], 0\n\t" "ds_read_b128 v[136:139], v19 offset:12384\n\t" \
   "ds_read_b128 v[140:143], v20 offset:12384\n\t" "v_mfma_f32_16x16x32_bf16 v[164:167], v[144:147], v[40:43], 0\n\t" "v_sub_f32 v168, v168, v173\n\t" \
   "v_add_f32 v169, v169, v172\n\t" "v_sub_f32 v170, v170, v175\n\t" "v_mfma_f32_16x16x32_bf16 v[160:163], v[148:151], v[36:39], v[160:163]\n\t" \
   "v_add_f32 v171, v171, v174\n\t" "v_cvt_pk_bf16_f32 v14, v168, v169\n\t" "v_cvt_pk_bf16_f32 v15, v170, v171\n\t" \
   "v_mfma_f32_16x16x32_bf16 v[164:167], v[148:151], v[44:47], v[164:167]\n\t" "buffer_store_dwordx4 v[12:15], v21, %[rs], 0 offen offset:64 sc1\n\t" \
   "s_waitcnt lgkmcnt(2)\n\t" "v_mfma_f32_16x16x32_bf16 v[168:171], v[152:155], v[32:35], 0\n\t" "ds_read_b128 v[144:147], v19 offset:14448\n\t" \
   "ds_read_b128 v[148:151], v20 offset:14448\n\t" "v_mfma_f32_16x16x32_bf16 v[172:175], v[152:155], v[40:43], 0\n\t" "v_sub_f32 v160, v160, v165\n\t" \
   "v_add_f32 v161, v161, v164\n\t" "v_sub_f32 v162, v162, v167\n\t" "v_mfma_f32_16x16x32_bf16 v[168:171], v[156:159], v[36:39], v[168:171]\n\t" \
   "v_add_f32 v163, v163, v166\n\t" "v_cvt_pk_bf16_f32 v8, v160, v161\n\t" "v_cvt_pk_bf16_f32 v9, v162, v163\n\t" \
   "v_mfma_f32_16x16x32_bf16 v[172:175], v[156:159], v[44:47], v[172:175]\n\t" "s_waitcnt lgkmcnt(2)\n\t" \
   "v_mfma_f32_16x16x32_bf16 v[160:163], v[136:139], v[32:35], 0\n\t" "s_nop 0\n\t" "s_nop 0\n\t" \
   "v_mfma_f32_16x16x32_bf16 v[164:167], v[136:139], v[40:43], 0\n\t" "v_sub_f32 v168, v168, v173\n\t" "v_add_f32 v169, v169, v172\n\t" \
   "v_sub_f32 v170, v170, v175\n\t" "v_mfma_f32_16x16x32_bf16 v[160:163], v[140:143], v[36:39], v[160:163]\n\t" "v_add_f32 v171, v171, v174\n\t" \
   "v_cvt_pk_bf16_f32 v10, v168, v169\n\t" "v_cvt_pk_bf16_f32 v11, v170, v171\n\t" \
   "v_mfma_f32_16x16x32_bf16 v[164:167], v[140:143], v[44:47], v[164:167]\n\t" "buffer_store_dwordx4 v[8:11], v21, %[rs], 0 offen offset:128 sc1\n\t" \
   "s_waitcnt lgkmcnt(0)\n\t" "v_mfma_f32_16x16x32_bf16 v[168:171], v[144:147], v[32:35], 0\n\t" "s_nop 0\n\t" "s_nop 0\n\t" \
   "v_mfma_f32_16x16x32_bf16 v[172:175], v[144:147], v[40:43], 0\n\t" "v_sub_f32 v160, v160, v165\n\t" "v_add_f32 v161, v161, v164\n\t" \
   "v_sub_f32 v162, v162, v167\n\t" "v_mfma_f32_16x16x32_bf16 v[168:171], v[148:151], v[36:39], v[168:171]\n\t" "v_add_f32 v163, v163, v166\n\t" \
   "v_cvt_pk_bf16_f32 v12, v160, v161\n\t" "v_cvt_pk_bf16_f32 v13, v162, v163\n\t" \
   "v_mfma_f32_16x16x32_bf16 v[172:175], v[148:151], v[44:47], v[172:175]\n\t" "s_nop 7\n\t" "v_sub_f32 v168, v168, v173\n\t" \
   "v_add_f32 v169, v169, v172\n\t" "v_sub_f32 v170, v170, v175\n\t" "v_add_f32 v171, v171, v174\n\t" "v_cvt_pk_bf16_f32 v14, v168, v169\n\t" \
   "v_cvt_pk_bf16_f32 v15, v170, v171\n\t" "buffer_store_dwordx4 v[12:15], v21, %[rs], 0 offen offset:192 sc1\n\t" "s_nop 1"
#define COL_CLOB "v8", "v9", "v10", "v11", "v12", "v13", "v14", "v15", "v24", "v25", "v26", "v27", "v28", "v29", "v30", "v31", "v136", "v137", "v138", "v139", "v140", "v141", "v142", "v143", "v144", "v145", "v146", "v147", "v148", "v149", "v150", "v151", "v152", "v153", "v154", "v155", "v156", "v157", "v158", "v159", "v160", "v161", "v162", "v163", "v164", "v165", "v166", "v167", "v168", "v169", "v170", "v171", "v172", "v173", "v174", "v175", "v176", "v177", "v178", "v179", "v180", "v181", "v182", "v183", "v184", "v185", "v186", "v187", "v188", "v189", "v190", "v191"
#define IM_ASM \
   "s_mov_b32 s40, 0\n\t" "buffer_load_dwordx4 a[0:3], %[o0], %[rs], s40 offen offset:0 sc1\n\t" "s_mov_b32 s41, 20480\n\t" \
   "buffer_load_dwordx4 a[80:83], %[o0], %[rs], s41 offen offset:0 sc1\n\t" "s_mov_b32 s42, 40960\n\t" \
   "buffer_load_dwordx4 a[160:163], %[o1], %[rs], s42 offen offset:0 sc1\n\t" "buffer_load_dwordx4 a[4:7], %[o0], %[rs], s40 offen offset:1024 sc1\n\t" \
   "buffer_load_dwordx4 a[84:87], %[o0], %[rs], s41 offen offset:1024 sc1\n\t" \
   "buffer_load_dwordx4 a[164:167], %[o1], %[rs], s42 offen offset:1024 sc1\n\t" \
   "buffer_load_dwordx4 a[8:11], %[o0], %[rs], s40 offen offset:2048 sc1\n\t" \
   "buffer_load_dwordx4 a[88:91], %[o0], %[rs], s41 offen offset:2048 sc1\n\t" \
   "buffer_load_dwordx4 a[168:171], %[o1], %[rs], s42 offen offset:2048 sc1\n\t" \
   "buffer_load_dwordx4 a[12:15], %[o0], %[rs], s40 offen offset:3072 sc1\n\t" \
   "buffer_load_dwordx4 a[92:95], %[o0], %[rs], s41 offen offset:3072 sc1\n\t" \
   "buffer_load_dwordx4 a[172:175], %[o1], %[rs], s42 offen offset:3072 sc1\n\t" "s_mov_b32 s40, 4096\n\t" \
   "buffer_load_dwordx4 a[16:19], %[o0], %[rs], s40 offen offset:0 sc1\n\t" "s_mov_b32 s41, 24576\n\t" \
   "buffer_load_dwordx4 a[96:99], %[o0], %[rs], s41 offen offset:0 sc1\n\t" "s_mov_b32 s42, 45056\n\t" \
   "buffer_load_dwordx4 a[176:179], %[o1], %[rs], s42 offen offset:0 sc1\n\t" \
   "buffer_load_dwordx4 a[20:23], %[o0], %[rs], s40 offen offset:1024 sc1\n\t" \
   "buffer_load_dwordx4 a[100:103], %[o0], %[rs], s41 offen offset:1024 sc1\n\t" \
   "buffer_load_dwordx4 a[180:183], %[o1], %[rs], s42 offen offset:1024 sc1\n\t" \
   "buffer_load_dwordx4 a[24:27], %[o0], %[rs], s40 offen offset:2048 sc1\n\t" \
   "buffer_load_dwordx4 a[104:107], %[o0], %[rs], s41 offen offset:2048 sc1\n\t" \
   "buffer_load_dwordx4 a[184:187], %[o1], %[rs], s42 offen offset:2048 sc1\n\t" \
   "buffer_load_dwordx4 a[28:31], %[o0], %[rs], s40 offen offset:3072 sc1\n\t" \
   "buffer_load_dwordx4 a[108:111], %[o0], %[rs], s41 offen offset:3072 sc1\n\t" \
   "buffer_load_dwordx4 a[188:191], %[o1], %[rs], s42 offen offset:3072 sc1\n\t" "s_mov_b32 s40, 8192\n\t" \
   "buffer_load_dwordx4 a[32:35], %[o0], %[rs], s40 offen offset:0 sc1\n\t" "s_mov_b32 s41, 28672\n\t" \
   "buffer_load_dwordx4 a[112:115], %[o0], %[rs], s41 offen offset:0 sc1\n\t" "s_mov_b32 s42, 49152\n\t" \
   "buffer_load_dwordx4 a[192:195], %[o1], %[rs], s42 offen offset:0 sc1\n\t" \
   "buffer_load_dwordx4 a[36:39], %[o0], %[rs], s40 offen offset:1024 sc1\n\t" \
   "buffer_load_dwordx4 a[116:119], %[o0], %[rs], s41 offen offset:1024 sc1\n\t" \
   "buffer_load_dwordx4 a[196:199], %[o1], %[rs], s42 offen offset:1024 sc1\n\t" \
   "buffer_load_dwordx4 a[40:43], %[o0], %[rs], s40 offen offset:2048 sc1\n\t" \
   "buffer_load_dwordx4 a[120:123], %[o0], %[rs], s41 offen offset:2048 sc1\n\t" \
   "buffer_load_dwordx4 a[200:203], %[o1], %[rs], s42 offen offset:2048 sc1\n\t" \
   "buffer_load_dwordx4 a[44:47], %[o0], %[rs], s40 offen offset:3072 sc1\n\t" \
   "buffer_load_dwordx4 a[124:127], %[o0], %[rs], s41 offen offset:3072 sc1\n\t" \
   "buffer_load_dwordx4 a[204:207], %[o1], %[rs], s42 offen offset:3072 sc1\n\t" "s_mov_b32 s40, 12288\n\t" \
   "buffer_load_dwordx4 a[48:51], %[o0], %[rs], s40 offen offset:0 sc1\n\t" "s_mov_b32 s41, 32768\n\t" \
   "buffer_load_dwordx4 a[128:131], %[o0], %[rs], s41 offen offset:0 sc1\n\t" "s_mov_b32 s42, 53248\n\t" \
   "buffer_load_dwordx4 a[208:211], %[o1], %[rs], s42 offen offset:0 sc1\n\t" \
   "buffer_load_dwordx4 a[52:55], %[o0], %[rs], s40 offen offset:1024 sc1\n\t" \
   "buffer_load_dwordx4 a[132:135], %[o0], %[rs], s41 offen offset:1024 sc1\n\t" \
   "buffer_load_dwordx4 a[212:215], %[o1], %[rs], s42 offen offset:1024 sc1\n\t" \
   "buffer_load_dwordx4 a[56:59], %[o0], %[rs], s40 offen offset:2048 sc1\n\t" \
   "buffer_load_dwordx4 a[136:139], %[o0], %[rs], s41 offen offset:2048 sc1\n\t" \
   "buffer_load_dwordx4 a[216:219], %[o1], %[rs], s42 offen offset:2048 sc1\n\t" \
   "buffer_load_dwordx4 a[60:63], %[o0], %[rs], s40 offen offset:3072 sc1\n\t" \
   "buffer_load_dwordx4 a[140:143], %[o0], %[rs], s41 offen offset:3072 sc1\n\t" \
   "buffer_load_dwordx4 a[220:223], %[o1], %[rs], s42 offen offset:3072 sc1\n\t" "s_mov_b32 s40, 16384\n\t" \
   "buffer_load_dwordx4 a[64:67], %[o0], %[rs], s40 offen offset:0 sc1\n\t" "s_mov_b32 s41, 36864\n\t" \
   "buffer_load_dwordx4 a[144:147], %[o0], %[rs], s41 offen offset:0 sc1\n\t" "s_mov_b32 s42, 57344\n\t" \
   "buffer_load_dwordx4 a[224:227], %[o1], %[rs], s42 offen offset:0 sc1\n\t" \
   "buffer_load_dwordx4 a[68:71], %[o0], %[rs], s40 offen offset:1024 sc1\n\t" \
   "buffer_load_dwordx4 a[148:151], %[o0], %[rs], s41 offen offset:1024 sc1\n\t" \
   "buffer_load_dwordx4 a[228:231], %[o1], %[rs], s42 offen offset:1024 sc1\n\t" \
   "buffer_load_dwordx4 a[72:75], %[o0], %[rs], s40 offen offset:2048 sc1\n\t" \
   "buffer_load_dwordx4 a[152:155], %[o0], %[rs], s41 offen offset:2048 sc1\n\t" \
   "buffer_load_dwordx4 a[232:235], %[o1], %[rs], s42 offen offset:2048 sc1\n\t" \
   "buffer_load_dwordx4 a[76:79], %[o0], %[rs], s40 offen offset:3072 sc1\n\t" \
   "buffer_load_dwordx4 a[156:159], %[o0], %[rs], s41 offen offset:3072 sc1\n\t" \
   "buffer_load_dwordx4 a[236:239], %[o1], %[rs], s42 offen offset:3072 sc1\n\t" "s_waitcnt vmcnt(0)"
#define IM_CLOB "s40", "s41", "s42"
#define ID_ASM \
   "s_waitcnt lgkmcnt(0)\n\t" "s_barrier\n\t" "ds_read_b128 v[32:35], %[la] offset:0\n\t" "ds_read_b128 v[36:39], %[la] offset:10240\n\t" \
   "ds_read_b128 v[40:43], %[la] offset:1024\n\t" "ds_read_b128 v[44:47], %[la] offset:11264\n\t" "ds_read_b128 v[48:51], %[la] offset:2048\n\t" \
   "ds_read_b128 v[52:55], %[la] offset:12288\n\t" "ds_read_b128 v[56:59], %[la] offset:3072\n\t" "ds_read_b128 v[60:63], %[la] offset:13312\n\t" \
   "ds_read_b128 v[64:67], %[la] offset:4096\n\t" "ds_read_b128 v[68:71], %[la] offset:14336\n\t" "ds_read_b128 v[72:75], %[la] offset:5120\n\t" \
   "ds_read_b128 v[76:79], %[la] offset:15360\n\t" "ds_read_b128 v[80:83], %[la] offset:6144\n\t" "ds_read_b128 v[84:87], %[la] offset:16384\n\t" \
   "ds_read_b128 v[88:91], %[la] offset:7168\n\t" "ds_read_b128 v[92:95], %[la] offset:17408\n\t" "ds_read_b128 v[96:99], %[la] offset:8192\n\t" \
   "ds_read_b128 v[100:103], %[la] offset:18432\n\t" "ds_read_b128 v[104:107], %[la] offset:9216\n\t" "ds_read_b128 v[108:111], %[la] offset:19456\n\t" \
   "v_lshlrev_b32 v152, 16, v136\n\t" "v_and_b32 v153, 0xffff0000, v136\n\t" "v_lshlrev_b32 v154, 16, v137\n\t" "v_and_b32 v155, 0xffff0000, v137\n\t" \
   "v_lshlrev_b32 v156, 16, v140\n\t" "v_and_b32 v157, 0xffff0000, v140\n\t" "v_lshlrev_b32 v158, 16, v141\n\t" "v_and_b32 v159, 0xffff0000, v141\n\t" \
   "v_lshlrev_b32 v160, 16, v138\n\t" "v_and_b32 v161, 0xffff0000, v138\n\t" "v_lshlrev_b32 v162, 16, v139\n\t" "v_and_b32 v163, 0xffff0000, v139\n\t" \
   "s_waitcnt lgkmcnt(15)\n\t" "v_mfma_f32_16x16x32_bf16 v[112:115], a[0:3], v[32:35], 0\n\t" "v_lshlrev_b32 v164, 16, v142\n\t" \
   "v_and_b32 v165, 0xffff0000, v142\n\t" "v_mfma_f32_16x16x32_bf16 v[116:119], a[4:7], v[36:39], 0\n\t" "v_lshlrev_b32 v166, 16, v143\n\t" \
   "v_and_b32 v167, 0xffff0000, v143\n\t" "v_mfma_f32_16x16x32_bf16 v[112:115], a[8:11], v[40:43], v[112:115]\n\t" "v_lshlrev_b32 v168, 16, v144\n\t" \
   "v_and_b32 v169, 0xffff0000, v144\n\t" "v_mfma_f32_16x16x32_bf16 v[116:119], a[12:15], v[44:47], v[116:119]\n\t" "v_lshlrev_b32 v170, 16, v145\n\t" \
   "v_and_b32 v171, 0xffff0000, v145\n\t" "v_mfma_f32_16x16x32_bf16 v[112:115], a[16:19], v[48:51], v[112:115]\n\t" "v_lshlrev_b32 v172, 16, v146\n\t" \
   "v_and_b32 v173, 0xffff0000, v146\n\t" "s_waitcnt lgkmcnt(14)\n\t" "v_mfma_f32_16x16x32_bf16 v[116:119], a[20:23], v[52:55], v[116:119]\n\t" \
   "v_lshlrev_b32 v174, 16, v147\n\t" "v_and_b32 v175, 0xffff0000, v147\n\t" "s_waitcnt lgkmcnt(13)\n\t" \
   "v_mfma_f32_16x16x32_bf16 v[112:115], a[24:27], v[56:59], v[112:115]\n\t" "s_waitcnt lgkmcnt(12)\n\t" \
   "v_mfma_f32_16x16x32_bf16 v[116:119], a[28:31], v[60:63], v[116:119]\n\t" "s_waitcnt lgkmcnt(11)\n\t" \
   "v_mfma_f32_16x16x32_bf16 v[112:115], a[32:35], v[64:67], v[112:115]\n\t" "s_waitcnt lgkmcnt(10)\n\t" \
   "v_mfma_f32_16x16x32_bf16 v[116:119], a[36:39], v[68:71], v[116:119]\n\t" "s_waitcnt lgkmcnt(9)\n\t" \
   "v_mfma_f32_16x16x32_bf16 v[112:115], a[40:43], v[72:75], v[112:115]\n\t" "s_waitcnt lgkmcnt(8)\n\t" \
   "v_mfma_f32_16x16x32_bf16 v[116:119], a[44:47], v[76:79], v[116:119]\n\t" "s_waitcnt lgkmcnt(7)\n\t" \
   "v_mfma_f32_16x16x32_bf16 v[112:115], a[48:51], v[80:83], v[112:115]\n\t" "s_waitcnt lgkmcnt(6)\n\t" \
   "v_mfma_f32_16x16x32_bf16 v[116:119], a[52:55], v[84:87], v[116:119]\n\t" "s_waitcnt lgkmcnt(5)\n\t" \
   "v_mfma_f32_16x16x32_bf16 v[112:115], a[56:59], v[88:91], v[112:115]\n\t" "s_waitcnt lgkmcnt(4)\n\t" \
   "v_mfma_f32_16x16x32_bf16 v[116:119], a[60:63], v[92:95], v[116:119]\n\t" "s_waitcnt lgkmcnt(3)\n\t" \
   "v_mfma_f32_16x16x32_bf16 v[112:115], a[64:67], v[96:99], v[112:115]\n\t" "s_waitcnt lgkmcnt(2)\n\t" \
   "v_mfma_f32_16x16x32_bf16 v[116:119], a[68:71], v[100:103], v[116:119]\n\t" "s_waitcnt lgkmcnt(1)\n\t" \
   "v_mfma_f32_16x16x32_bf16 v[112:115], a[72:75], v[104:107], v[112:115]\n\t" "s_waitcnt lgkmcnt(0)\n\t" \
   "v_mfma_f32_16x16x32_bf16 v[116:119], a[76:79], v[108:111], v[116:119]\n\t" "v_mfma_f32_16x16x32_bf16 v[120:123], a[80:83], v[32:35], 0\n\t" \
   "v_mfma_f32_16x16x32_bf16 v[124:127], a[84:87], v[36:39], 0\n\t" "v_mfma_f32_16x16x32_bf16 v[120:123], a[88:91], v[40:43], v[120:123]\n\t" \
   "s_nop 1\n\t" "v_add_f32 v176, v112, v116\n\t" "v_sub_f32 v180, v112, v116\n\t" \
   "v_mfma_f32_16x16x32_bf16 v[124:127], a[92:95], v[44:47], v[124:127]\n\t" "v_add_f32 v177, v113, v117\n\t" "v_sub_f32 v181, v113, v117\n\t" \
   "v_mfma_f32_16x16x32_bf16 v[120:123], a[96:99], v[48:51], v[120:123]\n\t" "v_add_f32 v178, v114, v118\n\t" "v_sub_f32 v182, v114, v118\n\t" \
   "v_mfma_f32_16x16x32_bf16 v[124:127], a[100:103], v[52:55], v[124:127]\n\t" "v_add_f32 v179, v115, v119\n\t" "v_sub_f32 v183, v115, v119\n\t" \
   "v_mfma_f32_16x16x32_bf16 v[120:123], a[104:107], v[56:59], v[120:123]\n\t" "v_cvt_pk_bf16_f32 v184, v176, v177\n\t" \
   "v_cvt_pk_bf16_f32 v185, v178, v179\n\t" "v_mfma_f32_16x16x32_bf16 v[124:127], a[108:111], v[60:63], v[124:127]\n\t" \
   "v_cvt_pk_bf16_f32 v186, v180, v181\n\t" "v_cvt_pk_bf16_f32 v187, v182, v183\n\t" \
   "v_mfma_f32_16x16x32_bf16 v[120:123], a[112:115], v[64:67], v[120:123]\n\t" "v_lshlrev_b32 v176, 16, v184\n\t" \
   "v_and_b32 v177, 0xffff0000, v184\n\t" "v_mfma_f32_16x16x32_bf16 v[124:127], a[116:119], v[68:71], v[124:127]\n\t" \
   "v_lshlrev_b32 v178, 16, v185\n\t" "v_and_b32 v179, 0xffff0000, v185\n\t" \
   "v_mfma_f32_16x16x32_bf16 v[120:123], a[120:123], v[72:75], v[120:123]\n\t" "v_lshlrev_b32 v180, 16, v186\n\t" \
   "v_and_b32 v181, 0xffff0000, v186\n\t" "v_mfma_f32_16x16x32_bf16 v[124:127], a[124:127], v[76:79], v[124:127]\n\t" \
   "v_lshlrev_b32 v182, 16, v187\n\t" "v_and_b32 v183, 0xffff0000, v187\n\t" \
   "v_mfma_f32_16x16x32_bf16 v[120:123], a[128:131], v[80:83], v[120:123]\n\t" "v_add_f32 v176, v176, v152\n\t" "v_add_f32 v177, v177, v153\n\t" \
   "v_mfma_f32_16x16x32_bf16 v[124:127], a[132:135], v[84:87], v[124:127]\n\t" "v_add_f32 v178, v178, v154\n\t" "v_add_f32 v179, v179, v155\n\t" \
   "v_mfma_f32_16x16x32_bf16 v[120:123], a[136:139], v[88:91], v[120:123]\n\t" "v_add_f32 v180, v180, v156\n\t" "v_add_f32 v181, v181, v157\n\t" \
   "v_mfma_f32_16x16x32_bf16 v[124:127], a[140:143], v[92:95], v[124:127]\n\t" "v_add_f32 v182, v182, v158\n\t" "v_add_f32 v183, v183, v159\n\t" \
   "v_mfma_f32_16x16x32_bf16 v[120:123], a[144:147], v[96:99], v[120:123]\n\t" "v_cvt_pk_bf16_f32 v136, v176, v177\n\t" \
   "v_cvt_pk_bf16_f32 v137, v178, v179\n\t" "v_mfma_f32_16x16x32_bf16 v[124:127], a[148:151], v[100:103], v[124:127]\n\t" \
   "v_cvt_pk_bf16_f32 v140, v180, v181\n\t" "v_cvt_pk_bf16_f32 v141, v182, v183\n\t" \
   "v_mfma_f32_16x16x32_bf16 v[120:123], a[152:155], v[104:107], v[120:123]\n\t" \
   "v_mfma_f32_16x16x32_bf16 v[124:127], a[156:159], v[108:111], v[124:127]\n\t" "v_mfma_f32_16x16x32_bf16 v[128:131], a[160:163], v[32:35], 0\n\t" \
   "v_mfma_f32_16x16x32_bf16 v[132:135], a[164:167], v[36:39], 0\n\t" "v_mfma_f32_16x16x32_bf16 v[128:131], a[168:171], v[40:43], v[128:131]\n\t" \
   "s_nop 1\n\t" "v_add_f32 v176, v120, v124\n\t" "v_sub_f32 v180, v120, v124\n\t" \
   "v_mfma_f32_16x16x32_bf16 v[132:135], a[172:175], v[44:47], v[132:135]\n\t" "v_add_f32 v177, v121, v125\n\t" "v_sub_f32 v181, v121, v125\n\t" \
   "v_mfma_f32_16x16x32_bf16 v[128:131], a[176:179], v[48:51], v[128:131]\n\t" "v_add_f32 v178, v122, v126\n\t" "v_sub_f32 v182, v122, v126\n\t" \
   "v_mfma_f32_16x16x32_bf16 v[132:135], a[180:183], v[52:55], v[132:135]\n\t" "v_add_f32 v179, v123, v127\n\t" "v_sub_f32 v183, v123, v127\n\t" \
   "v_mfma_f32_16x16x32_bf16 v[128:131], a[184:187], v[56:59], v[128:131]\n\t" "v_cvt_pk_bf16_f32 v184, v176, v177\n\t" \
   "v_cvt_pk_bf16_f32 v185, v178, v179\n\t" "v_mfma_f32_16x16x32_bf16 v[132:135], a[188:191], v[60:63], v[132:135]\n\t" \
   "v_cvt_pk_bf16_f32 v186, v180, v181\n\t" "v_cvt_pk_bf16_f32 v187, v182, v183\n\t" \
   "v_mfma_f32_16x16x32_bf16 v[128:131], a[192:195], v[64:67], v[128:131]\n\t" "v_lshlrev_b32 v176, 16, v184\n\t" \
   "v_and_b32 v177, 0xffff0000, v184\n\t" "v_mfma_f32_16x16x32_bf16 v[132:135], a[196:199], v[68:71], v[132:135]\n\t" \
   "v_lshlrev_b32 v178, 16, v185\n\t" "v_and_b32 v179, 0xffff0000, v185\n\t" \
   "v_mfma_f32_16x16x32_bf16 v[128:131], a[200:203], v[72:75], v[128:131]\n\t" "v_lshlrev_b32 v180, 16, v186\n\t" \
   "v_and_b32 v181, 0xffff0000, v186\n\t" "v_mfma_f32_16x16x32_bf16 v[132:135], a[204:207], v[76:79], v[132:135]\n\t" \
   "v_lshlrev_b32 v182, 16, v187\n\t" "v_and_b32 v183, 0xffff0000, v187\n\t" \
   "v_mfma_f32_16x16x32_bf16 v[128:131], a[208:211], v[80:83], v[128:131]\n\t" "v_add_f32 v176, v176, v160\n\t" "v_add_f32 v177, v177, v161\n\t" \
   "v_mfma_f32_16x16x32_bf16 v[132:135], a[212:215], v[84:87], v[132:135]\n\t" "v_add_f32 v178, v178, v162\n\t" "v_add_f32 v179, v179, v163\n\t" \
   "v_mfma_f32_16x16x32_bf16 v[128:131], a[216:219], v[88:91], v[128:131]\n\t" "v_add_f32 v180, v180, v164\n\t" "v_add_f32 v181, v181, v165\n\t" \
   "v_mfma_f32_16x16x32_bf16 v[132:135], a[220:223], v[92:95], v[132:135]\n\t" "v_add_f32 v182, v182, v166\n\t" "v_add_f32 v183, v183, v167\n\t" \
   "v_mfma_f32_16x16x32_bf16 v[128:131], a[224:227], v[96:99], v[128:131]\n\t" "v_cvt_pk_bf16_f32 v138, v176, v177\n\t" \
   "v_cvt_pk_bf16_f32 v139, v178, v179\n\t" "v_mfma_f32_16x16x32_bf16 v[132:135], a[228:231], v[100:103], v[132:135]\n\t" \
   "v_cvt_pk_bf16_f32 v142, v180, v181\n\t" "v_cvt_pk_bf16_f32 v143, v182, v183\n\t" \
   "v_mfma_f32_16x16x32_bf16 v[128:131], a[232:235], v[104:107], v[128:131]\n\t" \
   "v_mfma_f32_16x16x32_bf16 v[132:135], a[236:239], v[108:111], v[132:135]\n\t" "s_nop 7\n\t" "v_add_f32 v176, v128, v132\n\t" \
   "v_sub_f32 v180, v128, v132\n\t" "v_add_f32 v177, v129, v133\n\t" "v_sub_f32 v181, v129, v133\n\t" "v_add_f32 v178, v130, v134\n\t" \
   "v_sub_f32 v182, v130, v134\n\t" "v_add_f32 v179, v131, v135\n\t" "v_sub_f32 v183, v131, v135\n\t" "v_cvt_pk_bf16_f32 v184, v176, v177\n\t" \
   "v_cvt_pk_bf16_f32 v185, v178, v179\n\t" "v_cvt_pk_bf16_f32 v186, v180, v181\n\t" "v_cvt_pk_bf16_f32 v187, v182, v183\n\t" \
   "v_lshlrev_b32 v176, 16, v184\n\t" "v_and_b32 v177, 0xffff0000, v184\n\t" "v_lshlrev_b32 v178, 16, v185\n\t" "v_and_b32 v179, 0xffff0000, v185\n\t" \
   "v_lshlrev_b32 v180, 16, v186\n\t" "v_and_b32 v181, 0xffff0000, v186\n\t" "v_lshlrev_b32 v182, 16, v187\n\t" "v_and_b32 v183, 0xffff0000, v187\n\t" \
   "v_add_f32 v176, v176, v168\n\t" "v_add_f32 v177, v177, v169\n\t" "v_add_f32 v178, v178, v170\n\t" "v_add_f32 v179, v179, v171\n\t" \
   "v_add_f32 v180, v180, v172\n\t" "v_add_f32 v181, v181, v173\n\t" "v_add_f32 v182, v182, v174\n\t" "v_add_f32 v183, v183, v175\n\t" \
   "v_cvt_pk_bf16_f32 v144, v176, v177\n\t" "v_cvt_pk_bf16_f32 v145, v178, v179\n\t" "v_cvt_pk_bf16_f32 v146, v180, v181\n\t" \
   "v_cvt_pk_bf16_f32 v147, v182, v183\n\t" "s_nop 1"
#define ID_CLOB "v32", "v33", "v34", "v35", "v36", "v37", "v38", "v39", "v40", "v41", "v42", "v43", "v44", "v45", "v46", "v47", "v48", "v49", "v50", "v51", "v52", "v53", "v54", "v55", "v56", "v57", "v58", "v59", "v60", "v61", "v62", "v63", "v64", "v65", "v66", "v67", "v68", "v69", "v70", "v71", "v72", "v73", "v74", "v75", "v76", "v77", "v78", "v79", "v80", "v81", "v82", "v83", "v84", "v85", "v86", "v87", "v88", "v89", "v90", "v91", "v92", "v93", "v94", "v95", "v96", "v97", "v98", "v99", "v100", "v101", "v102", "v103", "v104", "v105", "v106", "v107", "v108", "v109", "v110", "v111", "v112", "v113", "v114", "v115", "v116", "v117", "v118", "v119", "v120", "v121", "v122", "v123", "v124", "v125", "v126", "v127", "v128", "v129", "v130", "v131", "v132", "v133", "v134", "v135", "v152", "v153", "v154", "v155", "v156", "v157", "v158", "v159", "v160", "v161", "v162", "v163", "v164", "v165", "v166", "v167", "v168", "v169", "v170", "v171", "v172", "v173", "v174", "v175", "v176", "v177", "v178", "v179", "v180", "v181", "v182", "v183", "v184", "v185", "v186", "v187"
#define OU_ASM \
   "s_add_u32 s40, %[so], 0\n\t" "buffer_load_dwordx4 a[0:3], %[o], %[rs], s40 offen offset:0 sc1\n\t" \
   "buffer_load_dwordx4 a[32:35], %[o], %[rs], s40 offen offset:1024 sc1\n\t" \
   "buffer_load_dwordx4 a[64:67], %[o], %[rs], s40 offen offset:2048 sc1\n\t" \
   "buffer_load_dwordx4 a[96:99], %[o], %[rs], s40 offen offset:3072 sc1\n\t" "s_add_u32 s40, %[so], 4096\n\t" \
   "buffer_load_dwordx4 a[128:131], %[o], %[rs], s40 offen offset:0 sc1\n\t" \
   "buffer_load_dwordx4 a[160:163], %[o], %[rs], s40 offen offset:1024 sc1\n\t" \
   "buffer_load_dwordx4 a[192:195], %[o], %[rs], s40 offen offset:2048 sc1\n\t" \
   "buffer_load_dwordx4 a[224:227], %[o], %[rs], s40 offen offset:3072 sc1\n\t" "s_add_u32 s40, %[so], 32768\n\t" \
   "buffer_load_dwordx4 a[4:7], %[o], %[rs], s40 offen offset:0 sc1\n\t" "buffer_load_dwordx4 a[36:39], %[o], %[rs], s40 offen offset:1024 sc1\n\t" \
   "buffer_load_dwordx4 a[68:71], %[o], %[rs], s40 offen offset:2048 sc1\n\t" \
   "buffer_load_dwordx4 a[100:103], %[o], %[rs], s40 offen offset:3072 sc1\n\t" "s_add_u32 s40, %[so], 36864\n\t" \
   "buffer_load_dwordx4 a[132:135], %[o], %[rs], s40 offen offset:0 sc1\n\t" \
   "buffer_load_dwordx4 a[164:167], %[o], %[rs], s40 offen offset:1024 sc1\n\t" \
   "buffer_load_dwordx4 a[196:199], %[o], %[rs], s40 offen offset:2048 sc1\n\t" \
   "buffer_load_dwordx4 a[228:231], %[o], %[rs], s40 offen offset:3072 sc1\n\t" "s_add_u32 s40, %[so], 65536\n\t" \
   "buffer_load_dwordx4 a[8:11], %[o], %[rs], s40 offen offset:0 sc1\n\t" "buffer_load_dwordx4 a[40:43], %[o], %[rs], s40 offen offset:1024 sc1\n\t" \
   "buffer_load_dwordx4 a[72:75], %[o], %[rs], s40 offen offset:2048 sc1\n\t" \
   "buffer_load_dwordx4 a[104:107], %[o], %[rs], s40 offen offset:3072 sc1\n\t" "s_add_u32 s40, %[so], 69632\n\t" \
   "buffer_load_dwordx4 a[136:139], %[o], %[rs], s40 offen offset:0 sc1\n\t" \
   "buffer_load_dwordx4 a[168:171], %[o], %[rs], s40 offen offset:1024 sc1\n\t" \
   "buffer_load_dwordx4 a[200:203], %[o], %[rs], s40 offen offset:2048 sc1\n\t" \
   "buffer_load_dwordx4 a[232:235], %[o], %[rs], s40 offen offset:3072 sc1\n\t" "s_add_u32 s40, %[so], 98304\n\t" \
   "buffer_load_dwordx4 a[12:15], %[o], %[rs], s40 offen offset:0 sc1\n\t" "buffer_load_dwordx4 a[44:47], %[o], %[rs], s40 offen offset:1024 sc1\n\t" \
   "buffer_load_dwordx4 a[76:79], %[o], %[rs], s40 offen offset:2048 sc1\n\t" \
   "buffer_load_dwordx4 a[108:111], %[o], %[rs], s40 offen offset:3072 sc1\n\t" "s_add_u32 s40, %[so], 102400\n\t" \
   "buffer_load_dwordx4 a[140:143], %[o], %[rs], s40 offen offset:0 sc1\n\t" \
   "buffer_load_dwordx4 a[172:175], %[o], %[rs], s40 offen offset:1024 sc1\n\t" \
   "buffer_load_dwordx4 a[204:207], %[o], %[rs], s40 offen offset:2048 sc1\n\t" \
   "buffer_load_dwordx4 a[236:239], %[o], %[rs], s40 offen offset:3072 sc1\n\t" "s_add_u32 s40, %[so], 131072\n\t" \
   "buffer_load_dwordx4 a[16:19], %[o], %[rs], s40 offen offset:0 sc1\n\t" "buffer_load_dwordx4 a[48:51], %[o], %[rs], s40 offen offset:1024 sc1\n\t" \
   "buffer_load_dwordx4 a[80:83], %[o], %[rs], s40 offen offset:2048 sc1\n\t" \
   "buffer_load_dwordx4 a[112:115], %[o], %[rs], s40 offen offset:3072 sc1\n\t" "s_add_u32 s40, %[so], 135168\n\t" \
   "buffer_load_dwordx4 a[144:147], %[o], %[rs], s40 offen offset:0 sc1\n\t" \
   "buffer_load_dwordx4 a[176:179], %[o], %[rs], s40 offen offset:1024 sc1\n\t" \
   "buffer_load_dwordx4 a[208:211], %[o], %[rs], s40 offen offset:2048 sc1\n\t" \
   "buffer_load_dwordx4 a[240:243], %[o], %[rs], s40 offen offset:3072 sc1\n\t" "s_add_u32 s40, %[so], 163840\n\t" \
   "buffer_load_dwordx4 a[20:23], %[o], %[rs], s40 offen offset:0 sc1\n\t" "buffer_load_dwordx4 a[52:55], %[o], %[rs], s40 offen offset:1024 sc1\n\t" \
   "buffer_load_dwordx4 a[84:87], %[o], %[rs], s40 offen offset:2048 sc1\n\t" \
   "buffer_load_dwordx4 a[116:119], %[o], %[rs], s40 offen offset:3072 sc1\n\t" "s_add_u32 s40, %[so], 167936\n\t" \
   "buffer_load_dwordx4 a[148:151], %[o], %[rs], s40 offen offset:0 sc1\n\t" \
   "buffer_load_dwordx4 a[180:183], %[o], %[rs], s40 offen offset:1024 sc1\n\t" \
   "buffer_load_dwordx4 a[212:215], %[o], %[rs], s40 offen offset:2048 sc1\n\t" \
   "buffer_load_dwordx4 a[244:247], %[o], %[rs], s40 offen offset:3072 sc1\n\t" "s_add_u32 s40, %[so], 196608\n\t" \
   "buffer_load_dwordx4 a[24:27], %[o], %[rs], s40 offen offset:0 sc1\n\t" "buffer_load_dwordx4 a[56:59], %[o], %[rs], s40 offen offset:1024 sc1\n\t" \
   "buffer_load_dwordx4 a[88:91], %[o], %[rs], s40 offen offset:2048 sc1\n\t" \
   "buffer_load_dwordx4 a[120:123], %[o], %[rs], s40 offen offset:3072 sc1\n\t" "s_add_u32 s40, %[so], 200704\n\t" \
   "buffer_load_dwordx4 a[152:155], %[o], %[rs], s40 offen offset:0 sc1\n\t" \
   "buffer_load_dwordx4 a[184:187], %[o], %[rs], s40 offen offset:1024 sc1\n\t" \
   "buffer_load_dwordx4 a[216:219], %[o], %[rs], s40 offen offset:2048 sc1\n\t" \
   "buffer_load_dwordx4 a[248:251], %[o], %[rs], s40 offen offset:3072 sc1\n\t" "s_add_u32 s40, %[so], 229376\n\t" \
   "buffer_load_dwordx4 a[28:31], %[o], %[rs], s40 offen offset:0 sc1\n\t" "buffer_load_dwordx4 a[60:63], %[o], %[rs], s40 offen offset:1024 sc1\n\t" \
   "buffer_load_dwordx4 a[92:95], %[o], %[rs], s40 offen offset:2048 sc1\n\t" \
   "buffer_load_dwordx4 a[124:127], %[o], %[rs], s40 offen offset:3072 sc1\n\t" "s_add_u32 s40, %[so], 233472\n\t" \
   "buffer_load_dwordx4 a[156:159], %[o], %[rs], s40 offen offset:0 sc1\n\t" \
   "buffer_load_dwordx4 a[188:191], %[o], %[rs], s40 offen offset:1024 sc1\n\t" \
   "buffer_load_dwordx4 a[220:223], %[o], %[rs], s40 offen offset:2048 sc1\n\t" \
   "buffer_load_dwordx4 a[252:255], %[o], %[rs], s40 offen offset:3072 sc1\n\t" "s_waitcnt vmcnt(0)"
#define OU_CLOB "s40"
#define OY_ASM \
   "s_add_u32 s40, %[sy], 0\n\t" "buffer_load_dwordx4 v[192:195], %[yo], %[rs], s40 offen offset:0 sc1\n\t" "s_add_u32 s40, %[sy], 16384\n\t" \
   "buffer_load_dwordx4 v[196:199], %[yo], %[rs], s40 offen offset:0 sc1\n\t" "s_add_u32 s40, %[sy], 32768\n\t" \
   "buffer_load_dwordx4 v[200:203], %[yo], %[rs], s40 offen offset:0 sc1\n\t" "s_add_u32 s40, %[sy], 49152\n\t" \
   "buffer_load_dwordx4 v[204:207], %[yo], %[rs], s40 offen offset:0 sc1\n\t" "s_add_u32 s40, %[sy], 65536\n\t" \
   "buffer_load_dwordx4 v[208:211], %[yo], %[rs], s40 offen offset:0 sc1\n\t" "s_add_u32 s40, %[sy], 81920\n\t" \
   "buffer_load_dwordx4 v[212:215], %[yo], %[rs], s40 offen offset:0 sc1\n\t" "s_add_u32 s40, %[sy], 98304\n\t" \
   "buffer_load_dwordx4 v[216:219], %[yo], %[rs], s40 offen offset:0 sc1\n\t" "s_add_u32 s40, %[sy], 114688\n\t" \
   "buffer_load_dwordx4 v[220:223], %[yo], %[rs], s40 offen offset:0 sc1\n\t" "s_add_u32 s40, %[sy], 0\n\t" \
   "buffer_load_dwordx4 v[224:227], %[yo], %[rs], s40 offen offset:1024 sc1\n\t" "s_add_u32 s40, %[sy], 16384\n\t" \
   "buffer_load_dwordx4 v[228:231], %[yo], %[rs], s40 offen offset:1024 sc1\n\t" "s_add_u32 s40, %[sy], 32768\n\t" \
   "buffer_load_dwordx4 v[232:235], %[yo], %[rs], s40 offen offset:1024 sc1\n\t" "s_add_u32 s40, %[sy], 49152\n\t" \
   "buffer_load_dwordx4 v[236:239], %[yo], %[rs], s40 offen offset:1024 sc1\n\t" "s_add_u32 s40, %[sy], 65536\n\t" \
   "buffer_load_dwordx4 v[240:243], %[yo], %[rs], s40 offen offset:1024 sc1\n\t" "s_add_u32 s40, %[sy], 81920\n\t" \
   "buffer_load_dwordx4 v[244:247], %[yo], %[rs], s40 offen offset:1024 sc1\n\t" "s_add_u32 s40, %[sy], 98304\n\t" \
   "buffer_load_dwordx4 v[248:251], %[yo], %[rs], s40 offen offset:1024 sc1\n\t" "s_add_u32 s40, %[sy], 114688\n\t" \
   "buffer_load_dwordx4 v[252:255], %[yo], %[rs], s40 offen offset:1024 sc1\n\t" "v_mov_b32 %[m], 0\n\t" "s_waitcnt vmcnt(15)\n\t" \
   "v_max3_u32 %[m], %[m], v192, v194\n\t" "s_waitcnt vmcnt(14)\n\t" "v_max3_u32 %[m], %[m], v196, v198\n\t" "s_waitcnt vmcnt(13)\n\t" \
   "v_max3_u32 %[m], %[m], v200, v202\n\t" "s_waitcnt vmcnt(12)\n\t" "v_max3_u32 %[m], %[m], v204, v206\n\t" "s_waitcnt vmcnt(11)\n\t" \
   "v_max3_u32 %[m], %[m], v208, v210\n\t" "s_waitcnt vmcnt(10)\n\t" "v_max3_u32 %[m], %[m], v212, v214\n\t" "s_waitcnt vmcnt(9)\n\t" \
   "v_max3_u32 %[m], %[m], v216, v218\n\t" "s_waitcnt vmcnt(8)\n\t" "v_max3_u32 %[m], %[m], v220, v222\n\t" "s_waitcnt vmcnt(7)\n\t" \
   "v_max3_u32 %[m], %[m], v224, v226\n\t" "s_waitcnt vmcnt(6)\n\t" "v_max3_u32 %[m], %[m], v228, v230\n\t" "s_waitcnt vmcnt(5)\n\t" \
   "v_max3_u32 %[m], %[m], v232, v234\n\t" "s_waitcnt vmcnt(4)\n\t" "v_max3_u32 %[m], %[m], v236, v238\n\t" "s_waitcnt vmcnt(3)\n\t" \
   "v_max3_u32 %[m], %[m], v240, v242\n\t" "s_waitcnt vmcnt(2)\n\t" "v_max3_u32 %[m], %[m], v244, v246\n\t" "s_waitcnt vmcnt(1)\n\t" \
   "v_max3_u32 %[m], %[m], v248, v250\n\t" "s_waitcnt vmcnt(0)\n\t" "v_max3_u32 %[m], %[m], v252, v254\n\t" "s_nop 0"
#define OY_CLOB "s40"
#define O1_ASM \
   "s_add_u32 s40, %[sp], 0\n\t" "buffer_load_dwordx4 v[112:115], %[po], %[rs], s40 offen offset:0 sc1\n\t" \
   "buffer_load_dwordx4 v[116:119], %[po], %[rs], s40 offen offset:2048 sc1\n\t" "s_add_u32 s40, %[sp], 4096\n\t" \
   "buffer_load_dwordx4 v[120:123], %[po], %[rs], s40 offen offset:0 sc1\n\t" \
   "buffer_load_dwordx4 v[124:127], %[po], %[rs], s40 offen offset:2048 sc1\n\t" "s_add_u32 s40, %[sp], 8192\n\t" \
   "buffer_load_dwordx4 v[128:131], %[po], %[rs], s40 offen offset:0 sc1\n\t" \
   "buffer_load_dwordx4 v[132:135], %[po], %[rs], s40 offen offset:2048 sc1\n\t" "s_add_u32 s40, %[sp], 12288\n\t" \
   "buffer_load_dwordx4 v[136:139], %[po], %[rs], s40 offen offset:0 sc1\n\t" \
   "buffer_load_dwordx4 v[140:143], %[po], %[rs], s40 offen offset:2048 sc1\n\t" \
   "buffer_load_dwordx4 v[144:147], %[mo], %[rs], %[sm] offen offset:0 sc1\n\t" \
   "buffer_load_dwordx4 v[148:151], %[mo], %[rs], %[sm] offen offset:2048 sc1\n\t" "v_mov_b32 v160, 16\n\t" \
   "buffer_atomic_smax %[at], %[ao], %[rs], 0 offen offset:4 sc1\n\t" "buffer_atomic_add v160, %[ao], %[rs], 0 offen sc1\n\t" "s_waitcnt vmcnt(11)\n\t" \
   "v_mfma_f32_16x16x32_bf16 v[32:35], a[0:3], v[112:115], 0\n\t" "v_mfma_f32_16x16x32_bf16 v[36:39], a[4:7], v[112:115], 0\n\t" \
   "v_mfma_f32_16x16x32_bf16 v[40:43], a[8:11], v[112:115], 0\n\t" "v_mfma_f32_16x16x32_bf16 v[44:47], a[12:15], v[112:115], 0\n\t" \
   "v_mfma_f32_16x16x32_bf16 v[48:51], a[16:19], v[112:115], 0\n\t" "v_mfma_f32_16x16x32_bf16 v[52:55], a[20:23], v[112:115], 0\n\t" \
   "v_mfma_f32_16x16x32_bf16 v[56:59], a[24:27], v[112:115], 0\n\t" "v_mfma_f32_16x16x32_bf16 v[60:63], a[28:31], v[112:115], 0\n\t" \
   "s_waitcnt vmcnt(10)\n\t" "v_mfma_f32_16x16x32_bf16 v[32:35], a[32:35], v[116:119], v[32:35]\n\t" \
   "v_mfma_f32_16x16x32_bf16 v[36:39], a[36:39], v[116:119], v[36:39]\n\t" "v_mfma_f32_16x16x32_bf16 v[40:43], a[40:43], v[116:119], v[40:43]\n\t" \
   "v_mfma_f32_16x16x32_bf16 v[44:47], a[44:47], v[116:119], v[44:47]\n\t" "v_mfma_f32_16x16x32_bf16 v[48:51], a[48:51], v[116:119], v[48:51]\n\t" \
   "v_mfma_f32_16x16x32_bf16 v[52:55], a[52:55], v[116:119], v[52:55]\n\t" "v_mfma_f32_16x16x32_bf16 v[56:59], a[56:59], v[116:119], v[56:59]\n\t" \
   "v_mfma_f32_16x16x32_bf16 v[60:63], a[60:63], v[116:119], v[60:63]\n\t" "s_waitcnt vmcnt(9)\n\t" \
   "v_mfma_f32_16x16x32_bf16 v[32:35], a[64:67], v[120:123], v[32:35]\n\t" "v_mfma_f32_16x16x32_bf16 v[36:39], a[68:71], v[120:123], v[36:39]\n\t" \
   "v_mfma_f32_16x16x32_bf16 v[40:43], a[72:75], v[120:123], v[40:43]\n\t" "v_mfma_f32_16x16x32_bf16 v[44:47], a[76:79], v[120:123], v[44:47]\n\t" \
   "v_mfma_f32_16x16x32_bf16 v[48:51], a[80:83], v[120:123], v[48:51]\n\t" "v_mfma_f32_16x16x32_bf16 v[52:55], a[84:87], v[120:123], v[52:55]\n\t" \
   "v_mfma_f32_16x16x32_bf16 v[56:59], a[88:91], v[120:123], v[56:59]\n\t" "v_mfma_f32_16x16x32_bf16 v[60:63], a[92:95], v[120:123], v[60:63]\n\t" \
   "s_waitcnt vmcnt(8)\n\t" "v_mfma_f32_16x16x32_bf16 v[32:35], a[96:99], v[124:127], v[32:35]\n\t" \
   "v_mfma_f32_16x16x32_bf16 v[36:39], a[100:103], v[124:127], v[36:39]\n\t" "v_mfma_f32_16x16x32_bf16 v[40:43], a[104:107], v[124:127], v[40:43]\n\t" \
   "v_mfma_f32_16x16x32_bf16 v[44:47], a[108:111], v[124:127], v[44:47]\n\t" "v_mfma_f32_16x16x32_bf16 v[48:51], a[112:115], v[124:127], v[48:51]\n\t" \
   "v_mfma_f32_16x16x32_bf16 v[52:55], a[116:119], v[124:127], v[52:55]\n\t" "v_mfma_f32_16x16x32_bf16 v[56:59], a[120:123], v[124:127], v[56:59]\n\t" \
   "v_mfma_f32_16x16x32_bf16 v[60:63], a[124:127], v[124:127], v[60:63]\n\t" "s_waitcnt vmcnt(7)\n\t" \
   "v_mfma_f32_16x16x32_bf16 v[32:35], a[128:131], v[128:131], v[32:35]\n\t" "v_mfma_f32_16x16x32_bf16 v[36:39], a[132:135], v[128:131], v[36:39]\n\t" \
   "v_mfma_f32_16x16x32_bf16 v[40:43], a[136:139], v[128:131], v[40:43]\n\t" "v_mfma_f32_16x16x32_bf16 v[44:47], a[140:143], v[128:131], v[44:47]\n\t" \
   "v_mfma_f32_16x16x32_bf16 v[48:51], a[144:147], v[128:131], v[48:51]\n\t" "v_mfma_f32_16x16x32_bf16 v[52:55], a[148:151], v[128:131], v[52:55]\n\t" \
   "v_mfma_f32_16x16x32_bf16 v[56:59], a[152:155], v[128:131], v[56:59]\n\t" "v_mfma_f32_16x16x32_bf16 v[60:63], a[156:159], v[128:131], v[60:63]\n\t" \
   "s_waitcnt vmcnt(6)\n\t" "v_mfma_f32_16x16x32_bf16 v[32:35], a[160:163], v[132:135], v[32:35]\n\t" \
   "v_mfma_f32_16x16x32_bf16 v[36:39], a[164:167], v[132:135], v[36:39]\n\t" "v_mfma_f32_16x16x32_bf16 v[40:43], a[168:171], v[132:135], v[40:43]\n\t" \
   "v_mfma_f32_16x16x32_bf16 v[44:47], a[172:175], v[132:135], v[44:47]\n\t" "v_mfma_f32_16x16x32_bf16 v[48:51], a[176:179], v[132:135], v[48:51]\n\t" \
   "v_mfma_f32_16x16x32_bf16 v[52:55], a[180:183], v[132:135], v[52:55]\n\t" "v_mfma_f32_16x16x32_bf16 v[56:59], a[184:187], v[132:135], v[56:59]\n\t" \
   "v_mfma_f32_16x16x32_bf16 v[60:63], a[188:191], v[132:135], v[60:63]\n\t" "s_waitcnt vmcnt(5)\n\t" \
   "v_mfma_f32_16x16x32_bf16 v[32:35], a[192:195], v[136:139], v[32:35]\n\t" "v_mfma_f32_16x16x32_bf16 v[36:39], a[196:199], v[136:139], v[36:39]\n\t" \
   "v_mfma_f32_16x16x32_bf16 v[40:43], a[200:203], v[136:139], v[40:43]\n\t" "v_mfma_f32_16x16x32_bf16 v[44:47], a[204:207], v[136:139], v[44:47]\n\t" \
   "v_mfma_f32_16x16x32_bf16 v[48:51], a[208:211], v[136:139], v[48:51]\n\t" "v_mfma_f32_16x16x32_bf16 v[52:55], a[212:215], v[136:139], v[52:55]\n\t" \
   "v_mfma_f32_16x16x32_bf16 v[56:59], a[216:219], v[136:139], v[56:59]\n\t" "v_mfma_f32_16x16x32_bf16 v[60:63], a[220:223], v[136:139], v[60:63]\n\t" \
   "s_waitcnt vmcnt(4)\n\t" "s_waitcnt vmcnt(2)\n\t" "v_mfma_f32_16x16x32_bf16 v[32:35], a[224:227], v[140:143], v[32:35]\n\t" \
   "v_swap_b32 v145, v146\n\t" "v_mfma_f32_16x16x32_bf16 v[36:39], a[228:231], v[140:143], v[36:39]\n\t" "v_swap_b32 v149, v150\n\t" \
   "v_mfma_f32_16x16x32_bf16 v[40:43], a[232:235], v[140:143], v[40:43]\n\t" "v_mfma_f32_16x16x32_bf16 v[44:47], a[236:239], v[140:143], v[44:47]\n\t" \
   "v_mfma_f32_16x16x32_bf16 v[48:51], a[240:243], v[140:143], v[48:51]\n\t" "v_mfma_f32_16x16x32_bf16 v[52:55], a[244:247], v[140:143], v[52:55]\n\t" \
   "v_mfma_f32_16x16x32_bf16 v[56:59], a[248:251], v[140:143], v[56:59]\n\t" "v_mfma_f32_16x16x32_bf16 v[60:63], a[252:255], v[140:143], v[60:63]\n\t" \
   "v_mfma_f32_16x16x32_bf16 v[32:35], v[192:195], v[144:147], v[32:35]\n\t" "v_mfma_f32_16x16x32_bf16 v[36:39], v[196:199], v[144:147], v[36:39]\n\t" \
   "v_mfma_f32_16x16x32_bf16 v[40:43], v[200:203], v[144:147], v[40:43]\n\t" "v_mfma_f32_16x16x32_bf16 v[44:47], v[204:207], v[144:147], v[44:47]\n\t" \
   "v_mfma_f32_16x16x32_bf16 v[48:51], v[208:211], v[144:147], v[48:51]\n\t" "v_mfma_f32_16x16x32_bf16 v[52:55], v[212:215], v[144:147], v[52:55]\n\t" \
   "v_mfma_f32_16x16x32_bf16 v[56:59], v[216:219], v[144:147], v[56:59]\n\t" "v_mfma_f32_16x16x32_bf16 v[60:63], v[220:223], v[144:147], v[60:63]\n\t" \
   "v_mfma_f32_16x16x32_bf16 v[32:35], v[224:227], v[148:151], v[32:35]\n\t" "v_mfma_f32_16x16x32_bf16 v[36:39], v[228:231], v[148:151], v[36:39]\n\t" \
   "v_mfma_f32_16x16x32_bf16 v[40:43], v[232:235], v[148:151], v[40:43]\n\t" "v_mfma_f32_16x16x32_bf16 v[44:47], v[236:239], v[148:151], v[44:47]\n\t" \
   "ds_write_b128 %[lw], v[32:35] offset:0\n\t" "v_mfma_f32_16x16x32_bf16 v[48:51], v[240:243], v[148:151], v[48:51]\n\t" \
   "ds_write_b128 %[lw], v[36:39] offset:1024\n\t" "v_mfma_f32_16x16x32_bf16 v[52:55], v[244:247], v[148:151], v[52:55]\n\t" \
   "ds_write_b128 %[lw], v[40:43] offset:2048\n\t" "v_mfma_f32_16x16x32_bf16 v[56:59], v[248:251], v[148:151], v[56:59]\n\t" \
   "ds_write_b128 %[lw], v[44:47] offset:3072\n\t" "v_mfma_f32_16x16x32_bf16 v[60:63], v[252:255], v[148:151], v[60:63]\n\t" \
   "ds_write_b128 %[lw], v[48:51] offset:4096\n\t" "s_nop 1\n\t" "s_nop 3\n\t" "ds_write_b128 %[lw], v[52:55] offset:5120\n\t" "s_nop 3\n\t" \
   "ds_write_b128 %[lw], v[56:59] offset:6144\n\t" "s_nop 3\n\t" "ds_write_b128 %[lw], v[60:63] offset:7168\n\t" "s_waitcnt lgkmcnt(0)\n\t" \
   "s_barrier\n\t" "ds_read_b128 v[112:115], %[lr] offset:0\n\t" "ds_read_b128 v[116:119], %[lr] offset:256\n\t" \
   "ds_read_b128 v[120:123], %[lr] offset:8192\n\t" "ds_read_b128 v[124:127], %[lr] offset:8448\n\t" "ds_read_b128 v[128:131], %[lr] offset:16384\n\t" \
   "ds_read_b128 v[132:135], %[lr] offset:16640\n\t" "ds_read_b128 v[136:139], %[lr] offset:24576\n\t" \
   "ds_read_b128 v[140:143], %[lr] offset:24832\n\t" "s_waitcnt lgkmcnt(4)\n\t" "v_add_f32 v112, v112, v120\n\t" "v_add_f32 v113, v113, v121\n\t" \
   "v_add_f32 v114, v114, v122\n\t" "v_add_f32 v115, v115, v123\n\t" "v_add_f32 v116, v116, v124\n\t" "v_add_f32 v117, v117, v125\n\t" \
   "v_add_f32 v118, v118, v126\n\t" "v_add_f32 v119, v119, v127\n\t" "s_waitcnt lgkmcnt(0)\n\t" "v_add_f32 v128, v128, v136\n\t" \
   "v_add_f32 v129, v129, v137\n\t" "v_add_f32 v130, v130, v138\n\t" "v_add_f32 v131, v131, v139\n\t" "v_add_f32 v132, v132, v140\n\t" \
   "v_add_f32 v133, v133, v141\n\t" "v_add_f32 v134, v134, v142\n\t" "v_add_f32 v135, v135, v143\n\t" "v_add_f32 v112, v112, v128\n\t" \
   "v_add_f32 v113, v113, v129\n\t" "v_add_f32 v114, v114, v130\n\t" "v_add_f32 v115, v115, v131\n\t" "v_add_f32 v116, v116, v132\n\t" \
   "v_add_f32 v117, v117, v133\n\t" "v_add_f32 v118, v118, v134\n\t" "v_add_f32 v119, v119, v135\n\t" "v_cvt_pk_bf16_f32 v96, v112, v113\n\t" \
   "v_cvt_pk_bf16_f32 v97, v114, v115\n\t" "v_cvt_pk_bf16_f32 v98, v116, v117\n\t" "v_cvt_pk_bf16_f32 v99, v118, v119\n\t" "s_nop 1"
#define O1_CLOB "v32", "v33", "v34", "v35", "v36", "v37", "v38", "v39", "v40", "v41", "v42", "v43", "v44", "v45", "v46", "v47", "v48", "v49", "v50", "v51", "v52", "v53", "v54", "v55", "v56", "v57", "v58", "v59", "v60", "v61", "v62", "v63", "v64", "v65", "v66", "v67", "v68", "v69", "v70", "v71", "v72", "v73", "v74", "v75", "v76", "v77", "v78", "v79", "v80", "v81", "v82", "v83", "v84", "v85", "v86", "v87", "v88", "v89", "v90", "v91", "v92", "v93", "v94", "v95", "v112", "v113", "v114", "v115", "v116", "v117", "v118", "v119", "v120", "v121", "v122", "v123", "v124", "v125", "v126", "v127", "v128", "v129", "v130", "v131", "v132", "v133", "v134", "v135", "v136", "v137", "v138", "v139", "v140", "v141", "v142", "v143", "v144", "v145", "v146", "v147", "v148", "v149", "v150", "v151", "v152", "v153", "v154", "v155", "v156", "v157", "v158", "v159", "v160", "v161", "v162", "v163", "v164", "v165", "v166", "v167", "v168", "v169", "v170", "v171", "v172", "v173", "v174", "v175", "v176", "v177", "v178", "v179", "v180", "v181", "v182", "v183", "v184", "v185", "v186", "v187", "v188", "v189", "v190", "v191", "s40"
#define O2_ASM \
   "s_add_u32 s40, %[sp], 0\n\t" "buffer_load_dwordx4 v[112:115], %[po], %[rs], s40 offen offset:0 sc1\n\t" \
   "buffer_load_dwordx4 v[116:119], %[po], %[rs], s40 offen offset:2048 sc1\n\t" "s_add_u32 s40, %[sp], 4096\n\t" \
   "buffer_load_dwordx4 v[120:123], %[po], %[rs], s40 offen offset:0 sc1\n\t" \
   "buffer_load_dwordx4 v[124:127], %[po], %[rs], s40 offen offset:2048 sc1\n\t" "s_add_u32 s40, %[sp], 8192\n\t" \
   "buffer_load_dwordx4 v[128:131], %[po], %[rs], s40 offen offset:0 sc1\n\t" \
   "buffer_load_dwordx4 v[132:135], %[po], %[rs], s40 offen offset:2048 sc1\n\t" "s_add_u32 s40, %[sp], 12288\n\t" \
   "buffer_load_dwordx4 v[136:139], %[po], %[rs], s40 offen offset:0 sc1\n\t" \
   "buffer_load_dwordx4 v[140:143], %[po], %[rs], s40 offen offset:2048 sc1\n\t" "s_add_u32 s40, %[sp], 0\n\t" \
   "buffer_load_dwordx4 v[152:155], %[po], %[rs], s40 offen offset:1024 sc1\n\t" \
   "buffer_load_dwordx4 v[156:159], %[po], %[rs], s40 offen offset:3072 sc1\n\t" "s_add_u32 s40, %[sp], 4096\n\t" \
   "buffer_load_dwordx4 v[160:163], %[po], %[rs], s40 offen offset:1024 sc1\n\t" \
   "buffer_load_dwordx4 v[164:167], %[po], %[rs], s40 offen offset:3072 sc1\n\t" "s_add_u32 s40, %[sp], 8192\n\t" \
   "buffer_load_dwordx4 v[168:171], %[po], %[rs], s40 offen offset:1024 sc1\n\t" \
   "buffer_load_dwordx4 v[172:175], %[po], %[rs], s40 offen offset:3072 sc1\n\t" "s_add_u32 s40, %[sp], 12288\n\t" \
   "buffer_load_dwordx4 v[176:179], %[po], %[rs], s40 offen offset:1024 sc1\n\t" \
   "buffer_load_dwordx4 v[180:183], %[po], %[rs], s40 offen offset:3072 sc1\n\t" \
   "buffer_load_dwordx4 v[144:147], %[mo], %[rs], %[sm] offen offset:0 sc1\n\t" \
   "buffer_load_dwordx4 v[148:151], %[mo], %[rs], %[sm] offen offset:2048 sc1\n\t" \
   "buffer_load_dwordx4 v[184:187], %[mo], %[rs], %[sm] offen offset:1024 sc1\n\t" \
   "buffer_load_dwordx4 v[188:191], %[mo], %[rs], %[sm] offen offset:3072 sc1\n\t" "s_add_u32 s40, %[sy], 0\n\t" \
   "buffer_load_dwordx4 v[192:195], %[yo], %[rs], s40 offen offset:0 sc1\n\t" "s_add_u32 s40, %[sy], 16384\n\t" \
   "buffer_load_dwordx4 v[196:199], %[yo], %[rs], s40 offen offset:0 sc1\n\t" "s_add_u32 s40, %[sy], 32768\n\t" \
   "buffer_load_dwordx4 v[200:203], %[yo], %[rs], s40 offen offset:0 sc1\n\t" "s_add_u32 s40, %[sy], 49152\n\t" \
   "buffer_load_dwordx4 v[204:207], %[yo], %[rs], s40 offen offset:0 sc1\n\t" "s_add_u32 s40, %[sy], 65536\n\t" \
   "buffer_load_dwordx4 v[208:211], %[yo], %[rs], s40 offen offset:0 sc1\n\t" "s_add_u32 s40, %[sy], 81920\n\t" \
   "buffer_load_dwordx4 v[212:215], %[yo], %[rs], s40 offen offset:0 sc1\n\t" "s_add_u32 s40, %[sy], 98304\n\t" \
   "buffer_load_dwordx4 v[216:219], %[yo], %[rs], s40 offen offset:0 sc1\n\t" "s_add_u32 s40, %[sy], 114688\n\t" \
   "buffer_load_dwordx4 v[220:223], %[yo], %[rs], s40 offen offset:0 sc1\n\t" "s_add_u32 s40, %[sy], 0\n\t" \
   "buffer_load_dwordx4 v[224:227], %[yo], %[rs], s40 offen offset:1024 sc1\n\t" "s_add_u32 s40, %[sy], 16384\n\t" \
   "buffer_load_dwordx4 v[228:231], %[yo], %[rs], s40 offen offset:1024 sc1\n\t" "s_add_u32 s40, %[sy], 32768\n\t" \
   "buffer_load_dwordx4 v[232:235], %[yo], %[rs], s40 offen offset:1024 sc1\n\t" "s_add_u32 s40, %[sy], 49152\n\t" \
   "buffer_load_dwordx4 v[236:239], %[yo], %[rs], s40 offen offset:1024 sc1\n\t" "s_add_u32 s40, %[sy], 65536\n\t" \
   "buffer_load_dwordx4 v[240:243], %[yo], %[rs], s40 offen offset:1024 sc1\n\t" "s_add_u32 s40, %[sy], 81920\n\t" \
   "buffer_load_dwordx4 v[244:247], %[yo], %[rs], s40 offen offset:1024 sc1\n\t" "s_add_u32 s40, %[sy], 98304\n\t" \
   "buffer_load_dwordx4 v[248:251], %[yo], %[rs], s40 offen offset:1024 sc1\n\t" "s_add_u32 s40, %[sy], 114688\n\t" \
   "buffer_load_dwordx4 v[252:255], %[yo], %[rs], s40 offen offset:1024 sc1\n\t" "s_waitcnt vmcnt(35)\n\t" \
   "v_mfma_f32_16x16x32_bf16 v[32:35], a[0:3], v[112:115], 0\n\t" "v_mfma_f32_16x16x32_bf16 v[36:39], a[4:7], v[112:115], 0\n\t" \
   "v_mfma_f32_16x16x32_bf16 v[40:43], a[8:11], v[112:115], 0\n\t" "v_mfma_f32_16x16x32_bf16 v[44:47], a[12:15], v[112:115], 0\n\t" \
   "v_mfma_f32_16x16x32_bf16 v[48:51], a[16:19], v[112:115], 0\n\t" "v_mfma_f32_16x16x32_bf16 v[52:55], a[20:23], v[112:115], 0\n\t" \
   "v_mfma_f32_16x16x32_bf16 v[56:59], a[24:27], v[112:115], 0\n\t" "v_mfma_f32_16x16x32_bf16 v[60:63], a[28:31], v[112:115], 0\n\t" \
   "s_waitcnt vmcnt(34)\n\t" "v_mfma_f32_16x16x32_bf16 v[32:35], a[32:35], v[116:119], v[32:35]\n\t" \
   "v_mfma_f32_16x16x32_bf16 v[36:39], a[36:39], v[116:119], v[36:39]\n\t" "v_mfma_f32_16x16x32_bf16 v[40:43], a[40:43], v[116:119], v[40:43]\n\t" \
   "v_mfma_f32_16x16x32_bf16 v[44:47], a[44:47], v[116:119], v[44:47]\n\t" "v_mfma_f32_16x16x32_bf16 v[48:51], a[48:51], v[116:119], v[48:51]\n\t" \
   "v_mfma_f32_16x16x32_bf16 v[52:55], a[52:55], v[116:119], v[52:55]\n\t" "v_mfma_f32_16x16x32_bf16 v[56:59], a[56:59], v[116:119], v[56:59]\n\t" \
   "v_mfma_f32_16x16x32_bf16 v[60:63], a[60:63], v[116:119], v[60:63]\n\t" "s_waitcnt vmcnt(33)\n\t" \
   "v_mfma_f32_16x16x32_bf16 v[32:35], a[64:67], v[120:123], v[32:35]\n\t" "v_mfma_f32_16x16x32_bf16 v[36:39], a[68:71], v[120:123], v[36:39]\n\t" \
   "v_mfma_f32_16x16x32_bf16 v[40:43], a[72:75], v[120:123], v[40:43]\n\t" "v_mfma_f32_16x16x32_bf16 v[44:47], a[76:79], v[120:123], v[44:47]\n\t" \
   "v_mfma_f32_16x16x32_bf16 v[48:51], a[80:83], v[120:123], v[48:51]\n\t" "v_mfma_f32_16x16x32_bf16 v[52:55], a[84:87], v[120:123], v[52:55]\n\t" \
   "v_mfma_f32_16x16x32_bf16 v[56:59], a[88:91], v[120:123], v[56:59]\n\t" "v_mfma_f32_16x16x32_bf16 v[60:63], a[92:95], v[120:123], v[60:63]\n\t" \
   "s_waitcnt vmcnt(32)\n\t" "v_mfma_f32_16x16x32_bf16 v[32:35], a[96:99], v[124:127], v[32:35]\n\t" \
   "v_mfma_f32_16x16x32_bf16 v[36:39], a[100:103], v[124:127], v[36:39]\n\t" "v_mfma_f32_16x16x32_bf16 v[40:43], a[104:107], v[124:127], v[40:43]\n\t" \
   "v_mfma_f32_16x16x32_bf16 v[44:47], a[108:111], v[124:127], v[44:47]\n\t" "v_mfma_f32_16x16x32_bf16 v[48:51], a[112:115], v[124:127], v[48:51]\n\t" \
   "v_mfma_f32_16x16x32_bf16 v[52:55], a[116:119], v[124:127], v[52:55]\n\t" "v_mfma_f32_16x16x32_bf16 v[56:59], a[120:123], v[124:127], v[56:59]\n\t" \
   "v_mfma_f32_16x16x32_bf16 v[60:63], a[124:127], v[124:127], v[60:63]\n\t" "s_waitcnt vmcnt(31)\n\t" \
   "v_mfma_f32_16x16x32_bf16 v[32:35], a[128:131], v[128:131], v[32:35]\n\t" "v_mfma_f32_16x16x32_bf16 v[36:39], a[132:135], v[128:131], v[36:39]\n\t" \
   "v_mfma_f32_16x16x32_bf16 v[40:43], a[136:139], v[128:131], v[40:43]\n\t" "v_mfma_f32_16x16x32_bf16 v[44:47], a[140:143], v[128:131], v[44:47]\n\t" \
   "v_mfma_f32_16x16x32_bf16 v[48:51], a[144:147], v[128:131], v[48:51]\n\t" "v_mfma_f32_16x16x32_bf16 v[52:55], a[148:151], v[128:131], v[52:55]\n\t" \
   "v_mfma_f32_16x16x32_bf16 v[56:59], a[152:155], v[128:131], v[56:59]\n\t" "v_mfma_f32_16x16x32_bf16 v[60:63], a[156:159], v[128:131], v[60:63]\n\t" \
   "s_waitcnt vmcnt(30)\n\t" "v_mfma_f32_16x16x32_bf16 v[32:35], a[160:163], v[132:135], v[32:35]\n\t" \
   "v_mfma_f32_16x16x32_bf16 v[36:39], a[164:167], v[132:135], v[36:39]\n\t" "v_mfma_f32_16x16x32_bf16 v[40:43], a[168:171], v[132:135], v[40:43]\n\t" \
   "v_mfma_f32_16x16x32_bf16 v[44:47], a[172:175], v[132:135], v[44:47]\n\t" "v_mfma_f32_16x16x32_bf16 v[48:51], a[176:179], v[132:135], v[48:51]\n\t" \
   "v_mfma_f32_16x16x32_bf16 v[52:55], a[180:183], v[132:135], v[52:55]\n\t" "v_mfma_f32_16x16x32_bf16 v[56:59], a[184:187], v[132:135], v[56:59]\n\t" \
   "v_mfma_f32_16x16x32_bf16 v[60:63], a[188:191], v[132:135], v[60:63]\n\t" "s_waitcnt vmcnt(29)\n\t" \
   "v_mfma_f32_16x16x32_bf16 v[32:35], a[192:195], v[136:139], v[32:35]\n\t" "v_mfma_f32_16x16x32_bf16 v[36:39], a[196:199], v[136:139], v[36:39]\n\t" \
   "v_mfma_f32_16x16x32_bf16 v[40:43], a[200:203], v[136:139], v[40:43]\n\t" "v_mfma_f32_16x16x32_bf16 v[44:47], a[204:207], v[136:139], v[44:47]\n\t" \
   "v_mfma_f32_16x16x32_bf16 v[48:51], a[208:211], v[136:139], v[48:51]\n\t" "v_mfma_f32_16x16x32_bf16 v[52:55], a[212:215], v[136:139], v[52:55]\n\t" \
   "v_mfma_f32_16x16x32_bf16 v[56:59], a[216:219], v[136:139], v[56:59]\n\t" "v_mfma_f32_16x16x32_bf16 v[60:63], a[220:223], v[136:139], v[60:63]\n\t" \
   "s_waitcnt vmcnt(28)\n\t" "v_mfma_f32_16x16x32_bf16 v[32:35], a[224:227], v[140:143], v[32:35]\n\t" \
   "v_mfma_f32_16x16x32_bf16 v[36:39], a[228:231], v[140:143], v[36:39]\n\t" "v_mfma_f32_16x16x32_bf16 v[40:43], a[232:235], v[140:143], v[40:43]\n\t" \
   "v_mfma_f32_16x16x32_bf16 v[44:47], a[236:239], v[140:143], v[44:47]\n\t" "v_mfma_f32_16x16x32_bf16 v[48:51], a[240:243], v[140:143], v[48:51]\n\t" \
   "v_mfma_f32_16x16x32_bf16 v[52:55], a[244:247], v[140:143], v[52:55]\n\t" "v_mfma_f32_16x16x32_bf16 v[56:59], a[248:251], v[140:143], v[56:59]\n\t" \
   "v_mfma_f32_16x16x32_bf16 v[60:63], a[252:255], v[140:143], v[60:63]\n\t" "s_waitcnt vmcnt(27)\n\t" \
   "v_mfma_f32_16x16x32_bf16 v[64:67], a[0:3], v[152:155], 0\n\t" "v_mfma_f32_16x16x32_bf16 v[68:71], a[4:7], v[152:155], 0\n\t" \
   "v_mfma_f32_16x16x32_bf16 v[72:75], a[8:11], v[152:155], 0\n\t" "v_mfma_f32_16x16x32_bf16 v[76:79], a[12:15], v[152:155], 0\n\t" \
   "v_mfma_f32_16x16x32_bf16 v[80:83], a[16:19], v[152:155], 0\n\t" "v_mfma_f32_16x16x32_bf16 v[84:87], a[20:23], v[152:155], 0\n\t" \
   "v_mfma_f32_16x16x32_bf16 v[88:91], a[24:27], v[152:155], 0\n\t" "v_mfma_f32_16x16x32_bf16 v[92:95], a[28:31], v[152:155], 0\n\t" \
   "s_waitcnt vmcnt(26)\n\t" "v_mfma_f32_16x16x32_bf16 v[64:67], a[32:35], v[156:159], v[64:67]\n\t" \
   "v_mfma_f32_16x16x32_bf16 v[68:71], a[36:39], v[156:159], v[68:71]\n\t" "v_mfma_f32_16x16x32_bf16 v[72:75], a[40:43], v[156:159], v[72:75]\n\t" \
   "v_mfma_f32_16x16x32_bf16 v[76:79], a[44:47], v[156:159], v[76:79]\n\t" "v_mfma_f32_16x16x32_bf16 v[80:83], a[48:51], v[156:159], v[80:83]\n\t" \
   "v_mfma_f32_16x16x32_bf16 v[84:87], a[52:55], v[156:159], v[84:87]\n\t" "v_mfma_f32_16x16x32_bf16 v[88:91], a[56:59], v[156:159], v[88:91]\n\t" \
   "v_mfma_f32_16x16x32_bf16 v[92:95], a[60:63], v[156:159], v[92:95]\n\t" "s_waitcnt vmcnt(25)\n\t" \
   "v_mfma_f32_16x16x32_bf16 v[64:67], a[64:67], v[160:163], v[64:67]\n\t" "v_mfma_f32_16x16x32_bf16 v[68:71], a[68:71], v[160:163], v[68:71]\n\t" \
   "v_mfma_f32_16x16x32_bf16 v[72:75], a[72:75], v[160:163], v[72:75]\n\t" "v_mfma_f32_16x16x32_bf16 v[76:79], a[76:79], v[160:163], v[76:79]\n\t" \
   "v_mfma_f32_16x16x32_bf16 v[80:83], a[80:83], v[160:163], v[80:83]\n\t" "v_mfma_f32_16x16x32_bf16 v[84:87], a[84:87], v[160:163], v[84:87]\n\t" \
   "v_mfma_f32_16x16x32_bf16 v[88:91], a[88:91], v[160:163], v[88:91]\n\t" "v_mfma_f32_16x16x32_bf16 v[92:95], a[92:95], v[160:163], v[92:95]\n\t" \
   "s_waitcnt vmcnt(24)\n\t" "v_mfma_f32_16x16x32_bf16 v[64:67], a[96:99], v[164:167], v[64:67]\n\t" \
   "v_mfma_f32_16x16x32_bf16 v[68:71], a[100:103], v[164:167], v[68:71]\n\t" "v_mfma_f32_16x16x32_bf16 v[72:75], a[104:107], v[164:167], v[72:75]\n\t" \
   "v_mfma_f32_16x16x32_bf16 v[76:79], a[108:111], v[164:167], v[76:79]\n\t" "v_mfma_f32_16x16x32_bf16 v[80:83], a[112:115], v[164:167], v[80:83]\n\t" \
   "v_mfma_f32_16x16x32_bf16 v[84:87], a[116:119], v[164:167], v[84:87]\n\t" "v_mfma_f32_16x16x32_bf16 v[88:91], a[120:123], v[164:167], v[88:91]\n\t" \
   "v_mfma_f32_16x16x32_bf16 v[92:95], a[124:127], v[164:167], v[92:95]\n\t" "s_waitcnt vmcnt(23)\n\t" \
   "v_mfma_f32_16x16x32_bf16 v[64:67], a[128:131], v[168:171], v[64:67]\n\t" "v_mfma_f32_16x16x32_bf16 v[68:71], a[132:135], v[168:171], v[68:71]\n\t" \
   "v_mfma_f32_16x16x32_bf16 v[72:75], a[136:139], v[168:171], v[72:75]\n\t" "v_mfma_f32_16x16x32_bf16 v[76:79], a[140:143], v[168:171], v[76:79]\n\t" \
   "v_mfma_f32_16x16x32_bf16 v[80:83], a[144:147], v[168:171], v[80:83]\n\t" "v_mfma_f32_16x16x32_bf16 v[84:87], a[148:151], v[168:171], v[84:87]\n\t" \
   "v_mfma_f32_16x16x32_bf16 v[88:91], a[152:155], v[168:171], v[88:91]\n\t" "v_mfma_f32_16x16x32_bf16 v[92:95], a[156:159], v[168:171], v[92:95]\n\t" \
   "s_waitcnt vmcnt(22)\n\t" "v_mfma_f32_16x16x32_bf16 v[64:67], a[160:163], v[172:175], v[64:67]\n\t" \
   "v_mfma_f32_16x16x32_bf16 v[68:71], a[164:167], v[172:175], v[68:71]\n\t" "v_mfma_f32_16x16x32_bf16 v[72:75], a[168:171], v[172:175], v[72:75]\n\t" \
   "v_mfma_f32_16x16x32_bf16 v[76:79], a[172:175], v[172:175], v[76:79]\n\t" "v_mfma_f32_16x16x32_bf16 v[80:83], a[176:179], v[172:175], v[80:83]\n\t" \
   "v_mfma_f32_16x16x32_bf16 v[84:87], a[180:183], v[172:175], v[84:87]\n\t" "v_mfma_f32_16x16x32_bf16 v[88:91], a[184:187], v[172:175], v[88:91]\n\t" \
   "v_mfma_f32_16x16x32_bf16 v[92:95], a[188:191], v[172:175], v[92:95]\n\t" "s_waitcnt vmcnt(21)\n\t" \
   "v_mfma_f32_16x16x32_bf16 v[64:67], a[192:195], v[176:179], v[64:67]\n\t" "v_mfma_f32_16x16x32_bf16 v[68:71], a[196:199], v[176:179], v[68:71]\n\t" \
   "v_mfma_f32_16x16x32_bf16 v[72:75], a[200:203], v[176:179], v[72:75]\n\t" "v_mfma_f32_16x16x32_bf16 v[76:79], a[204:207], v[176:179], v[76:79]\n\t" \
   "v_mfma_f32_16x16x32_bf16 v[80:83], a[208:211], v[176:179], v[80:83]\n\t" "v_mfma_f32_16x16x32_bf16 v[84:87], a[212:215], v[176:179], v[84:87]\n\t" \
   "v_mfma_f32_16x16x32_bf16 v[88:91], a[216:219], v[176:179], v[88:91]\n\t" "v_mfma_f32_16x16x32_bf16 v[92:95], a[220:223], v[176:179], v[92:95]\n\t" \
   "s_waitcnt vmcnt(20)\n\t" "s_waitcnt vmcnt(16)\n\t" "v_mfma_f32_16x16x32_bf16 v[64:67], a[224:227], v[180:183], v[64:67]\n\t" \
   "v_swap_b32 v145, v146\n\t" "v_mfma_f32_16x16x32_bf16 v[68:71], a[228:231], v[180:183], v[68:71]\n\t" "v_swap_b32 v149, v150\n\t" \
   "v_mfma_f32_16x16x32_bf16 v[72:75], a[232:235], v[180:183], v[72:75]\n\t" "v_swap_b32 v185, v186\n\t" \
   "v_mfma_f32_16x16x32_bf16 v[76:79], a[236:239], v[180:183], v[76:79]\n\t" "v_swap_b32 v189, v190\n\t" \
   "v_mfma_f32_16x16x32_bf16 v[80:83], a[240:243], v[180:183], v[80:83]\n\t" "v_mfma_f32_16x16x32_bf16 v[84:87], a[244:247], v[180:183], v[84:87]\n\t" \
   "v_mfma_f32_16x16x32_bf16 v[88:91], a[248:251], v[180:183], v[88:91]\n\t" "v_mfma_f32_16x16x32_bf16 v[92:95], a[252:255], v[180:183], v[92:95]\n\t" \
   "s_waitcnt vmcnt(15)\n\t" "v_mfma_f32_16x16x32_bf16 v[32:35], v[192:195], v[144:147], v[32:35]\n\t" "s_waitcnt vmcnt(14)\n\t" \
   "v_mfma_f32_16x16x32_bf16 v[36:39], v[196:199], v[144:147], v[36:39]\n\t" "s_waitcnt vmcnt(13)\n\t" \
   "v_mfma_f32_16x16x32_bf16 v[40:43], v[200:203], v[144:147], v[40:43]\n\t" "s_waitcnt vmcnt(12)\n\t" \
   "v_mfma_f32_16x16x32_bf16 v[44:47], v[204:207], v[144:147], v[44:47]\n\t" "s_waitcnt vmcnt(11)\n\t" \
   "v_mfma_f32_16x16x32_bf16 v[48:51], v[208:211], v[144:147], v[48:51]\n\t" "s_waitcnt vmcnt(10)\n\t" \
   "v_mfma_f32_16x16x32_bf16 v[52:55], v[212:215], v[144:147], v[52:55]\n\t" "s_waitcnt vmcnt(9)\n\t" \
   "v_mfma_f32_16x16x32_bf16 v[56:59], v[216:219], v[144:147], v[56:59]\n\t" "s_waitcnt vmcnt(8)\n\t" \
   "v_mfma_f32_16x16x32_bf16 v[60:63], v[220:223], v[144:147], v[60:63]\n\t" "s_waitcnt vmcnt(7)\n\t" \
   "v_mfma_f32_16x16x32_bf16 v[32:35], v[224:227], v[148:151], v[32:35]\n\t" "s_waitcnt vmcnt(6)\n\t" \
   "v_mfma_f32_16x16x32_bf16 v[36:39], v[228:231], v[148:151], v[36:39]\n\t" "s_waitcnt vmcnt(5)\n\t" \
   "v_mfma_f32_16x16x32_bf16 v[40:43], v[232:235], v[148:151], v[40:43]\n\t" "s_waitcnt vmcnt(4)\n\t" \
   "v_mfma_f32_16x16x32_bf16 v[44:47], v[236:239], v[148:151], v[44:47]\n\t" "s_waitcnt vmcnt(3)\n\t" \
   "v_mfma_f32_16x16x32_bf16 v[48:51], v[240:243], v[148:151], v[48:51]\n\t" "s_waitcnt vmcnt(2)\n\t" \
   "v_mfma_f32_16x16x32_bf16 v[52:55], v[244:247], v[148:151], v[52:55]\n\t" "s_waitcnt vmcnt(1)\n\t" \
   "v_mfma_f32_16x16x32_bf16 v[56:59], v[248:251], v[148:151], v[56:59]\n\t" "s_waitcnt vmcnt(0)\n\t" \
   "v_mfma_f32_16x16x32_bf16 v[60:63], v[252:255], v[148:151], v[60:63]\n\t" "v_mfma_f32_16x16x32_bf16 v[64:67], v[192:195], v[184:187], v[64:67]\n\t" \
   "v_mfma_f32_16x16x32_bf16 v[68:71], v[196:199], v[184:187], v[68:71]\n\t" "v_mfma_f32_16x16x32_bf16 v[72:75], v[200:203], v[184:187], v[72:75]\n\t" \
   "v_mfma_f32_16x16x32_bf16 v[76:79], v[204:207], v[184:187], v[76:79]\n\t" "v_mfma_f32_16x16x32_bf16 v[80:83], v[208:211], v[184:187], v[80:83]\n\t" \
   "v_mfma_f32_16x16x32_bf16 v[84:87], v[212:215], v[184:187], v[84:87]\n\t" "v_mfma_f32_16x16x32_bf16 v[88:91], v[216:219], v[184:187], v[88:91]\n\t" \
   "v_mfma_f32_16x16x32_bf16 v[92:95], v[220:223], v[184:187], v[92:95]\n\t" "v_mfma_f32_16x16x32_bf16 v[64:67], v[224:227], v[188:191], v[64:67]\n\t" \
   "ds_write_b128 %[lw], v[32:35] offset:0\n\t" "v_mfma_f32_16x16x32_bf16 v[68:71], v[228:231], v[188:191], v[68:71]\n\t" \
   "ds_write_b128 %[lw], v[36:39] offset:1024\n\t" "v_mfma_f32_16x16x32_bf16 v[72:75], v[232:235], v[188:191], v[72:75]\n\t" \
   "ds_write_b128 %[lw], v[40:43] offset:2048\n\t" "v_mfma_f32_16x16x32_bf16 v[76:79], v[236:239], v[188:191], v[76:79]\n\t" \
   "ds_write_b128 %[lw], v[44:47] offset:3072\n\t" "ds_write_b128 %[lw], v[64:67] offset:32768\n\t" \
   "v_mfma_f32_16x16x32_bf16 v[80:83], v[240:243], v[188:191], v[80:83]\n\t" "ds_write_b128 %[lw], v[48:51] offset:4096\n\t" \
   "ds_write_b128 %[lw], v[68:71] offset:33792\n\t" "v_mfma_f32_16x16x32_bf16 v[84:87], v[244:247], v[188:191], v[84:87]\n\t" \
   "ds_write_b128 %[lw], v[52:55] offset:5120\n\t" "ds_write_b128 %[lw], v[72:75] offset:34816\n\t" \
   "v_mfma_f32_16x16x32_bf16 v[88:91], v[248:251], v[188:191], v[88:91]\n\t" "ds_write_b128 %[lw], v[56:59] offset:6144\n\t" \
   "ds_write_b128 %[lw], v[76:79] offset:35840\n\t" "v_mfma_f32_16x16x32_bf16 v[92:95], v[252:255], v[188:191], v[92:95]\n\t" \
   "ds_write_b128 %[lw], v[60:63] offset:7168\n\t" "ds_write_b128 %[lw], v[80:83] offset:36864\n\t" "s_nop 1\n\t" "s_nop 3\n\t" \
   "ds_write_b128 %[lw], v[84:87] offset:37888\n\t" "s_nop 3\n\t" "ds_write_b128 %[lw], v[88:91] offset:38912\n\t" "s_nop 3\n\t" \
   "ds_write_b128 %[lw], v[92:95] offset:39936\n\t" "s_waitcnt lgkmcnt(0)\n\t" "s_barrier\n\t" "ds_read_b128 v[112:115], %[lr] offset:0\n\t" \
   "ds_read_b128 v[116:119], %[lr] offset:256\n\t" "ds_read_b128 v[120:123], %[lr] offset:8192\n\t" "ds_read_b128 v[124:127], %[lr] offset:8448\n\t" \
   "ds_read_b128 v[128:131], %[lr] offset:16384\n\t" "ds_read_b128 v[132:135], %[lr] offset:16640\n\t" \
   "ds_read_b128 v[136:139], %[lr] offset:24576\n\t" "ds_read_b128 v[140:143], %[lr] offset:24832\n\t" \
   "ds_read_b128 v[144:147], %[lr] offset:32768\n\t" "ds_read_b128 v[148:151], %[lr] offset:33024\n\t" \
   "ds_read_b128 v[152:155], %[lr] offset:40960\n\t" "ds_read_b128 v[156:159], %[lr] offset:41216\n\t" \
   "ds_read_b128 v[160:163], %[lr] offset:49152\n\t" "ds_read_b128 v[164:167], %[lr] offset:49408\n\t" \
   "ds_read_b128 v[168:171], %[lr] offset:57344\n\t" "ds_read_b128 v[172:175], %[lr] offset:57600\n\t" "s_waitcnt lgkmcnt(12)\n\t" \
   "v_add_f32 v112, v112, v120\n\t" "v_add_f32 v113, v113, v121\n\t" "v_add_f32 v114, v114, v122\n\t" "v_add_f32 v115, v115, v123\n\t" \
   "v_add_f32 v116, v116, v124\n\t" "v_add_f32 v117, v117, v125\n\t" "v_add_f32 v118, v118, v126\n\t" "v_add_f32 v119, v119, v127\n\t" \
   "s_waitcnt lgkmcnt(8)\n\t" "v_add_f32 v128, v128, v136\n\t" "v_add_f32 v129, v129, v137\n\t" "v_add_f32 v130, v130, v138\n\t" \
   "v_add_f32 v131, v131, v139\n\t" "v_add_f32 v132, v132, v140\n\t" "v_add_f32 v133, v133, v141\n\t" "v_add_f32 v134, v134, v142\n\t" \
   "v_add_f32 v135, v135, v143\n\t" "v_add_f32 v112, v112, v128\n\t" "v_add_f32 v113, v113, v129\n\t" "v_add_f32 v114, v114, v130\n\t" \
   "v_add_f32 v115, v115, v131\n\t" "v_add_f32 v116, v116, v132\n\t" "v_add_f32 v117, v117, v133\n\t" "v_add_f32 v118, v118, v134\n\t" \
   "v_add_f32 v119, v119, v135\n\t" "v_cvt_pk_bf16_f32 v96, v112, v113\n\t" "v_cvt_pk_bf16_f32 v97, v114, v115\n\t" \
   "v_cvt_pk_bf16_f32 v98, v116, v117\n\t" "v_cvt_pk_bf16_f32 v99, v118, v119\n\t" "s_waitcnt lgkmcnt(4)\n\t" "v_add_f32 v144, v144, v152\n\t" \
   "v_add_f32 v145, v145, v153\n\t" "v_add_f32 v146, v146, v154\n\t" "v_add_f32 v147, v147, v155\n\t" "v_add_f32 v148, v148, v156\n\t" \
   "v_add_f32 v149, v149, v157\n\t" "v_add_f32 v150, v150, v158\n\t" "v_add_f32 v151, v151, v159\n\t" "s_waitcnt lgkmcnt(0)\n\t" \
   "v_add_f32 v160, v160, v168\n\t" "v_add_f32 v161, v161, v169\n\t" "v_add_f32 v162, v162, v170\n\t" "v_add_f32 v163, v163, v171\n\t" \
   "v_add_f32 v164, v164, v172\n\t" "v_add_f32 v165, v165, v173\n\t" "v_add_f32 v166, v166, v174\n\t" "v_add_f32 v167, v167, v175\n\t" \
   "v_add_f32 v144, v144, v160\n\t" "v_add_f32 v145, v145, v161\n\t" "v_add_f32 v146, v146, v162\n\t" "v_add_f32 v147, v147, v163\n\t" \
   "v_add_f32 v148, v148, v164\n\t" "v_add_f32 v149, v149, v165\n\t" "v_add_f32 v150, v150, v166\n\t" "v_add_f32 v151, v151, v167\n\t" \
   "v_cvt_pk_bf16_f32 v100, v144, v145\n\t" "v_cvt_pk_bf16_f32 v101, v146, v147\n\t" "v_cvt_pk_bf16_f32 v102, v148, v149\n\t" \
   "v_cvt_pk_bf16_f32 v103, v150, v151\n\t" "s_nop 1"
#define O2_CLOB "v32", "v33", "v34", "v35", "v36", "v37", "v38", "v39", "v40", "v41", "v42", "v43", "v44", "v45", "v46", "v47", "v48", "v49", "v50", "v51", "v52", "v53", "v54", "v55", "v56", "v57", "v58", "v59", "v60", "v61", "v62", "v63", "v64", "v65", "v66", "v67", "v68", "v69", "v70", "v71", "v72", "v73", "v74", "v75", "v76", "v77", "v78", "v79", "v80", "v81", "v82", "v83", "v84", "v85", "v86", "v87", "v88", "v89", "v90", "v91", "v92", "v93", "v94", "v95", "v112", "v113", "v114", "v115", "v116", "v117", "v118", "v119", "v120", "v121", "v122", "v123", "v124", "v125", "v126", "v127", "v128", "v129", "v130", "v131", "v132", "v133", "v134", "v135", "v136", "v137", "v138", "v139", "v140", "v141", "v142", "v143", "v144", "v145", "v146", "v147", "v148", "v149", "v150", "v151", "v152", "v153", "v154", "v155", "v156", "v157", "v158", "v159", "v160", "v161", "v162", "v163", "v164", "v165", "v166", "v167", "v168", "v169", "v170", "v171", "v172", "v173", "v174", "v175", "v176", "v177", "v178", "v179", "v180", "v181", "v182", "v183", "v184", "v185", "v186", "v187", "v188", "v189", "v190", "v191", "s40"
#define TZ 405
#define TZM 425
constexpr int OOB = 0x7FFFFFF0;
#define TDC 175
__device__ __forceinline__ void when(unsigned tz) {
  const unsigned t = __builtin_amdgcn_readfirstlane(*(unsigned*)(dyn_lds + L_WHEN)) + tz;
  while ((int)(t - rtc()) > 0 && (int)(t - rtc()) < 1000) __builtin_amdgcn_s_sleep(1);
}
constexpr int COL_I2 = 64 * 264, COL_LDS = COL_I2 + 8 * 2064;
__device__ __forceinline__ void job_col(int wv, unsigned char* ws, rsrc_t rs, unsigned lds, int jb, int go, int clr, bool all) {
  const int lane = lane_id(), g = lane >> 4, r = lane & 15, j = wv * 64 + lane;
  const int xin = (int)OFF_X1T + (jb == 160 ? 0 : jb) * XS * 8 + j * 16;
  v4u x[4], wf[4];
#pragma unroll
  for (int q = 0; q < 4; ++q) stc(rs, (int)OFF_ZT + jb * ZS * 8 + q * 4096 + j * 16, v4u{0xFFFFFFFFu, 0xFFFFFFFFu, 0xFFFFFFFFu, 0xFFFFFFFFu});
#pragma unroll
  for (int q = 0; q < 4; ++q) {
    const int o = (int)OFF_WT + (jb == 160 ? 320 : 2 * jb + (q >> 1)) * S_ * 4 + ((wv * 2 + (q & 1)) * 64 + lane) * 16;
    wf[q] = ldc(rs, o);
  }
  v32u k0, k1, k2;
  v8u k3, ad = (unsigned)lane;
  {
    const int n = 16 * wv + r;
    cis8(k0, n * (512 * (g & 1) + 256 * (g >> 1)), n * 32, n * 128, (r & 1) ? 0x80008000u : 0u);
    cis8(k1, r * g * 256, r * 64, r * 1024, 0x80000000u);
#pragma unroll
    for (int q = 0; q < 8; ++q) {
      const int i = -n * sigma(g, q), k = (r + 16 * (q >> 2)) * (16 * wv + 4 * g + (q & 3));
      const float sc = 1.0f / ((float)S_ * (float)D_);
      k0[16 + 2 * q] = __builtin_bit_cast(unsigned, cosi(i)); k0[17 + 2 * q] = __builtin_bit_cast(unsigned, sini(i));
      k1[16 + 2 * q] = __builtin_bit_cast(unsigned, cosi(k) * sc); k1[17 + 2 * q] = __builtin_bit_cast(unsigned, sini(k) * sc);
      k3[q] = k1[8 + q] ^ 0x80008000u;
    }
    ad[0] = lds + 264 * (16 * (g & 1) + 8 * (g >> 1) + (r >> 2)) + 32 * (r & 3);
    ad[1] = lds + COL_I2 + (2 * (r >> 3) + (r & 1)) * 2064 + ((r >> 1) & 3) * 512 + (g & 1) * 8;
    ad[2] = ad[1] + 256 + (((4 * (wv >> 1) + 2 * (g >> 1) + (wv & 1)) ^ ((r & 6) + 1)) << 4);
    ad[1] += ((4 * (wv >> 1) + 2 * (g >> 1) + (wv & 1)) ^ (r & 6)) << 4;
    ad[3] = lds + COL_I2 + r * 128 + ((g ^ (r >> 1)) << 4);
    ad[4] = lds + COL_I2 + r * 128 + (((4 + g) ^ (r >> 1)) << 4);
    ad[5] = (int)OFF_ZT + (jb * ZS + 32 * (16 * wv + r) + 2 * g) * 8;
    asm volatile("" : "+{v[32:63]}"(k0), "+{v[64:95]}"(k1), "+{v[128:135]}"(k3), "+{v[16:23]}"(ad));
  }
  const unsigned sh = jb == 160 ? 16u : 0u, mk = jb == 0 ? 0xFFFFu : ~0u, d = lds + (j >> 4) * 264 + r * 16;
#pragma unroll
  for (int q = 0; q < 16; ++q) { k2[2 * q] = wf[q >> 2][q & 3] << 16; k2[2 * q + 1] = wf[q >> 2][q & 3] & 0xFFFF0000u; }
  asm volatile("" : "+{v[96:127]}"(k2));
  fan_signal<FSP>(wv, ws, clr, jb);
  fan_wait<64, 20>(wv, ws, go, jb);
  PH(all ? 27 : 47)
#pragma unroll
  for (int q = 0; q < 4; ++q) x[q] = ldc(rs, q == 3 && wv == 3 ? OOB : xin + q * 4096);
  if (all) fan_wait<FSP, NCOL, false>(wv, ws, clr, jb);
  if (wv == 3) {
    when(TDC);
    for (unsigned spins = 0; spins < (1u << 16); ++spins) {
      x[3] = ldc(rs, xin + 3 * 4096);
      if (__builtin_expect(!__any(max(x[3].x, x[3].z) == 0xFFFFFFFFu), 1)) break;
      __builtin_amdgcn_s_sleep(2);
      asm volatile("" ::: "memory");
    }
  }
#define CSTAMP(i)
  CSTAMP(0)
#pragma unroll
  for (int q = 0; q < 4; ++q) {
    LDS_V2(d + q * 16 * 264) = v2u{(x[q].x >> sh) & mk, x[q].y};
    LDS_V2(d + q * 16 * 264 + 8) = v2u{(x[q].z >> sh) & mk, x[q].w};
  }
  CSTAMP(1)
  PH(all ? 28 : 48)
  asm volatile(COL_ASM ::"{v[32:63]}"(k0), "{v[64:95]}"(k1), "{v[96:127]}"(k2), "{v[128:135]}"(k3), "{v[16:23]}"(ad), [rs] "s"(rs) : COL_CLOB, "memory");
  CSTAMP(2)
  PH(all ? 29 : 49)
  CSTAMP(3)
}
struct XRows { v4u p[2]; v2u q[2]; };
__device__ __forceinline__ int xr_off(int rt, int hf, int wv, int lane, bool third) {
  const int o = (int)OFF_XR + ((rt * KT + hf * 5) * 64 + (lane & 48) + 2 * (lane & 7) + ((lane >> 3) & 1)) * 16;
  return third ? (wv < 2 ? o + 4096 + wv * 8 : OOB) : o + wv * 1024;
}
__device__ __forceinline__ void idft_x(XRows& x, rsrc_t rs, int rt, int hf, int wv, int lane) {
#pragma unroll
  for (int m = 0; m < 2; ++m) {
    x.p[m] = __builtin_amdgcn_raw_buffer_load_b128(rs, xr_off(rt, hf, wv, lane, false), m * 10240, AUX_SC1);
    x.q[m] = __builtin_amdgcn_raw_buffer_load_b64(rs, xr_off(rt, hf, wv, lane, true), m * 10240, AUX_SC1);
  }
}
__device__ __forceinline__ void idft_stage(rsrc_t rs, v4u* lds, int rt, int wv, int lane) {
  const int o = (int)OFF_ZT + ((lane >> 3) * ZS + rt * 16 + 2 * (lane & 7)) * 8;
  const bool more = lane >> 4 == wv;
  int off[9];
  v4u t[9];
#pragma unroll
  for (int i = 0; i < 9; ++i) {
    off[i] = i < 4 ? o + (wv * 32 + i * 8) * ZS * 8 : (more && (i < 8 || lane < 8) ? o + (96 + i * 8) * ZS * 8 : OOB);
    t[i] = ldc(rs, off[i]);
  }
  for (unsigned spins = 0; spins < (1u << 16); ++spins) {
    unsigned m = 0;
#pragma unroll
    for (int i = 0; i < 9; ++i) m = max(m, max(t[i].x, t[i].z));
    if (__builtin_expect(!__any(m == 0xFFFFFFFFu), 1)) break;
    __builtin_amdgcn_s_sleep(2);
#pragma unroll
    for (int i = 0; i < 9; ++i) {
      const bool again = max(t[i].x, t[i].z) == 0xFFFFFFFFu;
      const v4u u = ldc(rs, again ? off[i] : OOB);
      if (again) t[i] = u;
    }
    asm volatile("" ::: "memory");
  }
  if (more && lane < 8) {
    t[0].x = (t[0].x & 0xFFFFu) | (t[8].x << 16);
    t[0].z = (t[0].z & 0xFFFFu) | (t[8].z << 16);
  }
  v4u* dst = lds + (lane >> 3) * 16 + (lane & 7);
#pragma unroll
  for (int c = 0; c < 4; ++c) dst[wv * 128 + (c & 1) * 640 + (c >> 1) * 8] = v4u{t[0][c], t[1][c], t[2][c], t[3][c]};
  if (more) {
#pragma unroll
    for (int c = 0; c < 4; ++c) dst[512 + (c & 1) * 640 + (c >> 1) * 8] = v4u{t[4][c], t[5][c], t[6][c], t[7][c]};
  }
}
#define A_IN(a) "{a[0:31]}"(a[0]), "{a[32:63]}"(a[1]), "{a[64:95]}"(a[2]), "{a[96:127]}"(a[3]), "{a[128:159]}"(a[4]), "{a[160:191]}"(a[5]), "{a[192:223]}"(a[6]), "{a[224:255]}"(a[7])
#define A_OUT(a) "={a[0:31]}"(a[0]), "={a[32:63]}"(a[1]), "={a[64:95]}"(a[2]), "={a[96:127]}"(a[3]), "={a[128:159]}"(a[4]), "={a[160:191]}"(a[5]), \
                 "={a[192:223]}"(a[6]), "={a[224:255]}"(a[7])
__device__ __forceinline__ void idft_mul(XRows& x, const v32u (&a)[8], unsigned la) {
  asm volatile(ID_ASM : "+{v[136:139]}"(x.p[0]), "+{v[140:143]}"(x.p[1]), "+{v[144:145]}"(x.q[0]), "+{v[146:147]}"(x.q[1])
               : A_IN(a), [la] "v"(la) : ID_CLOB, "memory");
}
__device__ __forceinline__ void idft_matrix(v32u (&a)[8], rsrc_t rs, int hf, int wv, int lane) {
  const int o = (int)OFF_INV + hf * 10 * NFK * 1024 + lane * 16;
  asm volatile(IM_ASM : A_OUT(a)
               : [o0] "v"(o + wv * 2 * NFK * 1024), [o1] "v"(wv < 2 ? o + (wv + 6) * NFK * 1024 : OOB), [rs] "s"(rs) : IM_CLOB, "memory");
}
constexpr int NFIRST = 40;
__device__ __forceinline__ void job_idft(int wv_, unsigned char* ws, rsrc_t rs, v4u* lds, int rt0, int hf, unsigned long long* dbg) {
  const int lane = lane_id(), wv = wv_;
  v32u a[8];
  idft_matrix(a, rs, hf, wv, lane);
  fan_wait<FSP, NCOL>(wv_, ws, fan_off(1, 0, 0), rt0 >> 2);
  fan_wait<64, 20>(wv_, ws, fan_off(0, 1, 0), rt0 >> 2);
#pragma unroll 1
  for (int rt = rt0; rt < RT; rt += 32) {
    XRows x;
    idft_x(x, rs, rt, hf, wv, lane);
    if (rt == rt0) { when(TZM); PH(30) }
    idft_stage(rs, lds, rt, wv, lane);
    if (rt == rt0) { PH(50) }
    idft_mul(x, a, lds_base() + lane * 16);
#pragma unroll
    for (int m = 0; m < 2; ++m) {
      __builtin_amdgcn_raw_buffer_store_b128(x.p[m], rs, xr_off(rt, hf, wv, lane, false), m * 10240, AUX_SC1);
      __builtin_amdgcn_raw_buffer_store_b64(x.q[m], rs, xr_off(rt, hf, wv, lane, true), m * 10240, AUX_SC1);
    }
    const f32x2 st = rows_sum(frag_stat(x.p[0]) + frag_stat(x.p[1]) + frag_stat(v4u{x.q[0].x, x.q[0].y, x.q[1].x, x.q[1].y}));
    f32x2* sp = (f32x2*)(lds + 5120) + (lane & 15);
    if (lane < 16) sp[wv * 16] = st;
    asm volatile("s_waitcnt lgkmcnt(0)\n\ts_barrier" ::: "memory");
    if (wv == 0) {
      const f32x2 s0 = sp[0] + sp[16], s1 = sp[32] + sp[48];
      const f32x4 sv = lane < 16 ? f32x4{s0.x, s0.y, s1.x, s1.y} : f32x4{0, 0, 0, 0};
      stc(rs, lane < 16 + hf * 16 ? (int)OFF_ST + (rt * 16 + 2 * (lane & 7) + ((lane >> 3) & 1)) * 48 + (hf + (lane >> 4)) * 16 : OOB, __builtin_bit_cast(v4u, sv));
    }
    if (rt == rt0) { PH(31) }
    if (rt < NFIRST / 2) fan_signal<64>(wv_, ws, fan_off(1, 1, 0), rt * 2 + hf);
    else dep_signal(wv_, ws, cnt_off(1, CNT_IDFT, rt >> 2));
    if (rt < NFIRST / 2) { PH(51) }
  }
}

constexpr int NFAST = 62, NEARLY = 61;
#define TYL 160
#define TG0 190
#define TG1 165
__device__ __forceinline__ void job_out(int wv_, unsigned char* ws, rsrc_t rs, unsigned long long ysel, int layer, int s, int cg, int qt) {
  const int lane = lane_id(), wv = wv_, g = lane >> 4, r = lane & 15, q0 = max(qt, 0);
  const int yo = wv * 2048 + lane * 16;
  const int sy = __builtin_amdgcn_readfirstlane(pool_base(ws) + P_Y + y_region(ysel, s) + cg * 131072);
  const int xo = (int)OFF_XR + (((2 * s + q0) * KT + cg * 4 + wv) * 64 + lane) * 16;
  const v4u x0 = ldc(rs, xo), x1 = ldc(rs, qt < 0 ? xo + KT * 1024 : OOB);
  v32u u[8], y0, y1;
  asm volatile(OU_ASM : A_OUT(u) : [o] "v"(wv * 8192 + lane * 16), [so] "s"((int)OFF_UF + (s * 40 + cg * 8) * 32768), [rs] "s"(rs) : OU_CLOB, "scc", "memory");
  const int sw = s < NEARLY ? s : s - 2;
  if (s > 1) prog_prewait(wv_, ws, prog_off(layer, sw - 2, (s + cg * 7) & 31));
  prog_wait(wv_, ws, prog_off(layer, sw, 0), s < PWL);
  v8u acc;
#define OM_OPS(q0) A_IN(u), [po] "v"(wv * 16384 + lane * 16), [mo] "v"(wv * 4096 + lane * 16), [sp] "s"((int)OFF_PXF + s * 65536 + q0 * 1024),         \
               [sm] "s"((int)OFF_PXM + s * 16384 + q0 * 1024), [lw] "v"(lds_base() + wv * 8192 + lane * 16), [lr] "v"(lds_base() + wv * 2048 + g * 512 + r * 16), [rs] "s"(rs)
  PHT(1)
  if (s >= NEARLY) {
    unsigned nn;
    asm volatile("v_mov_b32 %0, -1" : "=v"(nn) : "v"(x0.x), "v"(x1.x));
    stc(rs, xo, v4u{nn, nn, nn, nn});
    stc(rs, qt < 0 ? xo + KT * 1024 : OOB, v4u{nn, nn, nn, nn});
    __builtin_amdgcn_raw_buffer_store_b64(v2u{nn, nn}, rs, TIDX < (qt < 0 ? 32 : 16) ? (int)OFF_ST + ((s * 32 + q0 * 16 + lane) * 6 + cg) * 8 : OOB, 0, AUX_SC1);
    when(TYL);
    for (int i = 0; i < 2; ++i) {
      unsigned m;
      asm volatile(OY_ASM : "={v[192:223]}"(y0), "={v[224:255]}"(y1), [m] "=&v"(m) : [yo] "v"(yo), [sy] "s"(sy), [rs] "s"(rs) : OY_CLOB, "scc", "memory");
      if (i == 0) prog_wait(wv_, ws, prog_off(layer, s, 0), false);
      if (!__any(m == 0xFFFFFFFFu)) break;
    }
    PHT(0)
#pragma unroll 1
    for (int q = qt < 0 ? 1 : qt;; q = 0) {
      const int lane = lane_id(), g = lane >> 4, r = lane & 15;
      asm volatile(O1_ASM : "=&{v[96:103]}"(acc) : OM_OPS(q), "{v[192:223]}"(y0), "{v[224:255]}"(y1), [ao] "v"(wv * 64 + lane == 0 ? cnt_off(layer, CNT_OUT, s) : OOB),
                   [at] "v"(rtc() & 0x7FFFFFFFu) : O1_CLOB, "scc", "memory");
      if (q == q0) break;
      *(v4u*)(dyn_lds + 65536 + wv * 1024 + lane_id() * 16) = v4u{acc[0], acc[1], acc[2], acc[3]};
      asm volatile("s_barrier" ::: "memory");
    }
    const v4u second = *(const v4u*)(dyn_lds + 65536 + wv * 1024 + lane_id() * 16);
    acc[4] = second.x, acc[5] = second.y, acc[6] = second.z, acc[7] = second.w;
    PHV(32 + (s - 61) * 4, pht0)
  } else {
    asm volatile(O2_ASM : "=&{v[96:103]}"(acc), "=&{v[192:223]}"(y0), "=&{v[224:255]}"(y1) : OM_OPS(0), [yo] "v"(yo), [sy] "s"(sy) : O2_CLOB, "scc", "memory");
  }
  f32x2* sp = (f32x2*)(dyn_lds + 81920);
#pragma unroll
  for (int q = 0; q < 2; ++q) {
    if (q && qt >= 0) break;
    const v4u x = q ? x1 : x0;
    const auto s0 = __builtin_amdgcn_permlane16_swap(acc[4 * q], acc[4 * q + 2], false, false);
    const auto s1 = __builtin_amdgcn_permlane16_swap(acc[4 * q + 1], acc[4 * q + 3], false, false);
    v4u y;
    y.x = pk(lo_f(x.x) + lo_f(s0[0]), hi_f(x.x) + hi_f(s0[0]));
    y.y = pk(lo_f(x.y) + lo_f(s0[1]), hi_f(x.y) + hi_f(s0[1]));
    y.z = pk(lo_f(x.z) + lo_f(s1[0]), hi_f(x.z) + hi_f(s1[0]));
    y.w = pk(lo_f(x.w) + lo_f(s1[1]), hi_f(x.w) + hi_f(s1[1]));
    stc(rs, xo + q * KT * 1024, y);
    const f32x2 st = rows_sum(frag_stat(y));
    if (lane < 16) sp[(wv * 2 + q) * 16 + lane] = st;
  }
  PHT(2)
  asm volatile("s_waitcnt lgkmcnt(0)\n\ts_barrier" ::: "memory");
  if (wv == 0) {
    sp += lane & 31;
    __builtin_amdgcn_raw_buffer_store_b64(__builtin_bit_cast(v2u, (sp[0] + sp[32]) + (sp[64] + sp[96])), rs,
                                          lane < (qt < 0 ? 32 : 16) ? (int)OFF_ST + ((s * 32 + q0 * 16 + lane) * 6 + cg) * 8 : OOB, 0, AUX_SC1);
  }
  dep_signal(wv_, ws, cnt_off(layer, CNT_OUT, s), qt < 0 ? 2u : 1u);
  PHT(3)
  if (s > 60) { PHV(33 + (s - 61) * 4, pht1) PHV(34 + (s - 61) * 4, pht2) PHV(35 + (s - 61) * 4, pht3) }
}
constexpr int JOB_LDS = 4 * NFK * 1024;
constexpr int FAN_LAST = fan_off(0, 1, 0) + 20 * 64;
#define OAUX 0
__device__ __forceinline__ void job_last(int wv_, unsigned char* ws, u16* out, int jb, int rt, int hf) {
  const rsrc_t rs = ws_rsrc(ws), ro = __builtin_amdgcn_make_buffer_rsrc(out, 0, S_ * D_ * 2, 0x00020000);
  const int wv = wv_;
  v32u a[8];
  idft_matrix(a, rs, hf, wv, lane_id());
  PH(26)
  if (jb >= 0) {
    job_col(wv_, ws, rs, lds_base(), jb, FAN_LAST, fan_off(0, 0, 0), true);
    PH(13)
  } else {
    fan_wait<FSP, NCOL>(wv_, ws, fan_off(0, 0, 0), rt);
    fan_wait<64, 20>(wv_, ws, FAN_LAST, rt);
  }
  const int lane = lane_id();
  v4u* lds = (v4u*)dyn_lds + 3072;
  XRows x;
  if (rt < 2 * NEARLY) idft_x(x, rs, rt, hf, wv, lane);
  when(TZ);
  PHT(0)
  if (rt >= 2 * NEARLY) idft_x(x, rs, rt, hf, wv, lane);
  idft_stage(rs, lds, rt, wv, lane);
  if (rt >= 2 * NEARLY && __any(max(max(x.p[0].x, x.p[0].z), max(max(x.p[1].x, x.p[1].z), max(x.q[0].x, x.q[1].x))) == ~0u)) idft_x(x, rs, rt, hf, wv, lane);
  PHT(1)
  idft_mul(x, a, lds_base() + 49152 + lane * 16);
  const int o = ((rt * 16 + 2 * (lane & 7) + ((lane >> 3) & 1)) * D_ + hf * 160 + (lane >> 5) * 8) * 2;
#pragma unroll
  for (int m = 0; m < 3; ++m) {
    const v4u y = m < 2 ? x.p[m] : v4u{x.q[0].x, x.q[0].y, x.q[1].x, x.q[1].y};
    const auto s0 = __builtin_amdgcn_permlane16_swap(y.x, y.z, false, false), s1 = __builtin_amdgcn_permlane16_swap(y.y, y.w, false, false);
    __builtin_amdgcn_raw_buffer_store_b128(v4u{s0[0], s1[0], s0[1], s1[1]}, ro,
                                           m < 2 ? o + m * 640 + wv * 64 + (lane & 16) * 2 : (wv < 2 ? o + 256 + wv * 32 + (lane & 16) * 40 : OOB), 0, OAUX);
  }
  PH(15)
  PHV(24, pht0) PHV(25, pht1)
}

constexpr unsigned SPIN_CAP = 1u << 16;
#define NFILL 24
#define FSPARE
#define FLAG 4

#define VM_LD128(dst, off, so, IMM) asm volatile("buffer_load_dwordx4 %0, %1, %2, %3 offen offset:" #IMM " sc1" : "=v"(dst) : "v"(off), "s"(rs), "s"(so) : "memory")
#define VM_LD32(dst, off, so) asm volatile("buffer_load_dword %0, %1, %2, %3 offen sc1" : "=v"(dst) : "v"(off), "s"(rs), "s"(so) : "memory")
#define VM_ST32(val, off, so) asm volatile("buffer_store_dword %0, %1, %2, %3 offen sc1" ::"v"(val), "v"(off), "s"(rs), "s"(so) : "memory")
#define VM_ST128(val, off, so) asm volatile("buffer_store_dwordx4 %0, %1, %2, %3 offen sc1\n\ts_nop 1" ::"v"(val), "v"(off), "s"(rs), "s"(so) : "memory")
#define VM_WAIT(N) asm volatile("s_waitcnt vmcnt(" #N ")" ::: "memory")
#define VM_WAIT1(N, a) asm volatile("s_waitcnt vmcnt(" #N ")" : "+v"(a)::"memory")
__device__ __forceinline__ v4u make_rs(const void* p, unsigned bytes) {
  const unsigned long long a = (unsigned long long)p;
  v4u r;
  r.x = (unsigned)__builtin_amdgcn_readfirstlane((unsigned)a);
  r.y = (unsigned)__builtin_amdgcn_readfirstlane((unsigned)(a >> 32) & 0xFFFFu);
  r.z = bytes;
  r.w = 0x00020000u;
  return r;
}
struct Clk0 { unsigned r, c; };
__device__ __forceinline__ Clk0 clk_sample() {
  unsigned long long r, c;
  asm volatile("s_memrealtime %0\n\ts_memtime %1\n\ts_waitcnt lgkmcnt(0)" : "=s"(r), "=s"(c)::"memory");
  return Clk0{(unsigned)r, (unsigned)c};
}
__device__ __forceinline__ v4u clk_init(Clk0 a) {
  const Clk0 b = clk_sample();
  unsigned k = 1400u;
  if (b.r - a.r >= 100u) k = (unsigned)((float)(b.r - a.r) * 32768.f / (float)(b.c - a.c));
  k = (unsigned)__builtin_amdgcn_readfirstlane((int)min(max(k, 1200u), 3000u));
  asm volatile("" : "+s"(k));
  return v4u{b.r * 8u + 16u, b.c, k, 0u};
}
#define LDSBAR() asm volatile("s_waitcnt lgkmcnt(0)\n\ts_barrier" ::: "memory")
#define MEMPIN() asm volatile("" ::: "memory")

__device__ __forceinline__ int ladder(int x, int i) { return copy_off(((x + (i >> 1)) & 3) | ((i & 1) << 2)); }

struct RecTune { int m1, m2, dl1, rob, c2, vis, dlt, du; };

#define STAT(r)
__device__ __forceinline__ unsigned max4(v4u v) { return max(max(v.x, v.y), max(v.z, v.w)); }
template <int NAP = 6>
__device__ __forceinline__ unsigned flag_finish(const v4u rs, int off, unsigned cv) {
  for (unsigned spins = 0; __any(cv == 0xFFFFFFFFu) && spins < SPIN_CAP; ++spins) {
    __builtin_amdgcn_s_sleep(NAP);
    v4u t;
    VM_LD128(t, off, 0, 0);
    VM_WAIT1(0, t);
    cv = max4(t);
  }
  return cv;
}
struct Pool {
  unsigned long long sel, s3, ysel; int base;
  __device__ __forceinline__ int near(int s) const { return (int)((s3 >> s) & 1ull) * 16384; }
  __device__ __forceinline__ int region(int s) const { return base + s * REG + (int)((sel >> s) & 1ull) * 8192; }
};
constexpr int L_QT = 10240, L_KT = 12288, L_CL = 14336, L_KQ = 16384;
constexpr int L_T = 14336;
constexpr int L_M2 = L_KQ + 65536;
constexpr int REC_ST = L_M2 + 12288;
constexpr int REC_LDS = REC_ST + 4096 + 64;

#define RSTAMP(i)
#define YSTAMP(n)
#define RST_CLOB
#define P1TRACE
#define PVTRACE
#define FINTRACE
#define P1_ASM \
   ".p2align 6\n\t" "s_nop 0\n\t" "s_nop 0\n\t" "s_nop 0\n\t" "s_nop 0\n\t" "s_mov_b32 s64, -1\n\t" "s_mov_b32 s76, 0\n\t" "s_mov_b32 s77, 0\n\t" \
   "s_mov_b32 s71, 63\n\t" "s_mov_b32 s47, 1300\n\t" "s_mov_b32 s49, 0\n\t" "s_mov_b32 s51, 0\n\t" "s_mov_b32 s52, 0\n\t" "s_mov_b32 s70, 0\n\t" \
   "s_mov_b32 s69, 0\n\t" "v_cmp_gt_i32 s[20:21], 0, v126\n\t" "v_cmp_gt_i32 s[22:23], 1, v126\n\t" "v_cmp_gt_i32 s[24:25], 2, v126\n\t" \
   "v_cmp_gt_i32 s[26:27], 3, v126\n\t" "v_cmp_gt_i32 s[28:29], 16, v126\n\t" "v_cmp_gt_i32 s[30:31], 17, v126\n\t" \
   "v_cmp_gt_i32 s[32:33], 18, v126\n\t" "v_cmp_gt_i32 s[34:35], 19, v126\n\t" "s_waitcnt vmcnt(0)\n\t" "s_lshr_b64 s[72:73], %[sel], s71\n\t" \
   "s_lshl_b32 s65, s71, 19\n\t" "s_and_b32 s72, s72, 1\n\t" "s_add_u32 s65, s65, %[pool]\n\t" "s_lshl_b32 s72, s72, 13\n\t" \
   "s_add_u32 s65, s65, s72\n\t" "s_lshr_b64 s[72:73], %[s3], s71\n\t" "s_and_b32 s54, s72, 1\n\t" "s_lshl_b32 s54, s54, 14\n\t" \
   "s_memtime s[62:63]\n\t" "s_memrealtime s[60:61]\n\t" "40:\n\t" "s_cmp_eq_u32 s64, 0\n\t" "s_cbranch_scc1 95f\n\t" "96:\n\t" \
   "s_add_i32 s72, s64, 2\n\t" "s_min_i32 s72, s72, 63\n\t" "s_lshl_b32 s59, s72, 12\n\t" "s_lshl_b32 s79, s72, 13\n\t" \
   "buffer_load_dword v108, v139, %[rs], s65 offen sc1\n\t" "s_cmp_gt_u32 %[wv], 1\n\t" "s_waitcnt vmcnt(4)\n\t" "s_cbranch_scc1 44f\n\t" \
   "buffer_load_dword v15, v140, %[rs], s59 offen sc1\n\t" "ds_write_b128 v127, v[16:19] offset:0\n\t" "ds_write_b128 v127, v[20:23] offset:1024\n\t" \
   "ds_write_b128 v127, v[24:27] offset:2048\n\t" "ds_write_b128 v127, v[28:31] offset:3072\n\t" "s_waitcnt lgkmcnt(0)\n\t" \
   "s_sub_u32 s74, s62, s57\n\t" "s_mul_i32 s74, s74, s58\n\t" "s_lshr_b32 s74, s74, 12\n\t" "s_add_u32 s74, s74, s56\n\t" "s_lshl_b32 s60, s60, 3\n\t" \
   "s_sub_u32 s75, s74, s60\n\t" "s_cmp_lt_u32 s75, 32\n\t" "s_cbranch_scc1 38f\n\t" "s_cmp_lt_i32 s75, 0\n\t" "s_cselect_b32 s61, 0, 31\n\t" \
   "s_add_u32 s74, s60, s61\n\t" "s_sub_i32 s75, s61, s75\n\t" "s_add_i32 s75, s75, 1\n\t" "s_ashr_i32 s75, s75, 1\n\t" "s_min_i32 s75, s75, 8\n\t" \
   "s_max_i32 s75, s75, -8\n\t" "s_mov_b32 s56, s74\n\t" "s_mov_b32 s57, s62\n\t" "s_add_u32 s58, s58, s75\n\t" "38:\n\t" "s_min_u32 s72, s70, 1\n\t" \
   "s_and_b32 s71, s74, -4\n\t" "s_or_b32 s71, s71, s72\n\t" "v_mov_b32 v111, s71\n\t" "buffer_store_dword v111, v131, %[rs], s65 offen sc1\n\t" \
   P1TRACE "s_barrier\n\t" "ds_read_b128 v[160:163], v122 offset:0\n\t" "ds_read_b128 v[164:167], v122 offset:1024\n\t" \
   "ds_read_b128 v[168:171], v122 offset:2048\n\t" "ds_read_b128 v[172:175], v122 offset:3072\n\t" "ds_read_b128 v[176:179], v122 offset:4096\n\t" \
   "ds_read_b128 v[180:183], v122 offset:5120\n\t" "ds_read_b128 v[184:187], v122 offset:6144\n\t" "ds_read_b128 v[188:191], v122 offset:7168\n\t" \
   "s_cmp_lt_i32 s64, 1\n\t" "s_cselect_b32 s72, 0x7ffffff0, 0\n\t" "s_max_i32 s71, s64, 1\n\t" "v_or_b32 v109, s72, v143\n\t" \
   "s_lshl_b32 s71, s71, 12\n\t" "s_nop 0\n\t" "s_sub_u32 s71, s71, 4096\n\t" "buffer_store_dword v111, v109, %[rs], s71 offen sc1\n\t" \
   "s_add_u32 s48, s69, s47\n\t" "s_cmp_eq_u32 s70, 0\n\t" "s_cselect_b32 s48, s48, -1\n\t" "s_mov_b32 s66, s65\n\t" "s_add_i32 s64, s64, 1\n\t" \
   "s_and_b32 s71, s64, 63\n\t" "s_lshr_b64 s[72:73], %[sel], s71\n\t" "s_lshl_b32 s65, s71, 19\n\t" "s_and_b32 s72, s72, 1\n\t" \
   "s_add_u32 s65, s65, %[pool]\n\t" "s_lshl_b32 s72, s72, 13\n\t" "s_add_u32 s65, s65, s72\n\t" "s_xor_b32 s67, s54, %[lad0]\n\t" \
   "s_xor_b32 s68, s67, 0x4000\n\t" "s_add_u32 s67, s67, s66\n\t" "s_add_u32 s68, s68, s66\n\t" "s_lshr_b64 s[72:73], %[s3], s71\n\t" \
   "s_and_b32 s54, s72, 1\n\t" "s_lshl_b32 s54, s54, 14\n\t" "s_cmp_eq_u32 s64, 0\n\t" "s_cselect_b32 s70, 1, 0\n\t" "s_and_b32 s55, %[wv], 1\n\t" \
   "s_lshl_b32 s55, s55, 14\n\t" "s_xor_b32 s55, s55, s54\n\t" "s_xor_b32 s50, s55, 0x4000\n\t" "s_add_u32 s55, s55, s65\n\t" \
   "s_add_u32 s50, s50, s65\n\t" "s_sub_u32 s53, s64, 63\n\t" "s_mul_i32 s53, s53, s64\n\t" "v_mov_b32 v48, 0\n\t" "v_mov_b32 v49, 0\n\t" \
   "v_mov_b32 v50, 0\n\t" "v_mov_b32 v51, 0\n\t" "v_mov_b32 v52, 0\n\t" "v_mov_b32 v53, 0\n\t" "v_mov_b32 v54, 0\n\t" "v_mov_b32 v55, 0\n\t" \
   "v_mov_b32 v56, 0\n\t" "v_mov_b32 v57, 0\n\t" "v_mov_b32 v58, 0\n\t" "v_mov_b32 v59, 0\n\t" "v_mov_b32 v60, 0\n\t" "v_mov_b32 v61, 0\n\t" \
   "v_mov_b32 v62, 0\n\t" "v_mov_b32 v63, 0\n\t" "s_waitcnt vmcnt(2)\n\t" "v_cmp_eq_u32 vcc, -1, v15\n\t" "s_nop 0\n\t" "s_cbranch_vccnz 141f\n\t" \
   "41:\n\t" "s_branch 48f\n\t" "44:\n\t" "v_mfma_f32_16x16x32_bf16 v[32:35], v[160:163], v[16:19], 0\n\t" "s_max_i32 s51, s64, 0\n\t" \
   "s_lshl_b32 s52, s51, 16\n\t" "s_lshl_b32 s51, s51, 14\n\t" "v_mfma_f32_16x16x32_bf16 v[36:39], v[176:179], v[16:19], 0\n\t" \
   "s_cmp_lt_i32 s64, 0\n\t" "s_cselect_b32 s49, 0x7ffffff0, 0\n\t" "s_add_i32 s72, s64, 1\n\t" \
   "v_mfma_f32_16x16x32_bf16 v[32:35], v[164:167], v[20:23], v[32:35]\n\t" "s_min_i32 s72, s72, 63\n\t" "s_lshl_b32 s79, s72, 13\n\t" \
   "v_mfma_f32_16x16x32_bf16 v[36:39], v[180:183], v[20:23], v[36:39]\n\t" "v_mfma_f32_16x16x32_bf16 v[32:35], v[168:171], v[24:27], v[32:35]\n\t" \
   "s_waitcnt lgkmcnt(0)\n\t" "v_mfma_f32_16x16x32_bf16 v[36:39], v[184:187], v[24:27], v[36:39]\n\t" "s_sub_u32 s74, s62, s57\n\t" \
   "s_mul_i32 s74, s74, s58\n\t" "s_lshr_b32 s74, s74, 12\n\t" "v_mfma_f32_16x16x32_bf16 v[32:35], v[172:175], v[28:31], v[32:35]\n\t" \
   "s_add_u32 s74, s74, s56\n\t" "s_lshl_b32 s60, s60, 3\n\t" "s_sub_u32 s75, s74, s60\n\t" \
   "v_mfma_f32_16x16x32_bf16 v[36:39], v[188:191], v[28:31], v[36:39]\n\t" "s_cmp_lt_u32 s75, 32\n\t" "s_cbranch_scc1 39f\n\t" \
   "s_cmp_lt_i32 s75, 0\n\t" "s_cselect_b32 s61, 0, 31\n\t" "s_add_u32 s74, s60, s61\n\t" "s_sub_i32 s75, s61, s75\n\t" "s_add_i32 s75, s75, 1\n\t" \
   "s_ashr_i32 s75, s75, 1\n\t" "s_min_i32 s75, s75, 8\n\t" "s_max_i32 s75, s75, -8\n\t" "s_mov_b32 s56, s74\n\t" "s_mov_b32 s57, s62\n\t" \
   "s_add_u32 s58, s58, s75\n\t" "39:\n\t" "s_barrier\n\t" "v_mfma_f32_16x16x32_bf16 v[40:43], v[144:147], v[16:19], 0\n\t" "s_min_u32 s72, s70, 1\n\t" \
   "s_and_b32 s71, s74, -4\n\t" "s_or_b32 s71, s71, s72\n\t" "v_mov_b32 v111, s71\n\t" \
   "v_mfma_f32_16x16x32_bf16 v[40:43], v[148:151], v[20:23], v[40:43]\n\t" "buffer_store_dword v111, v131, %[rs], s65 offen sc1\n\t" P1TRACE \
   "v_mov_b32 v48, 0\n\t" "v_mov_b32 v49, 0\n\t" "v_mfma_f32_16x16x32_bf16 v[40:43], v[152:155], v[24:27], v[40:43]\n\t" "v_mov_b32 v50, 0\n\t" \
   "v_mov_b32 v51, 0\n\t" "v_mov_b32 v52, 0\n\t" "v_mov_b32 v53, 0\n\t" "v_mfma_f32_16x16x32_bf16 v[40:43], v[156:159], v[28:31], v[40:43]\n\t" \
   "v_mov_b32 v54, 0\n\t" "v_mov_b32 v55, 0\n\t" "v_cndmask_b32 v32, v32, v125, s[20:21]\n\t" "v_cndmask_b32 v33, v33, v125, s[22:23]\n\t" \
   "v_cndmask_b32 v34, v34, v125, s[24:25]\n\t" "v_cndmask_b32 v35, v35, v125, s[26:27]\n\t" "v_cndmask_b32 v36, v36, v125, s[28:29]\n\t" \
   "v_cndmask_b32 v37, v37, v125, s[30:31]\n\t" "v_cndmask_b32 v38, v38, v125, s[32:33]\n\t" "v_cndmask_b32 v39, v39, v125, s[34:35]\n\t" \
   "v_exp_f32 v64, v32\n\t" "s_add_u32 s48, s69, s47\n\t" "s_cmp_eq_u32 s70, 0\n\t" "s_cselect_b32 s48, s48, -1\n\t" "v_exp_f32 v65, v33\n\t" \
   "s_mov_b32 s66, s65\n\t" "s_add_i32 s64, s64, 1\n\t" "s_and_b32 s71, s64, 63\n\t" "v_exp_f32 v66, v34\n\t" "s_lshr_b64 s[72:73], %[sel], s71\n\t" \
   "s_lshl_b32 s65, s71, 19\n\t" "s_and_b32 s72, s72, 1\n\t" "v_exp_f32 v67, v35\n\t" "s_add_u32 s65, s65, %[pool]\n\t" "s_lshl_b32 s72, s72, 13\n\t" \
   "s_add_u32 s65, s65, s72\n\t" "v_exp_f32 v68, v36\n\t" "s_xor_b32 s67, s54, %[lad0]\n\t" "s_xor_b32 s68, s67, 0x4000\n\t" \
   "s_add_u32 s67, s67, s66\n\t" "v_exp_f32 v69, v37\n\t" "s_add_u32 s68, s68, s66\n\t" "s_lshr_b64 s[72:73], %[s3], s71\n\t" \
   "s_and_b32 s54, s72, 1\n\t" "v_exp_f32 v70, v38\n\t" "s_lshl_b32 s54, s54, 14\n\t" "s_cmp_eq_u32 s64, 0\n\t" "s_cselect_b32 s70, 1, 0\n\t" \
   "v_exp_f32 v71, v39\n\t" "s_and_b32 s55, %[wv], 1\n\t" "s_lshl_b32 s55, s55, 14\n\t" "s_xor_b32 s55, s55, s54\n\t" "v_exp_f32 v72, v40\n\t" \
   "s_xor_b32 s50, s55, 0x4000\n\t" "s_add_u32 s55, s55, s65\n\t" "s_add_u32 s50, s50, s65\n\t" "v_exp_f32 v73, v41\n\t" "s_sub_u32 s53, s64, 63\n\t" \
   "s_mul_i32 s53, s53, s64\n\t" "v_mov_b32 v56, 0\n\t" "v_exp_f32 v74, v42\n\t" "v_mov_b32 v57, 0\n\t" "v_mov_b32 v58, 0\n\t" "v_mov_b32 v59, 0\n\t" \
   "v_exp_f32 v75, v43\n\t" "v_mov_b32 v60, 0\n\t" "v_mov_b32 v61, 0\n\t" "v_mov_b32 v62, 0\n\t" "v_mov_b32 v63, 0\n\t" "v_add_f32 v80, v64, v65\n\t" \
   "v_add_f32 v81, v66, v67\n\t" "v_add_f32 v82, v68, v69\n\t" "v_add_f32 v83, v70, v71\n\t" "v_add_f32 v84, v72, v73\n\t" \
   "v_add_f32 v85, v74, v75\n\t" "v_add_f32 v80, v80, v81\n\t" "v_add_f32 v82, v82, v83\n\t" "v_add_f32 v84, v84, v85\n\t" \
   "v_add_f32 v80, v80, v82\n\t" "v_fma_f32 v82, v80, 2.0, v84\n\t" "v_fma_f32 v80, v80, 2.0, v84\n\t" "ds_read_b128 v[160:163], v122 offset:0\n\t" \
   "ds_read_b128 v[164:167], v122 offset:1024\n\t" "v_permlane16_swap_b32 v80, v82\n\t" "v_add_f32 v81, v80, v82\n\t" "v_add_f32 v80, v80, v82\n\t" \
   "ds_read_b128 v[168:171], v122 offset:2048\n\t" "ds_read_b128 v[172:175], v122 offset:3072\n\t" "v_permlane32_swap_b32 v80, v81\n\t" \
   "v_add_f32 v80, v80, v81\n\t" "v_rcp_f32 v90, v80\n\t" "v_add_u32 v81, 0xe0800000, v80\n\t" "v_add_f32 v90, v90, v90\n\t" \
   "v_cmp_lt_u32 vcc, 0x40000000, v81\n\t" "ds_read_b128 v[176:179], v122 offset:4096\n\t" "ds_read_b128 v[180:183], v122 offset:5120\n\t" \
   "ds_read_b128 v[184:187], v122 offset:6144\n\t" "ds_read_b128 v[188:191], v122 offset:7168\n\t" "s_cbranch_vccnz 74f\n\t" "75:\n\t" \
   "v_pk_mul_f32 v[64:65], v[64:65], v[90:91] op_sel_hi:[1,0]\n\t" "v_pk_mul_f32 v[66:67], v[66:67], v[90:91] op_sel_hi:[1,0]\n\t" \
   "v_pk_mul_f32 v[68:69], v[68:69], v[90:91] op_sel_hi:[1,0]\n\t" "v_pk_mul_f32 v[70:71], v[70:71], v[90:91] op_sel_hi:[1,0]\n\t" \
   "v_pk_mul_f32 v[72:73], v[72:73], v[90:91] op_sel_hi:[1,0]\n\t" "v_pk_mul_f32 v[74:75], v[74:75], v[90:91] op_sel_hi:[1,0]\n\t" \
   "v_cvt_pk_bf16_f32 v44, v64, v65\n\t" "v_cvt_pk_bf16_f32 v46, v66, v67\n\t" "v_cvt_pk_bf16_f32 v45, v68, v69\n\t" \
   "v_cvt_pk_bf16_f32 v47, v70, v71\n\t" "v_cvt_pk_bf16_f32 v32, v72, v73\n\t" "v_cvt_pk_bf16_f32 v33, v74, v75\n\t" "s_waitcnt vmcnt(1)\n\t" "48:\n\t" \
   "v_readlane_b32 s69, v108, 0\n\t" "v_readlane_b32 s71, v108, 1\n\t" "s_cmp_eq_u32 s64, 64\n\t" "s_cbranch_scc1 90f\n\t" "s_cmp_eq_u32 s69, -1\n\t" \
   "s_cbranch_scc1 63f\n\t" "s_cmp_eq_u32 s71, -1\n\t" "s_cselect_b32 s47, s47, s71\n\t" "50:\n\t" "s_add_u32 s73, s69, %[m1]\n\t" RSTAMP(5) "51:\n\t" \
   "s_memrealtime s[60:61]\n\t" "s_memtime s[62:63]\n\t" "s_waitcnt lgkmcnt(0)\n\t" "s_sub_u32 s74, s62, s57\n\t" "s_mul_i32 s74, s74, s58\n\t" \
   "s_lshr_b32 s74, s74, 12\n\t" "s_add_u32 s74, s74, s56\n\t" "s_lshl_b32 s60, s60, 3\n\t" "s_sub_u32 s75, s74, s60\n\t" "s_cmp_lt_u32 s75, 32\n\t" \
   "s_cbranch_scc1 52f\n\t" "s_cmp_lt_i32 s75, 0\n\t" "s_cselect_b32 s61, 0, 31\n\t" "s_add_u32 s74, s60, s61\n\t" "s_sub_i32 s75, s61, s75\n\t" \
   "s_add_i32 s75, s75, 1\n\t" "s_ashr_i32 s75, s75, 1\n\t" "s_min_i32 s75, s75, 8\n\t" "s_max_i32 s75, s75, -8\n\t" "s_mov_b32 s56, s74\n\t" \
   "s_mov_b32 s57, s62\n\t" "s_add_u32 s58, s58, s75\n\t" "52:\n\t" "s_sub_i32 s74, s73, s74\n\t" "s_cmp_le_i32 s74, 150\n\t" "s_cbranch_scc1 53f\n\t" \
   "s_cmp_lt_i32 s74, 260\n\t" "s_cbranch_scc1 51b\n\t" "s_sleep 2\n\t" "s_cmp_lt_i32 s74, 32000\n\t" "s_cbranch_scc1 51b\n\t" "s_mov_b32 s74, 0\n\t" \
   "53:\n\t" "s_sub_i32 s74, s74, 56\n\t" "s_max_i32 s74, s74, 0\n\t" "s_mul_i32 s74, s74, 23\n\t" "s_lshr_b32 s74, s74, 5\n\t" \
   "s_and_b32 s74, s74, -4\n\t" "s_getpc_b64 s[60:61]\n\t" "54:\n\t" "s_sub_u32 s74, 55f-54b, s74\n\t" "s_add_u32 s60, s60, s74\n\t" \
   "s_addc_u32 s61, s61, 0\n\t" "s_setpc_b64 s[60:61]\n\t" "s_nop 3\n\t" "s_nop 3\n\t" "s_nop 3\n\t" "s_nop 3\n\t" "s_nop 3\n\t" "s_nop 3\n\t" \
   "s_nop 3\n\t" "s_nop 3\n\t" "s_nop 3\n\t" "s_nop 3\n\t" "s_nop 3\n\t" "s_nop 3\n\t" "s_nop 3\n\t" "s_nop 3\n\t" "s_nop 3\n\t" "s_nop 3\n\t" \
   "s_nop 3\n\t" "s_nop 3\n\t" "s_nop 3\n\t" "s_nop 3\n\t" "s_nop 3\n\t" "s_nop 3\n\t" "s_nop 3\n\t" "s_nop 3\n\t" "55:\n\t" "20:\n\t" RSTAMP(4) \
   "buffer_load_dwordx4 v[64:67], v128, %[rs], s67 offen sc1\n\t" "buffer_load_dwordx4 v[68:71], v129, %[rs], s67 offen sc1\n\t" \
   "buffer_load_dwordx4 v[72:75], v130, %[rs], s67 offen sc1\n\t" "s_nop 7\n\t" "s_nop 7\n\t" "s_nop 7\n\t" "s_cmp_lg_u32 s70, 0\n\t" \
   "s_cbranch_scc1 27f\n\t" "buffer_load_dwordx4 v[16:19], v138, %[rs], s79 offen offset:0 sc1\n\t" \
   "buffer_load_dwordx4 v[20:23], v138, %[rs], s79 offen offset:1024 sc1\n\t" \
   "buffer_load_dwordx4 v[24:27], v138, %[rs], s79 offen offset:2048 sc1\n\t" \
   "buffer_load_dwordx4 v[28:31], v138, %[rs], s79 offen offset:3072 sc1\n\t" "v_or_b32 v108, s49, v141\n\t" "v_or_b32 v109, s49, v142\n\t" \
   "buffer_store_dwordx4 v[44:47], v108, %[rs], s52 offen sc1\n\t" "buffer_store_dwordx2 v[32:33], v109, %[rs], s51 offen sc1\n\t" "28:\n\t" RSTAMP(3) \
   "s_waitcnt vmcnt(8)\n\t" "ds_write_b128 v112, v[64:67]\n\t" "v_smfmac_f32_16x16x64_bf16 v[48:51], v[64:67], a[0:7], v134\n\t" \
   "s_waitcnt vmcnt(7)\n\t" "ds_write_b128 v113, v[68:71]\n\t" "s_waitcnt vmcnt(6)\n\t" "ds_write_b128 v114, v[72:75]\n\t" RSTAMP(7) "2:\n\t" \
   "v_smfmac_f32_16x16x64_bf16 v[52:55], v[64:67], a[64:71], v134\n\t" "v_max3_u32 v110, v64, v66, v68\n\t" "v_max3_u32 v111, v70, v72, v74\n\t" \
   "v_smfmac_f32_16x16x64_bf16 v[48:51], v[68:71], a[8:15], v134\n\t" "v_max_u32 v110, v110, v111\n\t" "v_cmp_eq_u32 vcc, -1, v110\n\t" \
   "v_smfmac_f32_16x16x64_bf16 v[52:55], v[68:71], a[72:79], v134\n\t" "s_cbranch_vccnz 8f\n\t" \
   "v_smfmac_f32_16x16x64_bf16 v[48:51], v[72:75], a[16:23], v134\n\t" "s_waitcnt lgkmcnt(0)\n\t" "s_barrier\n\t" RSTAMP(0) \
   "ds_read_b128 v[76:79], v115\n\t" "ds_read_b128 v[80:83], v116\n\t" "v_smfmac_f32_16x16x64_bf16 v[52:55], v[72:75], a[80:87], v134\n\t" \
   "ds_read_b128 v[84:87], v117\n\t" "ds_read_b128 v[88:91], v118\n\t" "v_smfmac_f32_16x16x64_bf16 v[56:59], v[64:67], a[128:135], v134\n\t" \
   "ds_read_b128 v[92:95], v119\n\t" "ds_read_b128 v[96:99], v120\n\t" "v_smfmac_f32_16x16x64_bf16 v[60:63], v[64:67], a[192:199], v134\n\t" \
   "ds_read_b128 v[100:103], v121\n\t" "v_smfmac_f32_16x16x64_bf16 v[56:59], v[68:71], a[136:143], v134\n\t" \
   "v_smfmac_f32_16x16x64_bf16 v[60:63], v[68:71], a[200:207], v134\n\t" "s_waitcnt lgkmcnt(6)\n\t" \
   "v_smfmac_f32_16x16x64_bf16 v[48:51], v[76:79], a[24:31], v134\n\t" "v_smfmac_f32_16x16x64_bf16 v[52:55], v[76:79], a[88:95], v134\n\t" \
   "s_waitcnt lgkmcnt(5)\n\t" "v_smfmac_f32_16x16x64_bf16 v[48:51], v[80:83], a[32:39], v134\n\t" \
   "v_smfmac_f32_16x16x64_bf16 v[52:55], v[80:83], a[96:103], v134\n\t" "s_waitcnt lgkmcnt(4)\n\t" \
   "v_smfmac_f32_16x16x64_bf16 v[48:51], v[84:87], a[40:47], v134\n\t" "v_smfmac_f32_16x16x64_bf16 v[52:55], v[84:87], a[104:111], v134\n\t" \
   "s_waitcnt lgkmcnt(3)\n\t" "v_smfmac_f32_16x16x64_bf16 v[48:51], v[88:91], a[48:55], v134\n\t" \
   "v_smfmac_f32_16x16x64_bf16 v[52:55], v[88:91], a[112:119], v134\n\t" "s_waitcnt lgkmcnt(2)\n\t" \
   "v_smfmac_f32_16x16x64_bf16 v[48:51], v[92:95], a[56:63], v134\n\t" "v_smfmac_f32_16x16x64_bf16 v[52:55], v[92:95], a[120:127], v134\n\t" \
   "s_waitcnt lgkmcnt(0)\n\t" "v_smfmac_f32_16x16x64_bf16 v[48:51], v[96:99], v[192:199], v134\n\t" \
   "v_smfmac_f32_16x16x64_bf16 v[48:51], v[100:103], v[200:207], v134\n\t" "v_smfmac_f32_16x16x64_bf16 v[52:55], v[96:99], v[208:215], v134\n\t" \
   "v_smfmac_f32_16x16x64_bf16 v[52:55], v[100:103], v[216:223], v134\n\t" "v_smfmac_f32_16x16x64_bf16 v[56:59], v[72:75], a[144:151], v134\n\t" \
   "v_add_f32 v48, v48, v49\n\t" "v_add_f32 v49, v50, v51\n\t" "v_smfmac_f32_16x16x64_bf16 v[60:63], v[72:75], a[208:215], v134\n\t" \
   "v_mul_f32 v48, 0x3e0293ee, v48\n\t" "v_mul_f32 v49, 0x3e0293ee, v49\n\t" "v_smfmac_f32_16x16x64_bf16 v[56:59], v[76:79], a[152:159], v134\n\t" \
   "v_add_f32 v52, v52, v53\n\t" "v_add_f32 v53, v54, v55\n\t" "v_smfmac_f32_16x16x64_bf16 v[56:59], v[80:83], a[160:167], v134\n\t" \
   "v_mul_f32 v52, 0x3e0293ee, v52\n\t" "v_mul_f32 v53, 0x3e0293ee, v53\n\t" "v_smfmac_f32_16x16x64_bf16 v[56:59], v[84:87], a[168:175], v134\n\t" \
   "v_cvt_pk_bf16_f32 v48, v48, v49\n\t" "ds_write_b32 v135, v48 offset:10240\n\t" \
   "v_smfmac_f32_16x16x64_bf16 v[56:59], v[88:91], a[176:183], v134\n\t" "v_cvt_pk_bf16_f32 v52, v52, v53\n\t" \
   "ds_write_b32 v135, v52 offset:10496\n\t" "v_smfmac_f32_16x16x64_bf16 v[56:59], v[92:95], a[184:191], v134\n\t" \
   "v_smfmac_f32_16x16x64_bf16 v[56:59], v[96:99], v[224:231], v134\n\t" "v_smfmac_f32_16x16x64_bf16 v[56:59], v[100:103], v[232:239], v134\n\t" \
   "s_waitcnt lgkmcnt(0)\n\t" "s_barrier\n\t" "ds_read_b64_tr_b16 v[64:65], v136 offset:10240\n\t" "ds_read_b64_tr_b16 v[66:67], v136 offset:10496\n\t" \
   "v_smfmac_f32_16x16x64_bf16 v[60:63], v[76:79], a[216:223], v134\n\t" "ds_read_b64_tr_b16 v[68:69], v136 offset:10752\n\t" \
   "v_smfmac_f32_16x16x64_bf16 v[60:63], v[80:83], a[224:231], v134\n\t" "ds_read_b64_tr_b16 v[70:71], v136 offset:11008\n\t" \
   "v_smfmac_f32_16x16x64_bf16 v[60:63], v[84:87], a[232:239], v134\n\t" "ds_read_b64_tr_b16 v[72:73], v136 offset:11264\n\t" \
   "v_smfmac_f32_16x16x64_bf16 v[60:63], v[88:91], a[240:247], v134\n\t" "ds_read_b64_tr_b16 v[74:75], v136 offset:11520\n\t" \
   "v_add_f32 v56, v56, v57\n\t" "v_add_f32 v57, v58, v59\n\t" "v_smfmac_f32_16x16x64_bf16 v[60:63], v[92:95], a[248:255], v134\n\t" \
   "ds_read_b64_tr_b16 v[104:105], v136 offset:11776\n\t" "ds_read_b64_tr_b16 v[106:107], v136 offset:12032\n\t" \
   "v_smfmac_f32_16x16x64_bf16 v[60:63], v[96:99], v[240:247], v134\n\t" "v_cvt_pk_bf16_f32 v56, v56, v57\n\t" \
   "ds_write_b32 v135, v56 offset:12288\n\t" "v_smfmac_f32_16x16x64_bf16 v[60:63], v[100:103], v[248:255], v134\n\t" RSTAMP(6) \
   "s_waitcnt lgkmcnt(7)\n\t" "v_mfma_f32_16x16x32_bf16 v[48:51], v[160:163], v[64:67], 0\n\t" \
   "v_mfma_f32_16x16x32_bf16 v[52:55], v[176:179], v[64:67], 0\n\t" "s_waitcnt lgkmcnt(3)\n\t" "s_nop 0\n\t" "v_add_f32 v60, v60, v61\n\t" \
   "v_add_f32 v61, v62, v63\n\t" "v_cvt_pk_bf16_f32 v60, v60, v61\n\t" "ds_write_b32 v135, v60 offset:12544\n\t" \
   "v_mfma_f32_16x16x32_bf16 v[48:51], v[164:167], v[68:71], v[48:51]\n\t" "v_mfma_f32_16x16x32_bf16 v[52:55], v[180:183], v[68:71], v[52:55]\n\t" \
   "v_mfma_f32_16x16x32_bf16 v[48:51], v[168:171], v[72:75], v[48:51]\n\t" "v_mfma_f32_16x16x32_bf16 v[52:55], v[184:187], v[72:75], v[52:55]\n\t" \
   "s_waitcnt lgkmcnt(2)\n\t" "v_mfma_f32_16x16x32_bf16 v[48:51], v[172:175], v[104:107], v[48:51]\n\t" \
   "v_mfma_f32_16x16x32_bf16 v[52:55], v[188:191], v[104:107], v[52:55]\n\t" "s_waitcnt lgkmcnt(0)\n\t" "s_barrier\n\t" \
   "ds_read_b64_tr_b16 v[144:145], v136 offset:12288\n\t" "ds_read_b64_tr_b16 v[146:147], v136 offset:12544\n\t" \
   "ds_read_b64_tr_b16 v[148:149], v136 offset:12800\n\t" "ds_read_b64_tr_b16 v[150:151], v136 offset:13056\n\t" \
   "ds_read_b64_tr_b16 v[152:153], v136 offset:13312\n\t" "ds_read_b64_tr_b16 v[154:155], v136 offset:13568\n\t" \
   "ds_read_b64_tr_b16 v[156:157], v136 offset:13824\n\t" "ds_read_b64_tr_b16 v[158:159], v136 offset:14080\n\t" "s_cmp_eq_u32 s53, 0\n\t" \
   "s_cbranch_scc1 91f\n\t" "93:\n\t" RSTAMP(1) "v_cndmask_b32 v48, v48, v52, %[hi8]\n\t" "v_cndmask_b32 v49, v49, v53, %[hi8]\n\t" \
   "v_cndmask_b32 v50, v50, v54, %[hi8]\n\t" "v_cndmask_b32 v51, v51, v55, %[hi8]\n\t" "v_exp_f32 v80, v48\n\t" "v_exp_f32 v81, v49\n\t" \
   "s_waitcnt lgkmcnt(6)\n\t" "v_mfma_f32_16x16x32_bf16 v[76:79], v[144:147], v[64:67], 0\n\t" "v_exp_f32 v82, v50\n\t" "v_exp_f32 v83, v51\n\t" \
   "s_waitcnt lgkmcnt(4)\n\t" "v_mfma_f32_16x16x32_bf16 v[76:79], v[148:151], v[68:71], v[76:79]\n\t" "v_add_f32 v96, v80, v81\n\t" \
   "v_add_f32 v103, v82, v83\n\t" "s_waitcnt lgkmcnt(2)\n\t" "v_mfma_f32_16x16x32_bf16 v[76:79], v[152:155], v[72:75], v[76:79]\n\t" \
   "v_add_f32 v94, v96, v103\n\t" "v_add_f32 v96, v96, v103\n\t" "s_waitcnt lgkmcnt(0)\n\t" \
   "v_mfma_f32_16x16x32_bf16 v[76:79], v[156:159], v[104:107], v[76:79]\n\t" "v_permlane32_swap_b32 v96, v94\n\t" "v_add_f32 v96, v96, v94\n\t" \
   "s_nop 1\n\t" "v_add_f32_dpp v96, v96, v96 row_ror:8 row_mask:0xf bank_mask:0xf\n\t" "s_nop 2\n\t" "v_exp_f32 v88, v76\n\t" "v_exp_f32 v89, v77\n\t" \
   "v_add_f32 v103, v96, v88\n\t" "v_exp_f32 v90, v78\n\t" "v_add_f32 v103, v103, v89\n\t" "v_exp_f32 v91, v79\n\t" "v_add_f32 v103, v103, v90\n\t" \
   "v_add_f32 v97, v103, v91\n\t" "v_add_f32 v103, v103, v91\n\t" "s_nop 1\n\t" "v_permlane16_swap_b32 v103, v97\n\t" "v_add_f32 v103, v103, v97\n\t" \
   "v_rcp_f32 v100, v103\n\t" "v_add_u32 v94, 0xe0800000, v103\n\t" "v_pk_mul_f32 v[80:81], v[80:81], v[100:101] op_sel_hi:[1,0]\n\t" \
   "v_cmp_lt_u32 vcc, 0x40000000, v94\n\t" "v_pk_mul_f32 v[82:83], v[82:83], v[100:101] op_sel_hi:[1,0]\n\t" "s_cbranch_vccnz 70f\n\t" \
   "v_cvt_pk_bf16_f32 v48, v80, v81\n\t" "v_cvt_pk_bf16_f32 v49, v82, v83\n\t" "buffer_store_dwordx2 v[48:49], v132, %[rs], s55 offen sc1\n\t" \
   "v_pk_mul_f32 v[88:89], v[88:89], v[100:101] op_sel_hi:[1,0]\n\t" "v_pk_mul_f32 v[90:91], v[90:91], v[100:101] op_sel_hi:[1,0]\n\t" \
   "v_cvt_pk_bf16_f32 v52, v88, v89\n\t" "v_cvt_pk_bf16_f32 v53, v90, v91\n\t" "71:\n\t" \
   "buffer_store_dwordx2 v[52:53], v133, %[rs], s65 offen sc1\n\t" "s_memtime s[62:63]\n\t" "s_memrealtime s[60:61]\n\t" \
   "buffer_store_dwordx2 v[48:49], v132, %[rs], s50 offen sc1\n\t" RSTAMP(2) "s_branch 40b\n\t" "70:\n\t" "v_max3_f32 v94, v48, v49, v50\n\t" \
   "v_max3_f32 v102, v76, v77, v78\n\t" "v_max3_f32 v94, v94, v51, v79\n\t" "v_max_f32 v92, v94, v102\n\t" "s_nop 1\n\t" \
   "v_max_f32_dpp v102, v92, v92 row_ror:8 row_mask:0xf bank_mask:0xf\n\t" "v_max_f32_dpp v94, v92, v92 row_ror:8 row_mask:0xf bank_mask:0xf\n\t" \
   "s_nop 1\n\t" "v_permlane32_swap_b32 v102, v94\n\t" "v_max_f32 v95, v102, v94\n\t" "v_max_f32 v102, v102, v94\n\t" "s_nop 1\n\t" \
   "v_permlane16_swap_b32 v102, v95\n\t" "v_max_f32 v102, v102, v95\n\t" "v_sub_f32 v80, v48, v102\n\t" "v_sub_f32 v88, v76, v102\n\t" \
   "v_sub_f32 v81, v49, v102\n\t" "v_sub_f32 v89, v77, v102\n\t" "v_sub_f32 v82, v50, v102\n\t" "v_sub_f32 v90, v78, v102\n\t" \
   "v_sub_f32 v83, v51, v102\n\t" "v_sub_f32 v91, v79, v102\n\t" "v_exp_f32 v80, v80\n\t" "v_exp_f32 v88, v88\n\t" "v_exp_f32 v81, v81\n\t" \
   "v_exp_f32 v89, v89\n\t" "v_exp_f32 v82, v82\n\t" "v_exp_f32 v90, v90\n\t" "v_exp_f32 v83, v83\n\t" "v_exp_f32 v91, v91\n\t" "s_nop 0\n\t" \
   "v_add_f32 v96, v80, v81\n\t" "v_add_f32 v103, v82, v83\n\t" "v_add_f32 v94, v96, v103\n\t" "v_add_f32 v96, v96, v103\n\t" "s_nop 1\n\t" \
   "v_permlane32_swap_b32 v96, v94\n\t" "v_add_f32 v96, v96, v94\n\t" "s_nop 1\n\t" \
   "v_add_f32_dpp v96, v96, v96 row_ror:8 row_mask:0xf bank_mask:0xf\n\t" "v_add_f32 v103, v96, v88\n\t" "v_add_f32 v103, v103, v89\n\t" \
   "v_add_f32 v103, v103, v90\n\t" "v_add_f32 v97, v103, v91\n\t" "v_add_f32 v103, v103, v91\n\t" "s_nop 1\n\t" "v_permlane16_swap_b32 v103, v97\n\t" \
   "v_add_f32 v103, v103, v97\n\t" "v_rcp_f32 v100, v103\n\t" "s_nop 0\n\t" "v_pk_mul_f32 v[80:81], v[80:81], v[100:101] op_sel_hi:[1,0]\n\t" \
   "v_pk_mul_f32 v[82:83], v[82:83], v[100:101] op_sel_hi:[1,0]\n\t" "v_pk_mul_f32 v[88:89], v[88:89], v[100:101] op_sel_hi:[1,0]\n\t" \
   "v_pk_mul_f32 v[90:91], v[90:91], v[100:101] op_sel_hi:[1,0]\n\t" "v_cvt_pk_bf16_f32 v48, v80, v81\n\t" "v_cvt_pk_bf16_f32 v49, v82, v83\n\t" \
   "v_cvt_pk_bf16_f32 v52, v88, v89\n\t" "v_cvt_pk_bf16_f32 v53, v90, v91\n\t" "v_min_u32 v48, -2, v48\n\t" "v_min_u32 v52, -2, v52\n\t" \
   "buffer_store_dwordx2 v[48:49], v132, %[rs], s55 offen sc1\n\t" "s_branch 71b\n\t" "74:\n\t" "v_max3_f32 v80, v32, v33, v34\n\t" \
   "v_max3_f32 v81, v35, v36, v37\n\t" "v_max3_f32 v82, v38, v39, v40\n\t" "v_max3_f32 v83, v41, v42, v43\n\t" "v_max3_f32 v80, v80, v81, v82\n\t" \
   "v_max_f32 v81, v80, v83\n\t" "v_max_f32 v80, v80, v83\n\t" "s_nop 1\n\t" "v_permlane16_swap_b32 v80, v81\n\t" "v_max_f32 v82, v80, v81\n\t" \
   "v_max_f32 v80, v80, v81\n\t" "s_nop 1\n\t" "v_permlane32_swap_b32 v80, v82\n\t" "v_max_f32 v80, v80, v82\n\t" "v_sub_f32 v64, v32, v80\n\t" \
   "v_sub_f32 v65, v33, v80\n\t" "v_sub_f32 v66, v34, v80\n\t" "v_sub_f32 v67, v35, v80\n\t" "v_sub_f32 v68, v36, v80\n\t" \
   "v_sub_f32 v69, v37, v80\n\t" "v_sub_f32 v70, v38, v80\n\t" "v_sub_f32 v71, v39, v80\n\t" "v_sub_f32 v72, v40, v80\n\t" \
   "v_sub_f32 v73, v41, v80\n\t" "v_sub_f32 v74, v42, v80\n\t" "v_sub_f32 v75, v43, v80\n\t" "v_exp_f32 v64, v64\n\t" "v_exp_f32 v65, v65\n\t" \
   "v_exp_f32 v66, v66\n\t" "v_exp_f32 v67, v67\n\t" "v_exp_f32 v68, v68\n\t" "v_exp_f32 v69, v69\n\t" "v_exp_f32 v70, v70\n\t" \
   "v_exp_f32 v71, v71\n\t" "v_exp_f32 v72, v72\n\t" "v_exp_f32 v73, v73\n\t" "v_exp_f32 v74, v74\n\t" "v_exp_f32 v75, v75\n\t" "s_nop 0\n\t" \
   "v_add_f32 v80, v64, v65\n\t" "v_add_f32 v81, v66, v67\n\t" "v_add_f32 v82, v68, v69\n\t" "v_add_f32 v83, v70, v71\n\t" \
   "v_add_f32 v84, v72, v73\n\t" "v_add_f32 v85, v74, v75\n\t" "v_add_f32 v80, v80, v81\n\t" "v_add_f32 v82, v82, v83\n\t" \
   "v_add_f32 v84, v84, v85\n\t" "v_add_f32 v80, v80, v82\n\t" "v_fma_f32 v82, v80, 2.0, v84\n\t" "v_fma_f32 v80, v80, 2.0, v84\n\t" "s_nop 0\n\t" \
   "s_nop 0\n\t" "v_permlane16_swap_b32 v80, v82\n\t" "v_add_f32 v81, v80, v82\n\t" "v_add_f32 v80, v80, v82\n\t" "s_nop 0\n\t" "s_nop 0\n\t" \
   "v_permlane32_swap_b32 v80, v81\n\t" "v_add_f32 v80, v80, v81\n\t" "v_rcp_f32 v90, v80\n\t" "s_nop 0\n\t" "v_add_f32 v90, v90, v90\n\t" \
   "s_branch 75b\n\t" "63:\n\t" "s_cmp_lg_u32 s64, 0\n\t" "s_cbranch_scc1 64f\n\t" "s_sub_u32 s69, s56, 0x10000\n\t" "s_and_b32 s69, s69, -2\n\t" \
   "s_branch 50b\n\t" "64:\n\t" "s_mov_b32 s69, s48\n\t" "s_cmp_lt_u32 s64, 3\n\t" "s_cselect_b32 s69, -1, s69\n\t" "s_cmp_lg_u32 s69, -1\n\t" \
   "s_cbranch_scc1 50b\n\t" "s_add_u32 s76, s76, 0x1000000\n\t" "s_mov_b32 s73, 0\n\t" "65:\n\t" "s_add_u32 s73, s73, 1\n\t" \
   "s_min_u32 s72, s73, 7\n\t" "s_mul_i32 s72, s72, 5\n\t" "s_add_u32 s72, s72, %[xs]\n\t" "s_and_b32 s72, s72, 7\n\t" "s_lshl_b32 s71, %[xs], 7\n\t" \
   "v_subrev_u32 v109, s71, v139\n\t" "s_lshl_b32 s72, s72, 7\n\t" "s_add_u32 s72, s72, s66\n\t" "s_cmp_lt_u32 s73, 7\n\t" "s_cbranch_scc1 66f\n\t" \
   "s_sleep 4\n\t" "66:\n\t" "buffer_load_dword v108, v109, %[rs], s72 offen sc1\n\t" "s_waitcnt vmcnt(0)\n\t" "v_readfirstlane_b32 s69, v108\n\t" \
   "s_cmp_gt_u32 s73, 0x10000\n\t" "s_cbranch_scc1 50b\n\t" "s_cmp_eq_u32 s69, -1\n\t" "s_cbranch_scc1 65b\n\t" "s_branch 50b\n\t" "8:\n\t" \
   "s_nop 7\n\t" "s_waitcnt vmcnt(6) lgkmcnt(0)\n\t" "s_add_u32 s70, s70, 1\n\t" "s_cmp_gt_u32 s70, 2\n\t" "s_cbranch_scc1 81f\n\t" \
   "s_lshl_b32 s71, s70, 3\n\t" "s_lshl_b32 s71, 1, s71\n\t" "s_add_u32 s76, s76, s71\n\t" "81:\n\t" "v_mov_b32 v48, 0\n\t" "v_mov_b32 v49, 0\n\t" \
   "v_mov_b32 v50, 0\n\t" "v_mov_b32 v51, 0\n\t" "v_mov_b32 v52, 0\n\t" "v_mov_b32 v53, 0\n\t" "v_mov_b32 v54, 0\n\t" "v_mov_b32 v55, 0\n\t" \
   "v_mov_b32 v56, 0\n\t" "v_mov_b32 v57, 0\n\t" "v_mov_b32 v58, 0\n\t" "v_mov_b32 v59, 0\n\t" "v_mov_b32 v60, 0\n\t" "v_mov_b32 v61, 0\n\t" \
   "v_mov_b32 v62, 0\n\t" "v_mov_b32 v63, 0\n\t" "s_min_u32 s71, s70, 7\n\t" "s_lshr_b32 s67, s71, 1\n\t" "s_add_u32 s67, s67, %[xs]\n\t" \
   "s_and_b32 s67, s67, 3\n\t" "s_lshl_b32 s67, s67, 11\n\t" "s_and_b32 s72, s71, 1\n\t" "s_lshl_b32 s72, s72, 14\n\t" "s_add_u32 s67, s67, s72\n\t" \
   "s_add_u32 s67, s67, s66\n\t" "s_cmp_eq_u32 s70, 1\n\t" "s_cselect_b32 s67, s68, s67\n\t" "s_cmp_lt_u32 s70, 7\n\t" "s_cbranch_scc1 20b\n\t" \
   "s_sleep 3\n\t" "s_cmp_lt_u32 s70, 0x20000\n\t" "s_cbranch_scc1 20b\n\t" "s_mov_b32 s67, %[esc]\n\t" "s_mov_b32 s68, %[esc]\n\t" "s_branch 20b\n\t" \
   "27:\n\t" "v_mov_b32 v98, 0x7ffffff0\n\t" "buffer_store_dword v98, v98, %[rs], 0 offen\n\t" "buffer_store_dword v98, v98, %[rs], 0 offen\n\t" \
   "buffer_store_dword v98, v98, %[rs], 0 offen\n\t" "buffer_store_dword v98, v98, %[rs], 0 offen\n\t" \
   "buffer_store_dword v98, v98, %[rs], 0 offen\n\t" "buffer_store_dword v98, v98, %[rs], 0 offen\n\t" "s_branch 28b\n\t" "141:\n\t" \
   "s_mov_b32 s74, 0\n\t" "s_cmp_eq_u32 s64, 0\n\t" "s_cbranch_scc1 41b\n\t" "42:\n\t" "buffer_load_dword v15, v140, %[rs], s59 offen sc1\n\t" \
   "s_add_u32 s74, s74, 1\n\t" "s_waitcnt vmcnt(0)\n\t" "v_cmp_eq_u32 vcc, -1, v15\n\t" "s_cmp_gt_u32 s74, 0x10000\n\t" "s_cbranch_scc1 41b\n\t" \
   "s_cbranch_vccz 41b\n\t" "s_sleep 2\n\t" "s_branch 42b\n\t" "95:\n\t" "s_movk_i32 s59, 0x1000\n\t" "s_branch 197f\n\t" "97:\n\t" \
   "buffer_load_dwordx4 v[16:19], v138, %[rs], s79 offen offset:0 sc1\n\t" "buffer_load_dwordx4 v[20:23], v138, %[rs], s79 offen offset:1024 sc1\n\t" \
   "buffer_load_dwordx4 v[24:27], v138, %[rs], s79 offen offset:2048 sc1\n\t" \
   "buffer_load_dwordx4 v[28:31], v138, %[rs], s79 offen offset:3072 sc1\n\t" "v_or_b32 v108, s49, v141\n\t" "v_or_b32 v109, s49, v142\n\t" \
   "buffer_store_dwordx4 v[44:47], v108, %[rs], s52 offen sc1\n\t" "buffer_store_dwordx2 v[32:33], v109, %[rs], s51 offen sc1\n\t" \
   "s_waitcnt vmcnt(0)\n\t" "s_branch 96b\n\t" "197:\n\t" "s_mov_b32 s74, 0\n\t" "98:\n\t" "buffer_load_dword v15, v140, %[rs], s59 offen sc1\n\t" \
   "s_add_u32 s74, s74, 1\n\t" "s_waitcnt vmcnt(0)\n\t" "v_cmp_eq_u32 vcc, -1, v15\n\t" "s_cmp_gt_u32 s74, 0x10000\n\t" "s_cbranch_scc1 97b\n\t" \
   "s_cbranch_vccz 97b\n\t" "s_sleep 2\n\t" "s_branch 98b\n\t" "91:\n\t" "s_waitcnt vmcnt(0)\n\t" "s_cmp_lg_u32 s64, 0\n\t" "s_cbranch_scc1 40b\n\t" \
   "s_mov_b32 s59, 0\n\t" "s_branch 145f\n\t" "45:\n\t" "v_mbcnt_lo_u32_b32 v98, -1, 0\n\t" "v_mbcnt_hi_u32_b32 v98, -1, v98\n\t" \
   "v_lshlrev_b32 v98, 4, v98\n\t" "s_add_u32 s72, %[kf0], 4096\n\t" "buffer_load_dwordx4 v[160:163], v98, %[rs], %[kf0] offen offset:0 sc1\n\t" \
   "buffer_load_dwordx4 v[164:167], v98, %[rs], %[kf0] offen offset:1024 sc1\n\t" \
   "buffer_load_dwordx4 v[168:171], v98, %[rs], %[kf0] offen offset:2048 sc1\n\t" \
   "buffer_load_dwordx4 v[172:175], v98, %[rs], %[kf0] offen offset:3072 sc1\n\t" \
   "buffer_load_dwordx4 v[176:179], v98, %[rs], s72 offen offset:0 sc1\n\t" "buffer_load_dwordx4 v[180:183], v98, %[rs], s72 offen offset:1024 sc1\n\t" \
   "buffer_load_dwordx4 v[184:187], v98, %[rs], s72 offen offset:2048 sc1\n\t" \
   "buffer_load_dwordx4 v[188:191], v98, %[rs], s72 offen offset:3072 sc1\n\t" "s_waitcnt vmcnt(0)\n\t" \
   "v_mfma_f32_16x16x32_bf16 v[48:51], v[160:163], v[64:67], 0\n\t" "v_mfma_f32_16x16x32_bf16 v[52:55], v[176:179], v[64:67], 0\n\t" \
   "v_mfma_f32_16x16x32_bf16 v[48:51], v[164:167], v[68:71], v[48:51]\n\t" "v_mfma_f32_16x16x32_bf16 v[52:55], v[180:183], v[68:71], v[52:55]\n\t" \
   "v_mfma_f32_16x16x32_bf16 v[48:51], v[168:171], v[72:75], v[48:51]\n\t" "v_mfma_f32_16x16x32_bf16 v[52:55], v[184:187], v[72:75], v[52:55]\n\t" \
   "v_mfma_f32_16x16x32_bf16 v[48:51], v[172:175], v[104:107], v[48:51]\n\t" "v_mfma_f32_16x16x32_bf16 v[52:55], v[188:191], v[104:107], v[52:55]\n\t" \
   "s_nop 7\n\t" "s_branch 93b\n\t" "145:\n\t" "s_mov_b32 s74, 0\n\t" "46:\n\t" "buffer_load_dword v15, v140, %[rs], s59 offen sc1\n\t" \
   "s_add_u32 s74, s74, 1\n\t" "s_waitcnt vmcnt(0)\n\t" "v_cmp_eq_u32 vcc, -1, v15\n\t" "s_cmp_gt_u32 s74, 0x10000\n\t" "s_cbranch_scc1 45b\n\t" \
   "s_cbranch_vccz 45b\n\t" "s_sleep 2\n\t" "s_branch 46b\n\t" "90:\n\t" "v_or_b32 v108, s49, v141\n\t" "v_or_b32 v109, s49, v142\n\t" \
   "buffer_store_dwordx4 v[44:47], v108, %[rs], s52 offen sc1\n\t" "buffer_store_dwordx2 v[32:33], v109, %[rs], s51 offen sc1\n\t" \
   "s_waitcnt vmcnt(0) lgkmcnt(0)"
#define P1_CLOB "v15", "v16", "v17", "v18", "v19", "v20", "v21", "v22", "v23", "v24", "v25", "v26", "v27", "v28", "v29", "v30", "v31", "v32", "v33", "v34", "v35", "v36", "v37", "v38", "v39", "v40", "v41", "v42", "v43", "v44", "v45", "v46", "v47", "v48", "v49", "v50", "v51", "v52", "v53", "v54", "v55", "v56", "v57", "v58", "v59", "v60", "v61", "v62", "v63", "v64", "v65", "v66", "v67", "v68", "v69", "v70", "v71", "v72", "v73", "v74", "v75", "v76", "v77", "v78", "v79", "v80", "v81", "v82", "v83", "v84", "v85", "v86", "v87", "v88", "v89", "v90", "v91", "v92", "v93", "v94", "v95", "v96", "v97", "v98", "v99", "v100", "v101", "v102", "v103", "v104", "v105", "v106", "v107", "v108", "v109", "v110", "v111", "v144", "v145", "v146", "v147", "v148", "v149", "v150", "v151", "v152", "v153", "v154", "v155", "v156", "v157", "v158", "v159", "v160", "v161", "v162", "v163", "v164", "v165", "v166", "v167", "v168", "v169", "v170", "v171", "v172", "v173", "v174", "v175", "v176", "v177", "v178", "v179", "v180", "v181", "v182", "v183", "v184", "v185", "v186", "v187", "v188", "v189", "v190", "v191", "s60", "s61", "s62", "s63", "s64", "s65", "s66", "s67", "s68", "s69", "s70", "s71", "s72", "s73", "s74", "s75", "s47", "s48", "s49", "s50", "s51", "s52", "s53", "s54", "s55", "s79", "s20", "s21", "s22", "s23", "s24", "s25", "s26", "s27", "s28", "s29", "s30", "s31", "s32", "s33", "s34", "s35"
#define PV_ASM \
   ".p2align 6\n\t" "s_nop 0\n\t" "s_nop 0\n\t" "s_nop 0\n\t" "s_nop 0\n\t" "s_nop 0\n\t" "s_nop 0\n\t" "s_nop 0\n\t" "s_nop 0\n\t" \
   "s_mov_b32 s64, -1\n\t" "s_mov_b32 s76, 0\n\t" "s_mov_b32 s77, 0\n\t" "s_mov_b32 s71, 63\n\t" "s_mov_b32 s47, 1300\n\t" "s_mov_b32 s70, 0\n\t" \
   "s_mov_b32 s69, 0\n\t" "s_lshr_b64 s[72:73], %[sel], s71\n\t" "s_lshl_b32 s65, s71, 19\n\t" "s_and_b32 s72, s72, 1\n\t" \
   "s_add_u32 s65, s65, %[pool]\n\t" "s_lshl_b32 s72, s72, 13\n\t" "s_add_u32 s65, s65, s72\n\t" "s_lshr_b64 s[72:73], %[s3], s71\n\t" \
   "s_and_b32 s54, s72, 1\n\t" "s_lshl_b32 s54, s54, 14\n\t" "40:\n\t" "s_cmp_lt_i32 s64, 0\n\t" "s_cselect_b32 s72, 0x7ffffff0, 0\n\t" \
   "s_max_i32 s71, s64, 0\n\t" "v_or_b32 v109, s72, v143\n\t" "s_lshl_b32 s71, s71, 12\n\t" "v_mov_b32 v110, 1\n\t" "s_cmp_eq_u32 s64, 63\n\t" \
   "s_cbranch_scc1 70f\n\t" "71:\n\t" "s_waitcnt vmcnt(0)\n\t" RSTAMP(3) "s_barrier\n\t" "buffer_load_dword v108, v139, %[rs], s65 offen sc1\n\t" \
   "buffer_store_dword v110, v109, %[rs], s71 offen sc1\n\t" PVTRACE "s_add_u32 s48, s69, s47\n\t" "s_cmp_eq_u32 s70, 0\n\t" \
   "s_cselect_b32 s48, s48, -1\n\t" "s_mov_b32 s66, s65\n\t" "s_add_i32 s64, s64, 1\n\t" "s_and_b32 s71, s64, 63\n\t" \
   "s_lshr_b64 s[72:73], %[sel], s71\n\t" "s_lshl_b32 s65, s71, 19\n\t" "s_and_b32 s72, s72, 1\n\t" "s_add_u32 s65, s65, %[pool]\n\t" \
   "s_lshl_b32 s72, s72, 13\n\t" "s_add_u32 s65, s65, s72\n\t" "s_xor_b32 s67, s54, %[lad0]\n\t" "s_xor_b32 s68, s67, 0x4000\n\t" \
   "s_add_u32 s67, s67, s66\n\t" "s_add_u32 s68, s68, s66\n\t" "s_lshr_b64 s[72:73], %[s3], s71\n\t" "s_and_b32 s54, s72, 1\n\t" \
   "s_lshl_b32 s54, s54, 14\n\t" "s_mov_b32 s70, 0\n\t" "s_lshr_b64 s[72:73], %[ysel], s71\n\t" "s_mul_i32 s73, s71, 43\n\t" \
   "s_mul_i32 s49, s71, 80\n\t" "s_and_b32 s72, s72, 1\n\t" "s_lshr_b32 s73, s73, 7\n\t" "s_add_u32 s49, s49, s72\n\t" \
   "s_lshl4_add_u32 s49, s73, s49\n\t" "s_lshl_b32 s49, s49, 13\n\t" "s_add_u32 s49, s49, %[yb]\n\t" "s_add_u32 s50, s49, 32768\n\t" \
   "s_add_u32 s51, s49, 65536\n\t" "s_add_u32 s52, s49, 98304\n\t" "s_add_u32 s53, s49, 131072\n\t" "v_mov_b32 v48, 0\n\t" "v_mov_b32 v49, 0\n\t" \
   "v_mov_b32 v50, 0\n\t" "v_mov_b32 v51, 0\n\t" "v_mov_b32 v52, 0\n\t" "v_mov_b32 v53, 0\n\t" "v_mov_b32 v54, 0\n\t" "v_mov_b32 v55, 0\n\t" \
   "v_mov_b32 v144, 0\n\t" "v_mov_b32 v145, 0\n\t" "v_mov_b32 v146, 0\n\t" "v_mov_b32 v147, 0\n\t" "v_mov_b32 v148, 0\n\t" "v_mov_b32 v149, 0\n\t" \
   "v_mov_b32 v150, 0\n\t" "v_mov_b32 v151, 0\n\t" "v_mov_b32 v152, 0\n\t" "v_mov_b32 v153, 0\n\t" "v_mov_b32 v154, 0\n\t" "v_mov_b32 v155, 0\n\t" \
   "v_mov_b32 v156, 0\n\t" "v_mov_b32 v157, 0\n\t" "v_mov_b32 v158, 0\n\t" "v_mov_b32 v159, 0\n\t" "v_mov_b32 v160, 0\n\t" "v_mov_b32 v161, 0\n\t" \
   "v_mov_b32 v162, 0\n\t" "v_mov_b32 v163, 0\n\t" "s_cmp_eq_u32 s64, 64\n\t" "s_cbranch_scc1 90f\n\t" "s_waitcnt vmcnt(1)\n\t" \
   "v_readlane_b32 s69, v108, 0\n\t" "v_readlane_b32 s71, v108, 1\n\t" "s_cmp_eq_u32 s69, -1\n\t" "s_cbranch_scc1 63f\n\t" "s_cmp_eq_u32 s71, -1\n\t" \
   "s_cselect_b32 s47, s47, s71\n\t" "50:\n\t" "s_add_u32 s73, s69, %[m1c]\n\t" RSTAMP(5) "51:\n\t" "s_memrealtime s[60:61]\n\t" \
   "s_memtime s[62:63]\n\t" "s_waitcnt lgkmcnt(0)\n\t" "s_sub_u32 s74, s62, s57\n\t" "s_mul_i32 s74, s74, s58\n\t" "s_lshr_b32 s74, s74, 12\n\t" \
   "s_add_u32 s74, s74, s56\n\t" "s_lshl_b32 s60, s60, 3\n\t" "s_sub_u32 s75, s74, s60\n\t" "s_cmp_lt_u32 s75, 32\n\t" "s_cbranch_scc1 52f\n\t" \
   "s_cmp_lt_i32 s75, 0\n\t" "s_cselect_b32 s61, 0, 31\n\t" "s_add_u32 s74, s60, s61\n\t" "s_sub_i32 s75, s61, s75\n\t" "s_add_i32 s75, s75, 1\n\t" \
   "s_ashr_i32 s75, s75, 1\n\t" "s_min_i32 s75, s75, 8\n\t" "s_max_i32 s75, s75, -8\n\t" "s_mov_b32 s56, s74\n\t" "s_mov_b32 s57, s62\n\t" \
   "s_add_u32 s58, s58, s75\n\t" "52:\n\t" "s_sub_i32 s74, s73, s74\n\t" "s_cmp_le_i32 s74, 150\n\t" "s_cbranch_scc1 53f\n\t" \
   "s_cmp_lt_i32 s74, 260\n\t" "s_cbranch_scc1 51b\n\t" "s_sleep 2\n\t" "s_cmp_lt_i32 s74, 32000\n\t" "s_cbranch_scc1 51b\n\t" "s_mov_b32 s74, 0\n\t" \
   "53:\n\t" "s_sub_i32 s74, s74, 56\n\t" "s_max_i32 s74, s74, 0\n\t" "s_mul_i32 s74, s74, 23\n\t" "s_lshr_b32 s74, s74, 5\n\t" \
   "s_and_b32 s74, s74, -4\n\t" "s_getpc_b64 s[60:61]\n\t" "54:\n\t" "s_sub_u32 s74, 55f-54b, s74\n\t" "s_add_u32 s60, s60, s74\n\t" \
   "s_addc_u32 s61, s61, 0\n\t" "s_setpc_b64 s[60:61]\n\t" "s_nop 3\n\t" "s_nop 3\n\t" "s_nop 3\n\t" "s_nop 3\n\t" "s_nop 3\n\t" "s_nop 3\n\t" \
   "s_nop 3\n\t" "s_nop 3\n\t" "s_nop 3\n\t" "s_nop 3\n\t" "s_nop 3\n\t" "s_nop 3\n\t" "s_nop 3\n\t" "s_nop 3\n\t" "s_nop 3\n\t" "s_nop 3\n\t" \
   "s_nop 3\n\t" "s_nop 3\n\t" "s_nop 3\n\t" "s_nop 3\n\t" "s_nop 3\n\t" "s_nop 3\n\t" "s_nop 3\n\t" "s_nop 3\n\t" "55:\n\t" "20:\n\t" RSTAMP(4) \
   "buffer_load_dwordx4 v[64:67], v128, %[rs], s67 offen sc1\n\t" "buffer_load_dwordx4 v[68:71], v129, %[rs], s67 offen sc1\n\t" \
   "buffer_load_dwordx4 v[72:75], v130, %[rs], s67 offen sc1\n\t" "s_waitcnt vmcnt(2)\n\t" "ds_write_b128 v112, v[64:67]\n\t" \
   "v_smfmac_f32_16x16x64_bf16 v[48:51], v[64:67], a[0:7], v134\n\t" "s_waitcnt vmcnt(1)\n\t" "ds_write_b128 v113, v[68:71]\n\t" \
   "s_waitcnt vmcnt(0)\n\t" "ds_write_b128 v114, v[72:75]\n\t" RSTAMP(7) "v_smfmac_f32_16x16x64_bf16 v[52:55], v[64:67], a[64:71], v134\n\t" \
   "v_max3_u32 v110, v64, v66, v68\n\t" "v_max3_u32 v111, v70, v72, v74\n\t" "v_smfmac_f32_16x16x64_bf16 v[48:51], v[68:71], a[8:15], v134\n\t" \
   "v_max_u32 v110, v110, v111\n\t" "v_cmp_eq_u32 vcc, -1, v110\n\t" "v_smfmac_f32_16x16x64_bf16 v[52:55], v[68:71], a[72:79], v134\n\t" \
   "s_cbranch_vccnz 8f\n\t" "s_waitcnt lgkmcnt(0)\n\t" "s_barrier\n\t" RSTAMP(0) "ds_read_b128 v[76:79], v115\n\t" "ds_read_b128 v[80:83], v116\n\t" \
   "ds_read_b128 v[84:87], v117\n\t" "v_smfmac_f32_16x16x64_bf16 v[48:51], v[72:75], a[16:23], v134\n\t" "ds_read_b128 v[88:91], v118\n\t" \
   "ds_read_b128 v[92:95], v119\n\t" "v_smfmac_f32_16x16x64_bf16 v[52:55], v[72:75], a[80:87], v134\n\t" "ds_read_b128 v[96:99], v120\n\t" \
   "ds_read_b128 v[100:103], v121\n\t" "s_waitcnt lgkmcnt(6)\n\t" "v_smfmac_f32_16x16x64_bf16 v[48:51], v[76:79], a[24:31], v134\n\t" \
   "v_smfmac_f32_16x16x64_bf16 v[52:55], v[76:79], a[88:95], v134\n\t" "s_waitcnt lgkmcnt(5)\n\t" \
   "v_smfmac_f32_16x16x64_bf16 v[48:51], v[80:83], a[32:39], v134\n\t" "v_smfmac_f32_16x16x64_bf16 v[52:55], v[80:83], a[96:103], v134\n\t" \
   "s_waitcnt lgkmcnt(4)\n\t" "v_smfmac_f32_16x16x64_bf16 v[48:51], v[84:87], a[40:47], v134\n\t" \
   "v_smfmac_f32_16x16x64_bf16 v[52:55], v[84:87], a[104:111], v134\n\t" "s_waitcnt lgkmcnt(3)\n\t" \
   "v_smfmac_f32_16x16x64_bf16 v[48:51], v[88:91], a[48:55], v134\n\t" "v_smfmac_f32_16x16x64_bf16 v[52:55], v[88:91], a[112:119], v134\n\t" \
   "s_waitcnt lgkmcnt(0)\n\t" "v_smfmac_f32_16x16x64_bf16 v[48:51], v[92:95], a[56:63], v134\n\t" \
   "v_smfmac_f32_16x16x64_bf16 v[48:51], v[96:99], v[192:199], v134\n\t" "v_smfmac_f32_16x16x64_bf16 v[48:51], v[100:103], v[200:207], v134\n\t" \
   "v_smfmac_f32_16x16x64_bf16 v[52:55], v[92:95], a[120:127], v134\n\t" "v_smfmac_f32_16x16x64_bf16 v[52:55], v[96:99], v[208:215], v134\n\t" \
   "v_smfmac_f32_16x16x64_bf16 v[52:55], v[100:103], v[216:223], v134\n\t" "v_add_f32 v48, v48, v49\n\t" "v_add_f32 v49, v50, v51\n\t" \
   "v_cvt_pk_bf16_f32 v48, v48, v49\n\t" "ds_write_b32 v135, v48 offset:10240\n\t" "s_nop 3\n\t" "v_add_f32 v52, v52, v53\n\t" \
   "v_add_f32 v53, v54, v55\n\t" "v_cvt_pk_bf16_f32 v52, v52, v53\n\t" "ds_write_b32 v135, v52 offset:10496\n\t" "s_waitcnt lgkmcnt(0)\n\t" \
   "s_barrier\n\t" RSTAMP(6) "ds_read_b64_tr_b16 v[16:17], v136 offset:10240\n\t" "ds_read_b64_tr_b16 v[18:19], v136 offset:10496\n\t" \
   "ds_read_b64_tr_b16 v[20:21], v136 offset:10752\n\t" "ds_read_b64_tr_b16 v[22:23], v136 offset:11008\n\t" \
   "ds_read_b64_tr_b16 v[24:25], v136 offset:11264\n\t" "ds_read_b64_tr_b16 v[26:27], v136 offset:11520\n\t" \
   "ds_read_b64_tr_b16 v[28:29], v136 offset:11776\n\t" "ds_read_b64_tr_b16 v[30:31], v136 offset:12032\n\t" "s_waitcnt lgkmcnt(6)\n\t" \
   "v_smfmac_f32_16x16x64_bf16 v[144:147], v[16:19], a[128:135], v131\n\t" "v_smfmac_f32_16x16x64_bf16 v[148:151], v[16:19], a[160:167], v131\n\t" \
   "v_smfmac_f32_16x16x64_bf16 v[152:155], v[16:19], a[192:199], v131\n\t" "v_smfmac_f32_16x16x64_bf16 v[156:159], v[16:19], a[224:231], v131\n\t" \
   "v_smfmac_f32_16x16x64_bf16 v[160:163], v[16:19], v[224:231], v131\n\t" "s_waitcnt lgkmcnt(0)\n\t" \
   "v_smfmac_f32_16x16x64_bf16 v[144:147], v[20:23], a[136:143], v131\n\t" "v_smfmac_f32_16x16x64_bf16 v[144:147], v[24:27], a[144:151], v131\n\t" \
   "v_smfmac_f32_16x16x64_bf16 v[144:147], v[28:31], a[152:159], v131\n\t" "v_smfmac_f32_16x16x64_bf16 v[148:151], v[20:23], a[168:175], v131\n\t" \
   "v_smfmac_f32_16x16x64_bf16 v[148:151], v[24:27], a[176:183], v131\n\t" "v_smfmac_f32_16x16x64_bf16 v[148:151], v[28:31], a[184:191], v131\n\t" \
   "v_smfmac_f32_16x16x64_bf16 v[152:155], v[20:23], a[200:207], v131\n\t" "v_cvt_pk_bf16_f32 v164, v144, v145\n\t" \
   "v_cvt_pk_bf16_f32 v165, v146, v147\n\t" "v_smfmac_f32_16x16x64_bf16 v[152:155], v[24:27], a[208:215], v131\n\t" "v_min_u32 v164, -2, v164\n\t" \
   "buffer_store_dwordx2 v[164:165], v132, %[rs], s49 offen sc1\n\t" "v_smfmac_f32_16x16x64_bf16 v[152:155], v[28:31], a[216:223], v131\n\t" \
   "v_smfmac_f32_16x16x64_bf16 v[156:159], v[20:23], a[232:239], v131\n\t" "v_cvt_pk_bf16_f32 v166, v148, v149\n\t" \
   "v_cvt_pk_bf16_f32 v167, v150, v151\n\t" "v_smfmac_f32_16x16x64_bf16 v[156:159], v[24:27], a[240:247], v131\n\t" "v_min_u32 v166, -2, v166\n\t" \
   "buffer_store_dwordx2 v[166:167], v132, %[rs], s50 offen sc1\n\t" "v_smfmac_f32_16x16x64_bf16 v[156:159], v[28:31], a[248:255], v131\n\t" \
   "v_smfmac_f32_16x16x64_bf16 v[160:163], v[20:23], v[232:239], v131\n\t" "v_cvt_pk_bf16_f32 v168, v152, v153\n\t" \
   "v_cvt_pk_bf16_f32 v169, v154, v155\n\t" "v_smfmac_f32_16x16x64_bf16 v[160:163], v[24:27], v[240:247], v131\n\t" "v_min_u32 v168, -2, v168\n\t" \
   "buffer_store_dwordx2 v[168:169], v132, %[rs], s51 offen sc1\n\t" "v_smfmac_f32_16x16x64_bf16 v[160:163], v[28:31], v[248:255], v131\n\t" \
   "v_cvt_pk_bf16_f32 v170, v156, v157\n\t" "v_cvt_pk_bf16_f32 v171, v158, v159\n\t" "v_min_u32 v170, -2, v170\n\t" \
   "buffer_store_dwordx2 v[170:171], v132, %[rs], s52 offen sc1\n\t" "s_nop 3\n\t" "v_cvt_pk_bf16_f32 v172, v160, v161\n\t" \
   "v_cvt_pk_bf16_f32 v173, v162, v163\n\t" "v_min_u32 v172, -2, v172\n\t" "buffer_store_dwordx2 v[172:173], v132, %[rs], s53 offen sc1\n\t" RSTAMP(2) \
   "s_branch 40b\n\t" "70:\n\t" "s_mov_b32 s46, 0\n\t" "s_lshr_b64 s[72:73], %[sel], s46\n\t" "s_lshl_b32 s45, s46, 19\n\t" "s_and_b32 s72, s72, 1\n\t" \
   "s_add_u32 s45, s45, %[pool]\n\t" "s_lshl_b32 s72, s72, 13\n\t" "s_add_u32 s45, s45, s72\n\t" "v_mov_b32 v76, -1\n\t" "v_mov_b32 v77, -1\n\t" \
   "v_mov_b32 v78, -1\n\t" "v_mov_b32 v79, -1\n\t" "buffer_store_dwordx4 v[76:79], v133, %[rs], s45 offen sc1\n\t" \
   "buffer_store_dwordx4 v[76:79], v133, %[rs], s45 offen offset:1024 sc1\n\t" "s_branch 71b\n\t" "63:\n\t" "s_cmp_lg_u32 s64, 0\n\t" \
   "s_cbranch_scc1 64f\n\t" "s_sub_u32 s69, s56, 0x10000\n\t" "s_and_b32 s69, s69, -2\n\t" "s_branch 50b\n\t" "64:\n\t" "s_mov_b32 s69, s48\n\t" \
   "s_cmp_lt_u32 s64, 3\n\t" "s_cselect_b32 s69, -1, s69\n\t" "s_cmp_lg_u32 s69, -1\n\t" "s_cbranch_scc1 50b\n\t" "s_add_u32 s76, s76, 0x1000000\n\t" \
   "s_mov_b32 s73, 0\n\t" "65:\n\t" "s_add_u32 s73, s73, 1\n\t" "s_sleep 6\n\t" "s_and_b32 s72, s73, 3\n\t" "s_add_u32 s72, s72, 8\n\t" \
   "s_sub_u32 s72, s72, %[xs]\n\t" "s_lshl_b32 s72, s72, 7\n\t" "s_add_u32 s72, s72, s66\n\t" "buffer_load_dword v108, v139, %[rs], s72 offen sc1\n\t" \
   "s_waitcnt vmcnt(0)\n\t" "v_readfirstlane_b32 s69, v108\n\t" "s_cmp_gt_u32 s73, 0x10000\n\t" "s_cbranch_scc1 50b\n\t" "s_cmp_eq_u32 s69, -1\n\t" \
   "s_cbranch_scc1 65b\n\t" "s_branch 50b\n\t" "8:\n\t" "s_nop 7\n\t" "s_waitcnt lgkmcnt(0)\n\t" "s_add_u32 s70, s70, 1\n\t" "s_cmp_gt_u32 s70, 2\n\t" \
   "s_cbranch_scc1 81f\n\t" "s_lshl_b32 s71, s70, 3\n\t" "s_lshl_b32 s71, 1, s71\n\t" "s_add_u32 s76, s76, s71\n\t" "81:\n\t" "v_mov_b32 v48, 0\n\t" \
   "v_mov_b32 v49, 0\n\t" "v_mov_b32 v50, 0\n\t" "v_mov_b32 v51, 0\n\t" "v_mov_b32 v52, 0\n\t" "v_mov_b32 v53, 0\n\t" "v_mov_b32 v54, 0\n\t" \
   "v_mov_b32 v55, 0\n\t" "s_add_u32 s71, s70, 1\n\t" "s_min_u32 s71, s71, 7\n\t" "s_lshr_b32 s67, s71, 1\n\t" "s_add_u32 s67, s67, %[xs]\n\t" \
   "s_and_b32 s67, s67, 3\n\t" "s_lshl_b32 s67, s67, 11\n\t" "s_and_b32 s72, s71, 1\n\t" "s_lshl_b32 s72, s72, 14\n\t" "s_add_u32 s67, s67, s72\n\t" \
   "s_add_u32 s67, s67, s66\n\t" "s_cmp_lt_u32 s70, 6\n\t" "s_cbranch_scc1 20b\n\t" "s_sleep 3\n\t" "s_cmp_lt_u32 s70, 0x20000\n\t" \
   "s_cbranch_scc1 20b\n\t" "s_mov_b32 s67, %[esc]\n\t" "s_branch 20b\n\t" "90:\n\t" "s_waitcnt vmcnt(0) lgkmcnt(0)"
#define PV_CLOB "v16", "v17", "v18", "v19", "v20", "v21", "v22", "v23", "v24", "v25", "v26", "v27", "v28", "v29", "v30", "v31", "v48", "v49", "v50", "v51", "v52", "v53", "v54", "v55", "v56", "v57", "v58", "v59", "v60", "v61", "v62", "v63", "v64", "v65", "v66", "v67", "v68", "v69", "v70", "v71", "v72", "v73", "v74", "v75", "v76", "v77", "v78", "v79", "v80", "v81", "v82", "v83", "v84", "v85", "v86", "v87", "v88", "v89", "v90", "v91", "v92", "v93", "v94", "v95", "v96", "v97", "v98", "v99", "v100", "v101", "v102", "v103", "v104", "v105", "v106", "v107", "v108", "v109", "v110", "v111", "v144", "v145", "v146", "v147", "v148", "v149", "v150", "v151", "v152", "v153", "v154", "v155", "v156", "v157", "v158", "v159", "v160", "v161", "v162", "v163", "v164", "v165", "v166", "v167", "v168", "v169", "v170", "v171", "v172", "v173", "v174", "v175", "s60", "s61", "s62", "s63", "s64", "s65", "s66", "s67", "s68", "s69", "s70", "s71", "s72", "s73", "s74", "s75", "s45", "s46", "s47", "s48", "s49", "s50", "s51", "s52", "s53", "s54"
#define WAIT_ASM \
   "11:\n\t" "s_memrealtime s[60:61]\n\t" "s_memtime s[62:63]\n\t" "s_waitcnt lgkmcnt(0)\n\t" "s_sub_u32 s74, s62, s57\n\t" \
   "s_mul_i32 s74, s74, s58\n\t" "s_lshr_b32 s74, s74, 12\n\t" "s_add_u32 s74, s74, s56\n\t" "s_lshl_b32 s60, s60, 3\n\t" "s_sub_u32 s75, s74, s60\n\t" \
   "s_cmp_lt_u32 s75, 32\n\t" "s_cbranch_scc1 12f\n\t" "s_cmp_lt_i32 s75, 0\n\t" "s_cselect_b32 s61, 0, 31\n\t" "s_add_u32 s74, s60, s61\n\t" \
   "s_sub_i32 s75, s61, s75\n\t" "s_add_i32 s75, s75, 1\n\t" "s_ashr_i32 s75, s75, 1\n\t" "s_min_i32 s75, s75, 8\n\t" "s_max_i32 s75, s75, -8\n\t" \
   "s_mov_b32 s56, s74\n\t" "s_mov_b32 s57, s62\n\t" "s_add_u32 s58, s58, s75\n\t" "12:\n\t" "s_sub_i32 s74, %[ta], s74\n\t" \
   "s_cmp_le_i32 s74, 150\n\t" "s_cbranch_scc1 13f\n\t" "s_cmp_lt_i32 s74, 260\n\t" "s_cbranch_scc1 11b\n\t" "s_sleep 2\n\t" \
   "s_cmp_lt_i32 s74, 32000\n\t" "s_cbranch_scc1 11b\n\t" "s_mov_b32 s74, 0\n\t" "13:\n\t" "s_sub_i32 s74, s74, 56\n\t" "s_max_i32 s74, s74, 0\n\t" \
   "s_mul_i32 s74, s74, 23\n\t" "s_lshr_b32 s74, s74, 5\n\t" "s_and_b32 s74, s74, -4\n\t" "s_getpc_b64 s[60:61]\n\t" "14:\n\t" \
   "s_sub_u32 s74, 15f-14b, s74\n\t" "s_add_u32 s60, s60, s74\n\t" "s_addc_u32 s61, s61, 0\n\t" "s_setpc_b64 s[60:61]\n\t" "s_nop 3\n\t" "s_nop 3\n\t" \
   "s_nop 3\n\t" "s_nop 3\n\t" "s_nop 3\n\t" "s_nop 3\n\t" "s_nop 3\n\t" "s_nop 3\n\t" "s_nop 3\n\t" "s_nop 3\n\t" "s_nop 3\n\t" "s_nop 3\n\t" \
   "s_nop 3\n\t" "s_nop 3\n\t" "s_nop 3\n\t" "s_nop 3\n\t" "s_nop 3\n\t" "s_nop 3\n\t" "s_nop 3\n\t" "s_nop 3\n\t" "s_nop 3\n\t" "s_nop 3\n\t" \
   "s_nop 3\n\t" "s_nop 3\n\t" "15:\n\t" ""
#define WAIT_CLOB "s60", "s61", "s62", "s63", "s74", "s75"
#define FIN_ASM \
   ".p2align 6\n\t" "s_nop 0\n\t" "s_nop 0\n\t" "s_nop 0\n\t" "s_nop 0\n\t" "s_nop 0\n\t" "s_nop 0\n\t" "s_nop 0\n\t" "s_nop 0\n\t" "s_nop 0\n\t" \
   "s_nop 0\n\t" "s_nop 0\n\t" "s_mov_b32 s40, 0\n\t" "s_mov_b32 s76, 0\n\t" "s_mov_b32 s77, 0\n\t" "s_sub_u32 s45, s56, 0x10000\n\t" \
   "v_mov_b32 v13, -2\n\t" "v_mov_b32 v11, -1\n\t" "30:\n\t" "s_lshr_b64 s[72:73], %[sel], s40\n\t" "s_lshl_b32 s41, s40, 19\n\t" \
   "s_and_b32 s72, s72, 1\n\t" "s_add_u32 s41, s41, %[pool]\n\t" "s_lshl_b32 s72, s72, 13\n\t" "s_add_u32 s41, s41, s72\n\t" \
   "s_lshr_b64 s[72:73], %[s3], s40\n\t" "s_and_b32 s47, s72, 1\n\t" "s_lshl_b32 s47, s47, 14\n\t" "s_mul_i32 s49, s40, 1310720\n\t" \
   "s_add_u32 s49, s49, %[su]\n\t" "s_lshr_b64 s[72:73], %[ysel], s40\n\t" "s_mul_i32 s73, s40, 43\n\t" "s_mul_i32 s50, s40, 80\n\t" \
   "s_and_b32 s72, s72, 1\n\t" "s_lshr_b32 s73, s73, 7\n\t" "s_add_u32 s50, s50, s72\n\t" "s_lshl4_add_u32 s50, s73, s50\n\t" \
   "s_lshl_b32 s50, s50, 13\n\t" "s_add_u32 s50, s50, %[yb]\n\t" "s_add_u32 s51, s50, 4096\n\t" "s_lshl_b32 s52, s40, 12\n\t" \
   "s_add_u32 s52, s52, %[uf]\n\t" "s_add_u32 s69, s45, %[tu]\n\t" "v_max3_u32 v15, v8, v9, v10\n\t" "v_max_u32 v15, v15, v11\n\t" \
   "v_cmp_eq_u32 vcc, -1, v15\n\t" "s_add_u32 s40, s40, 1\n\t" "s_cbranch_vccnz 95f\n\t" "96:\n\t" "11:\n\t" "s_memrealtime s[60:61]\n\t" \
   "s_memtime s[62:63]\n\t" "s_waitcnt lgkmcnt(0)\n\t" "s_sub_u32 s74, s62, s57\n\t" "s_mul_i32 s74, s74, s58\n\t" "s_lshr_b32 s74, s74, 12\n\t" \
   "s_add_u32 s74, s74, s56\n\t" "s_lshl_b32 s60, s60, 3\n\t" "s_sub_u32 s75, s74, s60\n\t" "s_cmp_lt_u32 s75, 32\n\t" "s_cbranch_scc1 12f\n\t" \
   "s_cmp_lt_i32 s75, 0\n\t" "s_cselect_b32 s61, 0, 31\n\t" "s_add_u32 s74, s60, s61\n\t" "s_sub_i32 s75, s61, s75\n\t" "s_add_i32 s75, s75, 1\n\t" \
   "s_ashr_i32 s75, s75, 1\n\t" "s_min_i32 s75, s75, 8\n\t" "s_max_i32 s75, s75, -8\n\t" "s_mov_b32 s56, s74\n\t" "s_mov_b32 s57, s62\n\t" \
   "s_add_u32 s58, s58, s75\n\t" "12:\n\t" "s_sub_i32 s74, s69, s74\n\t" "s_cmp_le_i32 s74, 150\n\t" "s_cbranch_scc1 13f\n\t" \
   "s_cmp_lt_i32 s74, 260\n\t" "s_cbranch_scc1 11b\n\t" "s_sleep 2\n\t" "s_cmp_lt_i32 s74, 32000\n\t" "s_cbranch_scc1 11b\n\t" "s_mov_b32 s74, 0\n\t" \
   "13:\n\t" "s_sub_i32 s74, s74, 56\n\t" "s_max_i32 s74, s74, 0\n\t" "s_mul_i32 s74, s74, 23\n\t" "s_lshr_b32 s74, s74, 5\n\t" \
   "s_and_b32 s74, s74, -4\n\t" "s_getpc_b64 s[60:61]\n\t" "14:\n\t" "s_sub_u32 s74, 15f-14b, s74\n\t" "s_add_u32 s60, s60, s74\n\t" \
   "s_addc_u32 s61, s61, 0\n\t" "s_setpc_b64 s[60:61]\n\t" "s_nop 3\n\t" "s_nop 3\n\t" "s_nop 3\n\t" "s_nop 3\n\t" "s_nop 3\n\t" "s_nop 3\n\t" \
   "s_nop 3\n\t" "s_nop 3\n\t" "s_nop 3\n\t" "s_nop 3\n\t" "s_nop 3\n\t" "s_nop 3\n\t" "s_nop 3\n\t" "s_nop 3\n\t" "s_nop 3\n\t" "s_nop 3\n\t" \
   "s_nop 3\n\t" "s_nop 3\n\t" "s_nop 3\n\t" "s_nop 3\n\t" "s_nop 3\n\t" "s_nop 3\n\t" "s_nop 3\n\t" "s_nop 3\n\t" "15:\n\t" RSTAMP(4) \
   "buffer_load_dwordx4 a[0:3], v16, %[rs], s49 offen offset:0 sc1\n\t" "s_nop 0\n\t" \
   "buffer_load_dwordx4 a[4:7], v16, %[rs], s49 offen offset:1024 sc1\n\t" "s_nop 0\n\t" \
   "buffer_load_dwordx4 a[8:11], v16, %[rs], s49 offen offset:2048 sc1\n\t" "s_nop 0\n\t" \
   "buffer_load_dwordx4 a[12:15], v16, %[rs], s49 offen offset:3072 sc1\n\t" "s_nop 0\n\t" "s_add_u32 s49, s49, 4096\n\t" \
   "buffer_load_dwordx4 a[16:19], v16, %[rs], s49 offen offset:0 sc1\n\t" "s_nop 0\n\t" \
   "buffer_load_dwordx4 a[20:23], v16, %[rs], s49 offen offset:1024 sc1\n\t" "s_nop 0\n\t" \
   "buffer_load_dwordx4 a[24:27], v16, %[rs], s49 offen offset:2048 sc1\n\t" "s_nop 0\n\t" \
   "buffer_load_dwordx4 a[28:31], v16, %[rs], s49 offen offset:3072 sc1\n\t" "s_nop 0\n\t" "s_add_u32 s49, s49, 4096\n\t" \
   "buffer_load_dwordx4 a[32:35], v16, %[rs], s49 offen offset:0 sc1\n\t" "s_nop 0\n\t" \
   "buffer_load_dwordx4 a[36:39], v16, %[rs], s49 offen offset:1024 sc1\n\t" "s_nop 0\n\t" \
   "buffer_load_dwordx4 a[40:43], v16, %[rs], s49 offen offset:2048 sc1\n\t" "s_nop 0\n\t" \
   "buffer_load_dwordx4 a[44:47], v16, %[rs], s49 offen offset:3072 sc1\n\t" "s_nop 0\n\t" "s_add_u32 s49, s49, 4096\n\t" \
   "buffer_load_dwordx4 a[48:51], v16, %[rs], s49 offen offset:0 sc1\n\t" "s_nop 0\n\t" \
   "buffer_load_dwordx4 a[52:55], v16, %[rs], s49 offen offset:1024 sc1\n\t" "s_nop 0\n\t" \
   "buffer_load_dwordx4 a[56:59], v16, %[rs], s49 offen offset:2048 sc1\n\t" "s_nop 0\n\t" \
   "buffer_load_dwordx4 a[60:63], v16, %[rs], s49 offen offset:3072 sc1\n\t" "s_nop 0\n\t" "s_add_u32 s49, s49, 4096\n\t" \
   "buffer_load_dwordx4 a[64:67], v16, %[rs], s49 offen offset:0 sc1\n\t" "s_nop 0\n\t" \
   "buffer_load_dwordx4 a[68:71], v16, %[rs], s49 offen offset:1024 sc1\n\t" "s_nop 0\n\t" \
   "buffer_load_dwordx4 a[72:75], v16, %[rs], s49 offen offset:2048 sc1\n\t" "s_nop 0\n\t" \
   "buffer_load_dwordx4 a[76:79], v16, %[rs], s49 offen offset:3072 sc1\n\t" "s_nop 0\n\t" "s_add_u32 s49, s49, 4096\n\t" \
   "buffer_load_dwordx4 a[80:83], v16, %[rs], s49 offen offset:0 sc1\n\t" "s_nop 0\n\t" \
   "buffer_load_dwordx4 a[84:87], v16, %[rs], s49 offen offset:1024 sc1\n\t" "s_nop 0\n\t" \
   "buffer_load_dwordx4 a[88:91], v16, %[rs], s49 offen offset:2048 sc1\n\t" "s_nop 0\n\t" \
   "buffer_load_dwordx4 a[92:95], v16, %[rs], s49 offen offset:3072 sc1\n\t" "s_nop 0\n\t" "s_add_u32 s49, s49, 4096\n\t" \
   "buffer_load_dwordx4 a[96:99], v16, %[rs], s49 offen offset:0 sc1\n\t" "s_nop 0\n\t" \
   "buffer_load_dwordx4 a[100:103], v16, %[rs], s49 offen offset:1024 sc1\n\t" "s_nop 0\n\t" \
   "buffer_load_dwordx4 a[104:107], v16, %[rs], s49 offen offset:2048 sc1\n\t" "s_nop 0\n\t" \
   "buffer_load_dwordx4 a[108:111], v16, %[rs], s49 offen offset:3072 sc1\n\t" "s_nop 0\n\t" "s_add_u32 s49, s49, 4096\n\t" \
   "buffer_load_dwordx4 a[112:115], v16, %[rs], s49 offen offset:0 sc1\n\t" "s_nop 0\n\t" \
   "buffer_load_dwordx4 a[116:119], v16, %[rs], s49 offen offset:1024 sc1\n\t" "s_nop 0\n\t" \
   "buffer_load_dwordx4 a[120:123], v16, %[rs], s49 offen offset:2048 sc1\n\t" "s_nop 0\n\t" \
   "buffer_load_dwordx4 a[124:127], v16, %[rs], s49 offen offset:3072 sc1\n\t" "s_nop 0\n\t" "s_add_u32 s52, s52, 4096\n\t" \
   "buffer_load_dwordx4 v[8:11], v19, %[rs], s52 offen sc1\n\t" RSTAMP(0) "s_mov_b32 s46, 0\n\t" "s_xor_b32 s64, s47, %[lad0]\n\t" \
   "s_and_b32 s65, s64, 0x4000\n\t" "s_xor_b32 s64, s64, s65\n\t" "v_or_b32 v224, s65, v16\n\t" "v_or_b32 v225, s65, v17\n\t" \
   "v_xor_b32 v226, 0x4000, v224\n\t" "v_xor_b32 v227, 0x4000, v225\n\t" "s_add_u32 s64, s64, s41\n\t" "v_xor_b32 v14, s47, v20\n\t" \
   "v_xor_b32 v15, 0x4000, v14\n\t" "s_add_u32 s65, s64, 32768\n\t" "s_add_u32 s66, s64, 65536\n\t" "s_add_u32 s67, s64, 98304\n\t" \
   "s_add_u32 s68, s64, 131072\n\t" "s_add_u32 s69, s64, 163840\n\t" "s_add_u32 s70, s64, 196608\n\t" "s_add_u32 s71, s64, 229376\n\t" \
   "s_add_u32 s72, s64, 262144\n\t" "s_add_u32 s73, s64, 294912\n\t" "s_add_u32 s53, s41, 4096\n\t" "v_mov_b32 v28, 0\n\t" "v_mov_b32 v29, 0\n\t" \
   "v_mov_b32 v30, 0\n\t" "v_mov_b32 v31, 0\n\t" "19:\n\t" "ds_read_b128 v[24:27], v23\n\t" "s_waitcnt lgkmcnt(0)\n\t" \
   "v_readfirstlane_b32 s74, v26\n\t" "v_readfirstlane_b32 s44, v24\n\t" "v_readfirstlane_b32 s45, v25\n\t" "s_sub_u32 s48, s44, 110\n\t" \
   "s_cmp_lt_u32 s74, s40\n\t" "s_cbranch_scc1 18f\n\t" RSTAMP(7) "21:\n\t" "s_memrealtime s[60:61]\n\t" "s_memtime s[62:63]\n\t" \
   "s_waitcnt lgkmcnt(0)\n\t" "s_sub_u32 s74, s62, s57\n\t" "s_mul_i32 s74, s74, s58\n\t" "s_lshr_b32 s74, s74, 12\n\t" "s_add_u32 s74, s74, s56\n\t" \
   "s_lshl_b32 s60, s60, 3\n\t" "s_sub_u32 s75, s74, s60\n\t" "s_cmp_lt_u32 s75, 32\n\t" "s_cbranch_scc1 22f\n\t" "s_cmp_lt_i32 s75, 0\n\t" \
   "s_cselect_b32 s61, 0, 31\n\t" "s_add_u32 s74, s60, s61\n\t" "s_sub_i32 s75, s61, s75\n\t" "s_add_i32 s75, s75, 1\n\t" "s_ashr_i32 s75, s75, 1\n\t" \
   "s_min_i32 s75, s75, 8\n\t" "s_max_i32 s75, s75, -8\n\t" "s_mov_b32 s56, s74\n\t" "s_mov_b32 s57, s62\n\t" "s_add_u32 s58, s58, s75\n\t" "22:\n\t" \
   "s_sub_i32 s74, s48, s74\n\t" "s_cmp_le_i32 s74, 150\n\t" "s_cbranch_scc1 23f\n\t" "s_cmp_lt_i32 s74, 260\n\t" "s_cbranch_scc1 21b\n\t" \
   "s_sleep 2\n\t" "s_cmp_lt_i32 s74, 32000\n\t" "s_cbranch_scc1 21b\n\t" "s_mov_b32 s74, 0\n\t" "23:\n\t" "s_sub_i32 s74, s74, 56\n\t" \
   "s_max_i32 s74, s74, 0\n\t" "s_mul_i32 s74, s74, 23\n\t" "s_lshr_b32 s74, s74, 5\n\t" "s_and_b32 s74, s74, -4\n\t" "s_getpc_b64 s[60:61]\n\t" \
   "24:\n\t" "s_sub_u32 s74, 25f-24b, s74\n\t" "s_add_u32 s60, s60, s74\n\t" "s_addc_u32 s61, s61, 0\n\t" "s_setpc_b64 s[60:61]\n\t" "s_nop 3\n\t" \
   "s_nop 3\n\t" "s_nop 3\n\t" "s_nop 3\n\t" "s_nop 3\n\t" "s_nop 3\n\t" "s_nop 3\n\t" "s_nop 3\n\t" "s_nop 3\n\t" "s_nop 3\n\t" "s_nop 3\n\t" \
   "s_nop 3\n\t" "s_nop 3\n\t" "s_nop 3\n\t" "s_nop 3\n\t" "s_nop 3\n\t" "s_nop 3\n\t" "s_nop 3\n\t" "s_nop 3\n\t" "s_nop 3\n\t" "s_nop 3\n\t" \
   "s_nop 3\n\t" "s_nop 3\n\t" "s_nop 3\n\t" "25:\n\t" "buffer_load_dwordx4 v[32:35], v16, %[rs], s50 offen offset:0 sc1\n\t" \
   "buffer_load_dwordx4 v[36:39], v16, %[rs], s50 offen offset:1024 sc1\n\t" "buffer_load_dwordx4 v[40:43], v16, %[rs], s50 offen offset:2048 sc1\n\t" \
   "buffer_load_dwordx4 v[44:47], v16, %[rs], s50 offen offset:3072 sc1\n\t" "buffer_load_dwordx4 v[48:51], v16, %[rs], s51 offen offset:0 sc1\n\t" \
   "buffer_load_dwordx4 v[52:55], v16, %[rs], s51 offen offset:1024 sc1\n\t" "buffer_load_dwordx4 v[56:59], v16, %[rs], s51 offen offset:2048 sc1\n\t" \
   "buffer_load_dwordx4 v[60:63], v16, %[rs], s51 offen offset:3072 sc1\n\t" "s_nop 3\n\t" "s_nop 3\n\t" "s_nop 3\n\t" "s_nop 3\n\t" "s_nop 3\n\t" \
   "s_nop 3\n\t" "s_nop 3\n\t" "s_nop 3\n\t" "s_nop 3\n\t" "s_nop 3\n\t" "s_nop 3\n\t" "s_nop 3\n\t" "s_nop 3\n\t" "s_nop 3\n\t" "s_nop 3\n\t" \
   "s_nop 3\n\t" RSTAMP(6) "buffer_load_dwordx4 v[64:67], v224, %[rs], s64 offen offset:0 sc1\n\t" \
   "buffer_load_dwordx4 v[68:71], v224, %[rs], s64 offen offset:1024 sc1\n\t" "buffer_load_dwordx4 v[72:75], v224, %[rs], s65 offen offset:0 sc1\n\t" \
   "buffer_load_dwordx4 v[76:79], v224, %[rs], s65 offen offset:1024 sc1\n\t" "buffer_load_dwordx4 v[80:83], v224, %[rs], s66 offen offset:0 sc1\n\t" \
   "buffer_load_dwordx4 v[84:87], v224, %[rs], s66 offen offset:1024 sc1\n\t" "buffer_load_dwordx4 v[88:91], v224, %[rs], s67 offen offset:0 sc1\n\t" \
   "buffer_load_dwordx4 v[92:95], v224, %[rs], s67 offen offset:1024 sc1\n\t" "buffer_load_dwordx4 v[96:99], v224, %[rs], s68 offen offset:0 sc1\n\t" \
   "buffer_load_dwordx4 v[100:103], v224, %[rs], s68 offen offset:1024 sc1\n\t" \
   "buffer_load_dwordx4 v[104:107], v224, %[rs], s69 offen offset:0 sc1\n\t" \
   "buffer_load_dwordx4 v[108:111], v224, %[rs], s69 offen offset:1024 sc1\n\t" \
   "buffer_load_dwordx4 v[112:115], v224, %[rs], s70 offen offset:0 sc1\n\t" \
   "buffer_load_dwordx4 v[116:119], v224, %[rs], s70 offen offset:1024 sc1\n\t" \
   "buffer_load_dwordx4 v[120:123], v224, %[rs], s71 offen offset:0 sc1\n\t" \
   "buffer_load_dwordx4 v[124:127], v224, %[rs], s71 offen offset:1024 sc1\n\t" \
   "buffer_load_dwordx2 v[128:129], v225, %[rs], s72 offen offset:0 sc1\n\t" \
   "buffer_load_dwordx2 v[130:131], v225, %[rs], s72 offen offset:128 sc1\n\t" \
   "buffer_load_dwordx2 v[132:133], v225, %[rs], s72 offen offset:1024 sc1\n\t" \
   "buffer_load_dwordx2 v[134:135], v225, %[rs], s72 offen offset:1152 sc1\n\t" \
   "buffer_load_dwordx2 v[136:137], v225, %[rs], s73 offen offset:0 sc1\n\t" \
   "buffer_load_dwordx2 v[138:139], v225, %[rs], s73 offen offset:128 sc1\n\t" \
   "buffer_load_dwordx2 v[140:141], v225, %[rs], s73 offen offset:1024 sc1\n\t" \
   "buffer_load_dwordx2 v[142:143], v225, %[rs], s73 offen offset:1152 sc1\n\t" YSTAMP(24) "s_waitcnt vmcnt(23)\n\t" RSTAMP(1) \
   "v_smfmac_f32_16x16x64_bf16 v[28:31], v[64:67], a[0:7], v22\n\t" "s_waitcnt vmcnt(22)\n\t" \
   "v_smfmac_f32_16x16x64_bf16 v[28:31], v[68:71], a[8:15], v22\n\t" "s_waitcnt vmcnt(21)\n\t" \
   "v_smfmac_f32_16x16x64_bf16 v[28:31], v[72:75], a[16:23], v22\n\t" "s_waitcnt vmcnt(20)\n\t" \
   "v_smfmac_f32_16x16x64_bf16 v[28:31], v[76:79], a[24:31], v22\n\t" "s_waitcnt vmcnt(19)\n\t" \
   "v_smfmac_f32_16x16x64_bf16 v[28:31], v[80:83], a[32:39], v22\n\t" "s_waitcnt vmcnt(18)\n\t" \
   "v_smfmac_f32_16x16x64_bf16 v[28:31], v[84:87], a[40:47], v22\n\t" "s_waitcnt vmcnt(17)\n\t" \
   "v_smfmac_f32_16x16x64_bf16 v[28:31], v[88:91], a[48:55], v22\n\t" "s_waitcnt vmcnt(16)\n\t" \
   "v_smfmac_f32_16x16x64_bf16 v[28:31], v[92:95], a[56:63], v22\n\t" "s_waitcnt vmcnt(15)\n\t" \
   "v_smfmac_f32_16x16x64_bf16 v[28:31], v[96:99], a[64:71], v22\n\t" "s_waitcnt vmcnt(14)\n\t" \
   "v_smfmac_f32_16x16x64_bf16 v[28:31], v[100:103], a[72:79], v22\n\t" "s_waitcnt vmcnt(13)\n\t" \
   "v_smfmac_f32_16x16x64_bf16 v[28:31], v[104:107], a[80:87], v22\n\t" "s_waitcnt vmcnt(12)\n\t" \
   "v_smfmac_f32_16x16x64_bf16 v[28:31], v[108:111], a[88:95], v22\n\t" "s_waitcnt vmcnt(11)\n\t" \
   "v_smfmac_f32_16x16x64_bf16 v[28:31], v[112:115], a[96:103], v22\n\t" "s_waitcnt vmcnt(10)\n\t" \
   "v_smfmac_f32_16x16x64_bf16 v[28:31], v[116:119], a[104:111], v22\n\t" "s_waitcnt vmcnt(9)\n\t" \
   "v_smfmac_f32_16x16x64_bf16 v[28:31], v[120:123], a[112:119], v22\n\t" "s_waitcnt vmcnt(8)\n\t" \
   "v_smfmac_f32_16x16x64_bf16 v[28:31], v[124:127], a[120:127], v22\n\t" "s_waitcnt vmcnt(6)\n\t" \
   "v_smfmac_f32_16x16x64_bf16 v[28:31], v[128:131], v[32:39], v22\n\t" "s_waitcnt vmcnt(4)\n\t" \
   "v_smfmac_f32_16x16x64_bf16 v[28:31], v[132:135], v[40:47], v22\n\t" "s_waitcnt vmcnt(2)\n\t" \
   "v_smfmac_f32_16x16x64_bf16 v[28:31], v[136:139], v[48:55], v22\n\t" "s_waitcnt vmcnt(0)\n\t" \
   "v_smfmac_f32_16x16x64_bf16 v[28:31], v[140:143], v[56:63], v22\n\t" "s_nop 7\n\t" "v_add_f32 v28, v28, v29\n\t" "v_add_f32 v30, v30, v31\n\t" \
   "v_cvt_pk_bf16_f32 v24, v28, v30\n\t" "v_cmp_u_f32 vcc, v28, v30\n\t" "s_nop 0\n\t" \
   "v_mov_b32_dpp v25, v24 quad_perm:[1,0,3,2] row_mask:0xf bank_mask:0xf\n\t" "s_cbranch_vccnz 8f\n\t" "2:\n\t" RSTAMP(2) \
   "v_perm_b32 v26, v25, v24, v18\n\t" "s_nop 1\n\t" "v_min_u32_dpp v24, v26, v13 quad_perm:[0,1,0,1] row_mask:0xf bank_mask:0xf\n\t" \
   "v_mov_b32_dpp v25, v26 quad_perm:[2,3,2,3] row_mask:0xf bank_mask:0xf\n\t" "buffer_store_dwordx2 v[24:25], v14, %[rs], s41 offen sc1\n\t" \
   "buffer_store_dwordx2 v[24:25], v14, %[rs], s53 offen sc1\n\t" "s_memtime s[62:63]\n\t" "s_memrealtime s[60:61]\n\t" \
   "buffer_store_dwordx2 v[24:25], v15, %[rs], s41 offen sc1\n\t" "buffer_store_dwordx2 v[24:25], v15, %[rs], s53 offen sc1\n\t" RSTAMP(3) \
   "s_waitcnt lgkmcnt(0)\n\t" "s_sub_u32 s74, s62, s57\n\t" "s_mul_i32 s74, s74, s58\n\t" "s_lshr_b32 s74, s74, 12\n\t" "s_add_u32 s74, s74, s56\n\t" \
   "s_lshl_b32 s60, s60, 3\n\t" "s_sub_u32 s75, s74, s60\n\t" "s_cmp_lt_u32 s75, 32\n\t" "s_cbranch_scc1 38f\n\t" "s_cmp_lt_i32 s75, 0\n\t" \
   "s_cselect_b32 s61, 0, 31\n\t" "s_add_u32 s74, s60, s61\n\t" "s_sub_i32 s75, s61, s75\n\t" "s_add_i32 s75, s75, 1\n\t" "s_ashr_i32 s75, s75, 1\n\t" \
   "s_min_i32 s75, s75, 8\n\t" "s_max_i32 s75, s75, -8\n\t" "s_mov_b32 s56, s74\n\t" "s_mov_b32 s57, s62\n\t" "s_add_u32 s58, s58, s75\n\t" "38:\n\t" \
   "s_min_u32 s75, s46, 1\n\t" "s_and_b32 s74, s74, -4\n\t" "s_or_b32 s74, s74, s75\n\t" "v_mov_b32 v26, s74\n\t" \
   "buffer_store_dword v26, v21, %[rs], s41 offen sc1\n\t" FINTRACE "v_xor_b32 v23, 32, v23\n\t" "s_cmp_lt_u32 s40, 63\n\t" "s_cbranch_scc1 30b\n\t" \
   "s_branch 90f\n\t" "18:\n\t" "s_sleep 2\n\t" "s_branch 19b\n\t" "8:\n\t" "s_mov_b32 s74, 0\n\t" "87:\n\t" "v_max3_u32 v12, v32, v34, v36\n\t" \
   "v_max3_u32 v12, v12, v38, v40\n\t" "v_max3_u32 v12, v12, v42, v44\n\t" "v_max3_u32 v12, v12, v46, v48\n\t" "v_max3_u32 v12, v12, v50, v52\n\t" \
   "v_max3_u32 v12, v12, v54, v56\n\t" "v_max3_u32 v12, v12, v58, v60\n\t" "v_max_u32 v12, v12, v62\n\t" "v_cmp_eq_u32 vcc, -1, v12\n\t" \
   "s_cmp_gt_u32 s74, 0x10000\n\t" "s_cbranch_scc1 86f\n\t" "s_cbranch_vccz 86f\n\t" "s_cmp_eq_u32 s74, 0\n\t" "s_cselect_b32 s75, 0x100, 0\n\t" \
   "s_cbranch_scc1 88f\n\t" "s_sleep 3\n\t" "88:\n\t" "buffer_load_dwordx4 v[32:35], v16, %[rs], s50 offen offset:0 sc1\n\t" \
   "buffer_load_dwordx4 v[36:39], v16, %[rs], s50 offen offset:1024 sc1\n\t" "buffer_load_dwordx4 v[40:43], v16, %[rs], s50 offen offset:2048 sc1\n\t" \
   "buffer_load_dwordx4 v[44:47], v16, %[rs], s50 offen offset:3072 sc1\n\t" "buffer_load_dwordx4 v[48:51], v16, %[rs], s51 offen offset:0 sc1\n\t" \
   "buffer_load_dwordx4 v[52:55], v16, %[rs], s51 offen offset:1024 sc1\n\t" "buffer_load_dwordx4 v[56:59], v16, %[rs], s51 offen offset:2048 sc1\n\t" \
   "buffer_load_dwordx4 v[60:63], v16, %[rs], s51 offen offset:3072 sc1\n\t" "s_add_u32 s76, s76, s75\n\t" "s_add_u32 s74, s74, 1\n\t" \
   "s_waitcnt vmcnt(0)\n\t" "s_branch 87b\n\t" "86:\n\t" "v_mov_b32 v27, 0\n\t" "v_mov_b32 v28, 0\n\t" "v_mov_b32 v29, 0\n\t" "v_mov_b32 v30, 0\n\t" \
   "v_mov_b32 v31, 0\n\t" "v_max3_u32 v27, v27, v64, v66\n\t" "v_max3_u32 v27, v27, v68, v70\n\t" "v_max3_u32 v27, v27, v72, v74\n\t" \
   "v_max3_u32 v27, v27, v76, v78\n\t" "v_max3_u32 v27, v27, v80, v82\n\t" "v_max3_u32 v27, v27, v84, v86\n\t" "v_max3_u32 v27, v27, v88, v90\n\t" \
   "v_max3_u32 v27, v27, v92, v94\n\t" "v_max3_u32 v27, v27, v96, v98\n\t" "v_max3_u32 v27, v27, v100, v102\n\t" "v_max3_u32 v27, v27, v104, v106\n\t" \
   "v_max3_u32 v27, v27, v108, v110\n\t" "v_max3_u32 v27, v27, v112, v114\n\t" "v_max3_u32 v27, v27, v116, v118\n\t" \
   "v_max3_u32 v27, v27, v120, v122\n\t" "v_max3_u32 v27, v27, v124, v126\n\t" "v_max3_u32 v27, v27, v128, v130\n\t" \
   "v_max3_u32 v27, v27, v132, v134\n\t" "v_max3_u32 v27, v27, v136, v138\n\t" "v_max3_u32 v27, v27, v140, v142\n\t" "v_cmp_eq_u32 vcc, -1, v27\n\t" \
   "s_nop 0\n\t" "s_cbranch_vccnz 85f\n\t" "v_smfmac_f32_16x16x64_bf16 v[28:31], v[64:67], a[0:7], v22\n\t" \
   "v_smfmac_f32_16x16x64_bf16 v[28:31], v[68:71], a[8:15], v22\n\t" "v_smfmac_f32_16x16x64_bf16 v[28:31], v[72:75], a[16:23], v22\n\t" \
   "v_smfmac_f32_16x16x64_bf16 v[28:31], v[76:79], a[24:31], v22\n\t" "v_smfmac_f32_16x16x64_bf16 v[28:31], v[80:83], a[32:39], v22\n\t" \
   "v_smfmac_f32_16x16x64_bf16 v[28:31], v[84:87], a[40:47], v22\n\t" "v_smfmac_f32_16x16x64_bf16 v[28:31], v[88:91], a[48:55], v22\n\t" \
   "v_smfmac_f32_16x16x64_bf16 v[28:31], v[92:95], a[56:63], v22\n\t" "v_smfmac_f32_16x16x64_bf16 v[28:31], v[96:99], a[64:71], v22\n\t" \
   "v_smfmac_f32_16x16x64_bf16 v[28:31], v[100:103], a[72:79], v22\n\t" "v_smfmac_f32_16x16x64_bf16 v[28:31], v[104:107], a[80:87], v22\n\t" \
   "v_smfmac_f32_16x16x64_bf16 v[28:31], v[108:111], a[88:95], v22\n\t" "v_smfmac_f32_16x16x64_bf16 v[28:31], v[112:115], a[96:103], v22\n\t" \
   "v_smfmac_f32_16x16x64_bf16 v[28:31], v[116:119], a[104:111], v22\n\t" "v_smfmac_f32_16x16x64_bf16 v[28:31], v[120:123], a[112:119], v22\n\t" \
   "v_smfmac_f32_16x16x64_bf16 v[28:31], v[124:127], a[120:127], v22\n\t" "v_smfmac_f32_16x16x64_bf16 v[28:31], v[128:131], v[32:39], v22\n\t" \
   "v_smfmac_f32_16x16x64_bf16 v[28:31], v[132:135], v[40:47], v22\n\t" "v_smfmac_f32_16x16x64_bf16 v[28:31], v[136:139], v[48:55], v22\n\t" \
   "v_smfmac_f32_16x16x64_bf16 v[28:31], v[140:143], v[56:63], v22\n\t" "84:\n\t" "v_cmp_eq_u32 vcc, -1, v27\n\t" "s_cmp_gt_u32 s46, 0x10000\n\t" \
   "s_cbranch_scc1 83f\n\t" "s_cbranch_vccz 83f\n\t" "85:\n\t" "s_add_u32 s46, s46, 1\n\t" "s_cmp_gt_u32 s46, 2\n\t" "s_cbranch_scc1 81f\n\t" \
   "s_lshl_b32 s74, s46, 3\n\t" "s_lshl_b32 s74, 1, s74\n\t" "s_add_u32 s77, s77, s74\n\t" "81:\n\t" "s_min_u32 s74, s46, 7\n\t" \
   "s_lshr_b32 s64, s74, 1\n\t" "s_add_u32 s64, s64, %[xs]\n\t" "s_and_b32 s64, s64, 3\n\t" "s_lshl_b32 s64, s64, 11\n\t" "s_and_b32 s75, s74, 1\n\t" \
   "s_lshl_b32 s75, s75, 14\n\t" "s_xor_b32 s75, s75, s47\n\t" "s_xor_b32 s75, s75, %[lad0]\n\t" "s_and_b32 s75, s75, 0x4000\n\t" \
   "s_add_u32 s64, s64, s75\n\t" "s_add_u32 s64, s64, s41\n\t" "s_add_u32 s65, s64, 32768\n\t" "s_add_u32 s66, s64, 65536\n\t" \
   "s_add_u32 s67, s64, 98304\n\t" "s_add_u32 s68, s64, 131072\n\t" "s_add_u32 s69, s64, 163840\n\t" "s_add_u32 s70, s64, 196608\n\t" \
   "s_add_u32 s71, s64, 229376\n\t" "s_add_u32 s72, s64, 262144\n\t" "s_add_u32 s73, s64, 294912\n\t" "v_mov_b32 v28, 0\n\t" "v_mov_b32 v29, 0\n\t" \
   "v_mov_b32 v30, 0\n\t" "v_mov_b32 v31, 0\n\t" "v_mov_b32 v27, 0\n\t" "s_cmp_lt_u32 s46, 7\n\t" "s_cbranch_scc1 82f\n\t" "s_sleep 3\n\t" "82:\n\t" \
   RSTAMP(6) "buffer_load_dwordx4 v[64:67], v16, %[rs], s64 offen offset:0 sc1\n\t" \
   "buffer_load_dwordx4 v[68:71], v16, %[rs], s64 offen offset:1024 sc1\n\t" "buffer_load_dwordx4 v[72:75], v16, %[rs], s65 offen offset:0 sc1\n\t" \
   "buffer_load_dwordx4 v[76:79], v16, %[rs], s65 offen offset:1024 sc1\n\t" "buffer_load_dwordx4 v[80:83], v16, %[rs], s66 offen offset:0 sc1\n\t" \
   "buffer_load_dwordx4 v[84:87], v16, %[rs], s66 offen offset:1024 sc1\n\t" "buffer_load_dwordx4 v[88:91], v16, %[rs], s67 offen offset:0 sc1\n\t" \
   "buffer_load_dwordx4 v[92:95], v16, %[rs], s67 offen offset:1024 sc1\n\t" "buffer_load_dwordx4 v[96:99], v16, %[rs], s68 offen offset:0 sc1\n\t" \
   "buffer_load_dwordx4 v[100:103], v16, %[rs], s68 offen offset:1024 sc1\n\t" "buffer_load_dwordx4 v[104:107], v16, %[rs], s69 offen offset:0 sc1\n\t" \
   "buffer_load_dwordx4 v[108:111], v16, %[rs], s69 offen offset:1024 sc1\n\t" "buffer_load_dwordx4 v[112:115], v16, %[rs], s70 offen offset:0 sc1\n\t" \
   "buffer_load_dwordx4 v[116:119], v16, %[rs], s70 offen offset:1024 sc1\n\t" "buffer_load_dwordx4 v[120:123], v16, %[rs], s71 offen offset:0 sc1\n\t" \
   "buffer_load_dwordx4 v[124:127], v16, %[rs], s71 offen offset:1024 sc1\n\t" "buffer_load_dwordx2 v[128:129], v17, %[rs], s72 offen offset:0 sc1\n\t" \
   "buffer_load_dwordx2 v[130:131], v17, %[rs], s72 offen offset:128 sc1\n\t" \
   "buffer_load_dwordx2 v[132:133], v17, %[rs], s72 offen offset:1024 sc1\n\t" \
   "buffer_load_dwordx2 v[134:135], v17, %[rs], s72 offen offset:1152 sc1\n\t" "buffer_load_dwordx2 v[136:137], v17, %[rs], s73 offen offset:0 sc1\n\t" \
   "buffer_load_dwordx2 v[138:139], v17, %[rs], s73 offen offset:128 sc1\n\t" \
   "buffer_load_dwordx2 v[140:141], v17, %[rs], s73 offen offset:1024 sc1\n\t" \
   "buffer_load_dwordx2 v[142:143], v17, %[rs], s73 offen offset:1152 sc1\n\t" "s_waitcnt vmcnt(23)\n\t" \
   "v_smfmac_f32_16x16x64_bf16 v[28:31], v[64:67], a[0:7], v22\n\t" "v_max3_u32 v27, v27, v64, v66\n\t" "s_waitcnt vmcnt(22)\n\t" \
   "v_smfmac_f32_16x16x64_bf16 v[28:31], v[68:71], a[8:15], v22\n\t" "v_max3_u32 v27, v27, v68, v70\n\t" "s_waitcnt vmcnt(21)\n\t" \
   "v_smfmac_f32_16x16x64_bf16 v[28:31], v[72:75], a[16:23], v22\n\t" "v_max3_u32 v27, v27, v72, v74\n\t" "s_waitcnt vmcnt(20)\n\t" \
   "v_smfmac_f32_16x16x64_bf16 v[28:31], v[76:79], a[24:31], v22\n\t" "v_max3_u32 v27, v27, v76, v78\n\t" "s_waitcnt vmcnt(19)\n\t" \
   "v_smfmac_f32_16x16x64_bf16 v[28:31], v[80:83], a[32:39], v22\n\t" "v_max3_u32 v27, v27, v80, v82\n\t" "s_waitcnt vmcnt(18)\n\t" \
   "v_smfmac_f32_16x16x64_bf16 v[28:31], v[84:87], a[40:47], v22\n\t" "v_max3_u32 v27, v27, v84, v86\n\t" "s_waitcnt vmcnt(17)\n\t" \
   "v_smfmac_f32_16x16x64_bf16 v[28:31], v[88:91], a[48:55], v22\n\t" "v_max3_u32 v27, v27, v88, v90\n\t" "s_waitcnt vmcnt(16)\n\t" \
   "v_smfmac_f32_16x16x64_bf16 v[28:31], v[92:95], a[56:63], v22\n\t" "v_max3_u32 v27, v27, v92, v94\n\t" "s_waitcnt vmcnt(15)\n\t" \
   "v_smfmac_f32_16x16x64_bf16 v[28:31], v[96:99], a[64:71], v22\n\t" "v_max3_u32 v27, v27, v96, v98\n\t" "s_waitcnt vmcnt(14)\n\t" \
   "v_smfmac_f32_16x16x64_bf16 v[28:31], v[100:103], a[72:79], v22\n\t" "v_max3_u32 v27, v27, v100, v102\n\t" "s_waitcnt vmcnt(13)\n\t" \
   "v_smfmac_f32_16x16x64_bf16 v[28:31], v[104:107], a[80:87], v22\n\t" "v_max3_u32 v27, v27, v104, v106\n\t" "s_waitcnt vmcnt(12)\n\t" \
   "v_smfmac_f32_16x16x64_bf16 v[28:31], v[108:111], a[88:95], v22\n\t" "v_max3_u32 v27, v27, v108, v110\n\t" "s_waitcnt vmcnt(11)\n\t" \
   "v_smfmac_f32_16x16x64_bf16 v[28:31], v[112:115], a[96:103], v22\n\t" "v_max3_u32 v27, v27, v112, v114\n\t" "s_waitcnt vmcnt(10)\n\t" \
   "v_smfmac_f32_16x16x64_bf16 v[28:31], v[116:119], a[104:111], v22\n\t" "v_max3_u32 v27, v27, v116, v118\n\t" "s_waitcnt vmcnt(9)\n\t" \
   "v_smfmac_f32_16x16x64_bf16 v[28:31], v[120:123], a[112:119], v22\n\t" "v_max3_u32 v27, v27, v120, v122\n\t" "s_waitcnt vmcnt(8)\n\t" \
   "v_smfmac_f32_16x16x64_bf16 v[28:31], v[124:127], a[120:127], v22\n\t" "v_max3_u32 v27, v27, v124, v126\n\t" "s_waitcnt vmcnt(6)\n\t" \
   "v_smfmac_f32_16x16x64_bf16 v[28:31], v[128:131], v[32:39], v22\n\t" "v_max3_u32 v27, v27, v128, v130\n\t" "s_waitcnt vmcnt(4)\n\t" \
   "v_smfmac_f32_16x16x64_bf16 v[28:31], v[132:135], v[40:47], v22\n\t" "v_max3_u32 v27, v27, v132, v134\n\t" "s_waitcnt vmcnt(2)\n\t" \
   "v_smfmac_f32_16x16x64_bf16 v[28:31], v[136:139], v[48:55], v22\n\t" "v_max3_u32 v27, v27, v136, v138\n\t" "s_waitcnt vmcnt(0)\n\t" \
   "v_smfmac_f32_16x16x64_bf16 v[28:31], v[140:143], v[56:63], v22\n\t" "v_max3_u32 v27, v27, v140, v142\n\t" "s_branch 84b\n\t" "83:\n\t" \
   "s_or_b32 s46, s46, 0x10000000\n\t" "s_nop 4\n\t" "v_add_f32 v28, v28, v29\n\t" "v_add_f32 v30, v30, v31\n\t" "v_cvt_pk_bf16_f32 v24, v28, v30\n\t" \
   "s_nop 1\n\t" "v_mov_b32_dpp v25, v24 quad_perm:[1,0,3,2] row_mask:0xf bank_mask:0xf\n\t" "s_branch 2b\n\t" "95:\n\t" "s_mov_b32 s74, 0\n\t" \
   "97:\n\t" "buffer_load_dwordx4 v[8:11], v19, %[rs], s52 offen sc1\n\t" "s_add_u32 s74, s74, 1\n\t" "s_waitcnt vmcnt(0)\n\t" \
   "v_max3_u32 v15, v8, v9, v10\n\t" "v_max_u32 v15, v15, v11\n\t" "v_cmp_eq_u32 vcc, -1, v15\n\t" "s_cmp_gt_u32 s74, 0x10000\n\t" \
   "s_cbranch_scc1 96b\n\t" "s_cbranch_vccz 96b\n\t" "s_sleep 2\n\t" "s_branch 97b\n\t" "90:\n\t" "s_waitcnt vmcnt(0) lgkmcnt(0)"
#define FIN_CLOB "v8", "v9", "v10", "v11", "v12", "v13", "v14", "v15", "v24", "v25", "v26", "v27", "v28", "v29", "v30", "v31", "v32", "v33", "v34", "v35", "v36", "v37", "v38", "v39", "v40", "v41", "v42", "v43", "v44", "v45", "v46", "v47", "v48", "v49", "v50", "v51", "v52", "v53", "v54", "v55", "v56", "v57", "v58", "v59", "v60", "v61", "v62", "v63", "v64", "v65", "v66", "v67", "v68", "v69", "v70", "v71", "v72", "v73", "v74", "v75", "v76", "v77", "v78", "v79", "v80", "v81", "v82", "v83", "v84", "v85", "v86", "v87", "v88", "v89", "v90", "v91", "v92", "v93", "v94", "v95", "v96", "v97", "v98", "v99", "v100", "v101", "v102", "v103", "v104", "v105", "v106", "v107", "v108", "v109", "v110", "v111", "v112", "v113", "v114", "v115", "v116", "v117", "v118", "v119", "v120", "v121", "v122", "v123", "v124", "v125", "v126", "v127", "v128", "v129", "v130", "v131", "v132", "v133", "v134", "v135", "v136", "v137", "v138", "v139", "v140", "v141", "v142", "v143", "v144", "v145", "v146", "v147", "v148", "v149", "v150", "v151", "v152", "v153", "v154", "v155", "v156", "v157", "v158", "v159", "v160", "v161", "v162", "v163", "v164", "v165", "v166", "v167", "v168", "v169", "v170", "v171", "v172", "v173", "v174", "v175", "v176", "v177", "v178", "v179", "v180", "v181", "v182", "v183", "v184", "v185", "v186", "v187", "v188", "v189", "v190", "v191", "v192", "v193", "v194", "v195", "v196", "v197", "v198", "v199", "v200", "v201", "v202", "v203", "v204", "v205", "v206", "v207", "v208", "v209", "v210", "v211", "v212", "v213", "v214", "v215", "v216", "v217", "v218", "v219", "v220", "v221", "v222", "v223", "v224", "v225", "v226", "v227", "s40", "s41", "s42", "s43", "s44", "s45", "s46", "s47", "s48", "s49", "s50", "s51", "s52", "s53", "s60", "s61", "s62", "s63", "s64", "s65", "s66", "s67", "s68", "s69", "s70", "s71", "s72", "s73", "s74", "s75", "a0", "a1", "a2", "a3", "a4", "a5", "a6", "a7", "a8", "a9", "a10", "a11", "a12", "a13", "a14", "a15", "a16", "a17", "a18", "a19", "a20", "a21", "a22", "a23", "a24", "a25", "a26", "a27", "a28", "a29", "a30", "a31", "a32", "a33", "a34", "a35", "a36", "a37", "a38", "a39", "a40", "a41", "a42", "a43", "a44", "a45", "a46", "a47", "a48", "a49", "a50", "a51", "a52", "a53", "a54", "a55", "a56", "a57", "a58", "a59", "a60", "a61", "a62", "a63", "a64", "a65", "a66", "a67", "a68", "a69", "a70", "a71", "a72", "a73", "a74", "a75", "a76", "a77", "a78", "a79", "a80", "a81", "a82", "a83", "a84", "a85", "a86", "a87", "a88", "a89", "a90", "a91", "a92", "a93", "a94", "a95", "a96", "a97", "a98", "a99", "a100", "a101", "a102", "a103", "a104", "a105", "a106", "a107", "a108", "a109", "a110", "a111", "a112", "a113", "a114", "a115", "a116", "a117", "a118", "a119", "a120", "a121", "a122", "a123", "a124", "a125", "a126", "a127"
__device__ __forceinline__ void put4(v32u& d, int i, v4u v) { d[4 * i] = v.x; d[4 * i + 1] = v.y; d[4 * i + 2] = v.z; d[4 * i + 3] = v.w; }
#define LDP_ASM \
   "buffer_load_dword v26, v113, %[rw], 0 offen sc1\n\t" "s_mov_b32 s41, 0\n\t" "buffer_load_dwordx4 a[0:3], v16, %[ra], s41 offen\n\t" \
   "s_mov_b32 s41, 131072\n\t" "buffer_load_dwordx4 a[64:67], v16, %[ra], s41 offen\n\t" "s_mov_b32 s41, 524288\n\t" \
   "buffer_load_dwordx4 a[8:11], v16, %[ra], s41 offen\n\t" "s_mov_b32 s41, 655360\n\t" "buffer_load_dwordx4 a[72:75], v16, %[ra], s41 offen\n\t" \
   "s_mov_b32 s41, 1048576\n\t" "buffer_load_dwordx4 a[16:19], v16, %[ra], s41 offen\n\t" "s_mov_b32 s41, 1179648\n\t" \
   "buffer_load_dwordx4 a[80:83], v16, %[ra], s41 offen\n\t" "s_mov_b32 s41, 1572864\n\t" "buffer_load_dwordx4 a[24:27], v16, %[ra], s41 offen\n\t" \
   "s_mov_b32 s41, 1703936\n\t" "buffer_load_dwordx4 a[88:91], v16, %[ra], s41 offen\n\t" "s_mov_b32 s41, 2097152\n\t" \
   "buffer_load_dwordx4 a[32:35], v16, %[ra], s41 offen\n\t" "s_mov_b32 s41, 2228224\n\t" "buffer_load_dwordx4 a[96:99], v16, %[ra], s41 offen\n\t" \
   "s_mov_b32 s41, 2621440\n\t" "buffer_load_dwordx4 a[40:43], v16, %[ra], s41 offen\n\t" "s_mov_b32 s41, 2752512\n\t" \
   "buffer_load_dwordx4 a[104:107], v16, %[ra], s41 offen\n\t" "s_mov_b32 s41, 3145728\n\t" "buffer_load_dwordx4 a[48:51], v16, %[ra], s41 offen\n\t" \
   "s_mov_b32 s41, 3276800\n\t" "buffer_load_dwordx4 a[112:115], v16, %[ra], s41 offen\n\t" "s_mov_b32 s41, 3670016\n\t" \
   "buffer_load_dwordx4 a[56:59], v16, %[ra], s41 offen\n\t" "s_mov_b32 s41, 3801088\n\t" "buffer_load_dwordx4 a[120:123], v16, %[ra], s41 offen\n\t" \
   "s_waitcnt vmcnt(16)\n\t" "v_add_u32 v26, 1, v26\n\t" "buffer_store_dword v26, v114, %[rw], 0 offen sc1\n\t" "ds_write_b32 v115, v26\n\t" LDSTAMP(0) \
   "s_waitcnt vmcnt(15)\n\t" LDSTAMP(1) "ds_write_b128 v20, a[0:3] offset:0\n\t" "s_mov_b32 s41, 4194304\n\t" \
   "buffer_load_dwordx4 v[192:195], v16, %[ra], s41 offen\n\t" "s_waitcnt vmcnt(15)\n\t" "ds_write_b128 v20, a[64:67] offset:4096\n\t" \
   "s_mov_b32 s41, 4325376\n\t" "buffer_load_dwordx4 v[208:211], v16, %[ra], s41 offen\n\t" "s_waitcnt vmcnt(15)\n\t" \
   "ds_write_b128 v20, a[8:11] offset:8192\n\t" "s_mov_b32 s41, 4718592\n\t" "buffer_load_dwordx4 v[200:203], v16, %[ra], s41 offen\n\t" \
   "s_waitcnt vmcnt(15)\n\t" "ds_write_b128 v20, a[72:75] offset:12288\n\t" "s_mov_b32 s41, 4849664\n\t" \
   "buffer_load_dwordx4 v[216:219], v16, %[ra], s41 offen\n\t" "s_waitcnt vmcnt(15)\n\t" "ds_write_b128 v20, a[16:19] offset:16384\n\t" \
   "s_mov_b32 s41, 0\n\t" "buffer_load_dwordx4 a[128:131], v16, %[rb], s41 offen\n\t" "s_waitcnt vmcnt(15)\n\t" \
   "ds_write_b128 v20, a[80:83] offset:20480\n\t" "s_mov_b32 s41, 131072\n\t" "buffer_load_dwordx4 a[192:195], v16, %[rb], s41 offen\n\t" \
   "s_waitcnt vmcnt(15)\n\t" "ds_write_b128 v20, a[24:27] offset:24576\n\t" "s_mov_b32 s41, 524288\n\t" \
   "buffer_load_dwordx4 a[136:139], v16, %[rb], s41 offen\n\t" "s_waitcnt vmcnt(15)\n\t" "ds_write_b128 v20, a[88:91] offset:28672\n\t" \
   "s_mov_b32 s41, 655360\n\t" "buffer_load_dwordx4 a[200:203], v16, %[rb], s41 offen\n\t" "s_waitcnt vmcnt(15)\n\t" \
   "ds_write_b128 v20, a[32:35] offset:32768\n\t" "s_mov_b32 s41, 1048576\n\t" "buffer_load_dwordx4 a[144:147], v16, %[rb], s41 offen\n\t" \
   "s_waitcnt vmcnt(15)\n\t" "ds_write_b128 v20, a[96:99] offset:36864\n\t" "s_mov_b32 s41, 1179648\n\t" \
   "buffer_load_dwordx4 a[208:211], v16, %[rb], s41 offen\n\t" "s_waitcnt vmcnt(15)\n\t" "ds_write_b128 v20, a[40:43] offset:40960\n\t" \
   "s_mov_b32 s41, 1572864\n\t" "buffer_load_dwordx4 a[152:155], v16, %[rb], s41 offen\n\t" "s_waitcnt vmcnt(15)\n\t" \
   "ds_write_b128 v20, a[104:107] offset:45056\n\t" "s_mov_b32 s41, 1703936\n\t" "buffer_load_dwordx4 a[216:219], v16, %[rb], s41 offen\n\t" \
   "s_waitcnt vmcnt(15)\n\t" "ds_write_b128 v20, a[48:51] offset:49152\n\t" "s_mov_b32 s41, 2097152\n\t" \
   "buffer_load_dwordx4 a[160:163], v16, %[rb], s41 offen\n\t" "s_waitcnt vmcnt(15)\n\t" "ds_write_b128 v20, a[112:115] offset:53248\n\t" \
   "s_mov_b32 s41, 2228224\n\t" "buffer_load_dwordx4 a[224:227], v16, %[rb], s41 offen\n\t" "s_waitcnt vmcnt(15)\n\t" \
   "ds_write_b128 v20, a[56:59] offset:57344\n\t" "s_mov_b32 s41, 2621440\n\t" "buffer_load_dwordx4 a[168:171], v16, %[rb], s41 offen\n\t" \
   "s_waitcnt vmcnt(15)\n\t" "ds_write_b128 v20, a[120:123] offset:61440\n\t" "s_mov_b32 s41, 2752512\n\t" \
   "buffer_load_dwordx4 a[232:235], v16, %[rb], s41 offen\n\t" "s_waitcnt vmcnt(15)\n\t" "ds_write_b128 v23, v[192:195] offset:0\n\t" \
   "s_mov_b32 s41, 3145728\n\t" "buffer_load_dwordx4 a[176:179], v16, %[rb], s41 offen\n\t" "s_waitcnt vmcnt(15)\n\t" \
   "ds_write_b128 v23, v[208:211] offset:4096\n\t" "s_mov_b32 s41, 3276800\n\t" "buffer_load_dwordx4 a[240:243], v16, %[rb], s41 offen\n\t" \
   "s_waitcnt vmcnt(15)\n\t" "ds_write_b128 v23, v[200:203] offset:8192\n\t" "s_mov_b32 s41, 3670016\n\t" \
   "buffer_load_dwordx4 a[184:187], v16, %[rb], s41 offen\n\t" "s_waitcnt vmcnt(15)\n\t" "ds_write_b128 v23, v[216:219] offset:12288\n\t" \
   "s_mov_b32 s41, 3801088\n\t" "buffer_load_dwordx4 a[248:251], v16, %[rb], s41 offen\n\t" "s_waitcnt lgkmcnt(0)\n\t" "s_barrier\n\t" LDSTAMP(2) \
   "s_lshl_b32 s40, %[p0], 13\n\t" "v_add_u32 v24, s40, v18\n\t" "v_add_u32 v25, s40, v19\n\t" "s_add_u32 s40, s40, 8192\n\t" \
   "s_cmp_eq_u32 s40, 81920\n\t" "s_cselect_b32 s40, 0, s40\n\t" "ds_read_b64_tr_b16 a[0:1], v24 offset:0\n\t" \
   "ds_read_b64_tr_b16 a[2:3], v24 offset:4096\n\t" "ds_read_b64_tr_b16 a[64:65], v25 offset:0\n\t" "ds_read_b64_tr_b16 a[66:67], v25 offset:4096\n\t" \
   "v_add_u32 v24, s40, v18\n\t" "v_add_u32 v25, s40, v19\n\t" "s_add_u32 s40, s40, 8192\n\t" "s_cmp_eq_u32 s40, 81920\n\t" \
   "s_cselect_b32 s40, 0, s40\n\t" "ds_read_b64_tr_b16 a[8:9], v24 offset:0\n\t" "ds_read_b64_tr_b16 a[10:11], v24 offset:4096\n\t" \
   "ds_read_b64_tr_b16 a[72:73], v25 offset:0\n\t" "ds_read_b64_tr_b16 a[74:75], v25 offset:4096\n\t" "v_add_u32 v24, s40, v18\n\t" \
   "v_add_u32 v25, s40, v19\n\t" "s_add_u32 s40, s40, 8192\n\t" "s_cmp_eq_u32 s40, 81920\n\t" "s_cselect_b32 s40, 0, s40\n\t" \
   "ds_read_b64_tr_b16 a[16:17], v24 offset:0\n\t" "ds_read_b64_tr_b16 a[18:19], v24 offset:4096\n\t" "ds_read_b64_tr_b16 a[80:81], v25 offset:0\n\t" \
   "ds_read_b64_tr_b16 a[82:83], v25 offset:4096\n\t" "v_add_u32 v24, s40, v18\n\t" "v_add_u32 v25, s40, v19\n\t" "s_add_u32 s40, s40, 8192\n\t" \
   "s_cmp_eq_u32 s40, 81920\n\t" "s_cselect_b32 s40, 0, s40\n\t" "ds_read_b64_tr_b16 a[24:25], v24 offset:0\n\t" \
   "ds_read_b64_tr_b16 a[26:27], v24 offset:4096\n\t" "ds_read_b64_tr_b16 a[88:89], v25 offset:0\n\t" \
   "ds_read_b64_tr_b16 a[90:91], v25 offset:4096\n\t" "v_add_u32 v24, s40, v18\n\t" "v_add_u32 v25, s40, v19\n\t" "s_add_u32 s40, s40, 8192\n\t" \
   "s_cmp_eq_u32 s40, 81920\n\t" "s_cselect_b32 s40, 0, s40\n\t" "ds_read_b64_tr_b16 a[32:33], v24 offset:0\n\t" \
   "ds_read_b64_tr_b16 a[34:35], v24 offset:4096\n\t" "ds_read_b64_tr_b16 a[96:97], v25 offset:0\n\t" \
   "ds_read_b64_tr_b16 a[98:99], v25 offset:4096\n\t" "v_add_u32 v24, s40, v18\n\t" "v_add_u32 v25, s40, v19\n\t" "s_add_u32 s40, s40, 8192\n\t" \
   "s_cmp_eq_u32 s40, 81920\n\t" "s_cselect_b32 s40, 0, s40\n\t" "ds_read_b64_tr_b16 a[40:41], v24 offset:0\n\t" \
   "ds_read_b64_tr_b16 a[42:43], v24 offset:4096\n\t" "ds_read_b64_tr_b16 a[104:105], v25 offset:0\n\t" \
   "ds_read_b64_tr_b16 a[106:107], v25 offset:4096\n\t" "v_add_u32 v24, s40, v18\n\t" "v_add_u32 v25, s40, v19\n\t" "s_add_u32 s40, s40, 8192\n\t" \
   "s_cmp_eq_u32 s40, 81920\n\t" "s_cselect_b32 s40, 0, s40\n\t" "ds_read_b64_tr_b16 a[48:49], v24 offset:0\n\t" \
   "ds_read_b64_tr_b16 a[50:51], v24 offset:4096\n\t" "ds_read_b64_tr_b16 a[112:113], v25 offset:0\n\t" \
   "ds_read_b64_tr_b16 a[114:115], v25 offset:4096\n\t" "v_add_u32 v24, s40, v18\n\t" "v_add_u32 v25, s40, v19\n\t" "s_add_u32 s40, s40, 8192\n\t" \
   "s_cmp_eq_u32 s40, 81920\n\t" "s_cselect_b32 s40, 0, s40\n\t" "ds_read_b64_tr_b16 a[56:57], v24 offset:0\n\t" \
   "ds_read_b64_tr_b16 a[58:59], v24 offset:4096\n\t" "ds_read_b64_tr_b16 a[120:121], v25 offset:0\n\t" \
   "ds_read_b64_tr_b16 a[122:123], v25 offset:4096\n\t" "v_add_u32 v24, s40, v18\n\t" "v_add_u32 v25, s40, v19\n\t" "s_add_u32 s40, s40, 8192\n\t" \
   "s_cmp_eq_u32 s40, 81920\n\t" "s_cselect_b32 s40, 0, s40\n\t" "ds_read_b64_tr_b16 v[192:193], v24 offset:0\n\t" \
   "ds_read_b64_tr_b16 v[194:195], v24 offset:4096\n\t" "ds_read_b64_tr_b16 v[208:209], v25 offset:0\n\t" \
   "ds_read_b64_tr_b16 v[210:211], v25 offset:4096\n\t" "v_add_u32 v24, s40, v18\n\t" "v_add_u32 v25, s40, v19\n\t" "s_add_u32 s40, s40, 8192\n\t" \
   "s_cmp_eq_u32 s40, 81920\n\t" "s_cselect_b32 s40, 0, s40\n\t" "ds_read_b64_tr_b16 v[200:201], v24 offset:0\n\t" \
   "ds_read_b64_tr_b16 v[202:203], v24 offset:4096\n\t" "ds_read_b64_tr_b16 v[216:217], v25 offset:0\n\t" \
   "ds_read_b64_tr_b16 v[218:219], v25 offset:4096\n\t" "s_waitcnt vmcnt(15)\n\t" "s_waitcnt lgkmcnt(0)\n\t" "s_barrier\n\t" \
   "ds_write_b128 v20, a[128:131] offset:0\n\t" "s_mov_b32 s41, 4194304\n\t" "buffer_load_dwordx4 v[224:227], v16, %[rb], s41 offen\n\t" \
   "s_waitcnt vmcnt(15)\n\t" "ds_write_b128 v20, a[192:195] offset:4096\n\t" "s_mov_b32 s41, 4325376\n\t" \
   "buffer_load_dwordx4 v[240:243], v16, %[rb], s41 offen\n\t" "s_waitcnt vmcnt(15)\n\t" "ds_write_b128 v20, a[136:139] offset:8192\n\t" \
   "s_mov_b32 s41, 4718592\n\t" "buffer_load_dwordx4 v[232:235], v16, %[rb], s41 offen\n\t" "s_waitcnt vmcnt(15)\n\t" \
   "ds_write_b128 v20, a[200:203] offset:12288\n\t" "s_mov_b32 s41, 4849664\n\t" "buffer_load_dwordx4 v[248:251], v16, %[rb], s41 offen\n\t" \
   "s_waitcnt vmcnt(15)\n\t" "ds_write_b128 v20, a[144:147] offset:16384\n\t" "s_mov_b32 s41, 262144\n\t" \
   "buffer_load_dwordx4 a[4:7], v16, %[ra], s41 offen\n\t" "s_waitcnt vmcnt(15)\n\t" "ds_write_b128 v20, a[208:211] offset:20480\n\t" \
   "s_mov_b32 s41, 393216\n\t" "buffer_load_dwordx4 a[68:71], v16, %[ra], s41 offen\n\t" "s_waitcnt vmcnt(15)\n\t" \
   "ds_write_b128 v20, a[152:155] offset:24576\n\t" "s_mov_b32 s41, 786432\n\t" "buffer_load_dwordx4 a[12:15], v16, %[ra], s41 offen\n\t" \
   "s_waitcnt vmcnt(15)\n\t" "ds_write_b128 v20, a[216:219] offset:28672\n\t" "s_mov_b32 s41, 917504\n\t" \
   "buffer_load_dwordx4 a[76:79], v16, %[ra], s41 offen\n\t" "s_waitcnt vmcnt(15)\n\t" "ds_write_b128 v20, a[160:163] offset:32768\n\t" \
   "s_mov_b32 s41, 1310720\n\t" "buffer_load_dwordx4 a[20:23], v16, %[ra], s41 offen\n\t" "s_waitcnt vmcnt(15)\n\t" \
   "ds_write_b128 v20, a[224:227] offset:36864\n\t" "s_mov_b32 s41, 1441792\n\t" "buffer_load_dwordx4 a[84:87], v16, %[ra], s41 offen\n\t" \
   "s_waitcnt vmcnt(15)\n\t" "ds_write_b128 v20, a[168:171] offset:40960\n\t" "s_mov_b32 s41, 1835008\n\t" \
   "buffer_load_dwordx4 a[28:31], v16, %[ra], s41 offen\n\t" "s_waitcnt vmcnt(15)\n\t" "ds_write_b128 v20, a[232:235] offset:45056\n\t" \
   "s_mov_b32 s41, 1966080\n\t" "buffer_load_dwordx4 a[92:95], v16, %[ra], s41 offen\n\t" "s_waitcnt vmcnt(15)\n\t" \
   "ds_write_b128 v20, a[176:179] offset:49152\n\t" "s_mov_b32 s41, 2359296\n\t" "buffer_load_dwordx4 a[36:39], v16, %[ra], s41 offen\n\t" \
   "s_waitcnt vmcnt(15)\n\t" "ds_write_b128 v20, a[240:243] offset:53248\n\t" "s_mov_b32 s41, 2490368\n\t" \
   "buffer_load_dwordx4 a[100:103], v16, %[ra], s41 offen\n\t" "s_waitcnt vmcnt(15)\n\t" "ds_write_b128 v20, a[184:187] offset:57344\n\t" \
   "s_mov_b32 s41, 2883584\n\t" "buffer_load_dwordx4 a[44:47], v16, %[ra], s41 offen\n\t" "s_waitcnt vmcnt(15)\n\t" \
   "ds_write_b128 v20, a[248:251] offset:61440\n\t" "s_mov_b32 s41, 3014656\n\t" "buffer_load_dwordx4 a[108:111], v16, %[ra], s41 offen\n\t" \
   "s_waitcnt vmcnt(15)\n\t" "ds_write_b128 v23, v[224:227] offset:0\n\t" "s_mov_b32 s41, 3407872\n\t" \
   "buffer_load_dwordx4 a[52:55], v16, %[ra], s41 offen\n\t" "s_waitcnt vmcnt(15)\n\t" "ds_write_b128 v23, v[240:243] offset:4096\n\t" \
   "s_mov_b32 s41, 3538944\n\t" "buffer_load_dwordx4 a[116:119], v16, %[ra], s41 offen\n\t" "s_waitcnt vmcnt(15)\n\t" \
   "ds_write_b128 v23, v[232:235] offset:8192\n\t" "s_mov_b32 s41, 3932160\n\t" "buffer_load_dwordx4 a[60:63], v16, %[ra], s41 offen\n\t" \
   "s_waitcnt vmcnt(15)\n\t" "ds_write_b128 v23, v[248:251] offset:12288\n\t" "s_mov_b32 s41, 4063232\n\t" \
   "buffer_load_dwordx4 a[124:127], v16, %[ra], s41 offen\n\t" "s_waitcnt lgkmcnt(0)\n\t" "s_barrier\n\t" LDSTAMP(3) "s_lshl_b32 s40, %[p0], 13\n\t" \
   "v_add_u32 v24, s40, v18\n\t" "v_add_u32 v25, s40, v19\n\t" "s_add_u32 s40, s40, 8192\n\t" "s_cmp_eq_u32 s40, 81920\n\t" \
   "s_cselect_b32 s40, 0, s40\n\t" "ds_read_b64_tr_b16 a[128:129], v24 offset:0\n\t" "ds_read_b64_tr_b16 a[130:131], v24 offset:4096\n\t" \
   "ds_read_b64_tr_b16 a[192:193], v25 offset:0\n\t" "ds_read_b64_tr_b16 a[194:195], v25 offset:4096\n\t" "v_add_u32 v24, s40, v18\n\t" \
   "v_add_u32 v25, s40, v19\n\t" "s_add_u32 s40, s40, 8192\n\t" "s_cmp_eq_u32 s40, 81920\n\t" "s_cselect_b32 s40, 0, s40\n\t" \
   "ds_read_b64_tr_b16 a[136:137], v24 offset:0\n\t" "ds_read_b64_tr_b16 a[138:139], v24 offset:4096\n\t" \
   "ds_read_b64_tr_b16 a[200:201], v25 offset:0\n\t" "ds_read_b64_tr_b16 a[202:203], v25 offset:4096\n\t" "v_add_u32 v24, s40, v18\n\t" \
   "v_add_u32 v25, s40, v19\n\t" "s_add_u32 s40, s40, 8192\n\t" "s_cmp_eq_u32 s40, 81920\n\t" "s_cselect_b32 s40, 0, s40\n\t" \
   "ds_read_b64_tr_b16 a[144:145], v24 offset:0\n\t" "ds_read_b64_tr_b16 a[146:147], v24 offset:4096\n\t" \
   "ds_read_b64_tr_b16 a[208:209], v25 offset:0\n\t" "ds_read_b64_tr_b16 a[210:211], v25 offset:4096\n\t" "v_add_u32 v24, s40, v18\n\t" \
   "v_add_u32 v25, s40, v19\n\t" "s_add_u32 s40, s40, 8192\n\t" "s_cmp_eq_u32 s40, 81920\n\t" "s_cselect_b32 s40, 0, s40\n\t" \
   "ds_read_b64_tr_b16 a[152:153], v24 offset:0\n\t" "ds_read_b64_tr_b16 a[154:155], v24 offset:4096\n\t" \
   "ds_read_b64_tr_b16 a[216:217], v25 offset:0\n\t" "ds_read_b64_tr_b16 a[218:219], v25 offset:4096\n\t" "v_add_u32 v24, s40, v18\n\t" \
   "v_add_u32 v25, s40, v19\n\t" "s_add_u32 s40, s40, 8192\n\t" "s_cmp_eq_u32 s40, 81920\n\t" "s_cselect_b32 s40, 0, s40\n\t" \
   "ds_read_b64_tr_b16 a[160:161], v24 offset:0\n\t" "ds_read_b64_tr_b16 a[162:163], v24 offset:4096\n\t" \
   "ds_read_b64_tr_b16 a[224:225], v25 offset:0\n\t" "ds_read_b64_tr_b16 a[226:227], v25 offset:4096\n\t" "v_add_u32 v24, s40, v18\n\t" \
   "v_add_u32 v25, s40, v19\n\t" "s_add_u32 s40, s40, 8192\n\t" "s_cmp_eq_u32 s40, 81920\n\t" "s_cselect_b32 s40, 0, s40\n\t" \
   "ds_read_b64_tr_b16 a[168:169], v24 offset:0\n\t" "ds_read_b64_tr_b16 a[170:171], v24 offset:4096\n\t" \
   "ds_read_b64_tr_b16 a[232:233], v25 offset:0\n\t" "ds_read_b64_tr_b16 a[234:235], v25 offset:4096\n\t" "v_add_u32 v24, s40, v18\n\t" \
   "v_add_u32 v25, s40, v19\n\t" "s_add_u32 s40, s40, 8192\n\t" "s_cmp_eq_u32 s40, 81920\n\t" "s_cselect_b32 s40, 0, s40\n\t" \
   "ds_read_b64_tr_b16 a[176:177], v24 offset:0\n\t" "ds_read_b64_tr_b16 a[178:179], v24 offset:4096\n\t" \
   "ds_read_b64_tr_b16 a[240:241], v25 offset:0\n\t" "ds_read_b64_tr_b16 a[242:243], v25 offset:4096\n\t" "v_add_u32 v24, s40, v18\n\t" \
   "v_add_u32 v25, s40, v19\n\t" "s_add_u32 s40, s40, 8192\n\t" "s_cmp_eq_u32 s40, 81920\n\t" "s_cselect_b32 s40, 0, s40\n\t" \
   "ds_read_b64_tr_b16 a[184:185], v24 offset:0\n\t" "ds_read_b64_tr_b16 a[186:187], v24 offset:4096\n\t" \
   "ds_read_b64_tr_b16 a[248:249], v25 offset:0\n\t" "ds_read_b64_tr_b16 a[250:251], v25 offset:4096\n\t" "v_add_u32 v24, s40, v18\n\t" \
   "v_add_u32 v25, s40, v19\n\t" "s_add_u32 s40, s40, 8192\n\t" "s_cmp_eq_u32 s40, 81920\n\t" "s_cselect_b32 s40, 0, s40\n\t" \
   "ds_read_b64_tr_b16 v[224:225], v24 offset:0\n\t" "ds_read_b64_tr_b16 v[226:227], v24 offset:4096\n\t" \
   "ds_read_b64_tr_b16 v[240:241], v25 offset:0\n\t" "ds_read_b64_tr_b16 v[242:243], v25 offset:4096\n\t" "v_add_u32 v24, s40, v18\n\t" \
   "v_add_u32 v25, s40, v19\n\t" "s_add_u32 s40, s40, 8192\n\t" "s_cmp_eq_u32 s40, 81920\n\t" "s_cselect_b32 s40, 0, s40\n\t" \
   "ds_read_b64_tr_b16 v[232:233], v24 offset:0\n\t" "ds_read_b64_tr_b16 v[234:235], v24 offset:4096\n\t" \
   "ds_read_b64_tr_b16 v[248:249], v25 offset:0\n\t" "ds_read_b64_tr_b16 v[250:251], v25 offset:4096\n\t" "s_waitcnt vmcnt(15)\n\t" \
   "s_waitcnt lgkmcnt(0)\n\t" "s_barrier\n\t" "ds_write_b128 v20, a[4:7] offset:0\n\t" "s_mov_b32 s41, 4456448\n\t" \
   "buffer_load_dwordx4 v[196:199], v16, %[ra], s41 offen\n\t" "s_waitcnt vmcnt(15)\n\t" "ds_write_b128 v20, a[68:71] offset:4096\n\t" \
   "s_mov_b32 s41, 4587520\n\t" "buffer_load_dwordx4 v[212:215], v16, %[ra], s41 offen\n\t" "s_waitcnt vmcnt(15)\n\t" \
   "ds_write_b128 v20, a[12:15] offset:8192\n\t" "s_mov_b32 s41, 4980736\n\t" "buffer_load_dwordx4 v[204:207], v16, %[ra], s41 offen\n\t" \
   "s_waitcnt vmcnt(15)\n\t" "ds_write_b128 v20, a[76:79] offset:12288\n\t" "s_mov_b32 s41, 5111808\n\t" \
   "buffer_load_dwordx4 v[220:223], v16, %[ra], s41 offen\n\t" "s_waitcnt vmcnt(15)\n\t" "ds_write_b128 v20, a[20:23] offset:16384\n\t" \
   "s_mov_b32 s41, 262144\n\t" "buffer_load_dwordx4 a[132:135], v16, %[rb], s41 offen\n\t" "s_waitcnt vmcnt(15)\n\t" \
   "ds_write_b128 v20, a[84:87] offset:20480\n\t" "s_mov_b32 s41, 393216\n\t" "buffer_load_dwordx4 a[196:199], v16, %[rb], s41 offen\n\t" \
   "s_waitcnt vmcnt(15)\n\t" "ds_write_b128 v20, a[28:31] offset:24576\n\t" "s_mov_b32 s41, 786432\n\t" \
   "buffer_load_dwordx4 a[140:143], v16, %[rb], s41 offen\n\t" "s_waitcnt vmcnt(15)\n\t" "ds_write_b128 v20, a[92:95] offset:28672\n\t" \
   "s_mov_b32 s41, 917504\n\t" "buffer_load_dwordx4 a[204:207], v16, %[rb], s41 offen\n\t" "s_waitcnt vmcnt(15)\n\t" \
   "ds_write_b128 v20, a[36:39] offset:32768\n\t" "s_mov_b32 s41, 1310720\n\t" "buffer_load_dwordx4 a[148:151], v16, %[rb], s41 offen\n\t" \
   "s_waitcnt vmcnt(15)\n\t" "ds_write_b128 v20, a[100:103] offset:36864\n\t" "s_mov_b32 s41, 1441792\n\t" \
   "buffer_load_dwordx4 a[212:215], v16, %[rb], s41 offen\n\t" "s_waitcnt vmcnt(15)\n\t" "ds_write_b128 v20, a[44:47] offset:40960\n\t" \
   "s_mov_b32 s41, 1835008\n\t" "buffer_load_dwordx4 a[156:159], v16, %[rb], s41 offen\n\t" "s_waitcnt vmcnt(15)\n\t" \
   "ds_write_b128 v20, a[108:111] offset:45056\n\t" "s_mov_b32 s41, 1966080\n\t" "buffer_load_dwordx4 a[220:223], v16, %[rb], s41 offen\n\t" \
   "s_waitcnt vmcnt(15)\n\t" "ds_write_b128 v20, a[52:55] offset:49152\n\t" "s_mov_b32 s41, 2359296\n\t" \
   "buffer_load_dwordx4 a[164:167], v16, %[rb], s41 offen\n\t" "s_waitcnt vmcnt(15)\n\t" "ds_write_b128 v20, a[116:119] offset:53248\n\t" \
   "s_mov_b32 s41, 2490368\n\t" "buffer_load_dwordx4 a[228:231], v16, %[rb], s41 offen\n\t" "s_waitcnt vmcnt(15)\n\t" \
   "ds_write_b128 v20, a[60:63] offset:57344\n\t" "s_mov_b32 s41, 2883584\n\t" "buffer_load_dwordx4 a[172:175], v16, %[rb], s41 offen\n\t" \
   "s_waitcnt vmcnt(15)\n\t" "ds_write_b128 v20, a[124:127] offset:61440\n\t" "s_mov_b32 s41, 3014656\n\t" \
   "buffer_load_dwordx4 a[236:239], v16, %[rb], s41 offen\n\t" "s_waitcnt vmcnt(15)\n\t" "ds_write_b128 v23, v[196:199] offset:0\n\t" \
   "s_mov_b32 s41, 3407872\n\t" "buffer_load_dwordx4 a[180:183], v16, %[rb], s41 offen\n\t" "s_waitcnt vmcnt(15)\n\t" \
   "ds_write_b128 v23, v[212:215] offset:4096\n\t" "s_mov_b32 s41, 3538944\n\t" "buffer_load_dwordx4 a[244:247], v16, %[rb], s41 offen\n\t" \
   "s_waitcnt vmcnt(15)\n\t" "ds_write_b128 v23, v[204:207] offset:8192\n\t" "s_mov_b32 s41, 3932160\n\t" \
   "buffer_load_dwordx4 a[188:191], v16, %[rb], s41 offen\n\t" "s_waitcnt vmcnt(15)\n\t" "ds_write_b128 v23, v[220:223] offset:12288\n\t" \
   "s_mov_b32 s41, 4063232\n\t" "buffer_load_dwordx4 a[252:255], v16, %[rb], s41 offen\n\t" "s_waitcnt lgkmcnt(0)\n\t" "s_barrier\n\t" LDSTAMP(4) \
   "s_lshl_b32 s40, %[p0], 13\n\t" "v_add_u32 v24, s40, v18\n\t" "v_add_u32 v25, s40, v19\n\t" "s_add_u32 s40, s40, 8192\n\t" \
   "s_cmp_eq_u32 s40, 81920\n\t" "s_cselect_b32 s40, 0, s40\n\t" "ds_read_b64_tr_b16 a[4:5], v24 offset:0\n\t" \
   "ds_read_b64_tr_b16 a[6:7], v24 offset:4096\n\t" "ds_read_b64_tr_b16 a[68:69], v25 offset:0\n\t" "ds_read_b64_tr_b16 a[70:71], v25 offset:4096\n\t" \
   "v_add_u32 v24, s40, v18\n\t" "v_add_u32 v25, s40, v19\n\t" "s_add_u32 s40, s40, 8192\n\t" "s_cmp_eq_u32 s40, 81920\n\t" \
   "s_cselect_b32 s40, 0, s40\n\t" "ds_read_b64_tr_b16 a[12:13], v24 offset:0\n\t" "ds_read_b64_tr_b16 a[14:15], v24 offset:4096\n\t" \
   "ds_read_b64_tr_b16 a[76:77], v25 offset:0\n\t" "ds_read_b64_tr_b16 a[78:79], v25 offset:4096\n\t" "v_add_u32 v24, s40, v18\n\t" \
   "v_add_u32 v25, s40, v19\n\t" "s_add_u32 s40, s40, 8192\n\t" "s_cmp_eq_u32 s40, 81920\n\t" "s_cselect_b32 s40, 0, s40\n\t" \
   "ds_read_b64_tr_b16 a[20:21], v24 offset:0\n\t" "ds_read_b64_tr_b16 a[22:23], v24 offset:4096\n\t" "ds_read_b64_tr_b16 a[84:85], v25 offset:0\n\t" \
   "ds_read_b64_tr_b16 a[86:87], v25 offset:4096\n\t" "v_add_u32 v24, s40, v18\n\t" "v_add_u32 v25, s40, v19\n\t" "s_add_u32 s40, s40, 8192\n\t" \
   "s_cmp_eq_u32 s40, 81920\n\t" "s_cselect_b32 s40, 0, s40\n\t" "ds_read_b64_tr_b16 a[28:29], v24 offset:0\n\t" \
   "ds_read_b64_tr_b16 a[30:31], v24 offset:4096\n\t" "ds_read_b64_tr_b16 a[92:93], v25 offset:0\n\t" \
   "ds_read_b64_tr_b16 a[94:95], v25 offset:4096\n\t" "v_add_u32 v24, s40, v18\n\t" "v_add_u32 v25, s40, v19\n\t" "s_add_u32 s40, s40, 8192\n\t" \
   "s_cmp_eq_u32 s40, 81920\n\t" "s_cselect_b32 s40, 0, s40\n\t" "ds_read_b64_tr_b16 a[36:37], v24 offset:0\n\t" \
   "ds_read_b64_tr_b16 a[38:39], v24 offset:4096\n\t" "ds_read_b64_tr_b16 a[100:101], v25 offset:0\n\t" \
   "ds_read_b64_tr_b16 a[102:103], v25 offset:4096\n\t" "v_add_u32 v24, s40, v18\n\t" "v_add_u32 v25, s40, v19\n\t" "s_add_u32 s40, s40, 8192\n\t" \
   "s_cmp_eq_u32 s40, 81920\n\t" "s_cselect_b32 s40, 0, s40\n\t" "ds_read_b64_tr_b16 a[44:45], v24 offset:0\n\t" \
   "ds_read_b64_tr_b16 a[46:47], v24 offset:4096\n\t" "ds_read_b64_tr_b16 a[108:109], v25 offset:0\n\t" \
   "ds_read_b64_tr_b16 a[110:111], v25 offset:4096\n\t" "v_add_u32 v24, s40, v18\n\t" "v_add_u32 v25, s40, v19\n\t" "s_add_u32 s40, s40, 8192\n\t" \
   "s_cmp_eq_u32 s40, 81920\n\t" "s_cselect_b32 s40, 0, s40\n\t" "ds_read_b64_tr_b16 a[52:53], v24 offset:0\n\t" \
   "ds_read_b64_tr_b16 a[54:55], v24 offset:4096\n\t" "ds_read_b64_tr_b16 a[116:117], v25 offset:0\n\t" \
   "ds_read_b64_tr_b16 a[118:119], v25 offset:4096\n\t" "v_add_u32 v24, s40, v18\n\t" "v_add_u32 v25, s40, v19\n\t" "s_add_u32 s40, s40, 8192\n\t" \
   "s_cmp_eq_u32 s40, 81920\n\t" "s_cselect_b32 s40, 0, s40\n\t" "ds_read_b64_tr_b16 a[60:61], v24 offset:0\n\t" \
   "ds_read_b64_tr_b16 a[62:63], v24 offset:4096\n\t" "ds_read_b64_tr_b16 a[124:125], v25 offset:0\n\t" \
   "ds_read_b64_tr_b16 a[126:127], v25 offset:4096\n\t" "v_add_u32 v24, s40, v18\n\t" "v_add_u32 v25, s40, v19\n\t" "s_add_u32 s40, s40, 8192\n\t" \
   "s_cmp_eq_u32 s40, 81920\n\t" "s_cselect_b32 s40, 0, s40\n\t" "ds_read_b64_tr_b16 v[196:197], v24 offset:0\n\t" \
   "ds_read_b64_tr_b16 v[198:199], v24 offset:4096\n\t" "ds_read_b64_tr_b16 v[212:213], v25 offset:0\n\t" \
   "ds_read_b64_tr_b16 v[214:215], v25 offset:4096\n\t" "v_add_u32 v24, s40, v18\n\t" "v_add_u32 v25, s40, v19\n\t" "s_add_u32 s40, s40, 8192\n\t" \
   "s_cmp_eq_u32 s40, 81920\n\t" "s_cselect_b32 s40, 0, s40\n\t" "ds_read_b64_tr_b16 v[204:205], v24 offset:0\n\t" \
   "ds_read_b64_tr_b16 v[206:207], v24 offset:4096\n\t" "ds_read_b64_tr_b16 v[220:221], v25 offset:0\n\t" \
   "ds_read_b64_tr_b16 v[222:223], v25 offset:4096\n\t" "s_waitcnt vmcnt(15)\n\t" "s_waitcnt lgkmcnt(0)\n\t" "s_barrier\n\t" \
   "ds_write_b128 v20, a[132:135] offset:0\n\t" "s_mov_b32 s41, 4456448\n\t" "buffer_load_dwordx4 v[228:231], v16, %[rb], s41 offen\n\t" \
   "s_waitcnt vmcnt(15)\n\t" "ds_write_b128 v20, a[196:199] offset:4096\n\t" "s_mov_b32 s41, 4587520\n\t" \
   "buffer_load_dwordx4 v[244:247], v16, %[rb], s41 offen\n\t" "s_waitcnt vmcnt(15)\n\t" "ds_write_b128 v20, a[140:143] offset:8192\n\t" \
   "s_mov_b32 s41, 4980736\n\t" "buffer_load_dwordx4 v[236:239], v16, %[rb], s41 offen\n\t" "s_waitcnt vmcnt(15)\n\t" \
   "ds_write_b128 v20, a[204:207] offset:12288\n\t" "s_mov_b32 s41, 5111808\n\t" "buffer_load_dwordx4 v[252:255], v16, %[rb], s41 offen\n\t" \
   "s_waitcnt vmcnt(15)\n\t" "ds_write_b128 v20, a[148:151] offset:16384\n\t" "s_waitcnt vmcnt(14)\n\t" \
   "ds_write_b128 v20, a[212:215] offset:20480\n\t" "s_waitcnt vmcnt(13)\n\t" "ds_write_b128 v20, a[156:159] offset:24576\n\t" \
   "s_waitcnt vmcnt(12)\n\t" "ds_write_b128 v20, a[220:223] offset:28672\n\t" "s_waitcnt vmcnt(11)\n\t" \
   "ds_write_b128 v20, a[164:167] offset:32768\n\t" "s_waitcnt vmcnt(10)\n\t" "ds_write_b128 v20, a[228:231] offset:36864\n\t" \
   "buffer_load_dwordx4 v[28:31], v112, %[rw], 0 offen sc1\n\t" "s_waitcnt vmcnt(10)\n\t" "ds_write_b128 v20, a[172:175] offset:40960\n\t" \
   "s_waitcnt vmcnt(9)\n\t" "ds_write_b128 v20, a[236:239] offset:45056\n\t" "s_waitcnt vmcnt(8)\n\t" "ds_write_b128 v20, a[180:183] offset:49152\n\t" \
   "s_waitcnt vmcnt(7)\n\t" "ds_write_b128 v20, a[244:247] offset:53248\n\t" "s_waitcnt vmcnt(6)\n\t" "ds_write_b128 v20, a[188:191] offset:57344\n\t" \
   "s_waitcnt vmcnt(5)\n\t" "ds_write_b128 v20, a[252:255] offset:61440\n\t" "s_waitcnt vmcnt(4)\n\t" "ds_write_b128 v23, v[228:231] offset:0\n\t" \
   "s_waitcnt vmcnt(3)\n\t" "ds_write_b128 v23, v[244:247] offset:4096\n\t" "s_waitcnt vmcnt(2)\n\t" "ds_write_b128 v23, v[236:239] offset:8192\n\t" \
   "s_waitcnt vmcnt(1)\n\t" "ds_write_b128 v23, v[252:255] offset:12288\n\t" "s_waitcnt lgkmcnt(0)\n\t" "s_barrier\n\t" LDSTAMP(5) \
   "s_lshl_b32 s40, %[p0], 13\n\t" "v_add_u32 v24, s40, v18\n\t" "v_add_u32 v25, s40, v19\n\t" "s_add_u32 s40, s40, 8192\n\t" \
   "s_cmp_eq_u32 s40, 81920\n\t" "s_cselect_b32 s40, 0, s40\n\t" "ds_read_b64_tr_b16 a[132:133], v24 offset:0\n\t" \
   "ds_read_b64_tr_b16 a[134:135], v24 offset:4096\n\t" "ds_read_b64_tr_b16 a[196:197], v25 offset:0\n\t" \
   "ds_read_b64_tr_b16 a[198:199], v25 offset:4096\n\t" "v_add_u32 v24, s40, v18\n\t" "v_add_u32 v25, s40, v19\n\t" "s_add_u32 s40, s40, 8192\n\t" \
   "s_cmp_eq_u32 s40, 81920\n\t" "s_cselect_b32 s40, 0, s40\n\t" "ds_read_b64_tr_b16 a[140:141], v24 offset:0\n\t" \
   "ds_read_b64_tr_b16 a[142:143], v24 offset:4096\n\t" "ds_read_b64_tr_b16 a[204:205], v25 offset:0\n\t" \
   "ds_read_b64_tr_b16 a[206:207], v25 offset:4096\n\t" "v_add_u32 v24, s40, v18\n\t" "v_add_u32 v25, s40, v19\n\t" "s_add_u32 s40, s40, 8192\n\t" \
   "s_cmp_eq_u32 s40, 81920\n\t" "s_cselect_b32 s40, 0, s40\n\t" "ds_read_b64_tr_b16 a[148:149], v24 offset:0\n\t" \
   "ds_read_b64_tr_b16 a[150:151], v24 offset:4096\n\t" "ds_read_b64_tr_b16 a[212:213], v25 offset:0\n\t" \
   "ds_read_b64_tr_b16 a[214:215], v25 offset:4096\n\t" "v_add_u32 v24, s40, v18\n\t" "v_add_u32 v25, s40, v19\n\t" "s_add_u32 s40, s40, 8192\n\t" \
   "s_cmp_eq_u32 s40, 81920\n\t" "s_cselect_b32 s40, 0, s40\n\t" "ds_read_b64_tr_b16 a[156:157], v24 offset:0\n\t" \
   "ds_read_b64_tr_b16 a[158:159], v24 offset:4096\n\t" "ds_read_b64_tr_b16 a[220:221], v25 offset:0\n\t" \
   "ds_read_b64_tr_b16 a[222:223], v25 offset:4096\n\t" "v_add_u32 v24, s40, v18\n\t" "v_add_u32 v25, s40, v19\n\t" "s_add_u32 s40, s40, 8192\n\t" \
   "s_cmp_eq_u32 s40, 81920\n\t" "s_cselect_b32 s40, 0, s40\n\t" "ds_read_b64_tr_b16 a[164:165], v24 offset:0\n\t" \
   "ds_read_b64_tr_b16 a[166:167], v24 offset:4096\n\t" "ds_read_b64_tr_b16 a[228:229], v25 offset:0\n\t" \
   "ds_read_b64_tr_b16 a[230:231], v25 offset:4096\n\t" "v_add_u32 v24, s40, v18\n\t" "v_add_u32 v25, s40, v19\n\t" "s_add_u32 s40, s40, 8192\n\t" \
   "s_cmp_eq_u32 s40, 81920\n\t" "s_cselect_b32 s40, 0, s40\n\t" "ds_read_b64_tr_b16 a[172:173], v24 offset:0\n\t" \
   "ds_read_b64_tr_b16 a[174:175], v24 offset:4096\n\t" "ds_read_b64_tr_b16 a[236:237], v25 offset:0\n\t" \
   "ds_read_b64_tr_b16 a[238:239], v25 offset:4096\n\t" "v_add_u32 v24, s40, v18\n\t" "v_add_u32 v25, s40, v19\n\t" "s_add_u32 s40, s40, 8192\n\t" \
   "s_cmp_eq_u32 s40, 81920\n\t" "s_cselect_b32 s40, 0, s40\n\t" "ds_read_b64_tr_b16 a[180:181], v24 offset:0\n\t" \
   "ds_read_b64_tr_b16 a[182:183], v24 offset:4096\n\t" "ds_read_b64_tr_b16 a[244:245], v25 offset:0\n\t" \
   "ds_read_b64_tr_b16 a[246:247], v25 offset:4096\n\t" "v_add_u32 v24, s40, v18\n\t" "v_add_u32 v25, s40, v19\n\t" "s_add_u32 s40, s40, 8192\n\t" \
   "s_cmp_eq_u32 s40, 81920\n\t" "s_cselect_b32 s40, 0, s40\n\t" "ds_read_b64_tr_b16 a[188:189], v24 offset:0\n\t" \
   "ds_read_b64_tr_b16 a[190:191], v24 offset:4096\n\t" "ds_read_b64_tr_b16 a[252:253], v25 offset:0\n\t" \
   "ds_read_b64_tr_b16 a[254:255], v25 offset:4096\n\t" "v_add_u32 v24, s40, v18\n\t" "v_add_u32 v25, s40, v19\n\t" "s_add_u32 s40, s40, 8192\n\t" \
   "s_cmp_eq_u32 s40, 81920\n\t" "s_cselect_b32 s40, 0, s40\n\t" "ds_read_b64_tr_b16 v[228:229], v24 offset:0\n\t" \
   "ds_read_b64_tr_b16 v[230:231], v24 offset:4096\n\t" "ds_read_b64_tr_b16 v[244:245], v25 offset:0\n\t" \
   "ds_read_b64_tr_b16 v[246:247], v25 offset:4096\n\t" "v_add_u32 v24, s40, v18\n\t" "v_add_u32 v25, s40, v19\n\t" "s_add_u32 s40, s40, 8192\n\t" \
   "s_cmp_eq_u32 s40, 81920\n\t" "s_cselect_b32 s40, 0, s40\n\t" "ds_read_b64_tr_b16 v[236:237], v24 offset:0\n\t" \
   "ds_read_b64_tr_b16 v[238:239], v24 offset:4096\n\t" "ds_read_b64_tr_b16 v[252:253], v25 offset:0\n\t" \
   "ds_read_b64_tr_b16 v[254:255], v25 offset:4096\n\t" "s_waitcnt vmcnt(0) lgkmcnt(0)\n\t" "s_barrier"
#define LDP_CLOB "v24", "v25", "v26", "v32", "v33", "v34", "v35", "v36", "v37", "v38", "v39", "v40", "v41", "v42", "v43", "v44", "v45", "v46", "v47", "v48", "v49", "v50", "v51", "v52", "v53", "v54", "v55", "v56", "v57", "v58", "v59", "v60", "v61", "v62", "v63", "v64", "v65", "v66", "v67", "v68", "v69", "v70", "v71", "v72", "v73", "v74", "v75", "v76", "v77", "v78", "v79", "v80", "v81", "v82", "v83", "v84", "v85", "v86", "v87", "v88", "v89", "v90", "v91", "v92", "v93", "v94", "v95", "v96", "v97", "v98", "v99", "v100", "v101", "v102", "v103", "v104", "v105", "v106", "v107", "v108", "v109", "v110", "v111", "v120", "v121", "v122", "v123", "s40", "s41"
#define LDK_ASM \
   "buffer_load_dword v26, v113, %[rw], 0 offen sc1\n\t" "s_mov_b32 s41, 0\n\t" "buffer_load_dwordx4 v[32:35], v116, %[rx], s41 offen\n\t" \
   "s_mov_b32 s41, 10240\n\t" "buffer_load_dwordx4 v[36:39], v116, %[rx], s41 offen\n\t" "s_mov_b32 s41, 128\n\t" \
   "buffer_load_dwordx4 v[40:43], v116, %[rx], s41 offen\n\t" "s_mov_b32 s41, 10368\n\t" "buffer_load_dwordx4 v[44:47], v116, %[rx], s41 offen\n\t" \
   "s_mov_b32 s41, 256\n\t" "buffer_load_dwordx4 v[48:51], v116, %[rx], s41 offen\n\t" "s_mov_b32 s41, 10496\n\t" \
   "buffer_load_dwordx4 v[52:55], v116, %[rx], s41 offen\n\t" "s_mov_b32 s41, 384\n\t" "buffer_load_dwordx4 v[56:59], v116, %[rx], s41 offen\n\t" \
   "s_mov_b32 s41, 10624\n\t" "buffer_load_dwordx4 v[60:63], v116, %[rx], s41 offen\n\t" "s_mov_b32 s41, 512\n\t" \
   "buffer_load_dwordx4 v[64:67], v116, %[rx], s41 offen\n\t" "s_mov_b32 s41, 10752\n\t" "buffer_load_dwordx4 v[68:71], v116, %[rx], s41 offen\n\t" \
   "s_mov_b32 s41, 640\n\t" "buffer_load_dwordx4 v[72:75], v116, %[rx], s41 offen\n\t" "s_mov_b32 s41, 10880\n\t" \
   "buffer_load_dwordx4 v[76:79], v116, %[rx], s41 offen\n\t" "s_mov_b32 s41, 768\n\t" "buffer_load_dwordx4 v[80:83], v116, %[rx], s41 offen\n\t" \
   "s_mov_b32 s41, 11008\n\t" "buffer_load_dwordx4 v[84:87], v116, %[rx], s41 offen\n\t" "s_mov_b32 s41, 896\n\t" \
   "buffer_load_dwordx4 v[88:91], v116, %[rx], s41 offen\n\t" "s_mov_b32 s41, 11136\n\t" "buffer_load_dwordx4 v[92:95], v116, %[rx], s41 offen\n\t" \
   "s_mov_b32 s40, 0x3f803f80\n\t" "v_mov_b32 v120, 0\n\t" "v_mov_b32 v121, 0\n\t" "v_mov_b32 v122, 0\n\t" "v_mov_b32 v123, 0\n\t" \
   "s_waitcnt vmcnt(16)\n\t" "v_add_u32 v26, 1, v26\n\t" "buffer_store_dword v26, v114, %[rw], 0 offen sc1\n\t" "ds_write_b32 v115, v26\n\t" LDSTAMP(0) \
   "s_waitcnt vmcnt(15)\n\t" LDSTAMP(1) "ds_write_b64 v117, v[32:33] offset:0\n\t" "ds_write_b64 v117, v[34:35] offset:256\n\t" \
   "s_mov_b32 s41, 1024\n\t" "buffer_load_dwordx4 v[96:99], v116, %[rx], s41 offen\n\t" "v_dot2c_f32_bf16 v120, s40, v32\n\t" \
   "v_dot2c_f32_bf16 v121, v32, v32\n\t" "v_dot2c_f32_bf16 v120, s40, v33\n\t" "v_dot2c_f32_bf16 v121, v33, v33\n\t" \
   "v_dot2c_f32_bf16 v120, s40, v34\n\t" "v_dot2c_f32_bf16 v121, v34, v34\n\t" "v_dot2c_f32_bf16 v120, s40, v35\n\t" \
   "v_dot2c_f32_bf16 v121, v35, v35\n\t" "s_waitcnt vmcnt(15)\n\t" "ds_write_b64 v117, v[36:37] offset:128\n\t" \
   "ds_write_b64 v117, v[38:39] offset:384\n\t" "s_mov_b32 s41, 11264\n\t" "buffer_load_dwordx4 v[100:103], v116, %[rx], s41 offen\n\t" \
   "v_dot2c_f32_bf16 v122, s40, v36\n\t" "v_dot2c_f32_bf16 v123, v36, v36\n\t" "v_dot2c_f32_bf16 v122, s40, v37\n\t" \
   "v_dot2c_f32_bf16 v123, v37, v37\n\t" "v_dot2c_f32_bf16 v122, s40, v38\n\t" "v_dot2c_f32_bf16 v123, v38, v38\n\t" \
   "v_dot2c_f32_bf16 v122, s40, v39\n\t" "v_dot2c_f32_bf16 v123, v39, v39\n\t" "s_waitcnt vmcnt(15)\n\t" "ds_write_b64 v117, v[40:41] offset:2048\n\t" \
   "ds_write_b64 v117, v[42:43] offset:2304\n\t" "s_mov_b32 s41, 1152\n\t" "buffer_load_dwordx4 v[104:107], v116, %[rx], s41 offen\n\t" \
   "v_dot2c_f32_bf16 v120, s40, v40\n\t" "v_dot2c_f32_bf16 v121, v40, v40\n\t" "v_dot2c_f32_bf16 v120, s40, v41\n\t" \
   "v_dot2c_f32_bf16 v121, v41, v41\n\t" "v_dot2c_f32_bf16 v120, s40, v42\n\t" "v_dot2c_f32_bf16 v121, v42, v42\n\t" \
   "v_dot2c_f32_bf16 v120, s40, v43\n\t" "v_dot2c_f32_bf16 v121, v43, v43\n\t" "s_waitcnt vmcnt(15)\n\t" "ds_write_b64 v117, v[44:45] offset:2176\n\t" \
   "ds_write_b64 v117, v[46:47] offset:2432\n\t" "s_mov_b32 s41, 11392\n\t" "buffer_load_dwordx4 v[108:111], v116, %[rx], s41 offen\n\t" \
   "v_dot2c_f32_bf16 v122, s40, v44\n\t" "v_dot2c_f32_bf16 v123, v44, v44\n\t" "v_dot2c_f32_bf16 v122, s40, v45\n\t" \
   "v_dot2c_f32_bf16 v123, v45, v45\n\t" "v_dot2c_f32_bf16 v122, s40, v46\n\t" "v_dot2c_f32_bf16 v123, v46, v46\n\t" \
   "v_dot2c_f32_bf16 v122, s40, v47\n\t" "v_dot2c_f32_bf16 v123, v47, v47\n\t" "s_waitcnt vmcnt(15)\n\t" "ds_write_b64 v117, v[48:49] offset:4096\n\t" \
   "ds_write_b64 v117, v[50:51] offset:4352\n\t" "s_mov_b32 s41, 0\n\t" "buffer_load_dwordx4 a[0:3], v16, %[ra], s41 offen\n\t" \
   "v_dot2c_f32_bf16 v120, s40, v48\n\t" "v_dot2c_f32_bf16 v121, v48, v48\n\t" "v_dot2c_f32_bf16 v120, s40, v49\n\t" \
   "v_dot2c_f32_bf16 v121, v49, v49\n\t" "v_dot2c_f32_bf16 v120, s40, v50\n\t" "v_dot2c_f32_bf16 v121, v50, v50\n\t" \
   "v_dot2c_f32_bf16 v120, s40, v51\n\t" "v_dot2c_f32_bf16 v121, v51, v51\n\t" "s_waitcnt vmcnt(15)\n\t" "ds_write_b64 v117, v[52:53] offset:4224\n\t" \
   "ds_write_b64 v117, v[54:55] offset:4480\n\t" "s_mov_b32 s41, 131072\n\t" "buffer_load_dwordx4 a[4:7], v16, %[ra], s41 offen\n\t" \
   "v_dot2c_f32_bf16 v122, s40, v52\n\t" "v_dot2c_f32_bf16 v123, v52, v52\n\t" "v_dot2c_f32_bf16 v122, s40, v53\n\t" \
   "v_dot2c_f32_bf16 v123, v53, v53\n\t" "v_dot2c_f32_bf16 v122, s40, v54\n\t" "v_dot2c_f32_bf16 v123, v54, v54\n\t" \
   "v_dot2c_f32_bf16 v122, s40, v55\n\t" "v_dot2c_f32_bf16 v123, v55, v55\n\t" "s_waitcnt vmcnt(15)\n\t" "ds_write_b64 v117, v[56:57] offset:6144\n\t" \
   "ds_write_b64 v117, v[58:59] offset:6400\n\t" "s_mov_b32 s41, 262144\n\t" "buffer_load_dwordx4 a[16:19], v16, %[ra], s41 offen\n\t" \
   "v_dot2c_f32_bf16 v120, s40, v56\n\t" "v_dot2c_f32_bf16 v121, v56, v56\n\t" "v_dot2c_f32_bf16 v120, s40, v57\n\t" \
   "v_dot2c_f32_bf16 v121, v57, v57\n\t" "v_dot2c_f32_bf16 v120, s40, v58\n\t" "v_dot2c_f32_bf16 v121, v58, v58\n\t" \
   "v_dot2c_f32_bf16 v120, s40, v59\n\t" "v_dot2c_f32_bf16 v121, v59, v59\n\t" "s_waitcnt vmcnt(15)\n\t" "ds_write_b64 v117, v[60:61] offset:6272\n\t" \
   "ds_write_b64 v117, v[62:63] offset:6528\n\t" "s_mov_b32 s41, 393216\n\t" "buffer_load_dwordx4 a[20:23], v16, %[ra], s41 offen\n\t" \
   "v_dot2c_f32_bf16 v122, s40, v60\n\t" "v_dot2c_f32_bf16 v123, v60, v60\n\t" "v_dot2c_f32_bf16 v122, s40, v61\n\t" \
   "v_dot2c_f32_bf16 v123, v61, v61\n\t" "v_dot2c_f32_bf16 v122, s40, v62\n\t" "v_dot2c_f32_bf16 v123, v62, v62\n\t" \
   "v_dot2c_f32_bf16 v122, s40, v63\n\t" "v_dot2c_f32_bf16 v123, v63, v63\n\t" "s_waitcnt vmcnt(15)\n\t" "ds_write_b64 v117, v[64:65] offset:8192\n\t" \
   "ds_write_b64 v117, v[66:67] offset:8448\n\t" "s_mov_b32 s41, 524288\n\t" "buffer_load_dwordx4 a[32:35], v16, %[ra], s41 offen\n\t" \
   "v_dot2c_f32_bf16 v120, s40, v64\n\t" "v_dot2c_f32_bf16 v121, v64, v64\n\t" "v_dot2c_f32_bf16 v120, s40, v65\n\t" \
   "v_dot2c_f32_bf16 v121, v65, v65\n\t" "v_dot2c_f32_bf16 v120, s40, v66\n\t" "v_dot2c_f32_bf16 v121, v66, v66\n\t" \
   "v_dot2c_f32_bf16 v120, s40, v67\n\t" "v_dot2c_f32_bf16 v121, v67, v67\n\t" "s_waitcnt vmcnt(15)\n\t" "ds_write_b64 v117, v[68:69] offset:8320\n\t" \
   "ds_write_b64 v117, v[70:71] offset:8576\n\t" "s_mov_b32 s41, 655360\n\t" "buffer_load_dwordx4 a[36:39], v16, %[ra], s41 offen\n\t" \
   "v_dot2c_f32_bf16 v122, s40, v68\n\t" "v_dot2c_f32_bf16 v123, v68, v68\n\t" "v_dot2c_f32_bf16 v122, s40, v69\n\t" \
   "v_dot2c_f32_bf16 v123, v69, v69\n\t" "v_dot2c_f32_bf16 v122, s40, v70\n\t" "v_dot2c_f32_bf16 v123, v70, v70\n\t" \
   "v_dot2c_f32_bf16 v122, s40, v71\n\t" "v_dot2c_f32_bf16 v123, v71, v71\n\t" "s_waitcnt vmcnt(15)\n\t" "ds_write_b64 v117, v[72:73] offset:10240\n\t" \
   "ds_write_b64 v117, v[74:75] offset:10496\n\t" "s_mov_b32 s41, 786432\n\t" "buffer_load_dwordx4 a[48:51], v16, %[ra], s41 offen\n\t" \
   "v_dot2c_f32_bf16 v120, s40, v72\n\t" "v_dot2c_f32_bf16 v121, v72, v72\n\t" "v_dot2c_f32_bf16 v120, s40, v73\n\t" \
   "v_dot2c_f32_bf16 v121, v73, v73\n\t" "v_dot2c_f32_bf16 v120, s40, v74\n\t" "v_dot2c_f32_bf16 v121, v74, v74\n\t" \
   "v_dot2c_f32_bf16 v120, s40, v75\n\t" "v_dot2c_f32_bf16 v121, v75, v75\n\t" "s_waitcnt vmcnt(15)\n\t" "ds_write_b64 v117, v[76:77] offset:10368\n\t" \
   "ds_write_b64 v117, v[78:79] offset:10624\n\t" "s_mov_b32 s41, 917504\n\t" "buffer_load_dwordx4 a[52:55], v16, %[ra], s41 offen\n\t" \
   "v_dot2c_f32_bf16 v122, s40, v76\n\t" "v_dot2c_f32_bf16 v123, v76, v76\n\t" "v_dot2c_f32_bf16 v122, s40, v77\n\t" \
   "v_dot2c_f32_bf16 v123, v77, v77\n\t" "v_dot2c_f32_bf16 v122, s40, v78\n\t" "v_dot2c_f32_bf16 v123, v78, v78\n\t" \
   "v_dot2c_f32_bf16 v122, s40, v79\n\t" "v_dot2c_f32_bf16 v123, v79, v79\n\t" "s_waitcnt vmcnt(15)\n\t" "ds_write_b64 v117, v[80:81] offset:12288\n\t" \
   "ds_write_b64 v117, v[82:83] offset:12544\n\t" "s_mov_b32 s41, 1048576\n\t" "buffer_load_dwordx4 a[64:67], v16, %[ra], s41 offen\n\t" \
   "v_dot2c_f32_bf16 v120, s40, v80\n\t" "v_dot2c_f32_bf16 v121, v80, v80\n\t" "v_dot2c_f32_bf16 v120, s40, v81\n\t" \
   "v_dot2c_f32_bf16 v121, v81, v81\n\t" "v_dot2c_f32_bf16 v120, s40, v82\n\t" "v_dot2c_f32_bf16 v121, v82, v82\n\t" \
   "v_dot2c_f32_bf16 v120, s40, v83\n\t" "v_dot2c_f32_bf16 v121, v83, v83\n\t" "s_waitcnt vmcnt(15)\n\t" "ds_write_b64 v117, v[84:85] offset:12416\n\t" \
   "ds_write_b64 v117, v[86:87] offset:12672\n\t" "s_mov_b32 s41, 1179648\n\t" "buffer_load_dwordx4 a[68:71], v16, %[ra], s41 offen\n\t" \
   "v_dot2c_f32_bf16 v122, s40, v84\n\t" "v_dot2c_f32_bf16 v123, v84, v84\n\t" "v_dot2c_f32_bf16 v122, s40, v85\n\t" \
   "v_dot2c_f32_bf16 v123, v85, v85\n\t" "v_dot2c_f32_bf16 v122, s40, v86\n\t" "v_dot2c_f32_bf16 v123, v86, v86\n\t" \
   "v_dot2c_f32_bf16 v122, s40, v87\n\t" "v_dot2c_f32_bf16 v123, v87, v87\n\t" "s_waitcnt vmcnt(15)\n\t" "ds_write_b64 v117, v[88:89] offset:14336\n\t" \
   "ds_write_b64 v117, v[90:91] offset:14592\n\t" "s_mov_b32 s41, 0\n\t" "buffer_load_dwordx4 a[8:11], v16, %[rb], s41 offen\n\t" \
   "v_dot2c_f32_bf16 v120, s40, v88\n\t" "v_dot2c_f32_bf16 v121, v88, v88\n\t" "v_dot2c_f32_bf16 v120, s40, v89\n\t" \
   "v_dot2c_f32_bf16 v121, v89, v89\n\t" "v_dot2c_f32_bf16 v120, s40, v90\n\t" "v_dot2c_f32_bf16 v121, v90, v90\n\t" \
   "v_dot2c_f32_bf16 v120, s40, v91\n\t" "v_dot2c_f32_bf16 v121, v91, v91\n\t" "s_waitcnt vmcnt(15)\n\t" "ds_write_b64 v117, v[92:93] offset:14464\n\t" \
   "ds_write_b64 v117, v[94:95] offset:14720\n\t" "s_mov_b32 s41, 131072\n\t" "buffer_load_dwordx4 a[12:15], v16, %[rb], s41 offen\n\t" \
   "v_dot2c_f32_bf16 v122, s40, v92\n\t" "v_dot2c_f32_bf16 v123, v92, v92\n\t" "v_dot2c_f32_bf16 v122, s40, v93\n\t" \
   "v_dot2c_f32_bf16 v123, v93, v93\n\t" "v_dot2c_f32_bf16 v122, s40, v94\n\t" "v_dot2c_f32_bf16 v123, v94, v94\n\t" \
   "v_dot2c_f32_bf16 v122, s40, v95\n\t" "v_dot2c_f32_bf16 v123, v95, v95\n\t" "s_waitcnt vmcnt(15)\n\t" "ds_write_b64 v117, v[96:97] offset:16384\n\t" \
   "ds_write_b64 v117, v[98:99] offset:16640\n\t" "s_mov_b32 s41, 262144\n\t" "buffer_load_dwordx4 a[24:27], v16, %[rb], s41 offen\n\t" \
   "v_dot2c_f32_bf16 v120, s40, v96\n\t" "v_dot2c_f32_bf16 v121, v96, v96\n\t" "v_dot2c_f32_bf16 v120, s40, v97\n\t" \
   "v_dot2c_f32_bf16 v121, v97, v97\n\t" "v_dot2c_f32_bf16 v120, s40, v98\n\t" "v_dot2c_f32_bf16 v121, v98, v98\n\t" \
   "v_dot2c_f32_bf16 v120, s40, v99\n\t" "v_dot2c_f32_bf16 v121, v99, v99\n\t" "s_waitcnt vmcnt(15)\n\t" \
   "ds_write_b64 v117, v[100:101] offset:16512\n\t" "ds_write_b64 v117, v[102:103] offset:16768\n\t" "s_mov_b32 s41, 393216\n\t" \
   "buffer_load_dwordx4 a[28:31], v16, %[rb], s41 offen\n\t" "v_dot2c_f32_bf16 v122, s40, v100\n\t" "v_dot2c_f32_bf16 v123, v100, v100\n\t" \
   "v_dot2c_f32_bf16 v122, s40, v101\n\t" "v_dot2c_f32_bf16 v123, v101, v101\n\t" "v_dot2c_f32_bf16 v122, s40, v102\n\t" \
   "v_dot2c_f32_bf16 v123, v102, v102\n\t" "v_dot2c_f32_bf16 v122, s40, v103\n\t" "v_dot2c_f32_bf16 v123, v103, v103\n\t" "s_waitcnt vmcnt(15)\n\t" \
   "ds_write_b64 v117, v[104:105] offset:18432\n\t" "ds_write_b64 v117, v[106:107] offset:18688\n\t" "s_mov_b32 s41, 524288\n\t" \
   "buffer_load_dwordx4 a[40:43], v16, %[rb], s41 offen\n\t" "v_dot2c_f32_bf16 v120, s40, v104\n\t" "v_dot2c_f32_bf16 v121, v104, v104\n\t" \
   "v_dot2c_f32_bf16 v120, s40, v105\n\t" "v_dot2c_f32_bf16 v121, v105, v105\n\t" "v_dot2c_f32_bf16 v120, s40, v106\n\t" \
   "v_dot2c_f32_bf16 v121, v106, v106\n\t" "v_dot2c_f32_bf16 v120, s40, v107\n\t" "v_dot2c_f32_bf16 v121, v107, v107\n\t" "s_waitcnt vmcnt(15)\n\t" \
   "ds_write_b64 v117, v[108:109] offset:18560\n\t" "ds_write_b64 v117, v[110:111] offset:18816\n\t" "s_mov_b32 s41, 655360\n\t" \
   "buffer_load_dwordx4 a[44:47], v16, %[rb], s41 offen\n\t" "v_dot2c_f32_bf16 v122, s40, v108\n\t" "v_dot2c_f32_bf16 v123, v108, v108\n\t" \
   "v_dot2c_f32_bf16 v122, s40, v109\n\t" "v_dot2c_f32_bf16 v123, v109, v109\n\t" "v_dot2c_f32_bf16 v122, s40, v110\n\t" \
   "v_dot2c_f32_bf16 v123, v110, v110\n\t" "v_dot2c_f32_bf16 v122, s40, v111\n\t" "v_dot2c_f32_bf16 v123, v111, v111\n\t" "s_nop 3\n\t" \
   "v_add_f32_dpp v120, v120, v120 quad_perm:[1,0,3,2] row_mask:0xf bank_mask:0xf\n\t" \
   "v_add_f32_dpp v121, v121, v121 quad_perm:[1,0,3,2] row_mask:0xf bank_mask:0xf\n\t" \
   "v_add_f32_dpp v122, v122, v122 quad_perm:[1,0,3,2] row_mask:0xf bank_mask:0xf\n\t" \
   "v_add_f32_dpp v123, v123, v123 quad_perm:[1,0,3,2] row_mask:0xf bank_mask:0xf\n\t" \
   "v_add_f32_dpp v120, v120, v120 quad_perm:[2,3,0,1] row_mask:0xf bank_mask:0xf\n\t" \
   "v_add_f32_dpp v121, v121, v121 quad_perm:[2,3,0,1] row_mask:0xf bank_mask:0xf\n\t" \
   "v_add_f32_dpp v122, v122, v122 quad_perm:[2,3,0,1] row_mask:0xf bank_mask:0xf\n\t" \
   "v_add_f32_dpp v123, v123, v123 quad_perm:[2,3,0,1] row_mask:0xf bank_mask:0xf\n\t" \
   "v_add_f32_dpp v120, v120, v120 row_half_mirror row_mask:0xf bank_mask:0xf\n\t" \
   "v_add_f32_dpp v121, v121, v121 row_half_mirror row_mask:0xf bank_mask:0xf\n\t" \
   "v_add_f32_dpp v122, v122, v122 row_half_mirror row_mask:0xf bank_mask:0xf\n\t" \
   "v_add_f32_dpp v123, v123, v123 row_half_mirror row_mask:0xf bank_mask:0xf\n\t" "v_and_b32 v120, v119, v120\n\t" "v_and_b32 v121, v119, v121\n\t" \
   "v_and_b32 v122, v119, v122\n\t" "v_and_b32 v123, v119, v123\n\t" "ds_write_b64 v118, v[120:121]\n\t" "ds_write_b64 v118, v[122:123] offset:384\n\t" \
   "s_waitcnt vmcnt(15)\n\t" "ds_write_b128 v20, a[0:3] offset:0\n\t" "s_mov_b32 s41, 786432\n\t" \
   "buffer_load_dwordx4 a[56:59], v16, %[rb], s41 offen\n\t" "s_waitcnt vmcnt(15)\n\t" "ds_write_b128 v20, a[4:7] offset:4096\n\t" \
   "s_mov_b32 s41, 917504\n\t" "buffer_load_dwordx4 a[60:63], v16, %[rb], s41 offen\n\t" "s_waitcnt vmcnt(15)\n\t" \
   "ds_write_b128 v20, a[16:19] offset:8192\n\t" "s_mov_b32 s41, 1048576\n\t" "buffer_load_dwordx4 a[72:75], v16, %[rb], s41 offen\n\t" \
   "s_waitcnt vmcnt(15)\n\t" "ds_write_b128 v20, a[20:23] offset:12288\n\t" "s_mov_b32 s41, 1179648\n\t" \
   "buffer_load_dwordx4 a[76:79], v16, %[rb], s41 offen\n\t" "s_waitcnt vmcnt(15)\n\t" "ds_write_b128 v20, a[32:35] offset:16384\n\t" \
   "s_mov_b32 s41, 1310720\n\t" "buffer_load_dwordx4 a[80:83], v16, %[ra], s41 offen\n\t" "s_waitcnt vmcnt(15)\n\t" \
   "ds_write_b128 v20, a[36:39] offset:20480\n\t" "s_mov_b32 s41, 1441792\n\t" "buffer_load_dwordx4 a[84:87], v16, %[ra], s41 offen\n\t" \
   "s_waitcnt vmcnt(15)\n\t" "ds_write_b128 v20, a[48:51] offset:24576\n\t" "s_mov_b32 s41, 1572864\n\t" \
   "buffer_load_dwordx4 a[96:99], v16, %[ra], s41 offen\n\t" "s_waitcnt vmcnt(15)\n\t" "ds_write_b128 v20, a[52:55] offset:28672\n\t" \
   "s_mov_b32 s41, 1703936\n\t" "buffer_load_dwordx4 a[100:103], v16, %[ra], s41 offen\n\t" "s_waitcnt vmcnt(15)\n\t" \
   "ds_write_b128 v20, a[64:67] offset:32768\n\t" "s_mov_b32 s41, 1835008\n\t" "buffer_load_dwordx4 a[112:115], v16, %[ra], s41 offen\n\t" \
   "s_waitcnt vmcnt(15)\n\t" "ds_write_b128 v20, a[68:71] offset:36864\n\t" "s_mov_b32 s41, 1966080\n\t" \
   "buffer_load_dwordx4 a[116:119], v16, %[ra], s41 offen\n\t" "s_waitcnt lgkmcnt(0)\n\t" "s_barrier\n\t" LDSTAMP(2) \
   "ds_read_b64_tr_b16 a[0:1], v18 offset:0\n\t" "ds_read_b64_tr_b16 a[2:3], v18 offset:4096\n\t" "ds_read_b64_tr_b16 a[4:5], v19 offset:0\n\t" \
   "ds_read_b64_tr_b16 a[6:7], v19 offset:4096\n\t" "ds_read_b64_tr_b16 a[16:17], v18 offset:8192\n\t" \
   "ds_read_b64_tr_b16 a[18:19], v18 offset:12288\n\t" "ds_read_b64_tr_b16 a[20:21], v19 offset:8192\n\t" \
   "ds_read_b64_tr_b16 a[22:23], v19 offset:12288\n\t" "ds_read_b64_tr_b16 a[32:33], v18 offset:16384\n\t" \
   "ds_read_b64_tr_b16 a[34:35], v18 offset:20480\n\t" "ds_read_b64_tr_b16 a[36:37], v19 offset:16384\n\t" \
   "ds_read_b64_tr_b16 a[38:39], v19 offset:20480\n\t" "ds_read_b64_tr_b16 a[48:49], v18 offset:24576\n\t" \
   "ds_read_b64_tr_b16 a[50:51], v18 offset:28672\n\t" "ds_read_b64_tr_b16 a[52:53], v19 offset:24576\n\t" \
   "ds_read_b64_tr_b16 a[54:55], v19 offset:28672\n\t" "ds_read_b64_tr_b16 a[64:65], v18 offset:32768\n\t" \
   "ds_read_b64_tr_b16 a[66:67], v18 offset:36864\n\t" "ds_read_b64_tr_b16 a[68:69], v19 offset:32768\n\t" \
   "ds_read_b64_tr_b16 a[70:71], v19 offset:36864\n\t" "s_waitcnt vmcnt(15)\n\t" "s_waitcnt lgkmcnt(0)\n\t" "s_barrier\n\t" \
   "v_mov_b32 v64, 0x3f803f80\n\t" "v_mov_b32 v65, 0x3f803f80\n\t" "v_mov_b32 v66, 0x3f803f80\n\t" "v_mov_b32 v67, 0x3f803f80\n\t" \
   "ds_write_b128 v20, a[8:11] offset:0\n\t" "s_mov_b32 s41, 2097152\n\t" "buffer_load_dwordx4 a[128:131], v16, %[ra], s41 offen\n\t" \
   "v_mfma_f32_32x32x16_bf16 v[32:47], a[0:3], v[64:67], 0\n\t" "s_waitcnt vmcnt(15)\n\t" "ds_write_b128 v20, a[12:15] offset:4096\n\t" \
   "s_mov_b32 s41, 2228224\n\t" "buffer_load_dwordx4 a[132:135], v16, %[ra], s41 offen\n\t" \
   "v_mfma_f32_32x32x16_bf16 v[32:47], a[4:7], v[64:67], v[32:47]\n\t" "s_waitcnt vmcnt(15)\n\t" "ds_write_b128 v20, a[24:27] offset:8192\n\t" \
   "s_mov_b32 s41, 2359296\n\t" "buffer_load_dwordx4 a[144:147], v16, %[ra], s41 offen\n\t" \
   "v_mfma_f32_32x32x16_bf16 v[32:47], a[16:19], v[64:67], v[32:47]\n\t" "s_waitcnt vmcnt(15)\n\t" "ds_write_b128 v20, a[28:31] offset:12288\n\t" \
   "s_mov_b32 s41, 2490368\n\t" "buffer_load_dwordx4 a[148:151], v16, %[ra], s41 offen\n\t" \
   "v_mfma_f32_32x32x16_bf16 v[32:47], a[20:23], v[64:67], v[32:47]\n\t" "s_waitcnt vmcnt(15)\n\t" "ds_write_b128 v20, a[40:43] offset:16384\n\t" \
   "s_mov_b32 s41, 1310720\n\t" "buffer_load_dwordx4 a[88:91], v16, %[rb], s41 offen\n\t" \
   "v_mfma_f32_32x32x16_bf16 v[32:47], a[32:35], v[64:67], v[32:47]\n\t" "s_waitcnt vmcnt(15)\n\t" "ds_write_b128 v20, a[44:47] offset:20480\n\t" \
   "s_mov_b32 s41, 1441792\n\t" "buffer_load_dwordx4 a[92:95], v16, %[rb], s41 offen\n\t" \
   "v_mfma_f32_32x32x16_bf16 v[32:47], a[36:39], v[64:67], v[32:47]\n\t" "s_waitcnt vmcnt(15)\n\t" "ds_write_b128 v20, a[56:59] offset:24576\n\t" \
   "s_mov_b32 s41, 1572864\n\t" "buffer_load_dwordx4 a[104:107], v16, %[rb], s41 offen\n\t" \
   "v_mfma_f32_32x32x16_bf16 v[32:47], a[48:51], v[64:67], v[32:47]\n\t" "s_waitcnt vmcnt(15)\n\t" "ds_write_b128 v20, a[60:63] offset:28672\n\t" \
   "s_mov_b32 s41, 1703936\n\t" "buffer_load_dwordx4 a[108:111], v16, %[rb], s41 offen\n\t" \
   "v_mfma_f32_32x32x16_bf16 v[32:47], a[52:55], v[64:67], v[32:47]\n\t" "s_waitcnt vmcnt(15)\n\t" "ds_write_b128 v20, a[72:75] offset:32768\n\t" \
   "s_mov_b32 s41, 1835008\n\t" "buffer_load_dwordx4 a[120:123], v16, %[rb], s41 offen\n\t" \
   "v_mfma_f32_32x32x16_bf16 v[32:47], a[64:67], v[64:67], v[32:47]\n\t" "s_waitcnt vmcnt(15)\n\t" "ds_write_b128 v20, a[76:79] offset:36864\n\t" \
   "s_mov_b32 s41, 1966080\n\t" "buffer_load_dwordx4 a[124:127], v16, %[rb], s41 offen\n\t" \
   "v_mfma_f32_32x32x16_bf16 v[32:47], a[68:71], v[64:67], v[32:47]\n\t" "s_waitcnt lgkmcnt(0)\n\t" "s_barrier\n\t" LDSTAMP(3) \
   "ds_read_b64_tr_b16 a[8:9], v18 offset:0\n\t" "ds_read_b64_tr_b16 a[10:11], v18 offset:4096\n\t" "ds_read_b64_tr_b16 a[12:13], v19 offset:0\n\t" \
   "ds_read_b64_tr_b16 a[14:15], v19 offset:4096\n\t" "ds_read_b64_tr_b16 a[24:25], v18 offset:8192\n\t" \
   "ds_read_b64_tr_b16 a[26:27], v18 offset:12288\n\t" "ds_read_b64_tr_b16 a[28:29], v19 offset:8192\n\t" \
   "ds_read_b64_tr_b16 a[30:31], v19 offset:12288\n\t" "ds_read_b64_tr_b16 a[40:41], v18 offset:16384\n\t" \
   "ds_read_b64_tr_b16 a[42:43], v18 offset:20480\n\t" "ds_read_b64_tr_b16 a[44:45], v19 offset:16384\n\t" \
   "ds_read_b64_tr_b16 a[46:47], v19 offset:20480\n\t" "ds_read_b64_tr_b16 a[56:57], v18 offset:24576\n\t" \
   "ds_read_b64_tr_b16 a[58:59], v18 offset:28672\n\t" "ds_read_b64_tr_b16 a[60:61], v19 offset:24576\n\t" \
   "ds_read_b64_tr_b16 a[62:63], v19 offset:28672\n\t" "ds_read_b64_tr_b16 a[72:73], v18 offset:32768\n\t" \
   "ds_read_b64_tr_b16 a[74:75], v18 offset:36864\n\t" "ds_read_b64_tr_b16 a[76:77], v19 offset:32768\n\t" \
   "ds_read_b64_tr_b16 a[78:79], v19 offset:36864\n\t" "s_waitcnt vmcnt(15)\n\t" "s_waitcnt lgkmcnt(0)\n\t" "s_barrier\n\t" \
   "ds_write_b128 v20, a[80:83] offset:0\n\t" "s_mov_b32 s41, 2097152\n\t" "buffer_load_dwordx4 a[136:139], v16, %[rb], s41 offen\n\t" \
   "v_mfma_f32_32x32x16_bf16 v[48:63], a[8:11], v[64:67], 0\n\t" "s_waitcnt vmcnt(15)\n\t" "ds_write_b128 v20, a[84:87] offset:4096\n\t" \
   "s_mov_b32 s41, 2228224\n\t" "buffer_load_dwordx4 a[140:143], v16, %[rb], s41 offen\n\t" \
   "v_mfma_f32_32x32x16_bf16 v[48:63], a[12:15], v[64:67], v[48:63]\n\t" "s_waitcnt vmcnt(15)\n\t" "ds_write_b128 v20, a[96:99] offset:8192\n\t" \
   "s_mov_b32 s41, 2359296\n\t" "buffer_load_dwordx4 a[152:155], v16, %[rb], s41 offen\n\t" \
   "v_mfma_f32_32x32x16_bf16 v[48:63], a[24:27], v[64:67], v[48:63]\n\t" "s_waitcnt vmcnt(15)\n\t" "ds_write_b128 v20, a[100:103] offset:12288\n\t" \
   "s_mov_b32 s41, 2490368\n\t" "buffer_load_dwordx4 a[156:159], v16, %[rb], s41 offen\n\t" \
   "v_mfma_f32_32x32x16_bf16 v[48:63], a[28:31], v[64:67], v[48:63]\n\t" "s_waitcnt vmcnt(15)\n\t" "ds_write_b128 v20, a[112:115] offset:16384\n\t" \
   "s_mov_b32 s41, 2621440\n\t" "buffer_load_dwordx4 a[160:163], v16, %[ra], s41 offen\n\t" \
   "v_mfma_f32_32x32x16_bf16 v[48:63], a[40:43], v[64:67], v[48:63]\n\t" "s_waitcnt vmcnt(15)\n\t" "ds_write_b128 v20, a[116:119] offset:20480\n\t" \
   "s_mov_b32 s41, 2752512\n\t" "buffer_load_dwordx4 a[164:167], v16, %[ra], s41 offen\n\t" \
   "v_mfma_f32_32x32x16_bf16 v[48:63], a[44:47], v[64:67], v[48:63]\n\t" "s_waitcnt vmcnt(15)\n\t" "ds_write_b128 v20, a[128:131] offset:24576\n\t" \
   "s_mov_b32 s41, 2883584\n\t" "buffer_load_dwordx4 a[176:179], v16, %[ra], s41 offen\n\t" \
   "v_mfma_f32_32x32x16_bf16 v[48:63], a[56:59], v[64:67], v[48:63]\n\t" "s_waitcnt vmcnt(15)\n\t" "ds_write_b128 v20, a[132:135] offset:28672\n\t" \
   "s_mov_b32 s41, 3014656\n\t" "buffer_load_dwordx4 a[180:183], v16, %[ra], s41 offen\n\t" \
   "v_mfma_f32_32x32x16_bf16 v[48:63], a[60:63], v[64:67], v[48:63]\n\t" "s_waitcnt vmcnt(15)\n\t" "ds_write_b128 v20, a[144:147] offset:32768\n\t" \
   "s_mov_b32 s41, 3145728\n\t" "buffer_load_dwordx4 a[192:195], v16, %[ra], s41 offen\n\t" \
   "v_mfma_f32_32x32x16_bf16 v[48:63], a[72:75], v[64:67], v[48:63]\n\t" "s_waitcnt vmcnt(15)\n\t" "ds_write_b128 v20, a[148:151] offset:36864\n\t" \
   "s_mov_b32 s41, 3276800\n\t" "buffer_load_dwordx4 a[196:199], v16, %[ra], s41 offen\n\t" \
   "v_mfma_f32_32x32x16_bf16 v[48:63], a[76:79], v[64:67], v[48:63]\n\t" "s_waitcnt lgkmcnt(0)\n\t" "s_barrier\n\t" LDSTAMP(4) \
   "ds_read_b64_tr_b16 a[80:81], v18 offset:0\n\t" "ds_read_b64_tr_b16 a[82:83], v18 offset:4096\n\t" "ds_read_b64_tr_b16 a[84:85], v19 offset:0\n\t" \
   "ds_read_b64_tr_b16 a[86:87], v19 offset:4096\n\t" "ds_read_b64_tr_b16 a[96:97], v18 offset:8192\n\t" \
   "ds_read_b64_tr_b16 a[98:99], v18 offset:12288\n\t" "ds_read_b64_tr_b16 a[100:101], v19 offset:8192\n\t" \
   "ds_read_b64_tr_b16 a[102:103], v19 offset:12288\n\t" "ds_read_b64_tr_b16 a[112:113], v18 offset:16384\n\t" \
   "ds_read_b64_tr_b16 a[114:115], v18 offset:20480\n\t" "ds_read_b64_tr_b16 a[116:117], v19 offset:16384\n\t" \
   "ds_read_b64_tr_b16 a[118:119], v19 offset:20480\n\t" "ds_read_b64_tr_b16 a[128:129], v18 offset:24576\n\t" \
   "ds_read_b64_tr_b16 a[130:131], v18 offset:28672\n\t" "ds_read_b64_tr_b16 a[132:133], v19 offset:24576\n\t" \
   "ds_read_b64_tr_b16 a[134:135], v19 offset:28672\n\t" "ds_read_b64_tr_b16 a[144:145], v18 offset:32768\n\t" \
   "ds_read_b64_tr_b16 a[146:147], v18 offset:36864\n\t" "ds_read_b64_tr_b16 a[148:149], v19 offset:32768\n\t" \
   "ds_read_b64_tr_b16 a[150:151], v19 offset:36864\n\t" "s_waitcnt vmcnt(15)\n\t" "s_waitcnt lgkmcnt(0)\n\t" "s_barrier\n\t" \
   "ds_write_b128 v20, a[88:91] offset:0\n\t" "s_mov_b32 s41, 3407872\n\t" "buffer_load_dwordx4 a[208:211], v16, %[ra], s41 offen\n\t" \
   "v_mfma_f32_32x32x16_bf16 v[32:47], a[80:83], v[64:67], v[32:47]\n\t" "s_waitcnt vmcnt(15)\n\t" "ds_write_b128 v20, a[92:95] offset:4096\n\t" \
   "s_mov_b32 s41, 3538944\n\t" "buffer_load_dwordx4 a[212:215], v16, %[ra], s41 offen\n\t" \
   "v_mfma_f32_32x32x16_bf16 v[32:47], a[84:87], v[64:67], v[32:47]\n\t" "s_waitcnt vmcnt(15)\n\t" "ds_write_b128 v20, a[104:107] offset:8192\n\t" \
   "s_mov_b32 s41, 3670016\n\t" "buffer_load_dwordx4 a[224:227], v16, %[ra], s41 offen\n\t" \
   "v_mfma_f32_32x32x16_bf16 v[32:47], a[96:99], v[64:67], v[32:47]\n\t" "s_waitcnt vmcnt(15)\n\t" "ds_write_b128 v20, a[108:111] offset:12288\n\t" \
   "s_mov_b32 s41, 3801088\n\t" "buffer_load_dwordx4 a[228:231], v16, %[ra], s41 offen\n\t" \
   "v_mfma_f32_32x32x16_bf16 v[32:47], a[100:103], v[64:67], v[32:47]\n\t" "s_waitcnt vmcnt(15)\n\t" "ds_write_b128 v20, a[120:123] offset:16384\n\t" \
   "s_mov_b32 s41, 2621440\n\t" "buffer_load_dwordx4 a[168:171], v16, %[rb], s41 offen\n\t" \
   "v_mfma_f32_32x32x16_bf16 v[32:47], a[112:115], v[64:67], v[32:47]\n\t" "s_waitcnt vmcnt(15)\n\t" "ds_write_b128 v20, a[124:127] offset:20480\n\t" \
   "s_mov_b32 s41, 2752512\n\t" "buffer_load_dwordx4 a[172:175], v16, %[rb], s41 offen\n\t" \
   "v_mfma_f32_32x32x16_bf16 v[32:47], a[116:119], v[64:67], v[32:47]\n\t" "s_waitcnt vmcnt(15)\n\t" "ds_write_b128 v20, a[136:139] offset:24576\n\t" \
   "s_mov_b32 s41, 2883584\n\t" "buffer_load_dwordx4 a[184:187], v16, %[rb], s41 offen\n\t" \
   "v_mfma_f32_32x32x16_bf16 v[32:47], a[128:131], v[64:67], v[32:47]\n\t" "s_waitcnt vmcnt(15)\n\t" "ds_write_b128 v20, a[140:143] offset:28672\n\t" \
   "s_mov_b32 s41, 3014656\n\t" "buffer_load_dwordx4 a[188:191], v16, %[rb], s41 offen\n\t" \
   "v_mfma_f32_32x32x16_bf16 v[32:47], a[132:135], v[64:67], v[32:47]\n\t" "s_waitcnt vmcnt(15)\n\t" "ds_write_b128 v20, a[152:155] offset:32768\n\t" \
   "s_mov_b32 s41, 3145728\n\t" "buffer_load_dwordx4 a[200:203], v16, %[rb], s41 offen\n\t" \
   "v_mfma_f32_32x32x16_bf16 v[32:47], a[144:147], v[64:67], v[32:47]\n\t" "s_waitcnt vmcnt(15)\n\t" "ds_write_b128 v20, a[156:159] offset:36864\n\t" \
   "s_mov_b32 s41, 3276800\n\t" "buffer_load_dwordx4 a[204:207], v16, %[rb], s41 offen\n\t" \
   "v_mfma_f32_32x32x16_bf16 v[32:47], a[148:151], v[64:67], v[32:47]\n\t" "s_waitcnt lgkmcnt(0)\n\t" "s_barrier\n\t" LDSTAMP(5) \
   "ds_read_b64_tr_b16 a[88:89], v18 offset:0\n\t" "ds_read_b64_tr_b16 a[90:91], v18 offset:4096\n\t" "ds_read_b64_tr_b16 a[92:93], v19 offset:0\n\t" \
   "ds_read_b64_tr_b16 a[94:95], v19 offset:4096\n\t" "ds_read_b64_tr_b16 a[104:105], v18 offset:8192\n\t" \
   "ds_read_b64_tr_b16 a[106:107], v18 offset:12288\n\t" "ds_read_b64_tr_b16 a[108:109], v19 offset:8192\n\t" \
   "ds_read_b64_tr_b16 a[110:111], v19 offset:12288\n\t" "ds_read_b64_tr_b16 a[120:121], v18 offset:16384\n\t" \
   "ds_read_b64_tr_b16 a[122:123], v18 offset:20480\n\t" "ds_read_b64_tr_b16 a[124:125], v19 offset:16384\n\t" \
   "ds_read_b64_tr_b16 a[126:127], v19 offset:20480\n\t" "ds_read_b64_tr_b16 a[136:137], v18 offset:24576\n\t" \
   "ds_read_b64_tr_b16 a[138:139], v18 offset:28672\n\t" "ds_read_b64_tr_b16 a[140:141], v19 offset:24576\n\t" \
   "ds_read_b64_tr_b16 a[142:143], v19 offset:28672\n\t" "ds_read_b64_tr_b16 a[152:153], v18 offset:32768\n\t" \
   "ds_read_b64_tr_b16 a[154:155], v18 offset:36864\n\t" "ds_read_b64_tr_b16 a[156:157], v19 offset:32768\n\t" \
   "ds_read_b64_tr_b16 a[158:159], v19 offset:36864\n\t" "s_waitcnt vmcnt(15)\n\t" "s_waitcnt lgkmcnt(0)\n\t" "s_barrier\n\t" \
   "ds_write_b128 v20, a[160:163] offset:0\n\t" "s_mov_b32 s41, 3407872\n\t" "buffer_load_dwordx4 a[216:219], v16, %[rb], s41 offen\n\t" \
   "v_mfma_f32_32x32x16_bf16 v[48:63], a[88:91], v[64:67], v[48:63]\n\t" "s_waitcnt vmcnt(15)\n\t" "ds_write_b128 v20, a[164:167] offset:4096\n\t" \
   "s_mov_b32 s41, 3538944\n\t" "buffer_load_dwordx4 a[220:223], v16, %[rb], s41 offen\n\t" \
   "v_mfma_f32_32x32x16_bf16 v[48:63], a[92:95], v[64:67], v[48:63]\n\t" "s_waitcnt vmcnt(15)\n\t" "ds_write_b128 v20, a[176:179] offset:8192\n\t" \
   "s_mov_b32 s41, 3670016\n\t" "buffer_load_dwordx4 a[232:235], v16, %[rb], s41 offen\n\t" \
   "v_mfma_f32_32x32x16_bf16 v[48:63], a[104:107], v[64:67], v[48:63]\n\t" "s_waitcnt vmcnt(15)\n\t" "ds_write_b128 v20, a[180:183] offset:12288\n\t" \
   "s_mov_b32 s41, 3801088\n\t" "buffer_load_dwordx4 a[236:239], v16, %[rb], s41 offen\n\t" \
   "v_mfma_f32_32x32x16_bf16 v[48:63], a[108:111], v[64:67], v[48:63]\n\t" "s_waitcnt vmcnt(15)\n\t" "ds_write_b128 v20, a[192:195] offset:16384\n\t" \
   "s_mov_b32 s41, 3932160\n\t" "buffer_load_dwordx4 a[240:243], v16, %[ra], s41 offen\n\t" \
   "v_mfma_f32_32x32x16_bf16 v[48:63], a[120:123], v[64:67], v[48:63]\n\t" "s_waitcnt vmcnt(15)\n\t" "ds_write_b128 v20, a[196:199] offset:20480\n\t" \
   "s_mov_b32 s41, 4063232\n\t" "buffer_load_dwordx4 a[244:247], v16, %[ra], s41 offen\n\t" \
   "v_mfma_f32_32x32x16_bf16 v[48:63], a[124:127], v[64:67], v[48:63]\n\t" "s_waitcnt vmcnt(15)\n\t" "ds_write_b128 v20, a[208:211] offset:24576\n\t" \
   "s_mov_b32 s41, 4194304\n\t" "buffer_load_dwordx4 v[192:195], v16, %[ra], s41 offen\n\t" \
   "v_mfma_f32_32x32x16_bf16 v[48:63], a[136:139], v[64:67], v[48:63]\n\t" "s_waitcnt vmcnt(15)\n\t" "ds_write_b128 v20, a[212:215] offset:28672\n\t" \
   "s_mov_b32 s41, 4325376\n\t" "buffer_load_dwordx4 v[196:199], v16, %[ra], s41 offen\n\t" \
   "v_mfma_f32_32x32x16_bf16 v[48:63], a[140:143], v[64:67], v[48:63]\n\t" "s_waitcnt vmcnt(15)\n\t" "ds_write_b128 v20, a[224:227] offset:32768\n\t" \
   "s_mov_b32 s41, 4456448\n\t" "buffer_load_dwordx4 v[208:211], v16, %[ra], s41 offen\n\t" \
   "v_mfma_f32_32x32x16_bf16 v[48:63], a[152:155], v[64:67], v[48:63]\n\t" "s_waitcnt vmcnt(15)\n\t" "ds_write_b128 v20, a[228:231] offset:36864\n\t" \
   "s_mov_b32 s41, 4587520\n\t" "buffer_load_dwordx4 v[212:215], v16, %[ra], s41 offen\n\t" \
   "v_mfma_f32_32x32x16_bf16 v[48:63], a[156:159], v[64:67], v[48:63]\n\t" "s_waitcnt lgkmcnt(0)\n\t" "s_barrier\n\t" LDSTAMP(6) \
   "ds_read_b64_tr_b16 a[160:161], v18 offset:0\n\t" "ds_read_b64_tr_b16 a[162:163], v18 offset:4096\n\t" \
   "ds_read_b64_tr_b16 a[164:165], v19 offset:0\n\t" "ds_read_b64_tr_b16 a[166:167], v19 offset:4096\n\t" \
   "ds_read_b64_tr_b16 a[176:177], v18 offset:8192\n\t" "ds_read_b64_tr_b16 a[178:179], v18 offset:12288\n\t" \
   "ds_read_b64_tr_b16 a[180:181], v19 offset:8192\n\t" "ds_read_b64_tr_b16 a[182:183], v19 offset:12288\n\t" \
   "ds_read_b64_tr_b16 a[192:193], v18 offset:16384\n\t" "ds_read_b64_tr_b16 a[194:195], v18 offset:20480\n\t" \
   "ds_read_b64_tr_b16 a[196:197], v19 offset:16384\n\t" "ds_read_b64_tr_b16 a[198:199], v19 offset:20480\n\t" \
   "ds_read_b64_tr_b16 a[208:209], v18 offset:24576\n\t" "ds_read_b64_tr_b16 a[210:211], v18 offset:28672\n\t" \
   "ds_read_b64_tr_b16 a[212:213], v19 offset:24576\n\t" "ds_read_b64_tr_b16 a[214:215], v19 offset:28672\n\t" \
   "ds_read_b64_tr_b16 a[224:225], v18 offset:32768\n\t" "ds_read_b64_tr_b16 a[226:227], v18 offset:36864\n\t" \
   "ds_read_b64_tr_b16 a[228:229], v19 offset:32768\n\t" "ds_read_b64_tr_b16 a[230:231], v19 offset:36864\n\t" "s_waitcnt vmcnt(15)\n\t" \
   "s_waitcnt lgkmcnt(0)\n\t" "s_barrier\n\t" "ds_write_b128 v20, a[168:171] offset:0\n\t" "s_mov_b32 s41, 4718592\n\t" \
   "buffer_load_dwordx4 v[224:227], v16, %[ra], s41 offen\n\t" "v_mfma_f32_32x32x16_bf16 v[32:47], a[160:163], v[64:67], v[32:47]\n\t" \
   "s_waitcnt vmcnt(15)\n\t" "ds_write_b128 v20, a[172:175] offset:4096\n\t" "s_mov_b32 s41, 4849664\n\t" \
   "buffer_load_dwordx4 v[228:231], v16, %[ra], s41 offen\n\t" "v_mfma_f32_32x32x16_bf16 v[32:47], a[164:167], v[64:67], v[32:47]\n\t" \
   "s_waitcnt vmcnt(15)\n\t" "ds_write_b128 v20, a[184:187] offset:8192\n\t" "s_mov_b32 s41, 4980736\n\t" \
   "buffer_load_dwordx4 v[240:243], v16, %[ra], s41 offen\n\t" "v_mfma_f32_32x32x16_bf16 v[32:47], a[176:179], v[64:67], v[32:47]\n\t" \
   "s_waitcnt vmcnt(15)\n\t" "ds_write_b128 v20, a[188:191] offset:12288\n\t" "s_mov_b32 s41, 5111808\n\t" \
   "buffer_load_dwordx4 v[244:247], v16, %[ra], s41 offen\n\t" "v_mfma_f32_32x32x16_bf16 v[32:47], a[180:183], v[64:67], v[32:47]\n\t" \
   "s_waitcnt vmcnt(15)\n\t" "ds_write_b128 v20, a[200:203] offset:16384\n\t" "s_mov_b32 s41, 3932160\n\t" \
   "buffer_load_dwordx4 a[248:251], v16, %[rb], s41 offen\n\t" "v_mfma_f32_32x32x16_bf16 v[32:47], a[192:195], v[64:67], v[32:47]\n\t" \
   "s_waitcnt vmcnt(15)\n\t" "ds_write_b128 v20, a[204:207] offset:20480\n\t" "s_mov_b32 s41, 4063232\n\t" \
   "buffer_load_dwordx4 a[252:255], v16, %[rb], s41 offen\n\t" "v_mfma_f32_32x32x16_bf16 v[32:47], a[196:199], v[64:67], v[32:47]\n\t" \
   "s_waitcnt vmcnt(15)\n\t" "ds_write_b128 v20, a[216:219] offset:24576\n\t" "s_mov_b32 s41, 4194304\n\t" \
   "buffer_load_dwordx4 v[200:203], v16, %[rb], s41 offen\n\t" "v_mfma_f32_32x32x16_bf16 v[32:47], a[208:211], v[64:67], v[32:47]\n\t" \
   "s_waitcnt vmcnt(15)\n\t" "ds_write_b128 v20, a[220:223] offset:28672\n\t" "s_mov_b32 s41, 4325376\n\t" \
   "buffer_load_dwordx4 v[204:207], v16, %[rb], s41 offen\n\t" "v_mfma_f32_32x32x16_bf16 v[32:47], a[212:215], v[64:67], v[32:47]\n\t" \
   "s_waitcnt vmcnt(15)\n\t" "ds_write_b128 v20, a[232:235] offset:32768\n\t" "s_mov_b32 s41, 4456448\n\t" \
   "buffer_load_dwordx4 v[216:219], v16, %[rb], s41 offen\n\t" "v_mfma_f32_32x32x16_bf16 v[32:47], a[224:227], v[64:67], v[32:47]\n\t" \
   "s_waitcnt vmcnt(15)\n\t" "ds_write_b128 v20, a[236:239] offset:36864\n\t" "s_mov_b32 s41, 4587520\n\t" \
   "buffer_load_dwordx4 v[220:223], v16, %[rb], s41 offen\n\t" "v_mfma_f32_32x32x16_bf16 v[32:47], a[228:231], v[64:67], v[32:47]\n\t" \
   "s_waitcnt lgkmcnt(0)\n\t" "s_barrier\n\t" LDSTAMP(7) "ds_read_b64_tr_b16 a[168:169], v18 offset:0\n\t" \
   "ds_read_b64_tr_b16 a[170:171], v18 offset:4096\n\t" "ds_read_b64_tr_b16 a[172:173], v19 offset:0\n\t" \
   "ds_read_b64_tr_b16 a[174:175], v19 offset:4096\n\t" "ds_read_b64_tr_b16 a[184:185], v18 offset:8192\n\t" \
   "ds_read_b64_tr_b16 a[186:187], v18 offset:12288\n\t" "ds_read_b64_tr_b16 a[188:189], v19 offset:8192\n\t" \
   "ds_read_b64_tr_b16 a[190:191], v19 offset:12288\n\t" "ds_read_b64_tr_b16 a[200:201], v18 offset:16384\n\t" \
   "ds_read_b64_tr_b16 a[202:203], v18 offset:20480\n\t" "ds_read_b64_tr_b16 a[204:205], v19 offset:16384\n\t" \
   "ds_read_b64_tr_b16 a[206:207], v19 offset:20480\n\t" "ds_read_b64_tr_b16 a[216:217], v18 offset:24576\n\t" \
   "ds_read_b64_tr_b16 a[218:219], v18 offset:28672\n\t" "ds_read_b64_tr_b16 a[220:221], v19 offset:24576\n\t" \
   "ds_read_b64_tr_b16 a[222:223], v19 offset:28672\n\t" "ds_read_b64_tr_b16 a[232:233], v18 offset:32768\n\t" \
   "ds_read_b64_tr_b16 a[234:235], v18 offset:36864\n\t" "ds_read_b64_tr_b16 a[236:237], v19 offset:32768\n\t" \
   "ds_read_b64_tr_b16 a[238:239], v19 offset:36864\n\t" "s_waitcnt vmcnt(15)\n\t" "s_waitcnt lgkmcnt(0)\n\t" "s_barrier\n\t" \
   "ds_write_b128 v20, a[240:243] offset:0\n\t" "s_mov_b32 s41, 4718592\n\t" "buffer_load_dwordx4 v[232:235], v16, %[rb], s41 offen\n\t" \
   "v_mfma_f32_32x32x16_bf16 v[48:63], a[168:171], v[64:67], v[48:63]\n\t" "s_waitcnt vmcnt(15)\n\t" "ds_write_b128 v20, a[244:247] offset:4096\n\t" \
   "s_mov_b32 s41, 4849664\n\t" "buffer_load_dwordx4 v[236:239], v16, %[rb], s41 offen\n\t" \
   "v_mfma_f32_32x32x16_bf16 v[48:63], a[172:175], v[64:67], v[48:63]\n\t" "s_waitcnt vmcnt(15)\n\t" "ds_write_b128 v20, v[192:195] offset:8192\n\t" \
   "s_mov_b32 s41, 4980736\n\t" "buffer_load_dwordx4 v[248:251], v16, %[rb], s41 offen\n\t" \
   "v_mfma_f32_32x32x16_bf16 v[48:63], a[184:187], v[64:67], v[48:63]\n\t" "s_waitcnt vmcnt(15)\n\t" "ds_write_b128 v20, v[196:199] offset:12288\n\t" \
   "s_mov_b32 s41, 5111808\n\t" "buffer_load_dwordx4 v[252:255], v16, %[rb], s41 offen\n\t" \
   "v_mfma_f32_32x32x16_bf16 v[48:63], a[188:191], v[64:67], v[48:63]\n\t" "s_waitcnt vmcnt(15)\n\t" "ds_write_b128 v20, v[208:211] offset:16384\n\t" \
   "v_mfma_f32_32x32x16_bf16 v[48:63], a[200:203], v[64:67], v[48:63]\n\t" "s_waitcnt vmcnt(14)\n\t" "ds_write_b128 v20, v[212:215] offset:20480\n\t" \
   "v_mfma_f32_32x32x16_bf16 v[48:63], a[204:207], v[64:67], v[48:63]\n\t" "s_waitcnt vmcnt(13)\n\t" "ds_write_b128 v20, v[224:227] offset:24576\n\t" \
   "v_mfma_f32_32x32x16_bf16 v[48:63], a[216:219], v[64:67], v[48:63]\n\t" "s_waitcnt vmcnt(12)\n\t" "ds_write_b128 v20, v[228:231] offset:28672\n\t" \
   "v_mfma_f32_32x32x16_bf16 v[48:63], a[220:223], v[64:67], v[48:63]\n\t" "s_waitcnt vmcnt(11)\n\t" "ds_write_b128 v20, v[240:243] offset:32768\n\t" \
   "v_mfma_f32_32x32x16_bf16 v[48:63], a[232:235], v[64:67], v[48:63]\n\t" "s_waitcnt vmcnt(10)\n\t" "ds_write_b128 v20, v[244:247] offset:36864\n\t" \
   "v_mfma_f32_32x32x16_bf16 v[48:63], a[236:239], v[64:67], v[48:63]\n\t" "buffer_load_dwordx4 v[28:31], v112, %[rw], 0 offen sc1\n\t" \
   "s_waitcnt lgkmcnt(0)\n\t" "s_barrier\n\t" LDSTAMP(8) "ds_read_b64_tr_b16 a[240:241], v18 offset:0\n\t" \
   "ds_read_b64_tr_b16 a[242:243], v18 offset:4096\n\t" "ds_read_b64_tr_b16 a[244:245], v19 offset:0\n\t" \
   "ds_read_b64_tr_b16 a[246:247], v19 offset:4096\n\t" "ds_read_b64_tr_b16 v[192:193], v18 offset:8192\n\t" \
   "ds_read_b64_tr_b16 v[194:195], v18 offset:12288\n\t" "ds_read_b64_tr_b16 v[196:197], v19 offset:8192\n\t" \
   "ds_read_b64_tr_b16 v[198:199], v19 offset:12288\n\t" "ds_read_b64_tr_b16 v[208:209], v18 offset:16384\n\t" \
   "ds_read_b64_tr_b16 v[210:211], v18 offset:20480\n\t" "ds_read_b64_tr_b16 v[212:213], v19 offset:16384\n\t" \
   "ds_read_b64_tr_b16 v[214:215], v19 offset:20480\n\t" "ds_read_b64_tr_b16 v[224:225], v18 offset:24576\n\t" \
   "ds_read_b64_tr_b16 v[226:227], v18 offset:28672\n\t" "ds_read_b64_tr_b16 v[228:229], v19 offset:24576\n\t" \
   "ds_read_b64_tr_b16 v[230:231], v19 offset:28672\n\t" "ds_read_b64_tr_b16 v[240:241], v18 offset:32768\n\t" \
   "ds_read_b64_tr_b16 v[242:243], v18 offset:36864\n\t" "ds_read_b64_tr_b16 v[244:245], v19 offset:32768\n\t" \
   "ds_read_b64_tr_b16 v[246:247], v19 offset:36864\n\t" "s_waitcnt vmcnt(10)\n\t" "s_waitcnt lgkmcnt(0)\n\t" "s_barrier\n\t" \
   "ds_write_b128 v20, a[248:251] offset:0\n\t" "v_mfma_f32_32x32x16_bf16 v[32:47], a[240:243], v[64:67], v[32:47]\n\t" "s_waitcnt vmcnt(9)\n\t" \
   "ds_write_b128 v20, a[252:255] offset:4096\n\t" "v_mfma_f32_32x32x16_bf16 v[32:47], a[244:247], v[64:67], v[32:47]\n\t" "s_waitcnt vmcnt(8)\n\t" \
   "ds_write_b128 v20, v[200:203] offset:8192\n\t" "v_mfma_f32_32x32x16_bf16 v[32:47], v[192:195], v[64:67], v[32:47]\n\t" "s_waitcnt vmcnt(7)\n\t" \
   "ds_write_b128 v20, v[204:207] offset:12288\n\t" "v_mfma_f32_32x32x16_bf16 v[32:47], v[196:199], v[64:67], v[32:47]\n\t" "s_waitcnt vmcnt(6)\n\t" \
   "ds_write_b128 v20, v[216:219] offset:16384\n\t" "v_mfma_f32_32x32x16_bf16 v[32:47], v[208:211], v[64:67], v[32:47]\n\t" "s_waitcnt vmcnt(5)\n\t" \
   "ds_write_b128 v20, v[220:223] offset:20480\n\t" "v_mfma_f32_32x32x16_bf16 v[32:47], v[212:215], v[64:67], v[32:47]\n\t" "s_waitcnt vmcnt(4)\n\t" \
   "ds_write_b128 v20, v[232:235] offset:24576\n\t" "v_mfma_f32_32x32x16_bf16 v[32:47], v[224:227], v[64:67], v[32:47]\n\t" "s_waitcnt vmcnt(3)\n\t" \
   "ds_write_b128 v20, v[236:239] offset:28672\n\t" "v_mfma_f32_32x32x16_bf16 v[32:47], v[228:231], v[64:67], v[32:47]\n\t" "s_waitcnt vmcnt(2)\n\t" \
   "ds_write_b128 v20, v[248:251] offset:32768\n\t" "v_mfma_f32_32x32x16_bf16 v[32:47], v[240:243], v[64:67], v[32:47]\n\t" "s_waitcnt vmcnt(1)\n\t" \
   "ds_write_b128 v20, v[252:255] offset:36864\n\t" "v_mfma_f32_32x32x16_bf16 v[32:47], v[244:247], v[64:67], v[32:47]\n\t" "s_waitcnt lgkmcnt(0)\n\t" \
   "s_barrier\n\t" LDSTAMP(9) "ds_read_b64_tr_b16 a[248:249], v18 offset:0\n\t" "ds_read_b64_tr_b16 a[250:251], v18 offset:4096\n\t" \
   "ds_read_b64_tr_b16 a[252:253], v19 offset:0\n\t" "ds_read_b64_tr_b16 a[254:255], v19 offset:4096\n\t" \
   "ds_read_b64_tr_b16 v[200:201], v18 offset:8192\n\t" "ds_read_b64_tr_b16 v[202:203], v18 offset:12288\n\t" \
   "ds_read_b64_tr_b16 v[204:205], v19 offset:8192\n\t" "ds_read_b64_tr_b16 v[206:207], v19 offset:12288\n\t" \
   "ds_read_b64_tr_b16 v[216:217], v18 offset:16384\n\t" "ds_read_b64_tr_b16 v[218:219], v18 offset:20480\n\t" \
   "ds_read_b64_tr_b16 v[220:221], v19 offset:16384\n\t" "ds_read_b64_tr_b16 v[222:223], v19 offset:20480\n\t" \
   "ds_read_b64_tr_b16 v[232:233], v18 offset:24576\n\t" "ds_read_b64_tr_b16 v[234:235], v18 offset:28672\n\t" \
   "ds_read_b64_tr_b16 v[236:237], v19 offset:24576\n\t" "ds_read_b64_tr_b16 v[238:239], v19 offset:28672\n\t" \
   "ds_read_b64_tr_b16 v[248:249], v18 offset:32768\n\t" "ds_read_b64_tr_b16 v[250:251], v18 offset:36864\n\t" \
   "ds_read_b64_tr_b16 v[252:253], v19 offset:32768\n\t" "ds_read_b64_tr_b16 v[254:255], v19 offset:36864\n\t" "s_waitcnt vmcnt(0) lgkmcnt(0)\n\t" \
   "v_mfma_f32_32x32x16_bf16 v[48:63], a[248:251], v[64:67], v[48:63]\n\t" "v_mfma_f32_32x32x16_bf16 v[48:63], a[252:255], v[64:67], v[48:63]\n\t" \
   "v_mfma_f32_32x32x16_bf16 v[48:63], v[200:203], v[64:67], v[48:63]\n\t" "v_mfma_f32_32x32x16_bf16 v[48:63], v[204:207], v[64:67], v[48:63]\n\t" \
   "v_mfma_f32_32x32x16_bf16 v[48:63], v[216:219], v[64:67], v[48:63]\n\t" "v_mfma_f32_32x32x16_bf16 v[48:63], v[220:223], v[64:67], v[48:63]\n\t" \
   "v_mfma_f32_32x32x16_bf16 v[48:63], v[232:235], v[64:67], v[48:63]\n\t" "v_mfma_f32_32x32x16_bf16 v[48:63], v[236:239], v[64:67], v[48:63]\n\t" \
   "v_mfma_f32_32x32x16_bf16 v[48:63], v[248:251], v[64:67], v[48:63]\n\t" "v_mfma_f32_32x32x16_bf16 v[48:63], v[252:255], v[64:67], v[48:63]\n\t" \
   "s_mov_b32 exec_lo, 1\n\t" "s_mov_b32 exec_hi, 1\n\t" "s_nop 7\n\t" "s_nop 3\n\t" "ds_write_b128 v27, v[32:35] offset:0\n\t" \
   "ds_write_b128 v27, v[36:39] offset:16\n\t" "ds_write_b128 v27, v[40:43] offset:32\n\t" "ds_write_b128 v27, v[44:47] offset:48\n\t" \
   "ds_write_b128 v27, v[48:51] offset:64\n\t" "ds_write_b128 v27, v[52:55] offset:80\n\t" "ds_write_b128 v27, v[56:59] offset:96\n\t" \
   "ds_write_b128 v27, v[60:63] offset:112\n\t" "s_mov_b64 exec, -1\n\t" "s_waitcnt lgkmcnt(0)\n\t" "s_barrier"
#define LDK_CLOB "v24", "v25", "v26", "v32", "v33", "v34", "v35", "v36", "v37", "v38", "v39", "v40", "v41", "v42", "v43", "v44", "v45", "v46", "v47", "v48", "v49", "v50", "v51", "v52", "v53", "v54", "v55", "v56", "v57", "v58", "v59", "v60", "v61", "v62", "v63", "v64", "v65", "v66", "v67", "v68", "v69", "v70", "v71", "v72", "v73", "v74", "v75", "v76", "v77", "v78", "v79", "v80", "v81", "v82", "v83", "v84", "v85", "v86", "v87", "v88", "v89", "v90", "v91", "v92", "v93", "v94", "v95", "v96", "v97", "v98", "v99", "v100", "v101", "v102", "v103", "v104", "v105", "v106", "v107", "v108", "v109", "v110", "v111", "v120", "v121", "v122", "v123", "s40", "s41"
#define LDV_ASM \
   "buffer_load_dword v26, v113, %[rw], 0 offen sc1\n\t" "s_mov_b32 s41, 0\n\t" "buffer_load_dwordx4 v[32:35], v116, %[rx], s41 offen\n\t" \
   "s_mov_b32 s41, 10240\n\t" "buffer_load_dwordx4 v[36:39], v116, %[rx], s41 offen\n\t" "s_mov_b32 s41, 128\n\t" \
   "buffer_load_dwordx4 v[40:43], v116, %[rx], s41 offen\n\t" "s_mov_b32 s41, 10368\n\t" "buffer_load_dwordx4 v[44:47], v116, %[rx], s41 offen\n\t" \
   "s_mov_b32 s41, 256\n\t" "buffer_load_dwordx4 v[48:51], v116, %[rx], s41 offen\n\t" "s_mov_b32 s41, 10496\n\t" \
   "buffer_load_dwordx4 v[52:55], v116, %[rx], s41 offen\n\t" "s_mov_b32 s41, 384\n\t" "buffer_load_dwordx4 v[56:59], v116, %[rx], s41 offen\n\t" \
   "s_mov_b32 s41, 10624\n\t" "buffer_load_dwordx4 v[60:63], v116, %[rx], s41 offen\n\t" "s_mov_b32 s41, 512\n\t" \
   "buffer_load_dwordx4 v[64:67], v116, %[rx], s41 offen\n\t" "s_mov_b32 s41, 10752\n\t" "buffer_load_dwordx4 v[68:71], v116, %[rx], s41 offen\n\t" \
   "s_mov_b32 s41, 640\n\t" "buffer_load_dwordx4 v[72:75], v116, %[rx], s41 offen\n\t" "s_mov_b32 s41, 10880\n\t" \
   "buffer_load_dwordx4 v[76:79], v116, %[rx], s41 offen\n\t" "s_mov_b32 s41, 768\n\t" "buffer_load_dwordx4 v[80:83], v116, %[rx], s41 offen\n\t" \
   "s_mov_b32 s41, 11008\n\t" "buffer_load_dwordx4 v[84:87], v116, %[rx], s41 offen\n\t" "s_mov_b32 s41, 896\n\t" \
   "buffer_load_dwordx4 v[88:91], v116, %[rx], s41 offen\n\t" "s_mov_b32 s41, 11136\n\t" "buffer_load_dwordx4 v[92:95], v116, %[rx], s41 offen\n\t" \
   "s_mov_b32 s40, 0x3f803f80\n\t" "v_mov_b32 v120, 0\n\t" "v_mov_b32 v121, 0\n\t" "v_mov_b32 v122, 0\n\t" "v_mov_b32 v123, 0\n\t" \
   "s_waitcnt vmcnt(16)\n\t" "v_add_u32 v26, 1, v26\n\t" "buffer_store_dword v26, v114, %[rw], 0 offen sc1\n\t" "ds_write_b32 v115, v26\n\t" LDSTAMP(0) \
   "s_waitcnt vmcnt(15)\n\t" LDSTAMP(1) "ds_write_b64 v117, v[32:33] offset:0\n\t" "ds_write_b64 v117, v[34:35] offset:256\n\t" \
   "s_mov_b32 s41, 1024\n\t" "buffer_load_dwordx4 v[96:99], v116, %[rx], s41 offen\n\t" "v_dot2c_f32_bf16 v120, s40, v32\n\t" \
   "v_dot2c_f32_bf16 v121, v32, v32\n\t" "v_dot2c_f32_bf16 v120, s40, v33\n\t" "v_dot2c_f32_bf16 v121, v33, v33\n\t" \
   "v_dot2c_f32_bf16 v120, s40, v34\n\t" "v_dot2c_f32_bf16 v121, v34, v34\n\t" "v_dot2c_f32_bf16 v120, s40, v35\n\t" \
   "v_dot2c_f32_bf16 v121, v35, v35\n\t" "s_waitcnt vmcnt(15)\n\t" "ds_write_b64 v117, v[36:37] offset:128\n\t" \
   "ds_write_b64 v117, v[38:39] offset:384\n\t" "s_mov_b32 s41, 11264\n\t" "buffer_load_dwordx4 v[100:103], v116, %[rx], s41 offen\n\t" \
   "v_dot2c_f32_bf16 v122, s40, v36\n\t" "v_dot2c_f32_bf16 v123, v36, v36\n\t" "v_dot2c_f32_bf16 v122, s40, v37\n\t" \
   "v_dot2c_f32_bf16 v123, v37, v37\n\t" "v_dot2c_f32_bf16 v122, s40, v38\n\t" "v_dot2c_f32_bf16 v123, v38, v38\n\t" \
   "v_dot2c_f32_bf16 v122, s40, v39\n\t" "v_dot2c_f32_bf16 v123, v39, v39\n\t" "s_waitcnt vmcnt(15)\n\t" "ds_write_b64 v117, v[40:41] offset:2048\n\t" \
   "ds_write_b64 v117, v[42:43] offset:2304\n\t" "s_mov_b32 s41, 1152\n\t" "buffer_load_dwordx4 v[104:107], v116, %[rx], s41 offen\n\t" \
   "v_dot2c_f32_bf16 v120, s40, v40\n\t" "v_dot2c_f32_bf16 v121, v40, v40\n\t" "v_dot2c_f32_bf16 v120, s40, v41\n\t" \
   "v_dot2c_f32_bf16 v121, v41, v41\n\t" "v_dot2c_f32_bf16 v120, s40, v42\n\t" "v_dot2c_f32_bf16 v121, v42, v42\n\t" \
   "v_dot2c_f32_bf16 v120, s40, v43\n\t" "v_dot2c_f32_bf16 v121, v43, v43\n\t" "s_waitcnt vmcnt(15)\n\t" "ds_write_b64 v117, v[44:45] offset:2176\n\t" \
   "ds_write_b64 v117, v[46:47] offset:2432\n\t" "s_mov_b32 s41, 11392\n\t" "buffer_load_dwordx4 v[108:111], v116, %[rx], s41 offen\n\t" \
   "v_dot2c_f32_bf16 v122, s40, v44\n\t" "v_dot2c_f32_bf16 v123, v44, v44\n\t" "v_dot2c_f32_bf16 v122, s40, v45\n\t" \
   "v_dot2c_f32_bf16 v123, v45, v45\n\t" "v_dot2c_f32_bf16 v122, s40, v46\n\t" "v_dot2c_f32_bf16 v123, v46, v46\n\t" \
   "v_dot2c_f32_bf16 v122, s40, v47\n\t" "v_dot2c_f32_bf16 v123, v47, v47\n\t" "s_waitcnt vmcnt(15)\n\t" "ds_write_b64 v117, v[48:49] offset:4096\n\t" \
   "ds_write_b64 v117, v[50:51] offset:4352\n\t" "s_mov_b32 s41, 0\n\t" "buffer_load_dwordx4 a[0:3], v16, %[ra], s41 offen\n\t" \
   "v_dot2c_f32_bf16 v120, s40, v48\n\t" "v_dot2c_f32_bf16 v121, v48, v48\n\t" "v_dot2c_f32_bf16 v120, s40, v49\n\t" \
   "v_dot2c_f32_bf16 v121, v49, v49\n\t" "v_dot2c_f32_bf16 v120, s40, v50\n\t" "v_dot2c_f32_bf16 v121, v50, v50\n\t" \
   "v_dot2c_f32_bf16 v120, s40, v51\n\t" "v_dot2c_f32_bf16 v121, v51, v51\n\t" "s_waitcnt vmcnt(15)\n\t" "ds_write_b64 v117, v[52:53] offset:4224\n\t" \
   "ds_write_b64 v117, v[54:55] offset:4480\n\t" "s_mov_b32 s41, 131072\n\t" "buffer_load_dwordx4 a[4:7], v16, %[ra], s41 offen\n\t" \
   "v_dot2c_f32_bf16 v122, s40, v52\n\t" "v_dot2c_f32_bf16 v123, v52, v52\n\t" "v_dot2c_f32_bf16 v122, s40, v53\n\t" \
   "v_dot2c_f32_bf16 v123, v53, v53\n\t" "v_dot2c_f32_bf16 v122, s40, v54\n\t" "v_dot2c_f32_bf16 v123, v54, v54\n\t" \
   "v_dot2c_f32_bf16 v122, s40, v55\n\t" "v_dot2c_f32_bf16 v123, v55, v55\n\t" "s_waitcnt vmcnt(15)\n\t" "ds_write_b64 v117, v[56:57] offset:6144\n\t" \
   "ds_write_b64 v117, v[58:59] offset:6400\n\t" "s_mov_b32 s41, 262144\n\t" "buffer_load_dwordx4 a[8:11], v16, %[ra], s41 offen\n\t" \
   "v_dot2c_f32_bf16 v120, s40, v56\n\t" "v_dot2c_f32_bf16 v121, v56, v56\n\t" "v_dot2c_f32_bf16 v120, s40, v57\n\t" \
   "v_dot2c_f32_bf16 v121, v57, v57\n\t" "v_dot2c_f32_bf16 v120, s40, v58\n\t" "v_dot2c_f32_bf16 v121, v58, v58\n\t" \
   "v_dot2c_f32_bf16 v120, s40, v59\n\t" "v_dot2c_f32_bf16 v121, v59, v59\n\t" "s_waitcnt vmcnt(15)\n\t" "ds_write_b64 v117, v[60:61] offset:6272\n\t" \
   "ds_write_b64 v117, v[62:63] offset:6528\n\t" "s_mov_b32 s41, 393216\n\t" "buffer_load_dwordx4 a[12:15], v16, %[ra], s41 offen\n\t" \
   "v_dot2c_f32_bf16 v122, s40, v60\n\t" "v_dot2c_f32_bf16 v123, v60, v60\n\t" "v_dot2c_f32_bf16 v122, s40, v61\n\t" \
   "v_dot2c_f32_bf16 v123, v61, v61\n\t" "v_dot2c_f32_bf16 v122, s40, v62\n\t" "v_dot2c_f32_bf16 v123, v62, v62\n\t" \
   "v_dot2c_f32_bf16 v122, s40, v63\n\t" "v_dot2c_f32_bf16 v123, v63, v63\n\t" "s_waitcnt vmcnt(15)\n\t" "ds_write_b64 v117, v[64:65] offset:8192\n\t" \
   "ds_write_b64 v117, v[66:67] offset:8448\n\t" "s_mov_b32 s41, 524288\n\t" "buffer_load_dwordx4 a[16:19], v16, %[ra], s41 offen\n\t" \
   "v_dot2c_f32_bf16 v120, s40, v64\n\t" "v_dot2c_f32_bf16 v121, v64, v64\n\t" "v_dot2c_f32_bf16 v120, s40, v65\n\t" \
   "v_dot2c_f32_bf16 v121, v65, v65\n\t" "v_dot2c_f32_bf16 v120, s40, v66\n\t" "v_dot2c_f32_bf16 v121, v66, v66\n\t" \
   "v_dot2c_f32_bf16 v120, s40, v67\n\t" "v_dot2c_f32_bf16 v121, v67, v67\n\t" "s_waitcnt vmcnt(15)\n\t" "ds_write_b64 v117, v[68:69] offset:8320\n\t" \
   "ds_write_b64 v117, v[70:71] offset:8576\n\t" "s_mov_b32 s41, 655360\n\t" "buffer_load_dwordx4 a[20:23], v16, %[ra], s41 offen\n\t" \
   "v_dot2c_f32_bf16 v122, s40, v68\n\t" "v_dot2c_f32_bf16 v123, v68, v68\n\t" "v_dot2c_f32_bf16 v122, s40, v69\n\t" \
   "v_dot2c_f32_bf16 v123, v69, v69\n\t" "v_dot2c_f32_bf16 v122, s40, v70\n\t" "v_dot2c_f32_bf16 v123, v70, v70\n\t" \
   "v_dot2c_f32_bf16 v122, s40, v71\n\t" "v_dot2c_f32_bf16 v123, v71, v71\n\t" "s_waitcnt vmcnt(15)\n\t" "ds_write_b64 v117, v[72:73] offset:10240\n\t" \
   "ds_write_b64 v117, v[74:75] offset:10496\n\t" "s_mov_b32 s41, 786432\n\t" "buffer_load_dwordx4 a[24:27], v16, %[ra], s41 offen\n\t" \
   "v_dot2c_f32_bf16 v120, s40, v72\n\t" "v_dot2c_f32_bf16 v121, v72, v72\n\t" "v_dot2c_f32_bf16 v120, s40, v73\n\t" \
   "v_dot2c_f32_bf16 v121, v73, v73\n\t" "v_dot2c_f32_bf16 v120, s40, v74\n\t" "v_dot2c_f32_bf16 v121, v74, v74\n\t" \
   "v_dot2c_f32_bf16 v120, s40, v75\n\t" "v_dot2c_f32_bf16 v121, v75, v75\n\t" "s_waitcnt vmcnt(15)\n\t" "ds_write_b64 v117, v[76:77] offset:10368\n\t" \
   "ds_write_b64 v117, v[78:79] offset:10624\n\t" "s_mov_b32 s41, 917504\n\t" "buffer_load_dwordx4 a[28:31], v16, %[ra], s41 offen\n\t" \
   "v_dot2c_f32_bf16 v122, s40, v76\n\t" "v_dot2c_f32_bf16 v123, v76, v76\n\t" "v_dot2c_f32_bf16 v122, s40, v77\n\t" \
   "v_dot2c_f32_bf16 v123, v77, v77\n\t" "v_dot2c_f32_bf16 v122, s40, v78\n\t" "v_dot2c_f32_bf16 v123, v78, v78\n\t" \
   "v_dot2c_f32_bf16 v122, s40, v79\n\t" "v_dot2c_f32_bf16 v123, v79, v79\n\t" "s_waitcnt vmcnt(15)\n\t" "ds_write_b64 v117, v[80:81] offset:12288\n\t" \
   "ds_write_b64 v117, v[82:83] offset:12544\n\t" "s_mov_b32 s41, 1048576\n\t" "buffer_load_dwordx4 a[32:35], v16, %[ra], s41 offen\n\t" \
   "v_dot2c_f32_bf16 v120, s40, v80\n\t" "v_dot2c_f32_bf16 v121, v80, v80\n\t" "v_dot2c_f32_bf16 v120, s40, v81\n\t" \
   "v_dot2c_f32_bf16 v121, v81, v81\n\t" "v_dot2c_f32_bf16 v120, s40, v82\n\t" "v_dot2c_f32_bf16 v121, v82, v82\n\t" \
   "v_dot2c_f32_bf16 v120, s40, v83\n\t" "v_dot2c_f32_bf16 v121, v83, v83\n\t" "s_waitcnt vmcnt(15)\n\t" "ds_write_b64 v117, v[84:85] offset:12416\n\t" \
   "ds_write_b64 v117, v[86:87] offset:12672\n\t" "s_mov_b32 s41, 1179648\n\t" "buffer_load_dwordx4 a[36:39], v16, %[ra], s41 offen\n\t" \
   "v_dot2c_f32_bf16 v122, s40, v84\n\t" "v_dot2c_f32_bf16 v123, v84, v84\n\t" "v_dot2c_f32_bf16 v122, s40, v85\n\t" \
   "v_dot2c_f32_bf16 v123, v85, v85\n\t" "v_dot2c_f32_bf16 v122, s40, v86\n\t" "v_dot2c_f32_bf16 v123, v86, v86\n\t" \
   "v_dot2c_f32_bf16 v122, s40, v87\n\t" "v_dot2c_f32_bf16 v123, v87, v87\n\t" "s_waitcnt vmcnt(15)\n\t" "ds_write_b64 v117, v[88:89] offset:14336\n\t" \
   "ds_write_b64 v117, v[90:91] offset:14592\n\t" "s_mov_b32 s41, 1310720\n\t" "buffer_load_dwordx4 a[40:43], v16, %[ra], s41 offen\n\t" \
   "v_dot2c_f32_bf16 v120, s40, v88\n\t" "v_dot2c_f32_bf16 v121, v88, v88\n\t" "v_dot2c_f32_bf16 v120, s40, v89\n\t" \
   "v_dot2c_f32_bf16 v121, v89, v89\n\t" "v_dot2c_f32_bf16 v120, s40, v90\n\t" "v_dot2c_f32_bf16 v121, v90, v90\n\t" \
   "v_dot2c_f32_bf16 v120, s40, v91\n\t" "v_dot2c_f32_bf16 v121, v91, v91\n\t" "s_waitcnt vmcnt(15)\n\t" "ds_write_b64 v117, v[92:93] offset:14464\n\t" \
   "ds_write_b64 v117, v[94:95] offset:14720\n\t" "s_mov_b32 s41, 1441792\n\t" "buffer_load_dwordx4 a[44:47], v16, %[ra], s41 offen\n\t" \
   "v_dot2c_f32_bf16 v122, s40, v92\n\t" "v_dot2c_f32_bf16 v123, v92, v92\n\t" "v_dot2c_f32_bf16 v122, s40, v93\n\t" \
   "v_dot2c_f32_bf16 v123, v93, v93\n\t" "v_dot2c_f32_bf16 v122, s40, v94\n\t" "v_dot2c_f32_bf16 v123, v94, v94\n\t" \
   "v_dot2c_f32_bf16 v122, s40, v95\n\t" "v_dot2c_f32_bf16 v123, v95, v95\n\t" "s_waitcnt vmcnt(15)\n\t" "ds_write_b64 v117, v[96:97] offset:16384\n\t" \
   "ds_write_b64 v117, v[98:99] offset:16640\n\t" "s_mov_b32 s41, 1572864\n\t" "buffer_load_dwordx4 a[48:51], v16, %[ra], s41 offen\n\t" \
   "v_dot2c_f32_bf16 v120, s40, v96\n\t" "v_dot2c_f32_bf16 v121, v96, v96\n\t" "v_dot2c_f32_bf16 v120, s40, v97\n\t" \
   "v_dot2c_f32_bf16 v121, v97, v97\n\t" "v_dot2c_f32_bf16 v120, s40, v98\n\t" "v_dot2c_f32_bf16 v121, v98, v98\n\t" \
   "v_dot2c_f32_bf16 v120, s40, v99\n\t" "v_dot2c_f32_bf16 v121, v99, v99\n\t" "s_waitcnt vmcnt(15)\n\t" \
   "ds_write_b64 v117, v[100:101] offset:16512\n\t" "ds_write_b64 v117, v[102:103] offset:16768\n\t" "s_mov_b32 s41, 1703936\n\t" \
   "buffer_load_dwordx4 a[52:55], v16, %[ra], s41 offen\n\t" "v_dot2c_f32_bf16 v122, s40, v100\n\t" "v_dot2c_f32_bf16 v123, v100, v100\n\t" \
   "v_dot2c_f32_bf16 v122, s40, v101\n\t" "v_dot2c_f32_bf16 v123, v101, v101\n\t" "v_dot2c_f32_bf16 v122, s40, v102\n\t" \
   "v_dot2c_f32_bf16 v123, v102, v102\n\t" "v_dot2c_f32_bf16 v122, s40, v103\n\t" "v_dot2c_f32_bf16 v123, v103, v103\n\t" "s_waitcnt vmcnt(15)\n\t" \
   "ds_write_b64 v117, v[104:105] offset:18432\n\t" "ds_write_b64 v117, v[106:107] offset:18688\n\t" "s_mov_b32 s41, 1835008\n\t" \
   "buffer_load_dwordx4 a[56:59], v16, %[ra], s41 offen\n\t" "v_dot2c_f32_bf16 v120, s40, v104\n\t" "v_dot2c_f32_bf16 v121, v104, v104\n\t" \
   "v_dot2c_f32_bf16 v120, s40, v105\n\t" "v_dot2c_f32_bf16 v121, v105, v105\n\t" "v_dot2c_f32_bf16 v120, s40, v106\n\t" \
   "v_dot2c_f32_bf16 v121, v106, v106\n\t" "v_dot2c_f32_bf16 v120, s40, v107\n\t" "v_dot2c_f32_bf16 v121, v107, v107\n\t" "s_waitcnt vmcnt(15)\n\t" \
   "ds_write_b64 v117, v[108:109] offset:18560\n\t" "ds_write_b64 v117, v[110:111] offset:18816\n\t" "s_mov_b32 s41, 1966080\n\t" \
   "buffer_load_dwordx4 a[60:63], v16, %[ra], s41 offen\n\t" "v_dot2c_f32_bf16 v122, s40, v108\n\t" "v_dot2c_f32_bf16 v123, v108, v108\n\t" \
   "v_dot2c_f32_bf16 v122, s40, v109\n\t" "v_dot2c_f32_bf16 v123, v109, v109\n\t" "v_dot2c_f32_bf16 v122, s40, v110\n\t" \
   "v_dot2c_f32_bf16 v123, v110, v110\n\t" "v_dot2c_f32_bf16 v122, s40, v111\n\t" "v_dot2c_f32_bf16 v123, v111, v111\n\t" "s_nop 3\n\t" \
   "v_add_f32_dpp v120, v120, v120 quad_perm:[1,0,3,2] row_mask:0xf bank_mask:0xf\n\t" \
   "v_add_f32_dpp v121, v121, v121 quad_perm:[1,0,3,2] row_mask:0xf bank_mask:0xf\n\t" \
   "v_add_f32_dpp v122, v122, v122 quad_perm:[1,0,3,2] row_mask:0xf bank_mask:0xf\n\t" \
   "v_add_f32_dpp v123, v123, v123 quad_perm:[1,0,3,2] row_mask:0xf bank_mask:0xf\n\t" \
   "v_add_f32_dpp v120, v120, v120 quad_perm:[2,3,0,1] row_mask:0xf bank_mask:0xf\n\t" \
   "v_add_f32_dpp v121, v121, v121 quad_perm:[2,3,0,1] row_mask:0xf bank_mask:0xf\n\t" \
   "v_add_f32_dpp v122, v122, v122 quad_perm:[2,3,0,1] row_mask:0xf bank_mask:0xf\n\t" \
   "v_add_f32_dpp v123, v123, v123 quad_perm:[2,3,0,1] row_mask:0xf bank_mask:0xf\n\t" \
   "v_add_f32_dpp v120, v120, v120 row_half_mirror row_mask:0xf bank_mask:0xf\n\t" \
   "v_add_f32_dpp v121, v121, v121 row_half_mirror row_mask:0xf bank_mask:0xf\n\t" \
   "v_add_f32_dpp v122, v122, v122 row_half_mirror row_mask:0xf bank_mask:0xf\n\t" \
   "v_add_f32_dpp v123, v123, v123 row_half_mirror row_mask:0xf bank_mask:0xf\n\t" "v_and_b32 v120, v119, v120\n\t" "v_and_b32 v121, v119, v121\n\t" \
   "v_and_b32 v122, v119, v122\n\t" "v_and_b32 v123, v119, v123\n\t" "ds_write_b64 v118, v[120:121]\n\t" "ds_write_b64 v118, v[122:123] offset:384\n\t" \
   "s_waitcnt vmcnt(15)\n\t" "ds_write_b128 v20, a[0:3] offset:0\n\t" "s_mov_b32 s41, 2097152\n\t" \
   "buffer_load_dwordx4 a[64:67], v16, %[ra], s41 offen\n\t" "s_waitcnt vmcnt(15)\n\t" "ds_write_b128 v20, a[4:7] offset:4096\n\t" \
   "s_mov_b32 s41, 2228224\n\t" "buffer_load_dwordx4 a[68:71], v16, %[ra], s41 offen\n\t" "s_waitcnt vmcnt(15)\n\t" \
   "ds_write_b128 v20, a[8:11] offset:8192\n\t" "s_mov_b32 s41, 2359296\n\t" "buffer_load_dwordx4 a[72:75], v16, %[ra], s41 offen\n\t" \
   "s_waitcnt vmcnt(15)\n\t" "ds_write_b128 v20, a[12:15] offset:12288\n\t" "s_mov_b32 s41, 2490368\n\t" \
   "buffer_load_dwordx4 a[76:79], v16, %[ra], s41 offen\n\t" "s_waitcnt vmcnt(15)\n\t" "ds_write_b128 v20, a[16:19] offset:16384\n\t" \
   "s_mov_b32 s41, 2621440\n\t" "buffer_load_dwordx4 a[80:83], v16, %[ra], s41 offen\n\t" "s_waitcnt vmcnt(15)\n\t" \
   "ds_write_b128 v20, a[20:23] offset:20480\n\t" "s_mov_b32 s41, 2752512\n\t" "buffer_load_dwordx4 a[84:87], v16, %[ra], s41 offen\n\t" \
   "s_waitcnt vmcnt(15)\n\t" "ds_write_b128 v20, a[24:27] offset:24576\n\t" "s_mov_b32 s41, 2883584\n\t" \
   "buffer_load_dwordx4 a[88:91], v16, %[ra], s41 offen\n\t" "s_waitcnt vmcnt(15)\n\t" "ds_write_b128 v20, a[28:31] offset:28672\n\t" \
   "s_mov_b32 s41, 3014656\n\t" "buffer_load_dwordx4 a[92:95], v16, %[ra], s41 offen\n\t" "s_waitcnt vmcnt(15)\n\t" \
   "ds_write_b128 v20, a[32:35] offset:32768\n\t" "s_mov_b32 s41, 3145728\n\t" "buffer_load_dwordx4 a[96:99], v16, %[ra], s41 offen\n\t" \
   "s_waitcnt vmcnt(15)\n\t" "ds_write_b128 v20, a[36:39] offset:36864\n\t" "s_mov_b32 s41, 3276800\n\t" \
   "buffer_load_dwordx4 a[100:103], v16, %[ra], s41 offen\n\t" "s_waitcnt lgkmcnt(0)\n\t" "s_barrier\n\t" LDSTAMP(2) \
   "ds_read_b64_tr_b16 a[0:1], v18 offset:0\n\t" "ds_read_b64_tr_b16 a[2:3], v18 offset:4096\n\t" "ds_read_b64_tr_b16 a[4:5], v19 offset:0\n\t" \
   "ds_read_b64_tr_b16 a[6:7], v19 offset:4096\n\t" "ds_read_b64_tr_b16 a[8:9], v18 offset:8192\n\t" \
   "ds_read_b64_tr_b16 a[10:11], v18 offset:12288\n\t" "ds_read_b64_tr_b16 a[12:13], v19 offset:8192\n\t" \
   "ds_read_b64_tr_b16 a[14:15], v19 offset:12288\n\t" "ds_read_b64_tr_b16 a[16:17], v18 offset:16384\n\t" \
   "ds_read_b64_tr_b16 a[18:19], v18 offset:20480\n\t" "ds_read_b64_tr_b16 a[20:21], v19 offset:16384\n\t" \
   "ds_read_b64_tr_b16 a[22:23], v19 offset:20480\n\t" "ds_read_b64_tr_b16 a[24:25], v18 offset:24576\n\t" \
   "ds_read_b64_tr_b16 a[26:27], v18 offset:28672\n\t" "ds_read_b64_tr_b16 a[28:29], v19 offset:24576\n\t" \
   "ds_read_b64_tr_b16 a[30:31], v19 offset:28672\n\t" "ds_read_b64_tr_b16 a[32:33], v18 offset:32768\n\t" \
   "ds_read_b64_tr_b16 a[34:35], v18 offset:36864\n\t" "ds_read_b64_tr_b16 a[36:37], v19 offset:32768\n\t" \
   "ds_read_b64_tr_b16 a[38:39], v19 offset:36864\n\t" "s_waitcnt vmcnt(15)\n\t" "s_waitcnt lgkmcnt(0)\n\t" "s_barrier\n\t" \
   "v_mov_b32 v64, 0x3f803f80\n\t" "v_mov_b32 v65, 0x3f803f80\n\t" "v_mov_b32 v66, 0x3f803f80\n\t" "v_mov_b32 v67, 0x3f803f80\n\t" \
   "ds_write_b128 v20, a[40:43] offset:0\n\t" "s_mov_b32 s41, 3407872\n\t" "buffer_load_dwordx4 a[104:107], v16, %[ra], s41 offen\n\t" \
   "v_mfma_f32_32x32x16_bf16 v[32:47], a[0:3], v[64:67], 0\n\t" "s_waitcnt vmcnt(15)\n\t" "ds_write_b128 v20, a[44:47] offset:4096\n\t" \
   "s_mov_b32 s41, 3538944\n\t" "buffer_load_dwordx4 a[108:111], v16, %[ra], s41 offen\n\t" \
   "v_mfma_f32_32x32x16_bf16 v[32:47], a[4:7], v[64:67], v[32:47]\n\t" "s_waitcnt vmcnt(15)\n\t" "ds_write_b128 v20, a[48:51] offset:8192\n\t" \
   "s_mov_b32 s41, 3670016\n\t" "buffer_load_dwordx4 a[112:115], v16, %[ra], s41 offen\n\t" \
   "v_mfma_f32_32x32x16_bf16 v[32:47], a[8:11], v[64:67], v[32:47]\n\t" "s_waitcnt vmcnt(15)\n\t" "ds_write_b128 v20, a[52:55] offset:12288\n\t" \
   "s_mov_b32 s41, 3801088\n\t" "buffer_load_dwordx4 a[116:119], v16, %[ra], s41 offen\n\t" \
   "v_mfma_f32_32x32x16_bf16 v[32:47], a[12:15], v[64:67], v[32:47]\n\t" "s_waitcnt vmcnt(15)\n\t" "ds_write_b128 v20, a[56:59] offset:16384\n\t" \
   "s_mov_b32 s41, 3932160\n\t" "buffer_load_dwordx4 a[120:123], v16, %[ra], s41 offen\n\t" \
   "v_mfma_f32_32x32x16_bf16 v[32:47], a[16:19], v[64:67], v[32:47]\n\t" "s_waitcnt vmcnt(15)\n\t" "ds_write_b128 v20, a[60:63] offset:20480\n\t" \
   "s_mov_b32 s41, 4063232\n\t" "buffer_load_dwordx4 a[124:127], v16, %[ra], s41 offen\n\t" \
   "v_mfma_f32_32x32x16_bf16 v[32:47], a[20:23], v[64:67], v[32:47]\n\t" "s_waitcnt vmcnt(15)\n\t" "ds_write_b128 v20, a[64:67] offset:24576\n\t" \
   "s_mov_b32 s41, 4194304\n\t" "buffer_load_dwordx4 a[128:131], v16, %[ra], s41 offen\n\t" \
   "v_mfma_f32_32x32x16_bf16 v[32:47], a[24:27], v[64:67], v[32:47]\n\t" "s_waitcnt vmcnt(15)\n\t" "ds_write_b128 v20, a[68:71] offset:28672\n\t" \
   "s_mov_b32 s41, 4325376\n\t" "buffer_load_dwordx4 a[132:135], v16, %[ra], s41 offen\n\t" \
   "v_mfma_f32_32x32x16_bf16 v[32:47], a[28:31], v[64:67], v[32:47]\n\t" "s_waitcnt vmcnt(15)\n\t" "ds_write_b128 v20, a[72:75] offset:32768\n\t" \
   "s_mov_b32 s41, 4456448\n\t" "buffer_load_dwordx4 a[136:139], v16, %[ra], s41 offen\n\t" \
   "v_mfma_f32_32x32x16_bf16 v[32:47], a[32:35], v[64:67], v[32:47]\n\t" "s_waitcnt vmcnt(15)\n\t" "ds_write_b128 v20, a[76:79] offset:36864\n\t" \
   "s_mov_b32 s41, 4587520\n\t" "buffer_load_dwordx4 a[140:143], v16, %[ra], s41 offen\n\t" \
   "v_mfma_f32_32x32x16_bf16 v[32:47], a[36:39], v[64:67], v[32:47]\n\t" "s_waitcnt lgkmcnt(0)\n\t" "s_barrier\n\t" LDSTAMP(3) \
   "ds_read_b64_tr_b16 a[40:41], v18 offset:0\n\t" "ds_read_b64_tr_b16 a[42:43], v18 offset:4096\n\t" "ds_read_b64_tr_b16 a[44:45], v19 offset:0\n\t" \
   "ds_read_b64_tr_b16 a[46:47], v19 offset:4096\n\t" "ds_read_b64_tr_b16 a[48:49], v18 offset:8192\n\t" \
   "ds_read_b64_tr_b16 a[50:51], v18 offset:12288\n\t" "ds_read_b64_tr_b16 a[52:53], v19 offset:8192\n\t" \
   "ds_read_b64_tr_b16 a[54:55], v19 offset:12288\n\t" "ds_read_b64_tr_b16 a[56:57], v18 offset:16384\n\t" \
   "ds_read_b64_tr_b16 a[58:59], v18 offset:20480\n\t" "ds_read_b64_tr_b16 a[60:61], v19 offset:16384\n\t" \
   "ds_read_b64_tr_b16 a[62:63], v19 offset:20480\n\t" "ds_read_b64_tr_b16 a[64:65], v18 offset:24576\n\t" \
   "ds_read_b64_tr_b16 a[66:67], v18 offset:28672\n\t" "ds_read_b64_tr_b16 a[68:69], v19 offset:24576\n\t" \
   "ds_read_b64_tr_b16 a[70:71], v19 offset:28672\n\t" "ds_read_b64_tr_b16 a[72:73], v18 offset:32768\n\t" \
   "ds_read_b64_tr_b16 a[74:75], v18 offset:36864\n\t" "ds_read_b64_tr_b16 a[76:77], v19 offset:32768\n\t" \
   "ds_read_b64_tr_b16 a[78:79], v19 offset:36864\n\t" "s_waitcnt vmcnt(15)\n\t" "s_waitcnt lgkmcnt(0)\n\t" "s_barrier\n\t" \
   "ds_write_b128 v20, a[80:83] offset:0\n\t" "s_mov_b32 s41, 4718592\n\t" "buffer_load_dwordx4 a[144:147], v16, %[ra], s41 offen\n\t" \
   "v_mfma_f32_32x32x16_bf16 v[32:47], a[40:43], v[64:67], v[32:47]\n\t" "s_waitcnt vmcnt(15)\n\t" "ds_write_b128 v20, a[84:87] offset:4096\n\t" \
   "s_mov_b32 s41, 4849664\n\t" "buffer_load_dwordx4 a[148:151], v16, %[ra], s41 offen\n\t" \
   "v_mfma_f32_32x32x16_bf16 v[32:47], a[44:47], v[64:67], v[32:47]\n\t" "s_waitcnt vmcnt(15)\n\t" "ds_write_b128 v20, a[88:91] offset:8192\n\t" \
   "s_mov_b32 s41, 4980736\n\t" "buffer_load_dwordx4 a[152:155], v16, %[ra], s41 offen\n\t" \
   "v_mfma_f32_32x32x16_bf16 v[32:47], a[48:51], v[64:67], v[32:47]\n\t" "s_waitcnt vmcnt(15)\n\t" "ds_write_b128 v20, a[92:95] offset:12288\n\t" \
   "s_mov_b32 s41, 5111808\n\t" "buffer_load_dwordx4 a[156:159], v16, %[ra], s41 offen\n\t" \
   "v_mfma_f32_32x32x16_bf16 v[32:47], a[52:55], v[64:67], v[32:47]\n\t" "s_waitcnt vmcnt(15)\n\t" "ds_write_b128 v20, a[96:99] offset:16384\n\t" \
   "s_mov_b32 s41, 0\n\t" "buffer_load_dwordx4 a[160:163], v17, %[rb], s41 offen\n\t" \
   "v_mfma_f32_32x32x16_bf16 v[32:47], a[56:59], v[64:67], v[32:47]\n\t" "s_waitcnt vmcnt(15)\n\t" "ds_write_b128 v20, a[100:103] offset:20480\n\t" \
   "s_mov_b32 s41, 256\n\t" "buffer_load_dwordx4 a[176:179], v17, %[rb], s41 offen\n\t" \
   "v_mfma_f32_32x32x16_bf16 v[32:47], a[60:63], v[64:67], v[32:47]\n\t" "s_waitcnt vmcnt(15)\n\t" "ds_write_b128 v20, a[104:107] offset:24576\n\t" \
   "s_mov_b32 s41, 512\n\t" "buffer_load_dwordx4 a[192:195], v17, %[rb], s41 offen\n\t" \
   "v_mfma_f32_32x32x16_bf16 v[32:47], a[64:67], v[64:67], v[32:47]\n\t" "s_waitcnt vmcnt(15)\n\t" "ds_write_b128 v20, a[108:111] offset:28672\n\t" \
   "s_mov_b32 s41, 768\n\t" "buffer_load_dwordx4 a[208:211], v17, %[rb], s41 offen\n\t" \
   "v_mfma_f32_32x32x16_bf16 v[32:47], a[68:71], v[64:67], v[32:47]\n\t" "s_waitcnt vmcnt(15)\n\t" "ds_write_b128 v20, a[112:115] offset:32768\n\t" \
   "s_mov_b32 s41, 1024\n\t" "buffer_load_dwordx4 a[224:227], v17, %[rb], s41 offen\n\t" \
   "v_mfma_f32_32x32x16_bf16 v[32:47], a[72:75], v[64:67], v[32:47]\n\t" "s_waitcnt vmcnt(15)\n\t" "ds_write_b128 v20, a[116:119] offset:36864\n\t" \
   "s_mov_b32 s41, 20480\n\t" "buffer_load_dwordx4 a[240:243], v17, %[rb], s41 offen\n\t" \
   "v_mfma_f32_32x32x16_bf16 v[32:47], a[76:79], v[64:67], v[32:47]\n\t" "s_waitcnt lgkmcnt(0)\n\t" "s_barrier\n\t" LDSTAMP(4) \
   "ds_read_b64_tr_b16 a[80:81], v18 offset:0\n\t" "ds_read_b64_tr_b16 a[82:83], v18 offset:4096\n\t" "ds_read_b64_tr_b16 a[84:85], v19 offset:0\n\t" \
   "ds_read_b64_tr_b16 a[86:87], v19 offset:4096\n\t" "ds_read_b64_tr_b16 a[88:89], v18 offset:8192\n\t" \
   "ds_read_b64_tr_b16 a[90:91], v18 offset:12288\n\t" "ds_read_b64_tr_b16 a[92:93], v19 offset:8192\n\t" \
   "ds_read_b64_tr_b16 a[94:95], v19 offset:12288\n\t" "ds_read_b64_tr_b16 a[96:97], v18 offset:16384\n\t" \
   "ds_read_b64_tr_b16 a[98:99], v18 offset:20480\n\t" "ds_read_b64_tr_b16 a[100:101], v19 offset:16384\n\t" \
   "ds_read_b64_tr_b16 a[102:103], v19 offset:20480\n\t" "ds_read_b64_tr_b16 a[104:105], v18 offset:24576\n\t" \
   "ds_read_b64_tr_b16 a[106:107], v18 offset:28672\n\t" "ds_read_b64_tr_b16 a[108:109], v19 offset:24576\n\t" \
   "ds_read_b64_tr_b16 a[110:111], v19 offset:28672\n\t" "ds_read_b64_tr_b16 a[112:113], v18 offset:32768\n\t" \
   "ds_read_b64_tr_b16 a[114:115], v18 offset:36864\n\t" "ds_read_b64_tr_b16 a[116:117], v19 offset:32768\n\t" \
   "ds_read_b64_tr_b16 a[118:119], v19 offset:36864\n\t" "s_waitcnt vmcnt(15)\n\t" "s_waitcnt lgkmcnt(0)\n\t" "s_barrier\n\t" \
   "ds_write_b128 v20, a[120:123] offset:0\n\t" "s_mov_b32 s41, 20736\n\t" "buffer_load_dwordx4 v[192:195], v17, %[rb], s41 offen\n\t" \
   "v_mfma_f32_32x32x16_bf16 v[32:47], a[80:83], v[64:67], v[32:47]\n\t" "s_waitcnt vmcnt(15)\n\t" "ds_write_b128 v20, a[124:127] offset:4096\n\t" \
   "s_mov_b32 s41, 20992\n\t" "buffer_load_dwordx4 v[208:211], v17, %[rb], s41 offen\n\t" \
   "v_mfma_f32_32x32x16_bf16 v[32:47], a[84:87], v[64:67], v[32:47]\n\t" "s_waitcnt vmcnt(15)\n\t" "ds_write_b128 v20, a[128:131] offset:8192\n\t" \
   "s_mov_b32 s41, 21248\n\t" "buffer_load_dwordx4 v[224:227], v17, %[rb], s41 offen\n\t" \
   "v_mfma_f32_32x32x16_bf16 v[32:47], a[88:91], v[64:67], v[32:47]\n\t" "s_waitcnt vmcnt(15)\n\t" "ds_write_b128 v20, a[132:135] offset:12288\n\t" \
   "s_mov_b32 s41, 21504\n\t" "buffer_load_dwordx4 v[240:243], v17, %[rb], s41 offen\n\t" \
   "v_mfma_f32_32x32x16_bf16 v[32:47], a[92:95], v[64:67], v[32:47]\n\t" "s_waitcnt vmcnt(15)\n\t" "ds_write_b128 v20, a[136:139] offset:16384\n\t" \
   "s_mov_b32 s41, 40960\n\t" "buffer_load_dwordx4 a[164:167], v17, %[rb], s41 offen\n\t" \
   "v_mfma_f32_32x32x16_bf16 v[32:47], a[96:99], v[64:67], v[32:47]\n\t" "s_waitcnt vmcnt(15)\n\t" "ds_write_b128 v20, a[140:143] offset:20480\n\t" \
   "s_mov_b32 s41, 41216\n\t" "buffer_load_dwordx4 a[180:183], v17, %[rb], s41 offen\n\t" \
   "v_mfma_f32_32x32x16_bf16 v[32:47], a[100:103], v[64:67], v[32:47]\n\t" "s_waitcnt vmcnt(15)\n\t" "ds_write_b128 v20, a[144:147] offset:24576\n\t" \
   "s_mov_b32 s41, 41472\n\t" "buffer_load_dwordx4 a[196:199], v17, %[rb], s41 offen\n\t" \
   "v_mfma_f32_32x32x16_bf16 v[32:47], a[104:107], v[64:67], v[32:47]\n\t" "s_waitcnt vmcnt(15)\n\t" "ds_write_b128 v20, a[148:151] offset:28672\n\t" \
   "s_mov_b32 s41, 41728\n\t" "buffer_load_dwordx4 a[212:215], v17, %[rb], s41 offen\n\t" \
   "v_mfma_f32_32x32x16_bf16 v[32:47], a[108:111], v[64:67], v[32:47]\n\t" "s_waitcnt vmcnt(15)\n\t" "ds_write_b128 v20, a[152:155] offset:32768\n\t" \
   "s_mov_b32 s41, 41984\n\t" "buffer_load_dwordx4 a[228:231], v17, %[rb], s41 offen\n\t" \
   "v_mfma_f32_32x32x16_bf16 v[32:47], a[112:115], v[64:67], v[32:47]\n\t" "s_waitcnt vmcnt(15)\n\t" "ds_write_b128 v20, a[156:159] offset:36864\n\t" \
   "s_mov_b32 s41, 61440\n\t" "buffer_load_dwordx4 a[244:247], v17, %[rb], s41 offen\n\t" \
   "v_mfma_f32_32x32x16_bf16 v[32:47], a[116:119], v[64:67], v[32:47]\n\t" "s_waitcnt lgkmcnt(0)\n\t" "s_barrier\n\t" LDSTAMP(5) \
   "ds_read_b64_tr_b16 a[120:121], v18 offset:0\n\t" "ds_read_b64_tr_b16 a[122:123], v18 offset:4096\n\t" \
   "ds_read_b64_tr_b16 a[124:125], v19 offset:0\n\t" "ds_read_b64_tr_b16 a[126:127], v19 offset:4096\n\t" \
   "ds_read_b64_tr_b16 a[128:129], v18 offset:8192\n\t" "ds_read_b64_tr_b16 a[130:131], v18 offset:12288\n\t" \
   "ds_read_b64_tr_b16 a[132:133], v19 offset:8192\n\t" "ds_read_b64_tr_b16 a[134:135], v19 offset:12288\n\t" \
   "ds_read_b64_tr_b16 a[136:137], v18 offset:16384\n\t" "ds_read_b64_tr_b16 a[138:139], v18 offset:20480\n\t" \
   "ds_read_b64_tr_b16 a[140:141], v19 offset:16384\n\t" "ds_read_b64_tr_b16 a[142:143], v19 offset:20480\n\t" \
   "ds_read_b64_tr_b16 a[144:145], v18 offset:24576\n\t" "ds_read_b64_tr_b16 a[146:147], v18 offset:28672\n\t" \
   "ds_read_b64_tr_b16 a[148:149], v19 offset:24576\n\t" "ds_read_b64_tr_b16 a[150:151], v19 offset:28672\n\t" \
   "ds_read_b64_tr_b16 a[152:153], v18 offset:32768\n\t" "ds_read_b64_tr_b16 a[154:155], v18 offset:36864\n\t" \
   "ds_read_b64_tr_b16 a[156:157], v19 offset:32768\n\t" "ds_read_b64_tr_b16 a[158:159], v19 offset:36864\n\t" "s_waitcnt vmcnt(15)\n\t" \
   "s_waitcnt lgkmcnt(0)\n\t" "s_barrier\n\t" "ds_write_b32 v21, a160 offset:0\n\t" "ds_write_b32 v21, a161 offset:21120\n\t" \
   "ds_write_b32 v21, a162 offset:10560\n\t" "ds_write_b32 v21, a163 offset:31680\n\t" "s_mov_b32 s41, 61696\n\t" \
   "buffer_load_dwordx4 v[196:199], v17, %[rb], s41 offen\n\t" "v_mfma_f32_32x32x16_bf16 v[32:47], a[120:123], v[64:67], v[32:47]\n\t" \
   "s_waitcnt vmcnt(15)\n\t" "ds_write_b32 v21, a176 offset:64\n\t" "ds_write_b32 v21, a177 offset:21184\n\t" "ds_write_b32 v21, a178 offset:10624\n\t" \
   "ds_write_b32 v21, a179 offset:31744\n\t" "s_mov_b32 s41, 61952\n\t" "buffer_load_dwordx4 v[212:215], v17, %[rb], s41 offen\n\t" \
   "v_mfma_f32_32x32x16_bf16 v[32:47], a[124:127], v[64:67], v[32:47]\n\t" "s_waitcnt vmcnt(15)\n\t" "ds_write_b32 v21, a192 offset:128\n\t" \
   "ds_write_b32 v21, a193 offset:21248\n\t" "ds_write_b32 v21, a194 offset:10688\n\t" "ds_write_b32 v21, a195 offset:31808\n\t" \
   "s_mov_b32 s41, 62208\n\t" "buffer_load_dwordx4 v[228:231], v17, %[rb], s41 offen\n\t" \
   "v_mfma_f32_32x32x16_bf16 v[32:47], a[128:131], v[64:67], v[32:47]\n\t" "s_waitcnt vmcnt(15)\n\t" "ds_write_b32 v21, a208 offset:192\n\t" \
   "ds_write_b32 v21, a209 offset:21312\n\t" "ds_write_b32 v21, a210 offset:10752\n\t" "ds_write_b32 v21, a211 offset:31872\n\t" \
   "s_mov_b32 s41, 62464\n\t" "buffer_load_dwordx4 v[244:247], v17, %[rb], s41 offen\n\t" \
   "v_mfma_f32_32x32x16_bf16 v[32:47], a[132:135], v[64:67], v[32:47]\n\t" "s_waitcnt vmcnt(15)\n\t" "ds_write_b32 v21, a224 offset:256\n\t" \
   "ds_write_b32 v21, a225 offset:21376\n\t" "ds_write_b32 v21, a226 offset:10816\n\t" "ds_write_b32 v21, a227 offset:31936\n\t" \
   "s_mov_b32 s41, 81920\n\t" "buffer_load_dwordx4 a[168:171], v17, %[rb], s41 offen\n\t" \
   "v_mfma_f32_32x32x16_bf16 v[32:47], a[136:139], v[64:67], v[32:47]\n\t" "s_waitcnt vmcnt(15)\n\t" "ds_write_b32 v21, a240 offset:5248\n\t" \
   "ds_write_b32 v21, a241 offset:26368\n\t" "ds_write_b32 v21, a242 offset:15808\n\t" "ds_write_b32 v21, a243 offset:36928\n\t" \
   "s_mov_b32 s41, 82176\n\t" "buffer_load_dwordx4 a[184:187], v17, %[rb], s41 offen\n\t" \
   "v_mfma_f32_32x32x16_bf16 v[32:47], a[140:143], v[64:67], v[32:47]\n\t" "s_waitcnt vmcnt(15)\n\t" "ds_write_b32 v21, v192 offset:5312\n\t" \
   "ds_write_b32 v21, v193 offset:26432\n\t" "ds_write_b32 v21, v194 offset:15872\n\t" "ds_write_b32 v21, v195 offset:36992\n\t" \
   "s_mov_b32 s41, 82432\n\t" "buffer_load_dwordx4 a[200:203], v17, %[rb], s41 offen\n\t" \
   "v_mfma_f32_32x32x16_bf16 v[32:47], a[144:147], v[64:67], v[32:47]\n\t" "s_waitcnt vmcnt(15)\n\t" "ds_write_b32 v21, v208 offset:5376\n\t" \
   "ds_write_b32 v21, v209 offset:26496\n\t" "ds_write_b32 v21, v210 offset:15936\n\t" "ds_write_b32 v21, v211 offset:37056\n\t" \
   "s_mov_b32 s41, 82688\n\t" "buffer_load_dwordx4 a[216:219], v17, %[rb], s41 offen\n\t" \
   "v_mfma_f32_32x32x16_bf16 v[32:47], a[148:151], v[64:67], v[32:47]\n\t" "s_waitcnt vmcnt(15)\n\t" "ds_write_b32 v21, v224 offset:5440\n\t" \
   "ds_write_b32 v21, v225 offset:26560\n\t" "ds_write_b32 v21, v226 offset:16000\n\t" "ds_write_b32 v21, v227 offset:37120\n\t" \
   "s_mov_b32 s41, 82944\n\t" "buffer_load_dwordx4 a[232:235], v17, %[rb], s41 offen\n\t" \
   "v_mfma_f32_32x32x16_bf16 v[32:47], a[152:155], v[64:67], v[32:47]\n\t" "s_waitcnt vmcnt(15)\n\t" "ds_write_b32 v21, v240 offset:5504\n\t" \
   "ds_write_b32 v21, v241 offset:26624\n\t" "ds_write_b32 v21, v242 offset:16064\n\t" "ds_write_b32 v21, v243 offset:37184\n\t" \
   "s_mov_b32 s41, 102400\n\t" "buffer_load_dwordx4 a[248:251], v17, %[rb], s41 offen\n\t" \
   "v_mfma_f32_32x32x16_bf16 v[32:47], a[156:159], v[64:67], v[32:47]\n\t" "s_waitcnt lgkmcnt(0)\n\t" "s_barrier\n\t" LDSTAMP(6) \
   "ds_read_b64_tr_b16 a[160:161], v22 offset:0\n\t" "ds_read_b64_tr_b16 a[162:163], v22 offset:2624\n\t" \
   "ds_read_b64_tr_b16 a[176:177], v22 offset:5248\n\t" "ds_read_b64_tr_b16 a[178:179], v22 offset:7872\n\t" \
   "ds_read_b64_tr_b16 a[192:193], v22 offset:16\n\t" "ds_read_b64_tr_b16 a[194:195], v22 offset:2640\n\t" \
   "ds_read_b64_tr_b16 a[208:209], v22 offset:5264\n\t" "ds_read_b64_tr_b16 a[210:211], v22 offset:7888\n\t" \
   "ds_read_b64_tr_b16 a[224:225], v22 offset:32\n\t" "ds_read_b64_tr_b16 a[226:227], v22 offset:2656\n\t" \
   "ds_read_b64_tr_b16 a[240:241], v22 offset:5280\n\t" "ds_read_b64_tr_b16 a[242:243], v22 offset:7904\n\t" \
   "ds_read_b64_tr_b16 v[192:193], v22 offset:48\n\t" "ds_read_b64_tr_b16 v[194:195], v22 offset:2672\n\t" \
   "ds_read_b64_tr_b16 v[208:209], v22 offset:5296\n\t" "ds_read_b64_tr_b16 v[210:211], v22 offset:7920\n\t" \
   "ds_read_b64_tr_b16 v[224:225], v22 offset:64\n\t" "ds_read_b64_tr_b16 v[226:227], v22 offset:2688\n\t" \
   "ds_read_b64_tr_b16 v[240:241], v22 offset:5312\n\t" "ds_read_b64_tr_b16 v[242:243], v22 offset:7936\n\t" "s_waitcnt vmcnt(15)\n\t" \
   "s_waitcnt lgkmcnt(0)\n\t" "s_barrier\n\t" "ds_write_b32 v21, a164 offset:0\n\t" "ds_write_b32 v21, a165 offset:21120\n\t" \
   "ds_write_b32 v21, a166 offset:10560\n\t" "ds_write_b32 v21, a167 offset:31680\n\t" "s_mov_b32 s41, 102656\n\t" \
   "buffer_load_dwordx4 v[200:203], v17, %[rb], s41 offen\n\t" "s_waitcnt vmcnt(15)\n\t" "ds_write_b32 v21, a180 offset:64\n\t" \
   "ds_write_b32 v21, a181 offset:21184\n\t" "ds_write_b32 v21, a182 offset:10624\n\t" "ds_write_b32 v21, a183 offset:31744\n\t" \
   "s_mov_b32 s41, 102912\n\t" "buffer_load_dwordx4 v[216:219], v17, %[rb], s41 offen\n\t" "s_waitcnt vmcnt(15)\n\t" \
   "ds_write_b32 v21, a196 offset:128\n\t" "ds_write_b32 v21, a197 offset:21248\n\t" "ds_write_b32 v21, a198 offset:10688\n\t" \
   "ds_write_b32 v21, a199 offset:31808\n\t" "s_mov_b32 s41, 103168\n\t" "buffer_load_dwordx4 v[232:235], v17, %[rb], s41 offen\n\t" \
   "s_waitcnt vmcnt(15)\n\t" "ds_write_b32 v21, a212 offset:192\n\t" "ds_write_b32 v21, a213 offset:21312\n\t" \
   "ds_write_b32 v21, a214 offset:10752\n\t" "ds_write_b32 v21, a215 offset:31872\n\t" "s_mov_b32 s41, 103424\n\t" \
   "buffer_load_dwordx4 v[248:251], v17, %[rb], s41 offen\n\t" "s_waitcnt vmcnt(15)\n\t" "ds_write_b32 v21, a228 offset:256\n\t" \
   "ds_write_b32 v21, a229 offset:21376\n\t" "ds_write_b32 v21, a230 offset:10816\n\t" "ds_write_b32 v21, a231 offset:31936\n\t" \
   "s_mov_b32 s41, 122880\n\t" "buffer_load_dwordx4 a[172:175], v17, %[rb], s41 offen\n\t" "s_waitcnt vmcnt(15)\n\t" \
   "ds_write_b32 v21, a244 offset:5248\n\t" "ds_write_b32 v21, a245 offset:26368\n\t" "ds_write_b32 v21, a246 offset:15808\n\t" \
   "ds_write_b32 v21, a247 offset:36928\n\t" "s_mov_b32 s41, 123136\n\t" "buffer_load_dwordx4 a[188:191], v17, %[rb], s41 offen\n\t" \
   "s_waitcnt vmcnt(15)\n\t" "ds_write_b32 v21, v196 offset:5312\n\t" "ds_write_b32 v21, v197 offset:26432\n\t" \
   "ds_write_b32 v21, v198 offset:15872\n\t" "ds_write_b32 v21, v199 offset:36992\n\t" "s_mov_b32 s41, 123392\n\t" \
   "buffer_load_dwordx4 a[204:207], v17, %[rb], s41 offen\n\t" "s_waitcnt vmcnt(15)\n\t" "ds_write_b32 v21, v212 offset:5376\n\t" \
   "ds_write_b32 v21, v213 offset:26496\n\t" "ds_write_b32 v21, v214 offset:15936\n\t" "ds_write_b32 v21, v215 offset:37056\n\t" \
   "s_mov_b32 s41, 123648\n\t" "buffer_load_dwordx4 a[220:223], v17, %[rb], s41 offen\n\t" "s_waitcnt vmcnt(15)\n\t" \
   "ds_write_b32 v21, v228 offset:5440\n\t" "ds_write_b32 v21, v229 offset:26560\n\t" "ds_write_b32 v21, v230 offset:16000\n\t" \
   "ds_write_b32 v21, v231 offset:37120\n\t" "s_mov_b32 s41, 123904\n\t" "buffer_load_dwordx4 a[236:239], v17, %[rb], s41 offen\n\t" \
   "s_waitcnt vmcnt(15)\n\t" "ds_write_b32 v21, v244 offset:5504\n\t" "ds_write_b32 v21, v245 offset:26624\n\t" \
   "ds_write_b32 v21, v246 offset:16064\n\t" "ds_write_b32 v21, v247 offset:37184\n\t" "s_mov_b32 s41, 143360\n\t" \
   "buffer_load_dwordx4 a[252:255], v17, %[rb], s41 offen\n\t" "s_waitcnt lgkmcnt(0)\n\t" "s_barrier\n\t" LDSTAMP(7) \
   "ds_read_b64_tr_b16 a[164:165], v22 offset:0\n\t" "ds_read_b64_tr_b16 a[166:167], v22 offset:2624\n\t" \
   "ds_read_b64_tr_b16 a[180:181], v22 offset:5248\n\t" "ds_read_b64_tr_b16 a[182:183], v22 offset:7872\n\t" \
   "ds_read_b64_tr_b16 a[196:197], v22 offset:16\n\t" "ds_read_b64_tr_b16 a[198:199], v22 offset:2640\n\t" \
   "ds_read_b64_tr_b16 a[212:213], v22 offset:5264\n\t" "ds_read_b64_tr_b16 a[214:215], v22 offset:7888\n\t" \
   "ds_read_b64_tr_b16 a[228:229], v22 offset:32\n\t" "ds_read_b64_tr_b16 a[230:231], v22 offset:2656\n\t" \
   "ds_read_b64_tr_b16 a[244:245], v22 offset:5280\n\t" "ds_read_b64_tr_b16 a[246:247], v22 offset:7904\n\t" \
   "ds_read_b64_tr_b16 v[196:197], v22 offset:48\n\t" "ds_read_b64_tr_b16 v[198:199], v22 offset:2672\n\t" \
   "ds_read_b64_tr_b16 v[212:213], v22 offset:5296\n\t" "ds_read_b64_tr_b16 v[214:215], v22 offset:7920\n\t" \
   "ds_read_b64_tr_b16 v[228:229], v22 offset:64\n\t" "ds_read_b64_tr_b16 v[230:231], v22 offset:2688\n\t" \
   "ds_read_b64_tr_b16 v[244:245], v22 offset:5312\n\t" "ds_read_b64_tr_b16 v[246:247], v22 offset:7936\n\t" "s_waitcnt vmcnt(15)\n\t" \
   "s_waitcnt lgkmcnt(0)\n\t" "s_barrier\n\t" "ds_write_b32 v21, a168 offset:0\n\t" "ds_write_b32 v21, a169 offset:21120\n\t" \
   "ds_write_b32 v21, a170 offset:10560\n\t" "ds_write_b32 v21, a171 offset:31680\n\t" "s_mov_b32 s41, 143616\n\t" \
   "buffer_load_dwordx4 v[204:207], v17, %[rb], s41 offen\n\t" "s_waitcnt vmcnt(15)\n\t" "ds_write_b32 v21, a184 offset:64\n\t" \
   "ds_write_b32 v21, a185 offset:21184\n\t" "ds_write_b32 v21, a186 offset:10624\n\t" "ds_write_b32 v21, a187 offset:31744\n\t" \
   "s_mov_b32 s41, 143872\n\t" "buffer_load_dwordx4 v[220:223], v17, %[rb], s41 offen\n\t" "s_waitcnt vmcnt(15)\n\t" \
   "ds_write_b32 v21, a200 offset:128\n\t" "ds_write_b32 v21, a201 offset:21248\n\t" "ds_write_b32 v21, a202 offset:10688\n\t" \
   "ds_write_b32 v21, a203 offset:31808\n\t" "s_mov_b32 s41, 144128\n\t" "buffer_load_dwordx4 v[236:239], v17, %[rb], s41 offen\n\t" \
   "s_waitcnt vmcnt(15)\n\t" "ds_write_b32 v21, a216 offset:192\n\t" "ds_write_b32 v21, a217 offset:21312\n\t" \
   "ds_write_b32 v21, a218 offset:10752\n\t" "ds_write_b32 v21, a219 offset:31872\n\t" "s_mov_b32 s41, 144384\n\t" \
   "buffer_load_dwordx4 v[252:255], v17, %[rb], s41 offen\n\t" "s_waitcnt vmcnt(15)\n\t" "ds_write_b32 v21, a232 offset:256\n\t" \
   "ds_write_b32 v21, a233 offset:21376\n\t" "ds_write_b32 v21, a234 offset:10816\n\t" "ds_write_b32 v21, a235 offset:31936\n\t" \
   "s_waitcnt vmcnt(14)\n\t" "ds_write_b32 v21, a248 offset:5248\n\t" "ds_write_b32 v21, a249 offset:26368\n\t" \
   "ds_write_b32 v21, a250 offset:15808\n\t" "ds_write_b32 v21, a251 offset:36928\n\t" "s_waitcnt vmcnt(13)\n\t" \
   "ds_write_b32 v21, v200 offset:5312\n\t" "ds_write_b32 v21, v201 offset:26432\n\t" "ds_write_b32 v21, v202 offset:15872\n\t" \
   "ds_write_b32 v21, v203 offset:36992\n\t" "s_waitcnt vmcnt(12)\n\t" "ds_write_b32 v21, v216 offset:5376\n\t" \
   "ds_write_b32 v21, v217 offset:26496\n\t" "ds_write_b32 v21, v218 offset:15936\n\t" "ds_write_b32 v21, v219 offset:37056\n\t" \
   "s_waitcnt vmcnt(11)\n\t" "ds_write_b32 v21, v232 offset:5440\n\t" "ds_write_b32 v21, v233 offset:26560\n\t" \
   "ds_write_b32 v21, v234 offset:16000\n\t" "ds_write_b32 v21, v235 offset:37120\n\t" "s_waitcnt vmcnt(10)\n\t" \
   "ds_write_b32 v21, v248 offset:5504\n\t" "ds_write_b32 v21, v249 offset:26624\n\t" "ds_write_b32 v21, v250 offset:16064\n\t" \
   "ds_write_b32 v21, v251 offset:37184\n\t" "buffer_load_dwordx4 v[28:31], v112, %[rw], 0 offen sc1\n\t" "s_waitcnt lgkmcnt(0)\n\t" "s_barrier\n\t" \
   LDSTAMP(8) "ds_read_b64_tr_b16 a[168:169], v22 offset:0\n\t" "ds_read_b64_tr_b16 a[170:171], v22 offset:2624\n\t" \
   "ds_read_b64_tr_b16 a[184:185], v22 offset:5248\n\t" "ds_read_b64_tr_b16 a[186:187], v22 offset:7872\n\t" \
   "ds_read_b64_tr_b16 a[200:201], v22 offset:16\n\t" "ds_read_b64_tr_b16 a[202:203], v22 offset:2640\n\t" \
   "ds_read_b64_tr_b16 a[216:217], v22 offset:5264\n\t" "ds_read_b64_tr_b16 a[218:219], v22 offset:7888\n\t" \
   "ds_read_b64_tr_b16 a[232:233], v22 offset:32\n\t" "ds_read_b64_tr_b16 a[234:235], v22 offset:2656\n\t" \
   "ds_read_b64_tr_b16 a[248:249], v22 offset:5280\n\t" "ds_read_b64_tr_b16 a[250:251], v22 offset:7904\n\t" \
   "ds_read_b64_tr_b16 v[200:201], v22 offset:48\n\t" "ds_read_b64_tr_b16 v[202:203], v22 offset:2672\n\t" \
   "ds_read_b64_tr_b16 v[216:217], v22 offset:5296\n\t" "ds_read_b64_tr_b16 v[218:219], v22 offset:7920\n\t" \
   "ds_read_b64_tr_b16 v[232:233], v22 offset:64\n\t" "ds_read_b64_tr_b16 v[234:235], v22 offset:2688\n\t" \
   "ds_read_b64_tr_b16 v[248:249], v22 offset:5312\n\t" "ds_read_b64_tr_b16 v[250:251], v22 offset:7936\n\t" "s_waitcnt vmcnt(10)\n\t" \
   "s_waitcnt lgkmcnt(0)\n\t" "s_barrier\n\t" "ds_write_b32 v21, a172 offset:0\n\t" "ds_write_b32 v21, a173 offset:21120\n\t" \
   "ds_write_b32 v21, a174 offset:10560\n\t" "ds_write_b32 v21, a175 offset:31680\n\t" "s_waitcnt vmcnt(9)\n\t" "ds_write_b32 v21, a188 offset:64\n\t" \
   "ds_write_b32 v21, a189 offset:21184\n\t" "ds_write_b32 v21, a190 offset:10624\n\t" "ds_write_b32 v21, a191 offset:31744\n\t" \
   "s_waitcnt vmcnt(8)\n\t" "ds_write_b32 v21, a204 offset:128\n\t" "ds_write_b32 v21, a205 offset:21248\n\t" "ds_write_b32 v21, a206 offset:10688\n\t" \
   "ds_write_b32 v21, a207 offset:31808\n\t" "s_waitcnt vmcnt(7)\n\t" "ds_write_b32 v21, a220 offset:192\n\t" "ds_write_b32 v21, a221 offset:21312\n\t" \
   "ds_write_b32 v21, a222 offset:10752\n\t" "ds_write_b32 v21, a223 offset:31872\n\t" "s_waitcnt vmcnt(6)\n\t" "ds_write_b32 v21, a236 offset:256\n\t" \
   "ds_write_b32 v21, a237 offset:21376\n\t" "ds_write_b32 v21, a238 offset:10816\n\t" "ds_write_b32 v21, a239 offset:31936\n\t" \
   "s_waitcnt vmcnt(5)\n\t" "ds_write_b32 v21, a252 offset:5248\n\t" "ds_write_b32 v21, a253 offset:26368\n\t" \
   "ds_write_b32 v21, a254 offset:15808\n\t" "ds_write_b32 v21, a255 offset:36928\n\t" "s_waitcnt vmcnt(4)\n\t" \
   "ds_write_b32 v21, v204 offset:5312\n\t" "ds_write_b32 v21, v205 offset:26432\n\t" "ds_write_b32 v21, v206 offset:15872\n\t" \
   "ds_write_b32 v21, v207 offset:36992\n\t" "s_waitcnt vmcnt(3)\n\t" "ds_write_b32 v21, v220 offset:5376\n\t" \
   "ds_write_b32 v21, v221 offset:26496\n\t" "ds_write_b32 v21, v222 offset:15936\n\t" "ds_write_b32 v21, v223 offset:37056\n\t" \
   "s_waitcnt vmcnt(2)\n\t" "ds_write_b32 v21, v236 offset:5440\n\t" "ds_write_b32 v21, v237 offset:26560\n\t" \
   "ds_write_b32 v21, v238 offset:16000\n\t" "ds_write_b32 v21, v239 offset:37120\n\t" "s_waitcnt vmcnt(1)\n\t" \
   "ds_write_b32 v21, v252 offset:5504\n\t" "ds_write_b32 v21, v253 offset:26624\n\t" "ds_write_b32 v21, v254 offset:16064\n\t" \
   "ds_write_b32 v21, v255 offset:37184\n\t" "s_waitcnt lgkmcnt(0)\n\t" "s_barrier\n\t" LDSTAMP(9) "ds_read_b64_tr_b16 a[172:173], v22 offset:0\n\t" \
   "ds_read_b64_tr_b16 a[174:175], v22 offset:2624\n\t" "ds_read_b64_tr_b16 a[188:189], v22 offset:5248\n\t" \
   "ds_read_b64_tr_b16 a[190:191], v22 offset:7872\n\t" "ds_read_b64_tr_b16 a[204:205], v22 offset:16\n\t" \
   "ds_read_b64_tr_b16 a[206:207], v22 offset:2640\n\t" "ds_read_b64_tr_b16 a[220:221], v22 offset:5264\n\t" \
   "ds_read_b64_tr_b16 a[222:223], v22 offset:7888\n\t" "ds_read_b64_tr_b16 a[236:237], v22 offset:32\n\t" \
   "ds_read_b64_tr_b16 a[238:239], v22 offset:2656\n\t" "ds_read_b64_tr_b16 a[252:253], v22 offset:5280\n\t" \
   "ds_read_b64_tr_b16 a[254:255], v22 offset:7904\n\t" "ds_read_b64_tr_b16 v[204:205], v22 offset:48\n\t" \
   "ds_read_b64_tr_b16 v[206:207], v22 offset:2672\n\t" "ds_read_b64_tr_b16 v[220:221], v22 offset:5296\n\t" \
   "ds_read_b64_tr_b16 v[222:223], v22 offset:7920\n\t" "ds_read_b64_tr_b16 v[236:237], v22 offset:64\n\t" \
   "ds_read_b64_tr_b16 v[238:239], v22 offset:2688\n\t" "ds_read_b64_tr_b16 v[252:253], v22 offset:5312\n\t" \
   "ds_read_b64_tr_b16 v[254:255], v22 offset:7936\n\t" "s_waitcnt vmcnt(0) lgkmcnt(0)\n\t" "s_mov_b32 exec_lo, 1\n\t" "s_mov_b32 exec_hi, 1\n\t" \
   "s_nop 7\n\t" "s_nop 3\n\t" "ds_write_b128 v27, v[32:35] offset:0\n\t" "ds_write_b128 v27, v[36:39] offset:16\n\t" \
   "ds_write_b128 v27, v[40:43] offset:32\n\t" "ds_write_b128 v27, v[44:47] offset:48\n\t" "ds_write_b128 v27, v[48:51] offset:64\n\t" \
   "ds_write_b128 v27, v[52:55] offset:80\n\t" "ds_write_b128 v27, v[56:59] offset:96\n\t" "ds_write_b128 v27, v[60:63] offset:112\n\t" \
   "s_mov_b64 exec, -1\n\t" "s_waitcnt lgkmcnt(0)\n\t" "s_barrier"
#define LDV_CLOB "v24", "v25", "v26", "v32", "v33", "v34", "v35", "v36", "v37", "v38", "v39", "v40", "v41", "v42", "v43", "v44", "v45", "v46", "v47", "v48", "v49", "v50", "v51", "v52", "v53", "v54", "v55", "v56", "v57", "v58", "v59", "v60", "v61", "v62", "v63", "v64", "v65", "v66", "v67", "v68", "v69", "v70", "v71", "v72", "v73", "v74", "v75", "v76", "v77", "v78", "v79", "v80", "v81", "v82", "v83", "v84", "v85", "v86", "v87", "v88", "v89", "v90", "v91", "v92", "v93", "v94", "v95", "v96", "v97", "v98", "v99", "v100", "v101", "v102", "v103", "v104", "v105", "v106", "v107", "v108", "v109", "v110", "v111", "v120", "v121", "v122", "v123", "s40", "s41"
#define LDY_ASM \
   "buffer_load_dword v26, v113, %[rw], 0 offen sc1\n\t" "s_mov_b32 s41, 0\n\t" "buffer_load_dwordx4 a[0:3], v16, %[ra], s41 offen\n\t" \
   "s_mov_b32 s41, 131072\n\t" "buffer_load_dwordx4 a[64:67], v16, %[ra], s41 offen\n\t" "s_mov_b32 s41, 524288\n\t" \
   "buffer_load_dwordx4 a[8:11], v16, %[ra], s41 offen\n\t" "s_mov_b32 s41, 655360\n\t" "buffer_load_dwordx4 a[72:75], v16, %[ra], s41 offen\n\t" \
   "s_mov_b32 s41, 1048576\n\t" "buffer_load_dwordx4 a[16:19], v16, %[ra], s41 offen\n\t" "s_mov_b32 s41, 1179648\n\t" \
   "buffer_load_dwordx4 a[80:83], v16, %[ra], s41 offen\n\t" "s_mov_b32 s41, 1572864\n\t" "buffer_load_dwordx4 a[24:27], v16, %[ra], s41 offen\n\t" \
   "s_mov_b32 s41, 1703936\n\t" "buffer_load_dwordx4 a[88:91], v16, %[ra], s41 offen\n\t" "s_mov_b32 s41, 2097152\n\t" \
   "buffer_load_dwordx4 a[32:35], v16, %[ra], s41 offen\n\t" "s_mov_b32 s41, 2228224\n\t" "buffer_load_dwordx4 a[96:99], v16, %[ra], s41 offen\n\t" \
   "s_mov_b32 s41, 2621440\n\t" "buffer_load_dwordx4 a[40:43], v16, %[ra], s41 offen\n\t" "s_mov_b32 s41, 2752512\n\t" \
   "buffer_load_dwordx4 a[104:107], v16, %[ra], s41 offen\n\t" "s_mov_b32 s41, 3145728\n\t" "buffer_load_dwordx4 a[48:51], v16, %[ra], s41 offen\n\t" \
   "s_mov_b32 s41, 3276800\n\t" "buffer_load_dwordx4 a[112:115], v16, %[ra], s41 offen\n\t" "s_mov_b32 s41, 3670016\n\t" \
   "buffer_load_dwordx4 a[56:59], v16, %[ra], s41 offen\n\t" "s_mov_b32 s41, 3801088\n\t" "buffer_load_dwordx4 a[120:123], v16, %[ra], s41 offen\n\t" \
   "s_waitcnt vmcnt(16)\n\t" "v_add_u32 v26, 1, v26\n\t" "buffer_store_dword v26, v114, %[rw], 0 offen sc1\n\t" "ds_write_b32 v115, v26\n\t" LDSTAMP(0) \
   "s_waitcnt vmcnt(15)\n\t" LDSTAMP(1) "ds_write_b128 v20, a[0:3] offset:0\n\t" "s_mov_b32 s41, 4194304\n\t" \
   "buffer_load_dwordx4 v[192:195], v16, %[ra], s41 offen\n\t" "s_waitcnt vmcnt(15)\n\t" "ds_write_b128 v20, a[64:67] offset:4096\n\t" \
   "s_mov_b32 s41, 4325376\n\t" "buffer_load_dwordx4 v[208:211], v16, %[ra], s41 offen\n\t" "s_waitcnt vmcnt(15)\n\t" \
   "ds_write_b128 v20, a[8:11] offset:8192\n\t" "s_mov_b32 s41, 4718592\n\t" "buffer_load_dwordx4 v[200:203], v16, %[ra], s41 offen\n\t" \
   "s_waitcnt vmcnt(15)\n\t" "ds_write_b128 v20, a[72:75] offset:12288\n\t" "s_mov_b32 s41, 4849664\n\t" \
   "buffer_load_dwordx4 v[216:219], v16, %[ra], s41 offen\n\t" "s_waitcnt vmcnt(15)\n\t" "ds_write_b128 v20, a[16:19] offset:16384\n\t" \
   "s_mov_b32 s41, 262144\n\t" "buffer_load_dwordx4 a[4:7], v16, %[ra], s41 offen\n\t" "s_waitcnt vmcnt(15)\n\t" \
   "ds_write_b128 v20, a[80:83] offset:20480\n\t" "s_mov_b32 s41, 393216\n\t" "buffer_load_dwordx4 a[68:71], v16, %[ra], s41 offen\n\t" \
   "s_waitcnt vmcnt(15)\n\t" "ds_write_b128 v20, a[24:27] offset:24576\n\t" "s_mov_b32 s41, 786432\n\t" \
   "buffer_load_dwordx4 a[12:15], v16, %[ra], s41 offen\n\t" "s_waitcnt vmcnt(15)\n\t" "ds_write_b128 v20, a[88:91] offset:28672\n\t" \
   "s_mov_b32 s41, 917504\n\t" "buffer_load_dwordx4 a[76:79], v16, %[ra], s41 offen\n\t" "s_waitcnt vmcnt(15)\n\t" \
   "ds_write_b128 v20, a[32:35] offset:32768\n\t" "s_mov_b32 s41, 1310720\n\t" "buffer_load_dwordx4 a[20:23], v16, %[ra], s41 offen\n\t" \
   "s_waitcnt vmcnt(15)\n\t" "ds_write_b128 v20, a[96:99] offset:36864\n\t" "s_mov_b32 s41, 1441792\n\t" \
   "buffer_load_dwordx4 a[84:87], v16, %[ra], s41 offen\n\t" "s_waitcnt vmcnt(15)\n\t" "ds_write_b128 v20, a[40:43] offset:40960\n\t" \
   "s_mov_b32 s41, 1835008\n\t" "buffer_load_dwordx4 a[28:31], v16, %[ra], s41 offen\n\t" "s_waitcnt vmcnt(15)\n\t" \
   "ds_write_b128 v20, a[104:107] offset:45056\n\t" "s_mov_b32 s41, 1966080\n\t" "buffer_load_dwordx4 a[92:95], v16, %[ra], s41 offen\n\t" \
   "s_waitcnt vmcnt(15)\n\t" "ds_write_b128 v20, a[48:51] offset:49152\n\t" "s_mov_b32 s41, 2359296\n\t" \
   "buffer_load_dwordx4 a[36:39], v16, %[ra], s41 offen\n\t" "s_waitcnt vmcnt(15)\n\t" "ds_write_b128 v20, a[112:115] offset:53248\n\t" \
   "s_mov_b32 s41, 2490368\n\t" "buffer_load_dwordx4 a[100:103], v16, %[ra], s41 offen\n\t" "s_waitcnt vmcnt(15)\n\t" \
   "ds_write_b128 v20, a[56:59] offset:57344\n\t" "s_mov_b32 s41, 2883584\n\t" "buffer_load_dwordx4 a[44:47], v16, %[ra], s41 offen\n\t" \
   "s_waitcnt vmcnt(15)\n\t" "ds_write_b128 v20, a[120:123] offset:61440\n\t" "s_mov_b32 s41, 3014656\n\t" \
   "buffer_load_dwordx4 a[108:111], v16, %[ra], s41 offen\n\t" "s_waitcnt vmcnt(15)\n\t" "ds_write_b128 v23, v[192:195] offset:0\n\t" \
   "s_mov_b32 s41, 3407872\n\t" "buffer_load_dwordx4 a[52:55], v16, %[ra], s41 offen\n\t" "s_waitcnt vmcnt(15)\n\t" \
   "ds_write_b128 v23, v[208:211] offset:4096\n\t" "s_mov_b32 s41, 3538944\n\t" "buffer_load_dwordx4 a[116:119], v16, %[ra], s41 offen\n\t" \
   "s_waitcnt vmcnt(15)\n\t" "ds_write_b128 v23, v[200:203] offset:8192\n\t" "s_mov_b32 s41, 3932160\n\t" \
   "buffer_load_dwordx4 a[60:63], v16, %[ra], s41 offen\n\t" "s_waitcnt vmcnt(15)\n\t" "ds_write_b128 v23, v[216:219] offset:12288\n\t" \
   "s_mov_b32 s41, 4063232\n\t" "buffer_load_dwordx4 a[124:127], v16, %[ra], s41 offen\n\t" "s_waitcnt lgkmcnt(0)\n\t" "s_barrier\n\t" LDSTAMP(2) \
   "s_lshl_b32 s40, %[p0], 13\n\t" "v_add_u32 v24, s40, v18\n\t" "v_add_u32 v25, s40, v19\n\t" "s_add_u32 s40, s40, 8192\n\t" \
   "s_cmp_eq_u32 s40, 81920\n\t" "s_cselect_b32 s40, 0, s40\n\t" "ds_read_b64_tr_b16 a[0:1], v24 offset:0\n\t" \
   "ds_read_b64_tr_b16 a[2:3], v24 offset:4096\n\t" "ds_read_b64_tr_b16 a[64:65], v25 offset:0\n\t" "ds_read_b64_tr_b16 a[66:67], v25 offset:4096\n\t" \
   "v_add_u32 v24, s40, v18\n\t" "v_add_u32 v25, s40, v19\n\t" "s_add_u32 s40, s40, 8192\n\t" "s_cmp_eq_u32 s40, 81920\n\t" \
   "s_cselect_b32 s40, 0, s40\n\t" "ds_read_b64_tr_b16 a[8:9], v24 offset:0\n\t" "ds_read_b64_tr_b16 a[10:11], v24 offset:4096\n\t" \
   "ds_read_b64_tr_b16 a[72:73], v25 offset:0\n\t" "ds_read_b64_tr_b16 a[74:75], v25 offset:4096\n\t" "v_add_u32 v24, s40, v18\n\t" \
   "v_add_u32 v25, s40, v19\n\t" "s_add_u32 s40, s40, 8192\n\t" "s_cmp_eq_u32 s40, 81920\n\t" "s_cselect_b32 s40, 0, s40\n\t" \
   "ds_read_b64_tr_b16 a[16:17], v24 offset:0\n\t" "ds_read_b64_tr_b16 a[18:19], v24 offset:4096\n\t" "ds_read_b64_tr_b16 a[80:81], v25 offset:0\n\t" \
   "ds_read_b64_tr_b16 a[82:83], v25 offset:4096\n\t" "v_add_u32 v24, s40, v18\n\t" "v_add_u32 v25, s40, v19\n\t" "s_add_u32 s40, s40, 8192\n\t" \
   "s_cmp_eq_u32 s40, 81920\n\t" "s_cselect_b32 s40, 0, s40\n\t" "ds_read_b64_tr_b16 a[24:25], v24 offset:0\n\t" \
   "ds_read_b64_tr_b16 a[26:27], v24 offset:4096\n\t" "ds_read_b64_tr_b16 a[88:89], v25 offset:0\n\t" \
   "ds_read_b64_tr_b16 a[90:91], v25 offset:4096\n\t" "v_add_u32 v24, s40, v18\n\t" "v_add_u32 v25, s40, v19\n\t" "s_add_u32 s40, s40, 8192\n\t" \
   "s_cmp_eq_u32 s40, 81920\n\t" "s_cselect_b32 s40, 0, s40\n\t" "ds_read_b64_tr_b16 a[32:33], v24 offset:0\n\t" \
   "ds_read_b64_tr_b16 a[34:35], v24 offset:4096\n\t" "ds_read_b64_tr_b16 a[96:97], v25 offset:0\n\t" \
   "ds_read_b64_tr_b16 a[98:99], v25 offset:4096\n\t" "v_add_u32 v24, s40, v18\n\t" "v_add_u32 v25, s40, v19\n\t" "s_add_u32 s40, s40, 8192\n\t" \
   "s_cmp_eq_u32 s40, 81920\n\t" "s_cselect_b32 s40, 0, s40\n\t" "ds_read_b64_tr_b16 a[40:41], v24 offset:0\n\t" \
   "ds_read_b64_tr_b16 a[42:43], v24 offset:4096\n\t" "ds_read_b64_tr_b16 a[104:105], v25 offset:0\n\t" \
   "ds_read_b64_tr_b16 a[106:107], v25 offset:4096\n\t" "v_add_u32 v24, s40, v18\n\t" "v_add_u32 v25, s40, v19\n\t" "s_add_u32 s40, s40, 8192\n\t" \
   "s_cmp_eq_u32 s40, 81920\n\t" "s_cselect_b32 s40, 0, s40\n\t" "ds_read_b64_tr_b16 a[48:49], v24 offset:0\n\t" \
   "ds_read_b64_tr_b16 a[50:51], v24 offset:4096\n\t" "ds_read_b64_tr_b16 a[112:113], v25 offset:0\n\t" \
   "ds_read_b64_tr_b16 a[114:115], v25 offset:4096\n\t" "v_add_u32 v24, s40, v18\n\t" "v_add_u32 v25, s40, v19\n\t" "s_add_u32 s40, s40, 8192\n\t" \
   "s_cmp_eq_u32 s40, 81920\n\t" "s_cselect_b32 s40, 0, s40\n\t" "ds_read_b64_tr_b16 a[56:57], v24 offset:0\n\t" \
   "ds_read_b64_tr_b16 a[58:59], v24 offset:4096\n\t" "ds_read_b64_tr_b16 a[120:121], v25 offset:0\n\t" \
   "ds_read_b64_tr_b16 a[122:123], v25 offset:4096\n\t" "v_add_u32 v24, s40, v18\n\t" "v_add_u32 v25, s40, v19\n\t" "s_add_u32 s40, s40, 8192\n\t" \
   "s_cmp_eq_u32 s40, 81920\n\t" "s_cselect_b32 s40, 0, s40\n\t" "ds_read_b64_tr_b16 v[192:193], v24 offset:0\n\t" \
   "ds_read_b64_tr_b16 v[194:195], v24 offset:4096\n\t" "ds_read_b64_tr_b16 v[208:209], v25 offset:0\n\t" \
   "ds_read_b64_tr_b16 v[210:211], v25 offset:4096\n\t" "v_add_u32 v24, s40, v18\n\t" "v_add_u32 v25, s40, v19\n\t" "s_add_u32 s40, s40, 8192\n\t" \
   "s_cmp_eq_u32 s40, 81920\n\t" "s_cselect_b32 s40, 0, s40\n\t" "ds_read_b64_tr_b16 v[200:201], v24 offset:0\n\t" \
   "ds_read_b64_tr_b16 v[202:203], v24 offset:4096\n\t" "ds_read_b64_tr_b16 v[216:217], v25 offset:0\n\t" \
   "ds_read_b64_tr_b16 v[218:219], v25 offset:4096\n\t" "s_waitcnt vmcnt(15)\n\t" "s_waitcnt lgkmcnt(0)\n\t" "s_barrier\n\t" \
   "ds_write_b128 v20, a[4:7] offset:0\n\t" "s_mov_b32 s41, 4456448\n\t" "buffer_load_dwordx4 v[196:199], v16, %[ra], s41 offen\n\t" \
   "s_waitcnt vmcnt(15)\n\t" "ds_write_b128 v20, a[68:71] offset:4096\n\t" "s_mov_b32 s41, 4587520\n\t" \
   "buffer_load_dwordx4 v[212:215], v16, %[ra], s41 offen\n\t" "s_waitcnt vmcnt(15)\n\t" "ds_write_b128 v20, a[12:15] offset:8192\n\t" \
   "s_mov_b32 s41, 4980736\n\t" "buffer_load_dwordx4 v[204:207], v16, %[ra], s41 offen\n\t" "s_waitcnt vmcnt(15)\n\t" \
   "ds_write_b128 v20, a[76:79] offset:12288\n\t" "s_mov_b32 s41, 5111808\n\t" "buffer_load_dwordx4 v[220:223], v16, %[ra], s41 offen\n\t" \
   "s_waitcnt vmcnt(15)\n\t" "ds_write_b128 v20, a[20:23] offset:16384\n\t" "s_mov_b32 s41, 0\n\t" \
   "buffer_load_dwordx4 a[128:131], v17, %[rb], s41 offen\n\t" "s_waitcnt vmcnt(15)\n\t" "ds_write_b128 v20, a[84:87] offset:20480\n\t" \
   "s_mov_b32 s41, 256\n\t" "buffer_load_dwordx4 a[132:135], v17, %[rb], s41 offen\n\t" "s_waitcnt vmcnt(15)\n\t" \
   "ds_write_b128 v20, a[28:31] offset:24576\n\t" "s_mov_b32 s41, 512\n\t" "buffer_load_dwordx4 a[160:163], v17, %[rb], s41 offen\n\t" \
   "s_waitcnt vmcnt(15)\n\t" "ds_write_b128 v20, a[92:95] offset:28672\n\t" "s_mov_b32 s41, 768\n\t" \
   "buffer_load_dwordx4 a[164:167], v17, %[rb], s41 offen\n\t" "s_waitcnt vmcnt(15)\n\t" "ds_write_b128 v20, a[36:39] offset:32768\n\t" \
   "s_mov_b32 s41, 1024\n\t" "buffer_load_dwordx4 a[192:195], v17, %[rb], s41 offen\n\t" "s_waitcnt vmcnt(15)\n\t" \
   "ds_write_b128 v20, a[100:103] offset:36864\n\t" "s_mov_b32 s41, 20480\n\t" "buffer_load_dwordx4 a[196:199], v17, %[rb], s41 offen\n\t" \
   "s_waitcnt vmcnt(15)\n\t" "ds_write_b128 v20, a[44:47] offset:40960\n\t" "s_mov_b32 s41, 20736\n\t" \
   "buffer_load_dwordx4 a[224:227], v17, %[rb], s41 offen\n\t" "s_waitcnt vmcnt(15)\n\t" "ds_write_b128 v20, a[108:111] offset:45056\n\t" \
   "s_mov_b32 s41, 20992\n\t" "buffer_load_dwordx4 a[228:231], v17, %[rb], s41 offen\n\t" "s_waitcnt vmcnt(15)\n\t" \
   "ds_write_b128 v20, a[52:55] offset:49152\n\t" "s_mov_b32 s41, 21248\n\t" "buffer_load_dwordx4 v[224:227], v17, %[rb], s41 offen\n\t" \
   "s_waitcnt vmcnt(15)\n\t" "ds_write_b128 v20, a[116:119] offset:53248\n\t" "s_mov_b32 s41, 21504\n\t" \
   "buffer_load_dwordx4 v[228:231], v17, %[rb], s41 offen\n\t" "s_waitcnt vmcnt(15)\n\t" "ds_write_b128 v20, a[60:63] offset:57344\n\t" \
   "s_mov_b32 s41, 40960\n\t" "buffer_load_dwordx4 a[136:139], v17, %[rb], s41 offen\n\t" "s_waitcnt vmcnt(15)\n\t" \
   "ds_write_b128 v20, a[124:127] offset:61440\n\t" "s_mov_b32 s41, 41216\n\t" "buffer_load_dwordx4 a[140:143], v17, %[rb], s41 offen\n\t" \
   "s_waitcnt vmcnt(15)\n\t" "ds_write_b128 v23, v[196:199] offset:0\n\t" "s_mov_b32 s41, 41472\n\t" \
   "buffer_load_dwordx4 a[168:171], v17, %[rb], s41 offen\n\t" "s_waitcnt vmcnt(15)\n\t" "ds_write_b128 v23, v[212:215] offset:4096\n\t" \
   "s_mov_b32 s41, 41728\n\t" "buffer_load_dwordx4 a[172:175], v17, %[rb], s41 offen\n\t" "s_waitcnt vmcnt(15)\n\t" \
   "ds_write_b128 v23, v[204:207] offset:8192\n\t" "s_mov_b32 s41, 41984\n\t" "buffer_load_dwordx4 a[200:203], v17, %[rb], s41 offen\n\t" \
   "s_waitcnt vmcnt(15)\n\t" "ds_write_b128 v23, v[220:223] offset:12288\n\t" "s_mov_b32 s41, 61440\n\t" \
   "buffer_load_dwordx4 a[204:207], v17, %[rb], s41 offen\n\t" "s_waitcnt lgkmcnt(0)\n\t" "s_barrier\n\t" LDSTAMP(3) "s_lshl_b32 s40, %[p0], 13\n\t" \
   "v_add_u32 v24, s40, v18\n\t" "v_add_u32 v25, s40, v19\n\t" "s_add_u32 s40, s40, 8192\n\t" "s_cmp_eq_u32 s40, 81920\n\t" \
   "s_cselect_b32 s40, 0, s40\n\t" "ds_read_b64_tr_b16 a[4:5], v24 offset:0\n\t" "ds_read_b64_tr_b16 a[6:7], v24 offset:4096\n\t" \
   "ds_read_b64_tr_b16 a[68:69], v25 offset:0\n\t" "ds_read_b64_tr_b16 a[70:71], v25 offset:4096\n\t" "v_add_u32 v24, s40, v18\n\t" \
   "v_add_u32 v25, s40, v19\n\t" "s_add_u32 s40, s40, 8192\n\t" "s_cmp_eq_u32 s40, 81920\n\t" "s_cselect_b32 s40, 0, s40\n\t" \
   "ds_read_b64_tr_b16 a[12:13], v24 offset:0\n\t" "ds_read_b64_tr_b16 a[14:15], v24 offset:4096\n\t" "ds_read_b64_tr_b16 a[76:77], v25 offset:0\n\t" \
   "ds_read_b64_tr_b16 a[78:79], v25 offset:4096\n\t" "v_add_u32 v24, s40, v18\n\t" "v_add_u32 v25, s40, v19\n\t" "s_add_u32 s40, s40, 8192\n\t" \
   "s_cmp_eq_u32 s40, 81920\n\t" "s_cselect_b32 s40, 0, s40\n\t" "ds_read_b64_tr_b16 a[20:21], v24 offset:0\n\t" \
   "ds_read_b64_tr_b16 a[22:23], v24 offset:4096\n\t" "ds_read_b64_tr_b16 a[84:85], v25 offset:0\n\t" \
   "ds_read_b64_tr_b16 a[86:87], v25 offset:4096\n\t" "v_add_u32 v24, s40, v18\n\t" "v_add_u32 v25, s40, v19\n\t" "s_add_u32 s40, s40, 8192\n\t" \
   "s_cmp_eq_u32 s40, 81920\n\t" "s_cselect_b32 s40, 0, s40\n\t" "ds_read_b64_tr_b16 a[28:29], v24 offset:0\n\t" \
   "ds_read_b64_tr_b16 a[30:31], v24 offset:4096\n\t" "ds_read_b64_tr_b16 a[92:93], v25 offset:0\n\t" \
   "ds_read_b64_tr_b16 a[94:95], v25 offset:4096\n\t" "v_add_u32 v24, s40, v18\n\t" "v_add_u32 v25, s40, v19\n\t" "s_add_u32 s40, s40, 8192\n\t" \
   "s_cmp_eq_u32 s40, 81920\n\t" "s_cselect_b32 s40, 0, s40\n\t" "ds_read_b64_tr_b16 a[36:37], v24 offset:0\n\t" \
   "ds_read_b64_tr_b16 a[38:39], v24 offset:4096\n\t" "ds_read_b64_tr_b16 a[100:101], v25 offset:0\n\t" \
   "ds_read_b64_tr_b16 a[102:103], v25 offset:4096\n\t" "v_add_u32 v24, s40, v18\n\t" "v_add_u32 v25, s40, v19\n\t" "s_add_u32 s40, s40, 8192\n\t" \
   "s_cmp_eq_u32 s40, 81920\n\t" "s_cselect_b32 s40, 0, s40\n\t" "ds_read_b64_tr_b16 a[44:45], v24 offset:0\n\t" \
   "ds_read_b64_tr_b16 a[46:47], v24 offset:4096\n\t" "ds_read_b64_tr_b16 a[108:109], v25 offset:0\n\t" \
   "ds_read_b64_tr_b16 a[110:111], v25 offset:4096\n\t" "v_add_u32 v24, s40, v18\n\t" "v_add_u32 v25, s40, v19\n\t" "s_add_u32 s40, s40, 8192\n\t" \
   "s_cmp_eq_u32 s40, 81920\n\t" "s_cselect_b32 s40, 0, s40\n\t" "ds_read_b64_tr_b16 a[52:53], v24 offset:0\n\t" \
   "ds_read_b64_tr_b16 a[54:55], v24 offset:4096\n\t" "ds_read_b64_tr_b16 a[116:117], v25 offset:0\n\t" \
   "ds_read_b64_tr_b16 a[118:119], v25 offset:4096\n\t" "v_add_u32 v24, s40, v18\n\t" "v_add_u32 v25, s40, v19\n\t" "s_add_u32 s40, s40, 8192\n\t" \
   "s_cmp_eq_u32 s40, 81920\n\t" "s_cselect_b32 s40, 0, s40\n\t" "ds_read_b64_tr_b16 a[60:61], v24 offset:0\n\t" \
   "ds_read_b64_tr_b16 a[62:63], v24 offset:4096\n\t" "ds_read_b64_tr_b16 a[124:125], v25 offset:0\n\t" \
   "ds_read_b64_tr_b16 a[126:127], v25 offset:4096\n\t" "v_add_u32 v24, s40, v18\n\t" "v_add_u32 v25, s40, v19\n\t" "s_add_u32 s40, s40, 8192\n\t" \
   "s_cmp_eq_u32 s40, 81920\n\t" "s_cselect_b32 s40, 0, s40\n\t" "ds_read_b64_tr_b16 v[196:197], v24 offset:0\n\t" \
   "ds_read_b64_tr_b16 v[198:199], v24 offset:4096\n\t" "ds_read_b64_tr_b16 v[212:213], v25 offset:0\n\t" \
   "ds_read_b64_tr_b16 v[214:215], v25 offset:4096\n\t" "v_add_u32 v24, s40, v18\n\t" "v_add_u32 v25, s40, v19\n\t" "s_add_u32 s40, s40, 8192\n\t" \
   "s_cmp_eq_u32 s40, 81920\n\t" "s_cselect_b32 s40, 0, s40\n\t" "ds_read_b64_tr_b16 v[204:205], v24 offset:0\n\t" \
   "ds_read_b64_tr_b16 v[206:207], v24 offset:4096\n\t" "ds_read_b64_tr_b16 v[220:221], v25 offset:0\n\t" \
   "ds_read_b64_tr_b16 v[222:223], v25 offset:4096\n\t" "s_waitcnt vmcnt(15)\n\t" "s_waitcnt lgkmcnt(0)\n\t" "s_barrier\n\t" \
   "ds_write_b32 v21, a128 offset:0\n\t" "ds_write_b32 v21, a129 offset:21120\n\t" "ds_write_b32 v21, a130 offset:10560\n\t" \
   "ds_write_b32 v21, a131 offset:31680\n\t" "s_mov_b32 s41, 61696\n\t" "buffer_load_dwordx4 a[232:235], v17, %[rb], s41 offen\n\t" \
   "s_waitcnt vmcnt(15)\n\t" "ds_write_b32 v21, a132 offset:64\n\t" "ds_write_b32 v21, a133 offset:21184\n\t" "ds_write_b32 v21, a134 offset:10624\n\t" \
   "ds_write_b32 v21, a135 offset:31744\n\t" "s_mov_b32 s41, 61952\n\t" "buffer_load_dwordx4 a[236:239], v17, %[rb], s41 offen\n\t" \
   "s_waitcnt vmcnt(15)\n\t" "ds_write_b32 v21, a160 offset:128\n\t" "ds_write_b32 v21, a161 offset:21248\n\t" \
   "ds_write_b32 v21, a162 offset:10688\n\t" "ds_write_b32 v21, a163 offset:31808\n\t" "s_mov_b32 s41, 62208\n\t" \
   "buffer_load_dwordx4 v[232:235], v17, %[rb], s41 offen\n\t" "s_waitcnt vmcnt(15)\n\t" "ds_write_b32 v21, a164 offset:192\n\t" \
   "ds_write_b32 v21, a165 offset:21312\n\t" "ds_write_b32 v21, a166 offset:10752\n\t" "ds_write_b32 v21, a167 offset:31872\n\t" \
   "s_mov_b32 s41, 62464\n\t" "buffer_load_dwordx4 v[236:239], v17, %[rb], s41 offen\n\t" "s_waitcnt vmcnt(15)\n\t" \
   "ds_write_b32 v21, a192 offset:256\n\t" "ds_write_b32 v21, a193 offset:21376\n\t" "ds_write_b32 v21, a194 offset:10816\n\t" \
   "ds_write_b32 v21, a195 offset:31936\n\t" "s_mov_b32 s41, 81920\n\t" "buffer_load_dwordx4 a[144:147], v17, %[rb], s41 offen\n\t" \
   "s_waitcnt vmcnt(15)\n\t" "ds_write_b32 v21, a196 offset:5248\n\t" "ds_write_b32 v21, a197 offset:26368\n\t" \
   "ds_write_b32 v21, a198 offset:15808\n\t" "ds_write_b32 v21, a199 offset:36928\n\t" "s_mov_b32 s41, 82176\n\t" \
   "buffer_load_dwordx4 a[148:151], v17, %[rb], s41 offen\n\t" "s_waitcnt vmcnt(15)\n\t" "ds_write_b32 v21, a224 offset:5312\n\t" \
   "ds_write_b32 v21, a225 offset:26432\n\t" "ds_write_b32 v21, a226 offset:15872\n\t" "ds_write_b32 v21, a227 offset:36992\n\t" \
   "s_mov_b32 s41, 82432\n\t" "buffer_load_dwordx4 a[176:179], v17, %[rb], s41 offen\n\t" "s_waitcnt vmcnt(15)\n\t" \
   "ds_write_b32 v21, a228 offset:5376\n\t" "ds_write_b32 v21, a229 offset:26496\n\t" "ds_write_b32 v21, a230 offset:15936\n\t" \
   "ds_write_b32 v21, a231 offset:37056\n\t" "s_mov_b32 s41, 82688\n\t" "buffer_load_dwordx4 a[180:183], v17, %[rb], s41 offen\n\t" \
   "s_waitcnt vmcnt(15)\n\t" "ds_write_b32 v21, v224 offset:5440\n\t" "ds_write_b32 v21, v225 offset:26560\n\t" \
   "ds_write_b32 v21, v226 offset:16000\n\t" "ds_write_b32 v21, v227 offset:37120\n\t" "s_mov_b32 s41, 82944\n\t" \
   "buffer_load_dwordx4 a[208:211], v17, %[rb], s41 offen\n\t" "s_waitcnt vmcnt(15)\n\t" "ds_write_b32 v21, v228 offset:5504\n\t" \
   "ds_write_b32 v21, v229 offset:26624\n\t" "ds_write_b32 v21, v230 offset:16064\n\t" "ds_write_b32 v21, v231 offset:37184\n\t" \
   "s_mov_b32 s41, 102400\n\t" "buffer_load_dwordx4 a[212:215], v17, %[rb], s41 offen\n\t" "s_waitcnt lgkmcnt(0)\n\t" "s_barrier\n\t" LDSTAMP(4) \
   "ds_read_b64_tr_b16 a[128:129], v22 offset:0\n\t" "ds_read_b64_tr_b16 a[130:131], v22 offset:656\n\t" \
   "ds_read_b64_tr_b16 a[132:133], v22 offset:2624\n\t" "ds_read_b64_tr_b16 a[134:135], v22 offset:3280\n\t" \
   "ds_read_b64_tr_b16 a[160:161], v22 offset:16\n\t" "ds_read_b64_tr_b16 a[162:163], v22 offset:672\n\t" \
   "ds_read_b64_tr_b16 a[164:165], v22 offset:2640\n\t" "ds_read_b64_tr_b16 a[166:167], v22 offset:3296\n\t" \
   "ds_read_b64_tr_b16 a[192:193], v22 offset:32\n\t" "ds_read_b64_tr_b16 a[194:195], v22 offset:688\n\t" \
   "ds_read_b64_tr_b16 a[196:197], v22 offset:2656\n\t" "ds_read_b64_tr_b16 a[198:199], v22 offset:3312\n\t" \
   "ds_read_b64_tr_b16 a[224:225], v22 offset:48\n\t" "ds_read_b64_tr_b16 a[226:227], v22 offset:704\n\t" \
   "ds_read_b64_tr_b16 a[228:229], v22 offset:2672\n\t" "ds_read_b64_tr_b16 a[230:231], v22 offset:3328\n\t" \
   "ds_read_b64_tr_b16 v[224:225], v22 offset:64\n\t" "ds_read_b64_tr_b16 v[226:227], v22 offset:720\n\t" \
   "ds_read_b64_tr_b16 v[228:229], v22 offset:2688\n\t" "ds_read_b64_tr_b16 v[230:231], v22 offset:3344\n\t" "s_waitcnt vmcnt(15)\n\t" \
   "s_waitcnt lgkmcnt(0)\n\t" "s_barrier\n\t" "ds_write_b32 v21, a136 offset:0\n\t" "ds_write_b32 v21, a137 offset:21120\n\t" \
   "ds_write_b32 v21, a138 offset:10560\n\t" "ds_write_b32 v21, a139 offset:31680\n\t" "s_mov_b32 s41, 102656\n\t" \
   "buffer_load_dwordx4 a[240:243], v17, %[rb], s41 offen\n\t" "s_waitcnt vmcnt(15)\n\t" "ds_write_b32 v21, a140 offset:64\n\t" \
   "ds_write_b32 v21, a141 offset:21184\n\t" "ds_write_b32 v21, a142 offset:10624\n\t" "ds_write_b32 v21, a143 offset:31744\n\t" \
   "s_mov_b32 s41, 102912\n\t" "buffer_load_dwordx4 a[244:247], v17, %[rb], s41 offen\n\t" "s_waitcnt vmcnt(15)\n\t" \
   "ds_write_b32 v21, a168 offset:128\n\t" "ds_write_b32 v21, a169 offset:21248\n\t" "ds_write_b32 v21, a170 offset:10688\n\t" \
   "ds_write_b32 v21, a171 offset:31808\n\t" "s_mov_b32 s41, 103168\n\t" "buffer_load_dwordx4 v[240:243], v17, %[rb], s41 offen\n\t" \
   "s_waitcnt vmcnt(15)\n\t" "ds_write_b32 v21, a172 offset:192\n\t" "ds_write_b32 v21, a173 offset:21312\n\t" \
   "ds_write_b32 v21, a174 offset:10752\n\t" "ds_write_b32 v21, a175 offset:31872\n\t" "s_mov_b32 s41, 103424\n\t" \
   "buffer_load_dwordx4 v[244:247], v17, %[rb], s41 offen\n\t" "s_waitcnt vmcnt(15)\n\t" "ds_write_b32 v21, a200 offset:256\n\t" \
   "ds_write_b32 v21, a201 offset:21376\n\t" "ds_write_b32 v21, a202 offset:10816\n\t" "ds_write_b32 v21, a203 offset:31936\n\t" \
   "s_mov_b32 s41, 122880\n\t" "buffer_load_dwordx4 a[152:155], v17, %[rb], s41 offen\n\t" "s_waitcnt vmcnt(15)\n\t" \
   "ds_write_b32 v21, a204 offset:5248\n\t" "ds_write_b32 v21, a205 offset:26368\n\t" "ds_write_b32 v21, a206 offset:15808\n\t" \
   "ds_write_b32 v21, a207 offset:36928\n\t" "s_mov_b32 s41, 123136\n\t" "buffer_load_dwordx4 a[156:159], v17, %[rb], s41 offen\n\t" \
   "s_waitcnt vmcnt(15)\n\t" "ds_write_b32 v21, a232 offset:5312\n\t" "ds_write_b32 v21, a233 offset:26432\n\t" \
   "ds_write_b32 v21, a234 offset:15872\n\t" "ds_write_b32 v21, a235 offset:36992\n\t" "s_mov_b32 s41, 123392\n\t" \
   "buffer_load_dwordx4 a[184:187], v17, %[rb], s41 offen\n\t" "s_waitcnt vmcnt(15)\n\t" "ds_write_b32 v21, a236 offset:5376\n\t" \
   "ds_write_b32 v21, a237 offset:26496\n\t" "ds_write_b32 v21, a238 offset:15936\n\t" "ds_write_b32 v21, a239 offset:37056\n\t" \
   "s_mov_b32 s41, 123648\n\t" "buffer_load_dwordx4 a[188:191], v17, %[rb], s41 offen\n\t" "s_waitcnt vmcnt(15)\n\t" \
   "ds_write_b32 v21, v232 offset:5440\n\t" "ds_write_b32 v21, v233 offset:26560\n\t" "ds_write_b32 v21, v234 offset:16000\n\t" \
   "ds_write_b32 v21, v235 offset:37120\n\t" "s_mov_b32 s41, 123904\n\t" "buffer_load_dwordx4 a[216:219], v17, %[rb], s41 offen\n\t" \
   "s_waitcnt vmcnt(15)\n\t" "ds_write_b32 v21, v236 offset:5504\n\t" "ds_write_b32 v21, v237 offset:26624\n\t" \
   "ds_write_b32 v21, v238 offset:16064\n\t" "ds_write_b32 v21, v239 offset:37184\n\t" "s_mov_b32 s41, 143360\n\t" \
   "buffer_load_dwordx4 a[220:223], v17, %[rb], s41 offen\n\t" "s_waitcnt lgkmcnt(0)\n\t" "s_barrier\n\t" LDSTAMP(5) \
   "ds_read_b64_tr_b16 a[136:137], v22 offset:0\n\t" "ds_read_b64_tr_b16 a[138:139], v22 offset:656\n\t" \
   "ds_read_b64_tr_b16 a[140:141], v22 offset:2624\n\t" "ds_read_b64_tr_b16 a[142:143], v22 offset:3280\n\t" \
   "ds_read_b64_tr_b16 a[168:169], v22 offset:16\n\t" "ds_read_b64_tr_b16 a[170:171], v22 offset:672\n\t" \
   "ds_read_b64_tr_b16 a[172:173], v22 offset:2640\n\t" "ds_read_b64_tr_b16 a[174:175], v22 offset:3296\n\t" \
   "ds_read_b64_tr_b16 a[200:201], v22 offset:32\n\t" "ds_read_b64_tr_b16 a[202:203], v22 offset:688\n\t" \
   "ds_read_b64_tr_b16 a[204:205], v22 offset:2656\n\t" "ds_read_b64_tr_b16 a[206:207], v22 offset:3312\n\t" \
   "ds_read_b64_tr_b16 a[232:233], v22 offset:48\n\t" "ds_read_b64_tr_b16 a[234:235], v22 offset:704\n\t" \
   "ds_read_b64_tr_b16 a[236:237], v22 offset:2672\n\t" "ds_read_b64_tr_b16 a[238:239], v22 offset:3328\n\t" \
   "ds_read_b64_tr_b16 v[232:233], v22 offset:64\n\t" "ds_read_b64_tr_b16 v[234:235], v22 offset:720\n\t" \
   "ds_read_b64_tr_b16 v[236:237], v22 offset:2688\n\t" "ds_read_b64_tr_b16 v[238:239], v22 offset:3344\n\t" "s_waitcnt vmcnt(15)\n\t" \
   "s_waitcnt lgkmcnt(0)\n\t" "s_barrier\n\t" "ds_write_b32 v21, a144 offset:0\n\t" "ds_write_b32 v21, a145 offset:21120\n\t" \
   "ds_write_b32 v21, a146 offset:10560\n\t" "ds_write_b32 v21, a147 offset:31680\n\t" "s_mov_b32 s41, 143616\n\t" \
   "buffer_load_dwordx4 a[248:251], v17, %[rb], s41 offen\n\t" "s_waitcnt vmcnt(15)\n\t" "ds_write_b32 v21, a148 offset:64\n\t" \
   "ds_write_b32 v21, a149 offset:21184\n\t" "ds_write_b32 v21, a150 offset:10624\n\t" "ds_write_b32 v21, a151 offset:31744\n\t" \
   "s_mov_b32 s41, 143872\n\t" "buffer_load_dwordx4 a[252:255], v17, %[rb], s41 offen\n\t" "s_waitcnt vmcnt(15)\n\t" \
   "ds_write_b32 v21, a176 offset:128\n\t" "ds_write_b32 v21, a177 offset:21248\n\t" "ds_write_b32 v21, a178 offset:10688\n\t" \
   "ds_write_b32 v21, a179 offset:31808\n\t" "s_mov_b32 s41, 144128\n\t" "buffer_load_dwordx4 v[248:251], v17, %[rb], s41 offen\n\t" \
   "s_waitcnt vmcnt(15)\n\t" "ds_write_b32 v21, a180 offset:192\n\t" "ds_write_b32 v21, a181 offset:21312\n\t" \
   "ds_write_b32 v21, a182 offset:10752\n\t" "ds_write_b32 v21, a183 offset:31872\n\t" "s_mov_b32 s41, 144384\n\t" \
   "buffer_load_dwordx4 v[252:255], v17, %[rb], s41 offen\n\t" "s_waitcnt vmcnt(15)\n\t" "ds_write_b32 v21, a208 offset:256\n\t" \
   "ds_write_b32 v21, a209 offset:21376\n\t" "ds_write_b32 v21, a210 offset:10816\n\t" "ds_write_b32 v21, a211 offset:31936\n\t" \
   "s_waitcnt vmcnt(14)\n\t" "ds_write_b32 v21, a212 offset:5248\n\t" "ds_write_b32 v21, a213 offset:26368\n\t" \
   "ds_write_b32 v21, a214 offset:15808\n\t" "ds_write_b32 v21, a215 offset:36928\n\t" "s_waitcnt vmcnt(13)\n\t" \
   "ds_write_b32 v21, a240 offset:5312\n\t" "ds_write_b32 v21, a241 offset:26432\n\t" "ds_write_b32 v21, a242 offset:15872\n\t" \
   "ds_write_b32 v21, a243 offset:36992\n\t" "s_waitcnt vmcnt(12)\n\t" "ds_write_b32 v21, a244 offset:5376\n\t" \
   "ds_write_b32 v21, a245 offset:26496\n\t" "ds_write_b32 v21, a246 offset:15936\n\t" "ds_write_b32 v21, a247 offset:37056\n\t" \
   "s_waitcnt vmcnt(11)\n\t" "ds_write_b32 v21, v240 offset:5440\n\t" "ds_write_b32 v21, v241 offset:26560\n\t" \
   "ds_write_b32 v21, v242 offset:16000\n\t" "ds_write_b32 v21, v243 offset:37120\n\t" "s_waitcnt vmcnt(10)\n\t" \
   "ds_write_b32 v21, v244 offset:5504\n\t" "ds_write_b32 v21, v245 offset:26624\n\t" "ds_write_b32 v21, v246 offset:16064\n\t" \
   "ds_write_b32 v21, v247 offset:37184\n\t" "buffer_load_dwordx4 v[28:31], v112, %[rw], 0 offen sc1\n\t" "s_waitcnt lgkmcnt(0)\n\t" "s_barrier\n\t" \
   LDSTAMP(6) "ds_read_b64_tr_b16 a[144:145], v22 offset:0\n\t" "ds_read_b64_tr_b16 a[146:147], v22 offset:656\n\t" \
   "ds_read_b64_tr_b16 a[148:149], v22 offset:2624\n\t" "ds_read_b64_tr_b16 a[150:151], v22 offset:3280\n\t" \
   "ds_read_b64_tr_b16 a[176:177], v22 offset:16\n\t" "ds_read_b64_tr_b16 a[178:179], v22 offset:672\n\t" \
   "ds_read_b64_tr_b16 a[180:181], v22 offset:2640\n\t" "ds_read_b64_tr_b16 a[182:183], v22 offset:3296\n\t" \
   "ds_read_b64_tr_b16 a[208:209], v22 offset:32\n\t" "ds_read_b64_tr_b16 a[210:211], v22 offset:688\n\t" \
   "ds_read_b64_tr_b16 a[212:213], v22 offset:2656\n\t" "ds_read_b64_tr_b16 a[214:215], v22 offset:3312\n\t" \
   "ds_read_b64_tr_b16 a[240:241], v22 offset:48\n\t" "ds_read_b64_tr_b16 a[242:243], v22 offset:704\n\t" \
   "ds_read_b64_tr_b16 a[244:245], v22 offset:2672\n\t" "ds_read_b64_tr_b16 a[246:247], v22 offset:3328\n\t" \
   "ds_read_b64_tr_b16 v[240:241], v22 offset:64\n\t" "ds_read_b64_tr_b16 v[242:243], v22 offset:720\n\t" \
   "ds_read_b64_tr_b16 v[244:245], v22 offset:2688\n\t" "ds_read_b64_tr_b16 v[246:247], v22 offset:3344\n\t" "s_waitcnt vmcnt(10)\n\t" \
   "s_waitcnt lgkmcnt(0)\n\t" "s_barrier\n\t" "ds_write_b32 v21, a152 offset:0\n\t" "ds_write_b32 v21, a153 offset:21120\n\t" \
   "ds_write_b32 v21, a154 offset:10560\n\t" "ds_write_b32 v21, a155 offset:31680\n\t" "s_waitcnt vmcnt(9)\n\t" "ds_write_b32 v21, a156 offset:64\n\t" \
   "ds_write_b32 v21, a157 offset:21184\n\t" "ds_write_b32 v21, a158 offset:10624\n\t" "ds_write_b32 v21, a159 offset:31744\n\t" \
   "s_waitcnt vmcnt(8)\n\t" "ds_write_b32 v21, a184 offset:128\n\t" "ds_write_b32 v21, a185 offset:21248\n\t" "ds_write_b32 v21, a186 offset:10688\n\t" \
   "ds_write_b32 v21, a187 offset:31808\n\t" "s_waitcnt vmcnt(7)\n\t" "ds_write_b32 v21, a188 offset:192\n\t" "ds_write_b32 v21, a189 offset:21312\n\t" \
   "ds_write_b32 v21, a190 offset:10752\n\t" "ds_write_b32 v21, a191 offset:31872\n\t" "s_waitcnt vmcnt(6)\n\t" "ds_write_b32 v21, a216 offset:256\n\t" \
   "ds_write_b32 v21, a217 offset:21376\n\t" "ds_write_b32 v21, a218 offset:10816\n\t" "ds_write_b32 v21, a219 offset:31936\n\t" \
   "s_waitcnt vmcnt(5)\n\t" "ds_write_b32 v21, a220 offset:5248\n\t" "ds_write_b32 v21, a221 offset:26368\n\t" \
   "ds_write_b32 v21, a222 offset:15808\n\t" "ds_write_b32 v21, a223 offset:36928\n\t" "s_waitcnt vmcnt(4)\n\t" \
   "ds_write_b32 v21, a248 offset:5312\n\t" "ds_write_b32 v21, a249 offset:26432\n\t" "ds_write_b32 v21, a250 offset:15872\n\t" \
   "ds_write_b32 v21, a251 offset:36992\n\t" "s_waitcnt vmcnt(3)\n\t" "ds_write_b32 v21, a252 offset:5376\n\t" \
   "ds_write_b32 v21, a253 offset:26496\n\t" "ds_write_b32 v21, a254 offset:15936\n\t" "ds_write_b32 v21, a255 offset:37056\n\t" \
   "s_waitcnt vmcnt(2)\n\t" "ds_write_b32 v21, v248 offset:5440\n\t" "ds_write_b32 v21, v249 offset:26560\n\t" \
   "ds_write_b32 v21, v250 offset:16000\n\t" "ds_write_b32 v21, v251 offset:37120\n\t" "s_waitcnt vmcnt(1)\n\t" \
   "ds_write_b32 v21, v252 offset:5504\n\t" "ds_write_b32 v21, v253 offset:26624\n\t" "ds_write_b32 v21, v254 offset:16064\n\t" \
   "ds_write_b32 v21, v255 offset:37184\n\t" "s_waitcnt lgkmcnt(0)\n\t" "s_barrier\n\t" LDSTAMP(7) "ds_read_b64_tr_b16 a[152:153], v22 offset:0\n\t" \
   "ds_read_b64_tr_b16 a[154:155], v22 offset:656\n\t" "ds_read_b64_tr_b16 a[156:157], v22 offset:2624\n\t" \
   "ds_read_b64_tr_b16 a[158:159], v22 offset:3280\n\t" "ds_read_b64_tr_b16 a[184:185], v22 offset:16\n\t" \
   "ds_read_b64_tr_b16 a[186:187], v22 offset:672\n\t" "ds_read_b64_tr_b16 a[188:189], v22 offset:2640\n\t" \
   "ds_read_b64_tr_b16 a[190:191], v22 offset:3296\n\t" "ds_read_b64_tr_b16 a[216:217], v22 offset:32\n\t" \
   "ds_read_b64_tr_b16 a[218:219], v22 offset:688\n\t" "ds_read_b64_tr_b16 a[220:221], v22 offset:2656\n\t" \
   "ds_read_b64_tr_b16 a[222:223], v22 offset:3312\n\t" "ds_read_b64_tr_b16 a[248:249], v22 offset:48\n\t" \
   "ds_read_b64_tr_b16 a[250:251], v22 offset:704\n\t" "ds_read_b64_tr_b16 a[252:253], v22 offset:2672\n\t" \
   "ds_read_b64_tr_b16 a[254:255], v22 offset:3328\n\t" "ds_read_b64_tr_b16 v[248:249], v22 offset:64\n\t" \
   "ds_read_b64_tr_b16 v[250:251], v22 offset:720\n\t" "ds_read_b64_tr_b16 v[252:253], v22 offset:2688\n\t" \
   "ds_read_b64_tr_b16 v[254:255], v22 offset:3344\n\t" "s_waitcnt vmcnt(0) lgkmcnt(0)\n\t" "s_barrier"
#define LDY_CLOB "v24", "v25", "v26", "v32", "v33", "v34", "v35", "v36", "v37", "v38", "v39", "v40", "v41", "v42", "v43", "v44", "v45", "v46", "v47", "v48", "v49", "v50", "v51", "v52", "v53", "v54", "v55", "v56", "v57", "v58", "v59", "v60", "v61", "v62", "v63", "v64", "v65", "v66", "v67", "v68", "v69", "v70", "v71", "v72", "v73", "v74", "v75", "v76", "v77", "v78", "v79", "v80", "v81", "v82", "v83", "v84", "v85", "v86", "v87", "v88", "v89", "v90", "v91", "v92", "v93", "v94", "v95", "v96", "v97", "v98", "v99", "v100", "v101", "v102", "v103", "v104", "v105", "v106", "v107", "v108", "v109", "v110", "v111", "v120", "v121", "v122", "v123", "s40", "s41"
#define W_OPS "{a[0:31]}"(w[0]), "{a[32:63]}"(w[1]), "{a[64:95]}"(w[2]), "{a[96:127]}"(w[3]), "{a[128:159]}"(w[4]), "{a[160:191]}"(w[5]), \
              "{a[192:223]}"(w[6]), "{a[224:255]}"(w[7]), "{v[192:223]}"(w[8]), "{v[224:255]}"(w[9])

__device__ __forceinline__ int mem_lane(int lane) { return lane * 16; }
__device__ __forceinline__ int pair_of(int g) { return (g & 1) * 2 + (g >> 1); }

__device__ __forceinline__ void rec_p1(int wv_, const v32u (&w)[10], unsigned char* ws, const Pool& pool, int layer, int h, int xs, RecTune tn, unsigned long long* stamps) {
  const Clk0 c0 = clk_sample();
  const unsigned lds0 = (unsigned)(uintptr_t)(__attribute__((address_space(3))) unsigned char*)dyn_lds;
  const int lane = lane_id(), wv = wv_, g = lane >> 4, r = lane & 15;
  const v4u rs = make_rs(ws, (unsigned)OFF_END);
  const int p0 = wv < 3 ? 3 * wv : 8;
  v16u ad, la = (unsigned)lane;
  const int kqo = (int)(wv < 2 ? OFF_KF : OFF_QF) + ((h * RT + (wv & 1)) * 4 * 64 + lane) * 16;
  const int progo = (wv == 0 && lane == 0) ? prog_off(layer, 0, h) : OOB;
  {
    const int p2 = wv < 3 ? p0 + 2 : 0;
    ad[0] = W_MEM + (p0 >> 1) * WIN + (p0 & 1) * 1024 + mem_lane(lane);
    ad[1] = W_MEM + ((p0 + 1) >> 1) * WIN + ((p0 + 1) & 1) * 1024 + mem_lane(lane);
    ad[2] = W_MEM + (p2 >> 1) * WIN + (p2 & 1) * 1024 + mem_lane(lane);
    ad[3] = lane == 0 ? W_MISC + h * 64 + wv * 4 : OOB;
    ad[4] = copy_off(wv) + (h >> 2) * WIN + (h & 3) * 512 + (g >> 1) * 256 + (2 * (r & 7) + (r >> 3)) * 16 + (g & 1) * 8;
    ad[5] = g < 2 ? copy_off(wv + 4 * (r >> 3)) + (8 + (h >> 4)) * WIN + ((h >> 3) & 1) * 1024 + (((h >> 1) & 3) * 16 + (h & 1) * 8 + (r & 7)) * 16 + g * 8 : OOB;
    ad[6] = (r & 1) ? 0xEEEEu : 0x4444u;
    ad[7] = lds0 + (32 * wv + r) * 16 + g * 4;
    ad[8] = lds0 + (4 * g + (r >> 2)) * 16 + (r & 1) * 8;
    ad[9] = lds0 + L_M2 + wv * 3072 + lane * 16;
    ad[10] = kqo;
    ad[11] = M_TM + xs * 128 + (lane & 1) * 12;
    ad[12] = kqf_off(layer, 0, h) + (lane & (h < NFILL ? 7 : 3)) * 4;
    ad[13] = wv > 1 ? (int)OFF_PXF + ((h * 2 + (wv & 1)) * 64 + lane) * 16 : OOB;
    ad[14] = (g < 2 && wv > 1) ? (int)OFF_PXM + (((h >> 2) * 2 + (wv & 1)) * 64 + (h & 3) * 16 + r) * 16 + g * 8 : OOB;
    ad[15] = progo;
#pragma unroll
    for (int i = 0; i < 10; ++i) { int p = p0 + i; p = p >= 10 ? p - 10 : p; la[i] = lds0 + p * 1024 + lane * 16; }
    la[10] = lds0 + L_KQ + lane * 16;
    la[12] = lds0 + REC_ST + wv * 8;
    asm volatile("v_mov_b32 %0, 0xceabfb8f" : "=v"(la[13]));
    la[14] = 16 * (wv & 1) + r - 4 * g;
    la[15] = lds0 + L_KQ + (wv & 1) * 4096 + lane * 16;
  }
  __builtin_amdgcn_s_waitcnt(0x0F70);
  if (layer) __builtin_amdgcn_s_sleep(64);
  v4u clk = clk_init(c0);
  unsigned long long stat;
  asm volatile(P1_ASM
               : "=&{s[76:77]}"(stat), "+{s[56:59]}"(clk)
               : W_OPS, "{v[128:143]}"(ad), "{v[112:127]}"(la), [rs] "s"(rs), [sel] "s"(pool.sel), [pool] "s"(pool.base), [xs] "s"(xs),
                 [lad0] "s"(ladder(xs, xs & 1)), [s3] "s"(pool.s3), [m1] "s"(tn.m1), [l2s] "s"(lds0 + L_M2 + wv * 3072),
                 [hi8] "s"(0xFF00FF00FF00FF00ull), [wv] "s"(wv), [kf0] "s"((int)OFF_KF + h * RT * 4096),
                 [esc] "s"(pool.base + (NSEG - 1) * REG)
               : P1_CLOB RST_CLOB, "vcc", "scc", "m0", "memory");
  LDSBAR();
  {
    const unsigned one = 1u;
    const int so = (NSEG - 1) * 4096;
    VM_ST32(one, progo, so);
    VM_WAIT(0);
  }
}

__device__ __forceinline__ void rec_pv(int wv_, const v32u (&w)[10], unsigned char* ws, const Pool& pool, int layer, int h, int xs, RecTune tn, unsigned long long* stamps) {
  const Clk0 c0 = clk_sample();
  const unsigned lds0 = (unsigned)(uintptr_t)(__attribute__((address_space(3))) unsigned char*)dyn_lds;
  const int lane = lane_id(), wv = wv_, g = lane >> 4, r = lane & 15;
  const v4u rs = make_rs(ws, (unsigned)OFF_END);
  const int p0 = wv < 3 ? 3 * wv : 8, p2 = wv < 3 ? p0 + 2 : 0;
  v16u ad = (unsigned)lane, la = (unsigned)lane;
  ad[0] = W_MEM + (p0 >> 1) * WIN + (p0 & 1) * 1024 + mem_lane(lane);
  ad[1] = W_MEM + ((p0 + 1) >> 1) * WIN + ((p0 + 1) & 1) * 1024 + mem_lane(lane);
  ad[2] = W_MEM + (p2 >> 1) * WIN + (p2 & 1) * 1024 + mem_lane(lane);
  ad[3] = (r & 8) ? 0xEEEEu : 0x4444u;
  ad[4] = (wv * 10 + (g >> 1)) * 16384 + h * 256 + r * 16 + (g & 1) * 8;
  ad[5] = h * 16384 + wv * 2048 + lane * 16;
  ad[6] = (r & 1) ? 0xEEEEu : 0x4444u;
  ad[7] = lds0 + (32 * wv + r) * 16 + pair_of(g) * 4;
  ad[8] = lds0 + (4 * g + (r >> 2)) * 16 + (r & 1) * 8;
  ad[11] = M_TM + (4 + xs) * 128 + (lane & 1) * 12;
  ad[15] = (wv == 0 && lane == 0) ? prog_off(layer, 0, 32 + h) : OOB;
#pragma unroll
  for (int i = 0; i < 10; ++i) { int p = p0 + i; p = p >= 10 ? p - 10 : p; la[i] = lds0 + p * 1024 + lane * 16; }
  la[12] = lds0 + REC_ST + wv * 8;
  __builtin_amdgcn_s_waitcnt(0x0F70);
  if (layer) __builtin_amdgcn_s_sleep(64);
  v4u clk = clk_init(c0);
  unsigned long long stat;
  asm volatile(PV_ASM
               : "=&{s[76:77]}"(stat), "+{s[56:59]}"(clk)
               : W_OPS, "{v[128:143]}"(ad), "{v[112:127]}"(la), [rs] "s"(rs), [sel] "s"(pool.sel), [pool] "s"(pool.base), [xs] "s"(xs),
                 [lad0] "s"(ladder(xs, xs & 1)), [s3] "s"(pool.s3), [m1c] "s"(tn.m1 + tn.c2), [esc] "s"(pool.base + (NSEG - 1) * REG),
                 [ysel] "s"(pool.ysel), [yb] "s"(pool.base + P_Y)
               : PV_CLOB RST_CLOB, "vcc", "scc", "memory");
}

#define TSTART 5300
#define TSTART1 TSTART
#define TSTARU 3600
#define TD1 900u
#define TR2 400u
#define TEMA 1
#define TEMB 2
#define TBUP 48
#define TBDN 6
#define TLATE 520
#define TSEC 50
__device__ __forceinline__ unsigned track_dur(unsigned e, int m, int rob, int s) {
  const int lo = (int)e - (s < 8 ? 64 : (rob & 255)), hi = (int)e + (rob >> 8);
  if (s >= 8) m = (int)e + ((m - (int)e + (m > (int)e ? (1 << TEMA) - 1 : 0)) >> (m > (int)e ? TEMA : TEMB));
  return (unsigned)min(max(m, lo), hi);
}
#define TTS 200
#define TTU 240
__device__ __forceinline__ void rec_sched(unsigned char* ws, const Pool& pool, int layer, int c16, int xs, RecTune tn, unsigned long long* stamps) {
  const Clk0 c0 = clk_sample();
  const unsigned lds0 = (unsigned)(uintptr_t)(__attribute__((address_space(3))) unsigned char*)dyn_lds;
  const int lane = lane_id();
  const v4u rs = make_rs(ws, (unsigned)OFF_END);
  unsigned long long stat = 0;
  const int sm = M_STAMP + min(lane, 39) * 64, sp = W_MISC + (lane & 31) * 64;
  const int yo = M_TM + xs * 128, oc = (c16 == 0 && lane < 16) ? M_TM + lane * 128 : OOB;
  const unsigned cv = flag_finish<2>(rs, kqf_off(layer, 0, lane), 0xFFFFFFFFu);
  v4u clk = clk_init(c0);
  PH(3 + layer * 6)
  unsigned r2a = TR2, d1a = TD1, dd = TD1 + tn.m2, tp = 0, tm = 0, anchor = clk.x - dd;
  unsigned rr = (unsigned)min(max(wave_imax((int)(cv * 8u - clk.x) + (lane < 32 ? (layer ? TSTART1 : TSTART) : TSTARU)), 300), 4000);
  int bonus = 0;
  bool clean = false;
#pragma unroll 1
  for (int s = 0; s < NSEG - 1; ++s) {
    const int rgm = pool.region((s + NSEG - 1) & (NSEG - 1)), rg = pool.region(s);
    const unsigned slot = lds0 + L_T + (s & 1) * 32, ao = anchor;
    unsigned b2 = 1u;
    v4u b1 = {1u, 1u, 1u, 1u};
    if (s > 0) {
      asm volatile(WAIT_ASM : "+{s[56:59]}"(clk) : [ta] "s"(tm + TTS) : WAIT_CLOB, "scc", "memory");
      VM_LD32(b2, sm, rgm);
      VM_LD128(b1, sp, rgm, 0);
      asm volatile("s_waitcnt vmcnt(0)" : "+v"(b2), "+v"(b1)::"memory");
      anchor = tm;
      if (__builtin_expect(__any(b2 == 0xFFFFFFFFu || (int)(b2 - (tm + tn.m1 - tn.vis)) > 0), 0)) {
        STAT(3);
        for (unsigned spins = 0; __any(b2 == 0xFFFFFFFFu) && spins < SPIN_CAP; ++spins) {
          __builtin_amdgcn_s_sleep(2);
          VM_LD32(b2, sm, rgm);
          VM_WAIT1(0, b2);
        }
        const int d = wave_imax((int)(b2 - tm)) + tn.vis - tn.m1;
        anchor = tm + (d <= 0 ? 0 : (d <= tn.dlt ? tn.dlt + TSEC : d + TLATE));
      }
    }
    const unsigned tpo = tp, tmo = tm;
    tp = anchor + dd;
    tm = nosent(tp + rr);
    LDS_V4(slot) = v4u{tp, tm, (unsigned)(s + 1), anchor};
    if (anchor != tmo && s > 2) bonus = TBUP; else bonus = max(bonus - TBDN, 0);
    dd = d1a + tn.m2 + bonus;
    rr = r2a + bonus;
    {
      const v4u wq = v4u{tm, dd, rr, dd + rr};
      VM_ST128(wq, oc, rg);
    }
    if (s > 1 && !__any((b2 & 1u) != 0u)) r2a = track_dur(r2a, wave_imax((int)(b2 - tpo)), tn.rob, s);
    if (s > 1 && clean && !__any(((b1.x | b1.y | b1.z | b1.w) & 1u) != 0u))
      d1a = track_dur(d1a, wave_imax(max(max((int)(b1.x - ao), (int)(b1.y - ao)), max((int)(b1.z - ao), (int)(b1.w - ao)))), tn.rob, s);
    clean = anchor == tmo;
    while ((int)(tp + 40u - rtc() * 8u) > 0 && (int)(tp - rtc() * 8u) < 32000) __builtin_amdgcn_s_sleep(1);
    v4u lw;
    VM_LD128(lw, yo, rg, 0);
    VM_WAIT1(0, lw);
    const unsigned l0 = (unsigned)__builtin_amdgcn_readfirstlane(lw.x), l1 = (unsigned)__builtin_amdgcn_readfirstlane(lw.y),
                   l2 = (unsigned)__builtin_amdgcn_readfirstlane(lw.z);
    if (max(max(l0, l1), l2) != 0xFFFFFFFFu) { tm = l0; dd = l1; rr = l2; }
  }
}

__device__ __forceinline__ void rec_fin(unsigned char* ws, const Pool& pool, int layer, int c16, int xs, RecTune tn, unsigned long long* stamps) {
  const Clk0 c0 = clk_sample();
  const unsigned lds0 = (unsigned)(uintptr_t)(__attribute__((address_space(3))) unsigned char*)dyn_lds;
  const int lane = lane_id(), g = lane >> 4, r = lane & 15;
  const v4u rs = make_rs(ws, (unsigned)OFF_END);
  v8u ad;
  ad[0] = lane * 16;
  ad[1] = g * 256 + r * 8;
  ad[2] = (r & 1) ? 0x03020706u : 0x05040100u;
  ad[3] = (lane & 31) * 64;
  ad[4] = W_MEM + copy_off(((r >> 1) & 1) * 5) + (c16 >> 3) * WIN + (c16 & 7) * 256 + (2 * (2 * g + (r & 1)) + (r >> 3)) * 16 + ((r >> 2) & 1) * 8;
  ad[5] = lane == 0 ? M_STAMP + c16 * 64 : OOB;
  ad[6] = (r & 1) ? 0xEEEEu : 0x4444u;
  ad[7] = lds0 + L_T;
  __builtin_amdgcn_s_sleep(64);
  v4u clk = clk_init(c0);
  unsigned long long stat;
  asm volatile(FIN_ASM
               : "=&{s[76:77]}"(stat), "+{s[56:59]}"(clk), "+{v[16:23]}"(ad)
               : [rs] "s"(rs), [sel] "s"(pool.sel), [pool] "s"(pool.base), [xs] "s"(xs), [lad0] "s"(ladder(xs, xs & 1)), [s3] "s"(pool.s3),
                 [su] "s"((int)OFF_UF + c16 * 32768), [yb] "s"(pool.base + P_Y + c16 * 16384), [ysel] "s"(pool.ysel), [tu] "s"(TTU),
                 [stl] "s"(lds0 + REC_ST - 32), [uf] "s"(kqf_off(layer, 0, 32))
               : FIN_CLOB RST_CLOB, "vcc", "scc", "memory");
}

constexpr int PJ_BUF = 43 * 1024, PJ_X = 16384, PJ_LDS = PJ_X + 3 * PJ_BUF;
#define KQ_ASM \
   ".p2align 6\n\t" "s_nop 0\n\t" "s_mov_b32 s44, %[rdy]\n\t" "s_mov_b32 s45, 0\n\t" "s_mov_b32 s46, 0\n\t" "s_cmp_eq_u32 s44, -1\n\t" \
   "s_cbranch_scc1 68f\n\t" "s_mul_i32 s52, s45, 1536\n\t" "s_mul_i32 s51, s45, 40960\n\t" "s_add_u32 m0, s46, %[ldx]\n\t" "s_mov_b32 s53, s46\n\t" \
   "buffer_load_dwordx4 v16, %[rs], s51 offen offset:0 sc1 lds\n\t" "buffer_load_dwordx4 v16, %[rs], s51 offen offset:1024 sc1 lds\n\t" \
   "buffer_load_dwordx4 v16, %[rs], s51 offen offset:2048 sc1 lds\n\t" "buffer_load_dwordx4 v16, %[rs], s51 offen offset:3072 sc1 lds\n\t" \
   "s_add_u32 m0, m0, 4096\n\t" "s_add_u32 s51, s51, 4096\n\t" "buffer_load_dwordx4 v16, %[rs], s51 offen offset:0 sc1 lds\n\t" \
   "buffer_load_dwordx4 v16, %[rs], s51 offen offset:1024 sc1 lds\n\t" "buffer_load_dwordx4 v16, %[rs], s51 offen offset:2048 sc1 lds\n\t" \
   "buffer_load_dwordx4 v16, %[rs], s51 offen offset:3072 sc1 lds\n\t" "s_add_u32 m0, m0, 4096\n\t" "s_add_u32 s51, s51, 4096\n\t" \
   "buffer_load_dwordx4 v16, %[rs], s51 offen offset:0 sc1 lds\n\t" "buffer_load_dwordx4 v16, %[rs], s51 offen offset:1024 sc1 lds\n\t" \
   "s_add_u32 m0, s53, %[ld1]\n\t" "s_nop 0\n\t" "buffer_load_dwordx4 v17, %[rs], s52 offen sc1 lds\n\t" "s_mov_b32 s45, 1\n\t" \
   "s_mov_b32 s46, 44032\n\t" "s_mul_i32 s52, s45, 1536\n\t" "s_mul_i32 s51, s45, 40960\n\t" "s_add_u32 m0, s46, %[ldx]\n\t" "s_mov_b32 s53, s46\n\t" \
   "buffer_load_dwordx4 v16, %[rs], s51 offen offset:0 sc1 lds\n\t" "buffer_load_dwordx4 v16, %[rs], s51 offen offset:1024 sc1 lds\n\t" \
   "buffer_load_dwordx4 v16, %[rs], s51 offen offset:2048 sc1 lds\n\t" "buffer_load_dwordx4 v16, %[rs], s51 offen offset:3072 sc1 lds\n\t" \
   "s_add_u32 m0, m0, 4096\n\t" "s_add_u32 s51, s51, 4096\n\t" "buffer_load_dwordx4 v16, %[rs], s51 offen offset:0 sc1 lds\n\t" \
   "buffer_load_dwordx4 v16, %[rs], s51 offen offset:1024 sc1 lds\n\t" "buffer_load_dwordx4 v16, %[rs], s51 offen offset:2048 sc1 lds\n\t" \
   "buffer_load_dwordx4 v16, %[rs], s51 offen offset:3072 sc1 lds\n\t" "s_add_u32 m0, m0, 4096\n\t" "s_add_u32 s51, s51, 4096\n\t" \
   "buffer_load_dwordx4 v16, %[rs], s51 offen offset:0 sc1 lds\n\t" "buffer_load_dwordx4 v16, %[rs], s51 offen offset:1024 sc1 lds\n\t" \
   "s_add_u32 m0, s53, %[ld1]\n\t" "s_nop 0\n\t" "buffer_load_dwordx4 v17, %[rs], s52 offen sc1 lds\n\t" "68:\n\t" "v_mov_b32 v138, 0\n\t" \
   "v_mov_b32 v13, %[flw]\n\t" "v_and_b32 v12, -16, v13\n\t" "ds_write_b32 v13, v138\n\t" "ds_read_b128 v[32:35], v26 offset:0\n\t" \
   "ds_read_b128 v[36:39], v26 offset:16\n\t" "ds_read_b128 v[40:43], v26 offset:32\n\t" "ds_read_b128 v[44:47], v26 offset:48\n\t" \
   "ds_read_b128 v[48:51], v26 offset:64\n\t" "ds_read_b128 v[52:55], v26 offset:80\n\t" "ds_read_b128 v[56:59], v26 offset:96\n\t" \
   "ds_read_b128 v[60:63], v26 offset:112\n\t" "s_mov_b32 s40, 0\n\t" "s_mov_b32 s41, 0\n\t" "s_mov_b32 s42, 0\n\t" "s_mov_b32 s43, 44032\n\t" \
   "v_mov_b32 v28, v22\n\t" "v_mov_b32 v29, v23\n\t" "s_mov_b32 s45, 2\n\t" "s_mov_b32 s46, 88064\n\t" "s_waitcnt vmcnt(11) lgkmcnt(0)\n\t" \
   "s_barrier\n\t" "s_memrealtime s[54:55]\n\t" "v_mov_b32 v146, -1\n\t" "s_cmp_eq_u32 s44, -1\n\t" "s_cbranch_scc0 172f\n\t" "72:\n\t" \
   "ds_read_b128 v[160:163], v28 offset:0\n\t" "ds_read_b128 v[164:167], v28 offset:512\n\t" "ds_read_b128 v[168:171], v28 offset:1024\n\t" \
   "ds_read_b128 v[172:175], v28 offset:1536\n\t" "s_waitcnt lgkmcnt(0)\n\t" "v_mov_b32 v31, s54\n\t" "v_min_u32 v31, -2, v31\n\t" "1:\n\t" \
   "v_mov_b32 v26, v28\n\t" "v_mov_b32 v27, v29\n\t" "s_waitcnt lgkmcnt(2)\n\t" "v_mfma_f32_32x32x16_bf16 v[64:79], a[0:3], v[160:163], 0\n\t" \
   "ds_read_b128 v[176:179], v26 offset:2048\n\t" "v_mfma_f32_32x32x16_bf16 v[80:95], a[8:11], v[160:163], 0\n\t" "v_mul_f32 v11, 0x3e0293ee, v10\n\t" \
   "s_lshl_b32 s47, s41, 13\n\t" "s_sub_i32 s47, s47, 8192\n\t" "s_max_i32 s47, s47, 0\n\t" "s_cmp_eq_u32 s40, 0\n\t" \
   "s_cselect_b32 s48, 0x7ffffff0, 0\n\t" "v_mfma_f32_32x32x16_bf16 v[64:79], a[4:7], v[164:167], v[64:79]\n\t" \
   "ds_read_b128 v[180:183], v26 offset:2560\n\t" "v_or_b32 v30, s48, v21\n\t" "v_fmac_f32 v96, v32, v8\n\t" "v_fmac_f32 v97, v33, v8\n\t" \
   "v_fmac_f32 v98, v34, v8\n\t" "v_mfma_f32_32x32x16_bf16 v[80:95], a[12:15], v[164:167], v[80:95]\n\t" "v_fmac_f32 v99, v35, v8\n\t" \
   "v_fmac_f32 v100, v36, v8\n\t" "v_fmac_f32 v101, v37, v8\n\t" "v_fmac_f32 v102, v38, v8\n\t" "v_fmac_f32 v103, v39, v8\n\t" \
   "s_waitcnt lgkmcnt(2)\n\t" "v_mfma_f32_32x32x16_bf16 v[64:79], a[16:19], v[168:171], v[64:79]\n\t" "ds_read_b128 v[184:187], v26 offset:3072\n\t" \
   "v_fmac_f32 v104, v40, v8\n\t" "v_fmac_f32 v105, v41, v8\n\t" "v_fmac_f32 v106, v42, v8\n\t" "v_fmac_f32 v107, v43, v8\n\t" \
   "v_mfma_f32_32x32x16_bf16 v[80:95], a[24:27], v[168:171], v[80:95]\n\t" "v_fmac_f32 v108, v44, v8\n\t" "v_fmac_f32 v109, v45, v8\n\t" \
   "v_fmac_f32 v110, v46, v8\n\t" "v_fmac_f32 v111, v47, v8\n\t" "v_mul_f32 v96, v96, v11\n\t" "v_mul_f32 v97, v97, v11\n\t" \
   "v_mfma_f32_32x32x16_bf16 v[64:79], a[20:23], v[172:175], v[64:79]\n\t" "ds_read_b128 v[188:191], v26 offset:3584\n\t" "v_mul_f32 v98, v98, v11\n\t" \
   "v_mul_f32 v99, v99, v11\n\t" "v_mul_f32 v100, v100, v11\n\t" "v_mul_f32 v101, v101, v11\n\t" \
   "v_mfma_f32_32x32x16_bf16 v[80:95], a[28:31], v[172:175], v[80:95]\n\t" "v_mul_f32 v102, v102, v11\n\t" "v_mul_f32 v103, v103, v11\n\t" \
   "v_mul_f32 v104, v104, v11\n\t" "v_mul_f32 v105, v105, v11\n\t" "v_mul_f32 v106, v106, v11\n\t" "s_waitcnt lgkmcnt(2)\n\t" \
   "v_mfma_f32_32x32x16_bf16 v[64:79], a[32:35], v[176:179], v[64:79]\n\t" "ds_read_b128 v[160:163], v26 offset:4096\n\t" \
   "v_mul_f32 v107, v107, v11\n\t" "v_mul_f32 v108, v108, v11\n\t" "v_mul_f32 v109, v109, v11\n\t" "v_mul_f32 v110, v110, v11\n\t" \
   "v_mfma_f32_32x32x16_bf16 v[80:95], a[40:43], v[176:179], v[80:95]\n\t" "v_mul_f32 v111, v111, v11\n\t" "s_add_u32 s48, s47, %[qf]\n\t" \
   "v_cvt_pk_bf16_f32 v96, v96, v97\n\t" "v_cvt_pk_bf16_f32 v97, v98, v99\n\t" "v_cvt_pk_bf16_f32 v98, v104, v105\n\t" \
   "v_cvt_pk_bf16_f32 v99, v106, v107\n\t" "v_mfma_f32_32x32x16_bf16 v[64:79], a[36:39], v[180:183], v[64:79]\n\t" \
   "ds_read_b128 v[164:167], v26 offset:4608\n\t" "v_mfma_f32_32x32x16_bf16 v[80:95], a[44:47], v[180:183], v[80:95]\n\t" \
   "buffer_store_dwordx4 v[96:99], v30, %[rs], s48 offen offset:0 sc1\n\t" "s_waitcnt lgkmcnt(2)\n\t" \
   "v_mfma_f32_32x32x16_bf16 v[64:79], a[48:51], v[184:187], v[64:79]\n\t" "ds_read_b128 v[168:171], v26 offset:5120\n\t" \
   "v_cvt_pk_bf16_f32 v100, v100, v101\n\t" "v_cvt_pk_bf16_f32 v101, v102, v103\n\t" "v_cvt_pk_bf16_f32 v102, v108, v109\n\t" \
   "v_cvt_pk_bf16_f32 v103, v110, v111\n\t" "v_mfma_f32_32x32x16_bf16 v[80:95], a[56:59], v[184:187], v[80:95]\n\t" \
   "buffer_store_dwordx4 v[100:103], v30, %[rs], s48 offen offset:512 sc1\n\t" "v_fmac_f32 v112, v48, v8\n\t" \
   "v_mfma_f32_32x32x16_bf16 v[64:79], a[52:55], v[188:191], v[64:79]\n\t" "ds_read_b128 v[172:175], v26 offset:5632\n\t" \
   "v_fmac_f32 v113, v49, v8\n\t" "v_fmac_f32 v114, v50, v8\n\t" "v_fmac_f32 v115, v51, v8\n\t" "v_fmac_f32 v116, v52, v8\n\t" \
   "v_mfma_f32_32x32x16_bf16 v[80:95], a[60:63], v[188:191], v[80:95]\n\t" "v_fmac_f32 v117, v53, v8\n\t" "v_fmac_f32 v118, v54, v8\n\t" \
   "v_fmac_f32 v119, v55, v8\n\t" "v_fmac_f32 v120, v56, v8\n\t" "v_fmac_f32 v121, v57, v8\n\t" "s_waitcnt lgkmcnt(2)\n\t" \
   "v_mfma_f32_32x32x16_bf16 v[64:79], a[64:67], v[160:163], v[64:79]\n\t" "ds_read_b128 v[176:179], v26 offset:6144\n\t" \
   "v_fmac_f32 v122, v58, v8\n\t" "v_fmac_f32 v123, v59, v8\n\t" "v_fmac_f32 v124, v60, v8\n\t" "v_fmac_f32 v125, v61, v8\n\t" \
   "v_mfma_f32_32x32x16_bf16 v[80:95], a[72:75], v[160:163], v[80:95]\n\t" "v_fmac_f32 v126, v62, v8\n\t" "v_fmac_f32 v127, v63, v8\n\t" \
   "v_mul_f32 v112, v112, v10\n\t" "v_mul_f32 v113, v113, v10\n\t" "v_mul_f32 v114, v114, v10\n\t" "v_mul_f32 v115, v115, v10\n\t" \
   "v_mfma_f32_32x32x16_bf16 v[64:79], a[68:71], v[164:167], v[64:79]\n\t" "ds_read_b128 v[180:183], v26 offset:6656\n\t" \
   "v_mul_f32 v116, v116, v10\n\t" "v_mul_f32 v117, v117, v10\n\t" "v_mul_f32 v118, v118, v10\n\t" "v_mul_f32 v119, v119, v10\n\t" \
   "v_mfma_f32_32x32x16_bf16 v[80:95], a[76:79], v[164:167], v[80:95]\n\t" "v_mul_f32 v120, v120, v10\n\t" "v_mul_f32 v121, v121, v10\n\t" \
   "v_mul_f32 v122, v122, v10\n\t" "v_mul_f32 v123, v123, v10\n\t" "v_mul_f32 v124, v124, v10\n\t" "s_waitcnt lgkmcnt(2)\n\t" \
   "v_mfma_f32_32x32x16_bf16 v[64:79], a[80:83], v[168:171], v[64:79]\n\t" "ds_read_b128 v[184:187], v26 offset:7168\n\t" \
   "v_mul_f32 v125, v125, v10\n\t" "v_mul_f32 v126, v126, v10\n\t" "v_mul_f32 v127, v127, v10\n\t" "s_add_u32 s48, s47, %[kf]\n\t" \
   "v_mfma_f32_32x32x16_bf16 v[80:95], a[88:91], v[168:171], v[80:95]\n\t" "v_cvt_pk_bf16_f32 v112, v112, v113\n\t" \
   "v_cvt_pk_bf16_f32 v113, v114, v115\n\t" "v_cvt_pk_bf16_f32 v114, v120, v121\n\t" "v_cvt_pk_bf16_f32 v115, v122, v123\n\t" \
   "ds_read_b128 v[128:131], v27 offset:0\n\t" "v_mfma_f32_32x32x16_bf16 v[64:79], a[84:87], v[172:175], v[64:79]\n\t" \
   "ds_read_b128 v[188:191], v26 offset:7680\n\t" "ds_read_b128 v[132:135], v27 offset:16\n\t" "ds_read_b64 v[136:137], v27 offset:32\n\t" \
   "v_mfma_f32_32x32x16_bf16 v[80:95], a[92:95], v[172:175], v[80:95]\n\t" "buffer_store_dwordx4 v[112:115], v30, %[rs], s48 offen offset:0 sc1\n\t" \
   "s_waitcnt lgkmcnt(5)\n\t" "v_mfma_f32_32x32x16_bf16 v[64:79], a[96:99], v[176:179], v[64:79]\n\t" "ds_read_b128 v[160:163], v26 offset:8192\n\t" \
   "v_cvt_pk_bf16_f32 v116, v116, v117\n\t" "v_cvt_pk_bf16_f32 v117, v118, v119\n\t" "v_cvt_pk_bf16_f32 v118, v124, v125\n\t" \
   "v_cvt_pk_bf16_f32 v119, v126, v127\n\t" "v_mfma_f32_32x32x16_bf16 v[80:95], a[104:107], v[176:179], v[80:95]\n\t" \
   "buffer_store_dwordx4 v[116:119], v30, %[rs], s48 offen offset:512 sc1\n\t" \
   "v_mfma_f32_32x32x16_bf16 v[64:79], a[100:103], v[180:183], v[64:79]\n\t" "ds_read_b128 v[164:167], v26 offset:8704\n\t" \
   "v_add_u32 v28, s43, v22\n\t" "v_add_u32 v29, s43, v23\n\t" "v_mfma_f32_32x32x16_bf16 v[80:95], a[108:111], v[180:183], v[80:95]\n\t" \
   "s_waitcnt lgkmcnt(4)\n\t" "v_mfma_f32_32x32x16_bf16 v[64:79], a[112:115], v[184:187], v[64:79]\n\t" "ds_read_b128 v[168:171], v26 offset:9216\n\t" \
   "v_mfma_f32_32x32x16_bf16 v[80:95], a[120:123], v[184:187], v[80:95]\n\t" "ds_read_b128 v[140:143], v12\n\t" "s_waitcnt lgkmcnt(4)\n\t" \
   "v_add_f32 v128, v128, v130\n\t" "v_add_f32 v129, v129, v131\n\t" "v_add_f32 v132, v132, v134\n\t" "v_add_f32 v133, v133, v135\n\t" \
   "v_mfma_f32_32x32x16_bf16 v[64:79], a[116:119], v[188:191], v[64:79]\n\t" "ds_read_b128 v[172:175], v26 offset:9728\n\t" \
   "v_add_f32 v128, v128, v136\n\t" "v_add_f32 v129, v129, v137\n\t" "v_add_f32 v128, v128, v132\n\t" "v_add_f32 v129, v129, v133\n\t" \
   "v_mfma_f32_32x32x16_bf16 v[80:95], a[124:127], v[188:191], v[80:95]\n\t" "v_mul_f32 v128, 0xbacccccd, v128\n\t" \
   "v_mul_f32 v129, 0x3acccccd, v129\n\t" "v_fma_f32 v129, -v128, v128, v129\n\t" "v_max_f32 v129, 0, v129\n\t" \
   "v_cvt_pk_bf16_f32 v129, v129, v128\n\t" "s_waitcnt lgkmcnt(3)\n\t" "v_mfma_f32_32x32x16_bf16 v[64:79], a[128:131], v[160:163], v[64:79]\n\t" \
   "ds_read_b128 v[176:179], v26 offset:10240\n\t" "s_waitcnt lgkmcnt(2)\n\t" "v_min3_u32 v140, v140, v141, v142\n\t" "v_min_u32 v140, v140, v143\n\t" \
   "v_cmp_ge_u32 vcc, v140, v138\n\t" "v_mfma_f32_32x32x16_bf16 v[80:95], a[136:139], v[160:163], v[80:95]\n\t" "s_cbranch_vccz 150f\n\t" "50:\n\t" \
   "s_mul_i32 s52, s45, 1536\n\t" "s_mul_i32 s51, s45, 40960\n\t" "s_add_u32 m0, s46, %[ldx]\n\t" "s_mov_b32 s53, s46\n\t" \
   "v_mfma_f32_32x32x16_bf16 v[64:79], a[132:135], v[164:167], v[64:79]\n\t" "ds_read_b128 v[180:183], v26 offset:10752\n\t" \
   "buffer_load_dwordx4 v16, %[rs], s51 offen offset:0 sc1 lds\n\t" "v_and_b32 v8, 0xffff0000, v129\n\t" \
   "v_mfma_f32_32x32x16_bf16 v[80:95], a[140:143], v[164:167], v[80:95]\n\t" "v_lshlrev_b32 v129, 16, v129\n\t" "v_add_f32 v129, 0x3727c5ac, v129\n\t" \
   "v_cvt_pk_bf16_f32 v129, v129, v129\n\t" "v_lshlrev_b32 v129, 16, v129\n\t" "s_waitcnt lgkmcnt(2)\n\t" \
   "v_mfma_f32_32x32x16_bf16 v[64:79], a[144:147], v[168:171], v[64:79]\n\t" "ds_read_b128 v[184:187], v26 offset:11264\n\t" \
   "v_sqrt_f32 v129, v129\n\t" "s_nop 0\n\t" "v_cvt_pk_bf16_f32 v129, v129, v129\n\t" \
   "v_mfma_f32_32x32x16_bf16 v[80:95], a[152:155], v[168:171], v[80:95]\n\t" "buffer_load_dwordx4 v16, %[rs], s51 offen offset:1024 sc1 lds\n\t" \
   "v_lshlrev_b32 v129, 16, v129\n\t" "v_rcp_f32 v10, v129\n\t" "v_mfma_f32_32x32x16_bf16 v[64:79], a[148:151], v[172:175], v[64:79]\n\t" \
   "ds_read_b128 v[188:191], v26 offset:11776\n\t" "v_mfma_f32_32x32x16_bf16 v[80:95], a[156:159], v[172:175], v[80:95]\n\t" "s_waitcnt lgkmcnt(2)\n\t" \
   "v_mfma_f32_32x32x16_bf16 v[64:79], a[160:163], v[176:179], v[64:79]\n\t" "ds_read_b128 v[160:163], v26 offset:12288\n\t" \
   "buffer_load_dwordx4 v16, %[rs], s51 offen offset:2048 sc1 lds\n\t" "v_mfma_f32_32x32x16_bf16 v[80:95], a[168:171], v[176:179], v[80:95]\n\t" \
   "v_mfma_f32_32x32x16_bf16 v[64:79], a[164:167], v[180:183], v[64:79]\n\t" "ds_read_b128 v[164:167], v26 offset:12800\n\t" \
   "v_mfma_f32_32x32x16_bf16 v[80:95], a[172:175], v[180:183], v[80:95]\n\t" "buffer_load_dwordx4 v16, %[rs], s51 offen offset:3072 sc1 lds\n\t" \
   "s_add_u32 m0, m0, 4096\n\t" "s_add_u32 s51, s51, 4096\n\t" "s_waitcnt lgkmcnt(2)\n\t" \
   "v_mfma_f32_32x32x16_bf16 v[64:79], a[176:179], v[184:187], v[64:79]\n\t" "ds_read_b128 v[168:171], v26 offset:13312\n\t" "s_waitcnt vmcnt(8)\n\t" \
   "v_add_u32 v138, 1, v138\n\t" "ds_write_b32 v13, v138\n\t" "v_mfma_f32_32x32x16_bf16 v[80:95], a[184:187], v[184:187], v[80:95]\n\t" \
   "v_mfma_f32_32x32x16_bf16 v[64:79], a[180:183], v[188:191], v[64:79]\n\t" "ds_read_b128 v[172:175], v26 offset:13824\n\t" \
   "buffer_load_dwordx4 v16, %[rs], s51 offen offset:0 sc1 lds\n\t" "v_mfma_f32_32x32x16_bf16 v[80:95], a[188:191], v[188:191], v[80:95]\n\t" \
   "s_waitcnt lgkmcnt(3)\n\t" "v_mfma_f32_32x32x16_bf16 v[64:79], a[192:195], v[160:163], v[64:79]\n\t" "ds_read_b128 v[176:179], v26 offset:14336\n\t" \
   "v_mfma_f32_32x32x16_bf16 v[80:95], a[200:203], v[160:163], v[80:95]\n\t" "buffer_load_dwordx4 v16, %[rs], s51 offen offset:1024 sc1 lds\n\t" \
   "v_mfma_f32_32x32x16_bf16 v[64:79], a[196:199], v[164:167], v[64:79]\n\t" "ds_read_b128 v[180:183], v26 offset:14848\n\t" \
   "v_mfma_f32_32x32x16_bf16 v[80:95], a[204:207], v[164:167], v[80:95]\n\t" "s_waitcnt lgkmcnt(2)\n\t" \
   "v_mfma_f32_32x32x16_bf16 v[64:79], a[208:211], v[168:171], v[64:79]\n\t" "ds_read_b128 v[184:187], v26 offset:15360\n\t" \
   "buffer_load_dwordx4 v16, %[rs], s51 offen offset:2048 sc1 lds\n\t" "v_mfma_f32_32x32x16_bf16 v[80:95], a[216:219], v[168:171], v[80:95]\n\t" \
   "v_mfma_f32_32x32x16_bf16 v[64:79], a[212:215], v[172:175], v[64:79]\n\t" "ds_read_b128 v[188:191], v26 offset:15872\n\t" \
   "v_mfma_f32_32x32x16_bf16 v[80:95], a[220:223], v[172:175], v[80:95]\n\t" "buffer_load_dwordx4 v16, %[rs], s51 offen offset:3072 sc1 lds\n\t" \
   "s_add_u32 m0, m0, 4096\n\t" "s_add_u32 s51, s51, 4096\n\t" "s_waitcnt lgkmcnt(2)\n\t" \
   "v_mfma_f32_32x32x16_bf16 v[64:79], a[224:227], v[176:179], v[64:79]\n\t" "ds_read_b128 v[160:163], v26 offset:16384\n\t" \
   "v_mfma_f32_32x32x16_bf16 v[80:95], a[232:235], v[176:179], v[80:95]\n\t" "v_mfma_f32_32x32x16_bf16 v[64:79], a[228:231], v[180:183], v[64:79]\n\t" \
   "ds_read_b128 v[164:167], v26 offset:16896\n\t" "buffer_load_dwordx4 v16, %[rs], s51 offen offset:0 sc1 lds\n\t" \
   "v_mfma_f32_32x32x16_bf16 v[80:95], a[236:239], v[180:183], v[80:95]\n\t" "s_waitcnt lgkmcnt(2)\n\t" \
   "v_mfma_f32_32x32x16_bf16 v[64:79], a[240:243], v[184:187], v[64:79]\n\t" "ds_read_b128 v[168:171], v26 offset:17408\n\t" "s_cmp_lt_i32 s40, 1\n\t" \
   "s_cselect_b32 s49, 0x7ffffff0, 0\n\t" "v_or_b32 v9, s49, v20\n\t" "s_sub_i32 s49, s41, 1\n\t" \
   "v_mfma_f32_32x32x16_bf16 v[80:95], a[248:251], v[184:187], v[80:95]\n\t" "s_max_i32 s49, s49, 0\n\t" "s_lshl_b32 s49, s49, 12\n\t" \
   "s_waitcnt vmcnt(9)\n\t" "buffer_store_dword v31, v9, %[rs], s49 offen sc1\n\t" \
   "v_mfma_f32_32x32x16_bf16 v[64:79], a[244:247], v[188:191], v[64:79]\n\t" "ds_read_b128 v[172:175], v26 offset:17920\n\t" \
   "v_mfma_f32_32x32x16_bf16 v[80:95], a[252:255], v[188:191], v[80:95]\n\t" "buffer_load_dwordx4 v16, %[rs], s51 offen offset:1024 sc1 lds\n\t" \
   "s_add_u32 m0, s53, %[ld1]\n\t" "s_nop 0\n\t" "s_waitcnt lgkmcnt(2)\n\t" "v_mfma_f32_32x32x16_bf16 v[64:79], v[192:195], v[160:163], v[64:79]\n\t" \
   "ds_read_b128 v[176:179], v26 offset:18432\n\t" "buffer_load_dwordx4 v17, %[rs], s52 offen sc1 lds\n\t" "ds_read_b128 v[140:143], v12\n\t" \
   "v_mfma_f32_32x32x16_bf16 v[80:95], v[200:203], v[160:163], v[80:95]\n\t" "v_mfma_f32_32x32x16_bf16 v[64:79], v[196:199], v[164:167], v[64:79]\n\t" \
   "ds_read_b128 v[180:183], v26 offset:18944\n\t" "v_mfma_f32_32x32x16_bf16 v[80:95], v[204:207], v[164:167], v[80:95]\n\t" "s_waitcnt lgkmcnt(3)\n\t" \
   "v_mfma_f32_32x32x16_bf16 v[64:79], v[208:211], v[168:171], v[64:79]\n\t" "ds_read_b128 v[184:187], v26 offset:19456\n\t" "s_waitcnt lgkmcnt(2)\n\t" \
   "v_min3_u32 v140, v140, v141, v142\n\t" "v_min_u32 v140, v140, v143\n\t" "v_cmp_ge_u32 vcc, v140, v138\n\t" \
   "v_mfma_f32_32x32x16_bf16 v[80:95], v[216:219], v[168:171], v[80:95]\n\t" "s_cbranch_vccz 152f\n\t" "52:\n\t" \
   "v_mfma_f32_32x32x16_bf16 v[64:79], v[212:215], v[172:175], v[64:79]\n\t" "ds_read_b128 v[188:191], v26 offset:19968\n\t" \
   "v_mfma_f32_32x32x16_bf16 v[80:95], v[220:223], v[172:175], v[80:95]\n\t" "v_add_u32 v138, 1, v138\n\t" "ds_write_b32 v13, v138\n\t" \
   "s_waitcnt lgkmcnt(3)\n\t" "v_mfma_f32_32x32x16_bf16 v[64:79], v[224:227], v[176:179], v[64:79]\n\t" "ds_read_b128 v[160:163], v28 offset:0\n\t" \
   "s_add_u32 s45, s40, 3\n\t" "s_min_u32 s45, s45, 63\n\t" "s_mov_b32 s46, s42\n\t" \
   "v_mfma_f32_32x32x16_bf16 v[80:95], v[232:235], v[176:179], v[80:95]\n\t" "s_cmp_eq_u32 s44, -1\n\t" "s_cbranch_scc0 174f\n\t" "74:\n\t" \
   "s_mov_b32 s42, s43\n\t" "s_add_u32 s43, s43, 44032\n\t" "v_mfma_f32_32x32x16_bf16 v[64:79], v[228:231], v[180:183], v[64:79]\n\t" \
   "ds_read_b128 v[164:167], v28 offset:512\n\t" "s_cmp_eq_u32 s43, 132096\n\t" "s_cselect_b32 s43, 0, s43\n\t" "s_add_u32 s41, s41, 1\n\t" \
   "s_add_u32 s40, s40, 1\n\t" "v_mfma_f32_32x32x16_bf16 v[80:95], v[236:239], v[180:183], v[80:95]\n\t" "s_waitcnt lgkmcnt(3)\n\t" \
   "v_mfma_f32_32x32x16_bf16 v[64:79], v[240:243], v[184:187], v[64:79]\n\t" "ds_read_b128 v[168:171], v28 offset:1024\n\t" \
   "v_mfma_f32_32x32x16_bf16 v[80:95], v[248:251], v[184:187], v[80:95]\n\t" "v_mfma_f32_32x32x16_bf16 v[64:79], v[244:247], v[188:191], v[64:79]\n\t" \
   "ds_read_b128 v[172:175], v28 offset:1536\n\t" "v_mfma_f32_32x32x16_bf16 v[80:95], v[252:255], v[188:191], v[80:95]\n\t" "v_mov_b32 v26, v28\n\t" \
   "v_mov_b32 v27, v29\n\t" "s_waitcnt lgkmcnt(2)\n\t" "v_mfma_f32_32x32x16_bf16 v[96:111], a[0:3], v[160:163], 0\n\t" \
   "ds_read_b128 v[176:179], v26 offset:2048\n\t" "v_mfma_f32_32x32x16_bf16 v[112:127], a[8:11], v[160:163], 0\n\t" \
   "v_mul_f32 v11, 0x3e0293ee, v10\n\t" "s_lshl_b32 s47, s41, 13\n\t" "s_sub_i32 s47, s47, 8192\n\t" "v_fmac_f32 v64, v32, v8\n\t" \
   "v_fmac_f32 v65, v33, v8\n\t" "v_fmac_f32 v66, v34, v8\n\t" "v_mfma_f32_32x32x16_bf16 v[96:111], a[4:7], v[164:167], v[96:111]\n\t" \
   "ds_read_b128 v[180:183], v26 offset:2560\n\t" "v_fmac_f32 v67, v35, v8\n\t" "v_fmac_f32 v68, v36, v8\n\t" "v_fmac_f32 v69, v37, v8\n\t" \
   "v_fmac_f32 v70, v38, v8\n\t" "v_mfma_f32_32x32x16_bf16 v[112:127], a[12:15], v[164:167], v[112:127]\n\t" "v_fmac_f32 v71, v39, v8\n\t" \
   "v_fmac_f32 v72, v40, v8\n\t" "v_fmac_f32 v73, v41, v8\n\t" "v_fmac_f32 v74, v42, v8\n\t" "v_fmac_f32 v75, v43, v8\n\t" "s_waitcnt lgkmcnt(2)\n\t" \
   "v_mfma_f32_32x32x16_bf16 v[96:111], a[16:19], v[168:171], v[96:111]\n\t" "ds_read_b128 v[184:187], v26 offset:3072\n\t" \
   "v_fmac_f32 v76, v44, v8\n\t" "v_fmac_f32 v77, v45, v8\n\t" "v_fmac_f32 v78, v46, v8\n\t" "v_fmac_f32 v79, v47, v8\n\t" \
   "v_mfma_f32_32x32x16_bf16 v[112:127], a[24:27], v[168:171], v[112:127]\n\t" "v_mul_f32 v64, v64, v11\n\t" "v_mul_f32 v65, v65, v11\n\t" \
   "v_mul_f32 v66, v66, v11\n\t" "v_mul_f32 v67, v67, v11\n\t" "v_mul_f32 v68, v68, v11\n\t" "v_mul_f32 v69, v69, v11\n\t" \
   "v_mfma_f32_32x32x16_bf16 v[96:111], a[20:23], v[172:175], v[96:111]\n\t" "ds_read_b128 v[188:191], v26 offset:3584\n\t" \
   "v_mul_f32 v70, v70, v11\n\t" "v_mul_f32 v71, v71, v11\n\t" "v_mul_f32 v72, v72, v11\n\t" "v_mul_f32 v73, v73, v11\n\t" \
   "v_mfma_f32_32x32x16_bf16 v[112:127], a[28:31], v[172:175], v[112:127]\n\t" "v_mul_f32 v74, v74, v11\n\t" "v_mul_f32 v75, v75, v11\n\t" \
   "v_mul_f32 v76, v76, v11\n\t" "v_mul_f32 v77, v77, v11\n\t" "v_mul_f32 v78, v78, v11\n\t" "s_waitcnt lgkmcnt(2)\n\t" \
   "v_mfma_f32_32x32x16_bf16 v[96:111], a[32:35], v[176:179], v[96:111]\n\t" "ds_read_b128 v[160:163], v26 offset:4096\n\t" \
   "v_mul_f32 v79, v79, v11\n\t" "s_add_u32 s48, s47, %[qf]\n\t" "v_cvt_pk_bf16_f32 v64, v64, v65\n\t" "v_cvt_pk_bf16_f32 v65, v66, v67\n\t" \
   "v_mfma_f32_32x32x16_bf16 v[112:127], a[40:43], v[176:179], v[112:127]\n\t" "v_cvt_pk_bf16_f32 v66, v72, v73\n\t" \
   "v_cvt_pk_bf16_f32 v67, v74, v75\n\t" "v_mfma_f32_32x32x16_bf16 v[96:111], a[36:39], v[180:183], v[96:111]\n\t" \
   "ds_read_b128 v[164:167], v26 offset:4608\n\t" "v_mfma_f32_32x32x16_bf16 v[112:127], a[44:47], v[180:183], v[112:127]\n\t" \
   "buffer_store_dwordx4 v[64:67], v21, %[rs], s48 offen offset:0 sc1\n\t" "s_waitcnt lgkmcnt(2)\n\t" \
   "v_mfma_f32_32x32x16_bf16 v[96:111], a[48:51], v[184:187], v[96:111]\n\t" "ds_read_b128 v[168:171], v26 offset:5120\n\t" \
   "v_cvt_pk_bf16_f32 v68, v68, v69\n\t" "v_cvt_pk_bf16_f32 v69, v70, v71\n\t" "v_cvt_pk_bf16_f32 v70, v76, v77\n\t" \
   "v_cvt_pk_bf16_f32 v71, v78, v79\n\t" "v_mfma_f32_32x32x16_bf16 v[112:127], a[56:59], v[184:187], v[112:127]\n\t" \
   "buffer_store_dwordx4 v[68:71], v21, %[rs], s48 offen offset:512 sc1\n\t" "v_fmac_f32 v80, v48, v8\n\t" \
   "v_mfma_f32_32x32x16_bf16 v[96:111], a[52:55], v[188:191], v[96:111]\n\t" "ds_read_b128 v[172:175], v26 offset:5632\n\t" \
   "v_fmac_f32 v81, v49, v8\n\t" "v_fmac_f32 v82, v50, v8\n\t" "v_fmac_f32 v83, v51, v8\n\t" "v_fmac_f32 v84, v52, v8\n\t" \
   "v_mfma_f32_32x32x16_bf16 v[112:127], a[60:63], v[188:191], v[112:127]\n\t" "v_fmac_f32 v85, v53, v8\n\t" "v_fmac_f32 v86, v54, v8\n\t" \
   "v_fmac_f32 v87, v55, v8\n\t" "v_fmac_f32 v88, v56, v8\n\t" "v_fmac_f32 v89, v57, v8\n\t" "s_waitcnt lgkmcnt(2)\n\t" \
   "v_mfma_f32_32x32x16_bf16 v[96:111], a[64:67], v[160:163], v[96:111]\n\t" "ds_read_b128 v[176:179], v26 offset:6144\n\t" \
   "v_fmac_f32 v90, v58, v8\n\t" "v_fmac_f32 v91, v59, v8\n\t" "v_fmac_f32 v92, v60, v8\n\t" "v_fmac_f32 v93, v61, v8\n\t" \
   "v_mfma_f32_32x32x16_bf16 v[112:127], a[72:75], v[160:163], v[112:127]\n\t" "v_fmac_f32 v94, v62, v8\n\t" "v_fmac_f32 v95, v63, v8\n\t" \
   "v_mul_f32 v80, v80, v10\n\t" "v_mul_f32 v81, v81, v10\n\t" "v_mul_f32 v82, v82, v10\n\t" "v_mul_f32 v83, v83, v10\n\t" \
   "v_mfma_f32_32x32x16_bf16 v[96:111], a[68:71], v[164:167], v[96:111]\n\t" "ds_read_b128 v[180:183], v26 offset:6656\n\t" \
   "v_mul_f32 v84, v84, v10\n\t" "v_mul_f32 v85, v85, v10\n\t" "v_mul_f32 v86, v86, v10\n\t" "v_mul_f32 v87, v87, v10\n\t" \
   "v_mfma_f32_32x32x16_bf16 v[112:127], a[76:79], v[164:167], v[112:127]\n\t" "v_mul_f32 v88, v88, v10\n\t" "v_mul_f32 v89, v89, v10\n\t" \
   "v_mul_f32 v90, v90, v10\n\t" "v_mul_f32 v91, v91, v10\n\t" "v_mul_f32 v92, v92, v10\n\t" "s_waitcnt lgkmcnt(2)\n\t" \
   "v_mfma_f32_32x32x16_bf16 v[96:111], a[80:83], v[168:171], v[96:111]\n\t" "ds_read_b128 v[184:187], v26 offset:7168\n\t" \
   "v_mul_f32 v93, v93, v10\n\t" "v_mul_f32 v94, v94, v10\n\t" "v_mul_f32 v95, v95, v10\n\t" "s_add_u32 s48, s47, %[kf]\n\t" \
   "v_mfma_f32_32x32x16_bf16 v[112:127], a[88:91], v[168:171], v[112:127]\n\t" "v_cvt_pk_bf16_f32 v80, v80, v81\n\t" \
   "v_cvt_pk_bf16_f32 v81, v82, v83\n\t" "v_cvt_pk_bf16_f32 v82, v88, v89\n\t" "v_cvt_pk_bf16_f32 v83, v90, v91\n\t" \
   "ds_read_b128 v[128:131], v27 offset:0\n\t" "v_mfma_f32_32x32x16_bf16 v[96:111], a[84:87], v[172:175], v[96:111]\n\t" \
   "ds_read_b128 v[188:191], v26 offset:7680\n\t" "ds_read_b128 v[132:135], v27 offset:16\n\t" "ds_read_b64 v[136:137], v27 offset:32\n\t" \
   "v_mfma_f32_32x32x16_bf16 v[112:127], a[92:95], v[172:175], v[112:127]\n\t" "buffer_store_dwordx4 v[80:83], v21, %[rs], s48 offen offset:0 sc1\n\t" \
   "s_waitcnt lgkmcnt(5)\n\t" "v_mfma_f32_32x32x16_bf16 v[96:111], a[96:99], v[176:179], v[96:111]\n\t" "ds_read_b128 v[160:163], v26 offset:8192\n\t" \
   "v_cvt_pk_bf16_f32 v84, v84, v85\n\t" "v_cvt_pk_bf16_f32 v85, v86, v87\n\t" "v_cvt_pk_bf16_f32 v86, v92, v93\n\t" \
   "v_cvt_pk_bf16_f32 v87, v94, v95\n\t" "v_mfma_f32_32x32x16_bf16 v[112:127], a[104:107], v[176:179], v[112:127]\n\t" \
   "buffer_store_dwordx4 v[84:87], v21, %[rs], s48 offen offset:512 sc1\n\t" \
   "v_mfma_f32_32x32x16_bf16 v[96:111], a[100:103], v[180:183], v[96:111]\n\t" "ds_read_b128 v[164:167], v26 offset:8704\n\t" \
   "v_add_u32 v28, s43, v22\n\t" "v_add_u32 v29, s43, v23\n\t" "v_mfma_f32_32x32x16_bf16 v[112:127], a[108:111], v[180:183], v[112:127]\n\t" \
   "s_waitcnt lgkmcnt(4)\n\t" "v_mfma_f32_32x32x16_bf16 v[96:111], a[112:115], v[184:187], v[96:111]\n\t" \
   "ds_read_b128 v[168:171], v26 offset:9216\n\t" "v_mfma_f32_32x32x16_bf16 v[112:127], a[120:123], v[184:187], v[112:127]\n\t" \
   "ds_read_b128 v[140:143], v12\n\t" "s_waitcnt lgkmcnt(4)\n\t" "v_add_f32 v128, v128, v130\n\t" "v_add_f32 v129, v129, v131\n\t" \
   "v_add_f32 v132, v132, v134\n\t" "v_add_f32 v133, v133, v135\n\t" "v_mfma_f32_32x32x16_bf16 v[96:111], a[116:119], v[188:191], v[96:111]\n\t" \
   "ds_read_b128 v[172:175], v26 offset:9728\n\t" "v_add_f32 v128, v128, v136\n\t" "v_add_f32 v129, v129, v137\n\t" "v_add_f32 v128, v128, v132\n\t" \
   "v_add_f32 v129, v129, v133\n\t" "v_mfma_f32_32x32x16_bf16 v[112:127], a[124:127], v[188:191], v[112:127]\n\t" \
   "v_mul_f32 v128, 0xbacccccd, v128\n\t" "v_mul_f32 v129, 0x3acccccd, v129\n\t" "v_fma_f32 v129, -v128, v128, v129\n\t" "v_max_f32 v129, 0, v129\n\t" \
   "v_cvt_pk_bf16_f32 v129, v129, v128\n\t" "s_waitcnt lgkmcnt(3)\n\t" "v_mfma_f32_32x32x16_bf16 v[96:111], a[128:131], v[160:163], v[96:111]\n\t" \
   "ds_read_b128 v[176:179], v26 offset:10240\n\t" "s_waitcnt lgkmcnt(2)\n\t" "v_min3_u32 v140, v140, v141, v142\n\t" "v_min_u32 v140, v140, v143\n\t" \
   "v_cmp_ge_u32 vcc, v140, v138\n\t" "v_mfma_f32_32x32x16_bf16 v[112:127], a[136:139], v[160:163], v[112:127]\n\t" "s_cbranch_vccz 151f\n\t" "51:\n\t" \
   "s_mul_i32 s52, s45, 1536\n\t" "s_mul_i32 s51, s45, 40960\n\t" "s_add_u32 m0, s46, %[ldx]\n\t" "s_mov_b32 s53, s46\n\t" \
   "v_mfma_f32_32x32x16_bf16 v[96:111], a[132:135], v[164:167], v[96:111]\n\t" "ds_read_b128 v[180:183], v26 offset:10752\n\t" \
   "buffer_load_dwordx4 v16, %[rs], s51 offen offset:0 sc1 lds\n\t" "v_and_b32 v8, 0xffff0000, v129\n\t" \
   "v_mfma_f32_32x32x16_bf16 v[112:127], a[140:143], v[164:167], v[112:127]\n\t" "v_lshlrev_b32 v129, 16, v129\n\t" \
   "v_add_f32 v129, 0x3727c5ac, v129\n\t" "v_cvt_pk_bf16_f32 v129, v129, v129\n\t" "v_lshlrev_b32 v129, 16, v129\n\t" "s_waitcnt lgkmcnt(2)\n\t" \
   "v_mfma_f32_32x32x16_bf16 v[96:111], a[144:147], v[168:171], v[96:111]\n\t" "ds_read_b128 v[184:187], v26 offset:11264\n\t" \
   "v_sqrt_f32 v129, v129\n\t" "s_nop 0\n\t" "v_cvt_pk_bf16_f32 v129, v129, v129\n\t" \
   "v_mfma_f32_32x32x16_bf16 v[112:127], a[152:155], v[168:171], v[112:127]\n\t" "buffer_load_dwordx4 v16, %[rs], s51 offen offset:1024 sc1 lds\n\t" \
   "v_lshlrev_b32 v129, 16, v129\n\t" "v_rcp_f32 v10, v129\n\t" "v_mfma_f32_32x32x16_bf16 v[96:111], a[148:151], v[172:175], v[96:111]\n\t" \
   "ds_read_b128 v[188:191], v26 offset:11776\n\t" "v_mfma_f32_32x32x16_bf16 v[112:127], a[156:159], v[172:175], v[112:127]\n\t" \
   "s_waitcnt lgkmcnt(2)\n\t" "v_mfma_f32_32x32x16_bf16 v[96:111], a[160:163], v[176:179], v[96:111]\n\t" \
   "ds_read_b128 v[160:163], v26 offset:12288\n\t" "buffer_load_dwordx4 v16, %[rs], s51 offen offset:2048 sc1 lds\n\t" \
   "v_mfma_f32_32x32x16_bf16 v[112:127], a[168:171], v[176:179], v[112:127]\n\t" \
   "v_mfma_f32_32x32x16_bf16 v[96:111], a[164:167], v[180:183], v[96:111]\n\t" "ds_read_b128 v[164:167], v26 offset:12800\n\t" \
   "v_mfma_f32_32x32x16_bf16 v[112:127], a[172:175], v[180:183], v[112:127]\n\t" "buffer_load_dwordx4 v16, %[rs], s51 offen offset:3072 sc1 lds\n\t" \
   "s_add_u32 m0, m0, 4096\n\t" "s_add_u32 s51, s51, 4096\n\t" "s_waitcnt lgkmcnt(2)\n\t" \
   "v_mfma_f32_32x32x16_bf16 v[96:111], a[176:179], v[184:187], v[96:111]\n\t" "ds_read_b128 v[168:171], v26 offset:13312\n\t" "s_waitcnt vmcnt(8)\n\t" \
   "v_add_u32 v138, 1, v138\n\t" "ds_write_b32 v13, v138\n\t" "v_mfma_f32_32x32x16_bf16 v[112:127], a[184:187], v[184:187], v[112:127]\n\t" \
   "v_mfma_f32_32x32x16_bf16 v[96:111], a[180:183], v[188:191], v[96:111]\n\t" "ds_read_b128 v[172:175], v26 offset:13824\n\t" \
   "buffer_load_dwordx4 v16, %[rs], s51 offen offset:0 sc1 lds\n\t" "v_mfma_f32_32x32x16_bf16 v[112:127], a[188:191], v[188:191], v[112:127]\n\t" \
   "s_waitcnt lgkmcnt(3)\n\t" "v_mfma_f32_32x32x16_bf16 v[96:111], a[192:195], v[160:163], v[96:111]\n\t" \
   "ds_read_b128 v[176:179], v26 offset:14336\n\t" "v_mfma_f32_32x32x16_bf16 v[112:127], a[200:203], v[160:163], v[112:127]\n\t" \
   "buffer_load_dwordx4 v16, %[rs], s51 offen offset:1024 sc1 lds\n\t" "v_mfma_f32_32x32x16_bf16 v[96:111], a[196:199], v[164:167], v[96:111]\n\t" \
   "ds_read_b128 v[180:183], v26 offset:14848\n\t" "v_mfma_f32_32x32x16_bf16 v[112:127], a[204:207], v[164:167], v[112:127]\n\t" \
   "s_waitcnt lgkmcnt(2)\n\t" "v_mfma_f32_32x32x16_bf16 v[96:111], a[208:211], v[168:171], v[96:111]\n\t" \
   "ds_read_b128 v[184:187], v26 offset:15360\n\t" "buffer_load_dwordx4 v16, %[rs], s51 offen offset:2048 sc1 lds\n\t" \
   "v_mfma_f32_32x32x16_bf16 v[112:127], a[216:219], v[168:171], v[112:127]\n\t" \
   "v_mfma_f32_32x32x16_bf16 v[96:111], a[212:215], v[172:175], v[96:111]\n\t" "ds_read_b128 v[188:191], v26 offset:15872\n\t" \
   "v_mfma_f32_32x32x16_bf16 v[112:127], a[220:223], v[172:175], v[112:127]\n\t" "buffer_load_dwordx4 v16, %[rs], s51 offen offset:3072 sc1 lds\n\t" \
   "s_add_u32 m0, m0, 4096\n\t" "s_add_u32 s51, s51, 4096\n\t" "s_waitcnt lgkmcnt(2)\n\t" \
   "v_mfma_f32_32x32x16_bf16 v[96:111], a[224:227], v[176:179], v[96:111]\n\t" "ds_read_b128 v[160:163], v26 offset:16384\n\t" \
   "v_mfma_f32_32x32x16_bf16 v[112:127], a[232:235], v[176:179], v[112:127]\n\t" \
   "v_mfma_f32_32x32x16_bf16 v[96:111], a[228:231], v[180:183], v[96:111]\n\t" "ds_read_b128 v[164:167], v26 offset:16896\n\t" \
   "buffer_load_dwordx4 v16, %[rs], s51 offen offset:0 sc1 lds\n\t" "v_mfma_f32_32x32x16_bf16 v[112:127], a[236:239], v[180:183], v[112:127]\n\t" \
   "s_waitcnt lgkmcnt(2)\n\t" "v_mfma_f32_32x32x16_bf16 v[96:111], a[240:243], v[184:187], v[96:111]\n\t" \
   "ds_read_b128 v[168:171], v26 offset:17408\n\t" "s_cmp_lt_i32 s40, 1\n\t" "s_cselect_b32 s49, 0x7ffffff0, 0\n\t" "v_or_b32 v9, s49, v20\n\t" \
   "s_sub_i32 s49, s41, 1\n\t" "v_mfma_f32_32x32x16_bf16 v[112:127], a[248:251], v[184:187], v[112:127]\n\t" "s_max_i32 s49, s49, 0\n\t" \
   "s_lshl_b32 s49, s49, 12\n\t" "s_waitcnt vmcnt(9)\n\t" "buffer_store_dword v31, v9, %[rs], s49 offen sc1\n\t" \
   "v_mfma_f32_32x32x16_bf16 v[96:111], a[244:247], v[188:191], v[96:111]\n\t" "ds_read_b128 v[172:175], v26 offset:17920\n\t" \
   "v_mfma_f32_32x32x16_bf16 v[112:127], a[252:255], v[188:191], v[112:127]\n\t" "buffer_load_dwordx4 v16, %[rs], s51 offen offset:1024 sc1 lds\n\t" \
   "s_add_u32 m0, s53, %[ld1]\n\t" "s_nop 0\n\t" "s_waitcnt lgkmcnt(2)\n\t" "v_mfma_f32_32x32x16_bf16 v[96:111], v[192:195], v[160:163], v[96:111]\n\t" \
   "ds_read_b128 v[176:179], v26 offset:18432\n\t" "buffer_load_dwordx4 v17, %[rs], s52 offen sc1 lds\n\t" "ds_read_b128 v[140:143], v12\n\t" \
   "v_mfma_f32_32x32x16_bf16 v[112:127], v[200:203], v[160:163], v[112:127]\n\t" \
   "v_mfma_f32_32x32x16_bf16 v[96:111], v[196:199], v[164:167], v[96:111]\n\t" "ds_read_b128 v[180:183], v26 offset:18944\n\t" \
   "v_mfma_f32_32x32x16_bf16 v[112:127], v[204:207], v[164:167], v[112:127]\n\t" "s_waitcnt lgkmcnt(3)\n\t" \
   "v_mfma_f32_32x32x16_bf16 v[96:111], v[208:211], v[168:171], v[96:111]\n\t" "ds_read_b128 v[184:187], v26 offset:19456\n\t" \
   "s_waitcnt lgkmcnt(2)\n\t" "v_min3_u32 v140, v140, v141, v142\n\t" "v_min_u32 v140, v140, v143\n\t" "v_cmp_ge_u32 vcc, v140, v138\n\t" \
   "v_mfma_f32_32x32x16_bf16 v[112:127], v[216:219], v[168:171], v[112:127]\n\t" "s_cbranch_vccz 153f\n\t" "53:\n\t" \
   "v_mfma_f32_32x32x16_bf16 v[96:111], v[212:215], v[172:175], v[96:111]\n\t" "ds_read_b128 v[188:191], v26 offset:19968\n\t" \
   "v_mfma_f32_32x32x16_bf16 v[112:127], v[220:223], v[172:175], v[112:127]\n\t" "v_add_u32 v138, 1, v138\n\t" "ds_write_b32 v13, v138\n\t" \
   "s_waitcnt lgkmcnt(3)\n\t" "v_mfma_f32_32x32x16_bf16 v[96:111], v[224:227], v[176:179], v[96:111]\n\t" "ds_read_b128 v[160:163], v28 offset:0\n\t" \
   "s_add_u32 s45, s40, 3\n\t" "s_min_u32 s45, s45, 63\n\t" "s_mov_b32 s46, s42\n\t" \
   "v_mfma_f32_32x32x16_bf16 v[112:127], v[232:235], v[176:179], v[112:127]\n\t" "s_cmp_eq_u32 s44, -1\n\t" "s_cbranch_scc0 178f\n\t" "78:\n\t" \
   "s_mov_b32 s42, s43\n\t" "s_add_u32 s43, s43, 44032\n\t" "v_mfma_f32_32x32x16_bf16 v[96:111], v[228:231], v[180:183], v[96:111]\n\t" \
   "ds_read_b128 v[164:167], v28 offset:512\n\t" "s_cmp_eq_u32 s43, 132096\n\t" "s_cselect_b32 s43, 0, s43\n\t" "s_add_u32 s41, s41, 1\n\t" \
   "s_add_u32 s40, s40, 1\n\t" "v_mfma_f32_32x32x16_bf16 v[112:127], v[236:239], v[180:183], v[112:127]\n\t" "s_waitcnt lgkmcnt(3)\n\t" \
   "v_mfma_f32_32x32x16_bf16 v[96:111], v[240:243], v[184:187], v[96:111]\n\t" "ds_read_b128 v[168:171], v28 offset:1024\n\t" \
   "v_mfma_f32_32x32x16_bf16 v[112:127], v[248:251], v[184:187], v[112:127]\n\t" \
   "v_mfma_f32_32x32x16_bf16 v[96:111], v[244:247], v[188:191], v[96:111]\n\t" "ds_read_b128 v[172:175], v28 offset:1536\n\t" \
   "v_mfma_f32_32x32x16_bf16 v[112:127], v[252:255], v[188:191], v[112:127]\n\t" "s_cmp_lt_u32 s40, 64\n\t" "s_cbranch_scc1 1b\n\t" "s_nop 7\n\t" \
   "v_mul_f32 v11, 0x3e0293ee, v10\n\t" "s_lshl_b32 s47, s41, 13\n\t" "s_sub_i32 s47, s47, 8192\n\t" "v_fmac_f32 v96, v32, v8\n\t" \
   "v_fmac_f32 v97, v33, v8\n\t" "v_fmac_f32 v98, v34, v8\n\t" "v_fmac_f32 v99, v35, v8\n\t" "v_fmac_f32 v100, v36, v8\n\t" \
   "v_fmac_f32 v101, v37, v8\n\t" "v_fmac_f32 v102, v38, v8\n\t" "v_fmac_f32 v103, v39, v8\n\t" "v_fmac_f32 v104, v40, v8\n\t" \
   "v_fmac_f32 v105, v41, v8\n\t" "v_fmac_f32 v106, v42, v8\n\t" "v_fmac_f32 v107, v43, v8\n\t" "v_fmac_f32 v108, v44, v8\n\t" \
   "v_fmac_f32 v109, v45, v8\n\t" "v_fmac_f32 v110, v46, v8\n\t" "v_fmac_f32 v111, v47, v8\n\t" "v_mul_f32 v96, v96, v11\n\t" \
   "v_mul_f32 v97, v97, v11\n\t" "v_mul_f32 v98, v98, v11\n\t" "v_mul_f32 v99, v99, v11\n\t" "v_mul_f32 v100, v100, v11\n\t" \
   "v_mul_f32 v101, v101, v11\n\t" "v_mul_f32 v102, v102, v11\n\t" "v_mul_f32 v103, v103, v11\n\t" "v_mul_f32 v104, v104, v11\n\t" \
   "v_mul_f32 v105, v105, v11\n\t" "v_mul_f32 v106, v106, v11\n\t" "v_mul_f32 v107, v107, v11\n\t" "v_mul_f32 v108, v108, v11\n\t" \
   "v_mul_f32 v109, v109, v11\n\t" "v_mul_f32 v110, v110, v11\n\t" "v_mul_f32 v111, v111, v11\n\t" "s_add_u32 s48, s47, %[qf]\n\t" \
   "v_cvt_pk_bf16_f32 v96, v96, v97\n\t" "v_cvt_pk_bf16_f32 v97, v98, v99\n\t" "v_cvt_pk_bf16_f32 v98, v104, v105\n\t" \
   "v_cvt_pk_bf16_f32 v99, v106, v107\n\t" "buffer_store_dwordx4 v[96:99], v21, %[rs], s48 offen offset:0 sc1\n\t" \
   "v_cvt_pk_bf16_f32 v100, v100, v101\n\t" "v_cvt_pk_bf16_f32 v101, v102, v103\n\t" "v_cvt_pk_bf16_f32 v102, v108, v109\n\t" \
   "v_cvt_pk_bf16_f32 v103, v110, v111\n\t" "buffer_store_dwordx4 v[100:103], v21, %[rs], s48 offen offset:512 sc1\n\t" "v_fmac_f32 v112, v48, v8\n\t" \
   "v_fmac_f32 v113, v49, v8\n\t" "v_fmac_f32 v114, v50, v8\n\t" "v_fmac_f32 v115, v51, v8\n\t" "v_fmac_f32 v116, v52, v8\n\t" \
   "v_fmac_f32 v117, v53, v8\n\t" "v_fmac_f32 v118, v54, v8\n\t" "v_fmac_f32 v119, v55, v8\n\t" "v_fmac_f32 v120, v56, v8\n\t" \
   "v_fmac_f32 v121, v57, v8\n\t" "v_fmac_f32 v122, v58, v8\n\t" "v_fmac_f32 v123, v59, v8\n\t" "v_fmac_f32 v124, v60, v8\n\t" \
   "v_fmac_f32 v125, v61, v8\n\t" "v_fmac_f32 v126, v62, v8\n\t" "v_fmac_f32 v127, v63, v8\n\t" "v_mul_f32 v112, v112, v10\n\t" \
   "v_mul_f32 v113, v113, v10\n\t" "v_mul_f32 v114, v114, v10\n\t" "v_mul_f32 v115, v115, v10\n\t" "v_mul_f32 v116, v116, v10\n\t" \
   "v_mul_f32 v117, v117, v10\n\t" "v_mul_f32 v118, v118, v10\n\t" "v_mul_f32 v119, v119, v10\n\t" "v_mul_f32 v120, v120, v10\n\t" \
   "v_mul_f32 v121, v121, v10\n\t" "v_mul_f32 v122, v122, v10\n\t" "v_mul_f32 v123, v123, v10\n\t" "v_mul_f32 v124, v124, v10\n\t" \
   "v_mul_f32 v125, v125, v10\n\t" "v_mul_f32 v126, v126, v10\n\t" "v_mul_f32 v127, v127, v10\n\t" "s_add_u32 s48, s47, %[kf]\n\t" \
   "v_cvt_pk_bf16_f32 v112, v112, v113\n\t" "v_cvt_pk_bf16_f32 v113, v114, v115\n\t" "v_cvt_pk_bf16_f32 v114, v120, v121\n\t" \
   "v_cvt_pk_bf16_f32 v115, v122, v123\n\t" "buffer_store_dwordx4 v[112:115], v21, %[rs], s48 offen offset:0 sc1\n\t" \
   "v_cvt_pk_bf16_f32 v116, v116, v117\n\t" "v_cvt_pk_bf16_f32 v117, v118, v119\n\t" "v_cvt_pk_bf16_f32 v118, v124, v125\n\t" \
   "v_cvt_pk_bf16_f32 v119, v126, v127\n\t" "buffer_store_dwordx4 v[116:119], v21, %[rs], s48 offen offset:512 sc1\n\t" \
   "s_waitcnt vmcnt(0) lgkmcnt(0)\n\t" "s_cmp_lt_i32 s40, 1\n\t" "s_cselect_b32 s47, 0x7ffffff0, 0\n\t" "v_or_b32 v30, s47, v20\n\t" \
   "s_sub_i32 s47, s41, 1\n\t" "s_max_i32 s47, s47, 0\n\t" "s_lshl_b32 s47, s47, 12\n\t" "buffer_store_dword v31, v30, %[rs], s47 offen sc1\n\t" \
   "s_branch 9f\n\t" "172:\n\t" "v_cmp_lt_i32 vcc, 6, v146\n\t" "s_lshr_b32 s48, s45, 1\n\t" "s_or_b32 s44, s44, vcc_lo\n\t" "s_cmp_lt_u32 s40, 4\n\t" \
   "s_cbranch_scc1 272f\n\t" "buffer_load_dword v146, v19, %[rs], 0 offen sc1\n\t" "272:\n\t" "s_bitcmp1_b32 s44, s48\n\t" "s_cbranch_scc1 72b\n\t" \
   "s_mov_b32 s47, 0\n\t" "buffer_load_dword v30, v19, %[rs], 0 offen sc1\n\t" "s_waitcnt vmcnt(0)\n\t" "v_add_u32 v30, 1, v30\n\t" \
   "v_cmp_lt_u32 vcc, 7, v30\n\t" "s_or_b32 s44, s44, vcc_lo\n\t" "s_bitcmp1_b32 s44, s48\n\t" "s_cbranch_scc1 72b\n\t" "s_bitset1_b32 s44, s48\n\t" \
   "s_lshl_b32 s48, s48, 7\n\t" "s_add_u32 s48, s48, %[cnt]\n\t" "73:\n\t" "s_sleep 10\n\t" "buffer_load_dword v30, off, %[rs], s48 sc1\n\t" \
   "s_add_u32 s47, s47, 1\n\t" "s_waitcnt vmcnt(0)\n\t" "v_add_u32 v30, 1, v30\n\t" "v_cmp_lt_u32 vcc, 7, v30\n\t" "s_cmp_gt_u32 s47, 0x20000\n\t" \
   "s_cbranch_scc1 72b\n\t" "s_cbranch_vccz 73b\n\t" "s_branch 72b\n\t" "174:\n\t" "v_cmp_lt_i32 vcc, 6, v146\n\t" "s_lshr_b32 s48, s45, 1\n\t" \
   "s_or_b32 s44, s44, vcc_lo\n\t" "s_cmp_lt_u32 s40, 4\n\t" "s_cbranch_scc1 274f\n\t" "buffer_load_dword v146, v19, %[rs], 0 offen sc1\n\t" "274:\n\t" \
   "s_bitcmp1_b32 s44, s48\n\t" "s_cbranch_scc1 74b\n\t" "s_mov_b32 s47, 0\n\t" "buffer_load_dword v30, v19, %[rs], 0 offen sc1\n\t" \
   "s_waitcnt vmcnt(0)\n\t" "v_add_u32 v30, 1, v30\n\t" "v_cmp_lt_u32 vcc, 7, v30\n\t" "s_or_b32 s44, s44, vcc_lo\n\t" "s_bitcmp1_b32 s44, s48\n\t" \
   "s_cbranch_scc1 74b\n\t" "s_bitset1_b32 s44, s48\n\t" "s_lshl_b32 s48, s48, 7\n\t" "s_add_u32 s48, s48, %[cnt]\n\t" "75:\n\t" "s_sleep 10\n\t" \
   "buffer_load_dword v30, off, %[rs], s48 sc1\n\t" "s_add_u32 s47, s47, 1\n\t" "s_waitcnt vmcnt(0)\n\t" "v_add_u32 v30, 1, v30\n\t" \
   "v_cmp_lt_u32 vcc, 7, v30\n\t" "s_cmp_gt_u32 s47, 0x20000\n\t" "s_cbranch_scc1 74b\n\t" "s_cbranch_vccz 75b\n\t" "s_branch 74b\n\t" "178:\n\t" \
   "v_cmp_lt_i32 vcc, 6, v146\n\t" "s_lshr_b32 s48, s45, 1\n\t" "s_or_b32 s44, s44, vcc_lo\n\t" "s_cmp_lt_u32 s40, 4\n\t" "s_cbranch_scc1 278f\n\t" \
   "buffer_load_dword v146, v19, %[rs], 0 offen sc1\n\t" "278:\n\t" "s_bitcmp1_b32 s44, s48\n\t" "s_cbranch_scc1 78b\n\t" "s_mov_b32 s47, 0\n\t" \
   "buffer_load_dword v30, v19, %[rs], 0 offen sc1\n\t" "s_waitcnt vmcnt(0)\n\t" "v_add_u32 v30, 1, v30\n\t" "v_cmp_lt_u32 vcc, 7, v30\n\t" \
   "s_or_b32 s44, s44, vcc_lo\n\t" "s_bitcmp1_b32 s44, s48\n\t" "s_cbranch_scc1 78b\n\t" "s_bitset1_b32 s44, s48\n\t" "s_lshl_b32 s48, s48, 7\n\t" \
   "s_add_u32 s48, s48, %[cnt]\n\t" "79:\n\t" "s_sleep 10\n\t" "buffer_load_dword v30, off, %[rs], s48 sc1\n\t" "s_add_u32 s47, s47, 1\n\t" \
   "s_waitcnt vmcnt(0)\n\t" "v_add_u32 v30, 1, v30\n\t" "v_cmp_lt_u32 vcc, 7, v30\n\t" "s_cmp_gt_u32 s47, 0x20000\n\t" "s_cbranch_scc1 78b\n\t" \
   "s_cbranch_vccz 79b\n\t" "s_branch 78b\n\t" "150:\n\t" "ds_read_b128 v[140:143], v12\n\t" "s_waitcnt lgkmcnt(0)\n\t" \
   "v_min3_u32 v140, v140, v141, v142\n\t" "v_min_u32 v140, v140, v143\n\t" "v_cmp_ge_u32 vcc, v140, v138\n\t" "s_cbranch_vccz 150b\n\t" \
   "s_branch 50b\n\t" "152:\n\t" "ds_read_b128 v[140:143], v12\n\t" "s_waitcnt lgkmcnt(0)\n\t" "v_min3_u32 v140, v140, v141, v142\n\t" \
   "v_min_u32 v140, v140, v143\n\t" "v_cmp_ge_u32 vcc, v140, v138\n\t" "s_cbranch_vccz 152b\n\t" "s_branch 52b\n\t" "151:\n\t" \
   "ds_read_b128 v[140:143], v12\n\t" "s_waitcnt lgkmcnt(0)\n\t" "v_min3_u32 v140, v140, v141, v142\n\t" "v_min_u32 v140, v140, v143\n\t" \
   "v_cmp_ge_u32 vcc, v140, v138\n\t" "s_cbranch_vccz 151b\n\t" "s_branch 51b\n\t" "153:\n\t" "ds_read_b128 v[140:143], v12\n\t" \
   "s_waitcnt lgkmcnt(0)\n\t" "v_min3_u32 v140, v140, v141, v142\n\t" "v_min_u32 v140, v140, v143\n\t" "v_cmp_ge_u32 vcc, v140, v138\n\t" \
   "s_cbranch_vccz 153b\n\t" "s_branch 53b\n\t" "9:\n\t" "s_waitcnt vmcnt(0)"
#define KQ_CLOB "v8", "v9", "v10", "v11", "v12", "v13", "v14", "v15", "v32", "v33", "v34", "v35", "v36", "v37", "v38", "v39", "v40", "v41", "v42", "v43", "v44", "v45", "v46", "v47", "v48", "v49", "v50", "v51", "v52", "v53", "v54", "v55", "v56", "v57", "v58", "v59", "v60", "v61", "v62", "v63", "v64", "v65", "v66", "v67", "v68", "v69", "v70", "v71", "v72", "v73", "v74", "v75", "v76", "v77", "v78", "v79", "v80", "v81", "v82", "v83", "v84", "v85", "v86", "v87", "v88", "v89", "v90", "v91", "v92", "v93", "v94", "v95", "v96", "v97", "v98", "v99", "v100", "v101", "v102", "v103", "v104", "v105", "v106", "v107", "v108", "v109", "v110", "v111", "v112", "v113", "v114", "v115", "v116", "v117", "v118", "v119", "v120", "v121", "v122", "v123", "v124", "v125", "v126", "v127", "v128", "v129", "v130", "v131", "v132", "v133", "v134", "v135", "v136", "v137", "v138", "v139", "v140", "v141", "v142", "v143", "v144", "v145", "v146", "v147", "v148", "v149", "v150", "v151", "v160", "v161", "v162", "v163", "v164", "v165", "v166", "v167", "v168", "v169", "v170", "v171", "v172", "v173", "v174", "v175", "v176", "v177", "v178", "v179", "v180", "v181", "v182", "v183", "v184", "v185", "v186", "v187", "v188", "v189", "v190", "v191", "s40", "s41", "s42", "s43", "s44", "s45", "s46", "s47", "s48", "s49", "s50", "s51", "s52", "s53", "s54", "s55", "s60", "s61"
#define VU_ASM \
   ".p2align 6\n\t" "s_nop 0\n\t" "s_nop 0\n\t" "s_nop 0\n\t" "s_nop 0\n\t" "s_nop 0\n\t" "s_nop 0\n\t" "s_nop 0\n\t" "s_mov_b32 s44, %[rdy]\n\t" \
   "s_mov_b32 s45, 0\n\t" "s_mov_b32 s46, 0\n\t" "s_cmp_eq_u32 s44, -1\n\t" "s_cbranch_scc1 68f\n\t" "s_mul_i32 s52, s45, 1536\n\t" \
   "s_mul_i32 s51, s45, 40960\n\t" "s_add_u32 m0, s46, %[ldx]\n\t" "s_mov_b32 s53, s46\n\t" \
   "buffer_load_dwordx4 v16, %[rs], s51 offen offset:0 sc1 lds\n\t" "buffer_load_dwordx4 v16, %[rs], s51 offen offset:1024 sc1 lds\n\t" \
   "buffer_load_dwordx4 v16, %[rs], s51 offen offset:2048 sc1 lds\n\t" "buffer_load_dwordx4 v16, %[rs], s51 offen offset:3072 sc1 lds\n\t" \
   "s_add_u32 m0, m0, 4096\n\t" "s_add_u32 s51, s51, 4096\n\t" "buffer_load_dwordx4 v16, %[rs], s51 offen offset:0 sc1 lds\n\t" \
   "buffer_load_dwordx4 v16, %[rs], s51 offen offset:1024 sc1 lds\n\t" "buffer_load_dwordx4 v16, %[rs], s51 offen offset:2048 sc1 lds\n\t" \
   "buffer_load_dwordx4 v16, %[rs], s51 offen offset:3072 sc1 lds\n\t" "s_add_u32 m0, m0, 4096\n\t" "s_add_u32 s51, s51, 4096\n\t" \
   "buffer_load_dwordx4 v16, %[rs], s51 offen offset:0 sc1 lds\n\t" "buffer_load_dwordx4 v16, %[rs], s51 offen offset:1024 sc1 lds\n\t" \
   "s_add_u32 m0, s53, %[ld1]\n\t" "s_nop 0\n\t" "buffer_load_dwordx4 v17, %[rs], s52 offen sc1 lds\n\t" "s_mov_b32 s45, 1\n\t" \
   "s_mov_b32 s46, 44032\n\t" "s_mul_i32 s52, s45, 1536\n\t" "s_mul_i32 s51, s45, 40960\n\t" "s_add_u32 m0, s46, %[ldx]\n\t" "s_mov_b32 s53, s46\n\t" \
   "buffer_load_dwordx4 v16, %[rs], s51 offen offset:0 sc1 lds\n\t" "buffer_load_dwordx4 v16, %[rs], s51 offen offset:1024 sc1 lds\n\t" \
   "buffer_load_dwordx4 v16, %[rs], s51 offen offset:2048 sc1 lds\n\t" "buffer_load_dwordx4 v16, %[rs], s51 offen offset:3072 sc1 lds\n\t" \
   "s_add_u32 m0, m0, 4096\n\t" "s_add_u32 s51, s51, 4096\n\t" "buffer_load_dwordx4 v16, %[rs], s51 offen offset:0 sc1 lds\n\t" \
   "buffer_load_dwordx4 v16, %[rs], s51 offen offset:1024 sc1 lds\n\t" "buffer_load_dwordx4 v16, %[rs], s51 offen offset:2048 sc1 lds\n\t" \
   "buffer_load_dwordx4 v16, %[rs], s51 offen offset:3072 sc1 lds\n\t" "s_add_u32 m0, m0, 4096\n\t" "s_add_u32 s51, s51, 4096\n\t" \
   "buffer_load_dwordx4 v16, %[rs], s51 offen offset:0 sc1 lds\n\t" "buffer_load_dwordx4 v16, %[rs], s51 offen offset:1024 sc1 lds\n\t" \
   "s_add_u32 m0, s53, %[ld1]\n\t" "s_nop 0\n\t" "buffer_load_dwordx4 v17, %[rs], s52 offen sc1 lds\n\t" "68:\n\t" "v_mov_b32 v138, 0\n\t" \
   "v_mov_b32 v13, %[flw]\n\t" "v_and_b32 v12, -16, v13\n\t" "ds_write_b32 v13, v138\n\t" "ds_read_b128 v[32:35], v26 offset:0\n\t" \
   "ds_read_b128 v[36:39], v26 offset:16\n\t" "ds_read_b128 v[40:43], v26 offset:32\n\t" "ds_read_b128 v[44:47], v26 offset:48\n\t" \
   "s_mov_b32 s40, 0\n\t" "s_mov_b32 s41, 0\n\t" "s_mov_b32 s42, 0\n\t" "s_mov_b32 s43, 44032\n\t" "v_mov_b32 v28, v22\n\t" "v_mov_b32 v29, v23\n\t" \
   "s_mov_b32 s45, 2\n\t" "s_mov_b32 s46, 88064\n\t" "s_waitcnt vmcnt(11) lgkmcnt(0)\n\t" "s_barrier\n\t" "v_mov_b32 v14, v24\n\t" \
   "v_mov_b32 v15, v25\n\t" "v_mov_b32 v25, -1\n\t" "s_cmp_eq_u32 s44, -1\n\t" "s_cbranch_scc0 172f\n\t" "72:\n\t" \
   "ds_read_b128 v[160:163], v28 offset:0\n\t" "ds_read_b128 v[164:167], v28 offset:512\n\t" "ds_read_b128 v[168:171], v28 offset:1024\n\t" \
   "ds_read_b128 v[172:175], v28 offset:1536\n\t" "ds_read_b128 v[176:179], v28 offset:2048\n\t" "ds_read_b128 v[180:183], v28 offset:2560\n\t" \
   "ds_read_b128 v[184:187], v28 offset:3072\n\t" "v_mov_b32 v18, 0x7ffffff0\n\t" "s_mov_b32 s49, 0\n\t" "5:\n\t" "v_mov_b32 v26, v28\n\t" \
   "v_mov_b32 v27, v29\n\t" "s_waitcnt lgkmcnt(5)\n\t" "v_mfma_f32_32x32x16_bf16 v[48:63], a[0:3], v[160:163], 0\n\t" \
   "ds_read_b128 v[188:191], v26 offset:3584\n\t" "s_mul_i32 s52, s45, 1536\n\t" "s_mul_i32 s51, s45, 40960\n\t" "s_add_u32 m0, s46, %[ldx]\n\t" \
   "s_mov_b32 s53, s46\n\t" "v_mfma_f32_32x32x16_bf16 v[48:63], a[4:7], v[164:167], v[48:63]\n\t" "ds_read_b128 v[160:163], v26 offset:4096\n\t" \
   "ds_read_b128 v[128:131], v27 offset:0\n\t" "ds_read_b128 v[132:135], v27 offset:16\n\t" "s_waitcnt lgkmcnt(7)\n\t" \
   "v_mfma_f32_32x32x16_bf16 v[48:63], a[8:11], v[168:171], v[48:63]\n\t" "ds_read_b128 v[164:167], v26 offset:4608\n\t" \
   "ds_read_b64 v[136:137], v27 offset:32\n\t" "v_add_u32 v28, s43, v22\n\t" "v_add_u32 v29, s43, v23\n\t" \
   "v_mfma_f32_32x32x16_bf16 v[48:63], a[12:15], v[172:175], v[48:63]\n\t" "ds_read_b128 v[168:171], v26 offset:5120\n\t" \
   "buffer_load_dwordx4 v16, %[rs], s51 offen offset:0 sc1 lds\n\t" "s_waitcnt lgkmcnt(8)\n\t" \
   "v_mfma_f32_32x32x16_bf16 v[48:63], a[16:19], v[176:179], v[48:63]\n\t" "ds_read_b128 v[172:175], v26 offset:5632\n\t" \
   "v_mfma_f32_32x32x16_bf16 v[48:63], a[20:23], v[180:183], v[48:63]\n\t" "ds_read_b128 v[176:179], v26 offset:6144\n\t" "s_waitcnt lgkmcnt(3)\n\t" \
   "v_add_f32 v128, v128, v130\n\t" "v_add_f32 v129, v129, v131\n\t" "v_add_f32 v132, v132, v134\n\t" \
   "v_mfma_f32_32x32x16_bf16 v[48:63], a[24:27], v[184:187], v[48:63]\n\t" "ds_read_b128 v[180:183], v26 offset:6656\n\t" \
   "buffer_load_dwordx4 v16, %[rs], s51 offen offset:1024 sc1 lds\n\t" "v_add_f32 v133, v133, v135\n\t" \
   "v_mfma_f32_32x32x16_bf16 v[48:63], a[28:31], v[188:191], v[48:63]\n\t" "ds_read_b128 v[184:187], v26 offset:7168\n\t" \
   "v_add_f32 v128, v128, v136\n\t" "v_add_f32 v129, v129, v137\n\t" "v_add_f32 v128, v128, v132\n\t" \
   "v_mfma_f32_32x32x16_bf16 v[48:63], a[32:35], v[160:163], v[48:63]\n\t" "ds_read_b128 v[188:191], v26 offset:7680\n\t" \
   "v_add_f32 v129, v129, v133\n\t" "v_mul_f32 v128, 0xbacccccd, v128\n\t" "v_mul_f32 v129, 0x3acccccd, v129\n\t" \
   "v_fma_f32 v129, -v128, v128, v129\n\t" "v_mfma_f32_32x32x16_bf16 v[48:63], a[36:39], v[164:167], v[48:63]\n\t" \
   "ds_read_b128 v[160:163], v26 offset:8192\n\t" "buffer_load_dwordx4 v16, %[rs], s51 offen offset:2048 sc1 lds\n\t" "s_waitcnt lgkmcnt(5)\n\t" \
   "v_mfma_f32_32x32x16_bf16 v[48:63], a[40:43], v[168:171], v[48:63]\n\t" "ds_read_b128 v[164:167], v26 offset:8704\n\t" "v_max_f32 v129, 0, v129\n\t" \
   "v_cvt_pk_bf16_f32 v129, v129, v128\n\t" "v_and_b32 v8, 0xffff0000, v129\n\t" "v_lshlrev_b32 v129, 16, v129\n\t" \
   "v_mfma_f32_32x32x16_bf16 v[48:63], a[44:47], v[172:175], v[48:63]\n\t" "ds_read_b128 v[168:171], v26 offset:9216\n\t" \
   "v_add_f32 v129, 0x3727c5ac, v129\n\t" "v_cvt_pk_bf16_f32 v129, v129, v129\n\t" "v_lshlrev_b32 v129, 16, v129\n\t" "s_waitcnt lgkmcnt(5)\n\t" \
   "v_mfma_f32_32x32x16_bf16 v[48:63], a[48:51], v[176:179], v[48:63]\n\t" "ds_read_b128 v[172:175], v26 offset:9728\n\t" "v_sqrt_f32 v129, v129\n\t" \
   "s_nop 0\n\t" "v_cvt_pk_bf16_f32 v129, v129, v129\n\t" "v_mfma_f32_32x32x16_bf16 v[48:63], a[52:55], v[180:183], v[48:63]\n\t" \
   "ds_read_b128 v[176:179], v26 offset:10240\n\t" "v_lshlrev_b32 v129, 16, v129\n\t" "v_rcp_f32 v10, v129\n\t" "s_waitcnt lgkmcnt(5)\n\t" \
   "v_mfma_f32_32x32x16_bf16 v[48:63], a[56:59], v[184:187], v[48:63]\n\t" "ds_read_b128 v[180:183], v26 offset:10752\n\t" \
   "buffer_load_dwordx4 v16, %[rs], s51 offen offset:3072 sc1 lds\n\t" "s_add_u32 m0, m0, 4096\n\t" "s_add_u32 s51, s51, 4096\n\t" \
   "v_mfma_f32_32x32x16_bf16 v[48:63], a[60:63], v[188:191], v[48:63]\n\t" "ds_read_b128 v[184:187], v26 offset:11264\n\t" \
   "buffer_load_dwordx4 v16, %[rs], s51 offen offset:0 sc1 lds\n\t" "s_waitcnt lgkmcnt(5)\n\t" \
   "v_mfma_f32_32x32x16_bf16 v[48:63], a[64:67], v[160:163], v[48:63]\n\t" "ds_read_b128 v[188:191], v26 offset:11776\n\t" \
   "v_mfma_f32_32x32x16_bf16 v[48:63], a[68:71], v[164:167], v[48:63]\n\t" "ds_read_b128 v[160:163], v26 offset:12288\n\t" "s_waitcnt lgkmcnt(5)\n\t" \
   "v_mfma_f32_32x32x16_bf16 v[48:63], a[72:75], v[168:171], v[48:63]\n\t" "ds_read_b128 v[164:167], v26 offset:12800\n\t" \
   "buffer_load_dwordx4 v16, %[rs], s51 offen offset:1024 sc1 lds\n\t" "v_mfma_f32_32x32x16_bf16 v[48:63], a[76:79], v[172:175], v[48:63]\n\t" \
   "ds_read_b128 v[168:171], v26 offset:13312\n\t" "s_waitcnt lgkmcnt(5)\n\t" "v_mfma_f32_32x32x16_bf16 v[48:63], a[80:83], v[176:179], v[48:63]\n\t" \
   "ds_read_b128 v[172:175], v26 offset:13824\n\t" "v_mfma_f32_32x32x16_bf16 v[48:63], a[84:87], v[180:183], v[48:63]\n\t" \
   "ds_read_b128 v[176:179], v26 offset:14336\n\t" "buffer_load_dwordx4 v16, %[rs], s51 offen offset:2048 sc1 lds\n\t" "s_waitcnt lgkmcnt(5)\n\t" \
   "v_mfma_f32_32x32x16_bf16 v[48:63], a[88:91], v[184:187], v[48:63]\n\t" "ds_read_b128 v[180:183], v26 offset:14848\n\t" \
   "v_mfma_f32_32x32x16_bf16 v[48:63], a[92:95], v[188:191], v[48:63]\n\t" "ds_read_b128 v[184:187], v26 offset:15360\n\t" "s_waitcnt lgkmcnt(5)\n\t" \
   "v_mfma_f32_32x32x16_bf16 v[48:63], a[96:99], v[160:163], v[48:63]\n\t" "ds_read_b128 v[188:191], v26 offset:15872\n\t" \
   "v_mfma_f32_32x32x16_bf16 v[48:63], a[100:103], v[164:167], v[48:63]\n\t" "ds_read_b128 v[160:163], v26 offset:16384\n\t" "s_waitcnt lgkmcnt(5)\n\t" \
   "v_mfma_f32_32x32x16_bf16 v[48:63], a[104:107], v[168:171], v[48:63]\n\t" "ds_read_b128 v[164:167], v26 offset:16896\n\t" \
   "buffer_load_dwordx4 v16, %[rs], s51 offen offset:3072 sc1 lds\n\t" "s_add_u32 m0, m0, 4096\n\t" "s_add_u32 s51, s51, 4096\n\t" \
   "v_mfma_f32_32x32x16_bf16 v[48:63], a[108:111], v[172:175], v[48:63]\n\t" "ds_read_b128 v[168:171], v26 offset:17408\n\t" \
   "buffer_load_dwordx4 v16, %[rs], s51 offen offset:0 sc1 lds\n\t" "s_waitcnt lgkmcnt(5)\n\t" \
   "v_mfma_f32_32x32x16_bf16 v[48:63], a[112:115], v[176:179], v[48:63]\n\t" "ds_read_b128 v[172:175], v26 offset:17920\n\t" "s_cmp_lt_i32 s40, 1\n\t" \
   "s_cselect_b32 s47, 0x7ffffff0, 0\n\t" "v_or_b32 v30, s47, v20\n\t" "s_sub_i32 s47, s41, 1\n\t" \
   "v_mfma_f32_32x32x16_bf16 v[48:63], a[116:119], v[180:183], v[48:63]\n\t" "ds_read_b128 v[176:179], v26 offset:18432\n\t" \
   "s_max_i32 s47, s47, 0\n\t" "s_lshl_b32 s47, s47, 12\n\t" "s_waitcnt vmcnt(9)\n\t" "s_waitcnt lgkmcnt(5)\n\t" \
   "v_mfma_f32_32x32x16_bf16 v[48:63], a[120:123], v[184:187], v[48:63]\n\t" "ds_read_b128 v[180:183], v26 offset:18944\n\t" \
   "buffer_store_dword v31, v30, %[rs], s47 offen sc1\n\t" "v_mfma_f32_32x32x16_bf16 v[48:63], a[124:127], v[188:191], v[48:63]\n\t" \
   "ds_read_b128 v[184:187], v26 offset:19456\n\t" "s_waitcnt lgkmcnt(5)\n\t" "v_mfma_f32_32x32x16_bf16 v[48:63], a[128:131], v[160:163], v[48:63]\n\t" \
   "ds_read_b128 v[188:191], v26 offset:19968\n\t" "buffer_load_dwordx4 v16, %[rs], s51 offen offset:1024 sc1 lds\n\t" "s_add_u32 m0, s53, %[ld1]\n\t" \
   "s_nop 0\n\t" "v_mfma_f32_32x32x16_bf16 v[48:63], a[132:135], v[164:167], v[48:63]\n\t" "buffer_load_dwordx4 v17, %[rs], s52 offen sc1 lds\n\t" \
   "s_waitcnt lgkmcnt(4)\n\t" "v_mfma_f32_32x32x16_bf16 v[48:63], a[136:139], v[168:171], v[48:63]\n\t" \
   "v_mfma_f32_32x32x16_bf16 v[48:63], a[140:143], v[172:175], v[48:63]\n\t" "s_waitcnt lgkmcnt(2)\n\t" \
   "v_mfma_f32_32x32x16_bf16 v[48:63], a[144:147], v[176:179], v[48:63]\n\t" "v_mfma_f32_32x32x16_bf16 v[48:63], a[148:151], v[180:183], v[48:63]\n\t" \
   "s_waitcnt lgkmcnt(0)\n\t" "v_mfma_f32_32x32x16_bf16 v[48:63], a[152:155], v[184:187], v[48:63]\n\t" \
   "v_mfma_f32_32x32x16_bf16 v[48:63], a[156:159], v[188:191], v[48:63]\n\t" "s_memrealtime s[54:55]\n\t" "s_nop 7\n\t" "s_nop 2\n\t" \
   "v_fmac_f32 v48, v32, v8\n\t" "v_fmac_f32 v49, v33, v8\n\t" "v_fmac_f32 v50, v34, v8\n\t" "v_fmac_f32 v51, v35, v8\n\t" \
   "v_fmac_f32 v52, v36, v8\n\t" "v_fmac_f32 v53, v37, v8\n\t" "v_fmac_f32 v54, v38, v8\n\t" "v_fmac_f32 v55, v39, v8\n\t" \
   "v_fmac_f32 v56, v40, v8\n\t" "v_fmac_f32 v57, v41, v8\n\t" "v_fmac_f32 v58, v42, v8\n\t" "v_fmac_f32 v59, v43, v8\n\t" \
   "v_fmac_f32 v60, v44, v8\n\t" "v_fmac_f32 v61, v45, v8\n\t" "v_fmac_f32 v62, v46, v8\n\t" "v_fmac_f32 v63, v47, v8\n\t" \
   "v_mul_f32 v48, v48, v10\n\t" "v_mul_f32 v49, v49, v10\n\t" "v_mul_f32 v50, v50, v10\n\t" "v_mul_f32 v51, v51, v10\n\t" \
   "v_mul_f32 v52, v52, v10\n\t" "v_mul_f32 v53, v53, v10\n\t" "v_mul_f32 v54, v54, v10\n\t" "v_mul_f32 v55, v55, v10\n\t" \
   "v_mul_f32 v56, v56, v10\n\t" "v_mul_f32 v57, v57, v10\n\t" "v_mul_f32 v58, v58, v10\n\t" "v_mul_f32 v59, v59, v10\n\t" \
   "v_mul_f32 v60, v60, v10\n\t" "v_mul_f32 v61, v61, v10\n\t" "v_mul_f32 v62, v62, v10\n\t" "v_mul_f32 v63, v63, v10\n\t" \
   "v_cvt_pk_bf16_f32 v48, v48, v49\n\t" "v_cvt_pk_bf16_f32 v49, v50, v51\n\t" "v_cvt_pk_bf16_f32 v50, v52, v53\n\t" \
   "v_cvt_pk_bf16_f32 v51, v54, v55\n\t" "ds_write_b128 v14, v[48:51] offset:0\n\t" "v_cvt_pk_bf16_f32 v56, v56, v57\n\t" \
   "v_cvt_pk_bf16_f32 v57, v58, v59\n\t" "v_cvt_pk_bf16_f32 v58, v60, v61\n\t" "v_cvt_pk_bf16_f32 v59, v62, v63\n\t" \
   "ds_write_b128 v14, v[56:59] offset:1024\n\t" "s_waitcnt vmcnt(12) lgkmcnt(0)\n\t" "s_barrier\n\t" "s_mov_b32 s42, s43\n\t" \
   "s_add_u32 s43, s43, 44032\n\t" "s_cmp_eq_u32 s43, 132096\n\t" "s_cselect_b32 s43, 0, s43\n\t" "s_add_u32 s41, s41, 1\n\t" \
   "s_add_u32 s40, s40, 1\n\t" "v_xor_b32 v14, 0x2000, v14\n\t" "v_xor_b32 v15, 0x2000, v15\n\t" "s_cmp_eq_u32 s40, 1\n\t" "s_cbranch_scc0 6f\n\t" \
   "ds_read_b128 v[96:99], v15 offset:0\n\t" "ds_read_b128 v[100:103], v15 offset:1024\n\t" "ds_read_b128 v[104:107], v15 offset:2048\n\t" \
   "ds_read_b128 v[108:111], v15 offset:3072\n\t" "ds_read_b128 v[112:115], v15 offset:4096\n\t" "ds_read_b128 v[116:119], v15 offset:5120\n\t" \
   "ds_read_b128 v[120:123], v15 offset:6144\n\t" "ds_read_b128 v[124:127], v15 offset:7168\n\t" "s_sub_i32 s49, s41, 1\n\t" \
   "s_mul_i32 s49, s49, 1310720\n\t" "v_mov_b32 v31, s54\n\t" "v_min_u32 v31, -2, v31\n\t" "s_mov_b32 s45, 3\n\t" "s_mov_b32 s46, 0\n\t" \
   "s_waitcnt lgkmcnt(6)\n\t" "v_mfma_f32_32x32x16_bf16 v[64:79], v[96:99], a[160:163], 0\n\t" \
   "v_mfma_f32_32x32x16_bf16 v[64:79], v[100:103], a[176:179], v[64:79]\n\t" "s_waitcnt lgkmcnt(4)\n\t" \
   "v_mfma_f32_32x32x16_bf16 v[64:79], v[104:107], a[164:167], v[64:79]\n\t" "v_mfma_f32_32x32x16_bf16 v[64:79], v[108:111], a[180:183], v[64:79]\n\t" \
   "s_waitcnt lgkmcnt(2)\n\t" "v_mfma_f32_32x32x16_bf16 v[64:79], v[112:115], a[168:171], v[64:79]\n\t" \
   "v_mfma_f32_32x32x16_bf16 v[64:79], v[116:119], a[184:187], v[64:79]\n\t" "s_waitcnt lgkmcnt(0)\n\t" \
   "v_mfma_f32_32x32x16_bf16 v[64:79], v[120:123], a[172:175], v[64:79]\n\t" "v_mfma_f32_32x32x16_bf16 v[64:79], v[124:127], a[188:191], v[64:79]\n\t" \
   "v_mfma_f32_32x32x16_bf16 v[80:95], v[96:99], a[192:195], 0\n\t" "v_mfma_f32_32x32x16_bf16 v[80:95], v[100:103], a[208:211], v[80:95]\n\t" \
   "s_add_u32 s50, s49, 0\n\t" "v_cvt_pk_bf16_f32 v64, v64, v65\n\t" "v_cvt_pk_bf16_f32 v65, v72, v73\n\t" "v_cvt_pk_bf16_f32 v66, v66, v67\n\t" \
   "v_cvt_pk_bf16_f32 v67, v74, v75\n\t" "v_mfma_f32_32x32x16_bf16 v[80:95], v[104:107], a[196:199], v[80:95]\n\t" \
   "buffer_store_dwordx4 v[64:67], v21, %[rs], s50 offen offset:0 sc1\n\t" "v_cvt_pk_bf16_f32 v68, v68, v69\n\t" \
   "v_mfma_f32_32x32x16_bf16 v[80:95], v[108:111], a[212:215], v[80:95]\n\t" "v_cvt_pk_bf16_f32 v69, v76, v77\n\t" \
   "v_cvt_pk_bf16_f32 v70, v70, v71\n\t" "v_cvt_pk_bf16_f32 v71, v78, v79\n\t" \
   "v_mfma_f32_32x32x16_bf16 v[80:95], v[112:115], a[200:203], v[80:95]\n\t" "buffer_store_dwordx4 v[68:71], v21, %[rs], s50 offen offset:512 sc1\n\t" \
   "v_mfma_f32_32x32x16_bf16 v[80:95], v[116:119], a[216:219], v[80:95]\n\t" "v_mfma_f32_32x32x16_bf16 v[80:95], v[120:123], a[204:207], v[80:95]\n\t" \
   "v_mfma_f32_32x32x16_bf16 v[80:95], v[124:127], a[220:223], v[80:95]\n\t" "v_mfma_f32_32x32x16_bf16 v[64:79], v[96:99], a[224:227], 0\n\t" \
   "v_mfma_f32_32x32x16_bf16 v[64:79], v[100:103], a[240:243], v[64:79]\n\t" "s_add_u32 s50, s49, 65536\n\t" "v_cvt_pk_bf16_f32 v80, v80, v81\n\t" \
   "v_cvt_pk_bf16_f32 v81, v88, v89\n\t" "v_cvt_pk_bf16_f32 v82, v82, v83\n\t" "v_cvt_pk_bf16_f32 v83, v90, v91\n\t" \
   "v_mfma_f32_32x32x16_bf16 v[64:79], v[104:107], a[228:231], v[64:79]\n\t" "buffer_store_dwordx4 v[80:83], v21, %[rs], s50 offen offset:0 sc1\n\t" \
   "v_cvt_pk_bf16_f32 v84, v84, v85\n\t" "v_mfma_f32_32x32x16_bf16 v[64:79], v[108:111], a[244:247], v[64:79]\n\t" \
   "v_cvt_pk_bf16_f32 v85, v92, v93\n\t" "v_cvt_pk_bf16_f32 v86, v86, v87\n\t" "v_cvt_pk_bf16_f32 v87, v94, v95\n\t" \
   "v_mfma_f32_32x32x16_bf16 v[64:79], v[112:115], a[232:235], v[64:79]\n\t" "buffer_store_dwordx4 v[84:87], v21, %[rs], s50 offen offset:512 sc1\n\t" \
   "v_mfma_f32_32x32x16_bf16 v[64:79], v[116:119], a[248:251], v[64:79]\n\t" "ds_read_b128 v[160:163], v28 offset:0\n\t" \
   "ds_read_b128 v[164:167], v28 offset:512\n\t" "ds_read_b128 v[168:171], v28 offset:1024\n\t" "ds_read_b128 v[172:175], v28 offset:1536\n\t" \
   "v_mfma_f32_32x32x16_bf16 v[64:79], v[120:123], a[236:239], v[64:79]\n\t" "ds_read_b128 v[176:179], v28 offset:2048\n\t" \
   "ds_read_b128 v[180:183], v28 offset:2560\n\t" "ds_read_b128 v[184:187], v28 offset:3072\n\t" \
   "v_mfma_f32_32x32x16_bf16 v[64:79], v[124:127], a[252:255], v[64:79]\n\t" "v_mfma_f32_32x32x16_bf16 v[80:95], v[96:99], v[192:195], 0\n\t" \
   "v_mfma_f32_32x32x16_bf16 v[80:95], v[100:103], v[208:211], v[80:95]\n\t" "s_add_u32 s50, s49, 131072\n\t" "v_cvt_pk_bf16_f32 v64, v64, v65\n\t" \
   "v_cvt_pk_bf16_f32 v65, v72, v73\n\t" "v_cvt_pk_bf16_f32 v66, v66, v67\n\t" "v_cvt_pk_bf16_f32 v67, v74, v75\n\t" \
   "v_mfma_f32_32x32x16_bf16 v[80:95], v[104:107], v[196:199], v[80:95]\n\t" "buffer_store_dwordx4 v[64:67], v21, %[rs], s50 offen offset:0 sc1\n\t" \
   "v_cvt_pk_bf16_f32 v68, v68, v69\n\t" "v_mfma_f32_32x32x16_bf16 v[80:95], v[108:111], v[212:215], v[80:95]\n\t" \
   "v_cvt_pk_bf16_f32 v69, v76, v77\n\t" "v_cvt_pk_bf16_f32 v70, v70, v71\n\t" "v_cvt_pk_bf16_f32 v71, v78, v79\n\t" \
   "v_mfma_f32_32x32x16_bf16 v[80:95], v[112:115], v[200:203], v[80:95]\n\t" "buffer_store_dwordx4 v[68:71], v21, %[rs], s50 offen offset:512 sc1\n\t" \
   "v_mfma_f32_32x32x16_bf16 v[80:95], v[116:119], v[216:219], v[80:95]\n\t" "v_mfma_f32_32x32x16_bf16 v[80:95], v[120:123], v[204:207], v[80:95]\n\t" \
   "v_mfma_f32_32x32x16_bf16 v[80:95], v[124:127], v[220:223], v[80:95]\n\t" "v_mfma_f32_32x32x16_bf16 v[140:155], v[96:99], v[224:227], 0\n\t" \
   "v_mfma_f32_32x32x16_bf16 v[140:155], v[100:103], v[240:243], v[140:155]\n\t" "s_add_u32 s50, s49, 196608\n\t" "v_cvt_pk_bf16_f32 v80, v80, v81\n\t" \
   "v_cvt_pk_bf16_f32 v81, v88, v89\n\t" "v_cvt_pk_bf16_f32 v82, v82, v83\n\t" "v_cvt_pk_bf16_f32 v83, v90, v91\n\t" \
   "v_mfma_f32_32x32x16_bf16 v[140:155], v[104:107], v[228:231], v[140:155]\n\t" \
   "buffer_store_dwordx4 v[80:83], v21, %[rs], s50 offen offset:0 sc1\n\t" "v_cvt_pk_bf16_f32 v84, v84, v85\n\t" \
   "v_mfma_f32_32x32x16_bf16 v[140:155], v[108:111], v[244:247], v[140:155]\n\t" "v_cvt_pk_bf16_f32 v85, v92, v93\n\t" \
   "v_cvt_pk_bf16_f32 v86, v86, v87\n\t" "v_cvt_pk_bf16_f32 v87, v94, v95\n\t" \
   "v_mfma_f32_32x32x16_bf16 v[140:155], v[112:115], v[232:235], v[140:155]\n\t" \
   "buffer_store_dwordx4 v[84:87], v21, %[rs], s50 offen offset:512 sc1\n\t" \
   "v_mfma_f32_32x32x16_bf16 v[140:155], v[116:119], v[248:251], v[140:155]\n\t" \
   "v_mfma_f32_32x32x16_bf16 v[140:155], v[120:123], v[236:239], v[140:155]\n\t" \
   "v_mfma_f32_32x32x16_bf16 v[140:155], v[124:127], v[252:255], v[140:155]\n\t" "s_nop 7\n\t" "s_nop 2\n\t" "s_add_u32 s50, s49, 262144\n\t" \
   "v_cvt_pk_bf16_f32 v140, v140, v141\n\t" "v_cvt_pk_bf16_f32 v141, v148, v149\n\t" "v_cvt_pk_bf16_f32 v142, v142, v143\n\t" \
   "v_cvt_pk_bf16_f32 v143, v150, v151\n\t" "buffer_store_dwordx4 v[140:143], v21, %[rs], s50 offen offset:0 sc1\n\t" \
   "v_cvt_pk_bf16_f32 v144, v144, v145\n\t" "v_cvt_pk_bf16_f32 v145, v152, v153\n\t" "v_cvt_pk_bf16_f32 v146, v146, v147\n\t" \
   "v_cvt_pk_bf16_f32 v147, v154, v155\n\t" "buffer_store_dwordx4 v[144:147], v21, %[rs], s50 offen offset:512 sc1\n\t" "s_branch 5b\n\t" "6:\n\t" \
   "ds_read_b128 v[160:163], v28 offset:0\n\t" "ds_read_b128 v[164:167], v28 offset:512\n\t" "ds_read_b128 v[168:171], v28 offset:1024\n\t" \
   "ds_read_b128 v[172:175], v28 offset:1536\n\t" "ds_read_b128 v[176:179], v28 offset:2048\n\t" "ds_read_b128 v[180:183], v28 offset:2560\n\t" \
   "ds_read_b128 v[184:187], v28 offset:3072\n\t" "s_mov_b32 s45, 4\n\t" "s_mov_b32 s46, 44032\n\t" "s_cmp_eq_u32 s44, -1\n\t" \
   "s_cbranch_scc0 176f\n\t" "76:\n\t" "1:\n\t" "v_mov_b32 v26, v28\n\t" "v_mov_b32 v27, v29\n\t" "s_waitcnt lgkmcnt(5)\n\t" \
   "v_mfma_f32_32x32x16_bf16 v[48:63], a[0:3], v[160:163], 0\n\t" "ds_read_b128 v[188:191], v26 offset:3584\n\t" "ds_read_b128 v[156:159], v12\n\t" \
   "v_mfma_f32_32x32x16_bf16 v[48:63], a[4:7], v[164:167], v[48:63]\n\t" "ds_read_b128 v[160:163], v26 offset:4096\n\t" \
   "s_add_u32 s50, s49, 262144\n\t" "v_cvt_pk_bf16_f32 v140, v140, v141\n\t" "v_cvt_pk_bf16_f32 v141, v148, v149\n\t" "s_waitcnt lgkmcnt(6)\n\t" \
   "v_mfma_f32_32x32x16_bf16 v[48:63], a[8:11], v[168:171], v[48:63]\n\t" "ds_read_b128 v[164:167], v26 offset:4608\n\t" "s_waitcnt lgkmcnt(2)\n\t" \
   "v_min3_u32 v156, v156, v157, v158\n\t" "v_min_u32 v156, v156, v159\n\t" "v_cmp_ge_u32 vcc, v156, v138\n\t" \
   "v_mfma_f32_32x32x16_bf16 v[48:63], a[12:15], v[172:175], v[48:63]\n\t" "ds_read_b128 v[168:171], v26 offset:5120\n\t" "s_cbranch_vccz 150f\n\t" \
   "50:\n\t" "ds_read_b128 v[96:99], v15 offset:0\n\t" "v_mfma_f32_32x32x16_bf16 v[48:63], a[16:19], v[176:179], v[48:63]\n\t" \
   "ds_read_b128 v[172:175], v26 offset:5632\n\t" "ds_read_b128 v[100:103], v15 offset:1024\n\t" "ds_read_b128 v[104:107], v15 offset:2048\n\t" \
   "ds_read_b128 v[108:111], v15 offset:3072\n\t" "v_mfma_f32_32x32x16_bf16 v[48:63], a[20:23], v[180:183], v[48:63]\n\t" \
   "ds_read_b128 v[176:179], v26 offset:6144\n\t" "ds_read_b128 v[112:115], v15 offset:4096\n\t" "ds_read_b128 v[116:119], v15 offset:5120\n\t" \
   "v_mfma_f32_32x32x16_bf16 v[48:63], a[24:27], v[184:187], v[48:63]\n\t" "ds_read_b128 v[180:183], v26 offset:6656\n\t" \
   "ds_read_b128 v[120:123], v15 offset:6144\n\t" "ds_read_b128 v[124:127], v15 offset:7168\n\t" "s_mul_i32 s52, s45, 1536\n\t" \
   "s_mul_i32 s51, s45, 40960\n\t" "s_add_u32 m0, s46, %[ldx]\n\t" "s_mov_b32 s53, s46\n\t" \
   "v_mfma_f32_32x32x16_bf16 v[48:63], a[28:31], v[188:191], v[48:63]\n\t" "ds_read_b128 v[184:187], v26 offset:7168\n\t" \
   "v_cvt_pk_bf16_f32 v142, v142, v143\n\t" "v_cvt_pk_bf16_f32 v143, v150, v151\n\t" "s_waitcnt lgkmcnt(9)\n\t" \
   "v_mfma_f32_32x32x16_bf16 v[64:79], v[96:99], a[160:163], 0\n\t" "buffer_store_dwordx4 v[140:143], v18, %[rs], s50 offen offset:0 sc1\n\t" \
   "v_mfma_f32_32x32x16_bf16 v[48:63], a[32:35], v[160:163], v[48:63]\n\t" "ds_read_b128 v[188:191], v26 offset:7680\n\t" \
   "v_cvt_pk_bf16_f32 v144, v144, v145\n\t" "v_cvt_pk_bf16_f32 v145, v152, v153\n\t" "v_cvt_pk_bf16_f32 v146, v146, v147\n\t" \
   "v_cvt_pk_bf16_f32 v147, v154, v155\n\t" "v_mfma_f32_32x32x16_bf16 v[48:63], a[36:39], v[164:167], v[48:63]\n\t" \
   "ds_read_b128 v[160:163], v26 offset:8192\n\t" "v_add_u32 v28, s43, v22\n\t" "v_add_u32 v29, s43, v23\n\t" \
   "v_mfma_f32_32x32x16_bf16 v[64:79], v[100:103], a[176:179], v[64:79]\n\t" \
   "buffer_store_dwordx4 v[144:147], v18, %[rs], s50 offen offset:512 sc1\n\t" "s_sub_i32 s49, s41, 1\n\t" "s_mul_i32 s49, s49, 1310720\n\t" \
   "v_mfma_f32_32x32x16_bf16 v[48:63], a[40:43], v[168:171], v[48:63]\n\t" "ds_read_b128 v[164:167], v26 offset:8704\n\t" \
   "ds_read_b128 v[128:131], v27 offset:0\n\t" "buffer_load_dwordx4 v16, %[rs], s51 offen offset:0 sc1 lds\n\t" \
   "v_mfma_f32_32x32x16_bf16 v[48:63], a[44:47], v[172:175], v[48:63]\n\t" "ds_read_b128 v[168:171], v26 offset:9216\n\t" \
   "ds_read_b128 v[132:135], v27 offset:16\n\t" "ds_read_b64 v[136:137], v27 offset:32\n\t" "s_waitcnt lgkmcnt(14)\n\t" \
   "v_mfma_f32_32x32x16_bf16 v[64:79], v[104:107], a[164:167], v[64:79]\n\t" "s_waitcnt lgkmcnt(10)\n\t" \
   "v_mfma_f32_32x32x16_bf16 v[48:63], a[48:51], v[176:179], v[48:63]\n\t" "ds_read_b128 v[172:175], v26 offset:9728\n\t" \
   "v_mfma_f32_32x32x16_bf16 v[48:63], a[52:55], v[180:183], v[48:63]\n\t" "ds_read_b128 v[176:179], v26 offset:10240\n\t" \
   "buffer_load_dwordx4 v16, %[rs], s51 offen offset:1024 sc1 lds\n\t" "v_mfma_f32_32x32x16_bf16 v[64:79], v[108:111], a[180:183], v[64:79]\n\t" \
   "s_waitcnt lgkmcnt(2)\n\t" "v_add_f32 v128, v128, v130\n\t" "v_add_f32 v129, v129, v131\n\t" "v_add_f32 v132, v132, v134\n\t" \
   "v_add_f32 v133, v133, v135\n\t" "v_mfma_f32_32x32x16_bf16 v[48:63], a[56:59], v[184:187], v[48:63]\n\t" \
   "ds_read_b128 v[180:183], v26 offset:10752\n\t" "v_add_f32 v128, v128, v136\n\t" "v_add_f32 v129, v129, v137\n\t" "v_add_f32 v128, v128, v132\n\t" \
   "v_add_f32 v129, v129, v133\n\t" "v_mfma_f32_32x32x16_bf16 v[48:63], a[60:63], v[188:191], v[48:63]\n\t" \
   "ds_read_b128 v[184:187], v26 offset:11264\n\t" "v_mul_f32 v128, 0xbacccccd, v128\n\t" "v_mul_f32 v129, 0x3acccccd, v129\n\t" \
   "v_fma_f32 v129, -v128, v128, v129\n\t" "v_max_f32 v129, 0, v129\n\t" "v_mfma_f32_32x32x16_bf16 v[64:79], v[112:115], a[168:171], v[64:79]\n\t" \
   "buffer_load_dwordx4 v16, %[rs], s51 offen offset:2048 sc1 lds\n\t" "v_cvt_pk_bf16_f32 v129, v129, v128\n\t" "v_and_b32 v8, 0xffff0000, v129\n\t" \
   "v_mfma_f32_32x32x16_bf16 v[48:63], a[64:67], v[160:163], v[48:63]\n\t" "ds_read_b128 v[188:191], v26 offset:11776\n\t" \
   "v_lshlrev_b32 v129, 16, v129\n\t" "v_add_f32 v129, 0x3727c5ac, v129\n\t" "v_cvt_pk_bf16_f32 v129, v129, v129\n\t" \
   "v_lshlrev_b32 v129, 16, v129\n\t" "v_mfma_f32_32x32x16_bf16 v[48:63], a[68:71], v[164:167], v[48:63]\n\t" \
   "ds_read_b128 v[160:163], v26 offset:12288\n\t" "v_sqrt_f32 v129, v129\n\t" "s_nop 0\n\t" "v_cvt_pk_bf16_f32 v129, v129, v129\n\t" \
   "v_mfma_f32_32x32x16_bf16 v[64:79], v[116:119], a[184:187], v[64:79]\n\t" "v_lshlrev_b32 v129, 16, v129\n\t" "v_rcp_f32 v10, v129\n\t" \
   "s_waitcnt lgkmcnt(5)\n\t" "v_mfma_f32_32x32x16_bf16 v[48:63], a[72:75], v[168:171], v[48:63]\n\t" "ds_read_b128 v[164:167], v26 offset:12800\n\t" \
   "v_mfma_f32_32x32x16_bf16 v[48:63], a[76:79], v[172:175], v[48:63]\n\t" "ds_read_b128 v[168:171], v26 offset:13312\n\t" \
   "v_mfma_f32_32x32x16_bf16 v[64:79], v[120:123], a[172:175], v[64:79]\n\t" "buffer_load_dwordx4 v16, %[rs], s51 offen offset:3072 sc1 lds\n\t" \
   "s_add_u32 m0, m0, 4096\n\t" "s_add_u32 s51, s51, 4096\n\t" "s_waitcnt lgkmcnt(5)\n\t" \
   "v_mfma_f32_32x32x16_bf16 v[48:63], a[80:83], v[176:179], v[48:63]\n\t" "ds_read_b128 v[172:175], v26 offset:13824\n\t" \
   "v_mfma_f32_32x32x16_bf16 v[48:63], a[84:87], v[180:183], v[48:63]\n\t" "ds_read_b128 v[176:179], v26 offset:14336\n\t" \
   "buffer_load_dwordx4 v16, %[rs], s51 offen offset:0 sc1 lds\n\t" "v_mfma_f32_32x32x16_bf16 v[64:79], v[124:127], a[188:191], v[64:79]\n\t" \
   "s_waitcnt lgkmcnt(5)\n\t" "v_mfma_f32_32x32x16_bf16 v[48:63], a[88:91], v[184:187], v[48:63]\n\t" "ds_read_b128 v[180:183], v26 offset:14848\n\t" \
   "v_mfma_f32_32x32x16_bf16 v[48:63], a[92:95], v[188:191], v[48:63]\n\t" "ds_read_b128 v[184:187], v26 offset:15360\n\t" "s_add_u32 s50, s49, 0\n\t" \
   "v_cvt_pk_bf16_f32 v64, v64, v65\n\t" "v_cvt_pk_bf16_f32 v65, v72, v73\n\t" "v_cvt_pk_bf16_f32 v66, v66, v67\n\t" \
   "v_mfma_f32_32x32x16_bf16 v[80:95], v[96:99], a[192:195], 0\n\t" "v_cvt_pk_bf16_f32 v67, v74, v75\n\t" \
   "buffer_load_dwordx4 v16, %[rs], s51 offen offset:1024 sc1 lds\n\t" "s_waitcnt lgkmcnt(5)\n\t" \
   "v_mfma_f32_32x32x16_bf16 v[48:63], a[96:99], v[160:163], v[48:63]\n\t" "ds_read_b128 v[188:191], v26 offset:15872\n\t" \
   "v_mfma_f32_32x32x16_bf16 v[48:63], a[100:103], v[164:167], v[48:63]\n\t" "ds_read_b128 v[160:163], v26 offset:16384\n\t" \
   "v_mfma_f32_32x32x16_bf16 v[80:95], v[100:103], a[208:211], v[80:95]\n\t" "buffer_store_dwordx4 v[64:67], v21, %[rs], s50 offen offset:0 sc1\n\t" \
   "s_waitcnt lgkmcnt(5)\n\t" "v_mfma_f32_32x32x16_bf16 v[48:63], a[104:107], v[168:171], v[48:63]\n\t" "ds_read_b128 v[164:167], v26 offset:16896\n\t" \
   "v_cvt_pk_bf16_f32 v68, v68, v69\n\t" "v_cvt_pk_bf16_f32 v69, v76, v77\n\t" "v_cvt_pk_bf16_f32 v70, v70, v71\n\t" \
   "v_cvt_pk_bf16_f32 v71, v78, v79\n\t" "v_mfma_f32_32x32x16_bf16 v[48:63], a[108:111], v[172:175], v[48:63]\n\t" \
   "ds_read_b128 v[168:171], v26 offset:17408\n\t" "s_waitcnt vmcnt(9)\n\t" "v_add_u32 v138, 1, v138\n\t" "ds_write_b32 v13, v138\n\t" \
   "v_mfma_f32_32x32x16_bf16 v[80:95], v[104:107], a[196:199], v[80:95]\n\t" "buffer_store_dwordx4 v[68:71], v21, %[rs], s50 offen offset:512 sc1\n\t" \
   "s_waitcnt lgkmcnt(6)\n\t" "v_mfma_f32_32x32x16_bf16 v[48:63], a[112:115], v[176:179], v[48:63]\n\t" "ds_read_b128 v[172:175], v26 offset:17920\n\t" \
   "buffer_load_dwordx4 v16, %[rs], s51 offen offset:2048 sc1 lds\n\t" "v_mfma_f32_32x32x16_bf16 v[48:63], a[116:119], v[180:183], v[48:63]\n\t" \
   "ds_read_b128 v[176:179], v26 offset:18432\n\t" "v_mfma_f32_32x32x16_bf16 v[80:95], v[108:111], a[212:215], v[80:95]\n\t" \
   "buffer_load_dwordx4 v16, %[rs], s51 offen offset:3072 sc1 lds\n\t" "s_add_u32 m0, m0, 4096\n\t" "s_add_u32 s51, s51, 4096\n\t" \
   "s_waitcnt lgkmcnt(6)\n\t" "v_mfma_f32_32x32x16_bf16 v[48:63], a[120:123], v[184:187], v[48:63]\n\t" "ds_read_b128 v[180:183], v26 offset:18944\n\t" \
   "v_mfma_f32_32x32x16_bf16 v[48:63], a[124:127], v[188:191], v[48:63]\n\t" "ds_read_b128 v[184:187], v26 offset:19456\n\t" \
   "v_mfma_f32_32x32x16_bf16 v[80:95], v[112:115], a[200:203], v[80:95]\n\t" "buffer_load_dwordx4 v16, %[rs], s51 offen offset:0 sc1 lds\n\t" \
   "s_waitcnt lgkmcnt(6)\n\t" "v_mfma_f32_32x32x16_bf16 v[48:63], a[128:131], v[160:163], v[48:63]\n\t" "ds_read_b128 v[188:191], v26 offset:19968\n\t" \
   "v_mfma_f32_32x32x16_bf16 v[48:63], a[132:135], v[164:167], v[48:63]\n\t" "v_mfma_f32_32x32x16_bf16 v[80:95], v[116:119], a[216:219], v[80:95]\n\t" \
   "s_waitcnt lgkmcnt(4)\n\t" "v_mfma_f32_32x32x16_bf16 v[48:63], a[136:139], v[168:171], v[48:63]\n\t" "s_cmp_lt_i32 s40, 2\n\t" \
   "s_cselect_b32 s47, 0x7ffffff0, 0\n\t" "v_or_b32 v30, s47, v20\n\t" "s_sub_i32 s47, s41, 2\n\t" "s_max_i32 s47, s47, 0\n\t" \
   "s_lshl_b32 s47, s47, 12\n\t" "v_mfma_f32_32x32x16_bf16 v[48:63], a[140:143], v[172:175], v[48:63]\n\t" "s_waitcnt vmcnt(11)\n\t" \
   "buffer_load_dwordx4 v16, %[rs], s51 offen offset:1024 sc1 lds\n\t" "s_add_u32 m0, s53, %[ld1]\n\t" "s_nop 0\n\t" \
   "v_mfma_f32_32x32x16_bf16 v[80:95], v[120:123], a[204:207], v[80:95]\n\t" "buffer_store_dword v31, v30, %[rs], s47 offen sc1\n\t" \
   "s_waitcnt lgkmcnt(2)\n\t" "v_mfma_f32_32x32x16_bf16 v[48:63], a[144:147], v[176:179], v[48:63]\n\t" "ds_read_b128 v[156:159], v12\n\t" \
   "v_mfma_f32_32x32x16_bf16 v[48:63], a[148:151], v[180:183], v[48:63]\n\t" "buffer_load_dwordx4 v17, %[rs], s52 offen sc1 lds\n\t" \
   "v_mfma_f32_32x32x16_bf16 v[80:95], v[124:127], a[220:223], v[80:95]\n\t" "s_waitcnt lgkmcnt(1)\n\t" \
   "v_mfma_f32_32x32x16_bf16 v[48:63], a[152:155], v[184:187], v[48:63]\n\t" "s_waitcnt lgkmcnt(0)\n\t" "v_min3_u32 v156, v156, v157, v158\n\t" \
   "v_min_u32 v156, v156, v159\n\t" "v_cmp_ge_u32 vcc, v156, v138\n\t" "s_cbranch_vccz 152f\n\t" "52:\n\t" \
   "v_mfma_f32_32x32x16_bf16 v[48:63], a[156:159], v[188:191], v[48:63]\n\t" "s_add_u32 s50, s49, 65536\n\t" "v_cvt_pk_bf16_f32 v80, v80, v81\n\t" \
   "v_cvt_pk_bf16_f32 v81, v88, v89\n\t" "v_cvt_pk_bf16_f32 v82, v82, v83\n\t" "v_cvt_pk_bf16_f32 v83, v90, v91\n\t" \
   "v_mfma_f32_32x32x16_bf16 v[64:79], v[96:99], a[224:227], 0\n\t" "buffer_store_dwordx4 v[80:83], v21, %[rs], s50 offen offset:0 sc1\n\t" \
   "v_cvt_pk_bf16_f32 v84, v84, v85\n\t" "v_mfma_f32_32x32x16_bf16 v[64:79], v[100:103], a[240:243], v[64:79]\n\t" "v_fmac_f32 v48, v32, v8\n\t" \
   "v_fmac_f32 v49, v33, v8\n\t" "v_fmac_f32 v50, v34, v8\n\t" "v_fmac_f32 v51, v35, v8\n\t" "v_fmac_f32 v52, v36, v8\n\t" \
   "v_fmac_f32 v53, v37, v8\n\t" "v_mfma_f32_32x32x16_bf16 v[64:79], v[104:107], a[228:231], v[64:79]\n\t" "v_fmac_f32 v54, v38, v8\n\t" \
   "v_fmac_f32 v55, v39, v8\n\t" "v_fmac_f32 v56, v40, v8\n\t" "v_fmac_f32 v57, v41, v8\n\t" "v_fmac_f32 v58, v42, v8\n\t" \
   "v_fmac_f32 v59, v43, v8\n\t" "v_mfma_f32_32x32x16_bf16 v[64:79], v[108:111], a[244:247], v[64:79]\n\t" "v_fmac_f32 v60, v44, v8\n\t" \
   "v_fmac_f32 v61, v45, v8\n\t" "v_fmac_f32 v62, v46, v8\n\t" "v_fmac_f32 v63, v47, v8\n\t" "v_mul_f32 v48, v48, v10\n\t" \
   "v_mul_f32 v49, v49, v10\n\t" "v_mfma_f32_32x32x16_bf16 v[64:79], v[112:115], a[232:235], v[64:79]\n\t" "v_mul_f32 v50, v50, v10\n\t" \
   "v_mul_f32 v51, v51, v10\n\t" "v_mul_f32 v52, v52, v10\n\t" "v_mul_f32 v53, v53, v10\n\t" "v_mul_f32 v54, v54, v10\n\t" \
   "v_mul_f32 v55, v55, v10\n\t" "v_mfma_f32_32x32x16_bf16 v[64:79], v[116:119], a[248:251], v[64:79]\n\t" "v_mul_f32 v56, v56, v10\n\t" \
   "v_mul_f32 v57, v57, v10\n\t" "v_mul_f32 v58, v58, v10\n\t" "v_mul_f32 v59, v59, v10\n\t" "v_mul_f32 v60, v60, v10\n\t" \
   "v_mul_f32 v61, v61, v10\n\t" "v_mfma_f32_32x32x16_bf16 v[64:79], v[120:123], a[236:239], v[64:79]\n\t" "v_cvt_pk_bf16_f32 v85, v92, v93\n\t" \
   "v_cvt_pk_bf16_f32 v86, v86, v87\n\t" "v_cvt_pk_bf16_f32 v87, v94, v95\n\t" \
   "buffer_store_dwordx4 v[84:87], v21, %[rs], s50 offen offset:512 sc1\n\t" "v_mfma_f32_32x32x16_bf16 v[64:79], v[124:127], a[252:255], v[64:79]\n\t" \
   "v_mul_f32 v62, v62, v10\n\t" "v_mul_f32 v63, v63, v10\n\t" "v_cvt_pk_bf16_f32 v48, v48, v49\n\t" "v_cvt_pk_bf16_f32 v49, v50, v51\n\t" \
   "v_cvt_pk_bf16_f32 v50, v52, v53\n\t" "v_cvt_pk_bf16_f32 v51, v54, v55\n\t" "v_mfma_f32_32x32x16_bf16 v[80:95], v[96:99], v[192:195], 0\n\t" \
   "ds_write_b128 v14, v[48:51] offset:0\n\t" "v_cvt_pk_bf16_f32 v56, v56, v57\n\t" "v_cvt_pk_bf16_f32 v57, v58, v59\n\t" \
   "v_mfma_f32_32x32x16_bf16 v[80:95], v[100:103], v[208:211], v[80:95]\n\t" "v_cvt_pk_bf16_f32 v58, v60, v61\n\t" \
   "v_cvt_pk_bf16_f32 v59, v62, v63\n\t" "ds_write_b128 v14, v[56:59] offset:1024\n\t" "v_add_u32 v138, 1, v138\n\t" "ds_write_b32 v13, v138\n\t" \
   "v_mfma_f32_32x32x16_bf16 v[80:95], v[104:107], v[196:199], v[80:95]\n\t" "s_add_u32 s50, s49, 131072\n\t" "v_cvt_pk_bf16_f32 v64, v64, v65\n\t" \
   "v_cvt_pk_bf16_f32 v65, v72, v73\n\t" "v_cvt_pk_bf16_f32 v66, v66, v67\n\t" "v_cvt_pk_bf16_f32 v67, v74, v75\n\t" "s_add_u32 s45, s40, 3\n\t" \
   "v_mfma_f32_32x32x16_bf16 v[80:95], v[108:111], v[212:215], v[80:95]\n\t" "buffer_store_dwordx4 v[64:67], v21, %[rs], s50 offen offset:0 sc1\n\t" \
   "v_cvt_pk_bf16_f32 v68, v68, v69\n\t" "v_mfma_f32_32x32x16_bf16 v[80:95], v[112:115], v[200:203], v[80:95]\n\t" \
   "v_cvt_pk_bf16_f32 v69, v76, v77\n\t" "v_cvt_pk_bf16_f32 v70, v70, v71\n\t" "v_cvt_pk_bf16_f32 v71, v78, v79\n\t" "s_min_u32 s45, s45, 63\n\t" \
   "s_mov_b32 s46, s42\n\t" "v_mfma_f32_32x32x16_bf16 v[80:95], v[116:119], v[216:219], v[80:95]\n\t" \
   "buffer_store_dwordx4 v[68:71], v21, %[rs], s50 offen offset:512 sc1\n\t" "v_mfma_f32_32x32x16_bf16 v[80:95], v[120:123], v[204:207], v[80:95]\n\t" \
   "s_cmp_eq_u32 s44, -1\n\t" "s_cbranch_scc0 174f\n\t" "74:\n\t" "ds_read_b128 v[160:163], v28 offset:0\n\t" \
   "ds_read_b128 v[164:167], v28 offset:512\n\t" "v_mfma_f32_32x32x16_bf16 v[80:95], v[124:127], v[220:223], v[80:95]\n\t" "s_mov_b32 s42, s43\n\t" \
   "ds_read_b128 v[168:171], v28 offset:1024\n\t" "s_add_u32 s43, s43, 44032\n\t" "s_cmp_eq_u32 s43, 132096\n\t" "s_cselect_b32 s43, 0, s43\n\t" \
   "v_mfma_f32_32x32x16_bf16 v[140:155], v[96:99], v[224:227], 0\n\t" "ds_read_b128 v[172:175], v28 offset:1536\n\t" \
   "ds_read_b128 v[176:179], v28 offset:2048\n\t" "s_add_u32 s41, s41, 1\n\t" "s_add_u32 s40, s40, 1\n\t" "v_xor_b32 v14, 0x2000, v14\n\t" \
   "v_mfma_f32_32x32x16_bf16 v[140:155], v[100:103], v[240:243], v[140:155]\n\t" "s_add_u32 s50, s49, 196608\n\t" "v_cvt_pk_bf16_f32 v80, v80, v81\n\t" \
   "v_cvt_pk_bf16_f32 v81, v88, v89\n\t" "v_cvt_pk_bf16_f32 v82, v82, v83\n\t" "v_cvt_pk_bf16_f32 v83, v90, v91\n\t" "v_xor_b32 v15, 0x2000, v15\n\t" \
   "v_mfma_f32_32x32x16_bf16 v[140:155], v[104:107], v[228:231], v[140:155]\n\t" \
   "buffer_store_dwordx4 v[80:83], v21, %[rs], s50 offen offset:0 sc1\n\t" "v_cvt_pk_bf16_f32 v84, v84, v85\n\t" \
   "v_mfma_f32_32x32x16_bf16 v[140:155], v[108:111], v[244:247], v[140:155]\n\t" "v_cvt_pk_bf16_f32 v85, v92, v93\n\t" \
   "v_cvt_pk_bf16_f32 v86, v86, v87\n\t" "v_cvt_pk_bf16_f32 v87, v94, v95\n\t" "ds_read_b128 v[180:183], v28 offset:2560\n\t" \
   "ds_read_b128 v[184:187], v28 offset:3072\n\t" "v_mfma_f32_32x32x16_bf16 v[140:155], v[112:115], v[232:235], v[140:155]\n\t" \
   "buffer_store_dwordx4 v[84:87], v21, %[rs], s50 offen offset:512 sc1\n\t" "v_mov_b32 v18, v21\n\t" \
   "v_mfma_f32_32x32x16_bf16 v[140:155], v[116:119], v[248:251], v[140:155]\n\t" \
   "v_mfma_f32_32x32x16_bf16 v[140:155], v[120:123], v[236:239], v[140:155]\n\t" \
   "v_mfma_f32_32x32x16_bf16 v[140:155], v[124:127], v[252:255], v[140:155]\n\t" "s_cmp_lt_u32 s40, 64\n\t" "s_cbranch_scc1 1b\n\t" "s_nop 7\n\t" \
   "s_nop 2\n\t" "s_waitcnt lgkmcnt(0)\n\t" "s_barrier\n\t" "s_add_u32 s50, s49, 262144\n\t" "v_cvt_pk_bf16_f32 v140, v140, v141\n\t" \
   "v_cvt_pk_bf16_f32 v141, v148, v149\n\t" "v_cvt_pk_bf16_f32 v142, v142, v143\n\t" "v_cvt_pk_bf16_f32 v143, v150, v151\n\t" \
   "buffer_store_dwordx4 v[140:143], v21, %[rs], s50 offen offset:0 sc1\n\t" "v_cvt_pk_bf16_f32 v144, v144, v145\n\t" \
   "v_cvt_pk_bf16_f32 v145, v152, v153\n\t" "v_cvt_pk_bf16_f32 v146, v146, v147\n\t" "v_cvt_pk_bf16_f32 v147, v154, v155\n\t" \
   "buffer_store_dwordx4 v[144:147], v21, %[rs], s50 offen offset:512 sc1\n\t" "s_sub_i32 s49, s41, 1\n\t" "s_mul_i32 s49, s49, 1310720\n\t" \
   "ds_read_b128 v[96:99], v15 offset:0\n\t" "ds_read_b128 v[100:103], v15 offset:1024\n\t" "ds_read_b128 v[104:107], v15 offset:2048\n\t" \
   "ds_read_b128 v[108:111], v15 offset:3072\n\t" "ds_read_b128 v[112:115], v15 offset:4096\n\t" "ds_read_b128 v[116:119], v15 offset:5120\n\t" \
   "ds_read_b128 v[120:123], v15 offset:6144\n\t" "ds_read_b128 v[124:127], v15 offset:7168\n\t" "s_waitcnt lgkmcnt(6)\n\t" \
   "v_mfma_f32_32x32x16_bf16 v[64:79], v[96:99], a[160:163], 0\n\t" "v_mfma_f32_32x32x16_bf16 v[64:79], v[100:103], a[176:179], v[64:79]\n\t" \
   "s_waitcnt lgkmcnt(4)\n\t" "v_mfma_f32_32x32x16_bf16 v[64:79], v[104:107], a[164:167], v[64:79]\n\t" \
   "v_mfma_f32_32x32x16_bf16 v[64:79], v[108:111], a[180:183], v[64:79]\n\t" "s_waitcnt lgkmcnt(2)\n\t" \
   "v_mfma_f32_32x32x16_bf16 v[64:79], v[112:115], a[168:171], v[64:79]\n\t" "v_mfma_f32_32x32x16_bf16 v[64:79], v[116:119], a[184:187], v[64:79]\n\t" \
   "s_waitcnt lgkmcnt(0)\n\t" "v_mfma_f32_32x32x16_bf16 v[64:79], v[120:123], a[172:175], v[64:79]\n\t" \
   "v_mfma_f32_32x32x16_bf16 v[64:79], v[124:127], a[188:191], v[64:79]\n\t" "v_mfma_f32_32x32x16_bf16 v[80:95], v[96:99], a[192:195], 0\n\t" \
   "v_mfma_f32_32x32x16_bf16 v[80:95], v[100:103], a[208:211], v[80:95]\n\t" "s_add_u32 s50, s49, 0\n\t" "v_cvt_pk_bf16_f32 v64, v64, v65\n\t" \
   "v_cvt_pk_bf16_f32 v65, v72, v73\n\t" "v_cvt_pk_bf16_f32 v66, v66, v67\n\t" "v_cvt_pk_bf16_f32 v67, v74, v75\n\t" \
   "v_mfma_f32_32x32x16_bf16 v[80:95], v[104:107], a[196:199], v[80:95]\n\t" "buffer_store_dwordx4 v[64:67], v21, %[rs], s50 offen offset:0 sc1\n\t" \
   "v_cvt_pk_bf16_f32 v68, v68, v69\n\t" "v_mfma_f32_32x32x16_bf16 v[80:95], v[108:111], a[212:215], v[80:95]\n\t" \
   "v_cvt_pk_bf16_f32 v69, v76, v77\n\t" "v_cvt_pk_bf16_f32 v70, v70, v71\n\t" "v_cvt_pk_bf16_f32 v71, v78, v79\n\t" \
   "v_mfma_f32_32x32x16_bf16 v[80:95], v[112:115], a[200:203], v[80:95]\n\t" "buffer_store_dwordx4 v[68:71], v21, %[rs], s50 offen offset:512 sc1\n\t" \
   "v_mfma_f32_32x32x16_bf16 v[80:95], v[116:119], a[216:219], v[80:95]\n\t" "v_mfma_f32_32x32x16_bf16 v[80:95], v[120:123], a[204:207], v[80:95]\n\t" \
   "v_mfma_f32_32x32x16_bf16 v[80:95], v[124:127], a[220:223], v[80:95]\n\t" "v_mfma_f32_32x32x16_bf16 v[64:79], v[96:99], a[224:227], 0\n\t" \
   "v_mfma_f32_32x32x16_bf16 v[64:79], v[100:103], a[240:243], v[64:79]\n\t" "s_add_u32 s50, s49, 65536\n\t" "v_cvt_pk_bf16_f32 v80, v80, v81\n\t" \
   "v_cvt_pk_bf16_f32 v81, v88, v89\n\t" "v_cvt_pk_bf16_f32 v82, v82, v83\n\t" "v_cvt_pk_bf16_f32 v83, v90, v91\n\t" \
   "v_mfma_f32_32x32x16_bf16 v[64:79], v[104:107], a[228:231], v[64:79]\n\t" "buffer_store_dwordx4 v[80:83], v21, %[rs], s50 offen offset:0 sc1\n\t" \
   "v_cvt_pk_bf16_f32 v84, v84, v85\n\t" "v_mfma_f32_32x32x16_bf16 v[64:79], v[108:111], a[244:247], v[64:79]\n\t" \
   "v_cvt_pk_bf16_f32 v85, v92, v93\n\t" "v_cvt_pk_bf16_f32 v86, v86, v87\n\t" "v_cvt_pk_bf16_f32 v87, v94, v95\n\t" \
   "v_mfma_f32_32x32x16_bf16 v[64:79], v[112:115], a[232:235], v[64:79]\n\t" "buffer_store_dwordx4 v[84:87], v21, %[rs], s50 offen offset:512 sc1\n\t" \
   "v_mfma_f32_32x32x16_bf16 v[64:79], v[116:119], a[248:251], v[64:79]\n\t" "v_mfma_f32_32x32x16_bf16 v[64:79], v[120:123], a[236:239], v[64:79]\n\t" \
   "v_mfma_f32_32x32x16_bf16 v[64:79], v[124:127], a[252:255], v[64:79]\n\t" "v_mfma_f32_32x32x16_bf16 v[80:95], v[96:99], v[192:195], 0\n\t" \
   "v_mfma_f32_32x32x16_bf16 v[80:95], v[100:103], v[208:211], v[80:95]\n\t" "s_add_u32 s50, s49, 131072\n\t" "v_cvt_pk_bf16_f32 v64, v64, v65\n\t" \
   "v_cvt_pk_bf16_f32 v65, v72, v73\n\t" "v_cvt_pk_bf16_f32 v66, v66, v67\n\t" "v_cvt_pk_bf16_f32 v67, v74, v75\n\t" \
   "v_mfma_f32_32x32x16_bf16 v[80:95], v[104:107], v[196:199], v[80:95]\n\t" "buffer_store_dwordx4 v[64:67], v21, %[rs], s50 offen offset:0 sc1\n\t" \
   "v_cvt_pk_bf16_f32 v68, v68, v69\n\t" "v_mfma_f32_32x32x16_bf16 v[80:95], v[108:111], v[212:215], v[80:95]\n\t" \
   "v_cvt_pk_bf16_f32 v69, v76, v77\n\t" "v_cvt_pk_bf16_f32 v70, v70, v71\n\t" "v_cvt_pk_bf16_f32 v71, v78, v79\n\t" \
   "v_mfma_f32_32x32x16_bf16 v[80:95], v[112:115], v[200:203], v[80:95]\n\t" "buffer_store_dwordx4 v[68:71], v21, %[rs], s50 offen offset:512 sc1\n\t" \
   "v_mfma_f32_32x32x16_bf16 v[80:95], v[116:119], v[216:219], v[80:95]\n\t" "v_mfma_f32_32x32x16_bf16 v[80:95], v[120:123], v[204:207], v[80:95]\n\t" \
   "v_mfma_f32_32x32x16_bf16 v[80:95], v[124:127], v[220:223], v[80:95]\n\t" "v_mfma_f32_32x32x16_bf16 v[140:155], v[96:99], v[224:227], 0\n\t" \
   "v_mfma_f32_32x32x16_bf16 v[140:155], v[100:103], v[240:243], v[140:155]\n\t" "s_add_u32 s50, s49, 196608\n\t" "v_cvt_pk_bf16_f32 v80, v80, v81\n\t" \
   "v_cvt_pk_bf16_f32 v81, v88, v89\n\t" "v_cvt_pk_bf16_f32 v82, v82, v83\n\t" "v_cvt_pk_bf16_f32 v83, v90, v91\n\t" \
   "v_mfma_f32_32x32x16_bf16 v[140:155], v[104:107], v[228:231], v[140:155]\n\t" \
   "buffer_store_dwordx4 v[80:83], v21, %[rs], s50 offen offset:0 sc1\n\t" "v_cvt_pk_bf16_f32 v84, v84, v85\n\t" \
   "v_mfma_f32_32x32x16_bf16 v[140:155], v[108:111], v[244:247], v[140:155]\n\t" "v_cvt_pk_bf16_f32 v85, v92, v93\n\t" \
   "v_cvt_pk_bf16_f32 v86, v86, v87\n\t" "v_cvt_pk_bf16_f32 v87, v94, v95\n\t" \
   "v_mfma_f32_32x32x16_bf16 v[140:155], v[112:115], v[232:235], v[140:155]\n\t" \
   "buffer_store_dwordx4 v[84:87], v21, %[rs], s50 offen offset:512 sc1\n\t" \
   "v_mfma_f32_32x32x16_bf16 v[140:155], v[116:119], v[248:251], v[140:155]\n\t" \
   "v_mfma_f32_32x32x16_bf16 v[140:155], v[120:123], v[236:239], v[140:155]\n\t" \
   "v_mfma_f32_32x32x16_bf16 v[140:155], v[124:127], v[252:255], v[140:155]\n\t" "s_nop 7\n\t" "s_nop 2\n\t" "s_add_u32 s50, s49, 262144\n\t" \
   "v_cvt_pk_bf16_f32 v140, v140, v141\n\t" "v_cvt_pk_bf16_f32 v141, v148, v149\n\t" "v_cvt_pk_bf16_f32 v142, v142, v143\n\t" \
   "v_cvt_pk_bf16_f32 v143, v150, v151\n\t" "buffer_store_dwordx4 v[140:143], v21, %[rs], s50 offen offset:0 sc1\n\t" \
   "v_cvt_pk_bf16_f32 v144, v144, v145\n\t" "v_cvt_pk_bf16_f32 v145, v152, v153\n\t" "v_cvt_pk_bf16_f32 v146, v146, v147\n\t" \
   "v_cvt_pk_bf16_f32 v147, v154, v155\n\t" "buffer_store_dwordx4 v[144:147], v21, %[rs], s50 offen offset:512 sc1\n\t" "s_waitcnt vmcnt(0)\n\t" \
   "s_cmp_lt_i32 s40, 2\n\t" "s_cselect_b32 s47, 0x7ffffff0, 0\n\t" "v_or_b32 v30, s47, v20\n\t" "s_sub_i32 s47, s41, 2\n\t" \
   "s_max_i32 s47, s47, 0\n\t" "s_lshl_b32 s47, s47, 12\n\t" "buffer_store_dword v31, v30, %[rs], s47 offen sc1\n\t" "s_cmp_lt_i32 s40, 1\n\t" \
   "s_cselect_b32 s47, 0x7ffffff0, 0\n\t" "v_or_b32 v30, s47, v20\n\t" "s_sub_i32 s47, s41, 1\n\t" "s_max_i32 s47, s47, 0\n\t" \
   "s_lshl_b32 s47, s47, 12\n\t" "buffer_store_dword v31, v30, %[rs], s47 offen sc1\n\t" "s_branch 9f\n\t" "172:\n\t" "v_cmp_lt_i32 vcc, 6, v25\n\t" \
   "s_lshr_b32 s48, s45, 1\n\t" "s_or_b32 s44, s44, vcc_lo\n\t" "s_cmp_lt_u32 s40, 4\n\t" "s_cbranch_scc1 272f\n\t" \
   "buffer_load_dword v25, v19, %[rs], 0 offen sc1\n\t" "272:\n\t" "s_bitcmp1_b32 s44, s48\n\t" "s_cbranch_scc1 72b\n\t" "s_mov_b32 s47, 0\n\t" \
   "buffer_load_dword v30, v19, %[rs], 0 offen sc1\n\t" "s_waitcnt vmcnt(0)\n\t" "v_add_u32 v30, 1, v30\n\t" "v_cmp_lt_u32 vcc, 7, v30\n\t" \
   "s_or_b32 s44, s44, vcc_lo\n\t" "s_bitcmp1_b32 s44, s48\n\t" "s_cbranch_scc1 72b\n\t" "s_bitset1_b32 s44, s48\n\t" "s_lshl_b32 s48, s48, 7\n\t" \
   "s_add_u32 s48, s48, %[cnt]\n\t" "73:\n\t" "s_sleep 10\n\t" "buffer_load_dword v30, off, %[rs], s48 sc1\n\t" "s_add_u32 s47, s47, 1\n\t" \
   "s_waitcnt vmcnt(0)\n\t" "v_add_u32 v30, 1, v30\n\t" "v_cmp_lt_u32 vcc, 7, v30\n\t" "s_cmp_gt_u32 s47, 0x20000\n\t" "s_cbranch_scc1 72b\n\t" \
   "s_cbranch_vccz 73b\n\t" "s_branch 72b\n\t" "176:\n\t" "v_cmp_lt_i32 vcc, 6, v25\n\t" "s_lshr_b32 s48, s45, 1\n\t" "s_or_b32 s44, s44, vcc_lo\n\t" \
   "s_cmp_lt_u32 s40, 4\n\t" "s_cbranch_scc1 276f\n\t" "buffer_load_dword v25, v19, %[rs], 0 offen sc1\n\t" "276:\n\t" "s_bitcmp1_b32 s44, s48\n\t" \
   "s_cbranch_scc1 76b\n\t" "s_mov_b32 s47, 0\n\t" "buffer_load_dword v30, v19, %[rs], 0 offen sc1\n\t" "s_waitcnt vmcnt(0)\n\t" \
   "v_add_u32 v30, 1, v30\n\t" "v_cmp_lt_u32 vcc, 7, v30\n\t" "s_or_b32 s44, s44, vcc_lo\n\t" "s_bitcmp1_b32 s44, s48\n\t" "s_cbranch_scc1 76b\n\t" \
   "s_bitset1_b32 s44, s48\n\t" "s_lshl_b32 s48, s48, 7\n\t" "s_add_u32 s48, s48, %[cnt]\n\t" "77:\n\t" "s_sleep 10\n\t" \
   "buffer_load_dword v30, off, %[rs], s48 sc1\n\t" "s_add_u32 s47, s47, 1\n\t" "s_waitcnt vmcnt(0)\n\t" "v_add_u32 v30, 1, v30\n\t" \
   "v_cmp_lt_u32 vcc, 7, v30\n\t" "s_cmp_gt_u32 s47, 0x20000\n\t" "s_cbranch_scc1 76b\n\t" "s_cbranch_vccz 77b\n\t" "s_branch 76b\n\t" "174:\n\t" \
   "v_cmp_lt_i32 vcc, 6, v25\n\t" "s_lshr_b32 s48, s45, 1\n\t" "s_or_b32 s44, s44, vcc_lo\n\t" "s_cmp_lt_u32 s40, 4\n\t" "s_cbranch_scc1 274f\n\t" \
   "buffer_load_dword v25, v19, %[rs], 0 offen sc1\n\t" "274:\n\t" "s_bitcmp1_b32 s44, s48\n\t" "s_cbranch_scc1 74b\n\t" "s_mov_b32 s47, 0\n\t" \
   "buffer_load_dword v30, v19, %[rs], 0 offen sc1\n\t" "s_waitcnt vmcnt(0)\n\t" "v_add_u32 v30, 1, v30\n\t" "v_cmp_lt_u32 vcc, 7, v30\n\t" \
   "s_or_b32 s44, s44, vcc_lo\n\t" "s_bitcmp1_b32 s44, s48\n\t" "s_cbranch_scc1 74b\n\t" "s_bitset1_b32 s44, s48\n\t" "s_lshl_b32 s48, s48, 7\n\t" \
   "s_add_u32 s48, s48, %[cnt]\n\t" "75:\n\t" "s_sleep 10\n\t" "buffer_load_dword v30, off, %[rs], s48 sc1\n\t" "s_add_u32 s47, s47, 1\n\t" \
   "s_waitcnt vmcnt(0)\n\t" "v_add_u32 v30, 1, v30\n\t" "v_cmp_lt_u32 vcc, 7, v30\n\t" "s_cmp_gt_u32 s47, 0x20000\n\t" "s_cbranch_scc1 74b\n\t" \
   "s_cbranch_vccz 75b\n\t" "s_branch 74b\n\t" "150:\n\t" "ds_read_b128 v[156:159], v12\n\t" "s_waitcnt lgkmcnt(0)\n\t" \
   "v_min3_u32 v156, v156, v157, v158\n\t" "v_min_u32 v156, v156, v159\n\t" "v_cmp_ge_u32 vcc, v156, v138\n\t" "s_cbranch_vccz 150b\n\t" \
   "s_branch 50b\n\t" "152:\n\t" "ds_read_b128 v[156:159], v12\n\t" "s_waitcnt lgkmcnt(0)\n\t" "v_min3_u32 v156, v156, v157, v158\n\t" \
   "v_min_u32 v156, v156, v159\n\t" "v_cmp_ge_u32 vcc, v156, v138\n\t" "s_cbranch_vccz 152b\n\t" "s_branch 52b\n\t" "9:\n\t" "s_waitcnt vmcnt(0)"
#define VU_CLOB "v8", "v9", "v10", "v11", "v12", "v13", "v14", "v15", "v32", "v33", "v34", "v35", "v36", "v37", "v38", "v39", "v40", "v41", "v42", "v43", "v44", "v45", "v46", "v47", "v48", "v49", "v50", "v51", "v52", "v53", "v54", "v55", "v56", "v57", "v58", "v59", "v60", "v61", "v62", "v63", "v64", "v65", "v66", "v67", "v68", "v69", "v70", "v71", "v72", "v73", "v74", "v75", "v76", "v77", "v78", "v79", "v80", "v81", "v82", "v83", "v84", "v85", "v86", "v87", "v88", "v89", "v90", "v91", "v92", "v93", "v94", "v95", "v96", "v97", "v98", "v99", "v100", "v101", "v102", "v103", "v104", "v105", "v106", "v107", "v108", "v109", "v110", "v111", "v112", "v113", "v114", "v115", "v116", "v117", "v118", "v119", "v120", "v121", "v122", "v123", "v124", "v125", "v126", "v127", "v128", "v129", "v130", "v131", "v132", "v133", "v134", "v135", "v136", "v137", "v138", "v139", "v140", "v141", "v142", "v143", "v144", "v145", "v146", "v147", "v148", "v149", "v150", "v151", "v152", "v153", "v154", "v155", "v156", "v157", "v158", "v159", "v160", "v161", "v162", "v163", "v164", "v165", "v166", "v167", "v168", "v169", "v170", "v171", "v172", "v173", "v174", "v175", "v176", "v177", "v178", "v179", "v180", "v181", "v182", "v183", "v184", "v185", "v186", "v187", "v188", "v189", "v190", "v191", "s40", "s41", "s42", "s43", "s44", "s45", "s46", "s47", "s48", "s49", "s50", "s51", "s52", "s53", "s54", "s55"
#define DF_ASM \
   "v_mov_b32 v80, 0x3f803f80\n\t" "v_mov_b32 v81, 0x3f803f80\n\t" "v_mov_b32 v82, 0x3f803f80\n\t" "v_mov_b32 v83, 0x3f803f80\n\t" \
   "s_mov_b32 s45, %[set]\n\t" "s_mov_b32 s46, 0\n\t" "v_mfma_f32_16x16x32_bf16 v[32:35], a[0:3], v[80:83], 0\n\t" \
   "v_mfma_f32_16x16x32_bf16 v[32:35], a[4:7], v[80:83], v[32:35]\n\t" "v_mfma_f32_16x16x32_bf16 v[32:35], a[8:11], v[80:83], v[32:35]\n\t" \
   "v_mfma_f32_16x16x32_bf16 v[32:35], a[12:15], v[80:83], v[32:35]\n\t" "v_mfma_f32_16x16x32_bf16 v[32:35], a[16:19], v[80:83], v[32:35]\n\t" \
   "v_mfma_f32_16x16x32_bf16 v[32:35], a[20:23], v[80:83], v[32:35]\n\t" "v_mfma_f32_16x16x32_bf16 v[32:35], a[24:27], v[80:83], v[32:35]\n\t" \
   "v_mfma_f32_16x16x32_bf16 v[32:35], a[28:31], v[80:83], v[32:35]\n\t" "v_mfma_f32_16x16x32_bf16 v[32:35], a[32:35], v[80:83], v[32:35]\n\t" \
   "v_mfma_f32_16x16x32_bf16 v[32:35], a[36:39], v[80:83], v[32:35]\n\t" "v_mfma_f32_16x16x32_bf16 v[32:35], a[40:43], v[80:83], v[32:35]\n\t" \
   "v_mfma_f32_16x16x32_bf16 v[32:35], a[44:47], v[80:83], v[32:35]\n\t" "v_mfma_f32_16x16x32_bf16 v[32:35], a[48:51], v[80:83], v[32:35]\n\t" \
   "v_mfma_f32_16x16x32_bf16 v[32:35], a[52:55], v[80:83], v[32:35]\n\t" "v_mfma_f32_16x16x32_bf16 v[32:35], a[56:59], v[80:83], v[32:35]\n\t" \
   "v_mfma_f32_16x16x32_bf16 v[32:35], a[60:63], v[80:83], v[32:35]\n\t" "v_mfma_f32_16x16x32_bf16 v[32:35], a[64:67], v[80:83], v[32:35]\n\t" \
   "v_mfma_f32_16x16x32_bf16 v[32:35], a[68:71], v[80:83], v[32:35]\n\t" "v_mfma_f32_16x16x32_bf16 v[32:35], a[72:75], v[80:83], v[32:35]\n\t" \
   "v_mfma_f32_16x16x32_bf16 v[32:35], a[76:79], v[80:83], v[32:35]\n\t" "v_mfma_f32_16x16x32_bf16 v[36:39], a[80:83], v[80:83], 0\n\t" \
   "v_mfma_f32_16x16x32_bf16 v[36:39], a[84:87], v[80:83], v[36:39]\n\t" "v_mfma_f32_16x16x32_bf16 v[36:39], a[88:91], v[80:83], v[36:39]\n\t" \
   "v_mfma_f32_16x16x32_bf16 v[36:39], a[92:95], v[80:83], v[36:39]\n\t" "v_mfma_f32_16x16x32_bf16 v[36:39], a[96:99], v[80:83], v[36:39]\n\t" \
   "v_mfma_f32_16x16x32_bf16 v[36:39], a[100:103], v[80:83], v[36:39]\n\t" "v_mfma_f32_16x16x32_bf16 v[36:39], a[104:107], v[80:83], v[36:39]\n\t" \
   "v_mfma_f32_16x16x32_bf16 v[36:39], a[108:111], v[80:83], v[36:39]\n\t" "v_mfma_f32_16x16x32_bf16 v[36:39], a[112:115], v[80:83], v[36:39]\n\t" \
   "v_mfma_f32_16x16x32_bf16 v[36:39], a[116:119], v[80:83], v[36:39]\n\t" "v_mfma_f32_16x16x32_bf16 v[36:39], a[120:123], v[80:83], v[36:39]\n\t" \
   "v_mfma_f32_16x16x32_bf16 v[36:39], a[124:127], v[80:83], v[36:39]\n\t" "v_mfma_f32_16x16x32_bf16 v[36:39], a[128:131], v[80:83], v[36:39]\n\t" \
   "v_mfma_f32_16x16x32_bf16 v[36:39], a[132:135], v[80:83], v[36:39]\n\t" "v_mfma_f32_16x16x32_bf16 v[36:39], a[136:139], v[80:83], v[36:39]\n\t" \
   "v_mfma_f32_16x16x32_bf16 v[36:39], a[140:143], v[80:83], v[36:39]\n\t" "v_mfma_f32_16x16x32_bf16 v[36:39], a[144:147], v[80:83], v[36:39]\n\t" \
   "v_mfma_f32_16x16x32_bf16 v[36:39], a[148:151], v[80:83], v[36:39]\n\t" "v_mfma_f32_16x16x32_bf16 v[36:39], a[152:155], v[80:83], v[36:39]\n\t" \
   "v_mfma_f32_16x16x32_bf16 v[36:39], a[156:159], v[80:83], v[36:39]\n\t" "s_add_u32 s48, s45, 60\n\t" "s_lshl_b32 s48, s48, 8\n\t" \
   "s_add_u32 s47, s48, %[xs4]\n\t" "v_mov_b32 v64, -1\n\t" "v_mov_b32 v65, -1\n\t" "v_mov_b32 v72, 0x7ffffff0\n\t" \
   "buffer_store_dwordx2 v[64:65], v21, %[rs], s48 offen offset:0 sc1\n\t" "buffer_store_dwordx2 v[64:65], v21, %[rs], s47 offen offset:0 sc1\n\t" \
   "buffer_store_dwordx2 v[64:65], v21, %[rs], s48 offen offset:128 sc1\n\t" "buffer_store_dwordx2 v[64:65], v21, %[rs], s47 offen offset:128 sc1\n\t" \
   "1:\n\t" "s_cmp_lt_u32 s45, 61\n\t" "s_cselect_b32 s40, 10, 160\n\t" "s_cselect_b64 s[42:43], 0, -1\n\t" "5:\n\t" "s_lshl_b32 s48, s45, 7\n\t" \
   "s_mov_b32 s47, 0\n\t" "s_mov_b32 s41, 0\n\t" "s_mov_b32 s50, 0\n\t" "s_cmp_lg_u32 %[ldx], %[ld0]\n\t" "s_cbranch_scc1 3f\n\t" "2:\n\t" \
   "buffer_load_dwordx2 v[64:65], v19, %[rs], s48 offen sc1\n\t" "s_add_u32 s47, s47, 1\n\t" "s_waitcnt vmcnt(0)\n\t" \
   "v_readfirstlane_b32 s44, v64\n\t" "v_readfirstlane_b32 s49, v65\n\t" "s_add_u32 s44, s44, 1\n\t" "s_cmp_ge_u32 s44, s40\n\t" \
   "s_cbranch_scc1 6f\n\t" "s_cmp_gt_u32 s47, 0x20000\n\t" "s_cbranch_scc1 3f\n\t" "s_cmp_lt_u32 s45, 56\n\t" "s_cbranch_scc1 4f\n\t" "s_sleep 3\n\t" \
   "s_cmp_lg_u32 s44, 0\n\t" "s_cbranch_scc1 2b\n\t" "s_sleep 8\n\t" "s_branch 2b\n\t" "4:\n\t" "s_sleep 60\n\t" "s_branch 2b\n\t" "6:\n\t" \
   "s_cmp_eq_u32 s40, 160\n\t" "s_cbranch_scc0 3f\n\t" "s_memrealtime s[54:55]\n\t" "s_waitcnt lgkmcnt(0)\n\t" "s_sub_u32 s50, s54, s49\n\t" \
   "s_and_b32 s50, s50, 0x7fffffff\n\t" "s_min_u32 s50, s50, %[tg]\n\t" "s_sub_u32 s50, %[tg], s50\n\t" "s_add_u32 s49, s54, s50\n\t" "3:\n\t" \
   "s_waitcnt vmcnt(0)\n\t" "s_barrier\n\t" "s_memrealtime s[54:55]\n\t" "s_cmp_lt_u32 s45, 60\n\t" "s_cselect_b32 s47, 0x7ffffff0, 0\n\t" \
   "v_or_b32 v30, s47, v20\n\t" "s_waitcnt lgkmcnt(0)\n\t" "s_cmp_lg_u32 s50, 0\n\t" "s_cselect_b32 s49, s49, s54\n\t" "v_mov_b32 v31, s49\n\t" \
   "v_min_u32 v31, -2, v31\n\t" "buffer_store_dword v31, v30, %[rs], 0 offen sc1\n\t" "s_cbranch_scc0 8f\n\t" "7:\n\t" "s_memrealtime s[54:55]\n\t" \
   "s_waitcnt lgkmcnt(0)\n\t" "s_sub_u32 s50, s49, s54\n\t" "s_cmp_gt_i32 s50, 0\n\t" "s_cbranch_scc1 7b\n\t" "8:\n\t" "s_barrier\n\t" \
   "s_mul_i32 s52, s45, 1536\n\t" "s_mul_i32 s51, s45, 40960\n\t" "s_add_u32 m0, s46, %[ldx]\n\t" "s_mov_b32 s53, s46\n\t" \
   "buffer_load_dwordx4 v16, %[rs], s51 offen offset:0 sc1 lds\n\t" "buffer_load_dwordx4 v16, %[rs], s51 offen offset:1024 sc1 lds\n\t" \
   "buffer_load_dwordx4 v16, %[rs], s51 offen offset:2048 sc1 lds\n\t" "buffer_load_dwordx4 v16, %[rs], s51 offen offset:3072 sc1 lds\n\t" \
   "s_add_u32 m0, m0, 4096\n\t" "s_add_u32 s51, s51, 4096\n\t" "buffer_load_dwordx4 v16, %[rs], s51 offen offset:0 sc1 lds\n\t" \
   "buffer_load_dwordx4 v16, %[rs], s51 offen offset:1024 sc1 lds\n\t" "buffer_load_dwordx4 v16, %[rs], s51 offen offset:2048 sc1 lds\n\t" \
   "buffer_load_dwordx4 v16, %[rs], s51 offen offset:3072 sc1 lds\n\t" "s_add_u32 m0, m0, 4096\n\t" "s_add_u32 s51, s51, 4096\n\t" \
   "buffer_load_dwordx4 v16, %[rs], s51 offen offset:0 sc1 lds\n\t" "buffer_load_dwordx4 v16, %[rs], s51 offen offset:1024 sc1 lds\n\t" \
   "s_add_u32 m0, s53, %[ld1]\n\t" "s_nop 0\n\t" "buffer_load_dwordx4 v17, %[rs], s52 offen sc1 lds\n\t" "v_add_u32 v26, s46, v22\n\t" \
   "v_add_u32 v27, s46, v23\n\t" "s_waitcnt vmcnt(0)\n\t" "s_barrier\n\t" "ds_read_b128 v[80:83], v26 offset:0\n\t" \
   "ds_read_b128 v[84:87], v26 offset:1024\n\t" "ds_read_b128 v[88:91], v26 offset:2048\n\t" "ds_read_b128 v[92:95], v26 offset:3072\n\t" \
   "ds_read_b128 v[96:99], v26 offset:4096\n\t" "ds_read_b128 v[100:103], v26 offset:5120\n\t" "ds_read_b128 v[104:107], v26 offset:6144\n\t" \
   "s_waitcnt lgkmcnt(5)\n\t" "v_mfma_f32_16x16x32_bf16 v[52:55], a[80:83], v[80:83], 0\n\t" "ds_read_b128 v[112:115], v27 offset:0\n\t" \
   "v_mfma_f32_16x16x32_bf16 v[48:51], a[0:3], v[80:83], 0\n\t" "ds_read_b128 v[108:111], v26 offset:7168\n\t" \
   "ds_read_b128 v[116:119], v27 offset:16\n\t" "v_mfma_f32_16x16x32_bf16 v[52:55], a[84:87], v[84:87], v[52:55]\n\t" \
   "ds_read_b64 v[120:121], v27 offset:32\n\t" "v_mfma_f32_16x16x32_bf16 v[48:51], a[4:7], v[84:87], v[48:51]\n\t" \
   "ds_read_b128 v[80:83], v26 offset:8192\n\t" "s_waitcnt lgkmcnt(8)\n\t" "v_mfma_f32_16x16x32_bf16 v[52:55], a[88:91], v[88:91], v[52:55]\n\t" \
   "ds_read_b128 v[122:125], v27 offset:768\n\t" "v_mfma_f32_16x16x32_bf16 v[48:51], a[8:11], v[88:91], v[48:51]\n\t" \
   "ds_read_b128 v[84:87], v26 offset:9216\n\t" "ds_read_b128 v[126:129], v27 offset:784\n\t" \
   "v_mfma_f32_16x16x32_bf16 v[52:55], a[92:95], v[92:95], v[52:55]\n\t" "v_mfma_f32_16x16x32_bf16 v[48:51], a[12:15], v[92:95], v[48:51]\n\t" \
   "ds_read_b128 v[88:91], v26 offset:10240\n\t" "ds_read_b64 v[130:131], v27 offset:800\n\t" "s_waitcnt lgkmcnt(11)\n\t" \
   "v_mfma_f32_16x16x32_bf16 v[52:55], a[96:99], v[96:99], v[52:55]\n\t" "s_lshl_b32 s48, s45, 8\n\t" \
   "v_mfma_f32_16x16x32_bf16 v[48:51], a[16:19], v[96:99], v[48:51]\n\t" "ds_read_b128 v[92:95], v26 offset:11264\n\t" "s_add_u32 s47, s48, %[xs4]\n\t" \
   "v_mfma_f32_16x16x32_bf16 v[52:55], a[100:103], v[100:103], v[52:55]\n\t" "v_mfma_f32_16x16x32_bf16 v[48:51], a[20:23], v[100:103], v[48:51]\n\t" \
   "ds_read_b128 v[96:99], v26 offset:12288\n\t" "s_waitcnt lgkmcnt(8)\n\t" "v_add_f32 v112, v112, v114\n\t" \
   "v_mfma_f32_16x16x32_bf16 v[52:55], a[104:107], v[104:107], v[52:55]\n\t" "v_add_f32 v113, v113, v115\n\t" \
   "v_mfma_f32_16x16x32_bf16 v[48:51], a[24:27], v[104:107], v[48:51]\n\t" "ds_read_b128 v[100:103], v26 offset:13312\n\t" \
   "v_mfma_f32_16x16x32_bf16 v[52:55], a[108:111], v[108:111], v[52:55]\n\t" "v_add_f32 v116, v116, v118\n\t" \
   "v_mfma_f32_16x16x32_bf16 v[48:51], a[28:31], v[108:111], v[48:51]\n\t" "ds_read_b128 v[104:107], v26 offset:14336\n\t" \
   "v_add_f32 v117, v117, v119\n\t" "s_waitcnt lgkmcnt(7)\n\t" "v_mfma_f32_16x16x32_bf16 v[52:55], a[112:115], v[80:83], v[52:55]\n\t" \
   "v_mfma_f32_16x16x32_bf16 v[48:51], a[32:35], v[80:83], v[48:51]\n\t" "ds_read_b128 v[108:111], v26 offset:15360\n\t" \
   "v_add_f32 v112, v112, v120\n\t" "v_mfma_f32_16x16x32_bf16 v[52:55], a[116:119], v[84:87], v[52:55]\n\t" "v_add_f32 v113, v113, v121\n\t" \
   "v_mfma_f32_16x16x32_bf16 v[48:51], a[36:39], v[84:87], v[48:51]\n\t" "ds_read_b128 v[80:83], v26 offset:16384\n\t" "v_add_f32 v112, v112, v116\n\t" \
   "s_waitcnt lgkmcnt(5)\n\t" "v_mfma_f32_16x16x32_bf16 v[52:55], a[120:123], v[88:91], v[52:55]\n\t" \
   "v_mfma_f32_16x16x32_bf16 v[48:51], a[40:43], v[88:91], v[48:51]\n\t" "ds_read_b128 v[84:87], v26 offset:17408\n\t" "v_add_f32 v113, v113, v117\n\t" \
   "v_mfma_f32_16x16x32_bf16 v[52:55], a[124:127], v[92:95], v[52:55]\n\t" "v_mul_f32 v112, 0xbacccccd, v112\n\t" \
   "v_mfma_f32_16x16x32_bf16 v[48:51], a[44:47], v[92:95], v[48:51]\n\t" "ds_read_b128 v[88:91], v26 offset:18432\n\t" "s_waitcnt lgkmcnt(5)\n\t" \
   "v_mfma_f32_16x16x32_bf16 v[52:55], a[128:131], v[96:99], v[52:55]\n\t" "v_mul_f32 v113, 0x3acccccd, v113\n\t" \
   "v_mfma_f32_16x16x32_bf16 v[48:51], a[48:51], v[96:99], v[48:51]\n\t" "ds_read_b128 v[92:95], v26 offset:19456\n\t" \
   "v_fma_f32 v113, -v112, v112, v113\n\t" "v_mfma_f32_16x16x32_bf16 v[52:55], a[132:135], v[100:103], v[52:55]\n\t" "v_max_f32 v113, 0, v113\n\t" \
   "v_mfma_f32_16x16x32_bf16 v[48:51], a[52:55], v[100:103], v[48:51]\n\t" "ds_read_b128 v[96:99], v26 offset:20480\n\t" "s_waitcnt lgkmcnt(5)\n\t" \
   "v_mfma_f32_16x16x32_bf16 v[52:55], a[136:139], v[104:107], v[52:55]\n\t" "v_cvt_pk_bf16_f32 v113, v113, v112\n\t" \
   "v_mfma_f32_16x16x32_bf16 v[48:51], a[56:59], v[104:107], v[48:51]\n\t" "ds_read_b128 v[100:103], v26 offset:21504\n\t" \
   "v_and_b32 v8, 0xffff0000, v113\n\t" "v_mfma_f32_16x16x32_bf16 v[52:55], a[140:143], v[108:111], v[52:55]\n\t" \
   "v_mfma_f32_16x16x32_bf16 v[48:51], a[60:63], v[108:111], v[48:51]\n\t" "ds_read_b128 v[104:107], v26 offset:22528\n\t" \
   "v_lshlrev_b32 v113, 16, v113\n\t" "s_waitcnt lgkmcnt(5)\n\t" "v_mfma_f32_16x16x32_bf16 v[52:55], a[144:147], v[80:83], v[52:55]\n\t" \
   "v_add_f32 v113, 0x3727c5ac, v113\n\t" "v_mfma_f32_16x16x32_bf16 v[48:51], a[64:67], v[80:83], v[48:51]\n\t" \
   "ds_read_b128 v[108:111], v26 offset:23552\n\t" "v_mfma_f32_16x16x32_bf16 v[52:55], a[148:151], v[84:87], v[52:55]\n\t" \
   "v_cvt_pk_bf16_f32 v113, v113, v113\n\t" "v_mfma_f32_16x16x32_bf16 v[48:51], a[68:71], v[84:87], v[48:51]\n\t" \
   "ds_read_b128 v[80:83], v26 offset:24576\n\t" "v_lshlrev_b32 v113, 16, v113\n\t" "s_waitcnt lgkmcnt(5)\n\t" \
   "v_mfma_f32_16x16x32_bf16 v[52:55], a[152:155], v[88:91], v[52:55]\n\t" "v_sqrt_f32 v113, v113\n\t" \
   "v_mfma_f32_16x16x32_bf16 v[48:51], a[72:75], v[88:91], v[48:51]\n\t" "ds_read_b128 v[84:87], v26 offset:25600\n\t" \
   "v_mfma_f32_16x16x32_bf16 v[52:55], a[156:159], v[92:95], v[52:55]\n\t" "s_nop 0\n\t" \
   "v_mfma_f32_16x16x32_bf16 v[48:51], a[76:79], v[92:95], v[48:51]\n\t" "ds_read_b128 v[88:91], v26 offset:26624\n\t" \
   "v_cvt_pk_bf16_f32 v113, v113, v113\n\t" "s_waitcnt lgkmcnt(5)\n\t" "v_mfma_f32_16x16x32_bf16 v[60:63], a[80:83], v[96:99], 0\n\t" \
   "v_mfma_f32_16x16x32_bf16 v[56:59], a[0:3], v[96:99], 0\n\t" "ds_read_b128 v[92:95], v26 offset:27648\n\t" "v_lshlrev_b32 v113, 16, v113\n\t" \
   "v_mfma_f32_16x16x32_bf16 v[60:63], a[84:87], v[100:103], v[60:63]\n\t" "v_rcp_f32 v10, v113\n\t" \
   "v_mfma_f32_16x16x32_bf16 v[56:59], a[4:7], v[100:103], v[56:59]\n\t" "ds_read_b128 v[96:99], v26 offset:28672\n\t" "s_waitcnt lgkmcnt(5)\n\t" \
   "v_mfma_f32_16x16x32_bf16 v[60:63], a[88:91], v[104:107], v[60:63]\n\t" "v_fmac_f32 v48, v32, v8\n\t" "v_fmac_f32 v49, v33, v8\n\t" \
   "v_mfma_f32_16x16x32_bf16 v[56:59], a[8:11], v[104:107], v[56:59]\n\t" "ds_read_b128 v[100:103], v26 offset:29696\n\t" "v_fmac_f32 v50, v34, v8\n\t" \
   "v_mfma_f32_16x16x32_bf16 v[60:63], a[92:95], v[108:111], v[60:63]\n\t" "v_fmac_f32 v51, v35, v8\n\t" \
   "v_mfma_f32_16x16x32_bf16 v[56:59], a[12:15], v[108:111], v[56:59]\n\t" "ds_read_b128 v[104:107], v26 offset:30720\n\t" \
   "v_fmac_f32 v52, v36, v8\n\t" "v_fmac_f32 v53, v37, v8\n\t" "s_waitcnt lgkmcnt(5)\n\t" \
   "v_mfma_f32_16x16x32_bf16 v[60:63], a[96:99], v[80:83], v[60:63]\n\t" "v_fmac_f32 v54, v38, v8\n\t" \
   "v_mfma_f32_16x16x32_bf16 v[56:59], a[16:19], v[80:83], v[56:59]\n\t" "ds_read_b128 v[108:111], v26 offset:31744\n\t" "v_fmac_f32 v55, v39, v8\n\t" \
   "v_mfma_f32_16x16x32_bf16 v[60:63], a[100:103], v[84:87], v[60:63]\n\t" "v_mul_f32 v48, v48, v10\n\t" \
   "v_mfma_f32_16x16x32_bf16 v[56:59], a[20:23], v[84:87], v[56:59]\n\t" "ds_read_b128 v[80:83], v26 offset:32768\n\t" "v_mul_f32 v49, v49, v10\n\t" \
   "v_mul_f32 v50, v50, v10\n\t" "s_waitcnt lgkmcnt(5)\n\t" "v_mfma_f32_16x16x32_bf16 v[60:63], a[104:107], v[88:91], v[60:63]\n\t" \
   "v_mul_f32 v51, v51, v10\n\t" "v_mfma_f32_16x16x32_bf16 v[56:59], a[24:27], v[88:91], v[56:59]\n\t" "ds_read_b128 v[84:87], v26 offset:33792\n\t" \
   "v_mul_f32 v52, v52, v10\n\t" "v_mfma_f32_16x16x32_bf16 v[60:63], a[108:111], v[92:95], v[60:63]\n\t" "v_mul_f32 v53, v53, v10\n\t" \
   "v_mul_f32 v54, v54, v10\n\t" "v_mfma_f32_16x16x32_bf16 v[56:59], a[28:31], v[92:95], v[56:59]\n\t" "ds_read_b128 v[88:91], v26 offset:34816\n\t" \
   "v_mul_f32 v55, v55, v10\n\t" "s_waitcnt lgkmcnt(5)\n\t" "v_mfma_f32_16x16x32_bf16 v[60:63], a[112:115], v[96:99], v[60:63]\n\t" \
   "v_cmp_u_f32 vcc, v48, v48\n\t" "s_and_b64 vcc, vcc, s[42:43]\n\t" "s_or_b32 s41, s41, vcc_lo\n\t" "v_cndmask_b32 v30, v21, v72, vcc\n\t" \
   "v_mfma_f32_16x16x32_bf16 v[56:59], a[32:35], v[96:99], v[56:59]\n\t" "ds_read_b128 v[92:95], v26 offset:35840\n\t" \
   "v_cvt_pk_bf16_f32 v64, v48, v49\n\t" "v_mfma_f32_16x16x32_bf16 v[60:63], a[116:119], v[100:103], v[60:63]\n\t" \
   "v_cvt_pk_bf16_f32 v65, v50, v51\n\t" "v_cvt_pk_bf16_f32 v66, v52, v53\n\t" "v_mfma_f32_16x16x32_bf16 v[56:59], a[36:39], v[100:103], v[56:59]\n\t" \
   "ds_read_b128 v[96:99], v26 offset:36864\n\t" "v_cvt_pk_bf16_f32 v67, v54, v55\n\t" "s_waitcnt lgkmcnt(5)\n\t" \
   "v_mfma_f32_16x16x32_bf16 v[60:63], a[120:123], v[104:107], v[60:63]\n\t" "buffer_store_dwordx2 v[64:65], v30, %[rs], s48 offen offset:0 sc1\n\t" \
   "v_mfma_f32_16x16x32_bf16 v[56:59], a[40:43], v[104:107], v[56:59]\n\t" "ds_read_b128 v[100:103], v26 offset:37888\n\t" \
   "buffer_store_dwordx2 v[66:67], v30, %[rs], s47 offen offset:0 sc1\n\t" "v_mfma_f32_16x16x32_bf16 v[60:63], a[124:127], v[108:111], v[60:63]\n\t" \
   "v_add_f32 v122, v122, v124\n\t" "v_add_f32 v123, v123, v125\n\t" "v_mfma_f32_16x16x32_bf16 v[56:59], a[44:47], v[108:111], v[56:59]\n\t" \
   "ds_read_b128 v[104:107], v26 offset:38912\n\t" "v_add_f32 v126, v126, v128\n\t" "s_waitcnt lgkmcnt(5)\n\t" \
   "v_mfma_f32_16x16x32_bf16 v[60:63], a[128:131], v[80:83], v[60:63]\n\t" "v_add_f32 v127, v127, v129\n\t" \
   "v_mfma_f32_16x16x32_bf16 v[56:59], a[48:51], v[80:83], v[56:59]\n\t" "ds_read_b128 v[108:111], v26 offset:39936\n\t" \
   "v_add_f32 v122, v122, v130\n\t" "v_add_f32 v123, v123, v131\n\t" "v_mfma_f32_16x16x32_bf16 v[60:63], a[132:135], v[84:87], v[60:63]\n\t" \
   "v_add_f32 v122, v122, v126\n\t" "v_mfma_f32_16x16x32_bf16 v[56:59], a[52:55], v[84:87], v[56:59]\n\t" "v_add_f32 v123, v123, v127\n\t" \
   "s_waitcnt lgkmcnt(4)\n\t" "v_mfma_f32_16x16x32_bf16 v[60:63], a[136:139], v[88:91], v[60:63]\n\t" "v_mul_f32 v122, 0xbacccccd, v122\n\t" \
   "v_mfma_f32_16x16x32_bf16 v[56:59], a[56:59], v[88:91], v[56:59]\n\t" "v_mul_f32 v123, 0x3acccccd, v123\n\t" "v_fma_f32 v123, -v122, v122, v123\n\t" \
   "v_mfma_f32_16x16x32_bf16 v[60:63], a[140:143], v[92:95], v[60:63]\n\t" "v_max_f32 v123, 0, v123\n\t" \
   "v_mfma_f32_16x16x32_bf16 v[56:59], a[60:63], v[92:95], v[56:59]\n\t" "v_cvt_pk_bf16_f32 v123, v123, v122\n\t" "s_waitcnt lgkmcnt(2)\n\t" \
   "v_mfma_f32_16x16x32_bf16 v[60:63], a[144:147], v[96:99], v[60:63]\n\t" "v_and_b32 v12, 0xffff0000, v123\n\t" "v_lshlrev_b32 v123, 16, v123\n\t" \
   "v_mfma_f32_16x16x32_bf16 v[56:59], a[64:67], v[96:99], v[56:59]\n\t" "v_add_f32 v123, 0x3727c5ac, v123\n\t" \
   "v_mfma_f32_16x16x32_bf16 v[60:63], a[148:151], v[100:103], v[60:63]\n\t" "v_cvt_pk_bf16_f32 v123, v123, v123\n\t" \
   "v_mfma_f32_16x16x32_bf16 v[56:59], a[68:71], v[100:103], v[56:59]\n\t" "v_lshlrev_b32 v123, 16, v123\n\t" "s_waitcnt lgkmcnt(0)\n\t" \
   "v_mfma_f32_16x16x32_bf16 v[60:63], a[152:155], v[104:107], v[60:63]\n\t" "v_sqrt_f32 v123, v123\n\t" "s_nop 0\n\t" \
   "v_mfma_f32_16x16x32_bf16 v[56:59], a[72:75], v[104:107], v[56:59]\n\t" "v_cvt_pk_bf16_f32 v123, v123, v123\n\t" \
   "v_mfma_f32_16x16x32_bf16 v[60:63], a[156:159], v[108:111], v[60:63]\n\t" "v_lshlrev_b32 v123, 16, v123\n\t" \
   "v_mfma_f32_16x16x32_bf16 v[56:59], a[76:79], v[108:111], v[56:59]\n\t" "v_rcp_f32 v132, v123\n\t" "s_xor_b32 s46, s46, 44032\n\t" "s_nop 6\n\t" \
   "v_fmac_f32 v56, v32, v12\n\t" "v_fmac_f32 v57, v33, v12\n\t" "v_fmac_f32 v58, v34, v12\n\t" "v_fmac_f32 v59, v35, v12\n\t" \
   "v_fmac_f32 v60, v36, v12\n\t" "v_fmac_f32 v61, v37, v12\n\t" "v_fmac_f32 v62, v38, v12\n\t" "v_fmac_f32 v63, v39, v12\n\t" \
   "v_mul_f32 v56, v56, v132\n\t" "v_mul_f32 v57, v57, v132\n\t" "v_mul_f32 v58, v58, v132\n\t" "v_mul_f32 v59, v59, v132\n\t" \
   "v_mul_f32 v60, v60, v132\n\t" "v_mul_f32 v61, v61, v132\n\t" "v_mul_f32 v62, v62, v132\n\t" "v_mul_f32 v63, v63, v132\n\t" \
   "v_cmp_u_f32 vcc, v56, v56\n\t" "s_and_b64 vcc, vcc, s[42:43]\n\t" "s_or_b32 s41, s41, vcc_lo\n\t" "v_cndmask_b32 v30, v21, v72, vcc\n\t" \
   "v_cvt_pk_bf16_f32 v68, v56, v57\n\t" "v_cvt_pk_bf16_f32 v69, v58, v59\n\t" "v_cvt_pk_bf16_f32 v70, v60, v61\n\t" \
   "v_cvt_pk_bf16_f32 v71, v62, v63\n\t" "buffer_store_dwordx2 v[68:69], v30, %[rs], s48 offen offset:128 sc1\n\t" \
   "buffer_store_dwordx2 v[70:71], v30, %[rs], s47 offen offset:128 sc1\n\t" "s_mov_b32 s40, 170\n\t" "s_mov_b64 s[42:43], 0\n\t" \
   "s_cmp_lg_u32 s41, 0\n\t" "s_cbranch_scc1 5b\n\t" "s_add_u32 s45, s45, 4\n\t" "s_cmp_lt_u32 s45, 64\n\t" "s_cbranch_scc1 1b\n\t" \
   "s_waitcnt vmcnt(0) lgkmcnt(0)"
#define DF_CLOB "v8", "v9", "v10", "v11", "v12", "v13", "v14", "v15", "v32", "v33", "v34", "v35", "v36", "v37", "v38", "v39", "v40", "v41", "v42", "v43", "v44", "v45", "v46", "v47", "v48", "v49", "v50", "v51", "v52", "v53", "v54", "v55", "v56", "v57", "v58", "v59", "v60", "v61", "v62", "v63", "v64", "v65", "v66", "v67", "v68", "v69", "v70", "v71", "v72", "v73", "v74", "v75", "v76", "v77", "v78", "v79", "v80", "v81", "v82", "v83", "v84", "v85", "v86", "v87", "v88", "v89", "v90", "v91", "v92", "v93", "v94", "v95", "v96", "v97", "v98", "v99", "v100", "v101", "v102", "v103", "v104", "v105", "v106", "v107", "v108", "v109", "v110", "v111", "v112", "v113", "v114", "v115", "v116", "v117", "v118", "v119", "v120", "v121", "v122", "v123", "v124", "v125", "v126", "v127", "v128", "v129", "v130", "v131", "v132", "v133", "v134", "v135", "s40", "s41", "s42", "s43", "s44", "s45", "s46", "s47", "s48", "s49", "s50", "s51", "s52", "s53", "s54", "s55"
template <bool VU>
__device__ __forceinline__ void job_proj(int wv_, const v32u (&w)[10], unsigned char* ws, const Pool& pool, int layer, int h) {
  const unsigned lds0 = (unsigned)(uintptr_t)(__attribute__((address_space(3))) unsigned char*)dyn_lds;
  const int lane = lane_id(), wv = wv_, g = lane >> 4, r = lane & 15;
  const v4u rs = make_rs(ws, (unsigned)OFF_END);
  v16u ad = (unsigned)lane;
  ad[0] = (int)OFF_XR + wv * 10240 + lane * 16;
  ad[1] = wv * 64 + lane < 96 ? (int)OFF_ST + wv * 1024 + lane * 16 : OOB;
  ad[3] = cnt_off(layer, CNT_IDFT, lane & 31);
  ad[4] = lane == 0 ? kqf_off(layer, 0, (VU ? 32 : 0) + h) + wv * 4 : OOB;
  const int q = (g >> 1) * 256 + r * 16;
  ad[5] = (VU ? (int)OFF_UF + (wv * 320 + h) * 1024 + (g & 1) * 32768 : (h * RT * 4 + wv) * 1024 + (g & 1) * 4096) + q;
  ad[6] = lds0 + PJ_X + (g & 1) * 20480 + q;
  ad[7] = lds0 + PJ_X + 40960 + (lane & 31) * 48;
  ad[8] = lds0 + wv * 2048 + lane * 16;
  ad[9] = lds0 + 8192 + lane * 16;
  ad[10] = lds0 + PJ_LDS + 1024 + wv * 256 + (lane >> 5) * 128;
  if (layer > 0) fan_wait<64, NFIRST>(wv_, ws, fan_off(1, 1, 0), h);
  PH(2 + layer * 6)
#define PJ_DBG
#define PJ_OPS                                                                                                                      \
  : W_OPS, [rs] "s"(rs), [rdy] "s"((layer - 1) | ((1 << NFIRST / 8) - 1)), [ldx] "s"(lds0 + PJ_X + wv * 10240),                        \
    [ld1] "s"(lds0 + PJ_X + 40960 + (wv < 2 ? wv : 2) * 1024), [qf] "s"((int)OFF_QF), [kf] "s"((int)OFF_KF), [cnt] "s"(cnt_off(layer, CNT_IDFT, 0)), \
    [flw] "s"(lds0 + PJ_LDS + 512 + wv * 4) PJ_DBG
  if constexpr (VU) asm volatile(VU_ASM : "+{v[16:31]}"(ad) PJ_OPS : VU_CLOB, "vcc", "scc", "m0", "memory");
  else asm volatile(KQ_ASM : "+{v[16:31]}"(ad) PJ_OPS : KQ_CLOB, "vcc", "scc", "m0", "memory");
  LDSBAR();
}

__device__ __forceinline__ unsigned dft_idx(int f, int d) { return (unsigned)((f == 1 ? 320 : f >> 1) * d + (((f & 1) && f != 1) ? 160 : 0)) % 640u; }
__device__ __forceinline__ float cos640(unsigned i) { return __builtin_amdgcn_cosf((float)i * (1.f / 640)); }
__device__ __forceinline__ void job_dft(int wv_, unsigned char* ws, int layer, int fg, int set) {
  const unsigned lds0 = (unsigned)(uintptr_t)(__attribute__((address_space(3))) unsigned char*)dyn_lds;
  const int lane = lane_id(), wv = wv_, g = lane >> 4, r = lane & 15;
  const v4u rs = make_rs(ws, (unsigned)OFF_END);
  v32u w[5];
#pragma unroll
  for (int t = 0; t < 2; ++t) {
    const int f = 16 * (fg * 8 + wv * 2 + t) + r;
    const unsigned step = dft_idx(f & ~(f != 1), 32);
#pragma unroll
    for (int q = 0; q < 4; ++q) {
      unsigned m0 = dft_idx(f, sigma(g, 2 * q)), m1 = dft_idx(f, sigma(g, 2 * q + 1));
#pragma unroll
      for (int kt = 0; kt < KT; ++kt) {
        const int i = t * KT + kt;
        w[i >> 3][(i & 7) * 4 + q] = pk(cos640(m0), cos640(m1));
        m0 += step; m0 = min(m0, m0 - 640u); m1 += step; m1 = min(m1, m1 - 640u);
      }
    }
  }
  v16u ad = (unsigned)lane;
  ad[0] = (int)OFF_XR + wv * 10240 + lane * 16;
  ad[1] = wv * 64 + lane < 96 ? (int)OFF_ST + wv * 1024 + lane * 16 : OOB;
  ad[3] = cnt_off(layer, CNT_OUT, 0);
  ad[4] = wv == 0 && lane < FNG ? fan_off(0, 1, 0) + lane * FAN_G + (layer * 20 + set * 5 + fg) * 64 : OOB;
  ad[5] = (int)OFF_X1T + (((fg * 8 + wv * 2) * 4 + g) * XS + r) * 8;
  ad[6] = lds0 + lane * 16;
  ad[7] = lds0 + 40960 + r * 48;
  asm volatile(DF_ASM
               : "+{v[16:31]}"(ad)
               : "{a[0:31]}"(w[0]), "{a[32:63]}"(w[1]), "{a[64:95]}"(w[2]), "{a[96:127]}"(w[3]), "{a[128:159]}"(w[4]), [rs] "s"(rs), [set] "s"(set), [xs4] "s"(4 * XS * 8), [tg] "s"(TG0 + (TG1 - TG0) * layer),
                 [ldx] "s"(lds0 + wv * 10240), [ld0] "s"(lds0), [ld1] "s"(lds0 + 40960 + (wv < 2 ? wv : 2) * 1024)
               : DF_CLOB, "vcc", "scc", "m0", "memory");
}

constexpr int NOUT = 44, NLATE = 108 - NFILL;
constexpr int L_HS = PJ_LDS, MAIN_LDS = L_HS + 2048;
static_assert(MAIN_LDS >= JOB_LDS && MAIN_LDS >= REC_LDS && MAIN_LDS <= 160 * 1024, "LDS");
struct Go { Pool pool; int rot; };
__device__ __forceinline__ void st32(rsrc_t rs, int off, unsigned v) { __builtin_amdgcn_raw_buffer_store_b32(v, rs, off, 0, AUX_SC1); }
__device__ __forceinline__ unsigned hello_load(rsrc_t rs, int b) { return __builtin_amdgcn_raw_buffer_load_b32(rs, HS_GO + b * 16, 0, AUX_SC1); }
__device__ __forceinline__ void hello(int wv, rsrc_t rs, int b, unsigned t) {
  if (wv == 0) {
    st32(rs, lane_id() == 0 ? HS_HI + b * 8 : OOB, t + 1u);
    LDS_U32(lds_base() + L_HS) = t + 1u;
  }
}
__device__ __forceinline__ int go_off(int b) {
  const int l = lane_id() & 3;
  return l == 0 ? HS_GO + b * 16 : (l == 3 ? HS_ASK + (b & 127) * 16 : HS_PAY + (b & 31) * 32 + l * 16 - 16);
}
__device__ __forceinline__ Go wait_go(int wv, unsigned char* ws, int b, v4u rep, v4u m) {
  const v4u rs = make_rs(ws, (unsigned)OFF_END);
  const int l = lane_id();
  const unsigned a = __builtin_amdgcn_readfirstlane(LDS_U32(lds_base() + L_HS));
  for (unsigned spins = 0; spins < (1u << 18); ++spins) {
    if (spins || b >= 128) {
      VM_LD128(m, go_off(b), 0, 0);
      VM_WAIT1(0, m);
    }
    const unsigned n = __builtin_amdgcn_readlane(m.y, 0);
    if (__builtin_amdgcn_readlane(m.x, 0) == a && __builtin_amdgcn_readlane(m.x, 1) == n && __builtin_amdgcn_readlane(m.x, 2) == n) break;
    const unsigned t = (wv & 2) ? ((wv & 1) ? m.w : m.z) : ((wv & 1) ? m.y : m.x);
    const bool again = b >= 128 && l == 3 && t != rep.x;
    rep.x = t;
    VM_ST128(rep, again ? HS_CAL - 128 * 64 + b * 64 + wv * 16 : OOB, 0);
    if (spins) __builtin_amdgcn_s_sleep(6);
  }
  VM_ST32(m.z, (wv == 0 && l == 0) ? HS_HI + b * 8 + 4 : OOB, 0);
  if (b >= 128) { PH(1) }
  const auto u64 = [](unsigned lo, int i, unsigned hi, int k) {
    return (unsigned)__builtin_amdgcn_readlane(lo, i) | (unsigned long long)(unsigned)__builtin_amdgcn_readlane(hi, k) << 32;
  };
  Go g;
  g.pool.sel = u64(m.y, 1, m.z, 1); g.pool.s3 = u64(m.w, 1, m.w, 2); g.pool.ysel = u64(m.y, 2, m.z, 2);
  g.pool.base = __builtin_amdgcn_readfirstlane(pool_base(ws));
  g.rot = __builtin_amdgcn_readlane(m.w, 0) & 7;
  return g;
}
#define LDSTAMP(n)
#define LDX
#define LDC
__device__ __forceinline__ v4u load_rows(v32u (&w)[10], int kind, const u16* A, const u16* B, const u16* x, int h, int wv, unsigned slot, unsigned char* ws, int b, bool first) {
  unsigned tr;
  const int l = lane_id(), i = l & 15, g = l >> 4;
  const v4u ra = make_rs(A, 640u * 8192u), rb = make_rs(B, 640u * 8192u), rs = make_rs(ws, (unsigned)OFF_END), rx = make_rs(x, S_ * D_ * 2);
  v8u ad, ex;
  const bool pj = kind == 1 || kind == 2;
  const int wr = 4 * wv + g, rr = 4 * (pj ? g >> 1 : g) + (i >> 2), gt = pj ? g & 1 : 0;
  ad[0] = wr * 8192 + h * 256 + i * 16;
  ad[1] = (h * 128 + wr) * 1280 + i * 16;
  ad[2] = slot + rr * 256 + ((2 * wv + gt) ^ (rr & 7)) * 32 + (i & 3) * 8;
  ad[3] = pj ? ad[2] + 2048u : ad[2] ^ 32u;
  ad[4] = slot + wr * 256 + ((i >> 1) ^ (wr & 7)) * 32 + (i & 1) * 16;
  ad[5] = slot + wr * 328 + ((i >> 2) * 2 + (i & 1)) * 8 + ((i >> 1) & 1) * 4;
  ad[6] = slot + (kind == 3 ? (16 * (g & 1) + 4 * (g >> 1) + ((i >> 2) & 1)) * 328 + (i >> 3) * 8 : rr * 328 + gt * 8) + (i & 3) * 10560 + wv * 80;
  ad[7] = ad[4] + 65536u;
  const unsigned im = lds_base() + PJ_X + (wv >> 1) * PJ_BUF;
  const bool hi = first && wv == 0;
  ex[0] = go_off(b);
  ex[1] = hi ? HS_GO + b * 16 : OOB;
  ex[2] = hi && l == 0 ? HS_HI + b * 8 : OOB;
  ex[3] = lds_base() + L_HS + (hi ? 0 : 4);
  ex[4] = (wv * 16 + (l >> 3)) * 1280 + (l & 7) * 16;
  ex[5] = im + ((wv & 1) * KT + ((l >> 2) & 1)) * 1024 + ((l & 1) * 32 + (l >> 3)) * 16 + (l & 2) * 4;
  ex[6] = im + 40960 + ((wv & 1) * 16 + (l >> 3)) * 48 + min(l & 7, 5) * 8;
  ex[7] = (l & 7) ? 0u : ~0u;
  v4u m;
#define LD_OPS                                                                                                                                       \
  : "={a[0:31]}"(w[0]), "={a[32:63]}"(w[1]), "={a[64:95]}"(w[2]), "={a[96:127]}"(w[3]), "={a[128:159]}"(w[4]), "={a[160:191]}"(w[5]),                \
    "={a[192:223]}"(w[6]), "={a[224:255]}"(w[7]), "={v[192:223]}"(w[8]), "={v[224:255]}"(w[9]), "={v[28:31]}"(m) LDX                                 \
  : "{v[16:23]}"(ad), "{v[112:119]}"(ex), [ra] "s"(ra), [rb] "s"(rb), [rw] "s"(rs), [rx] "s"(rx), [p0] "s"(wv < 3 ? 3 * wv : 8),                     \
    "{v27}"(lds_base() + L_HS + 1024 + wv * 256 + (l >> 5) * 128)                                                                                   \
  : LDP_CLOB LDC, "scc", "memory"
  if (kind == 0) asm volatile(LDP_ASM LD_OPS);
  else if (kind == 1) asm volatile(LDK_ASM LD_OPS);
  else if (kind == 2) asm volatile(LDV_ASM LD_OPS);
  else asm volatile(LDY_ASM LD_OPS);
  return m;
}
__device__ __forceinline__ v4u prep_jobs(int wv, unsigned char* ws, rsrc_t rs, const u16* __restrict__ x, const u16* __restrict__ mem0, int j) {
  const int lane = lane_id(), g = lane >> 4, r = lane & 15, tid = wv * 64 + lane, pb = pool_base(ws);
  const int xc = (int)xcc_id(), u = (j >> 3) * 4 + (xc & 3), page = xc < 4 ? u >> 2 : 16 + u % NYP;
  const int cb = pb + page * PAGE + M_CAL + (xc < 4 ? wv * REG + xc * 512 : 3 * REG - u / NYP * WIN + wv * 512);
  stc(rs, lane < 32 ? cb + (lane >> 3) * 8192 + (lane & 7) * 16 : OOB, v4u{1u, 2u, 3u, 4u});
  const unsigned char* p = ws + __builtin_amdgcn_readfirstlane(cb);
  unsigned t0, t1, t2, t3, t4;
  asm volatile("s_load_dword %0, %4, 0x80\n\ts_load_dword %1, %4, 0x2080\n\ts_load_dword %2, %4, 0x4080\n\ts_load_dword %3, %4, 0x6080"
               : "={s40}"(t0), "={s41}"(t1), "={s42}"(t2), "={s43}"(t3) : "s"(p) : "memory");
  const unsigned tg = hello_load(rs, 128 + j);
  v2u c[5][2];
  unsigned t[4];
#pragma unroll
  for (int q = 0; q < 5; ++q) {
    const u16* xr = x + (size_t)(j * 16 + r) * D_ + 32 * (5 * wv + q) + 4 * g;
    c[q][0] = *(const v2u*)xr; c[q][1] = *(const v2u*)(xr + 16);
  }
#pragma unroll
  for (int k = 0; k < 4; ++k) t[k] = *(const unsigned*)(mem0 + (size_t)((tid >> 1) & 7) * D_ + colmap((min(j >> 3, 4) * 8 + ((tid >> 4) & 7)) * 2 + (tid & 1), 2 * k));
  __builtin_amdgcn_sched_barrier(0);
  const v4u ones = {0xFFFFFFFFu, 0xFFFFFFFFu, 0xFFFFFFFFu, 0xFFFFFFFFu};
  const int o = (j * 256 + tid) * 16;
  const bool cal = (o & 0x79800) == 0x79800;
  stc(rs, (int)OFF_HO + o, ones);
  stc(rs, o + REG < (int)HO_FIRST ? (int)OFF_HO + REG + o : OOB, ones);
  stc(rs, (cal || (o >= W_MEM && o < W_MISC)) ? OOB : pb + (NSEG - 1) * REG + o, ones);
  stc(rs, cal ? OOB : pb + o, ones);
  stc(rs, pb + P_Y + o, ones);
  stc(rs, o < 8 * 16384 ? pb + P_Y + REG + o : OOB, ones);
  hello(wv, rs, 128 + j, tg);
  asm volatile("" ::"v"(tg));
  __builtin_amdgcn_sched_barrier(0);
#define CAL_T(r, o) "s_load_dword " r ", %5, " o "\n\ts_waitcnt lgkmcnt(0)\n\ts_memtime s[46:47]\n\ts_waitcnt lgkmcnt(0)\n\ts_mov_b32 " r ", s46\n\t"
  asm volatile(CAL_T("%0", "0x80") CAL_T("%1", "0") CAL_T("%2", "0x2000") CAL_T("%3", "0x4000") CAL_T("%4", "0x6000")
               : "+{s40}"(t0), "+{s41}"(t1), "+{s42}"(t2), "+{s43}"(t3), "={s44}"(t4) : "s"(p) : "s46", "s47", "memory");
  PH(16)
  __builtin_amdgcn_sched_barrier(0);
  f32x2 st = {0.f, 0.f};
#pragma unroll
  for (int q = 0; q < 5; ++q) {
    const v4u f = v4u{c[q][0].x, c[q][0].y, c[q][1].x, c[q][1].y};
    stc(rs, (int)OFF_XR + ((j * KT + 5 * wv + q) * 64 + lane) * 16, f);
    st += frag_stat(f);
  }
  stc(rs, j < 40 ? pb + (NSEG - 1) * REG + W_MEM + o : OOB, v4u{nosent(t[0]), nosent(t[1]), nosent(t[2]), nosent(t[3])});
  st = rows_sum(st);
  __builtin_amdgcn_raw_buffer_store_b64(__builtin_bit_cast(v2u, g ? f32x2{0.f, 0.f} : st), rs,
                                        (g == 0 || (g == 1 && wv == 0)) ? (int)OFF_ST + ((j * 16 + r) * 6 + (g ? 4 : wv)) * 8 : OOB, 0, AUX_SC1);
  const unsigned ask = __builtin_amdgcn_raw_buffer_load_b32(rs, HS_ASK + j * 16 + wv * 4, 0, AUX_SC1);
  const unsigned b0[4] = {min(t1 - t0, 9999u), min(t2 - t1, 9999u), min(t3 - t2, 9999u), min(t4 - t3, 9999u)}, sh = ((xc ^ wv) & 1) * 16;
  const v4u rep = {ask, (unsigned)page, b0[0] << sh | b0[2] << (16 - sh), b0[1] << sh | b0[3] << (16 - sh)};
  asm volatile("s_waitcnt vmcnt(0)" ::: "memory");
  stc(rs, lane == 0 ? HS_CAL + j * 64 + wv * 16 : OOB, rep);
  LDSBAR();
  PH(18)
  return rep;
}
__device__ __forceinline__ int wave_isum(int v) {
  asm volatile("s_nop 1\n\tv_add_u32_dpp %0, %0, %0 quad_perm:[1,0,3,2] row_mask:0xf bank_mask:0xf\n\t"
               "s_nop 1\n\tv_add_u32_dpp %0, %0, %0 quad_perm:[2,3,0,1] row_mask:0xf bank_mask:0xf\n\t"
               "s_nop 1\n\tv_add_u32_dpp %0, %0, %0 row_half_mirror row_mask:0xf bank_mask:0xf\n\t"
               "s_nop 1\n\tv_add_u32_dpp %0, %0, %0 row_mirror row_mask:0xf bank_mask:0xf\n\ts_nop 0"
               : "+v"(v));
  return (__builtin_amdgcn_readlane(v, 0) + __builtin_amdgcn_readlane(v, 16)) + (__builtin_amdgcn_readlane(v, 32) + __builtin_amdgcn_readlane(v, 48));
}
__device__ __forceinline__ Go lead(int wv, unsigned char* ws, const u16* x, const u16* mem0, int rot) {
  const v4u rs = make_rs(ws, (unsigned)OFF_END);
  const int t = wv * 64 + lane_id();
  int* acc = (int*)(dyn_lds + L_HS + 128);
#define VM_LD64(dst, off) asm volatile("buffer_load_dwordx2 %0, %1, %2, 0 offen sc1" : "=v"(dst) : "v"(off), "s"(rs) : "memory")
  v4u c0, c1;
  v2u hi;
  unsigned old, n = 0;
  VM_LD128(c0, HS_CAL + t * 32, 0, 0);
  VM_LD128(c1, HS_CAL + t * 32, 0, 16);
  VM_LD64(hi, HS_HI + t * 8);
  VM_LD32(old, HS_PAY + (t & 63) * 16, 0);
  asm volatile("s_waitcnt vmcnt(0)" : "+v"(c0), "+v"(c1), "+v"(hi), "+v"(old)::"memory");
  const unsigned r0 = c0.x + 1u, r1 = c1.x + 1u, q = hi.y + 1u;
  __builtin_amdgcn_raw_buffer_store_b64(v2u{r0, r1}, ws_rsrc(ws), HS_ASK + t * 8, 0, AUX_SC1);
  while (__any(old == n)) ++n;
  if (t < 192) acc[t] = 9999;
  prep_jobs(wv, ws, ws_rsrc(ws), x, mem0, 127);
  for (unsigned spins = 0; spins < (1u << 18); ++spins) {
    VM_LD128(c0, HS_CAL + t * 32, 0, 0);
    VM_LD128(c1, HS_CAL + t * 32, 0, 16);
    VM_LD64(hi, HS_HI + t * 8);
    asm volatile("s_waitcnt vmcnt(0)" : "+v"(c0), "+v"(c1), "+v"(hi)::"memory");
    if (!__any(t < 254 && (c0.x != r0 || c1.x != r1))) break;
    __builtin_amdgcn_s_sleep(2);
  }
  if (t < 254) {
    int *a0 = acc + min(c0.y, 47u) * 4, *a1 = acc + min(c1.y, 47u) * 4;
    atomicMin(a0, c0.z & 0xFFFF); atomicMin(a0 + 1, c0.z >> 16); atomicMin(a0 + 2, c0.w & 0xFFFF); atomicMin(a0 + 3, c0.w >> 16);
    atomicMin(a1, c1.z & 0xFFFF); atomicMin(a1 + 1, c1.z >> 16); atomicMin(a1 + 2, c1.w & 0xFFFF); atomicMin(a1 + 3, c1.w >> 16);
  }
  __syncthreads();
  PH(19)
  const int s = lane_id(), fx = (s & 4) ? -1 : 1, fy = ((s * 43 >> 7) & 1) ? -1 : 1;
  const v4i u = ((const v4i*)acc)[s >> 2], v = ((const v4i*)acc)[16 + (s * 43 >> 7)];
  int ax = min(u.x, u.y) - min(u.z, u.w), yx = min(v.z, v.w) - min(v.x, v.y);
  const int n0 = ax > 0 ? u.z : u.x, n1 = ax > 0 ? u.w : u.y, lu = min(n0, n1), lv = min(min(v.x, v.y), min(v.z, v.w)), near = 110 - wave_imax(-min(lu, lv));
  const int sa = (lu < near && abs(ax) > 60) ? (ax > 0 ? fx : -fx) : 0, sy = (lv < near && abs(yx) > 60) ? (yx > 0 ? fy : -fy) : 0;
  const int g = wave_isum(sa + sy), m = wave_isum(abs(sa) + abs(sy));
  const bool with = sa * g > 0;
  const int bs = wave_imax(with ? -n1 : -9999) - wave_imax(with ? -n0 : -9999);
  int b = n0 - n1;
  if (abs(g) * 8 > m * 7) {
    if (sa * g >= 0) { ax = g * fx; b = bs; }
    if (sy == 0) yx = g * fy;
  }
  Go go;
  go.pool.sel = __ballot(ax > 0);
  go.pool.s3 = __ballot(b > 0) ^ 0xAAAAAAAAAAAAAAAAull;
  go.pool.ysel = __ballot(yx > 0);
  go.pool.base = __builtin_amdgcn_readfirstlane(pool_base(ws));
  go.rot = rot;
  PH(20)
  {
    const unsigned long long v = (s & 1) ? go.pool.ysel : go.pool.sel;
    VM_ST128((v4u{n, (unsigned)v, (unsigned)(v >> 32), (unsigned)(go.pool.s3 >> (s & 1) * 32)}), wv == 0 ? HS_PAY + s * 16 : OOB, 0);
  }
  for (unsigned spins = 0; spins < (1u << 18); ++spins) {
    VM_ST128((v4u{hi.x, n, q, (unsigned)rot}), HS_GO + t * 16, 0);
    const unsigned sent = hi.x;
    do {
      __builtin_amdgcn_s_sleep(8);
      VM_LD64(hi, HS_HI + t * 8);
      VM_WAIT1(0, hi);
    } while (!__any(hi.x != sent) && __any(t < 255 && hi.y != q) && ++spins < (1u << 18));
    if (!__any(t < 255 && hi.y != q)) break;
  }
  return go;
}
__device__ __forceinline__ void late_jobs(int wv_, unsigned char* ws, rsrc_t rs, const u16* __restrict__ wr, const u16* __restrict__ wi, int j) {
  const int lane = lane_id(), wv = wv_, g = lane >> 4, r = lane & 15;
  unsigned* t32 = (unsigned*)dyn_lds;
  for (int o = (int)HO_FIRST + (j * 256 + wv * 64 + lane) * 16; o < 2 * (int)HO_LAYER; o += NLATE * 4096)
    stc(rs, (int)OFF_HO + o, v4u{0xFFFFFFFFu, 0xFFFFFFFFu, 0xFFFFFFFFu, 0xFFFFFFFFu});
#pragma unroll 1
  for (int fi = j * 4 + wv; fi < 20 * NFK; fi += NLATE * 4) {
    v4u v;
#pragma unroll
    for (int q = 0; q < 4; ++q) {
      const int e = fi % NFK, d = 16 * (fi / NFK) + r, f = 4 * (32 * (e >> 2) + 8 * q + 2 * (e & 2) + g) + 2 * (e & 1);
      const float sc = f ? 2.f : 1.f;
      v[q] = pk(cos640(dft_idx(f, d)) * sc, cos640(dft_idx(f + 1, d)) * sc);
    }
    stc(rs, (int)OFF_INV + (fi * 64 + lane) * 16, v);
  }
#pragma unroll 1
  for (int b = j; b < 32 * 6; b += NLATE) {
    const int m0 = (b & 31) * 64, k0 = (b >> 5) * 64, n0 = (b & 24) * 2 + (b & 7) * 256;
#pragma unroll
    for (int q = 0; q < 16; ++q) {
      const size_t i = (size_t)(n0 + q + 64 * wv) * 321 + min(k0 + lane, 320);
      t32[(q * 4 + wv) * 65 + lane] = (unsigned)wr[i] | ((unsigned)wi[i] << 16);
    }
    __syncthreads();
#pragma unroll
    for (int q = 0; q < 16; ++q) {
      const int k = q * 4 + wv;
      st32(rs, k0 + k < 321 ? (int)OFF_WT + ((k0 + k) * S_ + m0 + lane) * 4 : OOB, t32[lane * 65 + k]);
    }
    __syncthreads();
  }
  dep_signal(wv_, ws, cnt_off(0, CNT_IDFT, 0));
}
__device__ __forceinline__ void late_wait(int wv_, unsigned char* ws) {
  if (TIDX == 0) {
    for (unsigned spins = 0; spins < (1u << 17); ++spins) {
      if (__hip_atomic_load((gu32*)(ws + cnt_off(0, CNT_IDFT, 0)), __ATOMIC_RELAXED, __HIP_MEMORY_SCOPE_AGENT) == NLATE - 1) break;
      __builtin_amdgcn_s_sleep(16);
    }
  }
  __syncthreads();
}
#define FPACE 1200
#define FILLK 12
#define STR_(x) #x
#define STR(x) STR_(x)
__device__ __forceinline__ void fill_run(int wv, unsigned char* ws, const Pool& pool, int f, int n0, int n1) {
  const v4u rs = make_rs(ws, (unsigned)OFF_END), ones = {~0u, ~0u, ~0u, ~0u};
  const int lane = lane_id();
  unsigned due = rtc() * 8u + (unsigned)(f * 4 + wv) * (FPACE / 576);
  int fo = OOB;
#pragma unroll 1
  for (int n = n0; n <= n1; ++n) {
    const int t = n & 63, rg = pool.region(t), yr = pool.base + P_Y + y_region(pool.ysel, t) - 32 * 16384;
    if (n == NSEG) { PH(44) late_wait(wv, ws); }
    if (n == NSEG + FILLK + 2) { PH(45) }
    if (n == 127) { PH(46) }
    if ((n == NSEG + 1 || n == NSEG + FILLK + 1) && n < n1) {
      unsigned c;
      for (unsigned spins = 0; spins < SPIN_CAP; ++spins) {
        VM_LD32(c, cnt_off(0, CNT_OUT, min(lane, t > 1 ? NSEG - 1 : FILLK + 1)), 0);
        VM_WAIT1(0, c);
        if (!__any((c & 15u) != 9u)) break;
        __builtin_amdgcn_s_sleep(32);
      }
    }
#pragma unroll 1
    for (int k = 0; k < 144 / NFILL; ++k) {
      const int i = f * 4 + wv + 4 * NFILL * k;
      const bool on = t && n < n1 && (t < NSEG - 1 || i < 160 || i >= 240);
      const int so = (i < 256 ? rg : yr) + (i >> 3) * 16384 + (i & 7) * 1024;
      if (on) {
        unsigned now;
        while ((int)(due - (now = rtc() * 8u)) > 0) __builtin_amdgcn_s_sleep(1);
        due = max((int)(due - now), -FPACE * NFILL / 144) + now + FPACE * NFILL / 144;
      }
      VM_ST128(ones, on ? lane * 16 : OOB, so);
      if (k == FLAG - 1) {
        asm volatile("s_waitcnt vmcnt(" STR(FLAG) ")" ::: "memory");
        VM_ST32(1u, fo, 0);
      }
    }
    fo = t && lane < (t == NSEG - 1 ? 2 : 1) ? kqf_off(n >> 6, t - 1 + lane, f) + 16 + wv * 4 : OOB;
  }
}
#define TROB (24 * 256 + 16)
#define TC2 30
#define TVIS 32
#define TDLT 170
#define TM1 88
#define SPARE_US 200
#define SPARE_NAP 127
#define TM2 60
__global__ void __launch_bounds__(256, 1) k_main(unsigned char* ws, const u16* x, const u16* mem0, const u16* Wq, const u16* Wk, const u16* Wv, const u16* Wo,
                                                const u16* wr, const u16* wi, u16* out, long nlayer
                                                ) {
  v4u* smem = (v4u*)dyn_lds;
  const int wv_ = __builtin_amdgcn_readfirstlane(threadIdx.x >> 6);
  const RecTune tn = {TM1, TM2, 4, TROB, TC2, TVIS, TDLT, 1};
  unsigned long long* dbg = nullptr;
#define RSTAMP_(i)
  const rsrc_t rs = ws_rsrc(ws);
  PH(0)
  const int b = blockIdx.x, tt = b >> 3;
  const unsigned t00 = rtc();
  int rot = (int)(xcc_id() - b) & 7, lpl, ljb = -1;
  if (tt < 16) {
    v32u w[10];
    Go go;
    const unsigned lds0 = lds_base();
#pragma unroll 1
    for (int it = 0;; ++it) {
      const int p = (b + rot) & 7, h = (p & 3) * 8 + (tt & 7);
      const bool v = (tt & 8) != 0;
      const v4u m = load_rows(w, p < 4 ? (v ? 3 : 0) : (v ? 2 : 1), v ? Wv : Wq, v ? Wo : Wk, x, h, wv_, lds0 + (p < 4 ? 0 : PJ_X + 2 * PJ_BUF), ws, b, it == 0);
      PH(21 + (p >> 2))
      go = wait_go(wv_, ws, b, m, m);
      if (go.rot == rot || it) break;
      rot = go.rot;
    }
    Pool pool = go.pool;
    const int pl = (b + rot) & 7, role = pl < 4 ? (tt & 8) * 4 + pl * 8 + (tt & 7) : pl * 32 + tt;
    const int hd = (pl - 4) * 8 + (tt & 7);
#pragma unroll 1
    for (int layer = 0; layer < nlayer; ++layer) {
      int ro = role, xs = pl, wq = wv_, h2 = hd | (tt & 8) << 8;
      asm volatile("" : "+s"(ro), "+s"(xs), "+s"(wq), "+s"(h2), "+s"(ws), "+s"(pool.base));
      if (layer > 0) {
        const int jb = ro < 64 ? ro + 57 : (xs - 4) * 16 + (ro & 31);
        late_wait(wq, ws);
        if (ro < 64 || jb < 57) {
          job_col(wq, ws, ws_rsrc(ws), lds_base(), jb, fan_off(0, 1, 0), fan_off(1, 0, 0), false);
          __syncthreads();
          RSTAMP_(5)
          PH(7)
        } else fan_wait<64, 20>(wq, ws, fan_off(0, 1, 0), jb);
      }
      if (ro < 32) { rec_p1(wq, w, ws, pool, layer, ro, xs, tn, dbg); PH(4 + layer * 6) }
      else if (ro < 64) rec_pv(wq, w, ws, pool, layer, ro - 32, xs, tn, dbg);
      else {
        if (h2 >> 8) job_proj<true>(wq, w, ws, pool, layer, h2 & 31);
        else job_proj<false>(wq, w, ws, pool, layer, h2 & 31);
        RSTAMP_(2)
      }
    }
    lpl = pl;
    ljb = role < 64 ? role + 57 : (pl - 4) * 16 + (role & 31);
    ljb = role < 32 ? -1 : ljb + (role >= 64 && ljb >= 57);
  } else {
  const Go go = b == 255 ? lead(wv_, ws, x, mem0, rot) : wait_go(wv_, ws, b, prep_jobs(wv_, ws, rs, x, mem0, b - 128), v4u{0u, 0u, 0u, 0u});
  Pool pool = go.pool;
  const int pl = (b + go.rot) & 7;
  const int role = pl < 4 ? (tt < 26 ? 64 + pl * 10 + tt - 16 : 104 + pl * 6 + tt - 26) : pl * 32 + tt;
  if (tt < (pl < 4 ? 32 - NFILL / 4 : 27)) late_jobs(wv_, ws, rs, wr, wi, pl < 4 ? 44 + pl * (16 - NFILL / 4) + tt - 16 : (pl - 4) * 11 + tt - 16);
  else if (pl < 4) fill_run(wv_, ws, pool, (31 - tt) * 4 + pl, 1, (int)nlayer * NSEG);
  PH(23)
  lpl = pl;
  if (role >= 104 && role < 128) {
    while (rtc() - t00 < 100u * SPARE_US) __builtin_amdgcn_s_sleep(127);
    late_wait(wv_, ws);
    prog_prewait(wv_, ws, kqf_off((int)nlayer - 1, NSEG - 1, ((pl + 1) & 3) * 8 + tt - 26));
    __syncthreads();
    __builtin_amdgcn_s_sleep(SPARE_NAP);
    ljb = role - 39;
  } else if (role >= 64 && role < 104) {
#pragma unroll 1
    for (int layer = 0; layer < nlayer; ++layer) {
      if (layer > 0) {
        late_wait(wv_, ws);
        job_col(wv_, ws, rs, lds_base(), role + 57, fan_off(0, 1, 0), fan_off(1, 0, 0), false);
        __syncthreads();
        PH(7)
      }
      ((unsigned*)(dyn_lds + L_T))[lane_id() & 15] = 0u;
      __syncthreads();
      if (wv_ == 3) rec_fin(ws, pool, layer, role - 64, pl, tn, dbg);
      else if (wv_ == 0) rec_sched(ws, pool, layer, role - 64, pl, tn, dbg);
      __syncthreads();
    }
    ljb = role + 57;
  } else {
#pragma unroll 1
  for (int layer = 0; layer < nlayer; ++layer) {
    int wq = wv_;
    asm volatile("" : "+s"(wq));
    if (layer > 0) late_wait(wq, ws);
    if (layer > 0) job_idft(wq, ws, rs, smem, ((tt - 16) >> 1) * 4 + pl - 4, tt & 1, dbg + 60000 + role * 8);
    RSTAMP_(2)
    __syncthreads();
    if (tt >= 27) {
      job_dft(wq, ws, layer, tt - 27, pl - 4);
      __syncthreads();
      if (layer) { PH(14) }
    } else {
#pragma unroll 1
      for (int j = (tt - 16) * 4 + pl - 4; j < 640 - NFAST * 5; j += NOUT) {
        const int u = j / 5 - NFAST, s = u < 0 ? j / 5 : NFAST + (u >> 1);
        flags32_wait(wq, ws, kqf_off(layer, s, 32));
        job_out(wq, ws, rs, pool.ysel, layer, s, j % 5, u < 0 ? -1 : u & 1);
      }
    }
    RSTAMP_(3)
    PH((tt >= 27 ? 6 : 5) + layer * 6)
  }
  if (role == 4 * 32 + 23) ljb = 57;
  }
  }
  int wq = wv_, tl = tt;
  asm volatile("" : "+s"(wq), "+s"(ljb), "+s"(lpl), "+s"(tl), "+s"(ws));
  job_last(wq, ws, out, ljb, lpl * 16 + (tl >> 1), tl & 1);
}

extern "C" void kernel_launch(void* const* d_in, const int* in_sizes, int n_in, void* d_out, int out_size, void* d_ws, size_t ws_size,
                              hipStream_t stream) {
  const u16* x = (const u16*)d_in[0];
  const u16* mem0 = (const u16*)d_in[1];
  const u16* Wq = (const u16*)d_in[2];
  const u16* Wk = (const u16*)d_in[3];
  const u16* Wv = (const u16*)d_in[4];
  const u16* Wo = (const u16*)d_in[5];
  const u16* wr = (const u16*)d_in[8];
  const u16* wi = (const u16*)d_in[9];
  unsigned char* ws = (unsigned char*)d_ws;
  u16* out = (u16*)d_out;
  (void)hipFuncSetAttribute((const void*)k_main, hipFuncAttributeMaxDynamicSharedMemorySize, MAIN_LDS);
  k_main<<<256, 256, MAIN_LDS, stream>>>(ws, x, mem0, Wq, Wk, Wv, Wo, wr, wi, out, 2);
}
```
